```python
import torch

def _softmax(x, axis=-1):
    return torch.softmax(x, dim=axis)

def _gelu(x):
    return torch.nn.functional.gelu(x, approximate='tanh')

def _silu(x):
    return torch.nn.functional.silu(x)

def _sigmoid(x):
    return torch.sigmoid(x)

def _layernorm(x, axis=-1, eps=1e-05):
    m = x.mean(dim=axis, keepdim=True)
    v = x.var(dim=axis, keepdim=True, unbiased=False)
    return (x - m) / torch.sqrt(v + eps)

def _scatter_add(tgt, idx, src, axis=0):
    return tgt.clone().index_add_(axis, idx, src)

def _segment_sum(src, seg, n):
    out = torch.zeros((n,) + src.shape[1:], dtype=src.dtype)
    return out.index_add_(0, seg, src)

def _pad(x, pw):
    flat = tuple((v for pair in reversed(pw) for v in pair))
    return torch.nn.functional.pad(x, flat)
kernel_name = 'concat_heads__ssm_conv_h3__memory_aug_recurrent_mem_tr__prompt'

def setup_inputs():

    _w = lambda a: (lambda t: t.to(torch.bfloat16) if t.is_floating_point() else t)(torch.as_tensor(a))

    def _A_setup():
        B, S, D = (1, 2048, 1024)
        ks = 4
        return {'q': _w((lambda *s: torch.randn(*s, dtype=torch.bfloat16) * 0.05)(B, S, D)), 'k': _w((lambda *s: torch.randn(*s, dtype=torch.bfloat16) * 0.4)(B, S, D)), 'v': _w((lambda *s: torch.randn(*s, dtype=torch.bfloat16) * 0.4)(B, S, D)), 'shift_k': _w((lambda *s: torch.randn(*s, dtype=torch.bfloat16) * 0.4)(D, ks) * 0.5), 'ssm_k': _w((lambda *s: torch.randn(*s, dtype=torch.bfloat16) * 0.4)(D, S) * (1.0 / S ** 0.5)), 'ssm_D': _w((lambda *s: torch.randn(*s, dtype=torch.bfloat16) * 0.4)(D))}
    _A_in = _A_setup()

    def _B_setup():
        B, S, Dm = (1, 2048, 1024)
        H, Dh = (32, 128)
        M, seg = (8, 32)
        seg = min(seg, S)
        S = S // seg * seg
        return {'x': _w((lambda *s: torch.randn(*s, dtype=torch.bfloat16) * 0.05)(B, S, Dm)), 'mem0': _w((lambda *s: torch.randn(*s, dtype=torch.bfloat16) * 0.4)(B, M, Dm)), 'Wq': _w((lambda *s: torch.randn(*s, dtype=torch.bfloat16) * 6.4)(Dm, H, Dh) * Dm ** (-0.5)), 'Wk': _w((lambda *s: torch.randn(*s, dtype=torch.bfloat16) * 6.4)(Dm, H, Dh) * Dm ** (-0.5)), 'Wv': _w((lambda *s: torch.randn(*s, dtype=torch.bfloat16) * 0.4)(Dm, H, Dh) * Dm ** (-0.5)), 'Wo': _w((lambda *s: torch.randn(*s, dtype=torch.bfloat16) * 0.4)(H, Dh, Dm) * (H * Dh) ** (-0.5)), 'seg_len': seg}
    _B_in = _B_setup()
    return {'A_q': _A_in['q'], 'A_k': _A_in['k'], 'A_v': _A_in['v'], 'A_shift_k': _A_in['shift_k'], 'A_ssm_k': _A_in['ssm_k'], 'A_ssm_D': _A_in['ssm_D'], 'B_x': _B_in['x'], 'B_mem0': _B_in['mem0'], 'B_Wq': _B_in['Wq'], 'B_Wk': _B_in['Wk'], 'B_Wv': _B_in['Wv'], 'B_Wo': _B_in['Wo'], 'B_seg_len': _B_in['seg_len']}

def reference(A_q, A_k, A_v, A_shift_k, A_ssm_k, A_ssm_D, B_x, B_mem0, B_Wq, B_Wk, B_Wv, B_Wo, B_seg_len):

    def _A_ref(q, k, v, shift_k, ssm_k, ssm_D):
        B, S, D = q.shape
        ks = shift_k.shape[-1]
        k_pad = _pad(k, ((0, 0), (ks - 1, 0), (0, 0)))
        k_shift = torch.zeros_like(k)
        for t in range(ks):
            k_shift = k_shift + k_pad[:, t:t + S, :] * shift_k[:, ks - 1 - t][None, None, :]
        kv = k_shift * v
        L = 2 * S
        kv_pad = _pad(kv, ((0, 0), (0, S), (0, 0)))
        kf_pad = _pad(ssm_k, ((0, 0), (0, S)))
        kv_f = (lambda x, axes=(-2, -1): torch.fft.rfft2(x.float(), dim=axes))(kv_pad[:, None, :, :], axes=(1, 2))
        kf_f = (lambda x, axes=(-2, -1): torch.fft.rfft2(x.float(), dim=axes))(kf_pad[:, None, :], axes=(1, 2))
        kf_f = (lambda x, *d: x.permute(*d))(kf_f, 1, 2, 0)
        y_f = kv_f * kf_f[None, :, :, :]
        y = (lambda x, s, axes=(-2, -1): torch.fft.irfft2(x, s=s, dim=axes).to(torch.bfloat16))(y_f, (1, L), axes=(1, 2))[:, 0, :S, :]
        y = y + kv * ssm_D[None, None, :]
        return q * y

    def _B_ref(x, mem0, Wq, Wk, Wv, Wo, seg_len):
        B, S, Dm = x.shape
        H, Dh = (Wq.shape[1], Wq.shape[2])
        M = mem0.shape[1]
        scale = Dh ** (-0.5)
        n_seg = S // seg_len
        L = M + seg_len
        idx = torch.arange(L)
        mask = (idx[:, None] < M) | (idx[:, None] >= idx[None, :])
        mem = mem0
        outs = []
        for s in range(n_seg):
            xs = x[:, s * seg_len:(s + 1) * seg_len, :].clone()
            h = torch.concatenate([mem, xs], axis=1)
            q = torch.einsum('btd,dhe->bhte', h, Wq)
            k = torch.einsum('btd,dhe->bhte', h, Wk)
            v = torch.einsum('btd,dhe->bhte', h, Wv)
            scores = torch.einsum('bhqe,bhke->bhqk', q, k) * scale
            scores = torch.where(mask[None, None], scores, -1000000000.0)
            p = _softmax(scores, axis=-1)
            o = torch.einsum('bhqk,bhke->bhqe', p, v)
            o = torch.einsum('bhte,hed->btd', o, Wo)
            mem = o[:, :M, :].clone()
            outs.append(o[:, M:, :])
        return torch.concatenate(outs, axis=1)
    a = _A_ref(A_q, A_k, A_v, A_shift_k, A_ssm_k, A_ssm_D)
    b = _B_ref(B_x, B_mem0, B_Wq, B_Wk, B_Wv, B_Wo, B_seg_len)
    return torch.concatenate([a, b], axis=-1)
```

```cpp
#include <hip/hip_runtime.h>
#include <hip/hip_bf16.h>
#include <stdint.h>
#include <type_traits>

constexpr int S_LEN = 2048;
constexpr int D_A   = 1024;
constexpr int DM    = 1024;
constexpr int NH    = 32;
constexpr int DH    = 128;
constexpr int HD    = NH * DH;
constexpr int MEMR  = 8;
constexpr int SEG   = 32;
constexpr int NSEG  = S_LEN / SEG;
constexpr int OUTW  = 2048;

typedef unsigned short u16;
typedef unsigned long long u64;
typedef short bf16x8 __attribute__((ext_vector_type(8)));
typedef short bf16x4 __attribute__((ext_vector_type(4)));
typedef float f32x4 __attribute__((ext_vector_type(4)));
typedef float f32x16 __attribute__((ext_vector_type(16)));
typedef unsigned u32x4 __attribute__((ext_vector_type(4)));
typedef unsigned u32x2 __attribute__((ext_vector_type(2)));
#define LAS __attribute__((address_space(3)))
#define GAS __attribute__((address_space(1)))

constexpr size_t MB = 1u << 20;
constexpr size_t OFF_P   = 193 * MB;
constexpr size_t OFF_KM  = 198 * MB;
constexpr size_t OFF_KVT = 202 * MB;
constexpr size_t OFF_QX  = 206 * MB;
constexpr size_t OFF_KX  = 222 * MB;
constexpr size_t OFF_VX  = 238 * MB;
constexpr size_t PROF_AT = 0;

__device__ __forceinline__ float bf2f(u16 b) { return __uint_as_float(((unsigned)b) << 16); }
__device__ __forceinline__ u16 f2bf(float f) { __bf16 b = (__bf16)f; return __builtin_bit_cast(u16, b); }
__device__ __forceinline__ float rbf(float f) { return bf2f(f2bf(f)); }
__device__ __forceinline__ unsigned pk2(float a, float b) {
  typedef float f2 __attribute__((ext_vector_type(2)));
  typedef __bf16 b2 __attribute__((ext_vector_type(2)));
  f2 v = {a, b}; b2 r = __builtin_convertvector(v, b2); return __builtin_bit_cast(unsigned, r);
}

__device__ __forceinline__ int opaque_tid(int wv) {
  int l = __builtin_amdgcn_mbcnt_hi(~0u, __builtin_amdgcn_mbcnt_lo(~0u, 0u));
  asm volatile("" : "+v"(l));
  return wv * 64 + l;
}
__device__ __forceinline__ int swz_b(int row) { return ((row & 3) << 2) | ((row >> 2) & 3); }

#define KNOB(name, v) constexpr int name = v
#define EPS_M 330
KNOB(K_DEF, 98);
#define TR_W 2400
KNOB(W_STEP, 33); KNOB(W_OFF, 56);
KNOB(RHO, 3);
#define CALN 2
#define SEG_R 52
#define SEGL 60
#define XL_D 3
#define GROT (BM == 128 ? ((brow >> 7) & 1) * 16 : ((brow >> 8) & 3) * 8)
#define PU_NT 2
#define FILL0 4
#define ROT1 10
#define NSMAX 6
#define PFN 10
#define U_SIG2 9
#define X_RDY 40u
#define STEAL 8
#define G_NA 44u
#define G_CH 42
#define RROT (kind * 8)
#define U_NOW 1
#define WRONG 0
#define JOFF 0x08000c0410000102ull
#define KMASK 31
#define PX_NT 0
#define XA_NT 0
#define FILLN 1
#define OUTP "nt"
#define LT_R 3
#define Q_SLP 32
#define X_AH 1
#define GSEQ "v2Q0k2v3V2k3K2V3K3Q1Q2V4K4V5K5Q3Q4V6K6V7K7Q5Q6Q7"
#define LDNT(p) __builtin_nontemporal_load(p)
constexpr int CHK      = 16384;
constexpr int PAGE     = 2 << 20;
constexpr int SY_UM    = 0;
constexpr int SY_HOT   = 32 * PAGE;
constexpr int H_MEM    = 0;
constexpr int H_P      = 16 * CHK;
constexpr int H_ANN    = 28 * CHK;
constexpr int H_DLT    = H_P + 2 * CHK + 4096;
constexpr int H_TR     = H_P + 5 * CHK + 4096;
constexpr int H_BYTES  = 32 * CHK;
#define URING 32
constexpr int SY_SET   = 96 * PAGE;
constexpr int SY_EPOCH = SY_SET + 128;
constexpr int SY_ABORT = SY_SET + 256;
constexpr int SY_SEL   = SY_ABORT + 128;
constexpr int SY_CT    = SY_ABORT + 640;
constexpr int CT_QK = 24, CT_A1 = 40, CT_A2 = 56, CT_P = 200, CT_PD = 96, CT_ARR = 84, CT_INIT = 267, CT_QUEUE = 268, CT_K = 308, CT_V = CT_K + 32, CT_FL = CT_V + 32;
constexpr int CT_CAL = CT_FL + 20 * 16;
constexpr int CT_U = CT_CAL + 96, CT_TK = CT_U + 64;
constexpr int SY_BYTES = SY_CT + (CT_TK + 128) * 128;
static_assert(SY_BYTES <= 193 << 20, "hand-off buffers");
constexpr unsigned CT_U_NEED = 32;
constexpr int P_SEG = 81920;
#define SEGF 48
constexpr int SEG_FINE = SEGF, NLATE = NSEG - SEG_FINE;
constexpr unsigned SENT = 0xFFFFFFFFu;
constexpr unsigned SPIN_CAP = 1u << 19;
constexpr int NLF = 16;
constexpr int L_WTS = 0;
constexpr int L_MEM = L_WTS + 4 * NLF * 2048;
constexpr int L_XCH = L_MEM + 16384;
constexpr int L_XK  = L_XCH + 2048;
constexpr int L_KX  = L_XCH + 4096;
constexpr int L_PRF = L_KX + 8192;
constexpr int XRS = 40;
constexpr int CHAIN_LDS = 160 * 1024;
static_assert(L_XCH + 128 * XRS <= L_PRF && L_PRF + 4096 <= CHAIN_LDS, "lds");

typedef __bf16 bfv8 __attribute__((ext_vector_type(8)));
typedef __bf16 bfv16 __attribute__((ext_vector_type(16)));
typedef unsigned u32x8 __attribute__((ext_vector_type(8)));
typedef unsigned u32x12 __attribute__((ext_vector_type(12)));
typedef unsigned u32x16 __attribute__((ext_vector_type(16)));
typedef unsigned u32x32 __attribute__((ext_vector_type(32)));
struct Wts { u32x8 a[32]; u32x32 v[4]; };

#define STAMPL(i) do {} while (0)
#define PVAL(i, v) do {} while (0)
#define PDECL(base, str) do {} while (0)
#define PZERO do {} while (0)
#define PDUMP(mask) do {} while (0)
#define TA(x) do {} while (0)
#define TB(x) TS(x)
#define TS(x) do {} while (0)
#define SSTAMP(i) do {} while (0)
struct ChainArgs {
  u64* prof;
  const u16* mem0; const u16* Wq; const u16* Wk; const u16* Wv; const u16* Wo; const u16* X;
  u16* Qx; u16* Kx; u16* Vx; u16* Km; u16* P; u16* out; char* sync;
  const u16* Aq; const u16* Ak; const u16* Av; const u16* Ashift; const u16* Assm; const u16* AD; u16* kvT;
};
__device__ __forceinline__ GAS unsigned* ctr(const ChainArgs& a, int i) { return (GAS unsigned*)(a.sync + SY_CT + i * 128); }
__device__ __forceinline__ void ctr_add(const ChainArgs& a, int i, unsigned v) {
  __hip_atomic_fetch_add(ctr(a, i), v, __ATOMIC_RELAXED, __HIP_MEMORY_SCOPE_AGENT);
}

__device__ __forceinline__ __amdgpu_buffer_rsrc_t mk_rsrc(const void* p, unsigned bytes) {
  const u64 v = (u64)p;
  const unsigned lo = (unsigned)__builtin_amdgcn_readfirstlane((unsigned)v), hi = (unsigned)__builtin_amdgcn_readfirstlane((unsigned)(v >> 32));
  const u64 u = (u64)lo | ((u64)hi << 32);
  return __builtin_amdgcn_make_buffer_rsrc((void*)u, 0, bytes, 0x00020000);
}
__device__ __forceinline__ u32x4 own_rsrc(const void* p, unsigned bytes) {
  u32x4 r = {(unsigned)__builtin_amdgcn_readfirstlane((unsigned)(u64)p), (unsigned)__builtin_amdgcn_readfirstlane((unsigned)((u64)p >> 32)) & 0xffffu, bytes, 0x00020000u};
  asm volatile("s_nop 4" : "+s"(r));
  return r;
}
constexpr int LD_AUX = 16, ST_AUX = 16;
__device__ __forceinline__ u32x4 ld16(__amdgpu_buffer_rsrc_t rs, unsigned off) {
  return __builtin_amdgcn_raw_buffer_load_b128(rs, off, 0, LD_AUX);
}
__device__ __forceinline__ void st16(__amdgpu_buffer_rsrc_t rs, unsigned off, u32x4 v) {
  __builtin_amdgcn_raw_buffer_store_b128(v, rs, off, 0, ST_AUX);
}
__device__ __forceinline__ void st16s(u32x4 v, __amdgpu_buffer_rsrc_t rs, unsigned voff, unsigned soff) {
  __builtin_amdgcn_raw_buffer_store_b128(v, rs, voff, soff, ST_AUX);
  asm volatile("s_nop 1" :: "v"(v) : "memory");
}
__device__ __forceinline__ void st8(__amdgpu_buffer_rsrc_t rs, unsigned off, u32x2 v) {
  __builtin_amdgcn_raw_buffer_store_b64(v, rs, off, 0, ST_AUX);
}
__device__ __forceinline__ void st4(__amdgpu_buffer_rsrc_t rs, unsigned off, unsigned v) {
  __builtin_amdgcn_raw_buffer_store_b32(v, rs, off, 0, ST_AUX);
}
__device__ __forceinline__ unsigned ld4(__amdgpu_buffer_rsrc_t rs, unsigned off) {
  return __builtin_amdgcn_raw_buffer_load_b32(rs, off, 0, LD_AUX);
}
constexpr int TU = 16;
__device__ __forceinline__ unsigned cyc32() { return (unsigned)__builtin_amdgcn_s_memtime(); }
__device__ __forceinline__ unsigned to_sgpr(unsigned x) {
  unsigned r;
  asm volatile("s_nop 0\n\tv_readfirstlane_b32 %0, %1\n\ts_nop 4" : "=s"(r) : "v"(x));
  return r;
}
struct Clk {
  unsigned ce, te;
  unsigned c2t, t2c;
  __device__ __forceinline__ void edge(unsigned& c, unsigned& t) {
    u64 c0, r0, r1, r2, r3, r4, r5;
    asm volatile("s_memtime %0\n\ts_memrealtime %1\n\ts_nop 4\n\ts_memrealtime %2\n\ts_nop 4\n\ts_memrealtime %3\n\ts_nop 4\n\t"
                 "s_memrealtime %4\n\ts_nop 4\n\ts_memrealtime %5\n\ts_nop 4\n\ts_memrealtime %6\n\ts_waitcnt lgkmcnt(0)"
                 : "=&s"(c0), "=&s"(r0), "=&s"(r1), "=&s"(r2), "=&s"(r3), "=&s"(r4), "=&s"(r5) :: "memory");
    const unsigned x = (unsigned)r0;
    const unsigned n = ((unsigned)r1 == x) + ((unsigned)r2 == x) + ((unsigned)r3 == x) + ((unsigned)r4 == x) + ((unsigned)r5 == x);
    c = to_sgpr((unsigned)c0 + 24u * n); t = (x + 4u) * TU;
  }
  __device__ __forceinline__ void init() { edge(ce, te); c2t = 44000u; t2c = 97600u; }
  __device__ __forceinline__ void sync() {
    unsigned c, t;
    edge(c, t);
    set(c, t);
  }
  __device__ __forceinline__ void set(unsigned c, unsigned t) {
    if (c == ce) return;
    const float q = (float)(t - te) * __builtin_amdgcn_rcpf((float)(c - ce));
    const float qc = fminf(fmaxf(q, 0.51f), 1.99f);
    c2t = to_sgpr((unsigned)(qc * 65536.f));
    t2c = to_sgpr((unsigned)(__builtin_amdgcn_rcpf(qc) * 65536.f));
    ce = c; te = t;
  }
  __device__ __forceinline__ unsigned time(unsigned c) const { return te + ((min(c - ce, 30000u) * c2t) >> 16); }
  __device__ __forceinline__ unsigned now() const { return time(cyc32()); }
  __device__ __forceinline__ unsigned target(unsigned t) const {
    return to_sgpr(ce + (((unsigned)min(max((int)(t - te), 0), 20000) * t2c) >> 16));
  }
  __device__ __forceinline__ void wait(unsigned t) const { wait_c(target(t)); }
  __device__ __forceinline__ void wait_c(unsigned c) const {
    unsigned y;
    asm volatile("1:\n\ts_memtime vcc\n\ts_waitcnt lgkmcnt(0)\n\ts_sub_i32 %0, %1, vcc_lo\n\ts_cmp_gt_i32 %0, 128\n\ts_cbranch_scc1 1b\n\t"
                 "2:\n\ts_sub_i32 %0, %0, %2\n\ts_cmp_gt_i32 %0, %3\n\ts_cbranch_scc1 2b"
                 : "=&s"(y) : "s"(c), "n"(W_STEP), "n"(W_OFF) : "memory", "scc", "vcc");
  }
};
__device__ __forceinline__ unsigned nosent(unsigned d) { return min(d, SENT - 1u); }
__device__ __forceinline__ int arow(int row, int half) { return 4 * (row >> 1) + 2 * half + (row & 1); }
__device__ __forceinline__ unsigned umax4(u32x4 v) { return max(max(v[0], v[1]), max(v[2], v[3])); }
__device__ __forceinline__ void stamp_again(GAS u64* p) {
  __hip_atomic_store(p, (__builtin_amdgcn_s_memrealtime() & ~7ull) | (__hip_atomic_load(p, __ATOMIC_RELAXED, __HIP_MEMORY_SCOPE_AGENT) & 7ull),
                     __ATOMIC_RELAXED, __HIP_MEMORY_SCOPE_AGENT);
}
__device__ __forceinline__ bool poll_fail(unsigned& spins, GAS unsigned* abortw, int lane) {
  ++spins;
  if (spins > SPIN_CAP) {
    if (lane == 0) __hip_atomic_store(abortw, 1u, __ATOMIC_RELAXED, __HIP_MEMORY_SCOPE_AGENT);
    return true;
  }
  if ((spins & 511u) == 0u && lane == 0) {
    unsigned n = 0;
    for (int i = 0; i < 8; ++i) n += __hip_atomic_load(abortw + (SY_CT - SY_ABORT + (CT_ARR + i) * 128) / 4, __ATOMIC_RELAXED, __HIP_MEMORY_SCOPE_AGENT) + 1u;
    if (n < 256u) stamp_again((GAS u64*)(abortw - 32));
  }
  if ((spins & 63u) == 0u)
    return __builtin_amdgcn_readfirstlane(__hip_atomic_load(abortw, __ATOMIC_RELAXED, __HIP_MEMORY_SCOPE_AGENT)) != SENT;
  return false;
}
struct Gate {
  u64 entry; int arr; unsigned rot; bool open;
  __device__ __forceinline__ bool look(char* sync) {
    if (open) return true;
    u64 v;
    for (unsigned spins = 1;; ++spins) {
      v = __hip_atomic_load((GAS u64*)(sync + SY_EPOCH), __ATOMIC_RELAXED, __HIP_MEMORY_SCOPE_AGENT);
      v = (u64)(unsigned)__builtin_amdgcn_readfirstlane((unsigned)v) | ((u64)(unsigned)__builtin_amdgcn_readfirstlane((unsigned)(v >> 32)) << 32);
      if (v - entry + 2000u < (1ull << 31) || spins > 4 * SPIN_CAP) break;
      __builtin_amdgcn_s_sleep(8);
    }
    asm volatile("" ::: "memory");
    const bool same = rot == ((unsigned)v & 7u);
    rot = (unsigned)v & 7u; open = true;
    return same;
  }
  __device__ __forceinline__ void arrive(char* sync) {
    if (arr >= 0 && opaque_tid(0) == 0) __hip_atomic_fetch_add((GAS unsigned*)(sync + SY_CT + (CT_ARR + arr) * 128), 1u, __ATOMIC_RELAXED, __HIP_MEMORY_SCOPE_AGENT);
    arr = -1;
  }
  __device__ __forceinline__ bool pass(char* sync) { const bool r = look(sync); arrive(sync); return r; }
};
__device__ __forceinline__ unsigned ann_val(unsigned t, int s) { return (t << 8) | (unsigned)s; }
__device__ __forceinline__ unsigned tr_place(int s) { return (s & 2) * (3 * CHK / 2) + (s & 1) * 2048; }
__device__ __forceinline__ unsigned die_half() { return ((unsigned)__builtin_amdgcn_s_getreg(20 | (3 << 11)) >> 2) & 1u; }
typedef unsigned Sel __attribute__((ext_vector_type(3)));
__device__ __forceinline__ Sel sel_wait(char* sync, GAS unsigned* abortw, int lane, bool& dead, int ci = -1) {
  unsigned spins = 0;
  u64 v;
  const bool m = lane < 3 || ci < 0;
  const int off = m ? SY_SEL + min(lane, 2) * 8 : SY_CT + (lane == 3 ? CT_INIT : ci) * 128;
  while (true) {
    v = __hip_atomic_load((GAS u64*)(sync + off), __ATOMIC_RELAXED, __HIP_MEMORY_SCOPE_AGENT);
    if (__all(m ? (v >> 63) == 0 : (unsigned)v + 1u >= (lane == 3 ? 32u : 2u))) break;
    __builtin_amdgcn_s_sleep(4);
    if (dead || poll_fail(spins, abortw, lane)) { dead = true; v = 0; break; }
  }
  return (Sel){(unsigned)__builtin_amdgcn_readlane((unsigned)v, 0), (unsigned)__builtin_amdgcn_readlane((unsigned)v, 1), (unsigned)__builtin_amdgcn_readlane((unsigned)v, 2)};
}
__device__ __forceinline__ __amdgpu_buffer_rsrc_t rsrc_u(char* sync) { return mk_rsrc((void*)sync, SY_SET); }
__device__ __forceinline__ unsigned um_slot(int s, Sel sel) { return (unsigned)s * (64 * CHK) + ((sel[0] >> (s >> 1)) & 1u) * 8192; }
constexpr unsigned UXH = 8 * CHK;
__device__ __forceinline__ unsigned ux_base(int s, int h, Sel sel) {
  if (s >= URING) s -= URING;
  if (URING < 32 && s >= URING) s -= URING;
  return (s * 32 + h) * UXH + ((~(s < 16 ? sel[0] : (s < 32 ? sel[1] : sel[2])) >> (2 * (s & 15) + (h >> 4))) & 1u) * 8192;
}
__device__ __forceinline__ constexpr unsigned ux_tile(int t) { return (t >> 3) * CHK + (t & 7) * 1024; }
__device__ __forceinline__ __amdgpu_buffer_rsrc_t rsrc_hot(char* sync, Sel sel) {
  return mk_rsrc((void*)(sync + SY_HOT + (sel[1] & 1u) * 8192), H_BYTES - 8192);
}
template <int SLP = 4>
__device__ __forceinline__ void ctr_wait(GAS unsigned* c, unsigned need, GAS unsigned* abortw, int lane, bool& dead) {
  unsigned spins = 0;
  while (true) {
    unsigned v = __builtin_amdgcn_readfirstlane(__hip_atomic_load(c, __ATOMIC_RELAXED, __HIP_MEMORY_SCOPE_AGENT)) + 1u;
    if (v >= need) break;
    __builtin_amdgcn_s_sleep(SLP);
    if (dead || poll_fail(spins, abortw, lane)) { dead = true; break; }
  }
}
template <int N> __device__ __forceinline__ void wait_vm() { asm volatile("s_waitcnt vmcnt(%0)" :: "n"(N) : "memory"); }
__device__ __forceinline__ void lds_barrier() {
  asm volatile("s_waitcnt lgkmcnt(0)" ::: "memory");
  __builtin_amdgcn_s_barrier();
  asm volatile("" ::: "memory");
}
__device__ __forceinline__ f32x4 vzero() { f32x4 z = {0.f, 0.f, 0.f, 0.f}; asm volatile("" : "+v"(z)); return z; }
__device__ __forceinline__ f32x4 smf(u32x4 a, u32x8 b, f32x4 c, int idx) {
  return __builtin_amdgcn_smfmac_f32_16x16x64_bf16(__builtin_bit_cast(bfv8, a), __builtin_bit_cast(bfv16, b), c, idx, 0, 0);
}
__device__ __forceinline__ float red16max(float x) { auto r = __builtin_amdgcn_permlane16_swap(__float_as_uint(x), __float_as_uint(x), false, false); return fmaxf(__uint_as_float(r[0]), __uint_as_float(r[1])); }
__device__ __forceinline__ float red32max(float x) { auto r = __builtin_amdgcn_permlane32_swap(__float_as_uint(x), __float_as_uint(x), false, false); return fmaxf(__uint_as_float(r[0]), __uint_as_float(r[1])); }
__device__ __forceinline__ float red16add(float x) { auto r = __builtin_amdgcn_permlane16_swap(__float_as_uint(x), __float_as_uint(x), false, false); return __uint_as_float(r[0]) + __uint_as_float(r[1]); }
__device__ __forceinline__ float red32add(float x) { auto r = __builtin_amdgcn_permlane32_swap(__float_as_uint(x), __float_as_uint(x), false, false); return __uint_as_float(r[0]) + __uint_as_float(r[1]); }
constexpr bool blk_in_lds(int R, int b) {
  return R == 0 ? ((b & 7) == 1 || (b & 7) == 4) : (b < 16 ? (b & 3) == 0 : (b & 3) == 3);
}
constexpr int lds_index(int R, int b) { int n = 0; for (int i = 0; i < b; ++i) if (blk_in_lds(R, i)) ++n; return n; }
constexpr bool frag_in_lds(int R, int f) { return blk_in_lds(R, f >> 1); }
constexpr int frag_reg_slot(int R, int f) { int n = 0; for (int i = 0; i < f; ++i) if (!frag_in_lds(R, i)) ++n; return n; }

struct WSched { int ord[32]; int slot[32]; int issue[32]; int out[32]; int reg[32]; };
constexpr WSched make_wsched(int R) {
  WSched S{};
  int n = 0;
  for (int b = 0; b < 32; ++b) if (!blk_in_lds(R, b)) S.ord[n++] = b;
  for (int i = 0; i < n; ++i) S.reg[i] = frag_reg_slot(R, 2 * S.ord[i]);
  const int npure = n;
  for (int b = 0; b < 32; ++b) if (blk_in_lds(R, b)) S.ord[n++] = b;
  int seq[32] = {};
  int nissued = 0, next = 0;
  for (int i = 0; i < 10; ++i) { S.slot[i] = (i + 10 - npure % 10) % 10; S.issue[i] = -1; seq[i] = nissued++; }
  next = 10;
  S.out[0] = nissued - 1;
  for (int c = 0; c <= npure; ++c) {
    if (c >= 1) {
      const int k = S.slot[c - 1];
      if (next < npure) { S.slot[next] = k; S.issue[next] = c; seq[next] = nissued++; ++next; }
      else if (k < NLF / 2) { S.slot[npure + k] = k; S.issue[npure + k] = c; seq[npure + k] = nissued++; }
    }
    if (c + 1 < npure) S.out[c + 1] = nissued - 1 - seq[c + 1];
  }
  return S;
}
__device__ __forceinline__ u32x8 tr_frag(const char* sb, int tile, int lane) {
  u32x8 f;
#pragma unroll
  for (int q = 0; q < 4; ++q) {
    int row = (q >> 1) * 32 + 8 * (lane >> 4) + 4 * (q & 1) + ((lane & 15) >> 2);
    int p = lane & 3;
    int lc = 2 * tile + (p >> 1);
    int addr = row * 256 + ((lc ^ swz_b(row)) * 16) + 8 * (p & 1);
    bf16x4 v = __builtin_amdgcn_ds_read_tr16_b64_v4i16((LAS bf16x4*)(sb + addr));
    u32x2 u = __builtin_bit_cast(u32x2, v);
    f[2 * q] = u[0]; f[2 * q + 1] = u[1];
  }
  return f;
}
__device__ __forceinline__ void dma16(__amdgpu_buffer_rsrc_t rs, unsigned voff, unsigned soff, unsigned lds, bool nt = false, bool pad = true) {
  if (nt) asm volatile("s_mov_b32 m0, %0\n\ts_nop 3\n\tbuffer_load_dwordx4 %1, %2, %3 offen nt lds" :: "s"(lds), "v"(voff), "s"(rs), "s"(soff) : "m0", "memory");
  else if (pad) asm volatile("s_mov_b32 m0, %0\n\ts_nop 3\n\tbuffer_load_dwordx4 %1, %2, %3 offen lds" :: "s"(lds), "v"(voff), "s"(rs), "s"(soff) : "m0", "memory");
  else asm volatile("s_mov_b32 m0, %0\n\ts_nop 0\n\tbuffer_load_dwordx4 %1, %2, %3 offen lds" :: "s"(lds), "v"(voff), "s"(rs), "s"(soff) : "m0", "memory");
}
__device__ __forceinline__ u32x8 tr_frag2(const char* sb, int tile, int f, int lane) {
  u32x8 r;
#pragma unroll
  for (int c = 0; c < 2; ++c)
#pragma unroll
    for (int t = 0; t < 2; ++t) {
      const int row = 32 * c + 16 * f + 4 * (lane >> 4) + ((lane & 15) >> 2);
      const int p = lane & 3, lc = 2 * (tile + t) + (p >> 1);
      const u32x2 u = __builtin_bit_cast(u32x2, __builtin_amdgcn_ds_read_tr16_b64_v4i16((LAS bf16x4*)(sb + row * 256 + ((lc ^ swz_b(row)) * 16) + 8 * (p & 1))));
      r[4 * c + t] = u[0]; r[4 * c + 2 + t] = u[1];
    }
  return r;
}
template <int ROLE>
__device__ __forceinline__ void load_weights(const ChainArgs& a, int h, int w, int lane, char* smem, Wts& W) {
  u32x8 F[4];
#pragma unroll
  for (int i = 0; i < 32; ++i) asm volatile("" : "=a"(W.a[i]));
  constexpr WSched S = make_wsched(ROLE);
  constexpr int NP = 32 - NLF / 2;
  const __amdgpu_buffer_rsrc_t r0 = mk_rsrc(ROLE == 0 ? a.Wq : a.Wv, 8u << 20), r1 = mk_rsrc(ROLE == 0 ? a.Wk : a.Wo, 8u << 20);
  const unsigned lds0 = (unsigned)(uintptr_t)(LAS char*)smem;
  unsigned vo[2][4];
#pragma unroll
  for (int j = 0; j < 4; ++j) {
    const int row = (w * 4 + j) * 4 + (lane >> 4), lc = (lane & 15) ^ swz_b(row);
    vo[0][j] = row * HD * 2 + lc * 16; vo[1][j] = row * DM * 2 + lc * 16;
  }
  asm volatile("s_nop 4");
  auto stage = [&](int i) {
    const int blk = S.ord[i];
    const bool second = ROLE == 0 ? (blk & 1) : blk >= 16, wo = ROLE == 1 && second;
    const unsigned so = wo ? (unsigned)(((128 * h + 64 * (blk & 1)) * DM + 128 * (((blk - 16) >> 1) ^ (2 * (h & 3)))) * 2)
                           : (unsigned)((64 * ((ROLE == 0 ? blk >> 1 : blk) ^ (h & RHO)) * HD + 128 * h) * 2);
#pragma unroll
    for (int j = 0; j < 4; ++j) dma16(second ? r1 : r0, vo[wo][j], so, lds0 + 16384 * S.slot[i] + (w * 4 + j) * 1024, wo);
  };
#pragma unroll
  for (int i = 0; i < 32; ++i) if (S.issue[i] == -1) stage(i);
#pragma unroll
  for (int c = 0; c < NP; ++c) {
    asm volatile("s_waitcnt vmcnt(%0)" :: "n"(4 * S.out[c]) : "memory");
    lds_barrier();
#pragma unroll
    for (int i = 0; i < 32; ++i) if (S.issue[i] == c) stage(i);
    const char* sb = smem + 16384 * S.slot[c];
#pragma unroll
    for (int t = 0; t < 2; ++t) {
      u32x8 f = (ROLE == 1 && S.ord[c] >= 16) ? tr_frag2(sb, 2 * w, t, lane) : tr_frag(sb, 2 * w + t, lane);
      if (S.reg[c] < 32) {
#pragma unroll
        for (int i = 0; i < 8; ++i) asm volatile("v_accvgpr_write_b32 a[%1], %0" :: "v"(f[i]), "n"(8 * ((S.reg[c] + t) & 31) + i));
      }
      F[(S.reg[c] + t) & 3] = f;
    }
    if (S.reg[c] >= 32 && (S.reg[c] & 2)) {
      const u32x16 lo = __builtin_shufflevector(F[0], F[1], 0, 1, 2, 3, 4, 5, 6, 7, 8, 9, 10, 11, 12, 13, 14, 15);
      const u32x16 hi = __builtin_shufflevector(F[2], F[3], 0, 1, 2, 3, 4, 5, 6, 7, 8, 9, 10, 11, 12, 13, 14, 15);
      u32x32 x = __builtin_shufflevector(lo, hi, 0, 1, 2, 3, 4, 5, 6, 7, 8, 9, 10, 11, 12, 13, 14, 15, 16, 17, 18, 19, 20, 21, 22, 23, 24, 25, 26, 27, 28, 29, 30, 31);
      asm volatile("" : "+v"(x));
      W.v[(S.reg[c] >> 2) & 3] = x;
    }
  }
  static_assert(S.issue[31] < NP && S.issue[NP] < S.issue[31], "resident blocks are issued in slot order inside the loop");
#pragma unroll
  for (int j = 0; j < NLF / 2; ++j) {
    asm volatile("s_waitcnt vmcnt(%0)" :: "n"(4 * (NLF / 2 - 1 - j)) : "memory");
    lds_barrier();
    const bool two = ROLE == 1 && S.ord[NP + j] >= 16;
    u32x8 f0 = two ? tr_frag2(smem + 16384 * j, 2 * w, 0, lane) : tr_frag(smem + 16384 * j, 2 * w, lane);
    u32x8 f1 = two ? tr_frag2(smem + 16384 * j, 2 * w, 1, lane) : tr_frag(smem + 16384 * j, 2 * w + 1, lane);
    lds_barrier();
    char* d = smem + 16384 * j + w * 4096 + lane * 16;
    *(u32x4*)d = (u32x4){f0[0], f0[1], f0[2], f0[3]};
    *(u32x4*)(d + 1024) = (u32x4){f0[4], f0[5], f0[6], f0[7]};
    *(u32x4*)(d + 2048) = (u32x4){f1[0], f1[1], f1[2], f1[3]};
    *(u32x4*)(d + 3072) = (u32x4){f1[4], f1[5], f1[6], f1[7]};
  }
  lds_barrier();
}
template <int... Is> struct iseq {};
template <int N, int... Is> struct mkseq : mkseq<N - 1, N - 1, Is...> {};
template <int... Is> struct mkseq<0, Is...> { typedef iseq<Is...> type; };

#define SB0 __builtin_amdgcn_sched_barrier(0)
#define CAT8(x, y) ((u32x8){x[0], x[1], x[2], x[3], y[0], y[1], y[2], y[3]})
template <class F, int... I>
__device__ __forceinline__ void for_slots(iseq<I...>, F f) { (f(std::integral_constant<int, I>{}), ...); }
#define QK_BODY \
  "s_cmp_eq_u32 %[wv], 1\n\t" \
  "s_cbranch_scc1 91f\n\t" \
  "s_cmp_eq_u32 %[wv], 2\n\t" \
  "s_cbranch_scc1 92f\n\t" \
  "s_cmp_eq_u32 %[wv], 3\n\t" \
  "s_cbranch_scc1 93f\n\t" \
  "90:\n\t" \
  "s_mov_b32 s58, s42\n\t" \
  "20:\n\t" \
  "s_and_b32 s76, s44, 7\n\t" \
  "s_lshl_b32 s76, s76, 15\n\t" \
  "s_add_i32 s40, s54, s76\n\t" \
  "s_and_b32 s76, s44, 3\n\t" \
  "s_lshl_b32 s45, s76, 14\n\t" \
  "s_mul_i32 s47, s76, 0xc000\n\t" \
  "s_add_i32 s47, s47, 0x40000\n\t" \
  "s_add_i32 s49, s44, -1\n\t" \
  "s_and_b32 s48, s49, 2\n\t" \
  "s_mul_i32 s48, s48, 0x6000\n\t" \
  "s_and_b32 s76, s49, 1\n\t" \
  "s_lshl_b32 s76, s76, 11\n\t" \
  "s_add_i32 s48, s48, s76\n\t" \
  "s_lshl_b32 s50, s53, 8\n\t" \
  "s_mov_b32 s59, 0\n\t" \
  "s_mov_b32 s92, s51\n\t" \
  "s_mov_b32 s93, s53\n\t" \
  "s_xor_b32 s76, s40, 0x400\n\t" \
  "s_xor_b32 s77, s40, 0x800\n\t" \
  "s_xor_b32 s78, s40, 0xc00\n\t" \
  "s_cmp_lg_u32 s58, 0\n\t" \
  "s_cbranch_scc1 11f\n\t" \
  "10:\n\t" \
  "ds_read_b128 v[48:51], v102 offset:0\n\t" \
  "ds_read_b128 v[52:55], v102 offset:1024\n\t" \
  "ds_read_b128 v[56:59], v102 offset:2048\n\t" \
  "ds_read_b128 v[60:63], v102 offset:3072\n\t" \
  "ds_read_b128 v[16:19], v102 offset:16384\n\t" \
  "ds_read_b128 v[20:23], v102 offset:17408\n\t" \
  "ds_read_b128 v[24:27], v102 offset:18432\n\t" \
  "ds_read_b128 v[28:31], v102 offset:19456\n\t" \
  "ds_read_b128 v[84:87], v102 offset:49152\n\t" \
  "ds_read_b128 v[88:91], v102 offset:50176\n\t" \
  "ds_read_b128 v[92:95], v102 offset:51200\n\t" \
  "ds_read_b128 v[96:99], v102 offset:52224\n\t" \
  "s_waitcnt lgkmcnt(4)\n\t" \
  "ds_read_b128 v[0:3], v103 offset:16384\n\t" \
  "ds_read_b128 v[4:7], v103 offset:17408\n\t" \
  "ds_read_b128 v[8:11], v103 offset:18432\n\t" \
  "ds_read_b128 v[12:15], v103 offset:19456\n\t" \
  "s_bitcmp1_b32 s58, 1\n\t" \
  "s_cselect_b64 s[80:81], -1, 0\n\t" \
  "s_cmp_lg_u32 s58, 0\n\t" \
  "s_cbranch_scc1 17f\n\t" \
  "s_mov_b64 exec, 0xffff\n\t" \
  "16:\n\t" \
  "s_memtime vcc\n\t" \
  "s_waitcnt lgkmcnt(0)\n\t" \
  "s_sub_i32 s79, s41, vcc_lo\n\t" \
  "s_cmp_gt_i32 s79, 64\n\t" \
  "s_cbranch_scc1 16b\n\t" \
  "13:\n\t" \
  "buffer_load_dword v116, v112, %[rh], s48 offen sc1\n\t" \
  "s_waitcnt vmcnt(0)\n\t" \
  "v_mov_b32 v117, 20000\n\t" \
  "v_cmp_ne_u32_sdwa vcc, v116, s49 src0_sel:BYTE_0 src1_sel:DWORD\n\t" \
  "v_subrev_u32 v116, s50, v116\n\t" \
  "v_ashrrev_i32 v116, 8, v116\n\t" \
  "s_nop 1\n\t" \
  "v_max_i32_dpp v116, v116, v116 quad_perm:[1,0,3,2] row_mask:0xf bank_mask:0xf\n\t" \
  "s_cbranch_vccnz 14f\n\t" \
  "s_nop 0\n\t" \
  "v_max_i32_dpp v116, v116, v116 quad_perm:[2,3,0,1] row_mask:0xf bank_mask:0xf\n\t" \
  "s_nop 1\n\t" \
  "v_max_i32_dpp v116, v116, v116 row_half_mirror row_mask:0xf bank_mask:0xf\n\t" \
  "s_nop 1\n\t" \
  "v_max_i32_dpp v116, v116, v116 row_mirror row_mask:0xf bank_mask:0xf\n\t" \
  "v_med3_i32 v116, v116, 0, v117\n\t" \
  "v_mul_u32_u24 v116, s52, v116\n\t" \
  "v_lshrrev_b32 v116, 16, v116\n\t" \
  "s_mov_b64 exec, -1\n\t" \
  "v_readfirstlane_b32 s84, v116\n\t" \
  "s_add_i32 s84, s84, s51\n\t" \
  "1:\n\t" \
  "s_memtime vcc\n\t" \
  "s_waitcnt lgkmcnt(0)\n\t" \
  "s_sub_i32 s79, s84, vcc_lo\n\t" \
  "s_cmp_gt_i32 s79, 128\n\t" \
  "s_cbranch_scc1 1b\n\t" \
  "2:\n\t" \
  "s_sub_i32 s79, s79, 33\n\t" \
  "s_cmp_gt_i32 s79, 56\n\t" \
  "s_cbranch_scc1 2b\n\t" \
  "buffer_load_dwordx4 v[32:35], v100, %[rh], s40 offen sc1\n\t" \
  "s_nop 12\n\t" \
  "buffer_load_dwordx4 v[36:39], v100, %[rh], s76 offen sc1\n\t" \
  "s_nop 12\n\t" \
  "buffer_load_dwordx4 v[40:43], v100, %[rh], s77 offen sc1\n\t" \
  "s_nop 12\n\t" \
  "buffer_load_dwordx4 v[44:47], v100, %[rh], s78 offen sc1\n\t" \
  "s_memtime vcc\n\t" \
  "s_memrealtime s[82:83]\n\t" \
  "s_nop 4\n\t" \
  "s_memrealtime s[84:85]\n\t" \
  "s_nop 4\n\t" \
  "s_memrealtime s[56:57]\n\t" \
  "s_nop 4\n\t" \
  "s_memrealtime s[86:87]\n\t" \
  "s_nop 4\n\t" \
  "s_memrealtime s[88:89]\n\t" \
  "s_nop 4\n\t" \
  "s_memrealtime s[90:91]\n\t" \
  "s_mov_b32 s92, 0\n\t" \
  "s_waitcnt lgkmcnt(0)\n\t" \
  "s_cmp_eq_u32 s82, s84\n\t" \
  "s_addc_u32 s92, s92, 0\n\t" \
  "s_cmp_eq_u32 s82, s56\n\t" \
  "s_addc_u32 s92, s92, 0\n\t" \
  "s_cmp_eq_u32 s82, s86\n\t" \
  "s_addc_u32 s92, s92, 0\n\t" \
  "s_cmp_eq_u32 s82, s88\n\t" \
  "s_addc_u32 s92, s92, 0\n\t" \
  "s_cmp_eq_u32 s82, s90\n\t" \
  "s_addc_u32 s92, s92, 0\n\t" \
  "s_mul_i32 s92, s92, 24\n\t" \
  "s_add_i32 s92, s92, vcc_lo\n\t" \
  "s_lshl4_add_u32 s93, s82, 64\n\t" \
  "3:\n\t" \
  "s_sub_i32 s84, s92, s43\n\t" \
  "s_lshl_b32 s85, s93, 8\n\t" \
  "s_or_b32 s85, s85, s44\n\t" \
  "v_mov_b32 v118, s85\n\t" \
  "v_mov_b64 v[68:69], 0\n\t" \
  "v_mov_b64 v[70:71], 0\n\t" \
  "v_mov_b64 v[72:73], 0\n\t" \
  "v_mov_b64 v[74:75], 0\n\t" \
  "v_mov_b64 v[76:77], 0\n\t" \
  "v_mov_b64 v[78:79], 0\n\t" \
  "v_mov_b64 v[80:81], 0\n\t" \
  "v_mov_b64 v[82:83], 0\n\t" \
  "s_waitcnt lgkmcnt(0)\n\t" \
  "s_waitcnt vmcnt(3)\n\t" \
  "v_smfmac_f32_16x16x64_bf16 v[68:71], v[32:35], a[0:7], v104\n\t" \
  "ds_write_b128 v101, v[32:35] offset:0\n\t" \
  "v_smfmac_f32_16x16x64_bf16 v[72:75], v[32:35], a[8:15], v104\n\t" \
  "v_smfmac_f32_16x16x64_bf16 v[76:79], v[32:35], v[48:55], v104\n\t" \
  "v_smfmac_f32_16x16x64_bf16 v[80:83], v[32:35], v[56:63], v104\n\t" \
  "s_waitcnt vmcnt(2)\n\t" \
  "v_smfmac_f32_16x16x64_bf16 v[68:71], v[36:39], a[16:23], v104\n\t" \
  "ds_write_b128 v101, v[36:39] offset:1024\n\t" \
  "v_smfmac_f32_16x16x64_bf16 v[72:75], v[36:39], a[24:31], v104\n\t" \
  "v_smfmac_f32_16x16x64_bf16 v[76:79], v[36:39], a[32:39], v104\n\t" \
  "v_cmp_u_f32 vcc, v68, v69\n\t" \
  "v_smfmac_f32_16x16x64_bf16 v[80:83], v[36:39], a[40:47], v104\n\t" \
  "v_cmp_u_f32 s[82:83], v70, v71\n\t" \
  "s_waitcnt vmcnt(1)\n\t" \
  "v_smfmac_f32_16x16x64_bf16 v[68:71], v[40:43], v[16:23], v104\n\t" \
  "ds_write_b128 v101, v[40:43] offset:2048\n\t" \
  "v_smfmac_f32_16x16x64_bf16 v[72:75], v[40:43], v[24:31], v104\n\t" \
  "s_or_b64 vcc, vcc, s[82:83]\n\t" \
  "s_or_b64 vcc, vcc, s[80:81]\n\t" \
  "s_waitcnt lgkmcnt(1)\n\t" \
  "v_smfmac_f32_16x16x64_bf16 v[76:79], v[40:43], a[48:55], v104\n\t" \
  "s_cbranch_scc1 8f\n\t" \
  "5:\n\t" \
  "s_barrier\n\t" \
  "4:\n\t" \
  "s_memtime s[56:57]\n\t" \
  "ds_read_b128 v[48:51], v101 offset:4096\n\t" \
  "v_smfmac_f32_16x16x64_bf16 v[80:83], v[40:43], a[56:63], v104\n\t" \
  "ds_read_b128 v[52:55], v101 offset:5120\n\t" \
  "ds_read_b128 v[56:59], v101 offset:8192\n\t" \
  "s_waitcnt vmcnt(0)\n\t" \
  "v_smfmac_f32_16x16x64_bf16 v[68:71], v[44:47], a[64:71], v104\n\t" \
  "ds_write_b128 v101, v[44:47] offset:3072\n\t" \
  "v_smfmac_f32_16x16x64_bf16 v[72:75], v[44:47], a[72:79], v104\n\t" \
  "v_smfmac_f32_16x16x64_bf16 v[76:79], v[44:47], a[80:87], v104\n\t" \
  "v_cmp_u_f32 vcc, v68, v69\n\t" \
  "v_smfmac_f32_16x16x64_bf16 v[80:83], v[44:47], a[88:95], v104\n\t" \
  "v_cmp_u_f32 s[82:83], v70, v71\n\t" \
  "s_waitcnt lgkmcnt(3)\n\t" \
  "v_smfmac_f32_16x16x64_bf16 v[68:71], v[48:51], a[96:103], v104\n\t" \
  "ds_read_b128 v[60:63], v101 offset:9216\n\t" \
  "ds_read_b128 v[64:67], v101 offset:12288\n\t" \
  "v_smfmac_f32_16x16x64_bf16 v[72:75], v[48:51], a[104:111], v104\n\t" \
  "s_or_b64 vcc, vcc, s[82:83]\n\t" \
  "s_waitcnt lgkmcnt(2)\n\t" \
  "v_smfmac_f32_16x16x64_bf16 v[68:71], v[52:55], a[112:119], v104\n\t" \
  "s_cbranch_scc1 9f\n\t" \
  "6:\n\t" \
  "s_barrier\n\t" \
  "v_smfmac_f32_16x16x64_bf16 v[72:75], v[52:55], a[120:127], v104\n\t" \
  "s_waitcnt lgkmcnt(0)\n\t" \
  "ds_read_b128 v[48:51], v101 offset:13312\n\t" \
  "v_smfmac_f32_16x16x64_bf16 v[68:71], v[56:59], a[192:199], v104\n\t" \
  "ds_read_b128 v[52:55], v101 offset:6144\n\t" \
  "v_smfmac_f32_16x16x64_bf16 v[72:75], v[56:59], a[200:207], v104\n\t" \
  "s_sub_i32 s79, s56, s84\n\t" \
  "s_mul_hi_u32 s79, s79, s46\n\t" \
  "v_smfmac_f32_16x16x64_bf16 v[68:71], v[60:63], a[208:215], v104\n\t" \
  "ds_read_b128 v[56:59], v101 offset:7168\n\t" \
  "v_smfmac_f32_16x16x64_bf16 v[72:75], v[60:63], a[216:223], v104\n\t" \
  "v_lshl_add_u32 v116, s79, 9, v118\n\t" \
  "buffer_store_dword v116, v109, %[rh], s45 offen sc1\n\t" \
  "v_smfmac_f32_16x16x64_bf16 v[68:71], v[64:67], v[152:159], v104\n\t" \
  "ds_read_b128 v[60:63], v101 offset:10240\n\t" \
  "v_smfmac_f32_16x16x64_bf16 v[72:75], v[64:67], v[160:167], v104\n\t" \
  "s_waitcnt lgkmcnt(3)\n\t" \
  "v_smfmac_f32_16x16x64_bf16 v[68:71], v[48:51], v[168:175], v104\n\t" \
  "ds_read_b128 v[64:67], v101 offset:11264\n\t" \
  "ds_read_b128 v[16:19], v103 offset:49152\n\t" \
  "v_smfmac_f32_16x16x64_bf16 v[72:75], v[48:51], v[176:183], v104\n\t" \
  "s_waitcnt lgkmcnt(4)\n\t" \
  "v_smfmac_f32_16x16x64_bf16 v[68:71], v[52:55], v[84:91], v104\n\t" \
  "ds_read_b128 v[48:51], v101 offset:15360\n\t" \
  "ds_read_b128 v[20:23], v103 offset:50176\n\t" \
  "v_smfmac_f32_16x16x64_bf16 v[72:75], v[52:55], v[92:99], v104\n\t" \
  "s_waitcnt lgkmcnt(5)\n\t" \
  "v_smfmac_f32_16x16x64_bf16 v[68:71], v[56:59], a[160:167], v104\n\t" \
  "ds_read_b128 v[52:55], v101 offset:14336\n\t" \
  "ds_read_b128 v[24:27], v103 offset:51200\n\t" \
  "v_smfmac_f32_16x16x64_bf16 v[72:75], v[56:59], a[168:175], v104\n\t" \
  "ds_read_b128 v[28:31], v103 offset:52224\n\t" \
  "s_waitcnt lgkmcnt(7)\n\t" \
  "v_smfmac_f32_16x16x64_bf16 v[68:71], v[60:63], v[0:7], v104\n\t" \
  "ds_read_b128 v[32:35], v101 offset:20480\n\t" \
  "v_smfmac_f32_16x16x64_bf16 v[72:75], v[60:63], v[8:15], v104\n\t" \
  "ds_read_b128 v[36:39], v101 offset:21504\n\t" \
  "s_waitcnt lgkmcnt(8)\n\t" \
  "v_smfmac_f32_16x16x64_bf16 v[68:71], v[64:67], v[120:127], v104\n\t" \
  "ds_read_b128 v[0:3], v102 offset:32768\n\t" \
  "ds_read_b128 v[40:43], v101 offset:22528\n\t" \
  "v_smfmac_f32_16x16x64_bf16 v[72:75], v[64:67], v[128:135], v104\n\t" \
  "ds_read_b128 v[4:7], v102 offset:33792\n\t" \
  "s_waitcnt lgkmcnt(9)\n\t" \
  "v_smfmac_f32_16x16x64_bf16 v[68:71], v[48:51], v[216:223], v104\n\t" \
  "ds_read_b128 v[44:47], v101 offset:23552\n\t" \
  "s_waitcnt lgkmcnt(8)\n\t" \
  "v_smfmac_f32_16x16x64_bf16 v[68:71], v[52:55], v[16:23], v104\n\t" \
  "ds_read_b128 v[8:11], v102 offset:34816\n\t" \
  "v_smfmac_f32_16x16x64_bf16 v[72:75], v[48:51], v[224:231], v104\n\t" \
  "ds_read_b128 v[12:15], v102 offset:35840\n\t" \
  "s_waitcnt lgkmcnt(8)\n\t" \
  "v_smfmac_f32_16x16x64_bf16 v[72:75], v[52:55], v[24:31], v104\n\t" \
  "ds_read_b128 v[16:19], v103 offset:0\n\t" \
  "v_add_f32 v96, v68, v70\n\t" \
  "v_smfmac_f32_16x16x64_bf16 v[76:79], v[56:59], a[176:183], v104\n\t" \
  "ds_read_b128 v[20:23], v103 offset:1024\n\t" \
  "v_add_f32 v97, v69, v71\n\t" \
  "v_smfmac_f32_16x16x64_bf16 v[80:83], v[56:59], a[184:191], v104\n\t" \
  "v_add_f32 v98, v72, v74\n\t" \
  "v_cvt_pk_bf16_f32 v96, v96, v97\n\t" \
  "v_smfmac_f32_16x16x64_bf16 v[76:79], v[60:63], a[240:247], v104\n\t" \
  "ds_read_b128 v[56:59], v101 offset:4096\n\t" \
  "v_add_f32 v99, v73, v75\n\t" \
  "v_smfmac_f32_16x16x64_bf16 v[80:83], v[60:63], a[248:255], v104\n\t" \
  "ds_read_b128 v[24:27], v103 offset:2048\n\t" \
  "v_cvt_pk_bf16_f32 v98, v98, v99\n\t" \
  "v_smfmac_f32_16x16x64_bf16 v[76:79], v[64:67], v[136:143], v104\n\t" \
  "ds_write2st64_b32 v105, v96, v98 offset1:1\n\t" \
  "v_mov_b64 v[68:69], 0\n\t" \
  "v_smfmac_f32_16x16x64_bf16 v[80:83], v[64:67], v[144:151], v104\n\t" \
  "ds_read_b128 v[60:63], v101 offset:13312\n\t" \
  "v_mov_b64 v[70:71], 0\n\t" \
  "v_smfmac_f32_16x16x64_bf16 v[76:79], v[48:51], v[232:239], v104\n\t" \
  "ds_read_b128 v[64:67], v101 offset:6144\n\t" \
  "v_mov_b64 v[72:73], 0\n\t" \
  "v_smfmac_f32_16x16x64_bf16 v[80:83], v[48:51], v[240:247], v104\n\t" \
  "ds_read_b128 v[28:31], v103 offset:3072\n\t" \
  "v_mov_b64 v[74:75], 0\n\t" \
  "v_smfmac_f32_16x16x64_bf16 v[76:79], v[52:55], v[200:207], v104\n\t" \
  "ds_read_b128 v[48:51], v101 offset:8192\n\t" \
  "v_smfmac_f32_16x16x64_bf16 v[80:83], v[52:55], v[208:215], v104\n\t" \
  "s_waitcnt lgkmcnt(4)\n\t" \
  "s_barrier\n\t" \
  "ds_read_b64_tr_b16 v[84:85], v107 offset:0\n\t" \
  "v_smfmac_f32_16x16x64_bf16 v[76:79], v[56:59], v[0:7], v104\n\t" \
  "ds_read_b128 v[52:55], v101 offset:5120\n\t" \
  "ds_read_b64_tr_b16 v[86:87], v107 offset:128\n\t" \
  "v_smfmac_f32_16x16x64_bf16 v[80:83], v[56:59], v[8:15], v104\n\t" \
  "ds_read_b64_tr_b16 v[88:89], v107 offset:1024\n\t" \
  "ds_read_b64_tr_b16 v[90:91], v107 offset:1152\n\t" \
  "s_waitcnt lgkmcnt(8)\n\t" \
  "v_smfmac_f32_16x16x64_bf16 v[76:79], v[60:63], v[184:191], v104\n\t" \
  "ds_read_b128 v[56:59], v101 offset:9216\n\t" \
  "v_smfmac_f32_16x16x64_bf16 v[80:83], v[60:63], v[192:199], v104\n\t" \
  "ds_read_b128 v[0:3], v103 offset:32768\n\t" \
  "s_waitcnt lgkmcnt(9)\n\t" \
  "v_smfmac_f32_16x16x64_bf16 v[76:79], v[64:67], a[144:151], v104\n\t" \
  "ds_read_b128 v[60:63], v101 offset:12288\n\t" \
  "ds_read_b128 v[4:7], v103 offset:33792\n\t" \
  "s_waitcnt lgkmcnt(6)\n\t" \
  "v_smfmac_f32_16x16x64_bf16 v[68:71], v[84:87], v[32:39], v104\n\t" \
  "ds_read_b128 v[32:35], v101 offset:24576\n\t" \
  "ds_read_b128 v[36:39], v101 offset:25600\n\t" \
  "s_waitcnt lgkmcnt(6)\n\t" \
  "v_smfmac_f32_16x16x64_bf16 v[68:71], v[88:91], v[40:47], v104\n\t" \
  "ds_read_b128 v[40:43], v101 offset:26624\n\t" \
  "ds_read_b128 v[44:47], v101 offset:27648\n\t" \
  "v_smfmac_f32_16x16x64_bf16 v[80:83], v[64:67], a[152:159], v104\n\t" \
  "ds_read_b128 v[8:11], v103 offset:34816\n\t" \
  "v_smfmac_f32_16x16x64_bf16 v[76:79], v[48:51], v[16:23], v104\n\t" \
  "ds_read_b128 v[12:15], v103 offset:35840\n\t" \
  "v_add_f32 v92, v68, v70\n\t" \
  "v_smfmac_f32_16x16x64_bf16 v[80:83], v[48:51], v[24:31], v104\n\t" \
  "v_add_f32 v94, v69, v71\n\t" \
  "v_smfmac_f32_16x16x64_bf16 v[76:79], v[52:55], a[128:135], v104\n\t" \
  "s_waitcnt lgkmcnt(4)\n\t" \
  "v_smfmac_f32_16x16x64_bf16 v[72:75], v[84:87], v[32:39], v104\n\t" \
  "s_waitcnt lgkmcnt(2)\n\t" \
  "v_smfmac_f32_16x16x64_bf16 v[72:75], v[88:91], v[40:47], v104\n\t" \
  "v_mov_b32 v97, v94\n\t" \
  "v_smfmac_f32_16x16x64_bf16 v[80:83], v[52:55], a[136:143], v104\n\t" \
  "v_mov_b32_dpp v94, v92 row_ror:8 row_mask:0xf bank_mask:0x3\n\t" \
  "v_smfmac_f32_16x16x64_bf16 v[76:79], v[56:59], a[224:231], v104\n\t" \
  "v_mov_b32_dpp v92, v97 row_ror:8 row_mask:0xf bank_mask:0xc\n\t" \
  "v_smfmac_f32_16x16x64_bf16 v[76:79], v[60:63], v[0:7], v104\n\t" \
  "v_add_f32 v93, v72, v74\n\t" \
  "v_smfmac_f32_16x16x64_bf16 v[80:83], v[56:59], a[232:239], v104\n\t" \
  "v_add_f32 v95, v73, v75\n\t" \
  "s_waitcnt lgkmcnt(0)\n\t" \
  "v_smfmac_f32_16x16x64_bf16 v[80:83], v[60:63], v[8:15], v104\n\t" \
  "v_mov_b32 v97, v95\n\t" \
  "v_add_f32 v20, v76, v78\n\t" \
  "v_add_f32 v21, v77, v79\n\t" \
  "v_mov_b32_dpp v95, v93 row_ror:8 row_mask:0xf bank_mask:0x3\n\t" \
  "v_cvt_pk_bf16_f32 v20, v20, v21\n\t" \
  "v_mov_b32_dpp v93, v97 row_ror:8 row_mask:0xf bank_mask:0xc\n\t" \
  "ds_write_b32 v106, v20\n\t" \
  "v_max3_f32 v28, v92, v94, v95\n\t" \
  "v_max_f32 v28, v28, v93\n\t" \
  "v_add_f32 v23, v80, v82\n\t" \
  "v_add_f32 v21, v81, v83\n\t" \
  "v_max_f32_dpp v28, v28, v28 quad_perm:[1,0,3,2] row_mask:0xf bank_mask:0xf\n\t" \
  "v_cvt_pk_bf16_f32 v21, v23, v21\n\t" \
  "ds_write_b32 v106, v21 offset:256\n\t" \
  "v_max_f32_dpp v28, v28, v28 quad_perm:[2,3,0,1] row_mask:0xf bank_mask:0xf\n\t" \
  "v_mov_b64 v[0:1], 0\n\t" \
  "v_mov_b64 v[2:3], 0\n\t" \
  "v_max_f32_dpp v28, v28, v28 row_half_mirror row_mask:0xf bank_mask:0xf\n\t" \
  "v_mul_f32 v28, %[cs], v28\n\t" \
  "v_fma_f32 v92, v92, %[cs], -v28\n\t" \
  "v_fma_f32 v93, v93, %[cs], -v28\n\t" \
  "v_fma_f32 v94, v94, %[cs], -v28\n\t" \
  "v_fma_f32 v95, v95, %[cs], -v28\n\t" \
  "s_waitcnt lgkmcnt(0)\n\t" \
  "s_barrier\n\t" \
  "ds_read_b64_tr_b16 v[4:5], v108\n\t" \
  "ds_read_b64_tr_b16 v[6:7], v108 offset:64\n\t" \
  "ds_read_b64_tr_b16 v[8:9], v108 offset:512\n\t" \
  "ds_read_b64_tr_b16 v[10:11], v108 offset:576\n\t" \
  "ds_read_b64_tr_b16 v[12:13], v108 offset:1024\n\t" \
  "ds_read_b64_tr_b16 v[14:15], v108 offset:1088\n\t" \
  "ds_read_b64_tr_b16 v[16:17], v108 offset:1536\n\t" \
  "ds_read_b64_tr_b16 v[18:19], v108 offset:1600\n\t" \
  "v_exp_f32 v20, v92\n\t" \
  "v_exp_f32 v21, v93\n\t" \
  "v_exp_f32 v22, v94\n\t" \
  "v_exp_f32 v23, v95\n\t" \
  "s_waitcnt lgkmcnt(4)\n\t" \
  "v_smfmac_f32_16x16x64_bf16 v[0:3], v[84:87], v[4:11], v104\n\t" \
  "s_waitcnt lgkmcnt(0)\n\t" \
  "v_smfmac_f32_16x16x64_bf16 v[0:3], v[88:91], v[12:19], v104\n\t" \
  "v_add_f32 v29, v20, v21\n\t" \
  "v_add_f32 v29, v29, v22\n\t" \
  "v_add_f32 v29, v29, v23\n\t" \
  "s_nop 4\n\t" \
  "v_add_f32 v96, v0, v2\n\t" \
  "v_add_f32_dpp v96, v1, v3 quad_perm:[0,1,2,3] row_mask:0xf bank_mask:0xc\n\t" \
  "v_fma_f32 v96, v96, %[cs], -v28\n\t" \
  "v_exp_f32 v24, v96\n\t" \
  "s_nop 0\n\t" \
  "v_add_f32 v29, v29, v24\n\t" \
  "s_nop 0\n\t" \
  "v_mov_b32_dpp v25, v24 quad_perm:[1,0,3,2] row_mask:0xf bank_mask:0xf\n\t" \
  "v_add_f32_dpp v29, v29, v29 quad_perm:[1,0,3,2] row_mask:0xf bank_mask:0xf\n\t" \
  "s_nop 1\n\t" \
  "v_add_f32_dpp v29, v29, v29 quad_perm:[2,3,0,1] row_mask:0xf bank_mask:0xf\n\t" \
  "s_nop 1\n\t" \
  "v_add_f32_dpp v29, v29, v29 row_half_mirror row_mask:0xf bank_mask:0xf\n\t" \
  "v_rcp_f32 v26, v29\n\t" \
  "v_cmpx_gt_f32 vcc, 0x5d5e0b6b, v29\n\t" \
  "v_mul_f32 v20, v20, v26\n\t" \
  "v_mul_f32 v21, v21, v26\n\t" \
  "v_mul_f32 v22, v22, v26\n\t" \
  "v_mul_f32 v23, v23, v26\n\t" \
  "v_cvt_pk_bf16_f32 v20, v20, v21\n\t" \
  "v_cvt_pk_bf16_f32 v21, v22, v23\n\t" \
  "buffer_store_dwordx2 v[20:21], v110, %[rh], s47 offen sc1\n\t" \
  "v_mul_f32 v24, v24, v26\n\t" \
  "v_mul_f32 v25, v25, v26\n\t" \
  "v_cvt_pk_bf16_f32 v24, v24, v25\n\t" \
  "buffer_store_dword v24, v111, %[rh], s47 offen sc1\n\t" \
  "s_memtime s[76:77]\n\t" \
  "s_mov_b64 exec, -1\n\t" \
  "v_mov_b32 v97, v29\n\t" \
  "s_cmp_eq_u64 vcc, -1\n\t" \
  "s_cselect_b32 s42, 0, 8\n\t" \
  "s_waitcnt lgkmcnt(0)\n\t" \
  "s_mov_b32 s57, s76\n\t" \
  "s_add_i32 s41, s56, 1950\n\t" \
  "s_sub_i32 s76, s57, s56\n\t" \
  "s_add_i32 s77, s43, 8\n\t" \
  "s_min_u32 s76, s76, s77\n\t" \
  "s_sub_i32 s43, s43, 4\n\t" \
  "s_max_u32 s43, s43, s76\n\t" \
  "s_add_i32 s76, s45, 0x4000\n\t" \
  "s_and_b32 s76, s76, 0xc000\n\t" \
  "v_mov_b32 v116, -1\n\t" \
  "buffer_store_dword v116, v109, %[rh], s76 offen sc1\n\t" \
  "s_sub_i32 s76, s92, s51\n\t" \
  "s_sub_i32 s77, s93, s53\n\t" \
  "s_cmp_eq_u32 s76, 0\n\t" \
  "s_cbranch_scc1 21f\n\t" \
  "v_cvt_f32_u32 v117, s76\n\t" \
  "v_cvt_f32_u32 v116, s77\n\t" \
  "v_rcp_f32 v117, v117\n\t" \
  "s_mov_b32 s51, s92\n\t" \
  "v_mul_f32 v117, v117, v116\n\t" \
  "v_max_f32 v117, 0x3f028f5c, v117\n\t" \
  "v_min_f32 v117, 0x3ffeb852, v117\n\t" \
  "v_rcp_f32 v116, v117\n\t" \
  "v_mul_f32 v117, 0x4f000000, v117\n\t" \
  "v_mul_f32 v116, 0x47800000, v116\n\t" \
  "v_cvt_u32_f32 v117, v117\n\t" \
  "v_cvt_u32_f32 v116, v116\n\t" \
  "s_mov_b32 s53, s93\n\t" \
  "v_readfirstlane_b32 s46, v117\n\t" \
  "v_readfirstlane_b32 s52, v116\n\t" \
  "21:\n\t" \
  "s_add_i32 s44, s44, 1\n\t" \
  "s_and_b32 s58, s58, 4\n\t" \
  "s_and_b32 s76, s44, 64\n\t" \
  "s_or_b32 s42, s42, s76\n\t" \
  "s_cbranch_scc0 20b\n\t" \
  "s_branch 99f\n\t" \
  "14:\n\t" \
  "s_add_u32 s59, s59, 0x10000\n\t" \
  "s_cmp_lt_u32 s59, 0x100000\n\t" \
  "s_cbranch_scc1 13b\n\t" \
  "s_mov_b64 exec, -1\n\t" \
  "s_mov_b32 s42, 16\n\t" \
  "s_branch 99f\n\t" \
  "8:\n\t" \
  "s_bitcmp1_b32 s58, 1\n\t" \
  "s_cbranch_scc1 4b\n\t" \
  "s_bitcmp1_b32 s58, 2\n\t" \
  "s_cbranch_scc1 5b\n\t" \
  "v_max3_u32 v116, v32, v33, v34\n\t" \
  "v_max3_u32 v117, v35, v36, v37\n\t" \
  "v_max3_u32 v116, v116, v38, v39\n\t" \
  "v_max_u32 v116, v116, v117\n\t" \
  "v_cmp_eq_u32 vcc, -1, v116\n\t" \
  "s_cbranch_vccz 5b\n\t" \
  "s_branch 0f\n\t" \
  "9:\n\t" \
  "s_bitcmp1_b32 s58, 2\n\t" \
  "s_cbranch_scc1 6b\n\t" \
  "v_max3_u32 v116, v40, v41, v42\n\t" \
  "v_max3_u32 v117, v43, v44, v45\n\t" \
  "v_max3_u32 v116, v116, v46, v47\n\t" \
  "v_max_u32 v116, v116, v117\n\t" \
  "v_cmp_eq_u32 vcc, -1, v116\n\t" \
  "s_cbranch_vccz 6b\n\t" \
  "s_or_b32 s58, s58, 2\n\t" \
  "0:\n\t" \
  "s_or_b32 s58, s58, 1\n\t" \
  "11:\n\t" \
  "buffer_load_dwordx4 v[32:35], v100, %[rh], s40 offen sc1\n\t" \
  "buffer_load_dwordx4 v[36:39], v100, %[rh], s76 offen sc1\n\t" \
  "buffer_load_dwordx4 v[40:43], v100, %[rh], s77 offen sc1\n\t" \
  "buffer_load_dwordx4 v[44:47], v100, %[rh], s78 offen sc1\n\t" \
  "s_waitcnt lgkmcnt(0)\n\t" \
  "s_branch 10b\n\t" \
  "17:\n\t" \
  "s_add_u32 s59, s59, 1\n\t" \
  "s_bitcmp1_b32 s58, 2\n\t" \
  "s_cbranch_scc1 3b\n\t" \
  "s_waitcnt vmcnt(0)\n\t" \
  "v_max_u32 v116, v32, v33\n\t" \
  "v_max3_u32 v116, v116, v34, v35\n\t" \
  "v_max3_u32 v116, v116, v36, v37\n\t" \
  "v_max3_u32 v116, v116, v38, v39\n\t" \
  "v_max3_u32 v116, v116, v40, v41\n\t" \
  "v_max3_u32 v116, v116, v42, v43\n\t" \
  "v_max3_u32 v116, v116, v44, v45\n\t" \
  "v_max3_u32 v116, v116, v46, v47\n\t" \
  "v_cmp_eq_u32 vcc, -1, v116\n\t" \
  "s_cbranch_vccz 3b\n\t" \
  "s_bitcmp1_b32 s59, 6\n\t" \
  "s_cbranch_scc1 12f\n\t" \
  "buffer_load_dwordx4 v[32:35], v100, %[rh], s40 offen sc1\n\t" \
  "buffer_load_dwordx4 v[36:39], v100, %[rh], s76 offen sc1\n\t" \
  "buffer_load_dwordx4 v[40:43], v100, %[rh], s77 offen sc1\n\t" \
  "buffer_load_dwordx4 v[44:47], v100, %[rh], s78 offen sc1\n\t" \
  "s_branch 17b\n\t" \
  "12:\n\t" \
  "s_mov_b32 s42, s58\n\t" \
  "s_branch 99f\n\t" \
  "91:\n\t" \
  "s_mov_b32 s58, s42\n\t" \
  "20:\n\t" \
  "s_and_b32 s76, s44, 7\n\t" \
  "s_lshl_b32 s76, s76, 15\n\t" \
  "s_add_i32 s40, s54, s76\n\t" \
  "s_and_b32 s76, s44, 3\n\t" \
  "s_lshl_b32 s45, s76, 14\n\t" \
  "s_add_i32 s47, s44, 1\n\t" \
  "s_min_u32 s47, s47, 63\n\t" \
  "s_lshl_b32 s47, s47, 18\n\t" \
  "s_add_i32 s47, s47, s55\n\t" \
  "v_mov_b32 v118, 0\n\t" \
  "s_add_i32 s49, s44, -1\n\t" \
  "s_and_b32 s48, s49, 2\n\t" \
  "s_mul_i32 s48, s48, 0x6000\n\t" \
  "s_and_b32 s76, s49, 1\n\t" \
  "s_lshl_b32 s76, s76, 11\n\t" \
  "s_add_i32 s48, s48, s76\n\t" \
  "s_lshl_b32 s50, s53, 8\n\t" \
  "s_mov_b32 s59, 0\n\t" \
  "s_mov_b32 s92, s51\n\t" \
  "s_mov_b32 s93, s53\n\t" \
  "s_xor_b32 s76, s40, 0x400\n\t" \
  "s_xor_b32 s77, s40, 0x800\n\t" \
  "s_xor_b32 s78, s40, 0xc00\n\t" \
  "s_cmp_lg_u32 s58, 0\n\t" \
  "s_cbranch_scc1 11f\n\t" \
  "10:\n\t" \
  "ds_read_b128 v[48:51], v102 offset:32768\n\t" \
  "ds_read_b128 v[52:55], v102 offset:33792\n\t" \
  "ds_read_b128 v[56:59], v102 offset:34816\n\t" \
  "ds_read_b128 v[60:63], v102 offset:35840\n\t" \
  "ds_read_b128 v[16:19], v102 offset:49152\n\t" \
  "ds_read_b128 v[20:23], v102 offset:50176\n\t" \
  "ds_read_b128 v[24:27], v102 offset:51200\n\t" \
  "ds_read_b128 v[28:31], v102 offset:52224\n\t" \
  "ds_read_b128 v[84:87], v103 offset:16384\n\t" \
  "ds_read_b128 v[88:91], v103 offset:17408\n\t" \
  "ds_read_b128 v[92:95], v103 offset:18432\n\t" \
  "ds_read_b128 v[96:99], v103 offset:19456\n\t" \
  "s_waitcnt lgkmcnt(4)\n\t" \
  "ds_read_b128 v[0:3], v103 offset:49152\n\t" \
  "ds_read_b128 v[4:7], v103 offset:50176\n\t" \
  "ds_read_b128 v[8:11], v103 offset:51200\n\t" \
  "ds_read_b128 v[12:15], v103 offset:52224\n\t" \
  "s_bitcmp1_b32 s58, 1\n\t" \
  "s_cselect_b64 s[80:81], -1, 0\n\t" \
  "s_cmp_lg_u32 s58, 0\n\t" \
  "s_cbranch_scc1 17f\n\t" \
  "s_mov_b64 exec, 0xffff\n\t" \
  "16:\n\t" \
  "s_memtime vcc\n\t" \
  "s_waitcnt lgkmcnt(0)\n\t" \
  "s_sub_i32 s79, s41, vcc_lo\n\t" \
  "s_cmp_gt_i32 s79, 64\n\t" \
  "s_cbranch_scc1 16b\n\t" \
  "13:\n\t" \
  "buffer_load_dword v116, v112, %[rh], s48 offen sc1\n\t" \
  "s_waitcnt vmcnt(0)\n\t" \
  "v_mov_b32 v117, 20000\n\t" \
  "v_cmp_ne_u32_sdwa vcc, v116, s49 src0_sel:BYTE_0 src1_sel:DWORD\n\t" \
  "v_subrev_u32 v116, s50, v116\n\t" \
  "v_ashrrev_i32 v116, 8, v116\n\t" \
  "s_nop 1\n\t" \
  "v_max_i32_dpp v116, v116, v116 quad_perm:[1,0,3,2] row_mask:0xf bank_mask:0xf\n\t" \
  "s_cbranch_vccnz 14f\n\t" \
  "s_nop 0\n\t" \
  "v_max_i32_dpp v116, v116, v116 quad_perm:[2,3,0,1] row_mask:0xf bank_mask:0xf\n\t" \
  "s_nop 1\n\t" \
  "v_max_i32_dpp v116, v116, v116 row_half_mirror row_mask:0xf bank_mask:0xf\n\t" \
  "s_nop 1\n\t" \
  "v_max_i32_dpp v116, v116, v116 row_mirror row_mask:0xf bank_mask:0xf\n\t" \
  "v_med3_i32 v116, v116, 0, v117\n\t" \
  "v_mul_u32_u24 v116, s52, v116\n\t" \
  "v_lshrrev_b32 v116, 16, v116\n\t" \
  "s_mov_b64 exec, -1\n\t" \
  "v_readfirstlane_b32 s84, v116\n\t" \
  "s_add_i32 s84, s84, s51\n\t" \
  "1:\n\t" \
  "s_memtime vcc\n\t" \
  "s_waitcnt lgkmcnt(0)\n\t" \
  "s_sub_i32 s79, s84, vcc_lo\n\t" \
  "s_cmp_gt_i32 s79, 128\n\t" \
  "s_cbranch_scc1 1b\n\t" \
  "2:\n\t" \
  "s_sub_i32 s79, s79, 33\n\t" \
  "s_cmp_gt_i32 s79, 56\n\t" \
  "s_cbranch_scc1 2b\n\t" \
  "buffer_load_dwordx4 v[32:35], v100, %[rh], s40 offen sc1\n\t" \
  "s_nop 12\n\t" \
  "buffer_load_dwordx4 v[36:39], v100, %[rh], s76 offen sc1\n\t" \
  "s_nop 12\n\t" \
  "buffer_load_dwordx4 v[40:43], v100, %[rh], s77 offen sc1\n\t" \
  "s_nop 12\n\t" \
  "buffer_load_dwordx4 v[44:47], v100, %[rh], s78 offen sc1\n\t" \
  "s_memtime vcc\n\t" \
  "s_memrealtime s[82:83]\n\t" \
  "s_nop 4\n\t" \
  "s_memrealtime s[84:85]\n\t" \
  "s_nop 4\n\t" \
  "s_memrealtime s[56:57]\n\t" \
  "s_nop 4\n\t" \
  "s_memrealtime s[86:87]\n\t" \
  "s_nop 4\n\t" \
  "s_memrealtime s[88:89]\n\t" \
  "s_nop 4\n\t" \
  "s_memrealtime s[90:91]\n\t" \
  "s_mov_b32 s92, 0\n\t" \
  "s_waitcnt lgkmcnt(0)\n\t" \
  "s_cmp_eq_u32 s82, s84\n\t" \
  "s_addc_u32 s92, s92, 0\n\t" \
  "s_cmp_eq_u32 s82, s56\n\t" \
  "s_addc_u32 s92, s92, 0\n\t" \
  "s_cmp_eq_u32 s82, s86\n\t" \
  "s_addc_u32 s92, s92, 0\n\t" \
  "s_cmp_eq_u32 s82, s88\n\t" \
  "s_addc_u32 s92, s92, 0\n\t" \
  "s_cmp_eq_u32 s82, s90\n\t" \
  "s_addc_u32 s92, s92, 0\n\t" \
  "s_mul_i32 s92, s92, 24\n\t" \
  "s_add_i32 s92, s92, vcc_lo\n\t" \
  "s_lshl4_add_u32 s93, s82, 64\n\t" \
  "3:\n\t" \
  "v_mov_b64 v[68:69], 0\n\t" \
  "v_mov_b64 v[70:71], 0\n\t" \
  "v_mov_b64 v[72:73], 0\n\t" \
  "v_mov_b64 v[74:75], 0\n\t" \
  "v_mov_b64 v[76:77], 0\n\t" \
  "v_mov_b64 v[78:79], 0\n\t" \
  "v_mov_b64 v[80:81], 0\n\t" \
  "v_mov_b64 v[82:83], 0\n\t" \
  "s_waitcnt lgkmcnt(0)\n\t" \
  "s_waitcnt vmcnt(3)\n\t" \
  "v_smfmac_f32_16x16x64_bf16 v[68:71], v[32:35], a[96:103], v104\n\t" \
  "ds_write_b128 v101, v[32:35] offset:4096\n\t" \
  "v_smfmac_f32_16x16x64_bf16 v[72:75], v[32:35], a[104:111], v104\n\t" \
  "v_smfmac_f32_16x16x64_bf16 v[76:79], v[32:35], v[48:55], v104\n\t" \
  "v_smfmac_f32_16x16x64_bf16 v[80:83], v[32:35], v[56:63], v104\n\t" \
  "s_waitcnt vmcnt(2)\n\t" \
  "v_smfmac_f32_16x16x64_bf16 v[68:71], v[36:39], a[112:119], v104\n\t" \
  "ds_write_b128 v101, v[36:39] offset:5120\n\t" \
  "v_smfmac_f32_16x16x64_bf16 v[72:75], v[36:39], a[120:127], v104\n\t" \
  "v_smfmac_f32_16x16x64_bf16 v[76:79], v[36:39], a[128:135], v104\n\t" \
  "v_cmp_u_f32 vcc, v68, v69\n\t" \
  "v_smfmac_f32_16x16x64_bf16 v[80:83], v[36:39], a[136:143], v104\n\t" \
  "v_cmp_u_f32 s[82:83], v70, v71\n\t" \
  "s_waitcnt vmcnt(1)\n\t" \
  "v_smfmac_f32_16x16x64_bf16 v[68:71], v[40:43], v[16:23], v104\n\t" \
  "ds_write_b128 v101, v[40:43] offset:6144\n\t" \
  "v_smfmac_f32_16x16x64_bf16 v[72:75], v[40:43], v[24:31], v104\n\t" \
  "s_or_b64 vcc, vcc, s[82:83]\n\t" \
  "s_or_b64 vcc, vcc, s[80:81]\n\t" \
  "s_waitcnt lgkmcnt(1)\n\t" \
  "v_smfmac_f32_16x16x64_bf16 v[76:79], v[40:43], a[144:151], v104\n\t" \
  "s_cbranch_scc1 8f\n\t" \
  "5:\n\t" \
  "s_barrier\n\t" \
  "4:\n\t" \
  "s_memtime s[56:57]\n\t" \
  "ds_read_b128 v[48:51], v101 offset:8192\n\t" \
  "v_smfmac_f32_16x16x64_bf16 v[80:83], v[40:43], a[152:159], v104\n\t" \
  "ds_read_b128 v[52:55], v101 offset:9216\n\t" \
  "ds_read_b128 v[56:59], v101 offset:12288\n\t" \
  "s_waitcnt vmcnt(0)\n\t" \
  "v_smfmac_f32_16x16x64_bf16 v[68:71], v[44:47], a[160:167], v104\n\t" \
  "ds_write_b128 v101, v[44:47] offset:7168\n\t" \
  "v_smfmac_f32_16x16x64_bf16 v[72:75], v[44:47], a[168:175], v104\n\t" \
  "v_smfmac_f32_16x16x64_bf16 v[76:79], v[44:47], a[176:183], v104\n\t" \
  "v_cmp_u_f32 vcc, v68, v69\n\t" \
  "v_smfmac_f32_16x16x64_bf16 v[80:83], v[44:47], a[184:191], v104\n\t" \
  "v_cmp_u_f32 s[82:83], v70, v71\n\t" \
  "s_waitcnt lgkmcnt(3)\n\t" \
  "v_smfmac_f32_16x16x64_bf16 v[68:71], v[48:51], a[192:199], v104\n\t" \
  "ds_read_b128 v[60:63], v101 offset:13312\n\t" \
  "ds_read_b128 v[64:67], v101 offset:0\n\t" \
  "v_smfmac_f32_16x16x64_bf16 v[72:75], v[48:51], a[200:207], v104\n\t" \
  "s_or_b64 vcc, vcc, s[82:83]\n\t" \
  "s_waitcnt lgkmcnt(2)\n\t" \
  "v_smfmac_f32_16x16x64_bf16 v[68:71], v[52:55], a[208:215], v104\n\t" \
  "s_cbranch_scc1 9f\n\t" \
  "6:\n\t" \
  "s_barrier\n\t" \
  "v_smfmac_f32_16x16x64_bf16 v[72:75], v[52:55], a[216:223], v104\n\t" \
  "ds_read_b128 v[48:51], v101 offset:1024\n\t" \
  "v_smfmac_f32_16x16x64_bf16 v[68:71], v[56:59], v[152:159], v104\n\t" \
  "ds_read_b128 v[52:55], v101 offset:10240\n\t" \
  "v_smfmac_f32_16x16x64_bf16 v[72:75], v[56:59], v[160:167], v104\n\t" \
  "s_waitcnt lgkmcnt(3)\n\t" \
  "v_smfmac_f32_16x16x64_bf16 v[68:71], v[60:63], v[168:175], v104\n\t" \
  "ds_read_b128 v[56:59], v101 offset:11264\n\t" \
  "v_smfmac_f32_16x16x64_bf16 v[72:75], v[60:63], v[176:183], v104\n\t" \
  "s_waitcnt lgkmcnt(3)\n\t" \
  "v_smfmac_f32_16x16x64_bf16 v[68:71], v[64:67], a[0:7], v104\n\t" \
  "ds_read_b128 v[60:63], v101 offset:14336\n\t" \
  "v_smfmac_f32_16x16x64_bf16 v[72:75], v[64:67], a[8:15], v104\n\t" \
  "s_waitcnt lgkmcnt(3)\n\t" \
  "v_smfmac_f32_16x16x64_bf16 v[68:71], v[48:51], a[16:23], v104\n\t" \
  "ds_read_b128 v[64:67], v101 offset:15360\n\t" \
  "ds_read_b128 v[16:19], v102 offset:16384\n\t" \
  "v_smfmac_f32_16x16x64_bf16 v[72:75], v[48:51], a[24:31], v104\n\t" \
  "s_waitcnt lgkmcnt(4)\n\t" \
  "v_smfmac_f32_16x16x64_bf16 v[68:71], v[52:55], v[84:91], v104\n\t" \
  "ds_read_b128 v[48:51], v101 offset:3072\n\t" \
  "ds_read_b128 v[20:23], v102 offset:17408\n\t" \
  "v_smfmac_f32_16x16x64_bf16 v[72:75], v[52:55], v[92:99], v104\n\t" \
  "s_waitcnt lgkmcnt(5)\n\t" \
  "v_smfmac_f32_16x16x64_bf16 v[68:71], v[56:59], v[120:127], v104\n\t" \
  "ds_read_b128 v[52:55], v101 offset:2048\n\t" \
  "ds_read_b128 v[24:27], v102 offset:18432\n\t" \
  "v_smfmac_f32_16x16x64_bf16 v[72:75], v[56:59], v[128:135], v104\n\t" \
  "ds_read_b128 v[28:31], v102 offset:19456\n\t" \
  "s_waitcnt lgkmcnt(7)\n\t" \
  "v_smfmac_f32_16x16x64_bf16 v[68:71], v[60:63], v[0:7], v104\n\t" \
  "global_load_dword v92, v118, %[kc] sc1\n\t" \
  "v_smfmac_f32_16x16x64_bf16 v[72:75], v[60:63], v[8:15], v104\n\t" \
  "s_waitcnt lgkmcnt(6)\n\t" \
  "v_smfmac_f32_16x16x64_bf16 v[68:71], v[64:67], v[216:223], v104\n\t" \
  "ds_read_b128 v[0:3], v103 offset:0\n\t" \
  "v_smfmac_f32_16x16x64_bf16 v[72:75], v[64:67], v[224:231], v104\n\t" \
  "ds_read_b128 v[4:7], v103 offset:1024\n\t" \
  "s_waitcnt lgkmcnt(6)\n\t" \
  "v_smfmac_f32_16x16x64_bf16 v[68:71], v[48:51], a[64:71], v104\n\t" \
  "s_waitcnt lgkmcnt(4)\n\t" \
  "v_smfmac_f32_16x16x64_bf16 v[68:71], v[52:55], v[16:23], v104\n\t" \
  "ds_read_b128 v[8:11], v103 offset:2048\n\t" \
  "v_smfmac_f32_16x16x64_bf16 v[72:75], v[48:51], a[72:79], v104\n\t" \
  "ds_read_b128 v[12:15], v103 offset:3072\n\t" \
  "s_waitcnt lgkmcnt(4)\n\t" \
  "v_smfmac_f32_16x16x64_bf16 v[72:75], v[52:55], v[24:31], v104\n\t" \
  "ds_read_b128 v[16:19], v103 offset:32768\n\t" \
  "v_add_f32 v96, v68, v70\n\t" \
  "v_smfmac_f32_16x16x64_bf16 v[76:79], v[56:59], v[136:143], v104\n\t" \
  "ds_read_b128 v[20:23], v103 offset:33792\n\t" \
  "v_add_f32 v97, v69, v71\n\t" \
  "v_smfmac_f32_16x16x64_bf16 v[80:83], v[56:59], v[144:151], v104\n\t" \
  "v_add_f32 v98, v72, v74\n\t" \
  "v_cvt_pk_bf16_f32 v96, v96, v97\n\t" \
  "v_smfmac_f32_16x16x64_bf16 v[76:79], v[60:63], v[200:207], v104\n\t" \
  "ds_read_b128 v[56:59], v101 offset:8192\n\t" \
  "v_add_f32 v99, v73, v75\n\t" \
  "v_smfmac_f32_16x16x64_bf16 v[80:83], v[60:63], v[208:215], v104\n\t" \
  "ds_read_b128 v[24:27], v103 offset:34816\n\t" \
  "v_cvt_pk_bf16_f32 v98, v98, v99\n\t" \
  "v_smfmac_f32_16x16x64_bf16 v[76:79], v[64:67], v[232:239], v104\n\t" \
  "ds_write2st64_b32 v105, v96, v98 offset1:1\n\t" \
  "v_smfmac_f32_16x16x64_bf16 v[80:83], v[64:67], v[240:247], v104\n\t" \
  "ds_read_b128 v[60:63], v101 offset:1024\n\t" \
  "v_smfmac_f32_16x16x64_bf16 v[76:79], v[48:51], a[80:87], v104\n\t" \
  "ds_read_b128 v[64:67], v101 offset:10240\n\t" \
  "v_smfmac_f32_16x16x64_bf16 v[80:83], v[48:51], a[88:95], v104\n\t" \
  "ds_read_b128 v[28:31], v103 offset:35840\n\t" \
  "v_smfmac_f32_16x16x64_bf16 v[76:79], v[52:55], a[48:55], v104\n\t" \
  "ds_read_b128 v[48:51], v101 offset:12288\n\t" \
  "v_smfmac_f32_16x16x64_bf16 v[80:83], v[52:55], a[56:63], v104\n\t" \
  "s_waitcnt lgkmcnt(4)\n\t" \
  "s_barrier\n\t" \
  "v_smfmac_f32_16x16x64_bf16 v[76:79], v[56:59], v[0:7], v104\n\t" \
  "ds_read_b128 v[52:55], v101 offset:9216\n\t" \
  "v_smfmac_f32_16x16x64_bf16 v[80:83], v[56:59], v[8:15], v104\n\t" \
  "buffer_load_dwordx4 v[32:35], v100, %[rk], s47 offen offset:0 sc1\n\t" \
  "s_waitcnt lgkmcnt(4)\n\t" \
  "v_smfmac_f32_16x16x64_bf16 v[76:79], v[60:63], a[32:39], v104\n\t" \
  "ds_read_b128 v[56:59], v101 offset:13312\n\t" \
  "v_smfmac_f32_16x16x64_bf16 v[80:83], v[60:63], a[40:47], v104\n\t" \
  "ds_read_b128 v[0:3], v102 offset:0\n\t" \
  "s_waitcnt lgkmcnt(5)\n\t" \
  "v_smfmac_f32_16x16x64_bf16 v[76:79], v[64:67], a[240:247], v104\n\t" \
  "ds_read_b128 v[60:63], v101 offset:0\n\t" \
  "ds_read_b128 v[4:7], v102 offset:1024\n\t" \
  "buffer_load_dwordx4 v[36:39], v100, %[rk], s47 offen offset:1024 sc1\n\t" \
  "v_smfmac_f32_16x16x64_bf16 v[80:83], v[64:67], a[248:255], v104\n\t" \
  "ds_read_b128 v[8:11], v102 offset:2048\n\t" \
  "s_waitcnt lgkmcnt(6)\n\t" \
  "v_smfmac_f32_16x16x64_bf16 v[76:79], v[48:51], v[16:23], v104\n\t" \
  "ds_read_b128 v[12:15], v102 offset:3072\n\t" \
  "v_smfmac_f32_16x16x64_bf16 v[80:83], v[48:51], v[24:31], v104\n\t" \
  "buffer_load_dwordx4 v[40:43], v100, %[rk], s47 offen offset:2048 sc1\n\t" \
  "s_waitcnt lgkmcnt(6)\n\t" \
  "v_smfmac_f32_16x16x64_bf16 v[76:79], v[52:55], a[224:231], v104\n\t" \
  "v_smfmac_f32_16x16x64_bf16 v[80:83], v[52:55], a[232:239], v104\n\t" \
  "s_waitcnt lgkmcnt(5)\n\t" \
  "v_smfmac_f32_16x16x64_bf16 v[76:79], v[56:59], v[184:191], v104\n\t" \
  "s_waitcnt lgkmcnt(2)\n\t" \
  "v_smfmac_f32_16x16x64_bf16 v[76:79], v[60:63], v[0:7], v104\n\t" \
  "v_smfmac_f32_16x16x64_bf16 v[80:83], v[56:59], v[192:199], v104\n\t" \
  "s_waitcnt lgkmcnt(0)\n\t" \
  "v_smfmac_f32_16x16x64_bf16 v[80:83], v[60:63], v[8:15], v104\n\t" \
  "v_add_f32 v20, v76, v78\n\t" \
  "v_add_f32 v21, v77, v79\n\t" \
  "v_cvt_pk_bf16_f32 v20, v20, v21\n\t" \
  "ds_write_b32 v106, v20\n\t" \
  "s_nop 3\n\t" \
  "v_add_f32 v23, v80, v82\n\t" \
  "v_add_f32 v21, v81, v83\n\t" \
  "v_cvt_pk_bf16_f32 v21, v23, v21\n\t" \
  "ds_write_b32 v106, v21 offset:256\n\t" \
  "s_waitcnt lgkmcnt(0)\n\t" \
  "s_barrier\n\t" \
  "s_sleep 5\n\t" \
  "s_add_i32 s41, s56, 1950\n\t" \
  "s_sub_i32 s76, s92, s51\n\t" \
  "s_sub_i32 s77, s93, s53\n\t" \
  "s_cmp_eq_u32 s76, 0\n\t" \
  "s_cbranch_scc1 21f\n\t" \
  "v_cvt_f32_u32 v117, s76\n\t" \
  "v_cvt_f32_u32 v116, s77\n\t" \
  "v_rcp_f32 v117, v117\n\t" \
  "s_mov_b32 s51, s92\n\t" \
  "v_mul_f32 v117, v117, v116\n\t" \
  "v_max_f32 v117, 0x3f028f5c, v117\n\t" \
  "v_min_f32 v117, 0x3ffeb852, v117\n\t" \
  "v_rcp_f32 v116, v117\n\t" \
  "v_mul_f32 v117, 0x4f000000, v117\n\t" \
  "v_mul_f32 v116, 0x47800000, v116\n\t" \
  "v_cvt_u32_f32 v117, v117\n\t" \
  "v_cvt_u32_f32 v116, v116\n\t" \
  "s_mov_b32 s53, s93\n\t" \
  "v_readfirstlane_b32 s46, v117\n\t" \
  "v_readfirstlane_b32 s52, v116\n\t" \
  "21:\n\t" \
  "s_waitcnt vmcnt(0)\n\t" \
  "ds_write_b128 v101, v[32:35] offset:20480\n\t" \
  "ds_write_b128 v101, v[36:39] offset:21504\n\t" \
  "ds_write_b128 v101, v[40:43] offset:22528\n\t" \
  "v_readfirstlane_b32 s76, v92\n\t" \
  "s_add_i32 s77, s44, 2\n\t" \
  "s_min_u32 s77, s77, 63\n\t" \
  "s_cmp_lt_i32 s76, s77\n\t" \
  "s_cselect_b32 s42, 32, 0\n\t" \
  "ds_read_b64_tr_b16 v[0:1], v108 offset:0\n\t" \
  "ds_read_b64_tr_b16 v[2:3], v108 offset:64\n\t" \
  "ds_read_b64_tr_b16 v[4:5], v108 offset:1024\n\t" \
  "ds_read_b64_tr_b16 v[6:7], v108 offset:1088\n\t" \
  "s_lshl_b32 s76, s44, 16\n\t" \
  "s_waitcnt lgkmcnt(0)\n\t" \
  "buffer_store_dwordx4 v[0:3], v110, %[rm], s76 offen sc1\n\t" \
  "buffer_store_dwordx4 v[4:7], v110, %[rm], s76 offen offset:1024 sc1\n\t" \
  "s_add_i32 s44, s44, 1\n\t" \
  "s_and_b32 s58, s58, 4\n\t" \
  "s_and_b32 s76, s44, 64\n\t" \
  "s_or_b32 s42, s42, s76\n\t" \
  "s_cbranch_scc0 20b\n\t" \
  "s_branch 99f\n\t" \
  "14:\n\t" \
  "s_add_u32 s59, s59, 0x10000\n\t" \
  "s_cmp_lt_u32 s59, 0x100000\n\t" \
  "s_cbranch_scc1 13b\n\t" \
  "s_mov_b64 exec, -1\n\t" \
  "s_mov_b32 s42, 16\n\t" \
  "s_branch 99f\n\t" \
  "8:\n\t" \
  "s_bitcmp1_b32 s58, 1\n\t" \
  "s_cbranch_scc1 4b\n\t" \
  "s_bitcmp1_b32 s58, 2\n\t" \
  "s_cbranch_scc1 5b\n\t" \
  "v_max3_u32 v116, v32, v33, v34\n\t" \
  "v_max3_u32 v117, v35, v36, v37\n\t" \
  "v_max3_u32 v116, v116, v38, v39\n\t" \
  "v_max_u32 v116, v116, v117\n\t" \
  "v_cmp_eq_u32 vcc, -1, v116\n\t" \
  "s_cbranch_vccz 5b\n\t" \
  "s_branch 0f\n\t" \
  "9:\n\t" \
  "s_bitcmp1_b32 s58, 2\n\t" \
  "s_cbranch_scc1 6b\n\t" \
  "v_max3_u32 v116, v40, v41, v42\n\t" \
  "v_max3_u32 v117, v43, v44, v45\n\t" \
  "v_max3_u32 v116, v116, v46, v47\n\t" \
  "v_max_u32 v116, v116, v117\n\t" \
  "v_cmp_eq_u32 vcc, -1, v116\n\t" \
  "s_cbranch_vccz 6b\n\t" \
  "s_or_b32 s58, s58, 2\n\t" \
  "0:\n\t" \
  "s_or_b32 s58, s58, 1\n\t" \
  "11:\n\t" \
  "buffer_load_dwordx4 v[32:35], v100, %[rh], s40 offen sc1\n\t" \
  "buffer_load_dwordx4 v[36:39], v100, %[rh], s76 offen sc1\n\t" \
  "buffer_load_dwordx4 v[40:43], v100, %[rh], s77 offen sc1\n\t" \
  "buffer_load_dwordx4 v[44:47], v100, %[rh], s78 offen sc1\n\t" \
  "s_waitcnt lgkmcnt(0)\n\t" \
  "s_branch 10b\n\t" \
  "17:\n\t" \
  "s_add_u32 s59, s59, 1\n\t" \
  "s_bitcmp1_b32 s58, 2\n\t" \
  "s_cbranch_scc1 3b\n\t" \
  "s_waitcnt vmcnt(0)\n\t" \
  "v_max_u32 v116, v32, v33\n\t" \
  "v_max3_u32 v116, v116, v34, v35\n\t" \
  "v_max3_u32 v116, v116, v36, v37\n\t" \
  "v_max3_u32 v116, v116, v38, v39\n\t" \
  "v_max3_u32 v116, v116, v40, v41\n\t" \
  "v_max3_u32 v116, v116, v42, v43\n\t" \
  "v_max3_u32 v116, v116, v44, v45\n\t" \
  "v_max3_u32 v116, v116, v46, v47\n\t" \
  "v_cmp_eq_u32 vcc, -1, v116\n\t" \
  "s_cbranch_vccz 3b\n\t" \
  "s_bitcmp1_b32 s59, 6\n\t" \
  "s_cbranch_scc1 12f\n\t" \
  "buffer_load_dwordx4 v[32:35], v100, %[rh], s40 offen sc1\n\t" \
  "buffer_load_dwordx4 v[36:39], v100, %[rh], s76 offen sc1\n\t" \
  "buffer_load_dwordx4 v[40:43], v100, %[rh], s77 offen sc1\n\t" \
  "buffer_load_dwordx4 v[44:47], v100, %[rh], s78 offen sc1\n\t" \
  "s_branch 17b\n\t" \
  "12:\n\t" \
  "s_mov_b32 s42, s58\n\t" \
  "s_branch 99f\n\t" \
  "92:\n\t" \
  "s_mov_b32 s58, s42\n\t" \
  "20:\n\t" \
  "s_and_b32 s76, s44, 7\n\t" \
  "s_lshl_b32 s76, s76, 15\n\t" \
  "s_add_i32 s40, s54, s76\n\t" \
  "s_and_b32 s76, s44, 3\n\t" \
  "s_lshl_b32 s45, s76, 14\n\t" \
  "s_add_i32 s47, s44, 1\n\t" \
  "s_min_u32 s47, s47, 63\n\t" \
  "s_lshl_b32 s47, s47, 18\n\t" \
  "s_add_i32 s47, s47, s55\n\t" \
  "v_mov_b32 v118, 0\n\t" \
  "s_add_i32 s49, s44, -1\n\t" \
  "s_and_b32 s48, s49, 2\n\t" \
  "s_mul_i32 s48, s48, 0x6000\n\t" \
  "s_and_b32 s76, s49, 1\n\t" \
  "s_lshl_b32 s76, s76, 11\n\t" \
  "s_add_i32 s48, s48, s76\n\t" \
  "s_lshl_b32 s50, s53, 8\n\t" \
  "s_mov_b32 s59, 0\n\t" \
  "s_mov_b32 s92, s51\n\t" \
  "s_mov_b32 s93, s53\n\t" \
  "s_xor_b32 s76, s40, 0x400\n\t" \
  "s_xor_b32 s77, s40, 0x800\n\t" \
  "s_xor_b32 s78, s40, 0xc00\n\t" \
  "s_cmp_lg_u32 s58, 0\n\t" \
  "s_cbranch_scc1 11f\n\t" \
  "10:\n\t" \
  "ds_read_b128 v[48:51], v103 offset:0\n\t" \
  "ds_read_b128 v[52:55], v103 offset:1024\n\t" \
  "ds_read_b128 v[56:59], v103 offset:2048\n\t" \
  "ds_read_b128 v[60:63], v103 offset:3072\n\t" \
  "ds_read_b128 v[16:19], v103 offset:16384\n\t" \
  "ds_read_b128 v[20:23], v103 offset:17408\n\t" \
  "ds_read_b128 v[24:27], v103 offset:18432\n\t" \
  "ds_read_b128 v[28:31], v103 offset:19456\n\t" \
  "ds_read_b128 v[84:87], v103 offset:49152\n\t" \
  "ds_read_b128 v[88:91], v103 offset:50176\n\t" \
  "ds_read_b128 v[92:95], v103 offset:51200\n\t" \
  "ds_read_b128 v[96:99], v103 offset:52224\n\t" \
  "s_waitcnt lgkmcnt(4)\n\t" \
  "ds_read_b128 v[0:3], v102 offset:16384\n\t" \
  "ds_read_b128 v[4:7], v102 offset:17408\n\t" \
  "ds_read_b128 v[8:11], v102 offset:18432\n\t" \
  "ds_read_b128 v[12:15], v102 offset:19456\n\t" \
  "s_bitcmp1_b32 s58, 1\n\t" \
  "s_cselect_b64 s[80:81], -1, 0\n\t" \
  "s_cmp_lg_u32 s58, 0\n\t" \
  "s_cbranch_scc1 17f\n\t" \
  "s_mov_b64 exec, 0xffff\n\t" \
  "16:\n\t" \
  "s_memtime vcc\n\t" \
  "s_waitcnt lgkmcnt(0)\n\t" \
  "s_sub_i32 s79, s41, vcc_lo\n\t" \
  "s_cmp_gt_i32 s79, 64\n\t" \
  "s_cbranch_scc1 16b\n\t" \
  "13:\n\t" \
  "buffer_load_dword v116, v112, %[rh], s48 offen sc1\n\t" \
  "s_waitcnt vmcnt(0)\n\t" \
  "v_mov_b32 v117, 20000\n\t" \
  "v_cmp_ne_u32_sdwa vcc, v116, s49 src0_sel:BYTE_0 src1_sel:DWORD\n\t" \
  "v_subrev_u32 v116, s50, v116\n\t" \
  "v_ashrrev_i32 v116, 8, v116\n\t" \
  "s_nop 1\n\t" \
  "v_max_i32_dpp v116, v116, v116 quad_perm:[1,0,3,2] row_mask:0xf bank_mask:0xf\n\t" \
  "s_cbranch_vccnz 14f\n\t" \
  "s_nop 0\n\t" \
  "v_max_i32_dpp v116, v116, v116 quad_perm:[2,3,0,1] row_mask:0xf bank_mask:0xf\n\t" \
  "s_nop 1\n\t" \
  "v_max_i32_dpp v116, v116, v116 row_half_mirror row_mask:0xf bank_mask:0xf\n\t" \
  "s_nop 1\n\t" \
  "v_max_i32_dpp v116, v116, v116 row_mirror row_mask:0xf bank_mask:0xf\n\t" \
  "v_med3_i32 v116, v116, 0, v117\n\t" \
  "v_mul_u32_u24 v116, s52, v116\n\t" \
  "v_lshrrev_b32 v116, 16, v116\n\t" \
  "s_mov_b64 exec, -1\n\t" \
  "v_readfirstlane_b32 s84, v116\n\t" \
  "s_add_i32 s84, s84, s51\n\t" \
  "1:\n\t" \
  "s_memtime vcc\n\t" \
  "s_waitcnt lgkmcnt(0)\n\t" \
  "s_sub_i32 s79, s84, vcc_lo\n\t" \
  "s_cmp_gt_i32 s79, 128\n\t" \
  "s_cbranch_scc1 1b\n\t" \
  "2:\n\t" \
  "s_sub_i32 s79, s79, 33\n\t" \
  "s_cmp_gt_i32 s79, 56\n\t" \
  "s_cbranch_scc1 2b\n\t" \
  "buffer_load_dwordx4 v[32:35], v100, %[rh], s40 offen sc1\n\t" \
  "s_nop 12\n\t" \
  "buffer_load_dwordx4 v[36:39], v100, %[rh], s76 offen sc1\n\t" \
  "s_nop 12\n\t" \
  "buffer_load_dwordx4 v[40:43], v100, %[rh], s77 offen sc1\n\t" \
  "s_nop 12\n\t" \
  "buffer_load_dwordx4 v[44:47], v100, %[rh], s78 offen sc1\n\t" \
  "s_memtime vcc\n\t" \
  "s_memrealtime s[82:83]\n\t" \
  "s_nop 4\n\t" \
  "s_memrealtime s[84:85]\n\t" \
  "s_nop 4\n\t" \
  "s_memrealtime s[56:57]\n\t" \
  "s_nop 4\n\t" \
  "s_memrealtime s[86:87]\n\t" \
  "s_nop 4\n\t" \
  "s_memrealtime s[88:89]\n\t" \
  "s_nop 4\n\t" \
  "s_memrealtime s[90:91]\n\t" \
  "s_mov_b32 s92, 0\n\t" \
  "s_waitcnt lgkmcnt(0)\n\t" \
  "s_cmp_eq_u32 s82, s84\n\t" \
  "s_addc_u32 s92, s92, 0\n\t" \
  "s_cmp_eq_u32 s82, s56\n\t" \
  "s_addc_u32 s92, s92, 0\n\t" \
  "s_cmp_eq_u32 s82, s86\n\t" \
  "s_addc_u32 s92, s92, 0\n\t" \
  "s_cmp_eq_u32 s82, s88\n\t" \
  "s_addc_u32 s92, s92, 0\n\t" \
  "s_cmp_eq_u32 s82, s90\n\t" \
  "s_addc_u32 s92, s92, 0\n\t" \
  "s_mul_i32 s92, s92, 24\n\t" \
  "s_add_i32 s92, s92, vcc_lo\n\t" \
  "s_lshl4_add_u32 s93, s82, 64\n\t" \
  "3:\n\t" \
  "v_mov_b64 v[68:69], 0\n\t" \
  "v_mov_b64 v[70:71], 0\n\t" \
  "v_mov_b64 v[72:73], 0\n\t" \
  "v_mov_b64 v[74:75], 0\n\t" \
  "v_mov_b64 v[76:77], 0\n\t" \
  "v_mov_b64 v[78:79], 0\n\t" \
  "v_mov_b64 v[80:81], 0\n\t" \
  "v_mov_b64 v[82:83], 0\n\t" \
  "s_waitcnt lgkmcnt(0)\n\t" \
  "s_waitcnt vmcnt(3)\n\t" \
  "v_smfmac_f32_16x16x64_bf16 v[68:71], v[32:35], a[192:199], v104\n\t" \
  "ds_write_b128 v101, v[32:35] offset:8192\n\t" \
  "v_smfmac_f32_16x16x64_bf16 v[72:75], v[32:35], a[200:207], v104\n\t" \
  "v_smfmac_f32_16x16x64_bf16 v[76:79], v[32:35], v[48:55], v104\n\t" \
  "v_smfmac_f32_16x16x64_bf16 v[80:83], v[32:35], v[56:63], v104\n\t" \
  "s_waitcnt vmcnt(2)\n\t" \
  "v_smfmac_f32_16x16x64_bf16 v[68:71], v[36:39], a[208:215], v104\n\t" \
  "ds_write_b128 v101, v[36:39] offset:9216\n\t" \
  "v_smfmac_f32_16x16x64_bf16 v[72:75], v[36:39], a[216:223], v104\n\t" \
  "v_smfmac_f32_16x16x64_bf16 v[76:79], v[36:39], a[224:231], v104\n\t" \
  "v_cmp_u_f32 vcc, v68, v69\n\t" \
  "v_smfmac_f32_16x16x64_bf16 v[80:83], v[36:39], a[232:239], v104\n\t" \
  "v_cmp_u_f32 s[82:83], v70, v71\n\t" \
  "s_waitcnt vmcnt(1)\n\t" \
  "v_smfmac_f32_16x16x64_bf16 v[68:71], v[40:43], v[16:23], v104\n\t" \
  "ds_write_b128 v101, v[40:43] offset:10240\n\t" \
  "v_smfmac_f32_16x16x64_bf16 v[72:75], v[40:43], v[24:31], v104\n\t" \
  "s_or_b64 vcc, vcc, s[82:83]\n\t" \
  "s_or_b64 vcc, vcc, s[80:81]\n\t" \
  "s_waitcnt lgkmcnt(1)\n\t" \
  "v_smfmac_f32_16x16x64_bf16 v[76:79], v[40:43], a[240:247], v104\n\t" \
  "s_cbranch_scc1 8f\n\t" \
  "5:\n\t" \
  "s_barrier\n\t" \
  "4:\n\t" \
  "s_memtime s[56:57]\n\t" \
  "ds_read_b128 v[48:51], v101 offset:12288\n\t" \
  "v_smfmac_f32_16x16x64_bf16 v[80:83], v[40:43], a[248:255], v104\n\t" \
  "ds_read_b128 v[52:55], v101 offset:13312\n\t" \
  "ds_read_b128 v[56:59], v101 offset:0\n\t" \
  "s_waitcnt vmcnt(0)\n\t" \
  "v_smfmac_f32_16x16x64_bf16 v[68:71], v[44:47], v[120:127], v104\n\t" \
  "v_smfmac_f32_16x16x64_bf16 v[72:75], v[44:47], v[128:135], v104\n\t" \
  "ds_write_b128 v101, v[44:47] offset:11264\n\t" \
  "v_smfmac_f32_16x16x64_bf16 v[76:79], v[44:47], v[136:143], v104\n\t" \
  "v_cmp_u_f32 vcc, v68, v69\n\t" \
  "v_smfmac_f32_16x16x64_bf16 v[80:83], v[44:47], v[144:151], v104\n\t" \
  "v_cmp_u_f32 s[82:83], v70, v71\n\t" \
  "s_waitcnt lgkmcnt(3)\n\t" \
  "v_smfmac_f32_16x16x64_bf16 v[68:71], v[48:51], v[152:159], v104\n\t" \
  "ds_read_b128 v[60:63], v101 offset:1024\n\t" \
  "ds_read_b128 v[64:67], v101 offset:4096\n\t" \
  "v_smfmac_f32_16x16x64_bf16 v[72:75], v[48:51], v[160:167], v104\n\t" \
  "s_or_b64 vcc, vcc, s[82:83]\n\t" \
  "s_waitcnt lgkmcnt(2)\n\t" \
  "v_smfmac_f32_16x16x64_bf16 v[68:71], v[52:55], v[168:175], v104\n\t" \
  "s_cbranch_scc1 9f\n\t" \
  "6:\n\t" \
  "s_barrier\n\t" \
  "v_smfmac_f32_16x16x64_bf16 v[72:75], v[52:55], v[176:183], v104\n\t" \
  "ds_read_b128 v[48:51], v101 offset:5120\n\t" \
  "v_smfmac_f32_16x16x64_bf16 v[68:71], v[56:59], a[0:7], v104\n\t" \
  "ds_read_b128 v[52:55], v101 offset:14336\n\t" \
  "v_smfmac_f32_16x16x64_bf16 v[72:75], v[56:59], a[8:15], v104\n\t" \
  "s_waitcnt lgkmcnt(3)\n\t" \
  "v_smfmac_f32_16x16x64_bf16 v[68:71], v[60:63], a[16:23], v104\n\t" \
  "ds_read_b128 v[56:59], v101 offset:15360\n\t" \
  "v_smfmac_f32_16x16x64_bf16 v[72:75], v[60:63], a[24:31], v104\n\t" \
  "s_waitcnt lgkmcnt(3)\n\t" \
  "v_smfmac_f32_16x16x64_bf16 v[68:71], v[64:67], a[96:103], v104\n\t" \
  "ds_read_b128 v[60:63], v101 offset:2048\n\t" \
  "v_smfmac_f32_16x16x64_bf16 v[72:75], v[64:67], a[104:111], v104\n\t" \
  "s_waitcnt lgkmcnt(3)\n\t" \
  "v_smfmac_f32_16x16x64_bf16 v[68:71], v[48:51], a[112:119], v104\n\t" \
  "ds_read_b128 v[64:67], v101 offset:3072\n\t" \
  "ds_read_b128 v[16:19], v102 offset:49152\n\t" \
  "v_smfmac_f32_16x16x64_bf16 v[72:75], v[48:51], a[120:127], v104\n\t" \
  "s_waitcnt lgkmcnt(4)\n\t" \
  "v_smfmac_f32_16x16x64_bf16 v[68:71], v[52:55], v[84:91], v104\n\t" \
  "ds_read_b128 v[48:51], v101 offset:7168\n\t" \
  "ds_read_b128 v[20:23], v102 offset:50176\n\t" \
  "v_smfmac_f32_16x16x64_bf16 v[72:75], v[52:55], v[92:99], v104\n\t" \
  "s_waitcnt lgkmcnt(5)\n\t" \
  "v_smfmac_f32_16x16x64_bf16 v[68:71], v[56:59], v[216:223], v104\n\t" \
  "ds_read_b128 v[52:55], v101 offset:6144\n\t" \
  "ds_read_b128 v[24:27], v102 offset:51200\n\t" \
  "v_smfmac_f32_16x16x64_bf16 v[72:75], v[56:59], v[224:231], v104\n\t" \
  "ds_read_b128 v[28:31], v102 offset:52224\n\t" \
  "s_waitcnt lgkmcnt(7)\n\t" \
  "v_smfmac_f32_16x16x64_bf16 v[68:71], v[60:63], v[0:7], v104\n\t" \
  "v_smfmac_f32_16x16x64_bf16 v[72:75], v[60:63], v[8:15], v104\n\t" \
  "global_load_dword v92, v118, %[kc] sc1\n\t" \
  "s_waitcnt lgkmcnt(6)\n\t" \
  "v_smfmac_f32_16x16x64_bf16 v[68:71], v[64:67], a[64:71], v104\n\t" \
  "ds_read_b128 v[0:3], v103 offset:32768\n\t" \
  "v_smfmac_f32_16x16x64_bf16 v[72:75], v[64:67], a[72:79], v104\n\t" \
  "ds_read_b128 v[4:7], v103 offset:33792\n\t" \
  "s_waitcnt lgkmcnt(6)\n\t" \
  "v_smfmac_f32_16x16x64_bf16 v[68:71], v[48:51], a[160:167], v104\n\t" \
  "s_waitcnt lgkmcnt(4)\n\t" \
  "v_smfmac_f32_16x16x64_bf16 v[68:71], v[52:55], v[16:23], v104\n\t" \
  "ds_read_b128 v[8:11], v103 offset:34816\n\t" \
  "v_smfmac_f32_16x16x64_bf16 v[72:75], v[48:51], a[168:175], v104\n\t" \
  "ds_read_b128 v[12:15], v103 offset:35840\n\t" \
  "s_waitcnt lgkmcnt(4)\n\t" \
  "v_smfmac_f32_16x16x64_bf16 v[72:75], v[52:55], v[24:31], v104\n\t" \
  "ds_read_b128 v[16:19], v102 offset:0\n\t" \
  "v_add_f32 v96, v68, v70\n\t" \
  "v_smfmac_f32_16x16x64_bf16 v[76:79], v[56:59], v[232:239], v104\n\t" \
  "ds_read_b128 v[20:23], v102 offset:1024\n\t" \
  "v_add_f32 v97, v69, v71\n\t" \
  "v_smfmac_f32_16x16x64_bf16 v[80:83], v[56:59], v[240:247], v104\n\t" \
  "v_add_f32 v98, v72, v74\n\t" \
  "v_cvt_pk_bf16_f32 v96, v96, v97\n\t" \
  "v_smfmac_f32_16x16x64_bf16 v[76:79], v[60:63], a[48:55], v104\n\t" \
  "ds_read_b128 v[56:59], v101 offset:12288\n\t" \
  "v_add_f32 v99, v73, v75\n\t" \
  "v_smfmac_f32_16x16x64_bf16 v[80:83], v[60:63], a[56:63], v104\n\t" \
  "ds_read_b128 v[24:27], v102 offset:2048\n\t" \
  "v_cvt_pk_bf16_f32 v98, v98, v99\n\t" \
  "v_smfmac_f32_16x16x64_bf16 v[76:79], v[64:67], a[80:87], v104\n\t" \
  "ds_write2st64_b32 v105, v96, v98 offset1:1\n\t" \
  "v_smfmac_f32_16x16x64_bf16 v[80:83], v[64:67], a[88:95], v104\n\t" \
  "ds_read_b128 v[60:63], v101 offset:5120\n\t" \
  "v_smfmac_f32_16x16x64_bf16 v[76:79], v[48:51], a[176:183], v104\n\t" \
  "ds_read_b128 v[64:67], v101 offset:14336\n\t" \
  "v_smfmac_f32_16x16x64_bf16 v[80:83], v[48:51], a[184:191], v104\n\t" \
  "ds_read_b128 v[28:31], v102 offset:3072\n\t" \
  "v_smfmac_f32_16x16x64_bf16 v[76:79], v[52:55], a[144:151], v104\n\t" \
  "ds_read_b128 v[48:51], v101 offset:0\n\t" \
  "v_smfmac_f32_16x16x64_bf16 v[80:83], v[52:55], a[152:159], v104\n\t" \
  "s_waitcnt lgkmcnt(4)\n\t" \
  "s_barrier\n\t" \
  "v_smfmac_f32_16x16x64_bf16 v[76:79], v[56:59], v[0:7], v104\n\t" \
  "ds_read_b128 v[52:55], v101 offset:13312\n\t" \
  "v_smfmac_f32_16x16x64_bf16 v[80:83], v[56:59], v[8:15], v104\n\t" \
  "s_waitcnt lgkmcnt(4)\n\t" \
  "v_smfmac_f32_16x16x64_bf16 v[76:79], v[60:63], a[128:135], v104\n\t" \
  "ds_read_b128 v[56:59], v101 offset:1024\n\t" \
  "buffer_load_dwordx4 v[32:35], v100, %[rk], s47 offen offset:0 sc1\n\t" \
  "v_smfmac_f32_16x16x64_bf16 v[80:83], v[60:63], a[136:143], v104\n\t" \
  "ds_read_b128 v[0:3], v102 offset:32768\n\t" \
  "s_waitcnt lgkmcnt(5)\n\t" \
  "v_smfmac_f32_16x16x64_bf16 v[76:79], v[64:67], v[200:207], v104\n\t" \
  "ds_read_b128 v[60:63], v101 offset:4096\n\t" \
  "ds_read_b128 v[4:7], v102 offset:33792\n\t" \
  "v_smfmac_f32_16x16x64_bf16 v[80:83], v[64:67], v[208:215], v104\n\t" \
  "ds_read_b128 v[8:11], v102 offset:34816\n\t" \
  "buffer_load_dwordx4 v[36:39], v100, %[rk], s47 offen offset:1024 sc1\n\t" \
  "s_waitcnt lgkmcnt(6)\n\t" \
  "v_smfmac_f32_16x16x64_bf16 v[76:79], v[48:51], v[16:23], v104\n\t" \
  "ds_read_b128 v[12:15], v102 offset:35840\n\t" \
  "v_smfmac_f32_16x16x64_bf16 v[80:83], v[48:51], v[24:31], v104\n\t" \
  "s_waitcnt lgkmcnt(6)\n\t" \
  "v_smfmac_f32_16x16x64_bf16 v[76:79], v[52:55], v[184:191], v104\n\t" \
  "buffer_load_dwordx4 v[40:43], v100, %[rk], s47 offen offset:2048 sc1\n\t" \
  "v_smfmac_f32_16x16x64_bf16 v[80:83], v[52:55], v[192:199], v104\n\t" \
  "s_waitcnt lgkmcnt(5)\n\t" \
  "v_smfmac_f32_16x16x64_bf16 v[76:79], v[56:59], a[32:39], v104\n\t" \
  "s_waitcnt lgkmcnt(2)\n\t" \
  "v_smfmac_f32_16x16x64_bf16 v[76:79], v[60:63], v[0:7], v104\n\t" \
  "v_smfmac_f32_16x16x64_bf16 v[80:83], v[56:59], a[40:47], v104\n\t" \
  "s_waitcnt lgkmcnt(0)\n\t" \
  "v_smfmac_f32_16x16x64_bf16 v[80:83], v[60:63], v[8:15], v104\n\t" \
  "v_add_f32 v20, v76, v78\n\t" \
  "v_add_f32 v21, v77, v79\n\t" \
  "v_cvt_pk_bf16_f32 v20, v20, v21\n\t" \
  "ds_write_b32 v106, v20\n\t" \
  "s_nop 3\n\t" \
  "v_add_f32 v23, v80, v82\n\t" \
  "v_add_f32 v21, v81, v83\n\t" \
  "v_cvt_pk_bf16_f32 v21, v23, v21\n\t" \
  "ds_write_b32 v106, v21 offset:256\n\t" \
  "s_waitcnt lgkmcnt(0)\n\t" \
  "s_barrier\n\t" \
  "s_sleep 5\n\t" \
  "s_add_i32 s41, s56, 1950\n\t" \
  "s_sub_i32 s76, s92, s51\n\t" \
  "s_sub_i32 s77, s93, s53\n\t" \
  "s_cmp_eq_u32 s76, 0\n\t" \
  "s_cbranch_scc1 21f\n\t" \
  "v_cvt_f32_u32 v117, s76\n\t" \
  "v_cvt_f32_u32 v116, s77\n\t" \
  "v_rcp_f32 v117, v117\n\t" \
  "s_mov_b32 s51, s92\n\t" \
  "v_mul_f32 v117, v117, v116\n\t" \
  "v_max_f32 v117, 0x3f028f5c, v117\n\t" \
  "v_min_f32 v117, 0x3ffeb852, v117\n\t" \
  "v_rcp_f32 v116, v117\n\t" \
  "v_mul_f32 v117, 0x4f000000, v117\n\t" \
  "v_mul_f32 v116, 0x47800000, v116\n\t" \
  "v_cvt_u32_f32 v117, v117\n\t" \
  "v_cvt_u32_f32 v116, v116\n\t" \
  "s_mov_b32 s53, s93\n\t" \
  "v_readfirstlane_b32 s46, v117\n\t" \
  "v_readfirstlane_b32 s52, v116\n\t" \
  "21:\n\t" \
  "s_waitcnt vmcnt(0)\n\t" \
  "ds_write_b128 v101, v[32:35] offset:23552\n\t" \
  "ds_write_b128 v101, v[36:39] offset:24576\n\t" \
  "ds_write_b128 v101, v[40:43] offset:25600\n\t" \
  "v_readfirstlane_b32 s76, v92\n\t" \
  "s_add_i32 s77, s44, 2\n\t" \
  "s_min_u32 s77, s77, 63\n\t" \
  "s_cmp_lt_i32 s76, s77\n\t" \
  "s_cselect_b32 s42, 32, 0\n\t" \
  "v_mov_b32 v0, -1\n\t" \
  "v_mov_b32 v1, -1\n\t" \
  "v_mov_b32 v2, -1\n\t" \
  "v_mov_b32 v3, -1\n\t" \
  "s_and_b32 s76, s49, 3\n\t" \
  "s_mul_i32 s76, s76, 0xc000\n\t" \
  "s_add_i32 s76, s76, 0x40000\n\t" \
  "buffer_store_dwordx4 v[0:3], v110, %[rh], s76 offen sc1\n\t" \
  "s_add_i32 s44, s44, 1\n\t" \
  "s_and_b32 s58, s58, 4\n\t" \
  "s_and_b32 s76, s44, 64\n\t" \
  "s_or_b32 s42, s42, s76\n\t" \
  "s_cbranch_scc0 20b\n\t" \
  "s_branch 99f\n\t" \
  "14:\n\t" \
  "s_add_u32 s59, s59, 0x10000\n\t" \
  "s_cmp_lt_u32 s59, 0x100000\n\t" \
  "s_cbranch_scc1 13b\n\t" \
  "s_mov_b64 exec, -1\n\t" \
  "s_mov_b32 s42, 16\n\t" \
  "s_branch 99f\n\t" \
  "8:\n\t" \
  "s_bitcmp1_b32 s58, 1\n\t" \
  "s_cbranch_scc1 4b\n\t" \
  "s_bitcmp1_b32 s58, 2\n\t" \
  "s_cbranch_scc1 5b\n\t" \
  "v_max3_u32 v116, v32, v33, v34\n\t" \
  "v_max3_u32 v117, v35, v36, v37\n\t" \
  "v_max3_u32 v116, v116, v38, v39\n\t" \
  "v_max_u32 v116, v116, v117\n\t" \
  "v_cmp_eq_u32 vcc, -1, v116\n\t" \
  "s_cbranch_vccz 5b\n\t" \
  "s_branch 0f\n\t" \
  "9:\n\t" \
  "s_bitcmp1_b32 s58, 2\n\t" \
  "s_cbranch_scc1 6b\n\t" \
  "v_max3_u32 v116, v40, v41, v42\n\t" \
  "v_max3_u32 v117, v43, v44, v45\n\t" \
  "v_max3_u32 v116, v116, v46, v47\n\t" \
  "v_max_u32 v116, v116, v117\n\t" \
  "v_cmp_eq_u32 vcc, -1, v116\n\t" \
  "s_cbranch_vccz 6b\n\t" \
  "s_or_b32 s58, s58, 2\n\t" \
  "0:\n\t" \
  "s_or_b32 s58, s58, 1\n\t" \
  "11:\n\t" \
  "buffer_load_dwordx4 v[32:35], v100, %[rh], s40 offen sc1\n\t" \
  "buffer_load_dwordx4 v[36:39], v100, %[rh], s76 offen sc1\n\t" \
  "buffer_load_dwordx4 v[40:43], v100, %[rh], s77 offen sc1\n\t" \
  "buffer_load_dwordx4 v[44:47], v100, %[rh], s78 offen sc1\n\t" \
  "s_waitcnt lgkmcnt(0)\n\t" \
  "s_branch 10b\n\t" \
  "17:\n\t" \
  "s_add_u32 s59, s59, 1\n\t" \
  "s_bitcmp1_b32 s58, 2\n\t" \
  "s_cbranch_scc1 3b\n\t" \
  "s_waitcnt vmcnt(0)\n\t" \
  "v_max_u32 v116, v32, v33\n\t" \
  "v_max3_u32 v116, v116, v34, v35\n\t" \
  "v_max3_u32 v116, v116, v36, v37\n\t" \
  "v_max3_u32 v116, v116, v38, v39\n\t" \
  "v_max3_u32 v116, v116, v40, v41\n\t" \
  "v_max3_u32 v116, v116, v42, v43\n\t" \
  "v_max3_u32 v116, v116, v44, v45\n\t" \
  "v_max3_u32 v116, v116, v46, v47\n\t" \
  "v_cmp_eq_u32 vcc, -1, v116\n\t" \
  "s_cbranch_vccz 3b\n\t" \
  "s_bitcmp1_b32 s59, 6\n\t" \
  "s_cbranch_scc1 12f\n\t" \
  "buffer_load_dwordx4 v[32:35], v100, %[rh], s40 offen sc1\n\t" \
  "buffer_load_dwordx4 v[36:39], v100, %[rh], s76 offen sc1\n\t" \
  "buffer_load_dwordx4 v[40:43], v100, %[rh], s77 offen sc1\n\t" \
  "buffer_load_dwordx4 v[44:47], v100, %[rh], s78 offen sc1\n\t" \
  "s_branch 17b\n\t" \
  "12:\n\t" \
  "s_mov_b32 s42, s58\n\t" \
  "s_branch 99f\n\t" \
  "93:\n\t" \
  "s_mov_b32 s58, s42\n\t" \
  "20:\n\t" \
  "s_and_b32 s76, s44, 7\n\t" \
  "s_lshl_b32 s76, s76, 15\n\t" \
  "s_add_i32 s40, s54, s76\n\t" \
  "s_and_b32 s76, s44, 3\n\t" \
  "s_lshl_b32 s45, s76, 14\n\t" \
  "s_add_i32 s47, s44, 1\n\t" \
  "s_min_u32 s47, s47, 63\n\t" \
  "s_lshl_b32 s47, s47, 18\n\t" \
  "s_add_i32 s47, s47, s55\n\t" \
  "v_mov_b32 v118, 0\n\t" \
  "s_add_i32 s49, s44, -1\n\t" \
  "s_and_b32 s48, s49, 2\n\t" \
  "s_mul_i32 s48, s48, 0x6000\n\t" \
  "s_and_b32 s76, s49, 1\n\t" \
  "s_lshl_b32 s76, s76, 11\n\t" \
  "s_add_i32 s48, s48, s76\n\t" \
  "s_lshl_b32 s50, s53, 8\n\t" \
  "s_mov_b32 s59, 0\n\t" \
  "s_mov_b32 s92, s51\n\t" \
  "s_mov_b32 s93, s53\n\t" \
  "s_xor_b32 s76, s40, 0x400\n\t" \
  "s_xor_b32 s77, s40, 0x800\n\t" \
  "s_xor_b32 s78, s40, 0xc00\n\t" \
  "s_cmp_lg_u32 s58, 0\n\t" \
  "s_cbranch_scc1 11f\n\t" \
  "10:\n\t" \
  "ds_read_b128 v[48:51], v103 offset:32768\n\t" \
  "ds_read_b128 v[52:55], v103 offset:33792\n\t" \
  "ds_read_b128 v[56:59], v103 offset:34816\n\t" \
  "ds_read_b128 v[60:63], v103 offset:35840\n\t" \
  "ds_read_b128 v[16:19], v103 offset:49152\n\t" \
  "ds_read_b128 v[20:23], v103 offset:50176\n\t" \
  "ds_read_b128 v[24:27], v103 offset:51200\n\t" \
  "ds_read_b128 v[28:31], v103 offset:52224\n\t" \
  "ds_read_b128 v[84:87], v102 offset:16384\n\t" \
  "ds_read_b128 v[88:91], v102 offset:17408\n\t" \
  "ds_read_b128 v[92:95], v102 offset:18432\n\t" \
  "ds_read_b128 v[96:99], v102 offset:19456\n\t" \
  "s_waitcnt lgkmcnt(4)\n\t" \
  "ds_read_b128 v[0:3], v102 offset:49152\n\t" \
  "ds_read_b128 v[4:7], v102 offset:50176\n\t" \
  "ds_read_b128 v[8:11], v102 offset:51200\n\t" \
  "ds_read_b128 v[12:15], v102 offset:52224\n\t" \
  "s_bitcmp1_b32 s58, 1\n\t" \
  "s_cselect_b64 s[80:81], -1, 0\n\t" \
  "s_cmp_lg_u32 s58, 0\n\t" \
  "s_cbranch_scc1 17f\n\t" \
  "s_mov_b64 exec, 0xffff\n\t" \
  "16:\n\t" \
  "s_memtime vcc\n\t" \
  "s_waitcnt lgkmcnt(0)\n\t" \
  "s_sub_i32 s79, s41, vcc_lo\n\t" \
  "s_cmp_gt_i32 s79, 64\n\t" \
  "s_cbranch_scc1 16b\n\t" \
  "13:\n\t" \
  "buffer_load_dword v116, v112, %[rh], s48 offen sc1\n\t" \
  "s_waitcnt vmcnt(0)\n\t" \
  "v_mov_b32 v117, 20000\n\t" \
  "v_cmp_ne_u32_sdwa vcc, v116, s49 src0_sel:BYTE_0 src1_sel:DWORD\n\t" \
  "v_subrev_u32 v116, s50, v116\n\t" \
  "v_ashrrev_i32 v116, 8, v116\n\t" \
  "s_nop 1\n\t" \
  "v_max_i32_dpp v116, v116, v116 quad_perm:[1,0,3,2] row_mask:0xf bank_mask:0xf\n\t" \
  "s_cbranch_vccnz 14f\n\t" \
  "s_nop 0\n\t" \
  "v_max_i32_dpp v116, v116, v116 quad_perm:[2,3,0,1] row_mask:0xf bank_mask:0xf\n\t" \
  "s_nop 1\n\t" \
  "v_max_i32_dpp v116, v116, v116 row_half_mirror row_mask:0xf bank_mask:0xf\n\t" \
  "s_nop 1\n\t" \
  "v_max_i32_dpp v116, v116, v116 row_mirror row_mask:0xf bank_mask:0xf\n\t" \
  "v_med3_i32 v116, v116, 0, v117\n\t" \
  "v_mul_u32_u24 v116, s52, v116\n\t" \
  "v_lshrrev_b32 v116, 16, v116\n\t" \
  "s_mov_b64 exec, -1\n\t" \
  "v_readfirstlane_b32 s84, v116\n\t" \
  "s_add_i32 s84, s84, s51\n\t" \
  "1:\n\t" \
  "s_memtime vcc\n\t" \
  "s_waitcnt lgkmcnt(0)\n\t" \
  "s_sub_i32 s79, s84, vcc_lo\n\t" \
  "s_cmp_gt_i32 s79, 128\n\t" \
  "s_cbranch_scc1 1b\n\t" \
  "2:\n\t" \
  "s_sub_i32 s79, s79, 33\n\t" \
  "s_cmp_gt_i32 s79, 56\n\t" \
  "s_cbranch_scc1 2b\n\t" \
  "buffer_load_dwordx4 v[32:35], v100, %[rh], s40 offen sc1\n\t" \
  "s_nop 12\n\t" \
  "buffer_load_dwordx4 v[36:39], v100, %[rh], s76 offen sc1\n\t" \
  "s_nop 12\n\t" \
  "buffer_load_dwordx4 v[40:43], v100, %[rh], s77 offen sc1\n\t" \
  "s_nop 12\n\t" \
  "buffer_load_dwordx4 v[44:47], v100, %[rh], s78 offen sc1\n\t" \
  "s_memtime vcc\n\t" \
  "s_memrealtime s[82:83]\n\t" \
  "s_nop 4\n\t" \
  "s_memrealtime s[84:85]\n\t" \
  "s_nop 4\n\t" \
  "s_memrealtime s[56:57]\n\t" \
  "s_nop 4\n\t" \
  "s_memrealtime s[86:87]\n\t" \
  "s_nop 4\n\t" \
  "s_memrealtime s[88:89]\n\t" \
  "s_nop 4\n\t" \
  "s_memrealtime s[90:91]\n\t" \
  "s_mov_b32 s92, 0\n\t" \
  "s_waitcnt lgkmcnt(0)\n\t" \
  "s_cmp_eq_u32 s82, s84\n\t" \
  "s_addc_u32 s92, s92, 0\n\t" \
  "s_cmp_eq_u32 s82, s56\n\t" \
  "s_addc_u32 s92, s92, 0\n\t" \
  "s_cmp_eq_u32 s82, s86\n\t" \
  "s_addc_u32 s92, s92, 0\n\t" \
  "s_cmp_eq_u32 s82, s88\n\t" \
  "s_addc_u32 s92, s92, 0\n\t" \
  "s_cmp_eq_u32 s82, s90\n\t" \
  "s_addc_u32 s92, s92, 0\n\t" \
  "s_mul_i32 s92, s92, 24\n\t" \
  "s_add_i32 s92, s92, vcc_lo\n\t" \
  "s_lshl4_add_u32 s93, s82, 64\n\t" \
  "3:\n\t" \
  "v_mov_b64 v[68:69], 0\n\t" \
  "v_mov_b64 v[70:71], 0\n\t" \
  "v_mov_b64 v[72:73], 0\n\t" \
  "v_mov_b64 v[74:75], 0\n\t" \
  "v_mov_b64 v[76:77], 0\n\t" \
  "v_mov_b64 v[78:79], 0\n\t" \
  "v_mov_b64 v[80:81], 0\n\t" \
  "v_mov_b64 v[82:83], 0\n\t" \
  "s_waitcnt lgkmcnt(0)\n\t" \
  "s_waitcnt vmcnt(3)\n\t" \
  "v_smfmac_f32_16x16x64_bf16 v[68:71], v[32:35], v[152:159], v104\n\t" \
  "ds_write_b128 v101, v[32:35] offset:12288\n\t" \
  "v_smfmac_f32_16x16x64_bf16 v[72:75], v[32:35], v[160:167], v104\n\t" \
  "v_smfmac_f32_16x16x64_bf16 v[76:79], v[32:35], v[48:55], v104\n\t" \
  "v_smfmac_f32_16x16x64_bf16 v[80:83], v[32:35], v[56:63], v104\n\t" \
  "s_waitcnt vmcnt(2)\n\t" \
  "v_smfmac_f32_16x16x64_bf16 v[68:71], v[36:39], v[168:175], v104\n\t" \
  "ds_write_b128 v101, v[36:39] offset:13312\n\t" \
  "v_smfmac_f32_16x16x64_bf16 v[72:75], v[36:39], v[176:183], v104\n\t" \
  "v_smfmac_f32_16x16x64_bf16 v[76:79], v[36:39], v[184:191], v104\n\t" \
  "v_cmp_u_f32 vcc, v68, v69\n\t" \
  "v_smfmac_f32_16x16x64_bf16 v[80:83], v[36:39], v[192:199], v104\n\t" \
  "v_cmp_u_f32 s[82:83], v70, v71\n\t" \
  "s_waitcnt vmcnt(1)\n\t" \
  "v_smfmac_f32_16x16x64_bf16 v[68:71], v[40:43], v[16:23], v104\n\t" \
  "ds_write_b128 v101, v[40:43] offset:14336\n\t" \
  "v_smfmac_f32_16x16x64_bf16 v[72:75], v[40:43], v[24:31], v104\n\t" \
  "s_or_b64 vcc, vcc, s[82:83]\n\t" \
  "s_or_b64 vcc, vcc, s[80:81]\n\t" \
  "s_waitcnt lgkmcnt(1)\n\t" \
  "v_smfmac_f32_16x16x64_bf16 v[76:79], v[40:43], v[200:207], v104\n\t" \
  "s_cbranch_scc1 8f\n\t" \
  "5:\n\t" \
  "s_barrier\n\t" \
  "4:\n\t" \
  "s_memtime s[56:57]\n\t" \
  "ds_read_b128 v[48:51], v101 offset:0\n\t" \
  "v_smfmac_f32_16x16x64_bf16 v[80:83], v[40:43], v[208:215], v104\n\t" \
  "ds_read_b128 v[52:55], v101 offset:1024\n\t" \
  "ds_read_b128 v[56:59], v101 offset:4096\n\t" \
  "s_waitcnt vmcnt(0)\n\t" \
  "v_smfmac_f32_16x16x64_bf16 v[68:71], v[44:47], v[216:223], v104\n\t" \
  "v_smfmac_f32_16x16x64_bf16 v[72:75], v[44:47], v[224:231], v104\n\t" \
  "ds_write_b128 v101, v[44:47] offset:15360\n\t" \
  "v_smfmac_f32_16x16x64_bf16 v[76:79], v[44:47], v[232:239], v104\n\t" \
  "v_cmp_u_f32 vcc, v68, v69\n\t" \
  "v_smfmac_f32_16x16x64_bf16 v[80:83], v[44:47], v[240:247], v104\n\t" \
  "v_cmp_u_f32 s[82:83], v70, v71\n\t" \
  "s_waitcnt lgkmcnt(3)\n\t" \
  "v_smfmac_f32_16x16x64_bf16 v[68:71], v[48:51], a[0:7], v104\n\t" \
  "ds_read_b128 v[60:63], v101 offset:5120\n\t" \
  "ds_read_b128 v[64:67], v101 offset:8192\n\t" \
  "v_smfmac_f32_16x16x64_bf16 v[72:75], v[48:51], a[8:15], v104\n\t" \
  "s_or_b64 vcc, vcc, s[82:83]\n\t" \
  "s_waitcnt lgkmcnt(2)\n\t" \
  "v_smfmac_f32_16x16x64_bf16 v[68:71], v[52:55], a[16:23], v104\n\t" \
  "s_cbranch_scc1 9f\n\t" \
  "6:\n\t" \
  "s_barrier\n\t" \
  "v_smfmac_f32_16x16x64_bf16 v[72:75], v[52:55], a[24:31], v104\n\t" \
  "ds_read_b128 v[48:51], v101 offset:9216\n\t" \
  "v_smfmac_f32_16x16x64_bf16 v[68:71], v[56:59], a[96:103], v104\n\t" \
  "ds_read_b128 v[52:55], v101 offset:2048\n\t" \
  "v_smfmac_f32_16x16x64_bf16 v[72:75], v[56:59], a[104:111], v104\n\t" \
  "s_waitcnt lgkmcnt(3)\n\t" \
  "v_smfmac_f32_16x16x64_bf16 v[68:71], v[60:63], a[112:119], v104\n\t" \
  "ds_read_b128 v[56:59], v101 offset:3072\n\t" \
  "v_smfmac_f32_16x16x64_bf16 v[72:75], v[60:63], a[120:127], v104\n\t" \
  "s_waitcnt lgkmcnt(3)\n\t" \
  "v_smfmac_f32_16x16x64_bf16 v[68:71], v[64:67], a[192:199], v104\n\t" \
  "ds_read_b128 v[60:63], v101 offset:6144\n\t" \
  "v_smfmac_f32_16x16x64_bf16 v[72:75], v[64:67], a[200:207], v104\n\t" \
  "s_waitcnt lgkmcnt(3)\n\t" \
  "v_smfmac_f32_16x16x64_bf16 v[68:71], v[48:51], a[208:215], v104\n\t" \
  "ds_read_b128 v[64:67], v101 offset:7168\n\t" \
  "ds_read_b128 v[16:19], v103 offset:16384\n\t" \
  "v_smfmac_f32_16x16x64_bf16 v[72:75], v[48:51], a[216:223], v104\n\t" \
  "s_waitcnt lgkmcnt(4)\n\t" \
  "v_smfmac_f32_16x16x64_bf16 v[68:71], v[52:55], v[84:91], v104\n\t" \
  "ds_read_b128 v[48:51], v101 offset:11264\n\t" \
  "ds_read_b128 v[20:23], v103 offset:17408\n\t" \
  "v_smfmac_f32_16x16x64_bf16 v[72:75], v[52:55], v[92:99], v104\n\t" \
  "s_waitcnt lgkmcnt(5)\n\t" \
  "v_smfmac_f32_16x16x64_bf16 v[68:71], v[56:59], a[64:71], v104\n\t" \
  "ds_read_b128 v[52:55], v101 offset:10240\n\t" \
  "ds_read_b128 v[24:27], v103 offset:18432\n\t" \
  "v_smfmac_f32_16x16x64_bf16 v[72:75], v[56:59], a[72:79], v104\n\t" \
  "ds_read_b128 v[28:31], v103 offset:19456\n\t" \
  "s_waitcnt lgkmcnt(7)\n\t" \
  "v_smfmac_f32_16x16x64_bf16 v[68:71], v[60:63], v[0:7], v104\n\t" \
  "v_smfmac_f32_16x16x64_bf16 v[72:75], v[60:63], v[8:15], v104\n\t" \
  "s_waitcnt lgkmcnt(6)\n\t" \
  "v_smfmac_f32_16x16x64_bf16 v[68:71], v[64:67], a[160:167], v104\n\t" \
  "ds_read_b128 v[0:3], v102 offset:0\n\t" \
  "global_load_dword v92, v118, %[kc] sc1\n\t" \
  "v_smfmac_f32_16x16x64_bf16 v[72:75], v[64:67], a[168:175], v104\n\t" \
  "ds_read_b128 v[4:7], v102 offset:1024\n\t" \
  "s_waitcnt lgkmcnt(6)\n\t" \
  "v_smfmac_f32_16x16x64_bf16 v[68:71], v[48:51], v[120:127], v104\n\t" \
  "s_waitcnt lgkmcnt(4)\n\t" \
  "v_smfmac_f32_16x16x64_bf16 v[68:71], v[52:55], v[16:23], v104\n\t" \
  "ds_read_b128 v[8:11], v102 offset:2048\n\t" \
  "v_smfmac_f32_16x16x64_bf16 v[72:75], v[48:51], v[128:135], v104\n\t" \
  "ds_read_b128 v[12:15], v102 offset:3072\n\t" \
  "s_waitcnt lgkmcnt(4)\n\t" \
  "v_smfmac_f32_16x16x64_bf16 v[72:75], v[52:55], v[24:31], v104\n\t" \
  "ds_read_b128 v[16:19], v102 offset:32768\n\t" \
  "v_add_f32 v96, v68, v70\n\t" \
  "v_smfmac_f32_16x16x64_bf16 v[76:79], v[56:59], a[80:87], v104\n\t" \
  "ds_read_b128 v[20:23], v102 offset:33792\n\t" \
  "v_add_f32 v97, v69, v71\n\t" \
  "v_smfmac_f32_16x16x64_bf16 v[80:83], v[56:59], a[88:95], v104\n\t" \
  "v_add_f32 v98, v72, v74\n\t" \
  "v_cvt_pk_bf16_f32 v96, v96, v97\n\t" \
  "v_smfmac_f32_16x16x64_bf16 v[76:79], v[60:63], a[144:151], v104\n\t" \
  "ds_read_b128 v[56:59], v101 offset:0\n\t" \
  "v_add_f32 v99, v73, v75\n\t" \
  "v_smfmac_f32_16x16x64_bf16 v[80:83], v[60:63], a[152:159], v104\n\t" \
  "ds_read_b128 v[24:27], v102 offset:34816\n\t" \
  "v_cvt_pk_bf16_f32 v98, v98, v99\n\t" \
  "v_smfmac_f32_16x16x64_bf16 v[76:79], v[64:67], a[176:183], v104\n\t" \
  "ds_write2st64_b32 v105, v96, v98 offset1:1\n\t" \
  "v_smfmac_f32_16x16x64_bf16 v[80:83], v[64:67], a[184:191], v104\n\t" \
  "ds_read_b128 v[60:63], v101 offset:9216\n\t" \
  "v_smfmac_f32_16x16x64_bf16 v[76:79], v[48:51], v[136:143], v104\n\t" \
  "ds_read_b128 v[64:67], v101 offset:2048\n\t" \
  "v_smfmac_f32_16x16x64_bf16 v[80:83], v[48:51], v[144:151], v104\n\t" \
  "ds_read_b128 v[28:31], v102 offset:35840\n\t" \
  "v_smfmac_f32_16x16x64_bf16 v[76:79], v[52:55], a[240:247], v104\n\t" \
  "ds_read_b128 v[48:51], v101 offset:4096\n\t" \
  "v_smfmac_f32_16x16x64_bf16 v[80:83], v[52:55], a[248:255], v104\n\t" \
  "s_waitcnt lgkmcnt(4)\n\t" \
  "s_barrier\n\t" \
  "v_smfmac_f32_16x16x64_bf16 v[76:79], v[56:59], v[0:7], v104\n\t" \
  "ds_read_b128 v[52:55], v101 offset:1024\n\t" \
  "v_smfmac_f32_16x16x64_bf16 v[80:83], v[56:59], v[8:15], v104\n\t" \
  "s_waitcnt lgkmcnt(4)\n\t" \
  "v_smfmac_f32_16x16x64_bf16 v[76:79], v[60:63], a[224:231], v104\n\t" \
  "ds_read_b128 v[56:59], v101 offset:5120\n\t" \
  "v_smfmac_f32_16x16x64_bf16 v[80:83], v[60:63], a[232:239], v104\n\t" \
  "ds_read_b128 v[0:3], v103 offset:0\n\t" \
  "buffer_load_dwordx4 v[32:35], v100, %[rk], s47 offen offset:0 sc1\n\t" \
  "s_waitcnt lgkmcnt(5)\n\t" \
  "v_smfmac_f32_16x16x64_bf16 v[76:79], v[64:67], a[48:55], v104\n\t" \
  "ds_read_b128 v[60:63], v101 offset:8192\n\t" \
  "ds_read_b128 v[4:7], v103 offset:1024\n\t" \
  "v_smfmac_f32_16x16x64_bf16 v[80:83], v[64:67], a[56:63], v104\n\t" \
  "ds_read_b128 v[8:11], v103 offset:2048\n\t" \
  "s_waitcnt lgkmcnt(6)\n\t" \
  "v_smfmac_f32_16x16x64_bf16 v[76:79], v[48:51], v[16:23], v104\n\t" \
  "ds_read_b128 v[12:15], v103 offset:3072\n\t" \
  "buffer_load_dwordx4 v[36:39], v100, %[rk], s47 offen offset:1024 sc1\n\t" \
  "v_smfmac_f32_16x16x64_bf16 v[80:83], v[48:51], v[24:31], v104\n\t" \
  "s_waitcnt lgkmcnt(6)\n\t" \
  "v_smfmac_f32_16x16x64_bf16 v[76:79], v[52:55], a[32:39], v104\n\t" \
  "v_smfmac_f32_16x16x64_bf16 v[80:83], v[52:55], a[40:47], v104\n\t" \
  "s_waitcnt lgkmcnt(5)\n\t" \
  "v_smfmac_f32_16x16x64_bf16 v[76:79], v[56:59], a[128:135], v104\n\t" \
  "s_waitcnt lgkmcnt(2)\n\t" \
  "v_smfmac_f32_16x16x64_bf16 v[76:79], v[60:63], v[0:7], v104\n\t" \
  "v_smfmac_f32_16x16x64_bf16 v[80:83], v[56:59], a[136:143], v104\n\t" \
  "s_waitcnt lgkmcnt(0)\n\t" \
  "v_smfmac_f32_16x16x64_bf16 v[80:83], v[60:63], v[8:15], v104\n\t" \
  "v_add_f32 v20, v76, v78\n\t" \
  "v_add_f32 v21, v77, v79\n\t" \
  "v_cvt_pk_bf16_f32 v20, v20, v21\n\t" \
  "ds_write_b32 v106, v20\n\t" \
  "s_nop 3\n\t" \
  "v_add_f32 v23, v80, v82\n\t" \
  "v_add_f32 v21, v81, v83\n\t" \
  "v_cvt_pk_bf16_f32 v21, v23, v21\n\t" \
  "ds_write_b32 v106, v21 offset:256\n\t" \
  "s_waitcnt lgkmcnt(0)\n\t" \
  "s_barrier\n\t" \
  "s_sleep 5\n\t" \
  "s_add_i32 s41, s56, 1950\n\t" \
  "s_sub_i32 s76, s92, s51\n\t" \
  "s_sub_i32 s77, s93, s53\n\t" \
  "s_cmp_eq_u32 s76, 0\n\t" \
  "s_cbranch_scc1 21f\n\t" \
  "v_cvt_f32_u32 v117, s76\n\t" \
  "v_cvt_f32_u32 v116, s77\n\t" \
  "v_rcp_f32 v117, v117\n\t" \
  "s_mov_b32 s51, s92\n\t" \
  "v_mul_f32 v117, v117, v116\n\t" \
  "v_max_f32 v117, 0x3f028f5c, v117\n\t" \
  "v_min_f32 v117, 0x3ffeb852, v117\n\t" \
  "v_rcp_f32 v116, v117\n\t" \
  "v_mul_f32 v117, 0x4f000000, v117\n\t" \
  "v_mul_f32 v116, 0x47800000, v116\n\t" \
  "v_cvt_u32_f32 v117, v117\n\t" \
  "v_cvt_u32_f32 v116, v116\n\t" \
  "s_mov_b32 s53, s93\n\t" \
  "v_readfirstlane_b32 s46, v117\n\t" \
  "v_readfirstlane_b32 s52, v116\n\t" \
  "21:\n\t" \
  "s_waitcnt vmcnt(0)\n\t" \
  "ds_write_b128 v101, v[32:35] offset:26624\n\t" \
  "ds_write_b128 v101, v[36:39] offset:27648\n\t" \
  "v_readfirstlane_b32 s76, v92\n\t" \
  "s_add_i32 s77, s44, 2\n\t" \
  "s_min_u32 s77, s77, 63\n\t" \
  "s_cmp_lt_i32 s76, s77\n\t" \
  "s_cselect_b32 s42, 32, 0\n\t" \
  "s_add_i32 s44, s44, 1\n\t" \
  "s_and_b32 s58, s58, 4\n\t" \
  "s_and_b32 s76, s44, 64\n\t" \
  "s_or_b32 s42, s42, s76\n\t" \
  "s_cbranch_scc0 20b\n\t" \
  "s_branch 99f\n\t" \
  "14:\n\t" \
  "s_add_u32 s59, s59, 0x10000\n\t" \
  "s_cmp_lt_u32 s59, 0x100000\n\t" \
  "s_cbranch_scc1 13b\n\t" \
  "s_mov_b64 exec, -1\n\t" \
  "s_mov_b32 s42, 16\n\t" \
  "s_branch 99f\n\t" \
  "8:\n\t" \
  "s_bitcmp1_b32 s58, 1\n\t" \
  "s_cbranch_scc1 4b\n\t" \
  "s_bitcmp1_b32 s58, 2\n\t" \
  "s_cbranch_scc1 5b\n\t" \
  "v_max3_u32 v116, v32, v33, v34\n\t" \
  "v_max3_u32 v117, v35, v36, v37\n\t" \
  "v_max3_u32 v116, v116, v38, v39\n\t" \
  "v_max_u32 v116, v116, v117\n\t" \
  "v_cmp_eq_u32 vcc, -1, v116\n\t" \
  "s_cbranch_vccz 5b\n\t" \
  "s_branch 0f\n\t" \
  "9:\n\t" \
  "s_bitcmp1_b32 s58, 2\n\t" \
  "s_cbranch_scc1 6b\n\t" \
  "v_max3_u32 v116, v40, v41, v42\n\t" \
  "v_max3_u32 v117, v43, v44, v45\n\t" \
  "v_max3_u32 v116, v116, v46, v47\n\t" \
  "v_max_u32 v116, v116, v117\n\t" \
  "v_cmp_eq_u32 vcc, -1, v116\n\t" \
  "s_cbranch_vccz 6b\n\t" \
  "s_or_b32 s58, s58, 2\n\t" \
  "0:\n\t" \
  "s_or_b32 s58, s58, 1\n\t" \
  "11:\n\t" \
  "buffer_load_dwordx4 v[32:35], v100, %[rh], s40 offen sc1\n\t" \
  "buffer_load_dwordx4 v[36:39], v100, %[rh], s76 offen sc1\n\t" \
  "buffer_load_dwordx4 v[40:43], v100, %[rh], s77 offen sc1\n\t" \
  "buffer_load_dwordx4 v[44:47], v100, %[rh], s78 offen sc1\n\t" \
  "s_waitcnt lgkmcnt(0)\n\t" \
  "s_branch 10b\n\t" \
  "17:\n\t" \
  "s_add_u32 s59, s59, 1\n\t" \
  "s_bitcmp1_b32 s58, 2\n\t" \
  "s_cbranch_scc1 3b\n\t" \
  "s_waitcnt vmcnt(0)\n\t" \
  "v_max_u32 v116, v32, v33\n\t" \
  "v_max3_u32 v116, v116, v34, v35\n\t" \
  "v_max3_u32 v116, v116, v36, v37\n\t" \
  "v_max3_u32 v116, v116, v38, v39\n\t" \
  "v_max3_u32 v116, v116, v40, v41\n\t" \
  "v_max3_u32 v116, v116, v42, v43\n\t" \
  "v_max3_u32 v116, v116, v44, v45\n\t" \
  "v_max3_u32 v116, v116, v46, v47\n\t" \
  "v_cmp_eq_u32 vcc, -1, v116\n\t" \
  "s_cbranch_vccz 3b\n\t" \
  "s_bitcmp1_b32 s59, 6\n\t" \
  "s_cbranch_scc1 12f\n\t" \
  "buffer_load_dwordx4 v[32:35], v100, %[rh], s40 offen sc1\n\t" \
  "buffer_load_dwordx4 v[36:39], v100, %[rh], s76 offen sc1\n\t" \
  "buffer_load_dwordx4 v[40:43], v100, %[rh], s77 offen sc1\n\t" \
  "buffer_load_dwordx4 v[44:47], v100, %[rh], s78 offen sc1\n\t" \
  "s_branch 17b\n\t" \
  "12:\n\t" \
  "s_mov_b32 s42, s58\n\t" \
  "s_branch 99f\n\t" \
  "99:\n\t" \
  "s_waitcnt vmcnt(0) lgkmcnt(0)\n\t" \
  ""
#define VO_BODY \
  "s_cmp_eq_u32 %[wv], 1\n\t" \
  "s_cbranch_scc1 91f\n\t" \
  "s_cmp_eq_u32 %[wv], 2\n\t" \
  "s_cbranch_scc1 92f\n\t" \
  "s_cmp_eq_u32 %[wv], 3\n\t" \
  "s_cbranch_scc1 93f\n\t" \
  "90:\n\t" \
  "s_mov_b32 s58, s42\n\t" \
  "20:\n\t" \
  "s_and_b32 s76, s44, 7\n\t" \
  "s_lshl_b32 s76, s76, 15\n\t" \
  "s_add_i32 s40, s54, s76\n\t" \
  "s_and_b32 s76, s44, 3\n\t" \
  "s_lshl_b32 s45, s76, 14\n\t" \
  "s_and_b32 s47, s44, 63\n\t" \
  "s_lshr_b32 s76, s47, 1\n\t" \
  "s_lshl_b32 s47, s47, 20\n\t" \
  "s_add_i32 s47, s47, s55\n\t" \
  "s_bitcmp1_b32 %[sel], s76\n\t" \
  "s_cselect_b32 s76, 0x2000, 0\n\t" \
  "s_add_i32 s47, s47, s76\n\t" \
  "s_add_i32 s49, s44, -1\n\t" \
  "s_and_b32 s48, s49, 2\n\t" \
  "s_mul_i32 s48, s48, 0x6000\n\t" \
  "s_and_b32 s76, s49, 1\n\t" \
  "s_lshl_b32 s76, s76, 11\n\t" \
  "s_add_i32 s48, s48, s76\n\t" \
  "s_lshl_b32 s50, s53, 8\n\t" \
  "s_mov_b32 s59, 0\n\t" \
  "s_mov_b32 s92, s51\n\t" \
  "s_mov_b32 s93, s53\n\t" \
  "s_xor_b32 s76, s40, 0x400\n\t" \
  "s_xor_b32 s77, s40, 0x800\n\t" \
  "s_xor_b32 s78, s40, 0xc00\n\t" \
  "s_cmp_lg_u32 s58, 0\n\t" \
  "s_cbranch_scc1 11f\n\t" \
  "10:\n\t" \
  "ds_read_b128 v[48:51], v102 offset:0\n\t" \
  "ds_read_b128 v[52:55], v102 offset:1024\n\t" \
  "ds_read_b128 v[56:59], v102 offset:2048\n\t" \
  "ds_read_b128 v[60:63], v102 offset:3072\n\t" \
  "ds_read_b128 v[0:3], v102 offset:16384\n\t" \
  "ds_read_b128 v[4:7], v102 offset:17408\n\t" \
  "ds_read_b128 v[8:11], v102 offset:18432\n\t" \
  "ds_read_b128 v[12:15], v102 offset:19456\n\t" \
  "ds_read_b128 v[16:19], v102 offset:32768\n\t" \
  "ds_read_b128 v[20:23], v102 offset:33792\n\t" \
  "ds_read_b128 v[24:27], v102 offset:34816\n\t" \
  "ds_read_b128 v[28:31], v102 offset:35840\n\t" \
  "s_waitcnt lgkmcnt(4)\n\t" \
  "ds_read_b128 v[80:83], v102 offset:49152\n\t" \
  "ds_read_b128 v[84:87], v102 offset:50176\n\t" \
  "ds_read_b128 v[88:91], v102 offset:51200\n\t" \
  "ds_read_b128 v[92:95], v102 offset:52224\n\t" \
  "s_bitcmp1_b32 s58, 1\n\t" \
  "s_cselect_b64 s[80:81], -1, 0\n\t" \
  "s_cmp_lg_u32 s58, 0\n\t" \
  "s_cbranch_scc1 17f\n\t" \
  "s_mov_b64 exec, 0xffff\n\t" \
  "16:\n\t" \
  "s_memtime vcc\n\t" \
  "s_waitcnt lgkmcnt(0)\n\t" \
  "s_sub_i32 s79, s41, vcc_lo\n\t" \
  "s_cmp_gt_i32 s79, 64\n\t" \
  "s_cbranch_scc1 16b\n\t" \
  "13:\n\t" \
  "buffer_load_dword v116, v112, %[rh], s48 offen sc1\n\t" \
  "s_waitcnt vmcnt(0)\n\t" \
  "v_mov_b32 v117, 20000\n\t" \
  "v_cmp_ne_u32_sdwa vcc, v116, s49 src0_sel:BYTE_0 src1_sel:DWORD\n\t" \
  "v_subrev_u32 v116, s50, v116\n\t" \
  "v_ashrrev_i32 v116, 8, v116\n\t" \
  "s_nop 1\n\t" \
  "v_max_i32_dpp v116, v116, v116 quad_perm:[1,0,3,2] row_mask:0xf bank_mask:0xf\n\t" \
  "s_cbranch_vccnz 14f\n\t" \
  "s_nop 0\n\t" \
  "v_max_i32_dpp v116, v116, v116 quad_perm:[2,3,0,1] row_mask:0xf bank_mask:0xf\n\t" \
  "s_nop 1\n\t" \
  "v_max_i32_dpp v116, v116, v116 row_half_mirror row_mask:0xf bank_mask:0xf\n\t" \
  "s_nop 1\n\t" \
  "v_max_i32_dpp v116, v116, v116 row_mirror row_mask:0xf bank_mask:0xf\n\t" \
  "v_med3_i32 v116, v116, 0, v117\n\t" \
  "v_mul_u32_u24 v116, s52, v116\n\t" \
  "v_lshrrev_b32 v116, 16, v116\n\t" \
  "s_mov_b64 exec, -1\n\t" \
  "v_readfirstlane_b32 s84, v116\n\t" \
  "s_add_i32 s84, s84, s51\n\t" \
  "1:\n\t" \
  "s_memtime vcc\n\t" \
  "s_waitcnt lgkmcnt(0)\n\t" \
  "s_sub_i32 s79, s84, vcc_lo\n\t" \
  "s_cmp_gt_i32 s79, 128\n\t" \
  "s_cbranch_scc1 1b\n\t" \
  "2:\n\t" \
  "s_sub_i32 s79, s79, 33\n\t" \
  "s_cmp_gt_i32 s79, 56\n\t" \
  "s_cbranch_scc1 2b\n\t" \
  "buffer_load_dwordx4 v[32:35], v100, %[rh], s40 offen sc1\n\t" \
  "s_nop 12\n\t" \
  "buffer_load_dwordx4 v[36:39], v100, %[rh], s76 offen sc1\n\t" \
  "s_nop 12\n\t" \
  "buffer_load_dwordx4 v[40:43], v100, %[rh], s77 offen sc1\n\t" \
  "s_nop 12\n\t" \
  "buffer_load_dwordx4 v[44:47], v100, %[rh], s78 offen sc1\n\t" \
  "s_memtime vcc\n\t" \
  "s_memrealtime s[82:83]\n\t" \
  "s_nop 4\n\t" \
  "s_memrealtime s[84:85]\n\t" \
  "s_nop 4\n\t" \
  "s_memrealtime s[56:57]\n\t" \
  "s_nop 4\n\t" \
  "s_memrealtime s[86:87]\n\t" \
  "s_nop 4\n\t" \
  "s_memrealtime s[88:89]\n\t" \
  "s_nop 4\n\t" \
  "s_memrealtime s[90:91]\n\t" \
  "s_mov_b32 s92, 0\n\t" \
  "s_waitcnt lgkmcnt(0)\n\t" \
  "s_cmp_eq_u32 s82, s84\n\t" \
  "s_addc_u32 s92, s92, 0\n\t" \
  "s_cmp_eq_u32 s82, s56\n\t" \
  "s_addc_u32 s92, s92, 0\n\t" \
  "s_cmp_eq_u32 s82, s86\n\t" \
  "s_addc_u32 s92, s92, 0\n\t" \
  "s_cmp_eq_u32 s82, s88\n\t" \
  "s_addc_u32 s92, s92, 0\n\t" \
  "s_cmp_eq_u32 s82, s90\n\t" \
  "s_addc_u32 s92, s92, 0\n\t" \
  "s_mul_i32 s92, s92, 24\n\t" \
  "s_add_i32 s92, s92, vcc_lo\n\t" \
  "s_lshl4_add_u32 s93, s82, 64\n\t" \
  "3:\n\t" \
  "v_mov_b64 v[72:73], 0\n\t" \
  "v_mov_b64 v[74:75], 0\n\t" \
  "v_mov_b64 v[76:77], 0\n\t" \
  "v_mov_b64 v[78:79], 0\n\t" \
  "s_waitcnt lgkmcnt(0)\n\t" \
  "s_waitcnt vmcnt(3)\n\t" \
  "v_smfmac_f32_16x16x64_bf16 v[72:75], v[32:35], v[48:55], v104\n\t" \
  "ds_write_b128 v101, v[32:35] offset:0\n\t" \
  "v_smfmac_f32_16x16x64_bf16 v[76:79], v[32:35], v[56:63], v104\n\t" \
  "s_waitcnt vmcnt(2)\n\t" \
  "ds_write_b128 v101, v[36:39] offset:1024\n\t" \
  "v_smfmac_f32_16x16x64_bf16 v[72:75], v[36:39], a[0:7], v104\n\t" \
  "v_smfmac_f32_16x16x64_bf16 v[76:79], v[36:39], a[8:15], v104\n\t" \
  "s_nop 3\n\t" \
  "v_cmp_u_f32 vcc, v72, v73\n\t" \
  "v_cmp_u_f32 s[82:83], v74, v75\n\t" \
  "s_waitcnt vmcnt(1)\n\t" \
  "v_smfmac_f32_16x16x64_bf16 v[72:75], v[40:43], a[16:23], v104\n\t" \
  "s_or_b64 vcc, vcc, s[82:83]\n\t" \
  "s_or_b64 vcc, vcc, s[80:81]\n\t" \
  "v_smfmac_f32_16x16x64_bf16 v[76:79], v[40:43], a[24:31], v104\n\t" \
  "s_waitcnt lgkmcnt(0)\n\t" \
  "s_cbranch_scc1 8f\n\t" \
  "5:\n\t" \
  "s_barrier\n\t" \
  "4:\n\t" \
  "s_memtime s[56:57]\n\t" \
  "ds_read_b128 v[48:51], v101 offset:4096\n\t" \
  "ds_read_b128 v[52:55], v101 offset:5120\n\t" \
  "ds_read_b128 v[56:59], v101 offset:9216\n\t" \
  "s_waitcnt vmcnt(0)\n\t" \
  "v_smfmac_f32_16x16x64_bf16 v[72:75], v[44:47], a[32:39], v104\n\t" \
  "ds_read_b128 v[60:63], v101 offset:8192\n\t" \
  "ds_read_b128 v[64:67], v101 offset:13312\n\t" \
  "v_smfmac_f32_16x16x64_bf16 v[76:79], v[44:47], a[40:47], v104\n\t" \
  "ds_read_b128 v[68:71], v101 offset:12288\n\t" \
  "ds_write_b128 v101, v[40:43] offset:2048\n\t" \
  "s_nop 1\n\t" \
  "v_cmp_u_f32 vcc, v72, v73\n\t" \
  "v_cmp_u_f32 s[82:83], v74, v75\n\t" \
  "s_waitcnt lgkmcnt(6)\n\t" \
  "v_smfmac_f32_16x16x64_bf16 v[72:75], v[48:51], v[0:7], v104\n\t" \
  "s_or_b64 vcc, vcc, s[82:83]\n\t" \
  "v_smfmac_f32_16x16x64_bf16 v[76:79], v[48:51], v[8:15], v104\n\t" \
  "s_cbranch_scc1 9f\n\t" \
  "6:\n\t" \
  "s_waitcnt lgkmcnt(5)\n\t" \
  "v_smfmac_f32_16x16x64_bf16 v[72:75], v[52:55], a[48:55], v104\n\t" \
  "ds_write_b128 v101, v[44:47] offset:3072\n\t" \
  "v_smfmac_f32_16x16x64_bf16 v[76:79], v[52:55], a[56:63], v104\n\t" \
  "v_mov_b64 v[40:41], 0\n\t" \
  "s_waitcnt lgkmcnt(5)\n\t" \
  "v_smfmac_f32_16x16x64_bf16 v[72:75], v[56:59], a[96:103], v104\n\t" \
  "v_mov_b64 v[42:43], 0\n\t" \
  "v_smfmac_f32_16x16x64_bf16 v[76:79], v[56:59], a[104:111], v104\n\t" \
  "s_waitcnt lgkmcnt(4)\n\t" \
  "v_smfmac_f32_16x16x64_bf16 v[72:75], v[60:63], v[16:23], v104\n\t" \
  "s_waitcnt lgkmcnt(0)\n\t" \
  "s_barrier\n\t" \
  "v_smfmac_f32_16x16x64_bf16 v[76:79], v[60:63], v[24:31], v104\n\t" \
  "ds_read_b128 v[48:51], v101 offset:6144\n\t" \
  "ds_read_b128 v[52:55], v101 offset:10240\n\t" \
  "v_smfmac_f32_16x16x64_bf16 v[72:75], v[64:67], a[144:151], v104\n\t" \
  "ds_read_b128 v[56:59], v101 offset:14336\n\t" \
  "ds_read_b128 v[60:63], v101 offset:7168\n\t" \
  "v_smfmac_f32_16x16x64_bf16 v[76:79], v[64:67], a[152:159], v104\n\t" \
  "ds_read_b128 v[32:35], v101 offset:11264\n\t" \
  "ds_read_b128 v[36:39], v101 offset:15360\n\t" \
  "v_smfmac_f32_16x16x64_bf16 v[72:75], v[68:71], v[80:87], v104\n\t" \
  "ds_read_b128 v[0:3], v103 offset:0\n\t" \
  "v_mov_b64 v[44:45], 0\n\t" \
  "v_smfmac_f32_16x16x64_bf16 v[76:79], v[68:71], v[88:95], v104\n\t" \
  "v_mov_b64 v[46:47], 0\n\t" \
  "s_waitcnt lgkmcnt(5)\n\t" \
  "v_smfmac_f32_16x16x64_bf16 v[72:75], v[48:51], a[64:71], v104\n\t" \
  "ds_read_b128 v[4:7], v103 offset:1024\n\t" \
  "v_mov_b64 v[80:81], 0\n\t" \
  "v_smfmac_f32_16x16x64_bf16 v[72:75], v[52:55], a[112:119], v104\n\t" \
  "v_mov_b64 v[82:83], 0\n\t" \
  "s_waitcnt lgkmcnt(2)\n\t" \
  "v_smfmac_f32_16x16x64_bf16 v[72:75], v[56:59], a[160:167], v104\n\t" \
  "ds_read_b128 v[8:11], v103 offset:2048\n\t" \
  "v_mov_b64 v[84:85], 0\n\t" \
  "v_smfmac_f32_16x16x64_bf16 v[72:75], v[60:63], a[80:87], v104\n\t" \
  "ds_read_b128 v[12:15], v103 offset:3072\n\t" \
  "v_mov_b64 v[86:87], 0\n\t" \
  "v_smfmac_f32_16x16x64_bf16 v[72:75], v[32:35], a[128:135], v104\n\t" \
  "ds_read_b128 v[16:19], v103 offset:16384\n\t" \
  "v_mov_b64 v[88:89], 0\n\t" \
  "v_smfmac_f32_16x16x64_bf16 v[72:75], v[36:39], a[176:183], v104\n\t" \
  "ds_read_b128 v[20:23], v103 offset:17408\n\t" \
  "v_mov_b64 v[90:91], 0\n\t" \
  "v_smfmac_f32_16x16x64_bf16 v[76:79], v[48:51], a[72:79], v104\n\t" \
  "ds_read_b128 v[24:27], v103 offset:18432\n\t" \
  "v_mov_b64 v[92:93], 0\n\t" \
  "v_smfmac_f32_16x16x64_bf16 v[76:79], v[52:55], a[120:127], v104\n\t" \
  "v_add_f32 v96, v72, v74\n\t" \
  "v_add_f32 v97, v73, v75\n\t" \
  "v_cvt_pk_bf16_f32 v96, v96, v97\n\t" \
  "ds_write_b32 v105, v96\n\t" \
  "v_smfmac_f32_16x16x64_bf16 v[76:79], v[56:59], a[168:175], v104\n\t" \
  "ds_read_b128 v[28:31], v103 offset:19456\n\t" \
  "v_mov_b64 v[94:95], 0\n\t" \
  "v_smfmac_f32_16x16x64_bf16 v[76:79], v[60:63], a[88:95], v104\n\t" \
  "v_smfmac_f32_16x16x64_bf16 v[76:79], v[32:35], a[136:143], v104\n\t" \
  "v_smfmac_f32_16x16x64_bf16 v[76:79], v[36:39], a[184:191], v104\n\t" \
  "s_waitcnt lgkmcnt(1)\n\t" \
  "s_barrier\n\t" \
  "ds_read_b64_tr_b16 v[96:97], v107 offset:0\n\t" \
  "ds_read_b64_tr_b16 v[98:99], v107 offset:64\n\t" \
  "ds_read_b64_tr_b16 v[56:57], v107 offset:1024\n\t" \
  "ds_read_b64_tr_b16 v[58:59], v107 offset:1088\n\t" \
  "s_nop 1\n\t" \
  "v_add_f32 v116, v76, v78\n\t" \
  "v_add_f32 v117, v77, v79\n\t" \
  "v_cvt_pk_bf16_f32 v116, v116, v117\n\t" \
  "ds_write_b32 v105, v116 offset:256\n\t" \
  "s_waitcnt lgkmcnt(3)\n\t" \
  "v_smfmac_f32_16x16x64_bf16 v[40:43], v[96:99], a[192:199], v106\n\t" \
  "v_smfmac_f32_16x16x64_bf16 v[44:47], v[96:99], a[224:231], v106\n\t" \
  "s_waitcnt lgkmcnt(0)\n\t" \
  "s_barrier\n\t" \
  "ds_read_b64_tr_b16 v[52:53], v107 offset:256\n\t" \
  "ds_read_b64_tr_b16 v[54:55], v107 offset:320\n\t" \
  "v_smfmac_f32_16x16x64_bf16 v[40:43], v[56:59], a[208:215], v106\n\t" \
  "ds_read_b64_tr_b16 v[60:61], v107 offset:1280\n\t" \
  "ds_read_b64_tr_b16 v[62:63], v107 offset:1344\n\t" \
  "v_smfmac_f32_16x16x64_bf16 v[44:47], v[56:59], v[0:7], v106\n\t" \
  "v_smfmac_f32_16x16x64_bf16 v[80:83], v[96:99], a[240:247], v106\n\t" \
  "v_mov_b64 v[32:33], 0\n\t" \
  "v_smfmac_f32_16x16x64_bf16 v[84:87], v[96:99], v[136:143], v106\n\t" \
  "v_mov_b64 v[34:35], 0\n\t" \
  "v_smfmac_f32_16x16x64_bf16 v[80:83], v[56:59], v[120:127], v106\n\t" \
  "v_mov_b64 v[36:37], 0\n\t" \
  "v_smfmac_f32_16x16x64_bf16 v[84:87], v[56:59], v[16:23], v106\n\t" \
  "s_waitcnt lgkmcnt(2)\n\t" \
  "v_smfmac_f32_16x16x64_bf16 v[40:43], v[52:55], a[200:207], v106\n\t" \
  "v_smfmac_f32_16x16x64_bf16 v[44:47], v[52:55], a[232:239], v106\n\t" \
  "s_xor_b32 s76, s47, 0x40000\n\t" \
  "s_waitcnt lgkmcnt(0)\n\t" \
  "v_smfmac_f32_16x16x64_bf16 v[40:43], v[60:63], a[216:223], v106\n\t" \
  "v_mov_b64 v[38:39], 0\n\t" \
  "s_xor_b32 s77, s47, 0x80000\n\t" \
  "v_smfmac_f32_16x16x64_bf16 v[44:47], v[60:63], v[8:15], v106\n\t" \
  "s_xor_b32 s78, s47, 0xc0000\n\t" \
  "v_smfmac_f32_16x16x64_bf16 v[80:83], v[52:55], a[248:255], v106\n\t" \
  "ds_read_b128 v[0:3], v103 offset:32768\n\t" \
  "v_cvt_pk_bf16_f32 v64, v40, v41\n\t" \
  "v_smfmac_f32_16x16x64_bf16 v[84:87], v[52:55], v[144:151], v106\n\t" \
  "ds_read_b128 v[4:7], v103 offset:33792\n\t" \
  "v_cvt_pk_bf16_f32 v66, v42, v43\n\t" \
  "v_smfmac_f32_16x16x64_bf16 v[80:83], v[60:63], v[128:135], v106\n\t" \
  "ds_read_b128 v[8:11], v103 offset:34816\n\t" \
  "v_cvt_pk_bf16_f32 v65, v44, v45\n\t" \
  "v_smfmac_f32_16x16x64_bf16 v[84:87], v[60:63], v[24:31], v106\n\t" \
  "ds_read_b128 v[12:15], v103 offset:35840\n\t" \
  "v_cvt_pk_bf16_f32 v67, v46, v47\n\t" \
  "v_smfmac_f32_16x16x64_bf16 v[88:91], v[96:99], v[152:159], v106\n\t" \
  "buffer_store_dwordx4 v[64:67], v100, %[ru], s47 offen sc1\n\t" \
  "v_cvt_pk_bf16_f32 v68, v80, v81\n\t" \
  "v_smfmac_f32_16x16x64_bf16 v[92:95], v[96:99], v[184:191], v106\n\t" \
  "v_cvt_pk_bf16_f32 v70, v82, v83\n\t" \
  "v_smfmac_f32_16x16x64_bf16 v[88:91], v[52:55], v[160:167], v106\n\t" \
  "v_cvt_pk_bf16_f32 v69, v84, v85\n\t" \
  "v_smfmac_f32_16x16x64_bf16 v[92:95], v[52:55], v[192:199], v106\n\t" \
  "v_cvt_pk_bf16_f32 v71, v86, v87\n\t" \
  "v_smfmac_f32_16x16x64_bf16 v[88:91], v[56:59], v[168:175], v106\n\t" \
  "ds_read_b128 v[16:19], v103 offset:49152\n\t" \
  "buffer_store_dwordx4 v[68:71], v100, %[ru], s76 offen sc1\n\t" \
  "s_waitcnt lgkmcnt(3)\n\t" \
  "v_smfmac_f32_16x16x64_bf16 v[92:95], v[56:59], v[0:7], v106\n\t" \
  "ds_read_b128 v[20:23], v103 offset:50176\n\t" \
  "v_smfmac_f32_16x16x64_bf16 v[88:91], v[60:63], v[176:183], v106\n\t" \
  "ds_read_b128 v[24:27], v103 offset:51200\n\t" \
  "s_waitcnt lgkmcnt(3)\n\t" \
  "v_smfmac_f32_16x16x64_bf16 v[92:95], v[60:63], v[8:15], v106\n\t" \
  "ds_read_b128 v[28:31], v103 offset:52224\n\t" \
  "v_smfmac_f32_16x16x64_bf16 v[32:35], v[96:99], v[200:207], v106\n\t" \
  "v_cvt_pk_bf16_f32 v72, v88, v89\n\t" \
  "v_smfmac_f32_16x16x64_bf16 v[36:39], v[96:99], v[232:239], v106\n\t" \
  "v_cvt_pk_bf16_f32 v74, v90, v91\n\t" \
  "v_smfmac_f32_16x16x64_bf16 v[32:35], v[52:55], v[208:215], v106\n\t" \
  "v_cvt_pk_bf16_f32 v73, v92, v93\n\t" \
  "v_smfmac_f32_16x16x64_bf16 v[36:39], v[52:55], v[240:247], v106\n\t" \
  "v_cvt_pk_bf16_f32 v75, v94, v95\n\t" \
  "v_smfmac_f32_16x16x64_bf16 v[32:35], v[56:59], v[216:223], v106\n\t" \
  "buffer_store_dwordx4 v[72:75], v100, %[ru], s77 offen sc1\n\t" \
  "v_smfmac_f32_16x16x64_bf16 v[32:35], v[60:63], v[224:231], v106\n\t" \
  "s_waitcnt lgkmcnt(2)\n\t" \
  "v_smfmac_f32_16x16x64_bf16 v[36:39], v[56:59], v[16:23], v106\n\t" \
  "s_waitcnt lgkmcnt(0)\n\t" \
  "v_smfmac_f32_16x16x64_bf16 v[36:39], v[60:63], v[24:31], v106\n\t" \
  "v_cvt_pk_bf16_f32 v76, v32, v33\n\t" \
  "v_cvt_pk_bf16_f32 v78, v34, v35\n\t" \
  "s_nop 5\n\t" \
  "v_cvt_pk_bf16_f32 v77, v36, v37\n\t" \
  "v_cvt_pk_bf16_f32 v79, v38, v39\n\t" \
  "buffer_store_dwordx4 v[76:79], v100, %[ru], s78 offen sc1\n\t" \
  "s_memtime s[76:77]\n\t" \
  "s_mov_b32 s42, 0\n\t" \
  "s_waitcnt lgkmcnt(0)\n\t" \
  "s_mov_b32 s57, s76\n\t" \
  "s_add_i32 s41, s56, 1950\n\t" \
  "s_sub_i32 s76, s92, s51\n\t" \
  "s_sub_i32 s77, s93, s53\n\t" \
  "s_cmp_eq_u32 s76, 0\n\t" \
  "s_cbranch_scc1 21f\n\t" \
  "v_cvt_f32_u32 v117, s76\n\t" \
  "v_cvt_f32_u32 v116, s77\n\t" \
  "v_rcp_f32 v117, v117\n\t" \
  "s_mov_b32 s51, s92\n\t" \
  "v_mul_f32 v117, v117, v116\n\t" \
  "v_max_f32 v117, 0x3f028f5c, v117\n\t" \
  "v_min_f32 v117, 0x3ffeb852, v117\n\t" \
  "v_rcp_f32 v116, v117\n\t" \
  "v_mul_f32 v117, 0x4f000000, v117\n\t" \
  "v_mul_f32 v116, 0x47800000, v116\n\t" \
  "v_cvt_u32_f32 v117, v117\n\t" \
  "v_cvt_u32_f32 v116, v116\n\t" \
  "s_mov_b32 s53, s93\n\t" \
  "v_readfirstlane_b32 s46, v117\n\t" \
  "v_readfirstlane_b32 s52, v116\n\t" \
  "21:\n\t" \
  "s_add_i32 s44, s44, 1\n\t" \
  "s_and_b32 s58, s58, 4\n\t" \
  "s_and_b32 s76, s44, 64\n\t" \
  "s_or_b32 s42, s42, s76\n\t" \
  "s_cbranch_scc0 20b\n\t" \
  "s_branch 99f\n\t" \
  "14:\n\t" \
  "s_add_u32 s59, s59, 0x10000\n\t" \
  "s_cmp_lt_u32 s59, 0x100000\n\t" \
  "s_cbranch_scc1 13b\n\t" \
  "s_mov_b64 exec, -1\n\t" \
  "s_mov_b32 s42, 16\n\t" \
  "s_branch 99f\n\t" \
  "8:\n\t" \
  "s_bitcmp1_b32 s58, 1\n\t" \
  "s_cbranch_scc1 4b\n\t" \
  "s_bitcmp1_b32 s58, 2\n\t" \
  "s_cbranch_scc1 5b\n\t" \
  "v_max3_u32 v116, v32, v33, v34\n\t" \
  "v_max3_u32 v117, v35, v36, v37\n\t" \
  "v_max3_u32 v116, v116, v38, v39\n\t" \
  "v_max_u32 v116, v116, v117\n\t" \
  "v_cmp_eq_u32 vcc, -1, v116\n\t" \
  "s_cbranch_vccz 5b\n\t" \
  "s_branch 0f\n\t" \
  "9:\n\t" \
  "s_bitcmp1_b32 s58, 2\n\t" \
  "s_cbranch_scc1 6b\n\t" \
  "v_max3_u32 v116, v40, v41, v42\n\t" \
  "v_max3_u32 v117, v43, v44, v45\n\t" \
  "v_max3_u32 v116, v116, v46, v47\n\t" \
  "v_max_u32 v116, v116, v117\n\t" \
  "v_cmp_eq_u32 vcc, -1, v116\n\t" \
  "s_cbranch_vccz 6b\n\t" \
  "s_or_b32 s58, s58, 2\n\t" \
  "0:\n\t" \
  "s_or_b32 s58, s58, 1\n\t" \
  "11:\n\t" \
  "buffer_load_dwordx4 v[32:35], v100, %[rh], s40 offen sc1\n\t" \
  "buffer_load_dwordx4 v[36:39], v100, %[rh], s76 offen sc1\n\t" \
  "buffer_load_dwordx4 v[40:43], v100, %[rh], s77 offen sc1\n\t" \
  "buffer_load_dwordx4 v[44:47], v100, %[rh], s78 offen sc1\n\t" \
  "s_waitcnt lgkmcnt(0)\n\t" \
  "s_branch 10b\n\t" \
  "17:\n\t" \
  "s_add_u32 s59, s59, 1\n\t" \
  "s_bitcmp1_b32 s58, 2\n\t" \
  "s_cbranch_scc1 3b\n\t" \
  "s_waitcnt vmcnt(0)\n\t" \
  "v_max_u32 v116, v32, v33\n\t" \
  "v_max3_u32 v116, v116, v34, v35\n\t" \
  "v_max3_u32 v116, v116, v36, v37\n\t" \
  "v_max3_u32 v116, v116, v38, v39\n\t" \
  "v_max3_u32 v116, v116, v40, v41\n\t" \
  "v_max3_u32 v116, v116, v42, v43\n\t" \
  "v_max3_u32 v116, v116, v44, v45\n\t" \
  "v_max3_u32 v116, v116, v46, v47\n\t" \
  "v_cmp_eq_u32 vcc, -1, v116\n\t" \
  "s_cbranch_vccz 3b\n\t" \
  "s_bitcmp1_b32 s59, 6\n\t" \
  "s_cbranch_scc1 12f\n\t" \
  "buffer_load_dwordx4 v[32:35], v100, %[rh], s40 offen sc1\n\t" \
  "buffer_load_dwordx4 v[36:39], v100, %[rh], s76 offen sc1\n\t" \
  "buffer_load_dwordx4 v[40:43], v100, %[rh], s77 offen sc1\n\t" \
  "buffer_load_dwordx4 v[44:47], v100, %[rh], s78 offen sc1\n\t" \
  "s_branch 17b\n\t" \
  "12:\n\t" \
  "s_mov_b32 s42, s58\n\t" \
  "s_branch 99f\n\t" \
  "91:\n\t" \
  "s_mov_b32 s58, s42\n\t" \
  "20:\n\t" \
  "s_and_b32 s76, s44, 7\n\t" \
  "s_lshl_b32 s76, s76, 15\n\t" \
  "s_add_i32 s40, s54, s76\n\t" \
  "s_and_b32 s76, s44, 3\n\t" \
  "s_lshl_b32 s45, s76, 14\n\t" \
  "s_and_b32 s47, s44, 63\n\t" \
  "s_lshr_b32 s76, s47, 1\n\t" \
  "s_lshl_b32 s47, s47, 20\n\t" \
  "s_add_i32 s47, s47, s55\n\t" \
  "s_bitcmp1_b32 %[sel], s76\n\t" \
  "s_cselect_b32 s76, 0x2000, 0\n\t" \
  "s_add_i32 s47, s47, s76\n\t" \
  "s_add_i32 s49, s44, -1\n\t" \
  "s_and_b32 s48, s49, 2\n\t" \
  "s_mul_i32 s48, s48, 0x6000\n\t" \
  "s_and_b32 s76, s49, 1\n\t" \
  "s_lshl_b32 s76, s76, 11\n\t" \
  "s_add_i32 s48, s48, s76\n\t" \
  "s_lshl_b32 s50, s53, 8\n\t" \
  "s_mov_b32 s59, 0\n\t" \
  "s_mov_b32 s92, s51\n\t" \
  "s_mov_b32 s93, s53\n\t" \
  "s_xor_b32 s76, s40, 0x400\n\t" \
  "s_xor_b32 s77, s40, 0x800\n\t" \
  "s_xor_b32 s78, s40, 0xc00\n\t" \
  "s_cmp_lg_u32 s58, 0\n\t" \
  "s_cbranch_scc1 11f\n\t" \
  "10:\n\t" \
  "ds_read_b128 v[48:51], v102 offset:16384\n\t" \
  "ds_read_b128 v[52:55], v102 offset:17408\n\t" \
  "ds_read_b128 v[56:59], v102 offset:18432\n\t" \
  "ds_read_b128 v[60:63], v102 offset:19456\n\t" \
  "ds_read_b128 v[0:3], v102 offset:32768\n\t" \
  "ds_read_b128 v[4:7], v102 offset:33792\n\t" \
  "ds_read_b128 v[8:11], v102 offset:34816\n\t" \
  "ds_read_b128 v[12:15], v102 offset:35840\n\t" \
  "ds_read_b128 v[16:19], v102 offset:49152\n\t" \
  "ds_read_b128 v[20:23], v102 offset:50176\n\t" \
  "ds_read_b128 v[24:27], v102 offset:51200\n\t" \
  "ds_read_b128 v[28:31], v102 offset:52224\n\t" \
  "s_waitcnt lgkmcnt(4)\n\t" \
  "ds_read_b128 v[80:83], v102 offset:0\n\t" \
  "ds_read_b128 v[84:87], v102 offset:1024\n\t" \
  "ds_read_b128 v[88:91], v102 offset:2048\n\t" \
  "ds_read_b128 v[92:95], v102 offset:3072\n\t" \
  "s_bitcmp1_b32 s58, 1\n\t" \
  "s_cselect_b64 s[80:81], -1, 0\n\t" \
  "s_cmp_lg_u32 s58, 0\n\t" \
  "s_cbranch_scc1 17f\n\t" \
  "s_mov_b64 exec, 0xffff\n\t" \
  "16:\n\t" \
  "s_memtime vcc\n\t" \
  "s_waitcnt lgkmcnt(0)\n\t" \
  "s_sub_i32 s79, s41, vcc_lo\n\t" \
  "s_cmp_gt_i32 s79, 64\n\t" \
  "s_cbranch_scc1 16b\n\t" \
  "13:\n\t" \
  "buffer_load_dword v116, v112, %[rh], s48 offen sc1\n\t" \
  "s_waitcnt vmcnt(0)\n\t" \
  "v_mov_b32 v117, 20000\n\t" \
  "v_cmp_ne_u32_sdwa vcc, v116, s49 src0_sel:BYTE_0 src1_sel:DWORD\n\t" \
  "v_subrev_u32 v116, s50, v116\n\t" \
  "v_ashrrev_i32 v116, 8, v116\n\t" \
  "s_nop 1\n\t" \
  "v_max_i32_dpp v116, v116, v116 quad_perm:[1,0,3,2] row_mask:0xf bank_mask:0xf\n\t" \
  "s_cbranch_vccnz 14f\n\t" \
  "s_nop 0\n\t" \
  "v_max_i32_dpp v116, v116, v116 quad_perm:[2,3,0,1] row_mask:0xf bank_mask:0xf\n\t" \
  "s_nop 1\n\t" \
  "v_max_i32_dpp v116, v116, v116 row_half_mirror row_mask:0xf bank_mask:0xf\n\t" \
  "s_nop 1\n\t" \
  "v_max_i32_dpp v116, v116, v116 row_mirror row_mask:0xf bank_mask:0xf\n\t" \
  "v_med3_i32 v116, v116, 0, v117\n\t" \
  "v_mul_u32_u24 v116, s52, v116\n\t" \
  "v_lshrrev_b32 v116, 16, v116\n\t" \
  "s_mov_b64 exec, -1\n\t" \
  "v_readfirstlane_b32 s84, v116\n\t" \
  "s_add_i32 s84, s84, s51\n\t" \
  "1:\n\t" \
  "s_memtime vcc\n\t" \
  "s_waitcnt lgkmcnt(0)\n\t" \
  "s_sub_i32 s79, s84, vcc_lo\n\t" \
  "s_cmp_gt_i32 s79, 128\n\t" \
  "s_cbranch_scc1 1b\n\t" \
  "2:\n\t" \
  "s_sub_i32 s79, s79, 33\n\t" \
  "s_cmp_gt_i32 s79, 56\n\t" \
  "s_cbranch_scc1 2b\n\t" \
  "buffer_load_dwordx4 v[32:35], v100, %[rh], s40 offen sc1\n\t" \
  "s_nop 12\n\t" \
  "buffer_load_dwordx4 v[36:39], v100, %[rh], s76 offen sc1\n\t" \
  "s_nop 12\n\t" \
  "buffer_load_dwordx4 v[40:43], v100, %[rh], s77 offen sc1\n\t" \
  "s_nop 12\n\t" \
  "buffer_load_dwordx4 v[44:47], v100, %[rh], s78 offen sc1\n\t" \
  "s_memtime vcc\n\t" \
  "s_memrealtime s[82:83]\n\t" \
  "s_nop 4\n\t" \
  "s_memrealtime s[84:85]\n\t" \
  "s_nop 4\n\t" \
  "s_memrealtime s[56:57]\n\t" \
  "s_nop 4\n\t" \
  "s_memrealtime s[86:87]\n\t" \
  "s_nop 4\n\t" \
  "s_memrealtime s[88:89]\n\t" \
  "s_nop 4\n\t" \
  "s_memrealtime s[90:91]\n\t" \
  "s_mov_b32 s92, 0\n\t" \
  "s_waitcnt lgkmcnt(0)\n\t" \
  "s_cmp_eq_u32 s82, s84\n\t" \
  "s_addc_u32 s92, s92, 0\n\t" \
  "s_cmp_eq_u32 s82, s56\n\t" \
  "s_addc_u32 s92, s92, 0\n\t" \
  "s_cmp_eq_u32 s82, s86\n\t" \
  "s_addc_u32 s92, s92, 0\n\t" \
  "s_cmp_eq_u32 s82, s88\n\t" \
  "s_addc_u32 s92, s92, 0\n\t" \
  "s_cmp_eq_u32 s82, s90\n\t" \
  "s_addc_u32 s92, s92, 0\n\t" \
  "s_mul_i32 s92, s92, 24\n\t" \
  "s_add_i32 s92, s92, vcc_lo\n\t" \
  "s_lshl4_add_u32 s93, s82, 64\n\t" \
  "3:\n\t" \
  "v_mov_b64 v[72:73], 0\n\t" \
  "v_mov_b64 v[74:75], 0\n\t" \
  "v_mov_b64 v[76:77], 0\n\t" \
  "v_mov_b64 v[78:79], 0\n\t" \
  "s_waitcnt lgkmcnt(0)\n\t" \
  "s_waitcnt vmcnt(3)\n\t" \
  "v_smfmac_f32_16x16x64_bf16 v[72:75], v[32:35], v[48:55], v104\n\t" \
  "ds_write_b128 v101, v[32:35] offset:4096\n\t" \
  "v_smfmac_f32_16x16x64_bf16 v[76:79], v[32:35], v[56:63], v104\n\t" \
  "s_waitcnt vmcnt(2)\n\t" \
  "ds_write_b128 v101, v[36:39] offset:5120\n\t" \
  "v_smfmac_f32_16x16x64_bf16 v[72:75], v[36:39], a[48:55], v104\n\t" \
  "v_smfmac_f32_16x16x64_bf16 v[76:79], v[36:39], a[56:63], v104\n\t" \
  "s_nop 3\n\t" \
  "v_cmp_u_f32 vcc, v72, v73\n\t" \
  "v_cmp_u_f32 s[82:83], v74, v75\n\t" \
  "s_waitcnt vmcnt(1)\n\t" \
  "v_smfmac_f32_16x16x64_bf16 v[72:75], v[40:43], a[64:71], v104\n\t" \
  "s_or_b64 vcc, vcc, s[82:83]\n\t" \
  "s_or_b64 vcc, vcc, s[80:81]\n\t" \
  "v_smfmac_f32_16x16x64_bf16 v[76:79], v[40:43], a[72:79], v104\n\t" \
  "s_waitcnt lgkmcnt(0)\n\t" \
  "s_cbranch_scc1 8f\n\t" \
  "5:\n\t" \
  "s_barrier\n\t" \
  "4:\n\t" \
  "s_memtime s[56:57]\n\t" \
  "ds_read_b128 v[48:51], v101 offset:8192\n\t" \
  "ds_read_b128 v[52:55], v101 offset:9216\n\t" \
  "ds_read_b128 v[56:59], v101 offset:13312\n\t" \
  "s_waitcnt vmcnt(0)\n\t" \
  "v_smfmac_f32_16x16x64_bf16 v[72:75], v[44:47], a[80:87], v104\n\t" \
  "ds_read_b128 v[60:63], v101 offset:12288\n\t" \
  "ds_read_b128 v[64:67], v101 offset:1024\n\t" \
  "v_smfmac_f32_16x16x64_bf16 v[76:79], v[44:47], a[88:95], v104\n\t" \
  "ds_read_b128 v[68:71], v101 offset:0\n\t" \
  "ds_write_b128 v101, v[40:43] offset:6144\n\t" \
  "s_nop 1\n\t" \
  "v_cmp_u_f32 vcc, v72, v73\n\t" \
  "v_cmp_u_f32 s[82:83], v74, v75\n\t" \
  "s_waitcnt lgkmcnt(6)\n\t" \
  "v_smfmac_f32_16x16x64_bf16 v[72:75], v[48:51], v[0:7], v104\n\t" \
  "s_or_b64 vcc, vcc, s[82:83]\n\t" \
  "v_smfmac_f32_16x16x64_bf16 v[76:79], v[48:51], v[8:15], v104\n\t" \
  "s_cbranch_scc1 9f\n\t" \
  "6:\n\t" \
  "s_waitcnt lgkmcnt(5)\n\t" \
  "v_smfmac_f32_16x16x64_bf16 v[72:75], v[52:55], a[96:103], v104\n\t" \
  "ds_write_b128 v101, v[44:47] offset:7168\n\t" \
  "v_smfmac_f32_16x16x64_bf16 v[76:79], v[52:55], a[104:111], v104\n\t" \
  "v_mov_b64 v[40:41], 0\n\t" \
  "s_waitcnt lgkmcnt(5)\n\t" \
  "v_smfmac_f32_16x16x64_bf16 v[72:75], v[56:59], a[144:151], v104\n\t" \
  "v_mov_b64 v[42:43], 0\n\t" \
  "v_smfmac_f32_16x16x64_bf16 v[76:79], v[56:59], a[152:159], v104\n\t" \
  "s_waitcnt lgkmcnt(4)\n\t" \
  "v_smfmac_f32_16x16x64_bf16 v[72:75], v[60:63], v[16:23], v104\n\t" \
  "s_waitcnt lgkmcnt(0)\n\t" \
  "s_barrier\n\t" \
  "v_smfmac_f32_16x16x64_bf16 v[76:79], v[60:63], v[24:31], v104\n\t" \
  "ds_read_b128 v[48:51], v101 offset:10240\n\t" \
  "ds_read_b128 v[52:55], v101 offset:14336\n\t" \
  "v_smfmac_f32_16x16x64_bf16 v[72:75], v[64:67], a[0:7], v104\n\t" \
  "ds_read_b128 v[56:59], v101 offset:2048\n\t" \
  "ds_read_b128 v[60:63], v101 offset:11264\n\t" \
  "v_smfmac_f32_16x16x64_bf16 v[76:79], v[64:67], a[8:15], v104\n\t" \
  "ds_read_b128 v[32:35], v101 offset:15360\n\t" \
  "ds_read_b128 v[36:39], v101 offset:3072\n\t" \
  "v_smfmac_f32_16x16x64_bf16 v[72:75], v[68:71], v[80:87], v104\n\t" \
  "ds_read_b128 v[0:3], v103 offset:0\n\t" \
  "v_mov_b64 v[44:45], 0\n\t" \
  "v_smfmac_f32_16x16x64_bf16 v[76:79], v[68:71], v[88:95], v104\n\t" \
  "v_mov_b64 v[46:47], 0\n\t" \
  "s_waitcnt lgkmcnt(5)\n\t" \
  "v_smfmac_f32_16x16x64_bf16 v[72:75], v[48:51], a[112:119], v104\n\t" \
  "ds_read_b128 v[4:7], v103 offset:1024\n\t" \
  "v_mov_b64 v[80:81], 0\n\t" \
  "v_smfmac_f32_16x16x64_bf16 v[72:75], v[52:55], a[160:167], v104\n\t" \
  "v_mov_b64 v[82:83], 0\n\t" \
  "s_waitcnt lgkmcnt(2)\n\t" \
  "v_smfmac_f32_16x16x64_bf16 v[72:75], v[56:59], a[16:23], v104\n\t" \
  "ds_read_b128 v[8:11], v103 offset:2048\n\t" \
  "v_mov_b64 v[84:85], 0\n\t" \
  "v_smfmac_f32_16x16x64_bf16 v[72:75], v[60:63], a[128:135], v104\n\t" \
  "ds_read_b128 v[12:15], v103 offset:3072\n\t" \
  "v_mov_b64 v[86:87], 0\n\t" \
  "v_smfmac_f32_16x16x64_bf16 v[72:75], v[32:35], a[176:183], v104\n\t" \
  "ds_read_b128 v[16:19], v103 offset:16384\n\t" \
  "v_mov_b64 v[88:89], 0\n\t" \
  "v_smfmac_f32_16x16x64_bf16 v[72:75], v[36:39], a[32:39], v104\n\t" \
  "ds_read_b128 v[20:23], v103 offset:17408\n\t" \
  "v_mov_b64 v[90:91], 0\n\t" \
  "v_smfmac_f32_16x16x64_bf16 v[76:79], v[48:51], a[120:127], v104\n\t" \
  "ds_read_b128 v[24:27], v103 offset:18432\n\t" \
  "v_mov_b64 v[92:93], 0\n\t" \
  "v_smfmac_f32_16x16x64_bf16 v[76:79], v[52:55], a[168:175], v104\n\t" \
  "v_add_f32 v96, v72, v74\n\t" \
  "v_add_f32 v97, v73, v75\n\t" \
  "v_cvt_pk_bf16_f32 v96, v96, v97\n\t" \
  "ds_write_b32 v105, v96\n\t" \
  "v_smfmac_f32_16x16x64_bf16 v[76:79], v[56:59], a[24:31], v104\n\t" \
  "ds_read_b128 v[28:31], v103 offset:19456\n\t" \
  "v_mov_b64 v[94:95], 0\n\t" \
  "v_smfmac_f32_16x16x64_bf16 v[76:79], v[60:63], a[136:143], v104\n\t" \
  "v_smfmac_f32_16x16x64_bf16 v[76:79], v[32:35], a[184:191], v104\n\t" \
  "v_smfmac_f32_16x16x64_bf16 v[76:79], v[36:39], a[40:47], v104\n\t" \
  "s_waitcnt lgkmcnt(1)\n\t" \
  "s_barrier\n\t" \
  "ds_read_b64_tr_b16 v[96:97], v107 offset:0\n\t" \
  "ds_read_b64_tr_b16 v[98:99], v107 offset:64\n\t" \
  "ds_read_b64_tr_b16 v[56:57], v107 offset:1024\n\t" \
  "ds_read_b64_tr_b16 v[58:59], v107 offset:1088\n\t" \
  "s_nop 1\n\t" \
  "v_add_f32 v116, v76, v78\n\t" \
  "v_add_f32 v117, v77, v79\n\t" \
  "v_cvt_pk_bf16_f32 v116, v116, v117\n\t" \
  "ds_write_b32 v105, v116 offset:256\n\t" \
  "s_waitcnt lgkmcnt(3)\n\t" \
  "v_smfmac_f32_16x16x64_bf16 v[40:43], v[96:99], a[192:199], v106\n\t" \
  "v_smfmac_f32_16x16x64_bf16 v[44:47], v[96:99], a[224:231], v106\n\t" \
  "s_waitcnt lgkmcnt(0)\n\t" \
  "s_barrier\n\t" \
  "ds_read_b64_tr_b16 v[52:53], v107 offset:256\n\t" \
  "ds_read_b64_tr_b16 v[54:55], v107 offset:320\n\t" \
  "v_smfmac_f32_16x16x64_bf16 v[40:43], v[56:59], a[208:215], v106\n\t" \
  "ds_read_b64_tr_b16 v[60:61], v107 offset:1280\n\t" \
  "ds_read_b64_tr_b16 v[62:63], v107 offset:1344\n\t" \
  "v_smfmac_f32_16x16x64_bf16 v[44:47], v[56:59], v[0:7], v106\n\t" \
  "v_smfmac_f32_16x16x64_bf16 v[80:83], v[96:99], a[240:247], v106\n\t" \
  "v_mov_b64 v[32:33], 0\n\t" \
  "v_smfmac_f32_16x16x64_bf16 v[84:87], v[96:99], v[136:143], v106\n\t" \
  "v_mov_b64 v[34:35], 0\n\t" \
  "v_smfmac_f32_16x16x64_bf16 v[80:83], v[56:59], v[120:127], v106\n\t" \
  "v_mov_b64 v[36:37], 0\n\t" \
  "v_smfmac_f32_16x16x64_bf16 v[84:87], v[56:59], v[16:23], v106\n\t" \
  "s_waitcnt lgkmcnt(2)\n\t" \
  "v_smfmac_f32_16x16x64_bf16 v[40:43], v[52:55], a[200:207], v106\n\t" \
  "v_smfmac_f32_16x16x64_bf16 v[44:47], v[52:55], a[232:239], v106\n\t" \
  "s_xor_b32 s76, s47, 0x40000\n\t" \
  "s_waitcnt lgkmcnt(0)\n\t" \
  "v_smfmac_f32_16x16x64_bf16 v[40:43], v[60:63], a[216:223], v106\n\t" \
  "v_mov_b64 v[38:39], 0\n\t" \
  "s_xor_b32 s77, s47, 0x80000\n\t" \
  "v_smfmac_f32_16x16x64_bf16 v[44:47], v[60:63], v[8:15], v106\n\t" \
  "s_xor_b32 s78, s47, 0xc0000\n\t" \
  "v_smfmac_f32_16x16x64_bf16 v[80:83], v[52:55], a[248:255], v106\n\t" \
  "ds_read_b128 v[0:3], v103 offset:32768\n\t" \
  "v_cvt_pk_bf16_f32 v64, v40, v41\n\t" \
  "v_smfmac_f32_16x16x64_bf16 v[84:87], v[52:55], v[144:151], v106\n\t" \
  "ds_read_b128 v[4:7], v103 offset:33792\n\t" \
  "v_cvt_pk_bf16_f32 v66, v42, v43\n\t" \
  "v_smfmac_f32_16x16x64_bf16 v[80:83], v[60:63], v[128:135], v106\n\t" \
  "ds_read_b128 v[8:11], v103 offset:34816\n\t" \
  "v_cvt_pk_bf16_f32 v65, v44, v45\n\t" \
  "v_smfmac_f32_16x16x64_bf16 v[84:87], v[60:63], v[24:31], v106\n\t" \
  "ds_read_b128 v[12:15], v103 offset:35840\n\t" \
  "v_cvt_pk_bf16_f32 v67, v46, v47\n\t" \
  "v_smfmac_f32_16x16x64_bf16 v[88:91], v[96:99], v[152:159], v106\n\t" \
  "v_cvt_pk_bf16_f32 v68, v80, v81\n\t" \
  "v_smfmac_f32_16x16x64_bf16 v[92:95], v[96:99], v[184:191], v106\n\t" \
  "buffer_store_dwordx4 v[64:67], v100, %[ru], s47 offen sc1\n\t" \
  "v_cvt_pk_bf16_f32 v70, v82, v83\n\t" \
  "v_smfmac_f32_16x16x64_bf16 v[88:91], v[52:55], v[160:167], v106\n\t" \
  "v_cvt_pk_bf16_f32 v69, v84, v85\n\t" \
  "v_smfmac_f32_16x16x64_bf16 v[92:95], v[52:55], v[192:199], v106\n\t" \
  "v_cvt_pk_bf16_f32 v71, v86, v87\n\t" \
  "v_smfmac_f32_16x16x64_bf16 v[88:91], v[56:59], v[168:175], v106\n\t" \
  "ds_read_b128 v[16:19], v103 offset:49152\n\t" \
  "s_waitcnt lgkmcnt(3)\n\t" \
  "v_smfmac_f32_16x16x64_bf16 v[92:95], v[56:59], v[0:7], v106\n\t" \
  "ds_read_b128 v[20:23], v103 offset:50176\n\t" \
  "buffer_store_dwordx4 v[68:71], v100, %[ru], s76 offen sc1\n\t" \
  "v_smfmac_f32_16x16x64_bf16 v[88:91], v[60:63], v[176:183], v106\n\t" \
  "ds_read_b128 v[24:27], v103 offset:51200\n\t" \
  "s_waitcnt lgkmcnt(3)\n\t" \
  "v_smfmac_f32_16x16x64_bf16 v[92:95], v[60:63], v[8:15], v106\n\t" \
  "ds_read_b128 v[28:31], v103 offset:52224\n\t" \
  "v_smfmac_f32_16x16x64_bf16 v[32:35], v[96:99], v[200:207], v106\n\t" \
  "v_cvt_pk_bf16_f32 v72, v88, v89\n\t" \
  "v_smfmac_f32_16x16x64_bf16 v[36:39], v[96:99], v[232:239], v106\n\t" \
  "v_cvt_pk_bf16_f32 v74, v90, v91\n\t" \
  "v_smfmac_f32_16x16x64_bf16 v[32:35], v[52:55], v[208:215], v106\n\t" \
  "v_cvt_pk_bf16_f32 v73, v92, v93\n\t" \
  "v_smfmac_f32_16x16x64_bf16 v[36:39], v[52:55], v[240:247], v106\n\t" \
  "v_cvt_pk_bf16_f32 v75, v94, v95\n\t" \
  "v_smfmac_f32_16x16x64_bf16 v[32:35], v[56:59], v[216:223], v106\n\t" \
  "v_smfmac_f32_16x16x64_bf16 v[32:35], v[60:63], v[224:231], v106\n\t" \
  "buffer_store_dwordx4 v[72:75], v100, %[ru], s77 offen sc1\n\t" \
  "s_waitcnt lgkmcnt(2)\n\t" \
  "v_smfmac_f32_16x16x64_bf16 v[36:39], v[56:59], v[16:23], v106\n\t" \
  "s_waitcnt lgkmcnt(0)\n\t" \
  "v_smfmac_f32_16x16x64_bf16 v[36:39], v[60:63], v[24:31], v106\n\t" \
  "v_cvt_pk_bf16_f32 v76, v32, v33\n\t" \
  "v_cvt_pk_bf16_f32 v78, v34, v35\n\t" \
  "s_nop 5\n\t" \
  "v_cvt_pk_bf16_f32 v77, v36, v37\n\t" \
  "v_cvt_pk_bf16_f32 v79, v38, v39\n\t" \
  "buffer_store_dwordx4 v[76:79], v100, %[ru], s78 offen sc1\n\t" \
  "s_memtime s[76:77]\n\t" \
  "s_mov_b32 s42, 0\n\t" \
  "s_waitcnt lgkmcnt(0)\n\t" \
  "s_mov_b32 s57, s76\n\t" \
  "s_add_i32 s41, s56, 1950\n\t" \
  "s_sub_i32 s76, s92, s51\n\t" \
  "s_sub_i32 s77, s93, s53\n\t" \
  "s_cmp_eq_u32 s76, 0\n\t" \
  "s_cbranch_scc1 21f\n\t" \
  "v_cvt_f32_u32 v117, s76\n\t" \
  "v_cvt_f32_u32 v116, s77\n\t" \
  "v_rcp_f32 v117, v117\n\t" \
  "s_mov_b32 s51, s92\n\t" \
  "v_mul_f32 v117, v117, v116\n\t" \
  "v_max_f32 v117, 0x3f028f5c, v117\n\t" \
  "v_min_f32 v117, 0x3ffeb852, v117\n\t" \
  "v_rcp_f32 v116, v117\n\t" \
  "v_mul_f32 v117, 0x4f000000, v117\n\t" \
  "v_mul_f32 v116, 0x47800000, v116\n\t" \
  "v_cvt_u32_f32 v117, v117\n\t" \
  "v_cvt_u32_f32 v116, v116\n\t" \
  "s_mov_b32 s53, s93\n\t" \
  "v_readfirstlane_b32 s46, v117\n\t" \
  "v_readfirstlane_b32 s52, v116\n\t" \
  "21:\n\t" \
  "s_add_i32 s44, s44, 1\n\t" \
  "s_and_b32 s58, s58, 4\n\t" \
  "s_and_b32 s76, s44, 64\n\t" \
  "s_or_b32 s42, s42, s76\n\t" \
  "s_cbranch_scc0 20b\n\t" \
  "s_branch 99f\n\t" \
  "14:\n\t" \
  "s_add_u32 s59, s59, 0x10000\n\t" \
  "s_cmp_lt_u32 s59, 0x100000\n\t" \
  "s_cbranch_scc1 13b\n\t" \
  "s_mov_b64 exec, -1\n\t" \
  "s_mov_b32 s42, 16\n\t" \
  "s_branch 99f\n\t" \
  "8:\n\t" \
  "s_bitcmp1_b32 s58, 1\n\t" \
  "s_cbranch_scc1 4b\n\t" \
  "s_bitcmp1_b32 s58, 2\n\t" \
  "s_cbranch_scc1 5b\n\t" \
  "v_max3_u32 v116, v32, v33, v34\n\t" \
  "v_max3_u32 v117, v35, v36, v37\n\t" \
  "v_max3_u32 v116, v116, v38, v39\n\t" \
  "v_max_u32 v116, v116, v117\n\t" \
  "v_cmp_eq_u32 vcc, -1, v116\n\t" \
  "s_cbranch_vccz 5b\n\t" \
  "s_branch 0f\n\t" \
  "9:\n\t" \
  "s_bitcmp1_b32 s58, 2\n\t" \
  "s_cbranch_scc1 6b\n\t" \
  "v_max3_u32 v116, v40, v41, v42\n\t" \
  "v_max3_u32 v117, v43, v44, v45\n\t" \
  "v_max3_u32 v116, v116, v46, v47\n\t" \
  "v_max_u32 v116, v116, v117\n\t" \
  "v_cmp_eq_u32 vcc, -1, v116\n\t" \
  "s_cbranch_vccz 6b\n\t" \
  "s_or_b32 s58, s58, 2\n\t" \
  "0:\n\t" \
  "s_or_b32 s58, s58, 1\n\t" \
  "11:\n\t" \
  "buffer_load_dwordx4 v[32:35], v100, %[rh], s40 offen sc1\n\t" \
  "buffer_load_dwordx4 v[36:39], v100, %[rh], s76 offen sc1\n\t" \
  "buffer_load_dwordx4 v[40:43], v100, %[rh], s77 offen sc1\n\t" \
  "buffer_load_dwordx4 v[44:47], v100, %[rh], s78 offen sc1\n\t" \
  "s_waitcnt lgkmcnt(0)\n\t" \
  "s_branch 10b\n\t" \
  "17:\n\t" \
  "s_add_u32 s59, s59, 1\n\t" \
  "s_bitcmp1_b32 s58, 2\n\t" \
  "s_cbranch_scc1 3b\n\t" \
  "s_waitcnt vmcnt(0)\n\t" \
  "v_max_u32 v116, v32, v33\n\t" \
  "v_max3_u32 v116, v116, v34, v35\n\t" \
  "v_max3_u32 v116, v116, v36, v37\n\t" \
  "v_max3_u32 v116, v116, v38, v39\n\t" \
  "v_max3_u32 v116, v116, v40, v41\n\t" \
  "v_max3_u32 v116, v116, v42, v43\n\t" \
  "v_max3_u32 v116, v116, v44, v45\n\t" \
  "v_max3_u32 v116, v116, v46, v47\n\t" \
  "v_cmp_eq_u32 vcc, -1, v116\n\t" \
  "s_cbranch_vccz 3b\n\t" \
  "s_bitcmp1_b32 s59, 6\n\t" \
  "s_cbranch_scc1 12f\n\t" \
  "buffer_load_dwordx4 v[32:35], v100, %[rh], s40 offen sc1\n\t" \
  "buffer_load_dwordx4 v[36:39], v100, %[rh], s76 offen sc1\n\t" \
  "buffer_load_dwordx4 v[40:43], v100, %[rh], s77 offen sc1\n\t" \
  "buffer_load_dwordx4 v[44:47], v100, %[rh], s78 offen sc1\n\t" \
  "s_branch 17b\n\t" \
  "12:\n\t" \
  "s_mov_b32 s42, s58\n\t" \
  "s_branch 99f\n\t" \
  "92:\n\t" \
  "s_mov_b32 s58, s42\n\t" \
  "20:\n\t" \
  "s_and_b32 s76, s44, 7\n\t" \
  "s_lshl_b32 s76, s76, 15\n\t" \
  "s_add_i32 s40, s54, s76\n\t" \
  "s_and_b32 s76, s44, 3\n\t" \
  "s_lshl_b32 s45, s76, 14\n\t" \
  "s_and_b32 s47, s44, 63\n\t" \
  "s_lshr_b32 s76, s47, 1\n\t" \
  "s_lshl_b32 s47, s47, 20\n\t" \
  "s_add_i32 s47, s47, s55\n\t" \
  "s_bitcmp1_b32 %[sel], s76\n\t" \
  "s_cselect_b32 s76, 0x2000, 0\n\t" \
  "s_add_i32 s47, s47, s76\n\t" \
  "s_add_i32 s49, s44, -1\n\t" \
  "s_and_b32 s48, s49, 2\n\t" \
  "s_mul_i32 s48, s48, 0x6000\n\t" \
  "s_and_b32 s76, s49, 1\n\t" \
  "s_lshl_b32 s76, s76, 11\n\t" \
  "s_add_i32 s48, s48, s76\n\t" \
  "s_lshl_b32 s50, s53, 8\n\t" \
  "s_mov_b32 s59, 0\n\t" \
  "s_mov_b32 s92, s51\n\t" \
  "s_mov_b32 s93, s53\n\t" \
  "s_xor_b32 s76, s40, 0x400\n\t" \
  "s_xor_b32 s77, s40, 0x800\n\t" \
  "s_xor_b32 s78, s40, 0xc00\n\t" \
  "s_cmp_lg_u32 s58, 0\n\t" \
  "s_cbranch_scc1 11f\n\t" \
  "10:\n\t" \
  "ds_read_b128 v[48:51], v102 offset:32768\n\t" \
  "ds_read_b128 v[52:55], v102 offset:33792\n\t" \
  "ds_read_b128 v[56:59], v102 offset:34816\n\t" \
  "ds_read_b128 v[60:63], v102 offset:35840\n\t" \
  "ds_read_b128 v[0:3], v102 offset:49152\n\t" \
  "ds_read_b128 v[4:7], v102 offset:50176\n\t" \
  "ds_read_b128 v[8:11], v102 offset:51200\n\t" \
  "ds_read_b128 v[12:15], v102 offset:52224\n\t" \
  "ds_read_b128 v[16:19], v102 offset:0\n\t" \
  "ds_read_b128 v[20:23], v102 offset:1024\n\t" \
  "ds_read_b128 v[24:27], v102 offset:2048\n\t" \
  "ds_read_b128 v[28:31], v102 offset:3072\n\t" \
  "s_waitcnt lgkmcnt(4)\n\t" \
  "ds_read_b128 v[80:83], v102 offset:16384\n\t" \
  "ds_read_b128 v[84:87], v102 offset:17408\n\t" \
  "ds_read_b128 v[88:91], v102 offset:18432\n\t" \
  "ds_read_b128 v[92:95], v102 offset:19456\n\t" \
  "s_bitcmp1_b32 s58, 1\n\t" \
  "s_cselect_b64 s[80:81], -1, 0\n\t" \
  "s_cmp_lg_u32 s58, 0\n\t" \
  "s_cbranch_scc1 17f\n\t" \
  "s_mov_b64 exec, 0xffff\n\t" \
  "16:\n\t" \
  "s_memtime vcc\n\t" \
  "s_waitcnt lgkmcnt(0)\n\t" \
  "s_sub_i32 s79, s41, vcc_lo\n\t" \
  "s_cmp_gt_i32 s79, 64\n\t" \
  "s_cbranch_scc1 16b\n\t" \
  "13:\n\t" \
  "buffer_load_dword v116, v112, %[rh], s48 offen sc1\n\t" \
  "s_waitcnt vmcnt(0)\n\t" \
  "v_mov_b32 v117, 20000\n\t" \
  "v_cmp_ne_u32_sdwa vcc, v116, s49 src0_sel:BYTE_0 src1_sel:DWORD\n\t" \
  "v_subrev_u32 v116, s50, v116\n\t" \
  "v_ashrrev_i32 v116, 8, v116\n\t" \
  "s_nop 1\n\t" \
  "v_max_i32_dpp v116, v116, v116 quad_perm:[1,0,3,2] row_mask:0xf bank_mask:0xf\n\t" \
  "s_cbranch_vccnz 14f\n\t" \
  "s_nop 0\n\t" \
  "v_max_i32_dpp v116, v116, v116 quad_perm:[2,3,0,1] row_mask:0xf bank_mask:0xf\n\t" \
  "s_nop 1\n\t" \
  "v_max_i32_dpp v116, v116, v116 row_half_mirror row_mask:0xf bank_mask:0xf\n\t" \
  "s_nop 1\n\t" \
  "v_max_i32_dpp v116, v116, v116 row_mirror row_mask:0xf bank_mask:0xf\n\t" \
  "v_med3_i32 v116, v116, 0, v117\n\t" \
  "v_mul_u32_u24 v116, s52, v116\n\t" \
  "v_lshrrev_b32 v116, 16, v116\n\t" \
  "s_mov_b64 exec, -1\n\t" \
  "v_readfirstlane_b32 s84, v116\n\t" \
  "s_add_i32 s84, s84, s51\n\t" \
  "1:\n\t" \
  "s_memtime vcc\n\t" \
  "s_waitcnt lgkmcnt(0)\n\t" \
  "s_sub_i32 s79, s84, vcc_lo\n\t" \
  "s_cmp_gt_i32 s79, 128\n\t" \
  "s_cbranch_scc1 1b\n\t" \
  "2:\n\t" \
  "s_sub_i32 s79, s79, 33\n\t" \
  "s_cmp_gt_i32 s79, 56\n\t" \
  "s_cbranch_scc1 2b\n\t" \
  "buffer_load_dwordx4 v[32:35], v100, %[rh], s40 offen sc1\n\t" \
  "s_nop 12\n\t" \
  "buffer_load_dwordx4 v[36:39], v100, %[rh], s76 offen sc1\n\t" \
  "s_nop 12\n\t" \
  "buffer_load_dwordx4 v[40:43], v100, %[rh], s77 offen sc1\n\t" \
  "s_nop 12\n\t" \
  "buffer_load_dwordx4 v[44:47], v100, %[rh], s78 offen sc1\n\t" \
  "s_memtime vcc\n\t" \
  "s_memrealtime s[82:83]\n\t" \
  "s_nop 4\n\t" \
  "s_memrealtime s[84:85]\n\t" \
  "s_nop 4\n\t" \
  "s_memrealtime s[56:57]\n\t" \
  "s_nop 4\n\t" \
  "s_memrealtime s[86:87]\n\t" \
  "s_nop 4\n\t" \
  "s_memrealtime s[88:89]\n\t" \
  "s_nop 4\n\t" \
  "s_memrealtime s[90:91]\n\t" \
  "s_mov_b32 s92, 0\n\t" \
  "s_waitcnt lgkmcnt(0)\n\t" \
  "s_cmp_eq_u32 s82, s84\n\t" \
  "s_addc_u32 s92, s92, 0\n\t" \
  "s_cmp_eq_u32 s82, s56\n\t" \
  "s_addc_u32 s92, s92, 0\n\t" \
  "s_cmp_eq_u32 s82, s86\n\t" \
  "s_addc_u32 s92, s92, 0\n\t" \
  "s_cmp_eq_u32 s82, s88\n\t" \
  "s_addc_u32 s92, s92, 0\n\t" \
  "s_cmp_eq_u32 s82, s90\n\t" \
  "s_addc_u32 s92, s92, 0\n\t" \
  "s_mul_i32 s92, s92, 24\n\t" \
  "s_add_i32 s92, s92, vcc_lo\n\t" \
  "s_lshl4_add_u32 s93, s82, 64\n\t" \
  "3:\n\t" \
  "v_mov_b64 v[72:73], 0\n\t" \
  "v_mov_b64 v[74:75], 0\n\t" \
  "v_mov_b64 v[76:77], 0\n\t" \
  "v_mov_b64 v[78:79], 0\n\t" \
  "s_waitcnt lgkmcnt(0)\n\t" \
  "s_waitcnt vmcnt(3)\n\t" \
  "v_smfmac_f32_16x16x64_bf16 v[72:75], v[32:35], v[48:55], v104\n\t" \
  "ds_write_b128 v101, v[32:35] offset:8192\n\t" \
  "v_smfmac_f32_16x16x64_bf16 v[76:79], v[32:35], v[56:63], v104\n\t" \
  "s_waitcnt vmcnt(2)\n\t" \
  "ds_write_b128 v101, v[36:39] offset:9216\n\t" \
  "v_smfmac_f32_16x16x64_bf16 v[72:75], v[36:39], a[96:103], v104\n\t" \
  "v_smfmac_f32_16x16x64_bf16 v[76:79], v[36:39], a[104:111], v104\n\t" \
  "s_nop 3\n\t" \
  "v_cmp_u_f32 vcc, v72, v73\n\t" \
  "v_cmp_u_f32 s[82:83], v74, v75\n\t" \
  "s_waitcnt vmcnt(1)\n\t" \
  "v_smfmac_f32_16x16x64_bf16 v[72:75], v[40:43], a[112:119], v104\n\t" \
  "s_or_b64 vcc, vcc, s[82:83]\n\t" \
  "s_or_b64 vcc, vcc, s[80:81]\n\t" \
  "v_smfmac_f32_16x16x64_bf16 v[76:79], v[40:43], a[120:127], v104\n\t" \
  "s_waitcnt lgkmcnt(0)\n\t" \
  "s_cbranch_scc1 8f\n\t" \
  "5:\n\t" \
  "s_barrier\n\t" \
  "4:\n\t" \
  "s_memtime s[56:57]\n\t" \
  "ds_read_b128 v[48:51], v101 offset:12288\n\t" \
  "ds_read_b128 v[52:55], v101 offset:13312\n\t" \
  "ds_read_b128 v[56:59], v101 offset:1024\n\t" \
  "s_waitcnt vmcnt(0)\n\t" \
  "v_smfmac_f32_16x16x64_bf16 v[72:75], v[44:47], a[128:135], v104\n\t" \
  "ds_read_b128 v[60:63], v101 offset:0\n\t" \
  "ds_read_b128 v[64:67], v101 offset:5120\n\t" \
  "v_smfmac_f32_16x16x64_bf16 v[76:79], v[44:47], a[136:143], v104\n\t" \
  "ds_read_b128 v[68:71], v101 offset:4096\n\t" \
  "ds_write_b128 v101, v[40:43] offset:10240\n\t" \
  "s_nop 1\n\t" \
  "v_cmp_u_f32 vcc, v72, v73\n\t" \
  "v_cmp_u_f32 s[82:83], v74, v75\n\t" \
  "s_waitcnt lgkmcnt(6)\n\t" \
  "v_smfmac_f32_16x16x64_bf16 v[72:75], v[48:51], v[0:7], v104\n\t" \
  "s_or_b64 vcc, vcc, s[82:83]\n\t" \
  "v_smfmac_f32_16x16x64_bf16 v[76:79], v[48:51], v[8:15], v104\n\t" \
  "s_cbranch_scc1 9f\n\t" \
  "6:\n\t" \
  "s_waitcnt lgkmcnt(5)\n\t" \
  "v_smfmac_f32_16x16x64_bf16 v[72:75], v[52:55], a[144:151], v104\n\t" \
  "ds_write_b128 v101, v[44:47] offset:11264\n\t" \
  "v_smfmac_f32_16x16x64_bf16 v[76:79], v[52:55], a[152:159], v104\n\t" \
  "v_mov_b64 v[40:41], 0\n\t" \
  "s_waitcnt lgkmcnt(5)\n\t" \
  "v_smfmac_f32_16x16x64_bf16 v[72:75], v[56:59], a[0:7], v104\n\t" \
  "v_mov_b64 v[42:43], 0\n\t" \
  "v_smfmac_f32_16x16x64_bf16 v[76:79], v[56:59], a[8:15], v104\n\t" \
  "s_waitcnt lgkmcnt(4)\n\t" \
  "v_smfmac_f32_16x16x64_bf16 v[72:75], v[60:63], v[16:23], v104\n\t" \
  "s_waitcnt lgkmcnt(0)\n\t" \
  "s_barrier\n\t" \
  "v_smfmac_f32_16x16x64_bf16 v[76:79], v[60:63], v[24:31], v104\n\t" \
  "ds_read_b128 v[48:51], v101 offset:14336\n\t" \
  "ds_read_b128 v[52:55], v101 offset:2048\n\t" \
  "v_smfmac_f32_16x16x64_bf16 v[72:75], v[64:67], a[48:55], v104\n\t" \
  "ds_read_b128 v[56:59], v101 offset:6144\n\t" \
  "ds_read_b128 v[60:63], v101 offset:15360\n\t" \
  "v_smfmac_f32_16x16x64_bf16 v[76:79], v[64:67], a[56:63], v104\n\t" \
  "ds_read_b128 v[32:35], v101 offset:3072\n\t" \
  "ds_read_b128 v[36:39], v101 offset:7168\n\t" \
  "v_smfmac_f32_16x16x64_bf16 v[72:75], v[68:71], v[80:87], v104\n\t" \
  "ds_read_b128 v[0:3], v103 offset:0\n\t" \
  "v_mov_b64 v[44:45], 0\n\t" \
  "v_smfmac_f32_16x16x64_bf16 v[76:79], v[68:71], v[88:95], v104\n\t" \
  "v_mov_b64 v[46:47], 0\n\t" \
  "s_waitcnt lgkmcnt(5)\n\t" \
  "v_smfmac_f32_16x16x64_bf16 v[72:75], v[48:51], a[160:167], v104\n\t" \
  "ds_read_b128 v[4:7], v103 offset:1024\n\t" \
  "v_mov_b64 v[80:81], 0\n\t" \
  "v_smfmac_f32_16x16x64_bf16 v[72:75], v[52:55], a[16:23], v104\n\t" \
  "v_mov_b64 v[82:83], 0\n\t" \
  "s_waitcnt lgkmcnt(2)\n\t" \
  "v_smfmac_f32_16x16x64_bf16 v[72:75], v[56:59], a[64:71], v104\n\t" \
  "ds_read_b128 v[8:11], v103 offset:2048\n\t" \
  "v_mov_b64 v[84:85], 0\n\t" \
  "v_smfmac_f32_16x16x64_bf16 v[72:75], v[60:63], a[176:183], v104\n\t" \
  "ds_read_b128 v[12:15], v103 offset:3072\n\t" \
  "v_mov_b64 v[86:87], 0\n\t" \
  "v_smfmac_f32_16x16x64_bf16 v[72:75], v[32:35], a[32:39], v104\n\t" \
  "ds_read_b128 v[16:19], v103 offset:16384\n\t" \
  "v_mov_b64 v[88:89], 0\n\t" \
  "v_smfmac_f32_16x16x64_bf16 v[72:75], v[36:39], a[80:87], v104\n\t" \
  "ds_read_b128 v[20:23], v103 offset:17408\n\t" \
  "v_mov_b64 v[90:91], 0\n\t" \
  "v_smfmac_f32_16x16x64_bf16 v[76:79], v[48:51], a[168:175], v104\n\t" \
  "ds_read_b128 v[24:27], v103 offset:18432\n\t" \
  "v_mov_b64 v[92:93], 0\n\t" \
  "v_smfmac_f32_16x16x64_bf16 v[76:79], v[52:55], a[24:31], v104\n\t" \
  "v_add_f32 v96, v72, v74\n\t" \
  "v_add_f32 v97, v73, v75\n\t" \
  "v_cvt_pk_bf16_f32 v96, v96, v97\n\t" \
  "ds_write_b32 v105, v96\n\t" \
  "v_smfmac_f32_16x16x64_bf16 v[76:79], v[56:59], a[72:79], v104\n\t" \
  "ds_read_b128 v[28:31], v103 offset:19456\n\t" \
  "v_mov_b64 v[94:95], 0\n\t" \
  "v_smfmac_f32_16x16x64_bf16 v[76:79], v[60:63], a[184:191], v104\n\t" \
  "v_smfmac_f32_16x16x64_bf16 v[76:79], v[32:35], a[40:47], v104\n\t" \
  "v_smfmac_f32_16x16x64_bf16 v[76:79], v[36:39], a[88:95], v104\n\t" \
  "s_waitcnt lgkmcnt(1)\n\t" \
  "s_barrier\n\t" \
  "ds_read_b64_tr_b16 v[96:97], v107 offset:0\n\t" \
  "ds_read_b64_tr_b16 v[98:99], v107 offset:64\n\t" \
  "ds_read_b64_tr_b16 v[56:57], v107 offset:1024\n\t" \
  "ds_read_b64_tr_b16 v[58:59], v107 offset:1088\n\t" \
  "s_nop 1\n\t" \
  "v_add_f32 v116, v76, v78\n\t" \
  "v_add_f32 v117, v77, v79\n\t" \
  "v_cvt_pk_bf16_f32 v116, v116, v117\n\t" \
  "ds_write_b32 v105, v116 offset:256\n\t" \
  "s_waitcnt lgkmcnt(3)\n\t" \
  "v_smfmac_f32_16x16x64_bf16 v[40:43], v[96:99], a[192:199], v106\n\t" \
  "v_smfmac_f32_16x16x64_bf16 v[44:47], v[96:99], a[224:231], v106\n\t" \
  "s_waitcnt lgkmcnt(0)\n\t" \
  "s_barrier\n\t" \
  "ds_read_b64_tr_b16 v[52:53], v107 offset:256\n\t" \
  "ds_read_b64_tr_b16 v[54:55], v107 offset:320\n\t" \
  "v_smfmac_f32_16x16x64_bf16 v[40:43], v[56:59], a[208:215], v106\n\t" \
  "ds_read_b64_tr_b16 v[60:61], v107 offset:1280\n\t" \
  "ds_read_b64_tr_b16 v[62:63], v107 offset:1344\n\t" \
  "v_smfmac_f32_16x16x64_bf16 v[44:47], v[56:59], v[0:7], v106\n\t" \
  "v_smfmac_f32_16x16x64_bf16 v[80:83], v[96:99], a[240:247], v106\n\t" \
  "v_mov_b64 v[32:33], 0\n\t" \
  "v_smfmac_f32_16x16x64_bf16 v[84:87], v[96:99], v[136:143], v106\n\t" \
  "v_mov_b64 v[34:35], 0\n\t" \
  "v_smfmac_f32_16x16x64_bf16 v[80:83], v[56:59], v[120:127], v106\n\t" \
  "v_mov_b64 v[36:37], 0\n\t" \
  "v_smfmac_f32_16x16x64_bf16 v[84:87], v[56:59], v[16:23], v106\n\t" \
  "s_waitcnt lgkmcnt(2)\n\t" \
  "v_smfmac_f32_16x16x64_bf16 v[40:43], v[52:55], a[200:207], v106\n\t" \
  "v_smfmac_f32_16x16x64_bf16 v[44:47], v[52:55], a[232:239], v106\n\t" \
  "s_xor_b32 s76, s47, 0x40000\n\t" \
  "s_waitcnt lgkmcnt(0)\n\t" \
  "v_smfmac_f32_16x16x64_bf16 v[40:43], v[60:63], a[216:223], v106\n\t" \
  "v_mov_b64 v[38:39], 0\n\t" \
  "s_xor_b32 s77, s47, 0x80000\n\t" \
  "v_smfmac_f32_16x16x64_bf16 v[44:47], v[60:63], v[8:15], v106\n\t" \
  "s_xor_b32 s78, s47, 0xc0000\n\t" \
  "v_smfmac_f32_16x16x64_bf16 v[80:83], v[52:55], a[248:255], v106\n\t" \
  "ds_read_b128 v[0:3], v103 offset:32768\n\t" \
  "v_cvt_pk_bf16_f32 v64, v40, v41\n\t" \
  "v_smfmac_f32_16x16x64_bf16 v[84:87], v[52:55], v[144:151], v106\n\t" \
  "ds_read_b128 v[4:7], v103 offset:33792\n\t" \
  "v_cvt_pk_bf16_f32 v66, v42, v43\n\t" \
  "v_smfmac_f32_16x16x64_bf16 v[80:83], v[60:63], v[128:135], v106\n\t" \
  "ds_read_b128 v[8:11], v103 offset:34816\n\t" \
  "v_cvt_pk_bf16_f32 v65, v44, v45\n\t" \
  "v_smfmac_f32_16x16x64_bf16 v[84:87], v[60:63], v[24:31], v106\n\t" \
  "ds_read_b128 v[12:15], v103 offset:35840\n\t" \
  "v_cvt_pk_bf16_f32 v67, v46, v47\n\t" \
  "v_smfmac_f32_16x16x64_bf16 v[88:91], v[96:99], v[152:159], v106\n\t" \
  "v_cvt_pk_bf16_f32 v68, v80, v81\n\t" \
  "v_smfmac_f32_16x16x64_bf16 v[92:95], v[96:99], v[184:191], v106\n\t" \
  "v_cvt_pk_bf16_f32 v70, v82, v83\n\t" \
  "v_smfmac_f32_16x16x64_bf16 v[88:91], v[52:55], v[160:167], v106\n\t" \
  "buffer_store_dwordx4 v[64:67], v100, %[ru], s47 offen sc1\n\t" \
  "v_cvt_pk_bf16_f32 v69, v84, v85\n\t" \
  "v_smfmac_f32_16x16x64_bf16 v[92:95], v[52:55], v[192:199], v106\n\t" \
  "v_cvt_pk_bf16_f32 v71, v86, v87\n\t" \
  "v_smfmac_f32_16x16x64_bf16 v[88:91], v[56:59], v[168:175], v106\n\t" \
  "ds_read_b128 v[16:19], v103 offset:49152\n\t" \
  "s_waitcnt lgkmcnt(3)\n\t" \
  "v_smfmac_f32_16x16x64_bf16 v[92:95], v[56:59], v[0:7], v106\n\t" \
  "ds_read_b128 v[20:23], v103 offset:50176\n\t" \
  "v_smfmac_f32_16x16x64_bf16 v[88:91], v[60:63], v[176:183], v106\n\t" \
  "ds_read_b128 v[24:27], v103 offset:51200\n\t" \
  "buffer_store_dwordx4 v[68:71], v100, %[ru], s76 offen sc1\n\t" \
  "s_waitcnt lgkmcnt(3)\n\t" \
  "v_smfmac_f32_16x16x64_bf16 v[92:95], v[60:63], v[8:15], v106\n\t" \
  "ds_read_b128 v[28:31], v103 offset:52224\n\t" \
  "v_smfmac_f32_16x16x64_bf16 v[32:35], v[96:99], v[200:207], v106\n\t" \
  "v_cvt_pk_bf16_f32 v72, v88, v89\n\t" \
  "v_smfmac_f32_16x16x64_bf16 v[36:39], v[96:99], v[232:239], v106\n\t" \
  "v_cvt_pk_bf16_f32 v74, v90, v91\n\t" \
  "v_smfmac_f32_16x16x64_bf16 v[32:35], v[52:55], v[208:215], v106\n\t" \
  "v_cvt_pk_bf16_f32 v73, v92, v93\n\t" \
  "v_smfmac_f32_16x16x64_bf16 v[36:39], v[52:55], v[240:247], v106\n\t" \
  "v_cvt_pk_bf16_f32 v75, v94, v95\n\t" \
  "v_smfmac_f32_16x16x64_bf16 v[32:35], v[56:59], v[216:223], v106\n\t" \
  "v_smfmac_f32_16x16x64_bf16 v[32:35], v[60:63], v[224:231], v106\n\t" \
  "s_waitcnt lgkmcnt(2)\n\t" \
  "v_smfmac_f32_16x16x64_bf16 v[36:39], v[56:59], v[16:23], v106\n\t" \
  "buffer_store_dwordx4 v[72:75], v100, %[ru], s77 offen sc1\n\t" \
  "s_waitcnt lgkmcnt(0)\n\t" \
  "v_smfmac_f32_16x16x64_bf16 v[36:39], v[60:63], v[24:31], v106\n\t" \
  "v_cvt_pk_bf16_f32 v76, v32, v33\n\t" \
  "v_cvt_pk_bf16_f32 v78, v34, v35\n\t" \
  "s_nop 5\n\t" \
  "v_cvt_pk_bf16_f32 v77, v36, v37\n\t" \
  "v_cvt_pk_bf16_f32 v79, v38, v39\n\t" \
  "buffer_store_dwordx4 v[76:79], v100, %[ru], s78 offen sc1\n\t" \
  "s_memtime s[76:77]\n\t" \
  "s_mov_b32 s42, 0\n\t" \
  "s_waitcnt lgkmcnt(0)\n\t" \
  "s_mov_b32 s57, s76\n\t" \
  "s_add_i32 s41, s56, 1950\n\t" \
  "s_sub_i32 s76, s92, s51\n\t" \
  "s_sub_i32 s77, s93, s53\n\t" \
  "s_cmp_eq_u32 s76, 0\n\t" \
  "s_cbranch_scc1 21f\n\t" \
  "v_cvt_f32_u32 v117, s76\n\t" \
  "v_cvt_f32_u32 v116, s77\n\t" \
  "v_rcp_f32 v117, v117\n\t" \
  "s_mov_b32 s51, s92\n\t" \
  "v_mul_f32 v117, v117, v116\n\t" \
  "v_max_f32 v117, 0x3f028f5c, v117\n\t" \
  "v_min_f32 v117, 0x3ffeb852, v117\n\t" \
  "v_rcp_f32 v116, v117\n\t" \
  "v_mul_f32 v117, 0x4f000000, v117\n\t" \
  "v_mul_f32 v116, 0x47800000, v116\n\t" \
  "v_cvt_u32_f32 v117, v117\n\t" \
  "v_cvt_u32_f32 v116, v116\n\t" \
  "s_mov_b32 s53, s93\n\t" \
  "v_readfirstlane_b32 s46, v117\n\t" \
  "v_readfirstlane_b32 s52, v116\n\t" \
  "21:\n\t" \
  "s_add_i32 s44, s44, 1\n\t" \
  "s_and_b32 s58, s58, 4\n\t" \
  "s_and_b32 s76, s44, 64\n\t" \
  "s_or_b32 s42, s42, s76\n\t" \
  "s_cbranch_scc0 20b\n\t" \
  "s_branch 99f\n\t" \
  "14:\n\t" \
  "s_add_u32 s59, s59, 0x10000\n\t" \
  "s_cmp_lt_u32 s59, 0x100000\n\t" \
  "s_cbranch_scc1 13b\n\t" \
  "s_mov_b64 exec, -1\n\t" \
  "s_mov_b32 s42, 16\n\t" \
  "s_branch 99f\n\t" \
  "8:\n\t" \
  "s_bitcmp1_b32 s58, 1\n\t" \
  "s_cbranch_scc1 4b\n\t" \
  "s_bitcmp1_b32 s58, 2\n\t" \
  "s_cbranch_scc1 5b\n\t" \
  "v_max3_u32 v116, v32, v33, v34\n\t" \
  "v_max3_u32 v117, v35, v36, v37\n\t" \
  "v_max3_u32 v116, v116, v38, v39\n\t" \
  "v_max_u32 v116, v116, v117\n\t" \
  "v_cmp_eq_u32 vcc, -1, v116\n\t" \
  "s_cbranch_vccz 5b\n\t" \
  "s_branch 0f\n\t" \
  "9:\n\t" \
  "s_bitcmp1_b32 s58, 2\n\t" \
  "s_cbranch_scc1 6b\n\t" \
  "v_max3_u32 v116, v40, v41, v42\n\t" \
  "v_max3_u32 v117, v43, v44, v45\n\t" \
  "v_max3_u32 v116, v116, v46, v47\n\t" \
  "v_max_u32 v116, v116, v117\n\t" \
  "v_cmp_eq_u32 vcc, -1, v116\n\t" \
  "s_cbranch_vccz 6b\n\t" \
  "s_or_b32 s58, s58, 2\n\t" \
  "0:\n\t" \
  "s_or_b32 s58, s58, 1\n\t" \
  "11:\n\t" \
  "buffer_load_dwordx4 v[32:35], v100, %[rh], s40 offen sc1\n\t" \
  "buffer_load_dwordx4 v[36:39], v100, %[rh], s76 offen sc1\n\t" \
  "buffer_load_dwordx4 v[40:43], v100, %[rh], s77 offen sc1\n\t" \
  "buffer_load_dwordx4 v[44:47], v100, %[rh], s78 offen sc1\n\t" \
  "s_waitcnt lgkmcnt(0)\n\t" \
  "s_branch 10b\n\t" \
  "17:\n\t" \
  "s_add_u32 s59, s59, 1\n\t" \
  "s_bitcmp1_b32 s58, 2\n\t" \
  "s_cbranch_scc1 3b\n\t" \
  "s_waitcnt vmcnt(0)\n\t" \
  "v_max_u32 v116, v32, v33\n\t" \
  "v_max3_u32 v116, v116, v34, v35\n\t" \
  "v_max3_u32 v116, v116, v36, v37\n\t" \
  "v_max3_u32 v116, v116, v38, v39\n\t" \
  "v_max3_u32 v116, v116, v40, v41\n\t" \
  "v_max3_u32 v116, v116, v42, v43\n\t" \
  "v_max3_u32 v116, v116, v44, v45\n\t" \
  "v_max3_u32 v116, v116, v46, v47\n\t" \
  "v_cmp_eq_u32 vcc, -1, v116\n\t" \
  "s_cbranch_vccz 3b\n\t" \
  "s_bitcmp1_b32 s59, 6\n\t" \
  "s_cbranch_scc1 12f\n\t" \
  "buffer_load_dwordx4 v[32:35], v100, %[rh], s40 offen sc1\n\t" \
  "buffer_load_dwordx4 v[36:39], v100, %[rh], s76 offen sc1\n\t" \
  "buffer_load_dwordx4 v[40:43], v100, %[rh], s77 offen sc1\n\t" \
  "buffer_load_dwordx4 v[44:47], v100, %[rh], s78 offen sc1\n\t" \
  "s_branch 17b\n\t" \
  "12:\n\t" \
  "s_mov_b32 s42, s58\n\t" \
  "s_branch 99f\n\t" \
  "93:\n\t" \
  "s_mov_b32 s58, s42\n\t" \
  "20:\n\t" \
  "s_and_b32 s76, s44, 7\n\t" \
  "s_lshl_b32 s76, s76, 15\n\t" \
  "s_add_i32 s40, s54, s76\n\t" \
  "s_and_b32 s76, s44, 3\n\t" \
  "s_lshl_b32 s45, s76, 14\n\t" \
  "s_and_b32 s47, s44, 63\n\t" \
  "s_lshr_b32 s76, s47, 1\n\t" \
  "s_lshl_b32 s47, s47, 20\n\t" \
  "s_add_i32 s47, s47, s55\n\t" \
  "s_bitcmp1_b32 %[sel], s76\n\t" \
  "s_cselect_b32 s76, 0x2000, 0\n\t" \
  "s_add_i32 s47, s47, s76\n\t" \
  "s_add_i32 s49, s44, -1\n\t" \
  "s_and_b32 s48, s49, 2\n\t" \
  "s_mul_i32 s48, s48, 0x6000\n\t" \
  "s_and_b32 s76, s49, 1\n\t" \
  "s_lshl_b32 s76, s76, 11\n\t" \
  "s_add_i32 s48, s48, s76\n\t" \
  "s_lshl_b32 s50, s53, 8\n\t" \
  "s_mov_b32 s59, 0\n\t" \
  "s_mov_b32 s92, s51\n\t" \
  "s_mov_b32 s93, s53\n\t" \
  "s_xor_b32 s76, s40, 0x400\n\t" \
  "s_xor_b32 s77, s40, 0x800\n\t" \
  "s_xor_b32 s78, s40, 0xc00\n\t" \
  "s_cmp_lg_u32 s58, 0\n\t" \
  "s_cbranch_scc1 11f\n\t" \
  "10:\n\t" \
  "ds_read_b128 v[48:51], v102 offset:49152\n\t" \
  "ds_read_b128 v[52:55], v102 offset:50176\n\t" \
  "ds_read_b128 v[56:59], v102 offset:51200\n\t" \
  "ds_read_b128 v[60:63], v102 offset:52224\n\t" \
  "ds_read_b128 v[0:3], v102 offset:0\n\t" \
  "ds_read_b128 v[4:7], v102 offset:1024\n\t" \
  "ds_read_b128 v[8:11], v102 offset:2048\n\t" \
  "ds_read_b128 v[12:15], v102 offset:3072\n\t" \
  "ds_read_b128 v[16:19], v102 offset:16384\n\t" \
  "ds_read_b128 v[20:23], v102 offset:17408\n\t" \
  "ds_read_b128 v[24:27], v102 offset:18432\n\t" \
  "ds_read_b128 v[28:31], v102 offset:19456\n\t" \
  "s_waitcnt lgkmcnt(4)\n\t" \
  "ds_read_b128 v[80:83], v102 offset:32768\n\t" \
  "ds_read_b128 v[84:87], v102 offset:33792\n\t" \
  "ds_read_b128 v[88:91], v102 offset:34816\n\t" \
  "ds_read_b128 v[92:95], v102 offset:35840\n\t" \
  "s_bitcmp1_b32 s58, 1\n\t" \
  "s_cselect_b64 s[80:81], -1, 0\n\t" \
  "s_cmp_lg_u32 s58, 0\n\t" \
  "s_cbranch_scc1 17f\n\t" \
  "s_mov_b64 exec, 0xffff\n\t" \
  "16:\n\t" \
  "s_memtime vcc\n\t" \
  "s_waitcnt lgkmcnt(0)\n\t" \
  "s_sub_i32 s79, s41, vcc_lo\n\t" \
  "s_cmp_gt_i32 s79, 64\n\t" \
  "s_cbranch_scc1 16b\n\t" \
  "13:\n\t" \
  "buffer_load_dword v116, v112, %[rh], s48 offen sc1\n\t" \
  "s_waitcnt vmcnt(0)\n\t" \
  "v_mov_b32 v117, 20000\n\t" \
  "v_cmp_ne_u32_sdwa vcc, v116, s49 src0_sel:BYTE_0 src1_sel:DWORD\n\t" \
  "v_subrev_u32 v116, s50, v116\n\t" \
  "v_ashrrev_i32 v116, 8, v116\n\t" \
  "s_nop 1\n\t" \
  "v_max_i32_dpp v116, v116, v116 quad_perm:[1,0,3,2] row_mask:0xf bank_mask:0xf\n\t" \
  "s_cbranch_vccnz 14f\n\t" \
  "s_nop 0\n\t" \
  "v_max_i32_dpp v116, v116, v116 quad_perm:[2,3,0,1] row_mask:0xf bank_mask:0xf\n\t" \
  "s_nop 1\n\t" \
  "v_max_i32_dpp v116, v116, v116 row_half_mirror row_mask:0xf bank_mask:0xf\n\t" \
  "s_nop 1\n\t" \
  "v_max_i32_dpp v116, v116, v116 row_mirror row_mask:0xf bank_mask:0xf\n\t" \
  "v_med3_i32 v116, v116, 0, v117\n\t" \
  "v_mul_u32_u24 v116, s52, v116\n\t" \
  "v_lshrrev_b32 v116, 16, v116\n\t" \
  "s_mov_b64 exec, -1\n\t" \
  "v_readfirstlane_b32 s84, v116\n\t" \
  "s_add_i32 s84, s84, s51\n\t" \
  "1:\n\t" \
  "s_memtime vcc\n\t" \
  "s_waitcnt lgkmcnt(0)\n\t" \
  "s_sub_i32 s79, s84, vcc_lo\n\t" \
  "s_cmp_gt_i32 s79, 128\n\t" \
  "s_cbranch_scc1 1b\n\t" \
  "2:\n\t" \
  "s_sub_i32 s79, s79, 33\n\t" \
  "s_cmp_gt_i32 s79, 56\n\t" \
  "s_cbranch_scc1 2b\n\t" \
  "buffer_load_dwordx4 v[32:35], v100, %[rh], s40 offen sc1\n\t" \
  "s_nop 12\n\t" \
  "buffer_load_dwordx4 v[36:39], v100, %[rh], s76 offen sc1\n\t" \
  "s_nop 12\n\t" \
  "buffer_load_dwordx4 v[40:43], v100, %[rh], s77 offen sc1\n\t" \
  "s_nop 12\n\t" \
  "buffer_load_dwordx4 v[44:47], v100, %[rh], s78 offen sc1\n\t" \
  "s_memtime vcc\n\t" \
  "s_memrealtime s[82:83]\n\t" \
  "s_nop 4\n\t" \
  "s_memrealtime s[84:85]\n\t" \
  "s_nop 4\n\t" \
  "s_memrealtime s[56:57]\n\t" \
  "s_nop 4\n\t" \
  "s_memrealtime s[86:87]\n\t" \
  "s_nop 4\n\t" \
  "s_memrealtime s[88:89]\n\t" \
  "s_nop 4\n\t" \
  "s_memrealtime s[90:91]\n\t" \
  "s_mov_b32 s92, 0\n\t" \
  "s_waitcnt lgkmcnt(0)\n\t" \
  "s_cmp_eq_u32 s82, s84\n\t" \
  "s_addc_u32 s92, s92, 0\n\t" \
  "s_cmp_eq_u32 s82, s56\n\t" \
  "s_addc_u32 s92, s92, 0\n\t" \
  "s_cmp_eq_u32 s82, s86\n\t" \
  "s_addc_u32 s92, s92, 0\n\t" \
  "s_cmp_eq_u32 s82, s88\n\t" \
  "s_addc_u32 s92, s92, 0\n\t" \
  "s_cmp_eq_u32 s82, s90\n\t" \
  "s_addc_u32 s92, s92, 0\n\t" \
  "s_mul_i32 s92, s92, 24\n\t" \
  "s_add_i32 s92, s92, vcc_lo\n\t" \
  "s_lshl4_add_u32 s93, s82, 64\n\t" \
  "3:\n\t" \
  "v_mov_b64 v[72:73], 0\n\t" \
  "v_mov_b64 v[74:75], 0\n\t" \
  "v_mov_b64 v[76:77], 0\n\t" \
  "v_mov_b64 v[78:79], 0\n\t" \
  "s_waitcnt lgkmcnt(0)\n\t" \
  "s_waitcnt vmcnt(3)\n\t" \
  "v_smfmac_f32_16x16x64_bf16 v[72:75], v[32:35], v[48:55], v104\n\t" \
  "ds_write_b128 v101, v[32:35] offset:12288\n\t" \
  "v_smfmac_f32_16x16x64_bf16 v[76:79], v[32:35], v[56:63], v104\n\t" \
  "s_waitcnt vmcnt(2)\n\t" \
  "ds_write_b128 v101, v[36:39] offset:13312\n\t" \
  "v_smfmac_f32_16x16x64_bf16 v[72:75], v[36:39], a[144:151], v104\n\t" \
  "v_smfmac_f32_16x16x64_bf16 v[76:79], v[36:39], a[152:159], v104\n\t" \
  "s_nop 3\n\t" \
  "v_cmp_u_f32 vcc, v72, v73\n\t" \
  "v_cmp_u_f32 s[82:83], v74, v75\n\t" \
  "s_waitcnt vmcnt(1)\n\t" \
  "v_smfmac_f32_16x16x64_bf16 v[72:75], v[40:43], a[160:167], v104\n\t" \
  "s_or_b64 vcc, vcc, s[82:83]\n\t" \
  "s_or_b64 vcc, vcc, s[80:81]\n\t" \
  "v_smfmac_f32_16x16x64_bf16 v[76:79], v[40:43], a[168:175], v104\n\t" \
  "s_waitcnt lgkmcnt(0)\n\t" \
  "s_cbranch_scc1 8f\n\t" \
  "5:\n\t" \
  "s_barrier\n\t" \
  "4:\n\t" \
  "s_memtime s[56:57]\n\t" \
  "ds_read_b128 v[48:51], v101 offset:0\n\t" \
  "ds_read_b128 v[52:55], v101 offset:1024\n\t" \
  "ds_read_b128 v[56:59], v101 offset:5120\n\t" \
  "s_waitcnt vmcnt(0)\n\t" \
  "v_smfmac_f32_16x16x64_bf16 v[72:75], v[44:47], a[176:183], v104\n\t" \
  "ds_read_b128 v[60:63], v101 offset:4096\n\t" \
  "ds_read_b128 v[64:67], v101 offset:9216\n\t" \
  "v_smfmac_f32_16x16x64_bf16 v[76:79], v[44:47], a[184:191], v104\n\t" \
  "ds_read_b128 v[68:71], v101 offset:8192\n\t" \
  "ds_write_b128 v101, v[40:43] offset:14336\n\t" \
  "s_nop 1\n\t" \
  "v_cmp_u_f32 vcc, v72, v73\n\t" \
  "v_cmp_u_f32 s[82:83], v74, v75\n\t" \
  "s_waitcnt lgkmcnt(6)\n\t" \
  "v_smfmac_f32_16x16x64_bf16 v[72:75], v[48:51], v[0:7], v104\n\t" \
  "s_or_b64 vcc, vcc, s[82:83]\n\t" \
  "v_smfmac_f32_16x16x64_bf16 v[76:79], v[48:51], v[8:15], v104\n\t" \
  "s_cbranch_scc1 9f\n\t" \
  "6:\n\t" \
  "s_waitcnt lgkmcnt(5)\n\t" \
  "v_smfmac_f32_16x16x64_bf16 v[72:75], v[52:55], a[0:7], v104\n\t" \
  "ds_write_b128 v101, v[44:47] offset:15360\n\t" \
  "v_smfmac_f32_16x16x64_bf16 v[76:79], v[52:55], a[8:15], v104\n\t" \
  "v_mov_b64 v[40:41], 0\n\t" \
  "s_waitcnt lgkmcnt(5)\n\t" \
  "v_smfmac_f32_16x16x64_bf16 v[72:75], v[56:59], a[48:55], v104\n\t" \
  "v_mov_b64 v[42:43], 0\n\t" \
  "v_smfmac_f32_16x16x64_bf16 v[76:79], v[56:59], a[56:63], v104\n\t" \
  "s_waitcnt lgkmcnt(4)\n\t" \
  "v_smfmac_f32_16x16x64_bf16 v[72:75], v[60:63], v[16:23], v104\n\t" \
  "s_waitcnt lgkmcnt(0)\n\t" \
  "s_barrier\n\t" \
  "v_smfmac_f32_16x16x64_bf16 v[76:79], v[60:63], v[24:31], v104\n\t" \
  "ds_read_b128 v[48:51], v101 offset:2048\n\t" \
  "ds_read_b128 v[52:55], v101 offset:6144\n\t" \
  "v_smfmac_f32_16x16x64_bf16 v[72:75], v[64:67], a[96:103], v104\n\t" \
  "ds_read_b128 v[56:59], v101 offset:10240\n\t" \
  "ds_read_b128 v[60:63], v101 offset:3072\n\t" \
  "v_smfmac_f32_16x16x64_bf16 v[76:79], v[64:67], a[104:111], v104\n\t" \
  "ds_read_b128 v[32:35], v101 offset:7168\n\t" \
  "ds_read_b128 v[36:39], v101 offset:11264\n\t" \
  "v_smfmac_f32_16x16x64_bf16 v[72:75], v[68:71], v[80:87], v104\n\t" \
  "ds_read_b128 v[0:3], v103 offset:0\n\t" \
  "v_mov_b64 v[44:45], 0\n\t" \
  "v_smfmac_f32_16x16x64_bf16 v[76:79], v[68:71], v[88:95], v104\n\t" \
  "v_mov_b64 v[46:47], 0\n\t" \
  "s_waitcnt lgkmcnt(5)\n\t" \
  "v_smfmac_f32_16x16x64_bf16 v[72:75], v[48:51], a[16:23], v104\n\t" \
  "ds_read_b128 v[4:7], v103 offset:1024\n\t" \
  "v_mov_b64 v[80:81], 0\n\t" \
  "v_smfmac_f32_16x16x64_bf16 v[72:75], v[52:55], a[64:71], v104\n\t" \
  "v_mov_b64 v[82:83], 0\n\t" \
  "s_waitcnt lgkmcnt(2)\n\t" \
  "v_smfmac_f32_16x16x64_bf16 v[72:75], v[56:59], a[112:119], v104\n\t" \
  "ds_read_b128 v[8:11], v103 offset:2048\n\t" \
  "v_mov_b64 v[84:85], 0\n\t" \
  "v_smfmac_f32_16x16x64_bf16 v[72:75], v[60:63], a[32:39], v104\n\t" \
  "ds_read_b128 v[12:15], v103 offset:3072\n\t" \
  "v_mov_b64 v[86:87], 0\n\t" \
  "v_smfmac_f32_16x16x64_bf16 v[72:75], v[32:35], a[80:87], v104\n\t" \
  "ds_read_b128 v[16:19], v103 offset:16384\n\t" \
  "v_mov_b64 v[88:89], 0\n\t" \
  "v_smfmac_f32_16x16x64_bf16 v[72:75], v[36:39], a[128:135], v104\n\t" \
  "ds_read_b128 v[20:23], v103 offset:17408\n\t" \
  "v_mov_b64 v[90:91], 0\n\t" \
  "v_smfmac_f32_16x16x64_bf16 v[76:79], v[48:51], a[24:31], v104\n\t" \
  "ds_read_b128 v[24:27], v103 offset:18432\n\t" \
  "v_mov_b64 v[92:93], 0\n\t" \
  "v_smfmac_f32_16x16x64_bf16 v[76:79], v[52:55], a[72:79], v104\n\t" \
  "v_add_f32 v96, v72, v74\n\t" \
  "v_add_f32 v97, v73, v75\n\t" \
  "v_cvt_pk_bf16_f32 v96, v96, v97\n\t" \
  "ds_write_b32 v105, v96\n\t" \
  "v_smfmac_f32_16x16x64_bf16 v[76:79], v[56:59], a[120:127], v104\n\t" \
  "ds_read_b128 v[28:31], v103 offset:19456\n\t" \
  "v_mov_b64 v[94:95], 0\n\t" \
  "v_smfmac_f32_16x16x64_bf16 v[76:79], v[60:63], a[40:47], v104\n\t" \
  "v_smfmac_f32_16x16x64_bf16 v[76:79], v[32:35], a[88:95], v104\n\t" \
  "v_smfmac_f32_16x16x64_bf16 v[76:79], v[36:39], a[136:143], v104\n\t" \
  "s_waitcnt lgkmcnt(1)\n\t" \
  "s_barrier\n\t" \
  "ds_read_b64_tr_b16 v[96:97], v107 offset:0\n\t" \
  "ds_read_b64_tr_b16 v[98:99], v107 offset:64\n\t" \
  "ds_read_b64_tr_b16 v[56:57], v107 offset:1024\n\t" \
  "ds_read_b64_tr_b16 v[58:59], v107 offset:1088\n\t" \
  "s_nop 1\n\t" \
  "v_add_f32 v116, v76, v78\n\t" \
  "v_add_f32 v117, v77, v79\n\t" \
  "v_cvt_pk_bf16_f32 v116, v116, v117\n\t" \
  "ds_write_b32 v105, v116 offset:256\n\t" \
  "s_waitcnt lgkmcnt(3)\n\t" \
  "v_smfmac_f32_16x16x64_bf16 v[40:43], v[96:99], a[192:199], v106\n\t" \
  "v_smfmac_f32_16x16x64_bf16 v[44:47], v[96:99], a[224:231], v106\n\t" \
  "s_waitcnt lgkmcnt(0)\n\t" \
  "s_barrier\n\t" \
  "ds_read_b64_tr_b16 v[52:53], v107 offset:256\n\t" \
  "ds_read_b64_tr_b16 v[54:55], v107 offset:320\n\t" \
  "v_smfmac_f32_16x16x64_bf16 v[40:43], v[56:59], a[208:215], v106\n\t" \
  "ds_read_b64_tr_b16 v[60:61], v107 offset:1280\n\t" \
  "ds_read_b64_tr_b16 v[62:63], v107 offset:1344\n\t" \
  "v_smfmac_f32_16x16x64_bf16 v[44:47], v[56:59], v[0:7], v106\n\t" \
  "v_smfmac_f32_16x16x64_bf16 v[80:83], v[96:99], a[240:247], v106\n\t" \
  "v_mov_b64 v[32:33], 0\n\t" \
  "v_smfmac_f32_16x16x64_bf16 v[84:87], v[96:99], v[136:143], v106\n\t" \
  "v_mov_b64 v[34:35], 0\n\t" \
  "v_smfmac_f32_16x16x64_bf16 v[80:83], v[56:59], v[120:127], v106\n\t" \
  "v_mov_b64 v[36:37], 0\n\t" \
  "v_smfmac_f32_16x16x64_bf16 v[84:87], v[56:59], v[16:23], v106\n\t" \
  "s_waitcnt lgkmcnt(2)\n\t" \
  "v_smfmac_f32_16x16x64_bf16 v[40:43], v[52:55], a[200:207], v106\n\t" \
  "v_smfmac_f32_16x16x64_bf16 v[44:47], v[52:55], a[232:239], v106\n\t" \
  "s_xor_b32 s76, s47, 0x40000\n\t" \
  "s_waitcnt lgkmcnt(0)\n\t" \
  "v_smfmac_f32_16x16x64_bf16 v[40:43], v[60:63], a[216:223], v106\n\t" \
  "v_mov_b64 v[38:39], 0\n\t" \
  "s_xor_b32 s77, s47, 0x80000\n\t" \
  "v_smfmac_f32_16x16x64_bf16 v[44:47], v[60:63], v[8:15], v106\n\t" \
  "s_xor_b32 s78, s47, 0xc0000\n\t" \
  "v_smfmac_f32_16x16x64_bf16 v[80:83], v[52:55], a[248:255], v106\n\t" \
  "ds_read_b128 v[0:3], v103 offset:32768\n\t" \
  "v_cvt_pk_bf16_f32 v64, v40, v41\n\t" \
  "v_smfmac_f32_16x16x64_bf16 v[84:87], v[52:55], v[144:151], v106\n\t" \
  "ds_read_b128 v[4:7], v103 offset:33792\n\t" \
  "v_cvt_pk_bf16_f32 v66, v42, v43\n\t" \
  "v_smfmac_f32_16x16x64_bf16 v[80:83], v[60:63], v[128:135], v106\n\t" \
  "ds_read_b128 v[8:11], v103 offset:34816\n\t" \
  "v_cvt_pk_bf16_f32 v65, v44, v45\n\t" \
  "v_smfmac_f32_16x16x64_bf16 v[84:87], v[60:63], v[24:31], v106\n\t" \
  "ds_read_b128 v[12:15], v103 offset:35840\n\t" \
  "v_cvt_pk_bf16_f32 v67, v46, v47\n\t" \
  "v_smfmac_f32_16x16x64_bf16 v[88:91], v[96:99], v[152:159], v106\n\t" \
  "v_cvt_pk_bf16_f32 v68, v80, v81\n\t" \
  "v_smfmac_f32_16x16x64_bf16 v[92:95], v[96:99], v[184:191], v106\n\t" \
  "v_cvt_pk_bf16_f32 v70, v82, v83\n\t" \
  "v_smfmac_f32_16x16x64_bf16 v[88:91], v[52:55], v[160:167], v106\n\t" \
  "v_cvt_pk_bf16_f32 v69, v84, v85\n\t" \
  "v_smfmac_f32_16x16x64_bf16 v[92:95], v[52:55], v[192:199], v106\n\t" \
  "buffer_store_dwordx4 v[64:67], v100, %[ru], s47 offen sc1\n\t" \
  "v_cvt_pk_bf16_f32 v71, v86, v87\n\t" \
  "v_smfmac_f32_16x16x64_bf16 v[88:91], v[56:59], v[168:175], v106\n\t" \
  "ds_read_b128 v[16:19], v103 offset:49152\n\t" \
  "s_waitcnt lgkmcnt(3)\n\t" \
  "v_smfmac_f32_16x16x64_bf16 v[92:95], v[56:59], v[0:7], v106\n\t" \
  "ds_read_b128 v[20:23], v103 offset:50176\n\t" \
  "v_smfmac_f32_16x16x64_bf16 v[88:91], v[60:63], v[176:183], v106\n\t" \
  "ds_read_b128 v[24:27], v103 offset:51200\n\t" \
  "s_waitcnt lgkmcnt(3)\n\t" \
  "v_smfmac_f32_16x16x64_bf16 v[92:95], v[60:63], v[8:15], v106\n\t" \
  "ds_read_b128 v[28:31], v103 offset:52224\n\t" \
  "buffer_store_dwordx4 v[68:71], v100, %[ru], s76 offen sc1\n\t" \
  "v_smfmac_f32_16x16x64_bf16 v[32:35], v[96:99], v[200:207], v106\n\t" \
  "v_cvt_pk_bf16_f32 v72, v88, v89\n\t" \
  "v_smfmac_f32_16x16x64_bf16 v[36:39], v[96:99], v[232:239], v106\n\t" \
  "v_cvt_pk_bf16_f32 v74, v90, v91\n\t" \
  "v_smfmac_f32_16x16x64_bf16 v[32:35], v[52:55], v[208:215], v106\n\t" \
  "v_cvt_pk_bf16_f32 v73, v92, v93\n\t" \
  "v_smfmac_f32_16x16x64_bf16 v[36:39], v[52:55], v[240:247], v106\n\t" \
  "v_cvt_pk_bf16_f32 v75, v94, v95\n\t" \
  "v_smfmac_f32_16x16x64_bf16 v[32:35], v[56:59], v[216:223], v106\n\t" \
  "v_smfmac_f32_16x16x64_bf16 v[32:35], v[60:63], v[224:231], v106\n\t" \
  "s_waitcnt lgkmcnt(2)\n\t" \
  "v_smfmac_f32_16x16x64_bf16 v[36:39], v[56:59], v[16:23], v106\n\t" \
  "s_waitcnt lgkmcnt(0)\n\t" \
  "v_smfmac_f32_16x16x64_bf16 v[36:39], v[60:63], v[24:31], v106\n\t" \
  "buffer_store_dwordx4 v[72:75], v100, %[ru], s77 offen sc1\n\t" \
  "v_cvt_pk_bf16_f32 v76, v32, v33\n\t" \
  "v_cvt_pk_bf16_f32 v78, v34, v35\n\t" \
  "s_nop 5\n\t" \
  "v_cvt_pk_bf16_f32 v77, v36, v37\n\t" \
  "v_cvt_pk_bf16_f32 v79, v38, v39\n\t" \
  "buffer_store_dwordx4 v[76:79], v100, %[ru], s78 offen sc1\n\t" \
  "s_memtime s[76:77]\n\t" \
  "s_mov_b32 s42, 0\n\t" \
  "s_waitcnt lgkmcnt(0)\n\t" \
  "s_mov_b32 s57, s76\n\t" \
  "s_add_i32 s41, s56, 1950\n\t" \
  "s_sub_i32 s76, s92, s51\n\t" \
  "s_sub_i32 s77, s93, s53\n\t" \
  "s_cmp_eq_u32 s76, 0\n\t" \
  "s_cbranch_scc1 21f\n\t" \
  "v_cvt_f32_u32 v117, s76\n\t" \
  "v_cvt_f32_u32 v116, s77\n\t" \
  "v_rcp_f32 v117, v117\n\t" \
  "s_mov_b32 s51, s92\n\t" \
  "v_mul_f32 v117, v117, v116\n\t" \
  "v_max_f32 v117, 0x3f028f5c, v117\n\t" \
  "v_min_f32 v117, 0x3ffeb852, v117\n\t" \
  "v_rcp_f32 v116, v117\n\t" \
  "v_mul_f32 v117, 0x4f000000, v117\n\t" \
  "v_mul_f32 v116, 0x47800000, v116\n\t" \
  "v_cvt_u32_f32 v117, v117\n\t" \
  "v_cvt_u32_f32 v116, v116\n\t" \
  "s_mov_b32 s53, s93\n\t" \
  "v_readfirstlane_b32 s46, v117\n\t" \
  "v_readfirstlane_b32 s52, v116\n\t" \
  "21:\n\t" \
  "s_add_i32 s44, s44, 1\n\t" \
  "s_and_b32 s58, s58, 4\n\t" \
  "s_and_b32 s76, s44, 64\n\t" \
  "s_or_b32 s42, s42, s76\n\t" \
  "s_cbranch_scc0 20b\n\t" \
  "s_branch 99f\n\t" \
  "14:\n\t" \
  "s_add_u32 s59, s59, 0x10000\n\t" \
  "s_cmp_lt_u32 s59, 0x100000\n\t" \
  "s_cbranch_scc1 13b\n\t" \
  "s_mov_b64 exec, -1\n\t" \
  "s_mov_b32 s42, 16\n\t" \
  "s_branch 99f\n\t" \
  "8:\n\t" \
  "s_bitcmp1_b32 s58, 1\n\t" \
  "s_cbranch_scc1 4b\n\t" \
  "s_bitcmp1_b32 s58, 2\n\t" \
  "s_cbranch_scc1 5b\n\t" \
  "v_max3_u32 v116, v32, v33, v34\n\t" \
  "v_max3_u32 v117, v35, v36, v37\n\t" \
  "v_max3_u32 v116, v116, v38, v39\n\t" \
  "v_max_u32 v116, v116, v117\n\t" \
  "v_cmp_eq_u32 vcc, -1, v116\n\t" \
  "s_cbranch_vccz 5b\n\t" \
  "s_branch 0f\n\t" \
  "9:\n\t" \
  "s_bitcmp1_b32 s58, 2\n\t" \
  "s_cbranch_scc1 6b\n\t" \
  "v_max3_u32 v116, v40, v41, v42\n\t" \
  "v_max3_u32 v117, v43, v44, v45\n\t" \
  "v_max3_u32 v116, v116, v46, v47\n\t" \
  "v_max_u32 v116, v116, v117\n\t" \
  "v_cmp_eq_u32 vcc, -1, v116\n\t" \
  "s_cbranch_vccz 6b\n\t" \
  "s_or_b32 s58, s58, 2\n\t" \
  "0:\n\t" \
  "s_or_b32 s58, s58, 1\n\t" \
  "11:\n\t" \
  "buffer_load_dwordx4 v[32:35], v100, %[rh], s40 offen sc1\n\t" \
  "buffer_load_dwordx4 v[36:39], v100, %[rh], s76 offen sc1\n\t" \
  "buffer_load_dwordx4 v[40:43], v100, %[rh], s77 offen sc1\n\t" \
  "buffer_load_dwordx4 v[44:47], v100, %[rh], s78 offen sc1\n\t" \
  "s_waitcnt lgkmcnt(0)\n\t" \
  "s_branch 10b\n\t" \
  "17:\n\t" \
  "s_add_u32 s59, s59, 1\n\t" \
  "s_bitcmp1_b32 s58, 2\n\t" \
  "s_cbranch_scc1 3b\n\t" \
  "s_waitcnt vmcnt(0)\n\t" \
  "v_max_u32 v116, v32, v33\n\t" \
  "v_max3_u32 v116, v116, v34, v35\n\t" \
  "v_max3_u32 v116, v116, v36, v37\n\t" \
  "v_max3_u32 v116, v116, v38, v39\n\t" \
  "v_max3_u32 v116, v116, v40, v41\n\t" \
  "v_max3_u32 v116, v116, v42, v43\n\t" \
  "v_max3_u32 v116, v116, v44, v45\n\t" \
  "v_max3_u32 v116, v116, v46, v47\n\t" \
  "v_cmp_eq_u32 vcc, -1, v116\n\t" \
  "s_cbranch_vccz 3b\n\t" \
  "s_bitcmp1_b32 s59, 6\n\t" \
  "s_cbranch_scc1 12f\n\t" \
  "buffer_load_dwordx4 v[32:35], v100, %[rh], s40 offen sc1\n\t" \
  "buffer_load_dwordx4 v[36:39], v100, %[rh], s76 offen sc1\n\t" \
  "buffer_load_dwordx4 v[40:43], v100, %[rh], s77 offen sc1\n\t" \
  "buffer_load_dwordx4 v[44:47], v100, %[rh], s78 offen sc1\n\t" \
  "s_branch 17b\n\t" \
  "12:\n\t" \
  "s_mov_b32 s42, s58\n\t" \
  "s_branch 99f\n\t" \
  "99:\n\t" \
  "s_waitcnt vmcnt(0) lgkmcnt(0)\n\t" \
  ".Lchain_last:\n\t" \
  ""
#define CB_BODY \
  "s_nop 1\n\t" \
  "s_cmp_eq_u32 %[ux], -1\n\t" \
  "s_cbranch_scc1 25f\n\t" \
  "s_add_i32 s79, %[ux], 0x60000\n\t" \
  "buffer_load_dwordx4 a[12:15], v110, %[ru], s79 offen offset:0 sc1\n\t" \
  "buffer_load_dwordx4 a[44:47], v110, %[ru], s79 offen offset:1024 sc1\n\t" \
  "buffer_load_dwordx4 a[76:79], v111, %[ru], s79 offen offset:0 sc1\n\t" \
  "buffer_load_dwordx4 a[108:111], v111, %[ru], s79 offen offset:1024 sc1\n\t" \
  "s_add_i32 s79, s79, 0x20000\n\t" \
  "buffer_load_dwordx4 a[16:19], v110, %[ru], s79 offen offset:0 sc1\n\t" \
  "buffer_load_dwordx4 a[48:51], v110, %[ru], s79 offen offset:1024 sc1\n\t" \
  "buffer_load_dwordx4 a[80:83], v111, %[ru], s79 offen offset:0 sc1\n\t" \
  "buffer_load_dwordx4 a[112:115], v111, %[ru], s79 offen offset:1024 sc1\n\t" \
  "s_add_i32 s79, s79, 0x20000\n\t" \
  "buffer_load_dwordx4 a[20:23], v110, %[ru], s79 offen offset:0 sc1\n\t" \
  "buffer_load_dwordx4 a[52:55], v110, %[ru], s79 offen offset:1024 sc1\n\t" \
  "buffer_load_dwordx4 a[84:87], v111, %[ru], s79 offen offset:0 sc1\n\t" \
  "buffer_load_dwordx4 a[116:119], v111, %[ru], s79 offen offset:1024 sc1\n\t" \
  "s_add_i32 s79, s79, 0x20000\n\t" \
  "buffer_load_dwordx4 a[24:27], v110, %[ru], s79 offen offset:0 sc1\n\t" \
  "buffer_load_dwordx4 a[56:59], v110, %[ru], s79 offen offset:1024 sc1\n\t" \
  "buffer_load_dwordx4 a[88:91], v111, %[ru], s79 offen offset:0 sc1\n\t" \
  "buffer_load_dwordx4 a[120:123], v111, %[ru], s79 offen offset:1024 sc1\n\t" \
  "s_add_i32 s79, s79, 0x20000\n\t" \
  "buffer_load_dwordx4 a[28:31], v110, %[ru], s79 offen offset:0 sc1\n\t" \
  "buffer_load_dwordx4 a[60:63], v110, %[ru], s79 offen offset:1024 sc1\n\t" \
  "buffer_load_dwordx4 a[92:95], v111, %[ru], s79 offen offset:0 sc1\n\t" \
  "buffer_load_dwordx4 a[124:127], v111, %[ru], s79 offen offset:1024 sc1\n\t" \
  "25:\n\t" \
  "s_mov_b32 s92, -1\n\t" \
  "v_mov_b32 v41, 0\n\t" \
  "s_mov_b32 s58, s42\n\t" \
  "s_mov_b32 s59, 0\n\t" \
  "s_xor_b32 s76, s43, 0x400\n\t" \
  "s_xor_b32 s77, s43, 0x800\n\t" \
  "s_xor_b32 s78, s43, 0xc00\n\t" \
  "s_mov_b32 s84, 600\n\t" \
  "s_bitcmp1_b32 s58, 1\n\t" \
  "s_cbranch_scc1 11f\n\t" \
  "s_mov_b64 exec, 0xffff\n\t" \
  "s_mov_b32 s88, s49\n\t" \
  "v_mov_b32 v38, -1\n\t" \
  "s_add_i32 s85, s47, -1\n\t" \
  "s_bitcmp1_b32 s58, 3\n\t" \
  "s_cbranch_scc1 23f\n\t" \
  "16:\n\t" \
  "s_memtime vcc\n\t" \
  "s_waitcnt lgkmcnt(0)\n\t" \
  "s_sub_i32 s79, s40, vcc_lo\n\t" \
  "s_cmp_gt_i32 s79, 64\n\t" \
  "s_cbranch_scc1 16b\n\t" \
  "buffer_load_dword v38, v108, %[rh], 0 offen sc1\n\t" \
  "global_load_dword v40, v41, %[uc] sc1\n\t" \
  "s_cmp_eq_u32 %[ux], -1\n\t" \
  "s_cbranch_scc1 21f\n\t" \
  "buffer_load_dword v36, v109, %[rh], s54 offen sc1\n\t" \
  "s_mov_b64 exec, -1\n\t" \
  "s_add_i32 s79, %[ux], 0x100000\n\t" \
  "s_mov_b64 exec, 0xffff\n\t" \
  "s_waitcnt vmcnt(0)\n\t" \
  "s_branch 27f\n\t" \
  "21:\n\t" \
  "buffer_load_dword v36, v109, %[rh], s54 offen sc1\n\t" \
  "s_waitcnt vmcnt(0)\n\t" \
  "27:\n\t" \
  "v_cmp_ne_u32_sdwa vcc, v36, s85 src0_sel:BYTE_0 src1_sel:DWORD\n\t" \
  "v_subrev_u32 v36, s48, v36\n\t" \
  "v_ashrrev_i32 v36, 8, v36\n\t" \
  "s_nop 1\n\t" \
  "v_max_i32_dpp v36, v36, v36 quad_perm:[1,0,3,2] row_mask:0xf bank_mask:0xf\n\t" \
  "s_cbranch_vccnz 22f\n\t" \
  "24:\n\t" \
  "s_nop 0\n\t" \
  "v_max_i32_dpp v36, v36, v36 quad_perm:[2,3,0,1] row_mask:0xf bank_mask:0xf\n\t" \
  "s_nop 1\n\t" \
  "v_max_i32_dpp v36, v36, v36 row_half_mirror row_mask:0xf bank_mask:0xf\n\t" \
  "s_nop 1\n\t" \
  "v_max_i32_dpp v36, v36, v36 row_mirror row_mask:0xf bank_mask:0xf\n\t" \
  "v_add_u32 v36, 1056, v36\n\t" \
  "v_med3_i32 v36, v36, 0, v103\n\t" \
  "v_mul_u32_u24 v36, s50, v36\n\t" \
  "v_lshrrev_b32 v36, 16, v36\n\t" \
  "s_nop 0\n\t" \
  "v_readfirstlane_b32 s88, v36\n\t" \
  "v_readfirstlane_b32 s92, v40\n\t" \
  "s_add_i32 s88, s88, s49\n\t" \
  "s_cmp_ge_i32 s92, 31\n\t" \
  "s_cselect_b32 s92, s55, -1\n\t" \
  "23:\n\t" \
  "1:\n\t" \
  "s_memtime vcc\n\t" \
  "s_waitcnt lgkmcnt(0)\n\t" \
  "s_sub_i32 s79, s88, vcc_lo\n\t" \
  "s_cmp_gt_i32 s79, 128\n\t" \
  "s_cbranch_scc1 1b\n\t" \
  "2:\n\t" \
  "s_sub_i32 s79, s79, 33\n\t" \
  "s_cmp_gt_i32 s79, 56\n\t" \
  "s_cbranch_scc1 2b\n\t" \
  "13:\n\t" \
  "buffer_load_dword v36, v104, %[rh], s41 offen sc1\n\t" \
  "v_min_u32 v39, 1760, v38\n\t" \
  "s_nop 1\n\t" \
  "v_max_u32_dpp v39, v39, v39 quad_perm:[1,0,3,2] row_mask:0xf bank_mask:0xf\n\t" \
  "s_nop 1\n\t" \
  "v_max_u32_dpp v39, v39, v39 quad_perm:[2,3,0,1] row_mask:0xf bank_mask:0xf\n\t" \
  "s_nop 1\n\t" \
  "v_max_u32_dpp v39, v39, v39 row_half_mirror row_mask:0xf bank_mask:0xf\n\t" \
  "s_nop 1\n\t" \
  "v_max_u32_dpp v39, v39, v39 row_mirror row_mask:0xf bank_mask:0xf\n\t" \
  "s_nop 1\n\t" \
  "v_readfirstlane_b32 s89, v39\n\t" \
  "s_add_i32 s89, s89, s52\n\t" \
  "s_waitcnt vmcnt(0)\n\t" \
  "s_memtime s[86:87]\n\t" \
  "v_cmp_ne_u32_sdwa vcc, v36, s47 src0_sel:BYTE_0 src1_sel:DWORD\n\t" \
  "v_subrev_u32 v36, s48, v36\n\t" \
  "v_ashrrev_i32 v36, 8, v36\n\t" \
  "s_nop 1\n\t" \
  "v_max_i32_dpp v36, v36, v36 quad_perm:[1,0,3,2] row_mask:0xf bank_mask:0xf\n\t" \
  "s_nop 1\n\t" \
  "v_max_i32_dpp v36, v36, v36 quad_perm:[2,3,0,1] row_mask:0xf bank_mask:0xf\n\t" \
  "s_cbranch_vccnz 14f\n\t" \
  "15:\n\t" \
  "s_nop 0\n\t" \
  "v_max_i32_dpp v36, v36, v36 row_half_mirror row_mask:0xf bank_mask:0xf\n\t" \
  "v_add_u32 v37, 255, v36\n\t" \
  "v_med3_i32 v37, v37, 0, v103\n\t" \
  "v_mul_u32_u24 v37, s50, v37\n\t" \
  "v_readlane_b32 s82, v36, 0\n\t" \
  "v_lshrrev_b32 v37, 16, v37\n\t" \
  "s_waitcnt lgkmcnt(0)\n\t" \
  "s_sub_i32 s79, s49, s86\n\t" \
  "v_readlane_b32 s81, v37, 0\n\t" \
  "s_mov_b64 exec, -1\n\t" \
  "s_add_i32 s80, s81, -80\n\t" \
  "s_add_i32 s79, s79, -310\n\t" \
  "s_add_i32 s79, s79, s80\n\t" \
  "s_sub_i32 s83, s81, s80\n\t" \
  "s_sub_i32 s85, s86, s49\n\t" \
  "s_add_i32 s85, s85, s84\n\t" \
  "s_add_i32 s84, s80, -220\n\t" \
  "s_max_i32 s85, s85, s84\n\t" \
  "s_sub_i32 s85, s85, s81\n\t" \
  "s_max_i32 s85, s85, 0\n\t" \
  "s_mul_hi_u32 s85, s85, s51\n\t" \
  "s_lshl1_add_u32 s85, s85, s82\n\t" \
  "s_add_i32 s85, s85, s89\n\t" \
  "s_lshl_b32 s85, s85, 8\n\t" \
  "s_or_b32 s85, s85, s47\n\t" \
  "v_mov_b32 v37, s85\n\t" \
  "buffer_store_dword v37, v106, %[rh], s53 offen sc1\n\t" \
  "s_lshr_b32 s85, s48, 8\n\t" \
  "s_add_i32 s85, s85, s82\n\t" \
  "v_mov_b32 v36, s85\n\t" \
  "ds_write_b32 v107, v36\n\t" \
  "3:\n\t" \
  "s_sub_i32 s79, s79, 33\n\t" \
  "s_cmp_gt_i32 s79, 56\n\t" \
  "s_cbranch_scc1 3b\n\t" \
  "buffer_load_dwordx4 v[64:67], v97, %[ru], s43 offen offset:0 sc1\n\t" \
  "buffer_load_dwordx4 v[72:75], v97, %[ru], s43 offen offset:512 sc1\n\t" \
  "buffer_load_dwordx4 v[80:83], v97, %[ru], s76 offen offset:0 sc1\n\t" \
  "buffer_load_dwordx4 v[88:91], v97, %[ru], s76 offen offset:512 sc1\n\t" \
  "s_add_i32 s79, s79, 102\n\t" \
  "4:\n\t" \
  "s_sub_i32 s79, s79, 33\n\t" \
  "s_cmp_gt_i32 s79, 56\n\t" \
  "s_cbranch_scc1 4b\n\t" \
  "buffer_load_dwordx4 v[68:71], v97, %[ru], s77 offen offset:0 sc1\n\t" \
  "buffer_load_dwordx4 v[76:79], v97, %[ru], s77 offen offset:512 sc1\n\t" \
  "s_add_i32 s79, s79, s83\n\t" \
  "s_add_i32 s79, s79, -74\n\t" \
  "5:\n\t" \
  "s_sub_i32 s79, s79, 33\n\t" \
  "s_cmp_gt_i32 s79, 56\n\t" \
  "s_cbranch_scc1 5b\n\t" \
  "buffer_load_dwordx4 v[0:3], v96, %[rh], s44 offen offset:0 sc1\n\t" \
  "v_mov_b64 v[20:21], 0\n\t" \
  "v_mov_b64 v[22:23], 0\n\t" \
  "s_nop 10\n\t" \
  "buffer_load_dwordx4 v[4:7], v96, %[rh], s44 offen offset:1024 sc1\n\t" \
  "v_mov_b64 v[24:25], 0\n\t" \
  "v_mov_b64 v[26:27], 0\n\t" \
  "s_nop 10\n\t" \
  "buffer_load_dwordx4 v[8:11], v96, %[rh], s44 offen offset:2048 sc1\n\t" \
  "v_mov_b64 v[28:29], 0\n\t" \
  "v_mov_b64 v[30:31], 0\n\t" \
  "s_nop 10\n\t" \
  "buffer_load_dwordx4 v[12:15], v96, %[rh], s44 offen offset:3072 sc1\n\t" \
  "v_mov_b64 v[32:33], 0\n\t" \
  "v_mov_b64 v[34:35], 0\n\t" \
  "s_nop 10\n\t" \
  "s_sub_i32 s79, s79, s83\n\t" \
  "s_sub_i32 s79, s79, 234\n\t" \
  "9:\n\t" \
  "s_sub_i32 s79, s79, 33\n\t" \
  "s_cmp_gt_i32 s79, 56\n\t" \
  "s_cbranch_scc1 9b\n\t" \
  "buffer_load_dwordx4 v[84:87], v97, %[ru], s78 offen offset:0 sc1\n\t" \
  "buffer_load_dwordx4 v[92:95], v97, %[ru], s78 offen offset:512 sc1\n\t" \
  "s_nop 15\n\t" \
  "buffer_load_dwordx4 v[16:19], v98, %[rh], s45 offen offset:0 sc1\n\t" \
  "10:\n\t" \
  "s_waitcnt vmcnt(9)\n\t" \
  "v_swap_b32 v65, v80\n\t" \
  "v_swap_b32 v67, v82\n\t" \
  "v_swap_b32 v73, v88\n\t" \
  "v_swap_b32 v75, v90\n\t" \
  "s_waitcnt vmcnt(6)\n\t" \
  "v_smfmac_f32_16x16x64_bf16 v[20:23], v[0:3], a[0:7], v99\n\t" \
  "v_smfmac_f32_16x16x64_bf16 v[24:27], v[0:3], a[32:39], v99\n\t" \
  "v_smfmac_f32_16x16x64_bf16 v[28:31], v[0:3], a[64:71], v99\n\t" \
  "v_smfmac_f32_16x16x64_bf16 v[32:35], v[0:3], a[96:103], v99\n\t" \
  "s_waitcnt vmcnt(5)\n\t" \
  "v_smfmac_f32_16x16x64_bf16 v[20:23], v[4:7], a[8:15], v99\n\t" \
  "v_smfmac_f32_16x16x64_bf16 v[24:27], v[4:7], a[40:47], v99\n\t" \
  "v_smfmac_f32_16x16x64_bf16 v[28:31], v[4:7], a[72:79], v99\n\t" \
  "v_smfmac_f32_16x16x64_bf16 v[32:35], v[4:7], a[104:111], v99\n\t" \
  "s_waitcnt vmcnt(4)\n\t" \
  "v_smfmac_f32_16x16x64_bf16 v[20:23], v[8:11], a[16:23], v99\n\t" \
  "v_smfmac_f32_16x16x64_bf16 v[24:27], v[8:11], a[48:55], v99\n\t" \
  "v_smfmac_f32_16x16x64_bf16 v[28:31], v[8:11], a[80:87], v99\n\t" \
  "v_smfmac_f32_16x16x64_bf16 v[32:35], v[8:11], a[112:119], v99\n\t" \
  "s_waitcnt vmcnt(3)\n\t" \
  "v_smfmac_f32_16x16x64_bf16 v[20:23], v[12:15], a[24:31], v99\n\t" \
  "v_smfmac_f32_16x16x64_bf16 v[24:27], v[12:15], a[56:63], v99\n\t" \
  "v_smfmac_f32_16x16x64_bf16 v[28:31], v[12:15], a[88:95], v99\n\t" \
  "v_smfmac_f32_16x16x64_bf16 v[32:35], v[12:15], a[120:127], v99\n\t" \
  "s_waitcnt vmcnt(1)\n\t" \
  "v_swap_b32 v69, v84\n\t" \
  "v_swap_b32 v71, v86\n\t" \
  "v_swap_b32 v77, v92\n\t" \
  "v_swap_b32 v79, v94\n\t" \
  "s_nop 0\n\t" \
  "s_waitcnt vmcnt(0)\n\t" \
  "v_smfmac_f32_16x16x64_bf16 v[20:23], v[16:19], v[64:71], v99\n\t" \
  "v_smfmac_f32_16x16x64_bf16 v[24:27], v[16:19], v[72:79], v99\n\t" \
  "v_smfmac_f32_16x16x64_bf16 v[28:31], v[16:19], v[80:87], v99\n\t" \
  "v_add_f32 v36, v20, v22\n\t" \
  "v_smfmac_f32_16x16x64_bf16 v[32:35], v[16:19], v[88:95], v99\n\t" \
  "v_add_f32 v37, v21, v23\n\t" \
  "v_add_f32 v38, v24, v26\n\t" \
  "v_add_f32 v39, v25, v27\n\t" \
  "ds_write2st64_b32 v100, v36, v37 offset0:0 offset1:1\n\t" \
  "ds_write2st64_b32 v100, v38, v39 offset0:8 offset1:9\n\t" \
  "v_add_f32 v40, v28, v30\n\t" \
  "v_add_f32 v41, v29, v31\n\t" \
  "ds_write2st64_b32 v100, v40, v41 offset0:16 offset1:17\n\t" \
  "v_add_f32 v42, v32, v34\n\t" \
  "v_add_f32 v43, v33, v35\n\t" \
  "ds_write2st64_b32 v100, v42, v43 offset0:24 offset1:25\n\t" \
  "v_cmp_u_f32 vcc, v36, v37\n\t" \
  "v_cmp_u_f32 s[80:81], v38, v40\n\t" \
  "v_cmp_u_f32 s[82:83], v42, v42\n\t" \
  "s_or_b64 vcc, vcc, s[80:81]\n\t" \
  "s_or_b64 vcc, vcc, s[82:83]\n\t" \
  "s_cbranch_scc1 8f\n\t" \
  "6:\n\t" \
  "s_waitcnt lgkmcnt(0)\n\t" \
  "s_barrier\n\t" \
  "ds_read_b64 v[44:45], v101 offset:0\n\t" \
  "ds_read_b64 v[46:47], v101 offset:512\n\t" \
  "ds_read_b64 v[48:49], v101 offset:1024\n\t" \
  "ds_read_b64 v[50:51], v101 offset:1536\n\t" \
  "s_waitcnt lgkmcnt(2)\n\t" \
  "v_add_f32 v44, v44, v46\n\t" \
  "v_add_f32 v45, v45, v47\n\t" \
  "s_waitcnt lgkmcnt(0)\n\t" \
  "v_add_f32 v48, v48, v50\n\t" \
  "v_add_f32 v49, v49, v51\n\t" \
  "v_add_f32 v44, v44, v48\n\t" \
  "v_add_f32 v45, v45, v49\n\t" \
  "v_cvt_pk_bf16_f32 v44, v44, v45\n\t" \
  "buffer_store_dword v44, v102, %[rh], s46 offen sc1\n\t" \
  "s_memtime s[56:57]\n\t" \
  "s_mov_b32 s58, 16\n\t" \
  "s_cmp_eq_u32 s92, -1\n\t" \
  "s_cbranch_scc1 7f\n\t" \
  "s_nop 8\n\t" \
  "s_mov_b32 s58, 0\n\t" \
  "s_mov_b32 s79, s92\n\t" \
  "buffer_load_dwordx4 a[0:3], v110, %[ru], s79 offen offset:0 sc1\n\t" \
  "buffer_load_dwordx4 a[32:35], v110, %[ru], s79 offen offset:1024 sc1\n\t" \
  "buffer_load_dwordx4 a[64:67], v111, %[ru], s79 offen offset:0 sc1\n\t" \
  "buffer_load_dwordx4 a[96:99], v111, %[ru], s79 offen offset:1024 sc1\n\t" \
  "s_add_i32 s79, s79, 0x20000\n\t" \
  "buffer_load_dwordx4 a[4:7], v110, %[ru], s79 offen offset:0 sc1\n\t" \
  "buffer_load_dwordx4 a[36:39], v110, %[ru], s79 offen offset:1024 sc1\n\t" \
  "buffer_load_dwordx4 a[68:71], v111, %[ru], s79 offen offset:0 sc1\n\t" \
  "buffer_load_dwordx4 a[100:103], v111, %[ru], s79 offen offset:1024 sc1\n\t" \
  "s_add_i32 s79, s79, 0x20000\n\t" \
  "buffer_load_dwordx4 a[8:11], v110, %[ru], s79 offen offset:0 sc1\n\t" \
  "buffer_load_dwordx4 a[40:43], v110, %[ru], s79 offen offset:1024 sc1\n\t" \
  "buffer_load_dwordx4 a[72:75], v111, %[ru], s79 offen offset:0 sc1\n\t" \
  "buffer_load_dwordx4 a[104:107], v111, %[ru], s79 offen offset:1024 sc1\n\t" \
  "s_branch 7f\n\t" \
  "22:\n\t" \
  "s_bitcmp1_b32 s58, 2\n\t" \
  "s_cbranch_scc1 24b\n\t" \
  "s_add_u32 s59, s59, 0x10000\n\t" \
  "s_cmp_lt_u32 s59, 0x100000\n\t" \
  "s_cbranch_scc1 21b\n\t" \
  "s_mov_b64 exec, -1\n\t" \
  "s_mov_b32 s58, 1\n\t" \
  "s_branch 7f\n\t" \
  "14:\n\t" \
  "s_bitcmp1_b32 s58, 2\n\t" \
  "s_cbranch_scc1 15b\n\t" \
  "s_mov_b32 s84, 900\n\t" \
  "s_add_u32 s59, s59, 0x10000\n\t" \
  "s_cmp_lt_u32 s59, 0x100000\n\t" \
  "s_cbranch_scc1 13b\n\t" \
  "s_mov_b64 exec, -1\n\t" \
  "s_or_b32 s58, s58, 1\n\t" \
  "s_branch 7f\n\t" \
  "8:\n\t" \
  "s_bitcmp1_b32 s58, 2\n\t" \
  "s_cbranch_scc1 6b\n\t" \
  "s_mov_b32 s80, 0\n\t" \
  "17:\n\t" \
  "v_max_u32 v52, v0, v1\n\t" \
  "v_max3_u32 v52, v52, v2, v3\n\t" \
  "v_max3_u32 v52, v52, v4, v5\n\t" \
  "v_max3_u32 v52, v52, v6, v7\n\t" \
  "v_max3_u32 v52, v52, v8, v9\n\t" \
  "v_max3_u32 v52, v52, v10, v11\n\t" \
  "v_max3_u32 v52, v52, v12, v13\n\t" \
  "v_max3_u32 v52, v52, v14, v15\n\t" \
  "v_max3_u32 v52, v52, v16, v17\n\t" \
  "v_max3_u32 v52, v52, v18, v19\n\t" \
  "v_max_u32 v51, v64, v65\n\t" \
  "v_max3_u32 v51, v51, v66, v67\n\t" \
  "v_max3_u32 v51, v51, v68, v69\n\t" \
  "v_max3_u32 v51, v51, v70, v71\n\t" \
  "v_max3_u32 v51, v51, v72, v73\n\t" \
  "v_max3_u32 v51, v51, v74, v75\n\t" \
  "v_max3_u32 v51, v51, v76, v77\n\t" \
  "v_max3_u32 v51, v51, v78, v79\n\t" \
  "v_max3_u32 v51, v51, v80, v81\n\t" \
  "v_max3_u32 v51, v51, v82, v83\n\t" \
  "v_max3_u32 v51, v51, v84, v85\n\t" \
  "v_max3_u32 v51, v51, v86, v87\n\t" \
  "v_max3_u32 v51, v51, v88, v89\n\t" \
  "v_max3_u32 v51, v51, v90, v91\n\t" \
  "v_max3_u32 v51, v51, v92, v93\n\t" \
  "v_max3_u32 v51, v51, v94, v95\n\t" \
  "v_cmp_eq_u32 s[82:83], -1, v52\n\t" \
  "v_cmp_eq_u32 vcc, -1, v51\n\t" \
  "s_or_b64 s[84:85], vcc, s[82:83]\n\t" \
  "s_cbranch_scc0 18f\n\t" \
  "s_or_b32 s58, s58, 3\n\t" \
  "s_add_u32 s59, s59, 1\n\t" \
  "s_bitcmp1_b32 s59, 6\n\t" \
  "s_cbranch_scc1 7f\n\t" \
  "s_or_b32 s80, s80, 1\n\t" \
  "s_cbranch_vccz 19f\n\t" \
  "11:\n\t" \
  "s_mov_b32 s80, 3\n\t" \
  "buffer_load_dwordx4 v[64:67], v97, %[ru], s43 offen offset:0 sc1\n\t" \
  "buffer_load_dwordx4 v[72:75], v97, %[ru], s43 offen offset:512 sc1\n\t" \
  "buffer_load_dwordx4 v[80:83], v97, %[ru], s76 offen offset:0 sc1\n\t" \
  "buffer_load_dwordx4 v[88:91], v97, %[ru], s76 offen offset:512 sc1\n\t" \
  "buffer_load_dwordx4 v[68:71], v97, %[ru], s77 offen offset:0 sc1\n\t" \
  "buffer_load_dwordx4 v[76:79], v97, %[ru], s77 offen offset:512 sc1\n\t" \
  "buffer_load_dwordx4 v[84:87], v97, %[ru], s78 offen offset:0 sc1\n\t" \
  "buffer_load_dwordx4 v[92:95], v97, %[ru], s78 offen offset:512 sc1\n\t" \
  "19:\n\t" \
  "buffer_load_dwordx4 v[0:3], v96, %[rh], s44 offen offset:0 sc1\n\t" \
  "buffer_load_dwordx4 v[4:7], v96, %[rh], s44 offen offset:1024 sc1\n\t" \
  "buffer_load_dwordx4 v[8:11], v96, %[rh], s44 offen offset:2048 sc1\n\t" \
  "buffer_load_dwordx4 v[12:15], v96, %[rh], s44 offen offset:3072 sc1\n\t" \
  "buffer_load_dwordx4 v[16:19], v98, %[rh], s45 offen offset:0 sc1\n\t" \
  "s_waitcnt vmcnt(0)\n\t" \
  "s_bitcmp1_b32 s58, 2\n\t" \
  "s_cbranch_scc0 17b\n\t" \
  "18:\n\t" \
  "s_cmp_eq_u32 s80, 0\n\t" \
  "s_cbranch_scc1 6b\n\t" \
  "v_mov_b64 v[20:21], 0\n\t" \
  "v_mov_b64 v[22:23], 0\n\t" \
  "v_mov_b64 v[24:25], 0\n\t" \
  "v_mov_b64 v[26:27], 0\n\t" \
  "v_mov_b64 v[28:29], 0\n\t" \
  "v_mov_b64 v[30:31], 0\n\t" \
  "v_mov_b64 v[32:33], 0\n\t" \
  "v_mov_b64 v[34:35], 0\n\t" \
  "s_cmp_eq_u32 s80, 3\n\t" \
  "s_cbranch_scc1 10b\n\t" \
  "v_swap_b32 v65, v80\n\t" \
  "v_swap_b32 v67, v82\n\t" \
  "v_swap_b32 v73, v88\n\t" \
  "v_swap_b32 v75, v90\n\t" \
  "v_swap_b32 v69, v84\n\t" \
  "v_swap_b32 v71, v86\n\t" \
  "v_swap_b32 v77, v92\n\t" \
  "v_swap_b32 v79, v94\n\t" \
  "s_branch 10b\n\t" \
  "7:\n\t" \
  "s_waitcnt lgkmcnt(0)\n\t" \
  ""

#define V53 "v0", "v1", "v2", "v3", "v4", "v5", "v6", "v7", "v8", "v9", "v10", "v11", "v12", "v13", "v14", "v15", "v16", "v17", \
  "v18", "v19", "v20", "v21", "v22", "v23", "v24", "v25", "v26", "v27", "v28", "v29", "v30", "v31", "v32", "v33", "v34", "v35", \
  "v36", "v37", "v38", "v39", "v40", "v41", "v42", "v43", "v44", "v45", "v46", "v47", "v48", "v49", "v50", "v51", "v52"
#define V100 V53, "v53", "v54", "v55", "v56", "v57", "v58", "v59", "v60", "v61", "v62", "v63", "v64", "v65", "v66", "v67", "v68", "v69", "v70", \
  "v71", "v72", "v73", "v74", "v75", "v76", "v77", "v78", "v79", "v80", "v81", "v82", "v83", "v84", "v85", "v86", "v87", "v88", \
  "v89", "v90", "v91", "v116", "v117", "v118"
#define STEP_PV
#define CB_PV
#define CB_CL "s88", "s89",
#define STEPS(BODY, ...) asm volatile(BODY : "=&{v[92:99]}"(X), "+{s[40:55]}"(P) STEP_PV \
  : "{v[120:151]}"(W.v[0]), "{v[152:183]}"(W.v[1]), "{v[184:215]}"(W.v[2]), "{v[216:247]}"(W.v[3]), "{v[100:115]}"(C), [rh] "s"(rh), [wv] "s"(w), [ru] "s"(rum), [sel] "s"(sel[0]) __VA_ARGS__ \
  : "memory", "vcc", "scc", "s56", "s57", "s58", "s59", "s76", "s77", "s78", "s79", "s80", "s81", "s82", "s83", "s84", "s85", "s86", "s87", "s88", "s89", \
    "s90", "s91", "s92", "s93", "s94", "s95", "s96", "s97", V100)
__device__ __forceinline__ void keep_a(const Wts& W) {
#pragma unroll
  for (int i = 0; i < 32; ++i) asm volatile("" :: "a"(W.a[i]));
}
__device__ __forceinline__ char* lds_base(char* smem, int off) { asm volatile("" : "+v"(off)); return smem + off; }
__device__ __forceinline__ u32x2 tr4(unsigned p) { return __builtin_bit_cast(u32x2, __builtin_amdgcn_ds_read_tr16_b64_v4i16((LAS bf16x4*)(uintptr_t)p)); }
__device__ __forceinline__ float rb1(float x) { return __uint_as_float(pk2(0.f, x)); }

__device__ __forceinline__ void steps_init(u32x16& P, int h, int w, unsigned place) {
#pragma unroll
  for (int i = 0; i < 16; ++i) P[i] = 0;
  P[2] = 1; P[3] = 1490; P[14] = H_MEM + (w >> 1) * CHK + (w & 1) * 4096 + (h & RHO) * 1024; P[15] = place;
}
__device__ __forceinline__ void steps_enter(u32x16& P, Clk& ck, bool dead) {
  ck.sync();
  P[2] = (P[2] & 3u) | (dead ? 4u : 0u); P[6] = ck.c2t << 15; P[11] = ck.ce; P[12] = ck.t2c; P[13] = ck.te;
#pragma unroll
  for (int i = 0; i < 16; ++i) P[i] = __builtin_amdgcn_readfirstlane(P[i]);
}

__device__ __forceinline__ void role_qk(const ChainArgs& a, int h, char* smem, const int w, Gate& G) {
  int lane = opaque_tid(0);
  const int b = h; (void)b;
  u64* prof = a.prof; (void)prof;
  PDECL(L_PRF, 4);
  const float CS = 0.08838834764831845f * 1.4426950408889634f;
  GAS unsigned* abortw = (GAS unsigned*)(a.sync + SY_ABORT);
  bool dead = false;
  Wts W;
  Clk ck; ck.init();
  load_weights<0>(a, h, w, lane, smem, W);
  if (!G.pass(a.sync)) return;
  PZERO;
  const Sel sel = sel_wait(a.sync, abortw, lane, dead, CT_K + h);
  const __amdgpu_buffer_rsrc_t rh = rsrc_hot(a.sync, sel), rum = rsrc_u(a.sync);
  const __amdgpu_buffer_rsrc_t rk = mk_rsrc((void*)a.Kx, 16u << 20), rkm = mk_rsrc((void*)a.Km, 4u << 20);
#pragma unroll
  for (int i = 0; i < 2; ++i) *(u32x4*)(smem + L_KX + (2 * w + i) * 1024 + lane * 16) = ld16(rk, h * 8192 + (2 * w + i) * 1024 + lane * 16);
  auto px_o = [&](int l) { return (h >> 4) * CHK + (h & 15) * 512 + (((l >> 1) & 1) * 16 + arow(l >> 3, l & 1)) * 16 + ((l >> 2) & 1) * 8; };
  auto pm_o = [&](int l) { return (l & 1) ? 0x7fffffffu : 2 * CHK + h * 128 + (l >> 3) * 16 + (l & 6) * 2; };
  u32x16 C, P;
  {
    const int r = lane & 15, g = lane >> 4;
    const unsigned l0 = (unsigned)(uintptr_t)(LAS char*)smem;
    C[0] = lane * 16; C[1] = l0 + L_MEM + lane * 16; C[2] = l0 + L_WTS + w * 4096 + lane * 16; C[3] = C[2] + 65536;
    C[4] = (lane & 2) ? 0xEEEE : 0x4444;
    C[5] = l0 + L_XCH + (16 * w + 2 * (r >> 2) + (r & 1)) * 32 + g * 8 + ((r >> 1) & 1) * 4;
    C[6] = l0 + L_XK + (32 * w + r) * 16 + g * 4;
    C[7] = l0 + L_XCH + (8 * g + (r >> 2)) * 32 + (r & 3) * 8;
    C[8] = l0 + L_XK + (8 * g + (r >> 2)) * 16 + (r & 1) * 8 + (w ? (r & 2) * 256 : 0);
    C[9] = lane == 0 ? H_ANN + h * 128 : 0x7fffffffu;
    C[10] = w == 0 ? px_o(lane) : w == 1 ? (h * 4 + (r >> 3)) * 512 + (g * 8 + (r & 7)) * 16 :
            lane < 32 ? (h >> 4) * CHK + (h & 15) * 512 + lane * 16 : (lane < 40 ? 2 * CHK + h * 128 + (lane - 32) * 16 : 0x7fffffffu);
    C[11] = pm_o(lane);
    C[12] = H_TR + (lane & 15) * 128; C[13] = l0 + L_PRF + w * 1024 + (lane >> 6);
    C[14] = C[15] = C[12];
    asm volatile("" : "+v"(C));
  }
  SSTAMP(96 + h);
  steps_init(P, h, w, h * 8192 + (3 * w - 3) * 1024);
  GAS unsigned* kcp = ctr(a, CT_K + h);
  unsigned spins = 0;
  while (true) {
    u32x8 X; u32x16 pv; (void)pv;
    steps_enter(P, ck, dead);
    STEPS(QK_BODY, , [cs] "s"(CS), [rk] "s"(rk), [rm] "s"(rkm), [kc] "s"(kcp));
    keep_a(W);
    const unsigned res = __builtin_amdgcn_readfirstlane(P[2]), s = __builtin_amdgcn_readfirstlane(P[4]);
    lane = opaque_tid(0);
    if (res & 8u) {
      auto sw = [](float v, int k) { return __int_as_float(k == 0 ? __builtin_amdgcn_mov_dpp(__float_as_int(v), 0xB1, 0xf, 0xf, false) :
                                                           k == 1 ? __builtin_amdgcn_mov_dpp(__float_as_int(v), 0x4E, 0xf, 0xf, false) :
                                                                    __builtin_amdgcn_mov_dpp(__float_as_int(v), 0x141, 0xf, 0xf, false)); };
      float x[5], d = fmaxf(__uint_as_float(X[4]), 0.f);
      d = fmaxf(d, sw(d, 0)); d = fmaxf(d, sw(d, 1)); d = fmaxf(d, sw(d, 2));
#pragma unroll
      for (int i = 0; i < 5; ++i) x[i] = __builtin_amdgcn_exp2f(__uint_as_float(X[i]) - d);
      float z = ((x[0] + x[1]) + (x[2] + x[3])) + x[4];
      z += sw(z, 0); z += sw(z, 1); z += sw(z, 2);
      z = __builtin_amdgcn_rcpf(z);
      const unsigned pm = nosent(pk2(x[4] * z, sw(x[4], 0) * z)), psl = H_P + ((s - 1) & 3) * 3 * CHK;
      if (!(__uint_as_float(X[5]) < 1.0e18f)) {
        __builtin_amdgcn_raw_buffer_store_b64((u32x2){nosent(pk2(x[0] * z, x[1] * z)), nosent(pk2(x[2] * z, x[3] * z))}, rh, px_o(lane), psl, ST_AUX);
        __builtin_amdgcn_raw_buffer_store_b32(pm, rh, pm_o(lane), psl, ST_AUX);
      }
    }
    if (res & 64u) break;
    if (res & 32u) ctr_wait(kcp, min(s + 2, (unsigned)NSEG), abortw, lane, dead);
    if (res & 17u) {
      spins |= 63u;
      if (dead || poll_fail(spins, abortw, lane)) dead = true;
    }
  }
  PDUMP(0);
}

__device__ __forceinline__ void role_vo(const ChainArgs& a, int h, char* smem, const int w, Gate& G) {
  int lane = opaque_tid(0);
  const int b = 32 + h; (void)b;
  u64* prof = a.prof; (void)prof;
  PDECL(L_PRF, 4);
  GAS unsigned* abortw = (GAS unsigned*)(a.sync + SY_ABORT);
  bool dead = false;
  Wts W;
  Clk ck; ck.init();
  load_weights<1>(a, h, w, lane, smem, W);
  if (!G.pass(a.sync)) return;
  PZERO;
  const Sel sel = sel_wait(a.sync, abortw, lane, dead, CT_INIT);
  const __amdgpu_buffer_rsrc_t rh = rsrc_hot(a.sync, sel), rum = rsrc_u(a.sync);
  u32x16 C, P;
  {
    const int r = lane & 15, g = lane >> 4;
    const unsigned l0 = (unsigned)(uintptr_t)(LAS char*)smem;
#pragma unroll
    for (int i = 0; i < 16; ++i) C[i] = H_TR + (lane & 15) * 128;
    C[0] = lane * 16; C[1] = l0 + L_MEM + lane * 16; C[2] = l0 + L_WTS + w * 4096 + lane * 16; C[3] = C[2] + 65536;
    C[4] = (lane & 2) ? 0xEEEE : 0x4444;
    C[5] = l0 + L_XCH + (32 * w + r) * 16 + g * 4;
    C[6] = (lane & 8) ? 0xEEEE : 0x4444;
    C[7] = l0 + L_XCH + (32 * (g >> 1) + 8 * (g & 1) + (r >> 2)) * 16 + (r & 1) * 8;
    C[13] = l0 + L_PRF + w * 1024 + (lane >> 6);
    asm volatile("" : "+v"(C));
  }
  SSTAMP(128 + h);
  steps_init(P, h, w, (16 * (h & 3) + 4 * w + (h >> 3)) * CHK + (h & 7) * 1024);
  unsigned spins = 0;
  while (true) {
    u32x8 X; u32x16 pv; (void)pv;
    steps_enter(P, ck, dead);
    STEPS(VO_BODY);
    keep_a(W);
    lane = opaque_tid(0);
    if (__builtin_amdgcn_readfirstlane(P[2]) & 64u) break;
    spins |= 63u;
    if (dead || poll_fail(spins, abortw, lane)) dead = true;
  }
  PDUMP(0);
}

#define USLP 48
__device__ __forceinline__ void calibrate(const ChainArgs& a, int cal) {
  const int lane = opaque_tid(0);
  const __amdgpu_buffer_rsrc_t rs = mk_rsrc((void*)a.sync, SY_BYTES);
  unsigned best[2] = {1u << 30, 1u << 30};
  for (int i = 0; i < 6; ++i) {
    if (i >= CALN && min(best[0], best[1]) < 600u && max(best[0], best[1]) - min(best[0], best[1]) > 120u) break;
#pragma unroll
    for (int hf = 0; hf < 2; ++hf) {
      const unsigned off = cal * PAGE + hf * 8192 + (lane & 7) * 16;
      st4(rs, off, SENT);
      u64 c0, c1;
      asm volatile("s_waitcnt vmcnt(0)\n\ts_memtime %0\n\ts_waitcnt lgkmcnt(0)" : "=s"(c0) :: "memory");
      u32x4 v = ld16(rs, off);
      asm volatile("s_waitcnt vmcnt(0)\n\ts_memtime %0\n\ts_waitcnt lgkmcnt(0)" : "=s"(c1), "+v"(v) :: "memory");
      best[hf] = min(best[hf], (unsigned)(c1 - c0));
    }
  }
  const unsigned hs = (best[1] < best[0] ? 1u : 0u) ^ die_half();
  if (lane == 0) __hip_atomic_store(ctr(a, CT_CAL + cal), hs, __ATOMIC_RELAXED, __HIP_MEMORY_SCOPE_AGENT);
}
constexpr int TPC = 4;
constexpr int NCB = 64 / TPC;
__device__ __forceinline__ int cb_tile(int ci, int t) { return 16 * (ci >> 2) + 8 * (t >> 1) + 2 * (ci & 3) + (t & 1); }
__device__ __forceinline__ void role_cb(const ChainArgs& a, int cbi, char* smem, const int w, Gate& G) {
  const int lane = opaque_tid(0);
  const int b = 64 + cbi; (void)b;
  const int par = cbi >> 4, ci = cbi & 15;
  u64* prof = a.prof; (void)prof;
  PDECL(65536, 8); PZERO;
  GAS unsigned* abortw = (GAS unsigned*)(a.sync + SY_ABORT);
  const unsigned s1v = ~(unsigned)(lane >> 6);
  const u32x4 sv = {s1v, s1v, s1v, s1v};
  bool dead = false;
  {
    const int c0 = cbi % 3, q0 = (c0 + 1) % 3;
    const int nc = (34 - c0) / 3, nq = (34 - q0) / 3;
    auto pf = [&](const u16* base, long ld) {
#pragma unroll
      for (int j = 0; j < 4; ++j)
        __builtin_amdgcn_global_load_lds((const GAS void*)(base + (long)((w * 4 + j) * 4 + (lane >> 4)) * ld + (lane & 15) * 8), (LAS void*)(smem + 16384 + (w * 4 + j) * 1024), 16, 0, 0);
      wait_vm<24>();
    };
    for (int n = cbi / 3; n < PFN * nq && cbi != 1; n += nc) pf(a.Wq + (long)(64 * (n / nq)) * HD + 128 * (q0 + 3 * (n % nq)), HD);
    wait_vm<0>();
  }
  if (!G.pass(a.sync)) return;
  if (cbi == 1 && w < 3) {
    unsigned spins = 0, v;
    while (true) {
      v = __hip_atomic_load(ctr(a, CT_CAL + 32 * w + (lane & 31)), __ATOMIC_RELAXED, __HIP_MEMORY_SCOPE_AGENT);
      if (w == 2 || __all(v != SENT) || poll_fail(spins, abortw, lane)) break;
      __builtin_amdgcn_s_sleep(4);
    }
    const u64 m = __builtin_amdgcn_ballot_w64(((v ^ die_half()) & 1u) != 0) >> 32;
    if (lane == 0) __hip_atomic_store((GAS u64*)(a.sync + SY_SEL) + w, m, __ATOMIC_RELAXED, __HIP_MEMORY_SCOPE_AGENT);
  }
  const Sel sel = sel_wait(a.sync, abortw, lane, dead);
  SSTAMP(64 + cbi);
  const __amdgpu_buffer_rsrc_t rh = rsrc_hot(a.sync, sel), rum = rsrc_u(a.sync);
  const int tw = cb_tile(ci, w);
  const unsigned mo = (tw >> 5) * CHK + (tw & 31) * 256;
  const unsigned ml = (4 * (lane >> 4) + (lane & 3)) * 16 + ((lane >> 2) & 3) * 4;
  {
#pragma unroll
    for (int i = 0; i < 4; ++i) st16(rum, um_slot(0, sel) + (4 * ci + w) * CHK + (4 * par + i) * 1024 + lane * 16, sv);
    if (cbi >= 2) {
#pragma unroll
      for (int i = 0; i < 2; ++i) st16(rh, cbi * CHK + w * 2048 + i * 1024 + lane * 16, sv);
    }
    if (par == 0) st4(rh, H_MEM + mo + ml, nosent(*(const unsigned*)(a.mem0 + (2 * (lane >> 4) + (lane & 1)) * DM + 16 * tw + (lane & 14))));
    asm volatile("s_waitcnt vmcnt(0)" ::: "memory");
    lds_barrier();
    if (w == 0 && lane == 0) ctr_add(a, CT_INIT, 1u);
  }
  Wts W;
#pragma unroll
  for (int i = 0; i < 32; ++i) asm volatile("" : "=a"(W.a[i]));
  auto uctr = [&](int s) { return ctr(a, CT_U + s); };
  const unsigned uxo = ux_tile(cb_tile(ci, 0)) + lane * 16;
  auto ux_load = [&](int s) {
    ctr_wait<8>(uctr(s), CT_U_NEED, abortw, lane, dead);
    const unsigned so = to_sgpr(ux_base(s, w * 8, sel));
    const u32x4 rum = own_rsrc(a.sync, SY_SET);
#pragma unroll
    for (int i = 0; i < 8; ++i)
#pragma unroll
      for (int t = 0; t < TPC; ++t)
        asm volatile("buffer_load_dwordx4 a[%0:%1], %2, %3, %4 offen offset:%5 sc1" :: "n"(32 * t + 4 * i), "n"(32 * t + 4 * i + 3), "v"(t < 2 ? uxo : uxo + ux_tile(8)), "s"(rum),
                     "s"(so + i * UXH), "n"((t & 1) * 1024) : "memory");
  };
  Clk ck; ck.init();
  SSTAMP(0 + cbi);
  ux_load(par);
  SSTAMP(32 + cbi);
  ctr_wait(ctr(a, CT_INIT), 32u, abortw, lane, dead);
  const unsigned dlo = H_DLT + par * 2048;
  auto hq = [&](int n) { return (ci >> 2) ^ n; };
  u32x16 C;
  {
    const unsigned l0 = (unsigned)(uintptr_t)(LAS char*)smem;
    C[12] = (ci & 3) == w ? dlo + (lane & 15) * 128 : 0x7fffffffu; C[13] = H_TR + (lane & 15) * 128; C[14] = uxo; C[15] = uxo + ux_tile(8);
    C[0] = lane * 16; C[1] = (lane >> 5) * 4096 + (lane & 31) * 16;
    C[2] = 2 * CHK + (8 * w + hq(2 * (lane >> 5) + ((lane >> 1) & 1)) + 4 * ((lane >> 4) & 1)) * 128 + (2 * ((lane & 15) >> 2) + (lane & 1)) * 16;
    C[3] = (lane & 2) ? 0xEEEE : 0x4444;
    C[4] = l0 + (w * 128 + lane) * 4;
    C[5] = l0 + (w * 512 + ((lane >> 2) & 1) * 64 + (lane >> 4) * 16 + 4 * (lane & 3) + 2 * ((lane >> 3) & 1)) * 4;
    C[6] = lane * 4; C[7] = 20000 + (lane >> 6);
    C[8] = C[9] = (8 * w + (lane & 7)) * 128;
    C[10] = ((ci & 3) == w && lane == 0) ? H_TR + ci * 128 : 0x7fffffffu; C[11] = l0 + 8192 + 4 * w + (lane >> 6);
    asm volatile("" : "+v"(C));
  }
  unsigned tr = ck.te, dprev = K_DEF * TU, dprev2 = K_DEF * TU;
  unsigned uxp = SENT;
  LAS unsigned* tql = (LAS unsigned*)(smem + 8192);
  for (int s = par; s + 1 < NSEG; s += 2) {
    u32x16 P; u32x4 res; u32x16 pv; (void)pv; u32x4 pw; (void)pw;
    const bool more = s + 3 < NSEG;
    P[1] = H_ANN + (s & 3) * CHK; P[2] = (dead ? 4u : 0u) | (s ? 0u : 8u);
    P[3] = um_slot(s, sel) + (4 * ci + w) * CHK + hq(0) * 1024; P[4] = H_P + ((s & 3) * 3 + (w >> 1)) * CHK + (w & 1) * 4096; P[5] = H_P + (s & 3) * 3 * CHK;
    P[6] = H_MEM + ((s + 1) & 7) * 2 * CHK + mo; P[7] = s; P[13] = tr_place(s); P[14] = tr_place(s - 1); P[15] = more ? ux_base(s + 2, w * 8, sel) : SENT;
    GAS unsigned* uc = uctr(min(s + 2, NSEG - 1));
    unsigned tries = 0, spins = 0;
    while (true) {
      ck.sync();
      P[0] = ck.target(tr + TR_W); P[8] = ck.te << 8; P[9] = ck.ce; P[10] = ck.t2c; P[11] = ck.c2t << 15; P[12] = ck.te + EPS_M;
#pragma unroll
      for (int i = 0; i < 16; ++i) P[i] = __builtin_amdgcn_readfirstlane(P[i]);
      asm volatile(CB_BODY : "=&{s[56:59]}"(res) STEP_PV CB_PV : "{v[96:111]}"(C), "{s[40:55]}"(P), [rh] "s"(rh), [ru] "s"(rum), [uc] "s"(uc), [ux] "s"(uxp)
                   : "memory", "vcc", "scc", "s76", "s77", "s78", "s79", "s80", "s81", "s82", "s83", "s84", "s85", "s86", "s87", "s92", CB_CL V100, "v92", "v93", "v94", "v95");
      keep_a(W);
#pragma unroll
      for (int i = 0; i < 4; ++i) res[i] = __builtin_amdgcn_readfirstlane(res[i]);
      tries += res[3];
      uxp = SENT;
      if (__builtin_expect((res[2] & 1u) == 0, 1)) break;
      spins |= 63u;
      if (dead || poll_fail(spins, abortw, lane)) dead = true;
      P[2] = res[2] | (dead ? 4u : 0u);
    }
    tr = ck.time(res[0]);
    PVAL(1, __builtin_amdgcn_s_memrealtime() - (ck.now() - tr) / TU);
    const unsigned dl = min(min((tr - tql[0]) << 8, (tr - tql[1]) << 8), min((tr - tql[2]) << 8, (tr - tql[3]) << 8)) >> 8;
    if (w == 0 && lane == 0) st4(rh, dlo + ci * 128, min(dl, min(dprev, dprev2)));
    dprev2 = dprev; dprev = dl;
    if (w == 1 && lane == 0) {
      if (s >= SEG_FINE - 4) __hip_atomic_store(ctr(a, CT_FL + (s - SEG_FINE + 4) * 16 + ci), 0u, __ATOMIC_RELAXED, __HIP_MEMORY_SCOPE_AGENT);
      if (ci < 8) __hip_atomic_fetch_max((GAS int*)ctr(a, CT_QUEUE + 16 + ci), s, __ATOMIC_RELAXED, __HIP_MEMORY_SCOPE_AGENT);
    }
    if (lane < 16) st16(rh, H_MEM + ((s - 1) & 7) * 2 * CHK + mo + lane * 16, sv);
    if (res[2] == 0) uxp = P[15];
    else if (more) ux_load(s + 2);
    uxp = __builtin_amdgcn_readfirstlane(uxp);
  }
  PDUMP(2);
}

__device__ __forceinline__ void st16g(u16* p, u32x4 v, bool sc1) {
  if (sc1) asm volatile("global_store_dwordx4 %0, %1, off " OUTP "\n\ts_nop 1" :: "v"(p), "v"(v) : "memory");
  else *(u32x4*)p = v;
}
struct XaIn { bf16x8 k[3][4]; bf16x8 q[2][4]; };
__device__ __forceinline__ void xa_load(XaIn& in, const ChainArgs& a, int s, int h, int lane) {
  const __amdgpu_buffer_rsrc_t rq = mk_rsrc((void*)a.Qx, 16u << 20), rk = mk_rsrc((void*)a.Kx, 16u << 20), rkm = mk_rsrc((void*)a.Km, 4u << 20);
  const unsigned blk = (s * NH + h) * 8192 + lane * 16;
#pragma unroll
  for (int i = 0; i < 8; ++i) in.k[i >> 2][i & 3] = __builtin_bit_cast(bf16x8, __builtin_amdgcn_raw_buffer_load_b128(rk, blk + i * 1024, 0, LD_AUX | XA_NT));
#pragma unroll
  for (int kp = 0; kp < 4; ++kp) in.k[2][kp] = __builtin_bit_cast(bf16x8, ld16(rkm, s * 65536 + (h * 4 + kp) * 512 + ((lane >> 4) * 8 + (lane & 7)) * 16));
#pragma unroll
  for (int i = 0; i < 8; ++i) in.q[i >> 2][i & 3] = __builtin_bit_cast(bf16x8, __builtin_amdgcn_raw_buffer_load_b128(rq, blk + i * 1024, 0, LD_AUX | XA_NT));
}
__device__ __forceinline__ f32x4 xa_mask(int qt, int T, int lane) {
  const int g = lane >> 4, d = 16 * qt + (lane & 15) - 8 * (g & 1) - 4 * (g >> 1) - T;
  f32x4 m;
#pragma unroll
  for (int i = 0; i < 4; ++i) m[i] = 16 * (i >> 1) + 2 * (i & 1) <= d ? 0.f : -__builtin_inff();
  return m;
}
template <int N>
__device__ __forceinline__ void xa_finish(f32x4 s0, f32x4 s1, f32x4 s2, __amdgpu_buffer_rsrc_t rp, unsigned pbase, int qt, int h, int lane) {
  const float CS = 0.08838834764831845f * 1.4426950408889634f;
  float mx = fmaxf(fmaxf(s2[0], s2[1]), fmaxf(s2[2], s2[3]));
#pragma unroll
  for (int i = 0; i < N; ++i) mx = fmaxf(fmaxf(mx, s0[i]), s1[i]);
  mx = red32max(red16max(mx)) * CS;
  float sum = 0.f;
#pragma unroll
  for (int i = 0; i < 4; ++i) { s2[i] = __builtin_amdgcn_exp2f(fmaf(s2[i], CS, -mx)); sum += s2[i]; }
  sum *= 0.5f;
#pragma unroll
  for (int i = 0; i < N; ++i) { s0[i] = __builtin_amdgcn_exp2f(fmaf(s0[i], CS, -mx)); s1[i] = __builtin_amdgcn_exp2f(fmaf(s1[i], CS, -mx)); sum += s0[i] + s1[i]; }
  const float inv = __builtin_amdgcn_rcpf(red32add(red16add(sum)));
  unsigned d[4];
#pragma unroll
  for (int i = 0; i < 4; ++i) d[i] = i < N ? nosent(pk2(s0[i] * inv, s1[i] * inv)) : 0u;
  auto x = __builtin_amdgcn_permlane32_swap(d[0], d[2], false, false);
  auto y = __builtin_amdgcn_permlane32_swap(d[1], d[3], false, false);
  st16(rp, pbase + (unsigned)((qt * 32 + h) * 1024 + lane * 16), (u32x4){x[0], y[0], x[1], y[1]});
  const unsigned m0 = nosent(pk2(s2[0] * inv, s2[1] * inv)), m1 = nosent(pk2(s2[2] * inv, s2[3] * inv));
  auto z0 = __builtin_amdgcn_permlane16_swap(m0, m0, false, false);
  auto z1 = __builtin_amdgcn_permlane16_swap(m1, m1, false, false);
  st16(rp, lane < 16 ? pbase + (unsigned)(65536 + ((qt * 32 + h) * 16 + lane) * 16) : 0x7fffffffu, (u32x4){z0[0], z1[0], z0[1], z1[1]});
}
__device__ __forceinline__ void xa_compute(const XaIn& in, __amdgpu_buffer_rsrc_t rp, unsigned pbase, int h, int lane) {
  f32x4 st[2][3];
#pragma unroll
  for (int qt = 0; qt < 2; ++qt) {
    st[qt][0] = xa_mask(qt, 0, lane); st[qt][1] = xa_mask(qt, 1, lane); st[qt][2] = (f32x4){0.f, 0.f, 0.f, 0.f};
#pragma unroll
    for (int kp = 0; kp < 4; ++kp)
#pragma unroll
      for (int T = 0; T < 3; ++T) st[qt][T] = __builtin_amdgcn_mfma_f32_16x16x32_bf16(in.k[T][kp], in.q[qt][kp], st[qt][T], 0, 0, 0);
  }
  xa_finish<2>(st[0][0], st[0][1], st[0][2], rp, pbase, 0, h, lane);
  xa_finish<4>(st[1][0], st[1][1], st[1][2], rp, pbase, 1, h, lane);
}

__device__ __forceinline__ u32x4 pk8(f32x4 a, f32x4 b) { return (u32x4){pk2(a[0], a[1]), pk2(a[2], a[3]), pk2(b[0], b[1]), pk2(b[2], b[3])}; }

__device__ __forceinline__ void bg_drain() {
  asm volatile("s_waitcnt vmcnt(0)" ::: "memory");
  __builtin_amdgcn_s_barrier();
  asm volatile("" ::: "memory");
}
#define BG_SLP 64
#define A2N 2
constexpr int A2_NCH = A2N;
enum { K_U, K_P128, K_P256, K_A1, K_A2, K_A3, K_SWITCH, K_X, K_PU, K_XL, K_PUL, K_P192, K_XM, K_PUM, K_P64 };
constexpr int LMAX = 112;
struct TaskTab { unsigned c[2][LMAX]; int n[2]; };
constexpr TaskTab make_tab() {
  TaskTab M{};
  int n = 0;
  auto put = [&](int kind, int arg) { M.c[0][n++] = (unsigned)(kind * 256 + arg); };
  for (int i = 0; i < 4; ++i) put(K_U, i);
  for (int i = 0; i < 12; ++i) put(K_P128, i);
  M.n[0] = n;
  auto p1 = [&](int kind, int arg) { M.c[1][M.n[1]++] = (unsigned)(kind * 256 + arg); };
  for (int j = SEG_R; j < NSEG + X_AH; ++j) {
    if (j < SEGL) p1(K_XM, j); else if (j < NSEG) { p1(K_XL, j); p1(K_XL, 64 + j); }
    const int k = j - X_AH;
    if (k >= SEGL) for (int q = 0; q < 4; ++q) p1(K_PUL, 64 * q + k);
    else if (k >= SEG_R) { p1(K_PUM, k); p1(K_PUM, 128 + k); }
  }
  return M;
}
__device__ const TaskTab task_tab = make_tab();
constexpr int QWG = 20;
__attribute__((always_inline)) constexpr int first_code(int r) { return r < 4 ? K_U * 256 + r : K_P128 * 256 + r - 4; }
static_assert(SEG_R >= SEG_FINE && SEG_R >= 48 && SEG_R <= 56, "ticket slots");
struct Pick {
  static constexpr unsigned NJ = 7, N0 = make_tab().n[0], N1 = make_tab().n[1], A4 = 4 * A2_NCH, N4 = 8 * A4 + 128, NA = N0;
  const ChainArgs& a; int x, wv, far;
  unsigned v, o, seen; int kind, j, other;
  __device__ __forceinline__ void look() {
    const int lane = opaque_tid(0), s = x + 8 * (lane & 7);
    const int ci = lane < 8 ? CT_QK + (s >> 2) : lane < 16 ? CT_QUEUE + 24 + ((x + lane) & 7) : lane < 23 ? CT_P + s : lane < 24 ? CT_A2 + x : lane < 40 ? CT_TK + 16 * x + lane - 24 : CT_QUEUE + 8 * lane - 320 + x + (lane == 43 ? 8 : 0);
    v = ld4(mk_rsrc(a.sync + SY_CT, SY_BYTES - SY_CT), wv || lane > 43 || (lane >= 8 + far && lane < 16) ? 0x7fffffffu : ci * 128);
  }
  __device__ __forceinline__ bool claim(int own = 47) {
    const int lane = opaque_tid(0);
    const unsigned w = v + 1u, ha = __builtin_amdgcn_readlane(w, 40), h0 = ha < NA ? ha : NA + __builtin_amdgcn_readlane(w, 43), h1 = __builtin_amdgcn_readlane(w, 41), done = __builtin_amdgcn_readlane(w, 42);
    const u64 r = __builtin_amdgcn_ballot_w64(lane < 8 ? w >= X_RDY && done >= x + 8 * lane + 2 : w >= (lane < 16 ? N4 : lane == 23 ? 128u : (lane >> 3) == 3 ? 2u : lane == 39 ? 8u : (lane >> 3) == 4 ? 4u : 1u)) | (1ull << (16 + own));
    const unsigned r0 = (unsigned)r, JM = x + 48 < SEG_R ? 127u : 63u, pd = (unsigned)(r >> 32) | (JM ^ 127u), mp = r0 >> 16 & (r0 >> 24 | 128u) & ~pd & 255u, mx = r0 & ~(r0 >> 24) & JM, m0 = ~(r0 >> 8) & ((1u << far) - 1u);
    if (m0 == 0 && h0 >= N0) far = far == 1 ? STEAL : 0;
    const unsigned RM = (1u << ((NSEG - URING) / 8)) - 1u;
    kind = mp ? 3 : mx ? 2 : m0 && ha >= NA ? 4 : h0 < N0 ? (ha < NA ? 0 : 5) : far == 0 && h1 < N1 && (pd & RM) == RM && done + LT_R > (task_tab.c[1][min(h1, N1 - 1)] & 63u) ? 1 : -1;
    j = __builtin_ctz((mp ? mp : mx ? mx : m0) | 128u);
    if (kind == 4) j = (x + j) & 7;
    const unsigned q = kind == 5 ? CT_QUEUE + 32 + x : kind == 4 ? CT_QUEUE + 24 + j : kind < 2 ? CT_QUEUE + 8 * kind + x : CT_TK + 16 * x + 8 * (kind - 2) + j;
    const __amdgpu_buffer_rsrc_t rc = mk_rsrc(a.sync + SY_CT, SY_BYTES - SY_CT);
    unsigned qo = q * 128;
    o = kind == 4 ? A4 : 1u;
    asm volatile("" : "+v"(qo), "+v"(o));
    if ((wv | lane) == 0 && kind >= 0) o = __builtin_amdgcn_raw_ptr_buffer_atomic_add_i32(o, rc, qo, 0, 0);
    seen = done;
    other = far || (pd & 255u) != 255u;
    if (!other && h1 >= N1) kind = -2;
    return kind != -2;
  }
  __device__ __forceinline__ unsigned next() {
    unsigned spins = 0;
    while (kind != -2) {
      if (kind >= 0) {
        const unsigned e = __builtin_amdgcn_readfirstlane(o) + 1u, n = kind == 0 ? NA : kind == 5 ? N0 - NA : kind == 1 ? N1 : kind == 4 ? N4 : 2 * kind - 2, lim = kind == 3 && j == 7 ? 8u : n;
        const int k = kind;
        kind = -1;
        if (e < n && k == 5) return (NA + e) | x << 20;
        if (e < lim) return seen << 24 | (k << 16) | (k == 4 ? e | j << 20 : (k < 2 ? e : n * j + e) | x << 20);
        if (!other) break;
      } else if (spins && !far) __builtin_amdgcn_s_sleep(Q_SLP);
      look();
      if (!claim() || poll_fail(spins, (GAS unsigned*)(a.sync + SY_ABORT), opaque_tid(0))) break;
    }
    return SENT;
  }
};

#define DSP 4
#define SIGQ 2
__device__ __forceinline__ void task_w(const ChainArgs& a, int kind, int h, char* smem, int wv, Gate& G) {
  const int lane = opaque_tid(0), w = wv, r = lane & 15, g = lane >> 4;
  const __amdgpu_buffer_rsrc_t rw = mk_rsrc(kind == 0 ? a.Wq : (kind == 1 ? a.Wk : a.Wv), 8u << 20), rx = mk_rsrc(a.X, 4u << 20);
  const __amdgpu_buffer_rsrc_t ro = mk_rsrc((void*)(kind == 0 ? a.Qx : (kind == 1 ? a.Kx : a.Vx)), 16u << 20);
  const unsigned lds0 = (unsigned)(uintptr_t)(LAS char*)smem + w * 4096;
  unsigned vx[2], vw[4];
#pragma unroll
  for (int j = 0; j < 2; ++j) {
    const int rr = 8 * j + (lane >> 3);
    const int tb = kind == 0 ? rr : (kind == 1 ? 16 * ((rr >> 1) & 1) + 8 * ((rr >> 2) & 1) + 4 * (rr >> 3) + 2 * (rr & 1) : 8 * (rr >> 2) + (rr & 3));
    vx[j] = tb * DM * 2 + ((lane & 7) ^ (rr >> 1)) * 16 + w * 128;
  }
#pragma unroll
  for (int j = 0; j < 4; ++j) { const int row = (w * 4 + j) * 4 + g; vw[j] = row * HD * 2 + (r ^ swz_b(row)) * 16; }
  unsigned dT = (kind == 0 ? 16 : (kind == 1 ? 1 : 4)) * DM * 2;
  asm volatile("s_nop 4" : "+s"(dT));
  auto st_w = [&](int n) {
#pragma unroll
    for (int j = 0; j < 4; ++j) dma16(rw, vw[j], (unsigned)((64 * n * HD + 128 * h) * 2), lds0 + (n & 7) * 16384 + j * 1024);
  };
  auto st_x = [&](int s, int b, int q, int j, bool pad) { dma16(rx, vx[j & 1], s * 65536 + (j >> 1) * dT + q * 512, lds0 + (b * 4 + q) * 16384 + j * 1024, false, pad); };
  __builtin_amdgcn_s_waitcnt(0x0F70);
  u32x4 W[32][2];
#pragma unroll
  for (int n = 0; n < 7; ++n) st_w(n);
#pragma unroll
  for (int n = 0; n < 16; ++n) {
    wait_vm<24>();
    lds_barrier();
    if (n + 7 < 16) st_w(n + 7);
    else {
#pragma unroll
      for (int j = 0; j < 4; ++j) st_x((n - 9) >> 2, (n - 9) >> 2, (n - 9) & 3, j, true);
    }
#pragma unroll
    for (int t = 0; t < 2; ++t)
#pragma unroll
      for (int c = 0; c < 2; ++c) {
        u32x4 f;
#pragma unroll
        for (int q = 0; q < 2; ++q) {
          const int row = c * 32 + 8 * g + 4 * q + (r >> 2);
          const u32x2 u = __builtin_bit_cast(u32x2, __builtin_amdgcn_ds_read_tr16_b64_v4i16((LAS bf16x4*)(smem + (n & 7) * 16384 + row * 256 + (((4 * w + (r & 3)) ^ swz_b(row)) * 16) + 8 * t)));
          f[2 * q] = u[0]; f[2 * q + 1] = u[1];
        }
        asm volatile("" : "+a"(f));
        W[2 * n + c][t] = f;
      }
  }
  if (!G.pass(a.sync)) { bg_drain(); return; }
  SSTAMP(160 + h);
  char* xb[2][2];
#pragma unroll
  for (int i = 0; i < 4; ++i) xb[i >> 1][i & 1] = lds_base(smem, (i >> 1) * 65536 + (r >> 3) * 1024 + (r & 7) * 128 + ((4 * (i & 1) + g) ^ (r >> 1)) * 16);
  u32x4 xf[4][2], o;
  f32x4 acc[2][4];
  auto rd = [&](int b, int k, int T) { xf[k & 3][T] = *(const u32x4*)(xb[b][k & 1] + (k >> 1) * 4096 + T * 2048); };
  auto signal = [&](int s) {
    if (w == 0 && lane == 0) {
      if (kind) ctr_add(a, (kind == 1 ? CT_K : CT_V) + h, 1u);
      if (kind < 2 && (s & 3) == 3) ctr_add(a, CT_QK + (s >> 2), 1u);
    }
  };
  wait_vm<24>();
  lds_barrier();
#pragma unroll
  for (int i = 0; i < 6; ++i) rd(0, i >> 1, i & 1);
  auto seg = [&](int s, int b) {
    const unsigned so = s ? ((s - 1) * NH + h) * 8192 + w * 1024 : 0x7f000000u;
#pragma unroll
    for (int k = 0; k < 32; ++k)
#pragma unroll
      for (int i = 0; i < 4; ++i) {
        if (k == 0) asm volatile("v_mfma_f32_16x16x32_bf16 %0, %1, %2, 0" : "=&v"(acc[b][i]) : "a"(W[k][i & 1]), "v"(xf[k & 3][i >> 1]));
        else asm volatile("v_mfma_f32_16x16x32_bf16 %0, %1, %2, %0" : "+v"(acc[b][i]) : "a"(W[k][i & 1]), "v"(xf[k & 3][i >> 1]));
        SB0;
        const int q = k >> 3, n = 4 * k + i;
        if ((k & 7) == 5 && i == 0) {
          if (q == SIGQ) wait_vm<4 * SIGQ>(); else wait_vm<20>();
          __builtin_amdgcn_s_barrier();
          asm volatile("" ::: "memory");
        }
        if (i == 1 || i == 2) rd(k + 3 < 32 ? b : b ^ 1, (k + 3) & 31, i - 1);
        {
          const int m = (n + 128 - 23) & 127, j = (m & 31) / DSP, q0 = m >> 5;
          if (j < 4 && (m & 31) % DSP == 0) asm volatile("s_mov_b32 m0, %0" :: "s"(lds0 + ((b ^ (q0 == 0) ^ (n < 23)) * 4 + ((q0 + 3) & 3)) * 16384 + j * 1024) : "m0");
          if (j < 4 && (m & 31) % DSP == 1)
            asm volatile("buffer_load_dwordx4 %0, %1, %2 offen lds" :: "v"(vx[j & 1]), "s"(rx), "s"((s + 2 - (q0 == 0) - (n < 23)) * 65536 + (j >> 1) * dT + ((q0 + 3) & 3) * 512) : "memory");
        }
        if (k == 8 * SIGQ + 5 && i == 3 && s) signal(s - 1);
        if (k >= 1 && k < 5 && (i == 0 || i == 3)) { const f32x4 x = acc[b ^ 1][k - 1]; o[2 * (~k & 1) + (i & 1)] = pk2(x[2 * (i & 1)], x[2 * (i & 1) + 1]); }
        if ((k == 2 || k == 4) && i == 3) { __builtin_amdgcn_raw_buffer_store_b128(o, ro, (unsigned)(lane * 16 + (k >> 2) * 4096), so, ST_AUX); asm volatile("s_nop 0" :: "v"(o)); }
        SB0;
      }
  };
#pragma unroll
  for (int i = 0; i < 4; ++i) acc[1][i] = vzero();
  for (int s = 0; s < NSEG; s += 2) { seg(s, 0); seg(s + 1, 1); }
  asm volatile("s_nop 7" : "+v"(acc[1][0]), "+v"(acc[1][1]), "+v"(acc[1][2]), "+v"(acc[1][3]));
#pragma unroll
  for (int T = 0; T < 2; ++T) st16s(pk8(acc[1][2 * T], acc[1][2 * T + 1]), ro, lane * 16 + T * 4096, ((NSEG - 1) * NH + h) * 8192 + w * 1024);
  bg_drain();
  signal(NSEG - 1);
}
__device__ __forceinline__ float blo(unsigned d) { return __uint_as_float(d << 16); }
__device__ __forceinline__ float bhi(unsigned d) { return __uint_as_float(d & 0xffff0000u); }
constexpr int A2_CSTR = 4160;
constexpr int A2_UOFF = 8 * A2_CSTR + 16 * 80;
constexpr int A2_WLDS = A2_UOFF + 64 * 80;
static_assert(4 * A2_WLDS + 256 <= CHAIN_LDS - 16, "lds");
template <class F>
__device__ __forceinline__ void a2_wave(const ChainArgs& a, int d0, int d1, char* smem, int w, F hook) {
  const int lane = opaque_tid(0);
  char* Wb = smem + w * A2_WLDS;
  char* U = Wb + A2_UOFF;
  const __amdgpu_buffer_rsrc_t rf = mk_rsrc((void*)a.Assm, 4u << 20);
  const __amdgpu_buffer_rsrc_t rkv = mk_rsrc((void*)a.kvT, 4u << 20);
  u32x4 hi[4], lo[4], uv[4];
  auto ld_h = [&](int d) {
#pragma unroll
    for (int i = 0; i < 4; ++i) {
      hi[i] = __builtin_amdgcn_raw_buffer_load_b128(rf, (unsigned)((255 - 64 * i - lane) * 16), (unsigned)(d * 4096), 2);
      lo[i] = __builtin_amdgcn_raw_buffer_load_b128(rf, (unsigned)(max(254 - 64 * i - lane, 0) * 16), (unsigned)(d * 4096), 2);
    }
  };
  auto ld_u = [&](int d) {
#pragma unroll
    for (int i = 0; i < 4; ++i) uv[i] = __builtin_amdgcn_raw_buffer_load_b128(rkv, (unsigned)((i * 64 + lane) * 16), (unsigned)(d * 4096), LD_AUX);
  };
  {
    GAS unsigned* abortw = (GAS unsigned*)(a.sync + SY_ABORT);
    bool dead = false;
    ctr_wait<BG_SLP>(ctr(a, CT_A1 + (min(d0, D_A - 1) >> 7)), d0 < d1 ? 8u : 0u, abortw, lane, dead);
  }
  ld_h(d0); ld_u(d0);
  if (lane < 24) *(f32x4*)(Wb + (lane / 3) * A2_CSTR + (256 + lane % 3) * 16) = vzero();
#pragma unroll
  for (int i = 0; i < 2; ++i) if (64 * i + lane < 80) *(f32x4*)(U - 16 * 80 + (64 * i + lane) * 16) = vzero();
  const int r = lane & 15, g = lane >> 4;
#pragma unroll 1
  for (int d = d0; d < d1; ++d) {
#pragma unroll
    for (int i = 0; i < 4; ++i) {
      u32x4 l = lo[i];
      if (i == 3 && lane == 63) l = (u32x4){0u, 0u, 0u, 0u};
      const unsigned src[8] = {l[0], l[1], l[2], l[3], hi[i][0], hi[i][1], hi[i][2], hi[i][3]};
      unsigned f[15];
#pragma unroll
      for (int x = 0; x < 15; ++x) {
        if ((x & 1) == 0) f[x] = __builtin_amdgcn_alignbit(src[7 - x / 2], src[7 - x / 2], 16);
        else f[x] = __builtin_amdgcn_perm(src[7 - (x + 1) / 2], src[7 - (x - 1) / 2], 0x07060100u);
      }
#pragma unroll
      for (int m = 0; m < 8; ++m) *(u32x4*)(Wb + m * A2_CSTR + (64 * i + lane) * 16) = (u32x4){f[7 - m], f[9 - m], f[11 - m], f[13 - m]};
    }
#pragma unroll
    for (int i = 0; i < 4; ++i) {
      const int ch = i * 64 + lane;
      *(u32x4*)(U + (ch >> 2) * 80 + (ch & 3) * 16) = uv[i];
    }
    const float Dd = bf2f(a.AD[d]);
    asm volatile("" ::: "memory");
    ld_h(min(d + 1, d1 - 1)); ld_u(min(d + 1, d1 - 1));
    hook(0, d + 1 == d1);
    f32x4 acc[4][2];
#pragma unroll
    for (int i = 0; i < 8; ++i) acc[i >> 1][i & 1] = vzero();
    const char* pa = Wb + (r & 7) * A2_CSTR + 2 * (2040 - (r & 8) + 8 * g) - 3 * 1024 - 32 - 64;
    const char* pb = U + (r - 1) * 80 + 16 * g;
    bf16x8 fa[2][8], fb[2][4];
    auto rd = [&](int b, int n, int k) {
      if (n < 4) fb[b][n] = *(const bf16x8*)(pb + 80 * (16 * n + 1 - k));
      else fa[b][n - 4] = *(const bf16x8*)(pa + 1024 * (3 - ((n - 4) >> 1)) + 32 * (1 - (n & 1)) + 64 * (1 - k));
    };
#pragma unroll
    for (int n = 0; n < 12; ++n) rd(0, n, 0);
#pragma unroll 1
    for (int k = 0; k < 16; k += 2) {
#pragma unroll
      for (int b = 0; b < 2; ++b) {
        int q = 0;
#pragma unroll
        for (int j = 0; j < 4; ++j)
#pragma unroll
          for (int i = j; i < 4; ++i)
#pragma unroll
            for (int mt = 0; mt < 2; ++mt) {
              asm volatile("v_mfma_f32_16x16x32_bf16 %0, %1, %2, %0" : "+v"(acc[i][mt]) : "v"(fa[b][2 * j + mt]), "v"(fb[b][i - j]));
              SB0;
              if (q < 12) rd(b ^ 1, q, 1);
              if (q == 12) { pa -= 64; pb -= 80; }
              ++q;
              SB0;
            }
      }
    }
    asm volatile("s_nop 7" ::: "memory");
#pragma unroll
    for (int i = 0; i < 8; ++i) asm volatile("" : "+v"(acc[i >> 1][i & 1]));
    hook(1, d + 1 == d1);
#pragma unroll
    for (int i = 0; i < 8; ++i) {
      char* pz = U + (16 * (i >> 1) + r) * 80 + 32 * (i & 1) + 8 * g;
      const u32x2 kvv = *(const u32x2*)pz;
      const f32x4 y = acc[i >> 1][i & 1];
      *(u32x2*)pz = (u32x2){pk2(fmaf(blo(kvv[0]), Dd, y[0]), fmaf(bhi(kvv[0]), Dd, y[1])), pk2(fmaf(blo(kvv[1]), Dd, y[2]), fmaf(bhi(kvv[1]), Dd, y[3]))};
    }
#pragma unroll
    for (int i = 0; i < 4; ++i) {
      const int ch = i * 64 + lane;
      st16s(*(const u32x4*)(U + (ch >> 2) * 80 + (ch & 3) * 16), rkv, (unsigned)(ch * 16), (unsigned)(d * 4096));
    }
  }
  wait_vm<0>();
  if (lane == 0 && d0 < d1) ctr_add(a, CT_A2 + (d0 >> 7), d1 - d0);
}
__device__ __forceinline__ void task_u(const ChainArgs& a, int h, int s0, int s1, char* smem, int wv, Gate& G) {
  const int lane = opaque_tid(0);
  const int w = wv;
  GAS unsigned* abortw = (GAS unsigned*)(a.sync + SY_ABORT);
  bool dead = false;
  char* sb = smem + w * 32768;
  const __amdgpu_buffer_rsrc_t rwo = mk_rsrc(a.Wo, 8u << 20);
  const unsigned lds0 = (unsigned)(uintptr_t)(LAS char*)sb;
  unsigned wo4[4];
#pragma unroll
  for (int i = 0; i < 4; ++i) { const int row = i * 4 + (lane >> 4); wo4[i] = row * DM * 2 + (((lane & 15) ^ swz_b(row)) * 16); }
  asm volatile("s_nop 4");
  auto stage = [&](int p) {
#pragma unroll
    for (int i = 0; i < 16; ++i)
      dma16(rwo, wo4[i & 3], (unsigned)(((128 * h + 64 * (p & 1) + 16 * (i >> 2)) * DM + 256 * w + 128 * (p >> 1)) * 2), lds0 + (p & 1) * 16384 + i * 1024, true);
  };
  __builtin_amdgcn_s_waitcnt(0x0F70);
  u32x4 W[16][4];
  stage(0); stage(1);
#pragma unroll
  for (int p = 0; p < 4; ++p) {
    if (p < 3) wait_vm<16>(); else wait_vm<0>();
#pragma unroll
    for (int t = 0; t < 8; ++t) {
      const u32x8 f = tr_frag(sb + (p & 1) * 16384, t, lane);
      u32x4 f0 = {f[0], f[1], f[2], f[3]}, f1 = {f[4], f[5], f[6], f[7]};
      asm volatile("" : "+a"(f0), "+a"(f1));
      W[8 * (p >> 1) + t][2 * (p & 1)] = f0; W[8 * (p >> 1) + t][2 * (p & 1) + 1] = f1;
    }
    asm volatile("s_waitcnt lgkmcnt(0)" ::: "memory");
    if (p + 2 < 4) stage(p + 2);
  }
  lds_barrier();
  const __amdgpu_buffer_rsrc_t rv = mk_rsrc((void*)a.Vx, 16u << 20);
  const __amdgpu_buffer_rsrc_t ru = rsrc_u(a.sync);
  u32x4 V[2][8], pc[2];
  char* vl = lds_base(smem, lane * 16);
  volatile LAS unsigned* fl = (volatile LAS unsigned*)(smem + 16384);
  auto ld_v = [&](int s) {
#pragma unroll
    for (int i = 0; i < 2; ++i) pc[i] = __builtin_amdgcn_raw_buffer_load_b128(rv, (unsigned)(lane * 16 + i * 1024), (unsigned)((s * NH + h) * 8192 + w * 2048), LD_AUX);
  };
  auto put_v = [&](int b) {
#pragma unroll
    for (int i = 0; i < 2; ++i) *(u32x4*)(vl + b * 8192 + w * 2048 + i * 1024) = pc[i];
  };
  auto signal = [&](int s) {
    if (w == 0 && lane == 0) {
      ctr_add(a, CT_U + s, 1u);
    }
  };
  if (!G.pass(a.sync)) return;
  const unsigned s1v = ~(unsigned)(lane >> 6);
  const u32x4 sv = {s1v, s1v, s1v, s1v};
  SSTAMP(192 + h);
  const Sel sel = sel_wait(a.sync, abortw, lane, dead);
  const unsigned fo = (2 * h + (w >> 1)) * CHK + (w & 1) * 4096 + lane * 16;
  auto fill = [&](int s) {
#pragma unroll
    for (int i = 0; i < 4; ++i) __builtin_amdgcn_raw_buffer_store_b128(sv, ru, fo + i * 1024, s < NSEG ? um_slot(s, sel) : 0x7f000000u, ST_AUX);
  };
  int filled = 1;
  const __amdgpu_buffer_rsrc_t rc = mk_rsrc(a.sync + SY_CT, SY_BYTES - SY_CT);
  auto need = [&](int s, int k) { return k && s >= URING ? CT_PD + s - URING : CT_V + h; };
  auto look = [&](int s) { return ld4(rc, w == 0 && lane < 2 ? need(min(s, s1 - 1), lane) * 128 : 0x7fffffffu); };
  auto good = [&](unsigned cv, int s) { return __all(cv + 1u >= (lane == 0 ? min(s + 1, s1) : lane == 1 && s >= URING ? 4u : 1u)); };
  bool ok = false, pend = false, stop = true;
  auto seg = [&](int s, int b, u32x4 (&v)[8], u32x4 (&vn)[8]) {
    ld_v(min(s + 1, s1 - 1));
    const unsigned cv = look(s + 2);
    const unsigned ub = ux_base(s, h, sel) + ux_tile(16) * w;
    f32x4 c[2][2];
    u32x4 o;
    unsigned okn;
    if (filled <= s + 1) fill(filled++);
    SB0;
#pragma unroll
    for (int n = 0; n < 16; ++n) {
#pragma unroll
      for (int kp = 0; kp < 4; ++kp)
#pragma unroll
        for (int m = 0; m < 2; ++m) {
          if (kp == 0) asm volatile("v_mfma_f32_16x16x32_bf16 %0, %1, %2, 0" : "=&v"(c[n & 1][m]) : "v"(v[4 * m + kp]), "a"(W[n][kp]));
          else if (n == 15 && kp == 3 && m == 1) asm volatile("v_mfma_f32_16x16x32_bf16 %0, %1, %2, %0\n\ts_nop 7\n\ts_nop 3" : "+v"(c[n & 1][m]) : "v"(v[4 * m + kp]), "a"(W[n][kp]));
          else asm volatile("v_mfma_f32_16x16x32_bf16 %0, %1, %2, %0" : "+v"(c[n & 1][m]) : "v"(v[4 * m + kp]), "a"(W[n][kp]));
          SB0;
          const int q = 2 * kp + m - 5;
          if (n > 0 && q >= 0 && q < 2) { const f32x4 x = c[(n - 1) & 1][q]; o[2 * q] = pk2(x[0], x[1]); o[2 * q + 1] = pk2(x[2], x[3]); }
          if (n > 0 && q == 2) __builtin_amdgcn_raw_buffer_store_b128(o, ru, (unsigned)(lane * 16 + ((n - 1) & 3) * 1024), ub + ux_tile((n - 1) & 12), ST_AUX);
          if (n == U_SIG2 && q == 2) {
            wait_vm<U_SIG2>();
            put_v(b ^ 1);
            if (w == 0) fl[b] = good(cv, s + 2);
          }
          if (n == U_SIG2 + 1 && q == -3) { lds_barrier(); okn = fl[b]; if (pend) signal(s - 1); asm volatile("s_nop 1"); }
          if ((n == U_SIG2 + 2 || n == U_SIG2 + 3) && q < -1) vn[4 * (n - U_SIG2 - 2) + q + 5] = *(const u32x4*)(vl + (b ^ 1) * 8192 + (4 * (n - U_SIG2 - 2) + q + 5) * 1024);
          SB0;
        }
    }
    asm volatile("s_nop 1" :: "v"(o) : "memory");
    st16s(pk8(c[1][0], c[1][1]), ru, (unsigned)(lane * 16 + 3 * 1024), ub + ux_tile(12));
    pend = s + 1 < s1 && ok && s >= U_NOW;
    if (!pend) { bg_drain(); signal(s); }
    stop = !ok;
    ok = __builtin_amdgcn_readfirstlane(okn) != 0;
  };
  bool late = true;
  for (int s = s0; s < s1;) {
    if (stop) {
      while (filled <= s + (s ? FILLN : FILL0)) fill(filled++);
      if (w == 0) {
        ctr_wait<8>(ctr(a, need(s, 0)), s + 1, abortw, lane, dead);
        ctr_wait<16>(ctr(a, need(s, 1)), s >= URING ? 4u : 1u, abortw, lane, dead);
      }
      lds_barrier();
      ld_v(s);
      const unsigned cv = look(s + 1);
      put_v(0);
      if (w == 0) fl[2] = good(cv, s + 1);
      lds_barrier();
#pragma unroll
      for (int i = 0; i < 8; ++i) V[0][i] = *(const u32x4*)(vl + i * 1024);
      ok = __builtin_amdgcn_readfirstlane(fl[2]) != 0;
    }
    if (s >= 20 && late)
  {
      const __amdgpu_buffer_rsrc_t rk = mk_rsrc((char*)a.Km + SEG_FINE * 65536, NLATE * 65536), rp = mk_rsrc((char*)a.P + SEG_FINE * P_SEG, NLATE * P_SEG);
#pragma unroll
      for (int i = 0; i < NLATE / 2; ++i) st16(rk, ((h * 4 + w) * (NLATE / 2) + i) * 1024 + lane * 16, sv);
#pragma unroll
      for (int i = 0; i < NLATE * 5 / 8; ++i) st16(rp, ((h * 4 + w) * (NLATE * 5 / 8) + i) * 1024 + lane * 16, sv);
    }
    late = s < 20;
    seg(s, 0, V[0], V[1]); ++s;
    if (!stop && s < s1) { seg(s, 1, V[1], V[0]); ++s; }
  }
}
template <int HPW>
__device__ __forceinline__ void task_x(const ChainArgs& a, int s, int h0, int wv, Pick& pk) {
  const int lane = opaque_tid(0);
  const __amdgpu_buffer_rsrc_t rp = mk_rsrc((void*)a.P, NSEG * P_SEG);
  const int h = h0 + HPW * wv;
  XaIn in[2];
  bool dead = false;
  ctr_wait<16>(ctr(a, CT_QK + (s >> 2)), 64u, (GAS unsigned*)(a.sync + SY_ABORT), lane, dead);
  xa_load(in[0], a, s, h, lane);
#pragma unroll
  for (int i = 0; i < HPW; ++i) {
    if (i + 1 < HPW) xa_load(in[(i + 1) & 1], a, s, h + i + 1, lane);
    if (i + 2 == HPW) pk.look();
    if (i + 1 == HPW) pk.claim(s >> 3);
    SB0;
    xa_compute(in[i & 1], rp, (unsigned)(s * P_SEG), h + i, lane);
    SB0;
  }
  bg_drain();
  if (opaque_tid(wv) == 0) ctr_add(a, CT_P + s, 4 * HPW);
}
template <int SLP>
__device__ __forceinline__ void fl_wait(const ChainArgs& a, int s, int ci, int wv, int lane, bool& dead) {
  if (wv == 0) ctr_wait<SLP>(ctr(a, CT_FL + (s - SEG_FINE + 4) * 16 + ci), 1u, (GAS unsigned*)(a.sync + SY_ABORT), lane, dead);
  asm volatile("" ::: "memory");
  __builtin_amdgcn_s_barrier();
  asm volatile("" ::: "memory");
}
template <int NQ>
__device__ __forceinline__ void task_x_late(const ChainArgs& a, int s, int h0, int done, int wv, Pick& pk) {
  const int lane = opaque_tid(0);
  const int r = lane & 15, g = lane >> 4;
  const int h = h0 + (NQ == 1 ? wv >> 1 : wv), qt0 = NQ == 1 ? wv & 1 : 0;
  GAS unsigned* abortw = (GAS unsigned*)(a.sync + SY_ABORT);
  bool dead = false;
  ctr_wait<BG_SLP>(ctr(a, CT_QK + (s >> 2)), 64u, abortw, lane, dead);
  const __amdgpu_buffer_rsrc_t rq = mk_rsrc((void*)a.Qx, 16u << 20), rk = mk_rsrc((void*)a.Kx, 16u << 20);
  const __amdgpu_buffer_rsrc_t rp = mk_rsrc((void*)a.P, NSEG * P_SEG), rkm = mk_rsrc((void*)a.Km, 4u << 20);
  bf16x8 q[NQ][4];
  f32x4 st[NQ][2];
  u32x4 v[4];
  const unsigned kmo = (unsigned)(s * 65536 + h * 2048 + (g * 8 + (r & 7)) * 16);
  {
    bf16x8 k[8];
#pragma unroll
    for (int i = 0; i < 4 * NQ; ++i) q[i >> 2][i & 3] = __builtin_bit_cast(bf16x8, ld16(rq, (unsigned)((s * NH + h) * 8192 + (qt0 * 4 + i) * 1024 + lane * 16)));
#pragma unroll
    for (int i = 0; i < 8; ++i) k[i] = __builtin_bit_cast(bf16x8, ld16(rk, (unsigned)((s * NH + h) * 8192 + i * 1024 + lane * 16)));
#pragma unroll
    for (int kp = 0; kp < 4; ++kp) v[kp] = ld16(rkm, done > s ? kmo + kp * 512 : 0x7fffffffu);
    pk.look();
    if (done <= s) v[0][0] = SENT;
#pragma unroll
    for (int n = 0; n < NQ; ++n) {
      st[n][0] = xa_mask(qt0 + n, 0, lane); st[n][1] = xa_mask(qt0 + n, 1, lane);
#pragma unroll
      for (int i = 0; i < 8; ++i) st[n][i & 1] = __builtin_amdgcn_mfma_f32_16x16x32_bf16(k[4 * (i & 1) + (i >> 1)], q[n][i >> 1], st[n][i & 1], 0, 0, 0);
      asm volatile("" : "+v"(st[n][0]), "+v"(st[n][1]));
    }
  }
  if (done <= s - XL_D) { fl_wait<32>(a, s - XL_D, h0 >> 1, wv, lane, dead); pk.look(); }
  unsigned spins = 0;
  while (!__all(max(max(umax4(v[0]), umax4(v[1])), max(umax4(v[2]), umax4(v[3]))) != SENT)) {
    asm volatile("" ::: "memory");
#pragma unroll
    for (int kp = 0; kp < 4; ++kp) v[kp] = ld16(rkm, kmo + kp * 512);
    if (dead || poll_fail(spins, abortw, lane)) break;
  }
  pk.claim();
#pragma unroll
  for (int n = 0; n < NQ; ++n) {
    f32x4 st2 = vzero();
#pragma unroll
    for (int kp = 0; kp < 4; ++kp) st2 = __builtin_amdgcn_mfma_f32_16x16x32_bf16(__builtin_bit_cast(bf16x8, v[kp]), q[n][kp], st2, 0, 0, 0);
    if (NQ == 2 && n == 0) xa_finish<2>(st[n][0], st[n][1], st2, rp, (unsigned)(s * P_SEG), 0, h, lane);
    else xa_finish<4>(st[n][0], st[n][1], st2, rp, (unsigned)(s * P_SEG), qt0 + n, h, lane);
  }
}
template <int NQ>
__device__ __forceinline__ void task_pu_late(const ChainArgs& a, int s, int ci, int qt0, int done, Sel sel, char* smem, int wv, Pick& pk) {
  const int lane = opaque_tid(0);
  const int w = wv;
  GAS unsigned* abortw = (GAS unsigned*)(a.sync + SY_ABORT);
  bool dead = false;
  if (done <= s) ctr_wait<BG_SLP>(ctr(a, CT_U + s), CT_U_NEED, abortw, lane, dead);
  const __amdgpu_buffer_rsrc_t ru = rsrc_u(a.sync), rp = mk_rsrc((void*)a.P, NSEG * P_SEG);
  u32x4 u[4][8], m[4][2];
#pragma unroll
  for (int i = 0; i < 8; ++i)
#pragma unroll
    for (int t = 0; t < 4; ++t) u[t][i] = __builtin_amdgcn_raw_buffer_load_b128(ru, ux_base(s, w * 8 + i, sel) + ux_tile(cb_tile(ci, t)) + lane * 16, 0, LD_AUX);
  if (done <= min(s, NSEG - 2)) fl_wait<8>(a, min(s, NSEG - 2), ci, wv, lane, dead);
  unsigned spins = 0;
  const unsigned ub = um_slot(s, sel) + (4 * ci + w) * CHK + (lane >> 4) * 1024 + (lane & 15) * 16;
  u32x4 v[8], pf[NQ][10];
  bool vok = false;
  pk.look();
  while (true) {
    asm volatile("" ::: "memory");
    unsigned mx = 0;
    if (!vok) {
#pragma unroll
      for (int i = 0; i < 8; ++i) v[i] = ld16(ru, ub + (i >> 2) * 4096 + (i & 3) * 256);
    }
#pragma unroll
    for (int n = 0; n < NQ; ++n)
#pragma unroll
      for (int i = 0; i < 10; ++i)
        pf[n][i] = ld16(rp, (unsigned)(s * P_SEG + (i < 8 ? ((qt0 + n) * 32 + 8 * w + i) * 1024 : 65536 + ((qt0 + n) * 8 + 2 * w + i - 8) * 1024) + lane * 16));
#pragma unroll
    for (int i = 0; i < 8; ++i) mx = max(mx, umax4(v[i]));
    vok = __all(mx != SENT);
#pragma unroll
    for (int i = 0; i < 10 * NQ; ++i) mx = max(mx, umax4(pf[i / 10][i % 10]));
    if (__all(mx != SENT)) break;
    if (dead || poll_fail(spins, abortw, lane)) { dead = true; break; }
  }
  pk.claim();
#pragma unroll
  for (int t = 0; t < 4; ++t)
#pragma unroll
    for (int c = 0; c < 2; ++c) {
      const u32x4 x = v[4 * c + 2 * (t & 1)], y = v[4 * c + 2 * (t & 1) + 1];
      m[t][c] = (u32x4){x[t >> 1], x[(t >> 1) + 2], y[t >> 1], y[(t >> 1) + 2]};
    }
#pragma unroll
  for (int n = 0; n < NQ; ++n) {
    f32x4 acc[4];
#pragma unroll
    for (int t = 0; t < 4; ++t) acc[t] = vzero();
#pragma unroll
    for (int i = 0; i < 10; ++i)
#pragma unroll
      for (int t = 0; t < 4; ++t)
        acc[t] = __builtin_amdgcn_mfma_f32_16x16x32_bf16(__builtin_bit_cast(bf16x8, i < 8 ? u[t][i & 7] : m[t][i & 1]), __builtin_bit_cast(bf16x8, pf[n][i]), acc[t], 0, 0, 0);
#pragma unroll
    for (int t = 0; t < 4; ++t) *(f32x4*)(smem + n * 16384 + ((w * 4 + t) * 64 + lane) * 16) = acc[t];
  }
  lds_barrier();
  if (w < 2 * NQ) {
    const int tok = lane >> 2, t = 2 * (w & 1) + ((lane >> 1) & 1), g0 = 2 * (lane & 1);
    const char* sp = smem + (w >> 1) * 16384;
    f32x4 x = vzero(), y = vzero();
#pragma unroll
    for (int q = 0; q < 4; ++q) {
      x += *(const f32x4*)(sp + ((q * 4 + t) * 64 + g0 * 16 + tok) * 16);
      y += *(const f32x4*)(sp + ((q * 4 + t) * 64 + (g0 + 1) * 16 + tok) * 16);
    }
    st16g(a.out + (long)(SEG * s + 16 * (qt0 + (w >> 1)) + tok) * OUTW + DM + 16 * cb_tile(ci, 2 * (w & 1)) + 8 * (lane & 3), pk8(x, y), true);
  }
}
#define PUORD(n) ((n) % 5 == 4 ? 32 + (n) / 5 : (n) - (n) / 5)
__device__ __forceinline__ void task_pu(const ChainArgs& a, int s, int j, Sel sel, char* smem, int wv, Pick& pk) {
  const int lane = opaque_tid(0);
  const int w = wv;
  const int r = lane & 15, g = lane >> 4;
  const __amdgpu_buffer_rsrc_t ru = rsrc_u(a.sync), rp = mk_rsrc((void*)a.P, NSEG * P_SEG);
  const unsigned ux = ux_tile(16) * j + 2048 * w + lane * 16, ub0 = ux_base(s, 0, sel), ub1 = ux_base(s, 16, sel);
  const unsigned um = um_slot(s, sel) + (16 * j + 4 * w) * CHK + g * 1024 + r * 16;
  f32x4 acc[4][2];
#pragma unroll
  for (int t = 0; t < 4; ++t) { acc[t][0] = (f32x4){0.f, 0.f, 0.f, 0.f}; acc[t][1] = acc[t][0]; }
  u32x4 ub[2][4][4];
  auto ld_u = [&](int bi, u32x4 (&u)[4][4]) {
#pragma unroll
    for (int k = 0; k < 4; ++k) {
      const int ch = PUORD(bi * 4 + k);
#pragma unroll
      for (int t = 0; t < 4; ++t)
        u[k][t] = ch < 32 ? __builtin_amdgcn_raw_buffer_load_b128(ru, ux + (t & 1) * 1024, (ch < 16 ? ub0 : ub1) + (ch & 15) * UXH + (t >> 1) * CHK, LD_AUX | PX_NT)
                          : __builtin_amdgcn_raw_buffer_load_b128(ru, um + t * 256, ((ch - 32) >> 1) * CHK + (ch & 1) * 4096, LD_AUX | PU_NT);
    }
  };
  ld_u(0, ub[0]);
  {
    bool dead = false;
    ctr_wait<16>(ctr(a, CT_P + s), 32u, (GAS unsigned*)(a.sync + SY_ABORT), lane, dead);
    u32x4 pp[20];
#pragma unroll
    for (int i = 0; i < 20; ++i) pp[i] = ld16(rp, s * P_SEG + (w * 20 + i) * 1024 + lane * 16);
    SB0;
    ld_u(1, ub[1]);
    SB0;
#pragma unroll
    for (int i = 0; i < 20; ++i) *(u32x4*)(smem + (w * 20 + i) * 1024 + lane * 16) = pp[i];
  }
  lds_barrier();
#pragma unroll
  for (int bi = 0; bi < 10; ++bi) {
    if (bi > 0 && bi + 1 < 10) ld_u(bi + 1, ub[(bi + 1) & 1]);
    if (bi == 8) pk.look();
    if (bi == 9) pk.claim();
    SB0;
#pragma unroll
    for (int k = 0; k < 4; ++k) {
      const int ch = PUORD(bi * 4 + k);
      u32x4 f[4];
#pragma unroll
      for (int t = 0; t < 4; ++t) {
        const u32x4 x = ub[bi & 1][k][2 * (t & 1)], y = ub[bi & 1][k][2 * (t & 1) + 1];
        f[t] = ch < 32 ? ub[bi & 1][k][t] : (u32x4){x[t >> 1], x[(t >> 1) + 2], y[t >> 1], y[(t >> 1) + 2]};
      }
#pragma unroll
      for (int qt = 0; qt < 2; ++qt) {
        const bf16x8 pf = *(const bf16x8*)(smem + (ch < 32 ? (qt * 32 + ch) * 1024 : 65536 + (qt * 8 + ch - 32) * 1024) + lane * 16);
#pragma unroll
        for (int t = 0; t < 4; ++t) acc[t][qt] = __builtin_amdgcn_mfma_f32_16x16x32_bf16(__builtin_bit_cast(bf16x8, f[t]), pf, acc[t][qt], 0, 0, 0);
      }
    }
    SB0;
  }
  char* T = smem + 81920;
#pragma unroll
  for (int t = 0; t < 4; ++t)
#pragma unroll
    for (int qt = 0; qt < 2; ++qt)
      *(u32x2*)(T + (16 * qt + r) * 528 + (8 * (t >> 1) + 2 * w + (t & 1)) * 32 + g * 8) = (u32x2){pk2(acc[t][qt][0], acc[t][qt][1]), pk2(acc[t][qt][2], acc[t][qt][3])};
  lds_barrier();
  if (s < NSEG - URING && opaque_tid(wv) == 0) ctr_add(a, CT_PD + s, 1u);
#pragma unroll
  for (int i = 0; i < 4; ++i) {
    const int tok = 8 * w + 2 * i + (lane >> 5);
    st16g(a.out + (long)(SEG * s + tok) * OUTW + DM + 256 * j + 8 * (lane & 31), *(const u32x4*)(T + tok * 528 + (lane & 31) * 16), true);
  }
}

__device__ __forceinline__ void task_a1(const ChainArgs& a, int dp, int tq, char* smem, int wv, Pick& pk) {
  const int lane = opaque_tid(0);
  const int dg = 2 * dp + (wv & 1), t0 = tq * 256 + (wv >> 1) * 128;
  const int d = dg * 64 + 8 * (lane & 7);
  const int tb = t0 + 8 * (lane >> 3);
  char* T = smem + wv * 16384;
  u32x4 kk[2][11], vv[2][8], sh[4];
#pragma unroll
  for (int i = 0; i < 4; ++i) sh[i] = *(const u32x4*)(a.Ashift + d * 4 + 8 * i);
  SB0;
#pragma unroll
  for (int gq = 0; gq < 2; ++gq) {
#pragma unroll
    for (int i = 0; i < 11; ++i) {
      const int t = tb + 64 * gq - 3 + i;
      kk[gq][i] = LDNT((const u32x4*)(a.Ak + (long)(t < 0 ? 0 : t) * D_A + d));
    }
#pragma unroll
    for (int i = 0; i < 8; ++i) vv[gq][i] = LDNT((const u32x4*)(a.Av + (long)(tb + 64 * gq + i) * D_A + d));
    SB0;
  }
  pk.look();
#pragma unroll
  for (int i = 0; i < 3; ++i) if (tb == 0) kk[0][i] = (u32x4){0u, 0u, 0u, 0u};
#pragma unroll
  for (int gq = 0; gq < 2; ++gq) {
#pragma unroll
    for (int c = 0; c < 8; ++c) {
      const unsigned t01 = sh[c >> 1][2 * (c & 1)], t23 = sh[c >> 1][2 * (c & 1) + 1];
      const float s0 = blo(t01), s1 = bhi(t01), s2 = blo(t23), s3 = bhi(t23);
      float kf[11];
#pragma unroll
      for (int i = 0; i < 11; ++i) kf[i] = (c & 1) ? bhi(kk[gq][i][c >> 1]) : blo(kk[gq][i][c >> 1]);
      u32x4 o;
#pragma unroll
      for (int i = 0; i < 8; i += 2) {
        float r[2];
#pragma unroll
        for (int j = 0; j < 2; ++j)
          r[j] = fmaf(kf[i + j + 3], s0, fmaf(kf[i + j + 2], s1, fmaf(kf[i + j + 1], s2, kf[i + j] * s3))) * ((c & 1) ? bhi(vv[gq][i + j][c >> 1]) : blo(vv[gq][i + j][c >> 1]));
        o[i >> 1] = pk2(r[0], r[1]);
      }
      *(u32x4*)(T + (8 * (lane & 7) + c) * 256 + (((gq * 8 + (lane >> 3)) ^ (lane & 7)) * 16)) = o;
    }
    SB0;
  }
  pk.claim();
  const __amdgpu_buffer_rsrc_t rkv = mk_rsrc((void*)a.kvT, 4u << 20);
#pragma unroll
  for (int i = 0; i < 16; ++i) {
    const int row = 4 * i + (lane >> 4);
    const u32x4 o = *(const u32x4*)(T + row * 256 + (((lane & 15) ^ (row >> 3)) * 16));
    st16(rkv, (unsigned)(((dg * 64 + row) * S_LEN + t0 + (lane & 15) * 8) * 2), o);
  }
  bg_drain();
  if (opaque_tid(wv) == 0) ctr_add(a, CT_A1 + dp, 1u);
}

__device__ __forceinline__ void task_a3(const ChainArgs& a, int dp, int tq, char* smem, int wv, Pick& pk) {
  const int lane = opaque_tid(0);
  const int dg = 2 * dp + (wv & 1), t0 = tq * 256 + (wv >> 1) * 128;
  const int d = dg * 64 + 8 * (lane & 7);
  const int tb = t0 + 8 * (lane >> 3);
  const __amdgpu_buffer_rsrc_t rz = mk_rsrc((void*)a.kvT, 4u << 20);
  u32x4 qq[16], zz[8][2];
#pragma unroll
  for (int i = 0; i < 16; ++i) qq[i] = LDNT((const u32x4*)(a.Aq + (long)(tb + 64 * (i >> 3) + (i & 7)) * D_A + d));
  char* T = smem + wv * 16384;
  {
    u32x4 zr[16];
    pk.look();
#pragma unroll
    for (int i = 0; i < 16; ++i) zr[i] = ld16(rz, (unsigned)(((dg * 64 + 4 * i + (lane >> 4)) * S_LEN + t0 + (lane & 15) * 8) * 2));
#pragma unroll
    for (int i = 0; i < 16; ++i) {
      const int row = 4 * i + (lane >> 4);
      *(u32x4*)(T + row * 256 + (((lane & 15) ^ (row >> 3)) * 16)) = zr[i];
    }
  }
  pk.claim();
#pragma unroll
  for (int c = 0; c < 8; ++c)
#pragma unroll
    for (int gq = 0; gq < 2; ++gq) zz[c][gq] = *(const u32x4*)(T + (8 * (lane & 7) + c) * 256 + (((gq * 8 + (lane >> 3)) ^ (lane & 7)) * 16));
#pragma unroll
  for (int i = 0; i < 16; ++i) {
    u32x4 o;
#pragma unroll
    for (int c = 0; c < 8; c += 2) {
      const unsigned z0 = zz[c][i >> 3][(i >> 1) & 3], z1 = zz[c + 1][i >> 3][(i >> 1) & 3];
      o[c >> 1] = pk2(blo(qq[i][c >> 1]) * ((i & 1) ? bhi(z0) : blo(z0)), bhi(qq[i][c >> 1]) * ((i & 1) ? bhi(z1) : blo(z1)));
    }
    st16g(a.out + (long)(tb + 64 * (i >> 3) + (i & 7)) * OUTW + d, o, true);
  }
}

__device__ __forceinline__ void role_bg(const ChainArgs& a, char* smem, const int x_, int wv, int rank, Gate& G) {
  const unsigned rot0 = G.rot;
  constexpr TaskTab M = make_tab();
  constexpr unsigned NJ = Pick::NJ; (void)NJ;
  bool known = false;
  Sel sel = {0u, 0u, 0u};
  Pick pk{a, x_, wv, 1, 0u, 0u, 0u, -1, 0, 1};
  while (true) {
    asm volatile("" : "+s"(wv));
    pk.wv = wv;
    int t = rank, list = 0, x = x_, done = 0;
    if (rank < 0) {
      int toff = CHAIN_LDS - 16;
      asm volatile("" : "+v"(toff));
      volatile LAS int* tslot = (volatile LAS int*)(smem + toff);
      if (wv == 0) *tslot = (int)pk.next();
      __syncthreads();
      t = __builtin_amdgcn_readfirstlane(*tslot);
      __syncthreads();
      if (t < 0) break;
      done = t >> 24; x = (t >> 20) & 7; list = (t >> 16) & 7; t &= 0xffff;
    }
    if ((list & 3) && !known) { bool dead = false; sel = sel_wait(a.sync, (GAS unsigned*)(a.sync + SY_ABORT), opaque_tid(0), dead); known = true; }
    const int code = rank >= 0 ? first_code(rank) : (list < 2 ? task_tab.c[list][t] : (list == 4 ? (t < 8 * (int)Pick::A4 ? K_A1 * 256 + t / Pick::A4 : K_A2 * 256 + t - 8 * Pick::A4) : list == 2 ? K_X * 256 + t : (t < 28 ? K_PU * 256 + t : K_A3 * 256 + t - 28)));
    rank = -1;
    const int kind = __builtin_amdgcn_readfirstlane(code >> 8), arg = __builtin_amdgcn_readfirstlane(code & 255);
    if (kind == K_U) { task_u(a, 4 * x + arg, 0, NSEG, smem, wv, G); pk.o = 0; }
    else if (kind == K_P128) { task_w(a, 2 - (arg >> 2), 4 * x + (arg & 3), smem, wv, G); pk.o = 0; }
    else if (kind == K_A1) task_a1(a, x, arg, smem, wv, pk);
    else if (kind == K_A2) {
      pk.v = pk.o = 0;
      a2_wave(a, 128 * x + arg + A2_NCH * wv, 128 * x + min(arg + A2_NCH * (wv + 1), 128), smem, wv, [&](int k, bool last) { if (k == 0) pk.look(); else { pk.o = 0; if (last) pk.claim(); } });
    }
    else if (kind == K_A3) task_a3(a, x, arg, smem, wv, pk);
    else if (kind == K_X) task_x<4>(a, x + 8 * (arg >> 1), 16 * (arg & 1), wv, pk);
    else if (kind == K_PU) task_pu(a, x + 8 * (arg >> 2), arg & 3, sel, smem, wv, pk);
    else if (kind == K_XL) task_x_late<1>(a, arg & 63, 4 * x + 2 * (arg >> 6), done, wv, pk);
    else if (kind == K_XM) task_x_late<2>(a, arg & 63, 4 * x, done, wv, pk);
    else if (kind == K_PUM) task_pu_late<2>(a, arg & 63, 2 * x + (arg >> 7), 0, done, sel, smem, wv, pk);
    else task_pu_late<1>(a, arg & 63, 2 * x + (arg >> 7), (arg >> 6) & 1, done, sel, smem, wv, pk);
    if (G.rot != rot0) return;
  }
}

__global__ __launch_bounds__(256, 1) void chain_kernel(ChainArgs a) {
  extern __shared__ __attribute__((aligned(16))) char smem[];
  const int b = blockIdx.x;
  {
    u32x4 rc;
    asm volatile("s_getpc_b64 s[60:61]\n\t.Lchain_here:\n\ts_sub_u32 s60, s60, .Lchain_here-_Z12chain_kernel9ChainArgs\n\ts_subb_u32 s61, s61, 0\n\ts_and_b32 s61, s61, 0xffff\n\t"
                 "s_mov_b32 s62, .Lchain_end-_Z12chain_kernel9ChainArgs\n\ts_mov_b32 s63, 0x20000\n\t"
                 "buffer_load_dwordx4 a[252:255], %1, s[60:63], 0 offen\n\tbuffer_load_dwordx4 a[252:255], %1, s[60:63], s63 offen"
                 : "={s[60:63]}"(rc) : "v"((b >> 3) * 4096 + threadIdx.x * 16) : "scc", "a252", "a253", "a254", "a255");
  }
  int w = __builtin_amdgcn_readfirstlane(threadIdx.x >> 6);
  Gate G;
  G.entry = __builtin_amdgcn_s_memrealtime(); G.open = false; G.arr = w == 0 ? b & 7 : -1;
  G.rot = ((unsigned)__builtin_amdgcn_s_getreg(20 | (3 << 11)) - b + WRONG) & 7u;
  {
    volatile LAS unsigned* t = (volatile LAS unsigned*)smem;
    if (w == 0) { t[0] = (unsigned)G.entry; t[1] = (unsigned)(G.entry >> 32); }
    __syncthreads();
    G.entry = (u64)(unsigned)__builtin_amdgcn_readfirstlane(t[0]) | ((u64)(unsigned)__builtin_amdgcn_readfirstlane(t[1]) << 32);
    __syncthreads();
  }
  if (b == 0) {
    const int lane = opaque_tid(0);
    const __amdgpu_buffer_rsrc_t rs = mk_rsrc(a.sync + SY_ABORT, SY_BYTES - SY_ABORT);
    for (int i = w; i * 1024 < SY_BYTES - SY_ABORT; i += 4) {
      const unsigned o = i * 1024 + lane * 16, q = o - (SY_CT - SY_ABORT + CT_QUEUE * 128);
      st16(rs, o, (u32x4){q < 1024u ? QWG - 1u : SENT, SENT, SENT, SENT});
    }
    bg_drain();
    if (w == 0 && lane == 0) __hip_atomic_store((GAS u64*)(a.sync + SY_EPOCH), (__builtin_amdgcn_s_memrealtime() & ~7ull) | G.rot, __ATOMIC_RELAXED, __HIP_MEMORY_SCOPE_AGENT);
  }
  while (true) {
    const unsigned rot = G.rot;
    asm volatile("" : "+s"(w));
    const int x = (b + rot) & 7, j = ((b >> 3) + (int)((JOFF >> (8 * x)) & 31)) & 31;
    const int bi = (x - 3) * 32 + j, role = x < 3 ? (j + x) % 3 : 3;
    const bool cal = role == 3 && bi % QWG >= QWG - 4;
    if ((cal || role == 2) && w >= 2) {
      const int n = opaque_tid(w - 2);
      const unsigned x0 = __builtin_amdgcn_raw_buffer_load_b32(mk_rsrc(a.sync, 254u << 20), n * PAGE + 4096 + (j & 31) * 128, 0, 2);
      const unsigned x1 = __builtin_amdgcn_raw_buffer_load_b32(mk_rsrc(a.out, 8u << 20), n * PAGE + (j & 31) * 128, 0, 2);
      asm volatile("" :: "v"(x0), "v"(x1));
    }
    if (cal) {
      const int n = bi / QWG * 4 + (bi & 3);
      const __amdgpu_buffer_rsrc_t rx = mk_rsrc((void*)a.X, 4u << 20);
      u32x4 v[32];
#pragma unroll
      for (int i = 0; i < 32; ++i) v[i] = __builtin_amdgcn_raw_buffer_load_b128(rx, (unsigned)(n * 4096 + opaque_tid(w) * 16), (unsigned)(i * 131072), 0);
#pragma unroll
      for (int i = 0; i < 32; ++i) asm volatile("" :: "v"(v[i]));
      if (!G.pass(a.sync)) continue;
      if (w < 2) calibrate(a, n + 32 * w);
    }
    if (role == 0) role_qk(a, j, smem, w, G);
    else if (role == 1) role_vo(a, j, smem, w, G);
    else if (role == 2) role_cb(a, j, smem, w, G);
    if (G.rot == rot) role_bg(a, smem, role < 3 ? j & 7 : bi / QWG, w, role < 3 || cal ? -1 : bi % QWG, G);
    if (G.rot == rot) break;
    __syncthreads();
  }
  asm volatile(".Lchain_end:");
}

extern "C" void kernel_launch(void* const* d_in, const int* in_sizes, int n_in,
                              void* d_out, int out_size, void* d_ws, size_t ws_size,
                              hipStream_t stream) {
  const u16* A_q = (const u16*)d_in[0];
  const u16* A_k = (const u16*)d_in[1];
  const u16* A_v = (const u16*)d_in[2];
  const u16* A_shift = (const u16*)d_in[3];
  const u16* A_ssmk = (const u16*)d_in[4];
  const u16* A_D = (const u16*)d_in[5];
  const u16* B_x = (const u16*)d_in[6];
  const u16* B_mem0 = (const u16*)d_in[7];
  const u16* B_Wq = (const u16*)d_in[8];
  const u16* B_Wk = (const u16*)d_in[9];
  const u16* B_Wv = (const u16*)d_in[10];
  const u16* B_Wo = (const u16*)d_in[11];
  u16* out = (u16*)d_out;
  char* ws = (char*)(((uintptr_t)d_ws + (2u << 20) - 1) & ~(uintptr_t)((2u << 20) - 1));
  u16* Qx = (u16*)(ws + OFF_QX);
  u16* Kx = (u16*)(ws + OFF_KX);
  u16* Vx = (u16*)(ws + OFF_VX);
  u16* kvT = (u16*)(ws + OFF_KVT);
  hipFuncSetAttribute((const void*)chain_kernel, hipFuncAttributeMaxDynamicSharedMemorySize, CHAIN_LDS);
  ChainArgs ca;
  ca.prof = (u64*)((char*)d_ws + PROF_AT);
  ca.mem0 = B_mem0; ca.Wq = B_Wq; ca.Wk = B_Wk; ca.Wv = B_Wv; ca.Wo = B_Wo; ca.X = B_x;
  ca.Qx = Qx; ca.Kx = Kx; ca.Vx = Vx; ca.Km = (u16*)(ws + OFF_KM);
  ca.P = (u16*)(ws + OFF_P); ca.out = out; ca.sync = ws;
  ca.Aq = A_q; ca.Ak = A_k; ca.Av = A_v; ca.Ashift = A_shift; ca.Assm = A_ssmk; ca.AD = A_D; ca.kvT = kvT;
  chain_kernel<<<256, 256, CHAIN_LDS, stream>>>(ca);
}
```

```cpp
#include <hip/hip_runtime.h>
#include <hip/hip_bf16.h>
#include <stdint.h>
#include <type_traits>

constexpr int S_LEN = 2048;
constexpr int D_A   = 1024;
constexpr int DM    = 1024;
constexpr int NH    = 32;
constexpr int DH    = 128;
constexpr int HD    = NH * DH;
constexpr int MEMR  = 8;
constexpr int SEG   = 32;
constexpr int NSEG  = S_LEN / SEG;
constexpr int OUTW  = 2048;

typedef unsigned short u16;
typedef unsigned long long u64;
typedef short bf16x8 __attribute__((ext_vector_type(8)));
typedef short bf16x4 __attribute__((ext_vector_type(4)));
typedef float f32x4 __attribute__((ext_vector_type(4)));
typedef float f32x16 __attribute__((ext_vector_type(16)));
typedef unsigned u32x4 __attribute__((ext_vector_type(4)));
typedef unsigned u32x2 __attribute__((ext_vector_type(2)));
#define LAS __attribute__((address_space(3)))
#define GAS __attribute__((address_space(1)))

constexpr size_t MB = 1u << 20;
constexpr size_t OFF_KVT = 130 * MB;
constexpr size_t PROF_AT = 0;

__device__ __forceinline__ float bf2f(u16 b) { return __uint_as_float(((unsigned)b) << 16); }
__device__ __forceinline__ u16 f2bf(float f) { __bf16 b = (__bf16)f; return __builtin_bit_cast(u16, b); }
__device__ __forceinline__ float rbf(float f) { return bf2f(f2bf(f)); }
__device__ __forceinline__ unsigned pk2(float a, float b) {
  typedef float f2 __attribute__((ext_vector_type(2)));
  typedef __bf16 b2 __attribute__((ext_vector_type(2)));
  f2 v = {a, b}; b2 r = __builtin_convertvector(v, b2); return __builtin_bit_cast(unsigned, r);
}

__device__ __forceinline__ int opaque_tid(int wv) {
  int l;
  asm volatile("v_mbcnt_lo_u32_b32 %0, -1, 0\n\tv_mbcnt_hi_u32_b32 %0, -1, %0" : "=v"(l));
  return wv * 64 + l;
}
__device__ __forceinline__ int swz_b(int row) { return ((row & 3) << 2) | ((row >> 2) & 3); }

#define KNOB(name, v) constexpr int name = v
KNOB(K_DEF, 80);
KNOB(W_STEP, 33); KNOB(W_OFF, 56);
#define RHOV 3
KNOB(RHO, RHOV);
#define CALN 3
#define CALM 600u
#define CALT 700u
#define KQ_D 500
#define W_NOW 4
#define PF1 0
#define PF2 0
#define U_SIG2 11
#define U_LD 5
#define STEAL 8
#define U_NOW 6
#define WRONG 0
#define OUTP "nt"
#define Q_SLP 32
#define LDNT(p) __builtin_nontemporal_load(p)
constexpr int CHK      = 16384;
constexpr int PAGE     = 2 << 20;
constexpr int SY_UM    = 34 * PAGE;
constexpr int SY_HOT   = 32 * PAGE;
constexpr int H_MEM    = 0;
constexpr int H_P      = 16 * CHK;
constexpr int H_ANN    = 28 * CHK;
constexpr int H_DLT    = 32 * CHK;
constexpr int H_TR     = 33 * CHK;
constexpr int H_PX     = 64 * CHK;
constexpr int H_BYTES  = 128 * CHK;
constexpr int URING    = 16;
constexpr int SY_SET   = 64 * PAGE;
constexpr int SY_EPOCH = SY_SET + 128;
constexpr int SY_ABORT = SY_SET + 256;
constexpr int SY_SEL   = SY_EPOCH + 8;
constexpr int SY_CT    = SY_ABORT + 128;
constexpr int CT_ARR = 0, CT_INIT = 8, CT_QUEUE = 9, CT_DONE = 17, CT_A1 = 33, CT_A2 = 41, CT_TK = 49;
constexpr int CT_Q = 57, CT_K = CT_Q + 32, CT_V = CT_K + 32, CT_U = CT_V + 32, CT_CAL = CT_U + 32;
constexpr int SY_BYTES = SY_CT + (CT_CAL + 64) * 128;
constexpr unsigned SENT = 0xFFFFFFFFu;
constexpr unsigned SPIN_CAP = 1u << 19;
constexpr int NLF = 16;
constexpr int L_WTS = 0;
constexpr int L_MEM = L_WTS + 4 * NLF * 2048;
constexpr int L_XCH = L_MEM + 16384;
constexpr int L_XK  = L_XCH + 2048;
constexpr int L_KX  = L_XCH + 4096;
constexpr int L_PRF = L_KX + 8192;
constexpr int XRS = 40;
constexpr int CHAIN_LDS = 160 * 1024;
static_assert(L_XCH + 128 * XRS <= L_PRF && L_PRF + 4096 <= CHAIN_LDS, "lds");

typedef __bf16 bfv8 __attribute__((ext_vector_type(8)));
typedef __bf16 bfv16 __attribute__((ext_vector_type(16)));
typedef unsigned u32x8 __attribute__((ext_vector_type(8)));
typedef unsigned u32x12 __attribute__((ext_vector_type(12)));
typedef unsigned u32x16 __attribute__((ext_vector_type(16)));
typedef unsigned u32x32 __attribute__((ext_vector_type(32)));
struct Wts { u32x8 a[32]; u32x32 v[4]; };

#define STAMPL(i) do {} while (0)
#define PVAL(i, v) do {} while (0)
#define PDECL(base, str) do {} while (0)
#define PZERO do {} while (0)
#define PDUMP(mask) do {} while (0)
#define TA(x) do {} while (0)
#define TB(x) TS(x)
#define TS(x) do {} while (0)
#define SSTAMP(i) do {} while (0)
#define WST(c, i) do {} while (0)
struct ChainArgs {
  u64* prof;
  const u16* mem0; const u16* Wq; const u16* Wk; const u16* Wv; const u16* Wo; const u16* X;
  u16* out; char* sync;
  const u16* Aq; const u16* Ak; const u16* Av; const u16* Ashift; const u16* Assm; const u16* AD; u16* kvT;
};
__device__ __forceinline__ GAS unsigned* ctr(const ChainArgs& a, int i) { return (GAS unsigned*)(a.sync + SY_CT + i * 128); }
__device__ __forceinline__ void ctr_add(const ChainArgs& a, int i, unsigned v) {
  __hip_atomic_fetch_add(ctr(a, i), v, __ATOMIC_RELAXED, __HIP_MEMORY_SCOPE_AGENT);
}

__device__ __forceinline__ __amdgpu_buffer_rsrc_t mk_rsrc(const void* p, unsigned bytes) {
  const u64 v = (u64)p;
  const unsigned lo = (unsigned)__builtin_amdgcn_readfirstlane((unsigned)v), hi = (unsigned)__builtin_amdgcn_readfirstlane((unsigned)(v >> 32));
  const u64 u = (u64)lo | ((u64)hi << 32);
  return __builtin_amdgcn_make_buffer_rsrc((void*)u, 0, bytes, 0x00020000);
}
__device__ __forceinline__ u32x4 own_rsrc(const void* p, unsigned bytes) {
  u32x4 r = {(unsigned)__builtin_amdgcn_readfirstlane((unsigned)(u64)p), (unsigned)__builtin_amdgcn_readfirstlane((unsigned)((u64)p >> 32)) & 0xffffu, bytes, 0x00020000u};
  asm volatile("s_nop 4" : "+s"(r));
  return r;
}
constexpr int LD_AUX = 16, ST_AUX = 16;
__device__ __forceinline__ u32x4 ld16(__amdgpu_buffer_rsrc_t rs, unsigned off) {
  return __builtin_amdgcn_raw_buffer_load_b128(rs, off, 0, LD_AUX);
}
__device__ __forceinline__ void st16(__amdgpu_buffer_rsrc_t rs, unsigned off, u32x4 v) {
  __builtin_amdgcn_raw_buffer_store_b128(v, rs, off, 0, ST_AUX);
}
__device__ __forceinline__ void st16s(u32x4 v, __amdgpu_buffer_rsrc_t rs, unsigned voff, unsigned soff) {
  __builtin_amdgcn_raw_buffer_store_b128(v, rs, voff, soff, ST_AUX);
  asm volatile("s_nop 1" :: "v"(v) : "memory");
}
__device__ __forceinline__ void st8(__amdgpu_buffer_rsrc_t rs, unsigned off, u32x2 v) {
  __builtin_amdgcn_raw_buffer_store_b64(v, rs, off, 0, ST_AUX);
}
__device__ __forceinline__ void st4(__amdgpu_buffer_rsrc_t rs, unsigned off, unsigned v) {
  __builtin_amdgcn_raw_buffer_store_b32(v, rs, off, 0, ST_AUX);
}
__device__ __forceinline__ unsigned ld4(__amdgpu_buffer_rsrc_t rs, unsigned off) {
  return __builtin_amdgcn_raw_buffer_load_b32(rs, off, 0, LD_AUX);
}
constexpr int TU = 16;
__device__ __forceinline__ unsigned cyc32() { return (unsigned)__builtin_amdgcn_s_memtime(); }
__device__ __forceinline__ unsigned to_sgpr(unsigned x) {
  unsigned r;
  asm volatile("s_nop 0\n\tv_readfirstlane_b32 %0, %1\n\ts_nop 4" : "=s"(r) : "v"(x));
  return r;
}
struct Clk {
  unsigned ce, te;
  unsigned c2t, t2c;
  __device__ __forceinline__ void edge(unsigned& c, unsigned& t) {
    u64 c0, r0, r1, r2, r3, r4, r5;
    asm volatile("s_memtime %0\n\ts_memrealtime %1\n\ts_nop 4\n\ts_memrealtime %2\n\ts_nop 4\n\ts_memrealtime %3\n\ts_nop 4\n\t"
                 "s_memrealtime %4\n\ts_nop 4\n\ts_memrealtime %5\n\ts_nop 4\n\ts_memrealtime %6\n\ts_waitcnt lgkmcnt(0)"
                 : "=&s"(c0), "=&s"(r0), "=&s"(r1), "=&s"(r2), "=&s"(r3), "=&s"(r4), "=&s"(r5) :: "memory");
    const unsigned x = (unsigned)r0;
    const unsigned n = ((unsigned)r1 == x) + ((unsigned)r2 == x) + ((unsigned)r3 == x) + ((unsigned)r4 == x) + ((unsigned)r5 == x);
    c = to_sgpr((unsigned)c0 + 24u * n); t = (x + 4u) * TU;
  }
  __device__ __forceinline__ void init() { edge(ce, te); c2t = 44000u; t2c = 97600u; }
  __device__ __forceinline__ void sync() {
    unsigned c, t;
    edge(c, t);
    set(c, t);
  }
  __device__ __forceinline__ void set(unsigned c, unsigned t) {
    if (c == ce) return;
    const float q = (float)(t - te) * __builtin_amdgcn_rcpf((float)(c - ce));
    const float qc = fminf(fmaxf(q, 0.51f), 1.99f);
    c2t = to_sgpr((unsigned)(qc * 65536.f));
    t2c = to_sgpr((unsigned)(__builtin_amdgcn_rcpf(qc) * 65536.f));
    ce = c; te = t;
  }
  __device__ __forceinline__ unsigned time(unsigned c) const { return te + ((min(c - ce, 30000u) * c2t) >> 16); }
  __device__ __forceinline__ unsigned now() const { return time(cyc32()); }
  __device__ __forceinline__ unsigned target(unsigned t) const {
    return to_sgpr(ce + (((unsigned)min(max((int)(t - te), 0), 20000) * t2c) >> 16));
  }
  __device__ __forceinline__ void wait(unsigned t) const { wait_c(target(t)); }
  __device__ __forceinline__ void wait_c(unsigned c) const {
    unsigned y;
    asm volatile("1:\n\ts_memtime vcc\n\ts_waitcnt lgkmcnt(0)\n\ts_sub_i32 %0, %1, vcc_lo\n\ts_cmp_gt_i32 %0, 128\n\ts_cbranch_scc1 1b\n\t"
                 "2:\n\ts_sub_i32 %0, %0, %2\n\ts_cmp_gt_i32 %0, %3\n\ts_cbranch_scc1 2b"
                 : "=&s"(y) : "s"(c), "n"(W_STEP), "n"(W_OFF) : "memory", "scc", "vcc");
  }
};
__device__ __forceinline__ unsigned nosent(unsigned d) { return min(d, SENT - 1u); }
__device__ __forceinline__ int arow(int row, int half) { return 4 * (row >> 1) + 2 * half + (row & 1); }
__device__ __forceinline__ unsigned umax4(u32x4 v) { return max(max(v[0], v[1]), max(v[2], v[3])); }
__device__ __forceinline__ void stamp_again(GAS u64* p) {
  __hip_atomic_store(p, (__builtin_amdgcn_s_memrealtime() & ~7ull) | (__hip_atomic_load(p, __ATOMIC_RELAXED, __HIP_MEMORY_SCOPE_AGENT) & 7ull),
                     __ATOMIC_RELAXED, __HIP_MEMORY_SCOPE_AGENT);
}
__device__ __forceinline__ bool poll_fail(unsigned& spins, GAS unsigned* abortw, int lane) {
  ++spins;
  if (spins > SPIN_CAP) {
    if (lane == 0) __hip_atomic_store(abortw, 1u, __ATOMIC_RELAXED, __HIP_MEMORY_SCOPE_AGENT);
    return true;
  }
  if ((spins & 511u) == 0u && lane == 0) {
    unsigned n = 0;
    for (int i = 0; i < 8; ++i) n += __hip_atomic_load(abortw + (SY_CT - SY_ABORT + (CT_ARR + i) * 128) / 4, __ATOMIC_RELAXED, __HIP_MEMORY_SCOPE_AGENT) + 1u;
    if (n < 256u) stamp_again((GAS u64*)(abortw - 32));
  }
  if ((spins & 63u) == 0u)
    return __builtin_amdgcn_readfirstlane(__hip_atomic_load(abortw, __ATOMIC_RELAXED, __HIP_MEMORY_SCOPE_AGENT)) != SENT;
  return false;
}
struct Gate {
  u64 entry, sel; int arr; unsigned rot; bool open;
  __device__ __forceinline__ bool look(char* sync, u32x4 v) {
    if (open) return true;
    for (unsigned spins = 1;; ++spins) {
#pragma unroll
      for (int i = 0; i < 4; ++i) v[i] = __builtin_amdgcn_readfirstlane(v[i]);
      if (((u64)v[1] << 32 | v[0]) - entry + 2000u < (1ull << 31) || spins > 4 * SPIN_CAP) break;
      if (spins > 1) __builtin_amdgcn_s_sleep(8);
      v = ld16(mk_rsrc(sync + SY_EPOCH, 16), 0);
    }
    asm volatile("" ::: "memory");
    sel = (u64)v[3] << 32 | v[2];
    const bool same = rot == (v[0] & 7u);
    rot = v[0] & 7u; open = true;
    return same;
  }
  __device__ __forceinline__ void ask(char* sync, u32x4& v) { asm volatile("buffer_load_dwordx4 %0, off, %1, 0 sc1" : "=v"(v) : "s"(own_rsrc(sync + SY_EPOCH, 16)) : "memory"); }
  __device__ __forceinline__ void arrive(char* sync) {
    if (arr >= 0 && opaque_tid(0) == 0) __hip_atomic_fetch_add((GAS unsigned*)(sync + SY_CT + (CT_ARR + arr) * 128), 1u, __ATOMIC_RELAXED, __HIP_MEMORY_SCOPE_AGENT);
    arr = -1;
  }
  __device__ __forceinline__ bool pass(char* sync, u32x4 v = {0u, 0u, 0u, 0u}) { const bool r = look(sync, v); arrive(sync); return r; }
};
__device__ __forceinline__ unsigned ann_val(unsigned t, int s) { return (t << 8) | (unsigned)s; }
__device__ __forceinline__ unsigned tr_place(int s) { return (s & 2) * (CHK / 2) + (s & 1) * 4096; }
__device__ __forceinline__ unsigned die_half() { return ((unsigned)__builtin_amdgcn_s_getreg(20 | (3 << 11)) >> 2) & 1u; }
typedef u64 Sel;
__device__ __forceinline__ void start_wait(char* sync, GAS unsigned* abortw, int lane, bool& dead, int ci, int cj) {
  unsigned spins = 0;
  GAS unsigned* p = (GAS unsigned*)(sync + SY_CT + (lane == 0 ? CT_INIT : (lane == 1 ? ci : cj)) * 128);
  while (!__all(__hip_atomic_load(p, __ATOMIC_RELAXED, __HIP_MEMORY_SCOPE_AGENT) + 1u >= (lane == 0 ? 64u : 1u))) {
    __builtin_amdgcn_s_sleep(4);
    if (dead || poll_fail(spins, abortw, lane)) { dead = true; break; }
  }
}
__device__ __forceinline__ __amdgpu_buffer_rsrc_t rsrc_u(char* sync) { return mk_rsrc((void*)sync, SY_SET); }
__device__ __forceinline__ unsigned near_half(unsigned off, u64 sel) { return off + ((unsigned)(sel >> ((off >> 21) & 63)) & 1u) * 8192; }
__device__ __forceinline__ unsigned x_slot(int kind, int s, int h, Sel sel) {
  return kind == 2 ? near_half(32 * PAGE + (s * 32 + h) * CHK, ~sel) : near_half((s * 64 + 2 * h + 1 - kind) * CHK, sel);
}
__device__ __forceinline__ int hpos(int h) { return (h & 3) * 8 + (h >> 2); }
constexpr unsigned UXH = 8 * CHK;
__device__ __forceinline__ unsigned ux_base(int s, int h, Sel sel) { return near_half(((s & (URING - 1)) * 32 + h) * UXH, ~sel); }
__device__ __forceinline__ constexpr unsigned ux_tile(int t) { return (t >> 3) * CHK + (t & 7) * 1024; }
__device__ __forceinline__ __amdgpu_buffer_rsrc_t rsrc_hot(char* sync, Sel sel) {
  return mk_rsrc((void*)(sync + near_half(SY_HOT, sel)), H_BYTES - 8192);
}
template <int SLP = 4>
__device__ __forceinline__ void ctr_wait(GAS unsigned* c, unsigned need, GAS unsigned* abortw, int lane, bool& dead) {
  unsigned spins = 0;
  while (true) {
    unsigned v = __builtin_amdgcn_readfirstlane(__hip_atomic_load(c, __ATOMIC_RELAXED, __HIP_MEMORY_SCOPE_AGENT)) + 1u;
    if (v >= need) break;
    __builtin_amdgcn_s_sleep(SLP);
    if (dead || poll_fail(spins, abortw, lane)) { dead = true; break; }
  }
}
template <int N> __device__ __forceinline__ void wait_vm() { asm volatile("s_waitcnt vmcnt(%0)" :: "n"(N) : "memory"); }
__device__ __forceinline__ void lds_barrier() {
  asm volatile("s_waitcnt lgkmcnt(0)" ::: "memory");
  __builtin_amdgcn_s_barrier();
  asm volatile("" ::: "memory");
}
__device__ __forceinline__ f32x4 vzero() { f32x4 z = {0.f, 0.f, 0.f, 0.f}; asm volatile("" : "+v"(z)); return z; }
__device__ __forceinline__ f32x4 smf(u32x4 a, u32x8 b, f32x4 c, int idx) {
  return __builtin_amdgcn_smfmac_f32_16x16x64_bf16(__builtin_bit_cast(bfv8, a), __builtin_bit_cast(bfv16, b), c, idx, 0, 0);
}
__device__ __forceinline__ float red16max(float x) { auto r = __builtin_amdgcn_permlane16_swap(__float_as_uint(x), __float_as_uint(x), false, false); return fmaxf(__uint_as_float(r[0]), __uint_as_float(r[1])); }
__device__ __forceinline__ float red32max(float x) { auto r = __builtin_amdgcn_permlane32_swap(__float_as_uint(x), __float_as_uint(x), false, false); return fmaxf(__uint_as_float(r[0]), __uint_as_float(r[1])); }
__device__ __forceinline__ float red16add(float x) { auto r = __builtin_amdgcn_permlane16_swap(__float_as_uint(x), __float_as_uint(x), false, false); return __uint_as_float(r[0]) + __uint_as_float(r[1]); }
__device__ __forceinline__ float red32add(float x) { auto r = __builtin_amdgcn_permlane32_swap(__float_as_uint(x), __float_as_uint(x), false, false); return __uint_as_float(r[0]) + __uint_as_float(r[1]); }
constexpr bool blk_in_lds(int R, int b) {
  return R == 0 ? ((b & 7) == 1 || (b & 7) == 4) : (b < 16 ? (b & 3) == 0 : (b & 3) == 3);
}
constexpr int lds_index(int R, int b) { int n = 0; for (int i = 0; i < b; ++i) if (blk_in_lds(R, i)) ++n; return n; }
constexpr bool frag_in_lds(int R, int f) { return blk_in_lds(R, f >> 1); }
constexpr int frag_reg_slot(int R, int f) { int n = 0; for (int i = 0; i < f; ++i) if (!frag_in_lds(R, i)) ++n; return n; }

constexpr int m_block(int R, int m, int i) {
  return R == 0 ? 2 * (i < 12 ? (m ? i + i / 3 + 1 : i + (i + 1) / 3) : 4 * (i - 12) + 2 - 2 * m) + m : m ? 16 + 4 * (i & 3) + (i >> 2) : (i < 12 ? i + i / 3 + 1 : 4 * (i - 12));
}
#define VO_ORD 1
constexpr int w_block(int R, int i) { return R && VO_ORD ? m_block(R, i < 12, i % 12) : m_block(R, i & 1, i >> 1); }
#define W_BG 0xfedcba9876543210ull
#define W_HELP 0xfea74ull
#define W_NT 0
#define W_HELP2 0xdef32ull
#define U_ORD 0x3210
constexpr u64 bg_order(int m) { u64 r = 0; for (int n = 0; n < 16; ++n) r |= (u64)(m_block(0, m, (W_BG >> (4 * n)) & 15) >> 1) << (4 * n); return r; }
struct WSched { int ord[32]; int slot[32]; int issue[32]; int out[32]; int reg[32]; };
constexpr WSched make_wsched(int R) {
  WSched S{};
  int n = 0;
  for (int i = 0; i < 24; ++i) S.ord[n++] = w_block(R, i);
  for (int i = 0; i < n; ++i) S.reg[i] = frag_reg_slot(R, 2 * S.ord[i]);
  const int npure = n;
  for (int b = 0; b < 32; ++b) if (blk_in_lds(R, b)) S.ord[n++] = b;
  int seq[32] = {};
  int nissued = 0, next = 0;
  for (int i = 0; i < 10; ++i) { S.slot[i] = (i + 10 - npure % 10) % 10; S.issue[i] = -1; seq[i] = nissued++; }
  next = 10;
  S.out[0] = nissued - 1;
  for (int c = 0; c <= npure; ++c) {
    if (c >= 1) {
      const int k = S.slot[c - 1];
      if (next < npure) { S.slot[next] = k; S.issue[next] = c; seq[next] = nissued++; ++next; }
      else if (k < NLF / 2) { S.slot[npure + k] = k; S.issue[npure + k] = c; seq[npure + k] = nissued++; }
    }
    if (c + 1 < npure) S.out[c + 1] = nissued - 1 - seq[c + 1];
  }
  return S;
}
__device__ __forceinline__ int kcol(int k) { return 8 * ((k >> 1) & 1) + ((k & 1) ? 7 - (k >> 2) : k >> 2); }
template <bool PERM = false>
__device__ __forceinline__ u32x8 tr_frag(const char* sb, int tile, int lane) {
  u32x8 f;
#pragma unroll
  for (int q = 0; q < 4; ++q) {
    int row = (q >> 1) * 32 + 8 * (lane >> 4) + 4 * (q & 1) + ((lane & 15) >> 2);
    if (PERM) row = (row & ~15) + kcol(row & 15);
    int p = lane & 3;
    int lc = 2 * tile + (p >> 1);
    int addr = row * 256 + ((lc ^ swz_b(row)) * 16) + 8 * (p & 1);
    bf16x4 v = __builtin_amdgcn_ds_read_tr16_b64_v4i16((LAS bf16x4*)(sb + addr));
    u32x2 u = __builtin_bit_cast(u32x2, v);
    f[2 * q] = u[0]; f[2 * q + 1] = u[1];
  }
  return f;
}
__device__ __forceinline__ void dma16(__amdgpu_buffer_rsrc_t rs, unsigned voff, unsigned soff, unsigned lds, bool nt = false, bool pad = true) {
  if (nt) asm volatile("s_mov_b32 m0, %0\n\ts_nop 3\n\tbuffer_load_dwordx4 %1, %2, %3 offen nt lds" :: "s"(lds), "v"(voff), "s"(rs), "s"(soff) : "m0", "memory");
  else if (pad) asm volatile("s_mov_b32 m0, %0\n\ts_nop 3\n\tbuffer_load_dwordx4 %1, %2, %3 offen lds" :: "s"(lds), "v"(voff), "s"(rs), "s"(soff) : "m0", "memory");
  else asm volatile("s_mov_b32 m0, %0\n\ts_nop 0\n\tbuffer_load_dwordx4 %1, %2, %3 offen lds" :: "s"(lds), "v"(voff), "s"(rs), "s"(soff) : "m0", "memory");
}
__device__ __forceinline__ u32x8 tr_frag2(const char* sb, int tile, int f, int lane) {
  u32x8 r;
#pragma unroll
  for (int c = 0; c < 2; ++c)
#pragma unroll
    for (int t = 0; t < 2; ++t) {
      const int row = 32 * c + 16 * f + 4 * (lane >> 4) + ((lane & 15) >> 2);
      const int p = lane & 3, lc = 2 * (tile + t) + (p >> 1);
      const u32x2 u = __builtin_bit_cast(u32x2, __builtin_amdgcn_ds_read_tr16_b64_v4i16((LAS bf16x4*)(sb + row * 256 + ((lc ^ swz_b(row)) * 16) + 8 * (p & 1))));
      r[4 * c + t] = u[0]; r[4 * c + 2 + t] = u[1];
    }
  return r;
}
template <int ROLE>
__device__ __forceinline__ void load_weights(const ChainArgs& a, int h, int w, int lane, char* smem, Wts& W) {
#pragma unroll
  for (int i = 0; i < 4; ++i) asm volatile("" : "=v"(W.v[i]));
#pragma unroll
  for (int i = 0; i < 32; ++i) asm volatile("" : "=a"(W.a[i]));
  constexpr WSched S = make_wsched(ROLE);
  constexpr int NP = 32 - NLF / 2;
  const __amdgpu_buffer_rsrc_t r0 = mk_rsrc(ROLE == 0 ? a.Wq : a.Wv, 8u << 20), r1 = mk_rsrc(ROLE == 0 ? a.Wk : a.Wo, 8u << 20);
  const unsigned lds0 = (unsigned)(uintptr_t)(LAS char*)smem;
  unsigned vo[2][4];
#pragma unroll
  for (int j = 0; j < 4; ++j) {
    const int row = (w * 4 + j) * 4 + (lane >> 4), lc = (lane & 15) ^ swz_b(row);
    vo[0][j] = row * HD * 2 + lc * 16; vo[1][j] = row * DM * 2 + lc * 16;
  }
  asm volatile("s_nop 4");
  auto stage = [&](int i) {
    const int blk = S.ord[i];
    const bool second = ROLE == 0 ? (blk & 1) : blk >= 16, wo = ROLE == 1 && second;
    const unsigned so = wo ? (unsigned)(((128 * h + 64 * (blk & 1)) * DM + 128 * (((blk - 16) >> 1) ^ (2 * (h & 3)))) * 2)
                           : (unsigned)((64 * ((ROLE == 0 ? blk >> 1 : blk) ^ (h & RHO)) * HD + 128 * h) * 2);
#pragma unroll
    for (int j = 0; j < 4; ++j) dma16(second ? r1 : r0, vo[wo][j], so, lds0 + 16384 * S.slot[i] + (w * 4 + j) * 1024, W_NT & (ROLE ? 8 : 1));
  };
#pragma unroll
  for (int i = 0; i < 32; ++i) if (S.issue[i] == -1) stage(i);
#pragma unroll
  for (int c = 0; c < NP; ++c) {
    asm volatile("s_waitcnt vmcnt(%0)" :: "n"(4 * S.out[c]) : "memory");
    lds_barrier();
#pragma unroll
    for (int i = 0; i < 32; ++i) if (S.issue[i] == c) stage(i);
    const char* sb = smem + 16384 * S.slot[c];
#pragma unroll
    for (int t = 0; t < 2; ++t) {
      u32x8 f = (ROLE == 1 && S.ord[c] >= 16) ? tr_frag2(sb, 2 * w, t, lane) : tr_frag<true>(sb, 2 * w + t, lane);
      if (S.reg[c] < 32) {
#pragma unroll
        for (int i = 0; i < 8; ++i) asm volatile("v_accvgpr_write_b32 a[%1], %0" :: "v"(f[i]), "n"(8 * ((S.reg[c] + t) & 31) + i));
      }
      else {
#pragma unroll
        for (int i = 0; i < 8; ++i) W.v[(S.reg[c] >> 2) & 3][8 * ((S.reg[c] + t) & 3) + i] = f[i];
      }
    }
    if (S.reg[c] >= 32) asm volatile("" : "+v"(W.v[(S.reg[c] >> 2) & 3]));
  }
  static_assert(S.issue[31] < NP && S.issue[NP] < S.issue[31], "resident blocks are issued in slot order inside the loop");
#pragma unroll
  for (int j = 0; j < NLF / 2; ++j) {
    asm volatile("s_waitcnt vmcnt(%0)" :: "n"(4 * (NLF / 2 - 1 - j)) : "memory");
    lds_barrier();
    const bool two = ROLE == 1 && S.ord[NP + j] >= 16;
    u32x8 f0 = two ? tr_frag2(smem + 16384 * j, 2 * w, 0, lane) : tr_frag<true>(smem + 16384 * j, 2 * w, lane);
    u32x8 f1 = two ? tr_frag2(smem + 16384 * j, 2 * w, 1, lane) : tr_frag<true>(smem + 16384 * j, 2 * w + 1, lane);
    lds_barrier();
    char* d = smem + 16384 * j + w * 4096 + lane * 16;
    *(u32x4*)d = (u32x4){f0[0], f0[1], f0[2], f0[3]};
    *(u32x4*)(d + 1024) = (u32x4){f0[4], f0[5], f0[6], f0[7]};
    *(u32x4*)(d + 2048) = (u32x4){f1[0], f1[1], f1[2], f1[3]};
    *(u32x4*)(d + 3072) = (u32x4){f1[4], f1[5], f1[6], f1[7]};
  }
  lds_barrier();
}
template <int... Is> struct iseq {};
template <int N, int... Is> struct mkseq : mkseq<N - 1, N - 1, Is...> {};
template <int... Is> struct mkseq<0, Is...> { typedef iseq<Is...> type; };

#define SB0 __builtin_amdgcn_sched_barrier(0)
#define CAT8(x, y) ((u32x8){x[0], x[1], x[2], x[3], y[0], y[1], y[2], y[3]})
template <class F, int... I>
__device__ __forceinline__ void for_slots(iseq<I...>, F f) { (f(std::integral_constant<int, I>{}), ...); }
#define QK_BODY \
  "s_cmp_eq_u32 %[wv], 1\n\t" \
  "s_cbranch_scc1 91f\n\t" \
  "s_cmp_eq_u32 %[wv], 2\n\t" \
  "s_cbranch_scc1 92f\n\t" \
  "s_cmp_eq_u32 %[wv], 3\n\t" \
  "s_cbranch_scc1 93f\n\t" \
  "90:\n\t" \
  "s_mov_b32 s58, s42\n\t" \
  "20:\n\t" \
  "s_and_b32 s76, s44, 7\n\t" \
  "s_lshl_b32 s76, s76, 15\n\t" \
  "s_add_i32 s40, s54, s76\n\t" \
  "s_and_b32 s76, s44, 3\n\t" \
  "s_lshl_b32 s45, s76, 14\n\t" \
  "s_mul_i32 s47, s76, 0xc000\n\t" \
  "s_add_i32 s47, s47, 0x40000\n\t" \
  "s_add_i32 s49, s44, -1\n\t" \
  "s_and_b32 s48, s49, 2\n\t" \
  "s_lshl_b32 s48, s48, 13\n\t" \
  "s_and_b32 s76, s49, 1\n\t" \
  "s_lshl_b32 s76, s76, 12\n\t" \
  "s_add_i32 s48, s48, s76\n\t" \
  "s_lshl_b32 s50, s53, 8\n\t" \
  "s_mov_b32 s59, 0\n\t" \
  "s_mov_b32 s92, s51\n\t" \
  "s_mov_b32 s93, s53\n\t" \
  "s_xor_b32 s76, s40, 0x400\n\t" \
  "s_xor_b32 s77, s40, 0x800\n\t" \
  "s_xor_b32 s78, s40, 0xc00\n\t" \
  "s_cmp_lg_u32 s58, 0\n\t" \
  "s_cbranch_scc1 11f\n\t" \
  "10:\n\t" \
  "ds_read_b128 v[48:51], v102 offset:0\n\t" \
  "ds_read_b128 v[52:55], v102 offset:1024\n\t" \
  "ds_read_b128 v[56:59], v102 offset:2048\n\t" \
  "ds_read_b128 v[60:63], v102 offset:3072\n\t" \
  "ds_read_b128 v[16:19], v102 offset:16384\n\t" \
  "ds_read_b128 v[20:23], v102 offset:17408\n\t" \
  "ds_read_b128 v[24:27], v102 offset:18432\n\t" \
  "ds_read_b128 v[28:31], v102 offset:19456\n\t" \
  "ds_read_b128 v[84:87], v102 offset:49152\n\t" \
  "ds_read_b128 v[88:91], v102 offset:50176\n\t" \
  "ds_read_b128 v[92:95], v102 offset:51200\n\t" \
  "ds_read_b128 v[96:99], v102 offset:52224\n\t" \
  "s_waitcnt lgkmcnt(4)\n\t" \
  "ds_read_b128 v[0:3], v103 offset:16384\n\t" \
  "ds_read_b128 v[4:7], v103 offset:17408\n\t" \
  "ds_read_b128 v[8:11], v103 offset:18432\n\t" \
  "ds_read_b128 v[12:15], v103 offset:19456\n\t" \
  "s_bitcmp1_b32 s58, 1\n\t" \
  "s_cselect_b64 s[80:81], -1, 0\n\t" \
  "s_cmp_lg_u32 s58, 0\n\t" \
  "s_cbranch_scc1 17f\n\t" \
  "s_mov_b32 exec_hi, 0\n\t" \
  "16:\n\t" \
  "s_memtime vcc\n\t" \
  "s_waitcnt lgkmcnt(0)\n\t" \
  "s_sub_i32 s79, s41, vcc_lo\n\t" \
  "s_cmp_gt_i32 s79, 64\n\t" \
  "s_cbranch_scc1 16b\n\t" \
  "13:\n\t" \
  "buffer_load_dword v116, v112, %[rh], s48 offen sc1\n\t" \
  "s_waitcnt vmcnt(0)\n\t" \
  "15:\n\t" \
  "v_mov_b32 v117, 20000\n\t" \
  "v_cmp_ne_u32_sdwa vcc, v116, s49 src0_sel:BYTE_0 src1_sel:DWORD\n\t" \
  "v_subrev_u32 v116, s50, v116\n\t" \
  "v_ashrrev_i32 v116, 8, v116\n\t" \
  "s_nop 1\n\t" \
  "v_max_i32_dpp v116, v116, v116 quad_perm:[1,0,3,2] row_mask:0xf bank_mask:0xf\n\t" \
  "s_cbranch_vccnz 14f\n\t" \
  "s_nop 0\n\t" \
  "v_max_i32_dpp v116, v116, v116 quad_perm:[2,3,0,1] row_mask:0xf bank_mask:0xf\n\t" \
  "s_nop 1\n\t" \
  "v_max_i32_dpp v116, v116, v116 row_half_mirror row_mask:0xf bank_mask:0xf\n\t" \
  "s_nop 1\n\t" \
  "v_max_i32_dpp v116, v116, v116 row_mirror row_mask:0xf bank_mask:0xf\n\t" \
  "v_med3_i32 v116, v116, 0, v117\n\t" \
  "v_mul_u32_u24 v116, s52, v116\n\t" \
  "v_lshrrev_b32 v116, 16, v116\n\t" \
  "s_mov_b64 exec, -1\n\t" \
  "v_readfirstlane_b32 s84, v116\n\t" \
  "v_readlane_b32 s85, v116, 16\n\t" \
  "s_max_u32 s84, s84, s85\n\t" \
  "s_add_i32 s84, s84, s51\n\t" \
  "1:\n\t" \
  "s_memtime vcc\n\t" \
  "s_waitcnt lgkmcnt(0)\n\t" \
  "s_sub_i32 s79, s84, vcc_lo\n\t" \
  "s_cmp_gt_i32 s79, 128\n\t" \
  "s_cbranch_scc1 1b\n\t" \
  ".p2align 4\n\t" \
  "2:\n\t" \
  "s_sub_i32 s79, s79, 33\n\t" \
  "s_cmp_gt_i32 s79, 56\n\t" \
  "s_cbranch_scc1 2b\n\t" \
  "buffer_load_dwordx4 v[32:35], v100, %[rh], s40 offen sc1\n\t" \
  "s_nop 12\n\t" \
  "buffer_load_dwordx4 v[36:39], v100, %[rh], s76 offen sc1\n\t" \
  "s_nop 12\n\t" \
  "buffer_load_dwordx4 v[40:43], v100, %[rh], s77 offen sc1\n\t" \
  "s_nop 12\n\t" \
  "buffer_load_dwordx4 v[44:47], v100, %[rh], s78 offen sc1\n\t" \
  "s_memtime vcc\n\t" \
  "s_memrealtime s[82:83]\n\t" \
  "s_nop 4\n\t" \
  "s_memrealtime s[84:85]\n\t" \
  "s_nop 4\n\t" \
  "s_memrealtime s[56:57]\n\t" \
  "s_nop 4\n\t" \
  "s_memrealtime s[86:87]\n\t" \
  "s_nop 4\n\t" \
  "s_memrealtime s[88:89]\n\t" \
  "s_nop 4\n\t" \
  "s_memrealtime s[90:91]\n\t" \
  "s_mov_b32 s92, 0\n\t" \
  "s_waitcnt lgkmcnt(0)\n\t" \
  "s_cmp_eq_u32 s82, s84\n\t" \
  "s_addc_u32 s92, s92, 0\n\t" \
  "s_cmp_eq_u32 s82, s56\n\t" \
  "s_addc_u32 s92, s92, 0\n\t" \
  "s_cmp_eq_u32 s82, s86\n\t" \
  "s_addc_u32 s92, s92, 0\n\t" \
  "s_cmp_eq_u32 s82, s88\n\t" \
  "s_addc_u32 s92, s92, 0\n\t" \
  "s_cmp_eq_u32 s82, s90\n\t" \
  "s_addc_u32 s92, s92, 0\n\t" \
  "s_mul_i32 s92, s92, 24\n\t" \
  "s_add_i32 s92, s92, vcc_lo\n\t" \
  "s_lshl4_add_u32 s93, s82, 64\n\t" \
  "3:\n\t" \
  "s_sub_i32 s84, s92, s43\n\t" \
  "s_lshl_b32 s85, s93, 8\n\t" \
  "s_or_b32 s85, s85, s44\n\t" \
  "v_mov_b32 v118, s85\n\t" \
  "v_mov_b64 v[68:69], 0\n\t" \
  "v_mov_b64 v[70:71], 0\n\t" \
  "v_mov_b64 v[72:73], 0\n\t" \
  "v_mov_b64 v[74:75], 0\n\t" \
  "v_mov_b64 v[76:77], 0\n\t" \
  "v_mov_b64 v[78:79], 0\n\t" \
  "v_mov_b64 v[80:81], 0\n\t" \
  "v_mov_b64 v[82:83], 0\n\t" \
  "s_waitcnt lgkmcnt(0)\n\t" \
  "s_waitcnt vmcnt(3)\n\t" \
  "v_smfmac_f32_16x16x64_bf16 v[68:71], v[32:35], a[0:7], v104\n\t" \
  "ds_write_b128 v101, v[32:35] offset:0\n\t" \
  "v_smfmac_f32_16x16x64_bf16 v[72:75], v[32:35], a[8:15], v104\n\t" \
  "v_smfmac_f32_16x16x64_bf16 v[76:79], v[32:35], v[48:55], v104\n\t" \
  "v_smfmac_f32_16x16x64_bf16 v[80:83], v[32:35], v[56:63], v104\n\t" \
  "s_waitcnt vmcnt(2)\n\t" \
  "v_smfmac_f32_16x16x64_bf16 v[68:71], v[36:39], a[16:23], v104\n\t" \
  "ds_write_b128 v101, v[36:39] offset:1024\n\t" \
  "v_smfmac_f32_16x16x64_bf16 v[72:75], v[36:39], a[24:31], v104\n\t" \
  "v_smfmac_f32_16x16x64_bf16 v[76:79], v[36:39], a[32:39], v104\n\t" \
  "v_cmp_u_f32 vcc, v68, v69\n\t" \
  "v_smfmac_f32_16x16x64_bf16 v[80:83], v[36:39], a[40:47], v104\n\t" \
  "v_cmp_u_f32 s[82:83], v70, v71\n\t" \
  "s_waitcnt vmcnt(1)\n\t" \
  "v_smfmac_f32_16x16x64_bf16 v[68:71], v[40:43], v[16:23], v104\n\t" \
  "ds_write_b128 v101, v[40:43] offset:2048\n\t" \
  "v_smfmac_f32_16x16x64_bf16 v[72:75], v[40:43], v[24:31], v104\n\t" \
  "s_or_b64 vcc, vcc, s[82:83]\n\t" \
  "s_or_b64 vcc, vcc, s[80:81]\n\t" \
  "s_waitcnt lgkmcnt(1)\n\t" \
  "v_smfmac_f32_16x16x64_bf16 v[76:79], v[40:43], a[48:55], v104\n\t" \
  "s_cbranch_scc1 8f\n\t" \
  "5:\n\t" \
  "s_barrier\n\t" \
  "4:\n\t" \
  "s_memtime s[56:57]\n\t" \
  "ds_read_b128 v[48:51], v101 offset:4096\n\t" \
  "v_smfmac_f32_16x16x64_bf16 v[80:83], v[40:43], a[56:63], v104\n\t" \
  "ds_read_b128 v[52:55], v101 offset:5120\n\t" \
  "ds_read_b128 v[56:59], v101 offset:8192\n\t" \
  "s_waitcnt vmcnt(0)\n\t" \
  "v_smfmac_f32_16x16x64_bf16 v[68:71], v[44:47], a[64:71], v104\n\t" \
  "ds_write_b128 v101, v[44:47] offset:3072\n\t" \
  "v_smfmac_f32_16x16x64_bf16 v[72:75], v[44:47], a[72:79], v104\n\t" \
  "v_smfmac_f32_16x16x64_bf16 v[76:79], v[44:47], a[80:87], v104\n\t" \
  "v_cmp_u_f32 vcc, v68, v69\n\t" \
  "v_smfmac_f32_16x16x64_bf16 v[80:83], v[44:47], a[88:95], v104\n\t" \
  "v_cmp_u_f32 s[82:83], v70, v71\n\t" \
  "s_waitcnt lgkmcnt(3)\n\t" \
  "v_smfmac_f32_16x16x64_bf16 v[68:71], v[48:51], a[96:103], v104\n\t" \
  "ds_read_b128 v[60:63], v101 offset:9216\n\t" \
  "ds_read_b128 v[64:67], v101 offset:12288\n\t" \
  "v_smfmac_f32_16x16x64_bf16 v[72:75], v[48:51], a[104:111], v104\n\t" \
  "s_or_b64 vcc, vcc, s[82:83]\n\t" \
  "s_waitcnt lgkmcnt(2)\n\t" \
  "v_smfmac_f32_16x16x64_bf16 v[68:71], v[52:55], a[112:119], v104\n\t" \
  "s_cbranch_scc1 9f\n\t" \
  "6:\n\t" \
  "s_barrier\n\t" \
  "v_smfmac_f32_16x16x64_bf16 v[72:75], v[52:55], a[120:127], v104\n\t" \
  "s_waitcnt lgkmcnt(0)\n\t" \
  "ds_read_b128 v[48:51], v101 offset:13312\n\t" \
  "v_smfmac_f32_16x16x64_bf16 v[68:71], v[56:59], a[192:199], v104\n\t" \
  "ds_read_b128 v[52:55], v101 offset:6144\n\t" \
  "v_smfmac_f32_16x16x64_bf16 v[72:75], v[56:59], a[200:207], v104\n\t" \
  "s_sub_i32 s79, s56, s84\n\t" \
  "s_mul_hi_u32 s79, s79, s46\n\t" \
  "v_smfmac_f32_16x16x64_bf16 v[68:71], v[60:63], a[208:215], v104\n\t" \
  "ds_read_b128 v[56:59], v101 offset:7168\n\t" \
  "v_smfmac_f32_16x16x64_bf16 v[72:75], v[60:63], a[216:223], v104\n\t" \
  "v_lshl_add_u32 v116, s79, 9, v118\n\t" \
  "buffer_store_dword v116, v109, %[rh], s45 offen sc1\n\t" \
  "v_smfmac_f32_16x16x64_bf16 v[68:71], v[64:67], v[152:159], v104\n\t" \
  "ds_read_b128 v[60:63], v101 offset:10240\n\t" \
  "v_smfmac_f32_16x16x64_bf16 v[72:75], v[64:67], v[160:167], v104\n\t" \
  "s_waitcnt lgkmcnt(3)\n\t" \
  "v_smfmac_f32_16x16x64_bf16 v[68:71], v[48:51], v[168:175], v104\n\t" \
  "ds_read_b128 v[64:67], v101 offset:11264\n\t" \
  "ds_read_b128 v[16:19], v103 offset:49152\n\t" \
  "v_smfmac_f32_16x16x64_bf16 v[72:75], v[48:51], v[176:183], v104\n\t" \
  "s_waitcnt lgkmcnt(4)\n\t" \
  "v_smfmac_f32_16x16x64_bf16 v[68:71], v[52:55], v[84:91], v104\n\t" \
  "ds_read_b128 v[48:51], v101 offset:15360\n\t" \
  "ds_read_b128 v[20:23], v103 offset:50176\n\t" \
  "v_smfmac_f32_16x16x64_bf16 v[72:75], v[52:55], v[92:99], v104\n\t" \
  "s_waitcnt lgkmcnt(5)\n\t" \
  "v_smfmac_f32_16x16x64_bf16 v[68:71], v[56:59], a[160:167], v104\n\t" \
  "ds_read_b128 v[52:55], v101 offset:14336\n\t" \
  "ds_read_b128 v[24:27], v103 offset:51200\n\t" \
  "v_smfmac_f32_16x16x64_bf16 v[72:75], v[56:59], a[168:175], v104\n\t" \
  "ds_read_b128 v[28:31], v103 offset:52224\n\t" \
  "s_waitcnt lgkmcnt(7)\n\t" \
  "v_smfmac_f32_16x16x64_bf16 v[68:71], v[60:63], v[0:7], v104\n\t" \
  "ds_read_b128 v[32:35], v101 offset:20480\n\t" \
  "v_smfmac_f32_16x16x64_bf16 v[72:75], v[60:63], v[8:15], v104\n\t" \
  "ds_read_b128 v[36:39], v101 offset:21504\n\t" \
  "s_waitcnt lgkmcnt(8)\n\t" \
  "v_smfmac_f32_16x16x64_bf16 v[68:71], v[64:67], v[120:127], v104\n\t" \
  "ds_read_b128 v[0:3], v102 offset:32768\n\t" \
  "ds_read_b128 v[40:43], v101 offset:22528\n\t" \
  "v_smfmac_f32_16x16x64_bf16 v[72:75], v[64:67], v[128:135], v104\n\t" \
  "ds_read_b128 v[4:7], v102 offset:33792\n\t" \
  "s_waitcnt lgkmcnt(9)\n\t" \
  "v_smfmac_f32_16x16x64_bf16 v[68:71], v[48:51], v[216:223], v104\n\t" \
  "ds_read_b128 v[44:47], v101 offset:23552\n\t" \
  "s_waitcnt lgkmcnt(8)\n\t" \
  "v_smfmac_f32_16x16x64_bf16 v[68:71], v[52:55], v[16:23], v104\n\t" \
  "ds_read_b128 v[8:11], v102 offset:34816\n\t" \
  "v_smfmac_f32_16x16x64_bf16 v[72:75], v[48:51], v[224:231], v104\n\t" \
  "ds_read_b128 v[12:15], v102 offset:35840\n\t" \
  "s_waitcnt lgkmcnt(8)\n\t" \
  "v_smfmac_f32_16x16x64_bf16 v[72:75], v[52:55], v[24:31], v104\n\t" \
  "v_add_f32 v96, v68, v70\n\t" \
  "v_smfmac_f32_16x16x64_bf16 v[76:79], v[56:59], a[176:183], v104\n\t" \
  "v_add_f32 v97, v69, v71\n\t" \
  "v_cvt_pk_bf16_f32 v96, v96, v97\n\t" \
  "v_smfmac_f32_16x16x64_bf16 v[80:83], v[56:59], a[184:191], v104\n\t" \
  "v_add_f32 v98, v72, v74\n\t" \
  "v_add_f32 v99, v73, v75\n\t" \
  "v_smfmac_f32_16x16x64_bf16 v[76:79], v[60:63], a[240:247], v104\n\t" \
  "v_cvt_pk_bf16_f32 v98, v98, v99\n\t" \
  "ds_write2st64_b32 v105, v96, v98 offset1:1\n\t" \
  "v_smfmac_f32_16x16x64_bf16 v[80:83], v[60:63], a[248:255], v104\n\t" \
  "ds_read_b128 v[56:59], v101 offset:4096\n\t" \
  "v_mov_b64 v[68:69], 0\n\t" \
  "v_smfmac_f32_16x16x64_bf16 v[76:79], v[64:67], v[136:143], v104\n\t" \
  "ds_read_b128 v[16:19], v103 offset:0\n\t" \
  "v_mov_b64 v[70:71], 0\n\t" \
  "v_smfmac_f32_16x16x64_bf16 v[80:83], v[64:67], v[144:151], v104\n\t" \
  "ds_read_b128 v[20:23], v103 offset:1024\n\t" \
  "v_mov_b64 v[72:73], 0\n\t" \
  "v_smfmac_f32_16x16x64_bf16 v[76:79], v[48:51], v[232:239], v104\n\t" \
  "s_waitcnt lgkmcnt(3)\n\t" \
  "s_barrier\n\t" \
  "ds_read_b64_tr_b16 v[84:85], v107 offset:0\n\t" \
  "v_smfmac_f32_16x16x64_bf16 v[80:83], v[48:51], v[240:247], v104\n\t" \
  "ds_read_b64_tr_b16 v[86:87], v107 offset:128\n\t" \
  "ds_read_b64_tr_b16 v[88:89], v107 offset:1024\n\t" \
  "v_smfmac_f32_16x16x64_bf16 v[76:79], v[52:55], v[200:207], v104\n\t" \
  "ds_read_b128 v[60:63], v101 offset:13312\n\t" \
  "ds_read_b128 v[24:27], v103 offset:2048\n\t" \
  "ds_read_b64_tr_b16 v[90:91], v107 offset:1152\n\t" \
  "v_smfmac_f32_16x16x64_bf16 v[80:83], v[52:55], v[208:215], v104\n\t" \
  "ds_read_b128 v[64:67], v101 offset:6144\n\t" \
  "v_mov_b64 v[74:75], 0\n\t" \
  "s_waitcnt lgkmcnt(9)\n\t" \
  "v_smfmac_f32_16x16x64_bf16 v[76:79], v[56:59], v[0:7], v104\n\t" \
  "ds_read_b128 v[48:51], v101 offset:8192\n\t" \
  "ds_read_b128 v[28:31], v103 offset:3072\n\t" \
  "v_smfmac_f32_16x16x64_bf16 v[80:83], v[56:59], v[8:15], v104\n\t" \
  "s_waitcnt lgkmcnt(7)\n\t" \
  "v_smfmac_f32_16x16x64_bf16 v[68:71], v[84:87], v[32:39], v104\n\t" \
  "ds_read_b128 v[32:35], v101 offset:24576\n\t" \
  "ds_read_b128 v[36:39], v101 offset:25600\n\t" \
  "s_waitcnt lgkmcnt(5)\n\t" \
  "v_smfmac_f32_16x16x64_bf16 v[68:71], v[88:91], v[40:47], v104\n\t" \
  "ds_read_b128 v[40:43], v101 offset:26624\n\t" \
  "ds_read_b128 v[44:47], v101 offset:27648\n\t" \
  "v_smfmac_f32_16x16x64_bf16 v[76:79], v[60:63], v[184:191], v104\n\t" \
  "ds_read_b128 v[52:55], v101 offset:5120\n\t" \
  "v_smfmac_f32_16x16x64_bf16 v[80:83], v[60:63], v[192:199], v104\n\t" \
  "ds_read_b128 v[0:3], v103 offset:32768\n\t" \
  "v_add_f32 v92, v68, v70\n\t" \
  "s_waitcnt lgkmcnt(8)\n\t" \
  "v_smfmac_f32_16x16x64_bf16 v[76:79], v[64:67], a[144:151], v104\n\t" \
  "ds_read_b128 v[56:59], v101 offset:9216\n\t" \
  "v_add_f32 v94, v69, v71\n\t" \
  "s_waitcnt lgkmcnt(5)\n\t" \
  "v_smfmac_f32_16x16x64_bf16 v[72:75], v[84:87], v[32:39], v104\n\t" \
  "v_mov_b32 v97, v94\n\t" \
  "s_waitcnt lgkmcnt(3)\n\t" \
  "v_smfmac_f32_16x16x64_bf16 v[72:75], v[88:91], v[40:47], v104\n\t" \
  "v_mov_b32_dpp v94, v92 row_ror:8 row_mask:0xf bank_mask:0x3\n\t" \
  "v_smfmac_f32_16x16x64_bf16 v[80:83], v[64:67], a[152:159], v104\n\t" \
  "ds_read_b128 v[60:63], v101 offset:12288\n\t" \
  "ds_read_b128 v[4:7], v103 offset:33792\n\t" \
  "v_mov_b32_dpp v92, v97 row_ror:8 row_mask:0xf bank_mask:0xc\n\t" \
  "v_smfmac_f32_16x16x64_bf16 v[76:79], v[48:51], v[16:23], v104\n\t" \
  "ds_read_b128 v[8:11], v103 offset:34816\n\t" \
  "v_add_f32 v93, v72, v74\n\t" \
  "v_add_f32 v95, v73, v75\n\t" \
  "v_max_f32 v98, v92, v94\n\t" \
  "v_mov_b32 v97, v95\n\t" \
  "v_smfmac_f32_16x16x64_bf16 v[80:83], v[48:51], v[24:31], v104\n\t" \
  "ds_read_b128 v[12:15], v103 offset:35840\n\t" \
  "v_mov_b32_dpp v95, v93 row_ror:8 row_mask:0xf bank_mask:0x3\n\t" \
  "v_mov_b32_dpp v93, v97 row_ror:8 row_mask:0xf bank_mask:0xc\n\t" \
  "v_max3_f32 v98, v98, v93, v95\n\t" \
  "s_waitcnt lgkmcnt(6)\n\t" \
  "v_smfmac_f32_16x16x64_bf16 v[76:79], v[52:55], a[128:135], v104\n\t" \
  "v_max_f32_dpp v98, v98, v98 quad_perm:[1,0,3,2] row_mask:0xf bank_mask:0xf\n\t" \
  "s_nop 1\n\t" \
  "v_max_f32_dpp v98, v98, v98 quad_perm:[2,3,0,1] row_mask:0xf bank_mask:0xf\n\t" \
  "v_smfmac_f32_16x16x64_bf16 v[80:83], v[52:55], a[136:143], v104\n\t" \
  "s_nop 0\n\t" \
  "v_max_f32_dpp v98, v98, v98 row_half_mirror row_mask:0xf bank_mask:0xf\n\t" \
  "v_mul_f32 v98, %[cs], v98\n\t" \
  "v_fma_f32 v92, v92, %[cs], -v98\n\t" \
  "s_waitcnt lgkmcnt(4)\n\t" \
  "v_smfmac_f32_16x16x64_bf16 v[76:79], v[56:59], a[224:231], v104\n\t" \
  "v_fma_f32 v93, v93, %[cs], -v98\n\t" \
  "v_fma_f32 v94, v94, %[cs], -v98\n\t" \
  "v_fma_f32 v95, v95, %[cs], -v98\n\t" \
  "s_waitcnt lgkmcnt(2)\n\t" \
  "v_smfmac_f32_16x16x64_bf16 v[76:79], v[60:63], v[0:7], v104\n\t" \
  "v_exp_f32 v32, v92\n\t" \
  "v_exp_f32 v33, v93\n\t" \
  "v_exp_f32 v34, v94\n\t" \
  "v_smfmac_f32_16x16x64_bf16 v[80:83], v[56:59], a[232:239], v104\n\t" \
  "v_exp_f32 v35, v95\n\t" \
  "v_cvt_pk_bf16_f32 v36, v32, v33\n\t" \
  "v_cvt_pk_bf16_f32 v37, v34, v35\n\t" \
  "s_waitcnt lgkmcnt(0)\n\t" \
  "v_smfmac_f32_16x16x64_bf16 v[80:83], v[60:63], v[8:15], v104\n\t" \
  "buffer_store_dwordx2 v[36:37], v110, %[rh], s47 offen sc1\n\t" \
  "v_add_f32 v20, v76, v78\n\t" \
  "v_add_f32 v21, v77, v79\n\t" \
  "v_cvt_pk_bf16_f32 v20, v20, v21\n\t" \
  "ds_write_b32 v106, v20\n\t" \
  "s_nop 0\n\t" \
  "s_nop 0\n\t" \
  "s_nop 0\n\t" \
  "s_nop 0\n\t" \
  "v_add_f32 v23, v80, v82\n\t" \
  "v_add_f32 v21, v81, v83\n\t" \
  "v_cvt_pk_bf16_f32 v21, v23, v21\n\t" \
  "ds_write_b32 v106, v21 offset:256\n\t" \
  "s_waitcnt lgkmcnt(0)\n\t" \
  "s_barrier\n\t" \
  "ds_read_b64_tr_b16 v[4:5], v108\n\t" \
  "ds_read_b64_tr_b16 v[6:7], v108 offset:64\n\t" \
  "ds_read_b64_tr_b16 v[8:9], v108 offset:512\n\t" \
  "ds_read_b64_tr_b16 v[10:11], v108 offset:576\n\t" \
  "ds_read_b64_tr_b16 v[12:13], v108 offset:1024\n\t" \
  "ds_read_b64_tr_b16 v[14:15], v108 offset:1088\n\t" \
  "ds_read_b64_tr_b16 v[16:17], v108 offset:1536\n\t" \
  "ds_read_b64_tr_b16 v[18:19], v108 offset:1600\n\t" \
  "v_mov_b64 v[0:1], 0\n\t" \
  "v_mov_b64 v[2:3], 0\n\t" \
  "v_add_f32 v29, v32, v33\n\t" \
  "v_add_f32 v29, v29, v34\n\t" \
  "s_waitcnt lgkmcnt(4)\n\t" \
  "v_smfmac_f32_16x16x64_bf16 v[0:3], v[84:87], v[4:11], v104\n\t" \
  "s_waitcnt lgkmcnt(0)\n\t" \
  "v_smfmac_f32_16x16x64_bf16 v[0:3], v[88:91], v[12:19], v104\n\t" \
  "v_add_f32 v29, v29, v35\n\t" \
  "s_mov_b32 s76, 0xaaaaaaaa\n\t" \
  "s_mov_b32 s77, s76\n\t" \
  "s_nop 4\n\t" \
  "v_add_f32 v96, v0, v2\n\t" \
  "v_add_f32_dpp v96, v1, v3 quad_perm:[0,1,2,3] row_mask:0xf bank_mask:0xc\n\t" \
  "v_fma_f32 v96, v96, %[cs], -v98\n\t" \
  "v_exp_f32 v24, v96\n\t" \
  "s_nop 0\n\t" \
  "v_add_f32 v29, v29, v24\n\t" \
  "s_nop 0\n\t" \
  "v_mov_b32_dpp v25, v24 quad_perm:[1,0,3,2] row_mask:0xf bank_mask:0xf\n\t" \
  "v_add_f32_dpp v29, v29, v29 quad_perm:[1,0,3,2] row_mask:0xf bank_mask:0xf\n\t" \
  "v_cndmask_b32_e64 v24, v24, v114, s[76:77]\n\t" \
  "v_cndmask_b32_e64 v25, v25, v115, s[76:77]\n\t" \
  "v_add_f32_dpp v29, v29, v29 quad_perm:[2,3,0,1] row_mask:0xf bank_mask:0xf\n\t" \
  "s_nop 1\n\t" \
  "v_add_f32_dpp v29, v29, v29 row_half_mirror row_mask:0xf bank_mask:0xf\n\t" \
  "v_rcp_f32 v26, v29\n\t" \
  "v_cmpx_gt_f32 vcc, 0x5d5e0b6b, v29\n\t" \
  "v_mul_f32 v24, v24, v26\n\t" \
  "v_mul_f32 v25, v25, v26\n\t" \
  "v_cvt_pk_bf16_f32 v24, v24, v25\n\t" \
  "buffer_store_dword v24, v111, %[rh], s47 offen sc1\n\t" \
  "s_memtime s[76:77]\n\t" \
  "s_mov_b64 exec, -1\n\t" \
  "v_mov_b32 v97, v29\n\t" \
  "s_cmp_eq_u64 vcc, -1\n\t" \
  "s_cselect_b32 s42, 0, 8\n\t" \
  "s_waitcnt lgkmcnt(0)\n\t" \
  "s_mov_b32 s57, s76\n\t" \
  "s_barrier\n\t" \
  "s_add_i32 s41, s56, 1950\n\t" \
  "s_sub_i32 s76, s57, s56\n\t" \
  "s_add_i32 s77, s43, 8\n\t" \
  "s_min_u32 s76, s76, s77\n\t" \
  "s_sub_i32 s43, s43, 4\n\t" \
  "s_max_u32 s43, s43, s76\n\t" \
  "s_add_i32 s76, s45, 0x4000\n\t" \
  "s_and_b32 s76, s76, 0xc000\n\t" \
  "v_mov_b32 v116, -1\n\t" \
  "buffer_store_dword v116, v109, %[rh], s76 offen sc1\n\t" \
  "s_sub_i32 s76, s92, s51\n\t" \
  "s_sub_i32 s77, s93, s53\n\t" \
  "s_cmp_eq_u32 s76, 0\n\t" \
  "s_cbranch_scc1 21f\n\t" \
  "v_cvt_f32_u32 v117, s76\n\t" \
  "v_cvt_f32_u32 v116, s77\n\t" \
  "v_rcp_f32 v117, v117\n\t" \
  "s_mov_b32 s51, s92\n\t" \
  "v_mul_f32 v117, v117, v116\n\t" \
  "v_max_f32 v117, 0x3f028f5c, v117\n\t" \
  "v_min_f32 v117, 0x3ffeb852, v117\n\t" \
  "v_rcp_f32 v116, v117\n\t" \
  "v_mul_f32 v117, 0x4f000000, v117\n\t" \
  "v_mul_f32 v116, 0x47800000, v116\n\t" \
  "v_cvt_u32_f32 v117, v117\n\t" \
  "v_cvt_u32_f32 v116, v116\n\t" \
  "s_mov_b32 s53, s93\n\t" \
  "v_readfirstlane_b32 s46, v117\n\t" \
  "v_readfirstlane_b32 s52, v116\n\t" \
  "21:\n\t" \
  "s_add_i32 s44, s44, 1\n\t" \
  "s_and_b32 s58, s58, 4\n\t" \
  "s_and_b32 s76, s44, 64\n\t" \
  "s_or_b32 s42, s42, s76\n\t" \
  "s_cbranch_scc0 20b\n\t" \
  "s_branch 99f\n\t" \
  "14:\n\t" \
  "s_add_u32 s59, s59, 0x10000\n\t" \
  "s_cmp_lt_u32 s59, 0x100000\n\t" \
  "s_cbranch_scc1 13b\n\t" \
  "s_mov_b64 exec, -1\n\t" \
  "s_mov_b32 s42, 16\n\t" \
  "s_branch 99f\n\t" \
  "8:\n\t" \
  "s_bitcmp1_b32 s58, 1\n\t" \
  "s_cbranch_scc1 4b\n\t" \
  "s_bitcmp1_b32 s58, 2\n\t" \
  "s_cbranch_scc1 5b\n\t" \
  "v_max3_u32 v116, v32, v33, v34\n\t" \
  "v_max3_u32 v117, v35, v36, v37\n\t" \
  "v_max3_u32 v116, v116, v38, v39\n\t" \
  "v_max_u32 v116, v116, v117\n\t" \
  "v_cmp_eq_u32 vcc, -1, v116\n\t" \
  "s_cbranch_vccz 5b\n\t" \
  "s_branch 0f\n\t" \
  "9:\n\t" \
  "s_bitcmp1_b32 s58, 2\n\t" \
  "s_cbranch_scc1 6b\n\t" \
  "v_max3_u32 v116, v40, v41, v42\n\t" \
  "v_max3_u32 v117, v43, v44, v45\n\t" \
  "v_max3_u32 v116, v116, v46, v47\n\t" \
  "v_max_u32 v116, v116, v117\n\t" \
  "v_cmp_eq_u32 vcc, -1, v116\n\t" \
  "s_cbranch_vccz 6b\n\t" \
  "s_or_b32 s58, s58, 2\n\t" \
  "0:\n\t" \
  "s_or_b32 s58, s58, 1\n\t" \
  "11:\n\t" \
  "buffer_load_dwordx4 v[32:35], v100, %[rh], s40 offen sc1\n\t" \
  "buffer_load_dwordx4 v[36:39], v100, %[rh], s76 offen sc1\n\t" \
  "buffer_load_dwordx4 v[40:43], v100, %[rh], s77 offen sc1\n\t" \
  "buffer_load_dwordx4 v[44:47], v100, %[rh], s78 offen sc1\n\t" \
  "s_waitcnt lgkmcnt(0)\n\t" \
  "s_branch 10b\n\t" \
  "17:\n\t" \
  "s_add_u32 s59, s59, 1\n\t" \
  "s_bitcmp1_b32 s58, 2\n\t" \
  "s_cbranch_scc1 3b\n\t" \
  "s_waitcnt vmcnt(0)\n\t" \
  "v_max_u32 v116, v32, v33\n\t" \
  "v_max3_u32 v116, v116, v34, v35\n\t" \
  "v_max3_u32 v116, v116, v36, v37\n\t" \
  "v_max3_u32 v116, v116, v38, v39\n\t" \
  "v_max3_u32 v116, v116, v40, v41\n\t" \
  "v_max3_u32 v116, v116, v42, v43\n\t" \
  "v_max3_u32 v116, v116, v44, v45\n\t" \
  "v_max3_u32 v116, v116, v46, v47\n\t" \
  "v_cmp_eq_u32 vcc, -1, v116\n\t" \
  "s_cbranch_vccz 3b\n\t" \
  "s_bitcmp1_b32 s59, 6\n\t" \
  "s_cbranch_scc1 12f\n\t" \
  "buffer_load_dwordx4 v[32:35], v100, %[rh], s40 offen sc1\n\t" \
  "buffer_load_dwordx4 v[36:39], v100, %[rh], s76 offen sc1\n\t" \
  "buffer_load_dwordx4 v[40:43], v100, %[rh], s77 offen sc1\n\t" \
  "buffer_load_dwordx4 v[44:47], v100, %[rh], s78 offen sc1\n\t" \
  "s_branch 17b\n\t" \
  "12:\n\t" \
  "s_mov_b32 s42, s58\n\t" \
  "s_branch 99f\n\t" \
  "91:\n\t" \
  "s_mov_b32 s58, s42\n\t" \
  "s_mov_b32 s95, 0\n\t" \
  "20:\n\t" \
  "s_and_b32 s76, s44, 7\n\t" \
  "s_lshl_b32 s76, s76, 15\n\t" \
  "s_add_i32 s40, s54, s76\n\t" \
  "s_and_b32 s76, s44, 3\n\t" \
  "s_lshl_b32 s45, s76, 14\n\t" \
  "s_add_i32 s47, s44, 1\n\t" \
  "s_min_u32 s47, s47, 63\n\t" \
  "s_lshr_b32 s76, s47, 1\n\t" \
  "s_lshl_b32 s47, s47, 20\n\t" \
  "s_add_i32 s47, s47, s55\n\t" \
  "s_bitcmp1_b64 %[sel], s76\n\t" \
  "s_cselect_b32 s76, 0x2000, 0\n\t" \
  "s_add_i32 s47, s47, s76\n\t" \
  "s_lshr_b32 s76, s44, 1\n\t" \
  "s_lshl_b32 s94, s44, 20\n\t" \
  "s_bitcmp1_b64 %[sel], s76\n\t" \
  "s_cselect_b32 s76, 0x2000, 0\n\t" \
  "s_add_i32 s94, s94, s76\n\t" \
  "s_add_i32 s49, s44, -1\n\t" \
  "s_and_b32 s48, s49, 2\n\t" \
  "s_lshl_b32 s48, s48, 13\n\t" \
  "s_and_b32 s76, s49, 1\n\t" \
  "s_lshl_b32 s76, s76, 12\n\t" \
  "s_add_i32 s48, s48, s76\n\t" \
  "s_lshl_b32 s50, s53, 8\n\t" \
  "s_mov_b32 s59, 0\n\t" \
  "s_mov_b32 s92, s51\n\t" \
  "s_mov_b32 s93, s53\n\t" \
  "s_xor_b32 s76, s40, 0x400\n\t" \
  "s_xor_b32 s77, s40, 0x800\n\t" \
  "s_xor_b32 s78, s40, 0xc00\n\t" \
  "s_cmp_lg_u32 s58, 0\n\t" \
  "s_cbranch_scc1 11f\n\t" \
  "10:\n\t" \
  "ds_read_b128 v[48:51], v102 offset:32768\n\t" \
  "ds_read_b128 v[52:55], v102 offset:33792\n\t" \
  "ds_read_b128 v[56:59], v102 offset:34816\n\t" \
  "ds_read_b128 v[60:63], v102 offset:35840\n\t" \
  "ds_read_b128 v[16:19], v102 offset:49152\n\t" \
  "ds_read_b128 v[20:23], v102 offset:50176\n\t" \
  "ds_read_b128 v[24:27], v102 offset:51200\n\t" \
  "ds_read_b128 v[28:31], v102 offset:52224\n\t" \
  "ds_read_b128 v[84:87], v103 offset:16384\n\t" \
  "ds_read_b128 v[88:91], v103 offset:17408\n\t" \
  "ds_read_b128 v[92:95], v103 offset:18432\n\t" \
  "ds_read_b128 v[96:99], v103 offset:19456\n\t" \
  "s_waitcnt lgkmcnt(4)\n\t" \
  "ds_read_b128 v[0:3], v103 offset:49152\n\t" \
  "ds_read_b128 v[4:7], v103 offset:50176\n\t" \
  "ds_read_b128 v[8:11], v103 offset:51200\n\t" \
  "ds_read_b128 v[12:15], v103 offset:52224\n\t" \
  "s_bitcmp1_b32 s58, 1\n\t" \
  "s_cselect_b64 s[80:81], -1, 0\n\t" \
  "s_cmp_lg_u32 s58, 0\n\t" \
  "s_cbranch_scc1 17f\n\t" \
  "s_mov_b32 exec_hi, 0\n\t" \
  "s_cmp_eq_u32 s95, 0\n\t" \
  "s_cbranch_scc1 16f\n\t" \
  "s_waitcnt vmcnt(2)\n\t" \
  "v_mov_b32 v116, v118\n\t" \
  "s_branch 15f\n\t" \
  "16:\n\t" \
  "s_memtime vcc\n\t" \
  "s_waitcnt lgkmcnt(0)\n\t" \
  "s_sub_i32 s79, s41, vcc_lo\n\t" \
  "s_cmp_gt_i32 s79, 64\n\t" \
  "s_cbranch_scc1 16b\n\t" \
  "13:\n\t" \
  "buffer_load_dword v116, v112, %[rh], s48 offen sc1\n\t" \
  "s_waitcnt vmcnt(0)\n\t" \
  "15:\n\t" \
  "v_mov_b32 v117, 20000\n\t" \
  "v_cmp_ne_u32_sdwa vcc, v116, s49 src0_sel:BYTE_0 src1_sel:DWORD\n\t" \
  "v_subrev_u32 v116, s50, v116\n\t" \
  "v_ashrrev_i32 v116, 8, v116\n\t" \
  "s_nop 1\n\t" \
  "v_max_i32_dpp v116, v116, v116 quad_perm:[1,0,3,2] row_mask:0xf bank_mask:0xf\n\t" \
  "s_cbranch_vccnz 14f\n\t" \
  "s_nop 0\n\t" \
  "v_max_i32_dpp v116, v116, v116 quad_perm:[2,3,0,1] row_mask:0xf bank_mask:0xf\n\t" \
  "s_nop 1\n\t" \
  "v_max_i32_dpp v116, v116, v116 row_half_mirror row_mask:0xf bank_mask:0xf\n\t" \
  "s_nop 1\n\t" \
  "v_max_i32_dpp v116, v116, v116 row_mirror row_mask:0xf bank_mask:0xf\n\t" \
  "v_med3_i32 v116, v116, 0, v117\n\t" \
  "v_mul_u32_u24 v116, s52, v116\n\t" \
  "v_lshrrev_b32 v116, 16, v116\n\t" \
  "s_mov_b64 exec, -1\n\t" \
  "v_readfirstlane_b32 s84, v116\n\t" \
  "v_readlane_b32 s85, v116, 16\n\t" \
  "s_max_u32 s84, s84, s85\n\t" \
  "s_add_i32 s84, s84, s51\n\t" \
  "1:\n\t" \
  "s_memtime vcc\n\t" \
  "s_waitcnt lgkmcnt(0)\n\t" \
  "s_sub_i32 s79, s84, vcc_lo\n\t" \
  "s_cmp_gt_i32 s79, 128\n\t" \
  "s_cbranch_scc1 1b\n\t" \
  ".p2align 4\n\t" \
  "2:\n\t" \
  "s_sub_i32 s79, s79, 33\n\t" \
  "s_cmp_gt_i32 s79, 56\n\t" \
  "s_cbranch_scc1 2b\n\t" \
  "buffer_load_dwordx4 v[32:35], v100, %[rh], s40 offen sc1\n\t" \
  "s_nop 12\n\t" \
  "buffer_load_dwordx4 v[36:39], v100, %[rh], s76 offen sc1\n\t" \
  "s_nop 12\n\t" \
  "buffer_load_dwordx4 v[40:43], v100, %[rh], s77 offen sc1\n\t" \
  "s_nop 12\n\t" \
  "buffer_load_dwordx4 v[44:47], v100, %[rh], s78 offen sc1\n\t" \
  "s_memtime vcc\n\t" \
  "s_memrealtime s[82:83]\n\t" \
  "s_nop 4\n\t" \
  "s_memrealtime s[84:85]\n\t" \
  "s_nop 4\n\t" \
  "s_memrealtime s[56:57]\n\t" \
  "s_nop 4\n\t" \
  "s_memrealtime s[86:87]\n\t" \
  "s_nop 4\n\t" \
  "s_memrealtime s[88:89]\n\t" \
  "s_nop 4\n\t" \
  "s_memrealtime s[90:91]\n\t" \
  "s_mov_b32 s92, 0\n\t" \
  "s_waitcnt lgkmcnt(0)\n\t" \
  "s_cmp_eq_u32 s82, s84\n\t" \
  "s_addc_u32 s92, s92, 0\n\t" \
  "s_cmp_eq_u32 s82, s56\n\t" \
  "s_addc_u32 s92, s92, 0\n\t" \
  "s_cmp_eq_u32 s82, s86\n\t" \
  "s_addc_u32 s92, s92, 0\n\t" \
  "s_cmp_eq_u32 s82, s88\n\t" \
  "s_addc_u32 s92, s92, 0\n\t" \
  "s_cmp_eq_u32 s82, s90\n\t" \
  "s_addc_u32 s92, s92, 0\n\t" \
  "s_mul_i32 s92, s92, 24\n\t" \
  "s_add_i32 s92, s92, vcc_lo\n\t" \
  "s_lshl4_add_u32 s93, s82, 64\n\t" \
  "3:\n\t" \
  "v_mov_b64 v[68:69], 0\n\t" \
  "v_mov_b64 v[70:71], 0\n\t" \
  "v_mov_b64 v[72:73], 0\n\t" \
  "v_mov_b64 v[74:75], 0\n\t" \
  "v_mov_b64 v[76:77], 0\n\t" \
  "v_mov_b64 v[78:79], 0\n\t" \
  "v_mov_b64 v[80:81], 0\n\t" \
  "v_mov_b64 v[82:83], 0\n\t" \
  "s_waitcnt lgkmcnt(0)\n\t" \
  "s_waitcnt vmcnt(3)\n\t" \
  "v_smfmac_f32_16x16x64_bf16 v[68:71], v[32:35], a[96:103], v104\n\t" \
  "ds_write_b128 v101, v[32:35] offset:4096\n\t" \
  "v_smfmac_f32_16x16x64_bf16 v[72:75], v[32:35], a[104:111], v104\n\t" \
  "v_smfmac_f32_16x16x64_bf16 v[76:79], v[32:35], v[48:55], v104\n\t" \
  "v_smfmac_f32_16x16x64_bf16 v[80:83], v[32:35], v[56:63], v104\n\t" \
  "s_waitcnt vmcnt(2)\n\t" \
  "v_smfmac_f32_16x16x64_bf16 v[68:71], v[36:39], a[112:119], v104\n\t" \
  "ds_write_b128 v101, v[36:39] offset:5120\n\t" \
  "v_smfmac_f32_16x16x64_bf16 v[72:75], v[36:39], a[120:127], v104\n\t" \
  "v_smfmac_f32_16x16x64_bf16 v[76:79], v[36:39], a[128:135], v104\n\t" \
  "v_cmp_u_f32 vcc, v68, v69\n\t" \
  "v_smfmac_f32_16x16x64_bf16 v[80:83], v[36:39], a[136:143], v104\n\t" \
  "v_cmp_u_f32 s[82:83], v70, v71\n\t" \
  "s_waitcnt vmcnt(1)\n\t" \
  "v_smfmac_f32_16x16x64_bf16 v[68:71], v[40:43], v[16:23], v104\n\t" \
  "ds_write_b128 v101, v[40:43] offset:6144\n\t" \
  "v_smfmac_f32_16x16x64_bf16 v[72:75], v[40:43], v[24:31], v104\n\t" \
  "s_or_b64 vcc, vcc, s[82:83]\n\t" \
  "s_or_b64 vcc, vcc, s[80:81]\n\t" \
  "s_waitcnt lgkmcnt(1)\n\t" \
  "v_smfmac_f32_16x16x64_bf16 v[76:79], v[40:43], a[144:151], v104\n\t" \
  "s_cbranch_scc1 8f\n\t" \
  "5:\n\t" \
  "s_barrier\n\t" \
  "4:\n\t" \
  "s_memtime s[56:57]\n\t" \
  "ds_read_b128 v[48:51], v101 offset:8192\n\t" \
  "v_smfmac_f32_16x16x64_bf16 v[80:83], v[40:43], a[152:159], v104\n\t" \
  "ds_read_b128 v[52:55], v101 offset:9216\n\t" \
  "ds_read_b128 v[56:59], v101 offset:12288\n\t" \
  "s_waitcnt vmcnt(0)\n\t" \
  "v_smfmac_f32_16x16x64_bf16 v[68:71], v[44:47], a[160:167], v104\n\t" \
  "ds_write_b128 v101, v[44:47] offset:7168\n\t" \
  "v_smfmac_f32_16x16x64_bf16 v[72:75], v[44:47], a[168:175], v104\n\t" \
  "v_smfmac_f32_16x16x64_bf16 v[76:79], v[44:47], a[176:183], v104\n\t" \
  "v_cmp_u_f32 vcc, v68, v69\n\t" \
  "v_smfmac_f32_16x16x64_bf16 v[80:83], v[44:47], a[184:191], v104\n\t" \
  "v_cmp_u_f32 s[82:83], v70, v71\n\t" \
  "s_waitcnt lgkmcnt(3)\n\t" \
  "v_smfmac_f32_16x16x64_bf16 v[68:71], v[48:51], a[192:199], v104\n\t" \
  "ds_read_b128 v[60:63], v101 offset:13312\n\t" \
  "ds_read_b128 v[64:67], v101 offset:0\n\t" \
  "v_smfmac_f32_16x16x64_bf16 v[72:75], v[48:51], a[200:207], v104\n\t" \
  "s_or_b64 vcc, vcc, s[82:83]\n\t" \
  "s_waitcnt lgkmcnt(2)\n\t" \
  "v_smfmac_f32_16x16x64_bf16 v[68:71], v[52:55], a[208:215], v104\n\t" \
  "s_cbranch_scc1 9f\n\t" \
  "6:\n\t" \
  "s_barrier\n\t" \
  "v_smfmac_f32_16x16x64_bf16 v[72:75], v[52:55], a[216:223], v104\n\t" \
  "ds_read_b128 v[48:51], v101 offset:1024\n\t" \
  "v_smfmac_f32_16x16x64_bf16 v[68:71], v[56:59], v[152:159], v104\n\t" \
  "ds_read_b128 v[52:55], v101 offset:10240\n\t" \
  "v_smfmac_f32_16x16x64_bf16 v[72:75], v[56:59], v[160:167], v104\n\t" \
  "s_waitcnt lgkmcnt(3)\n\t" \
  "v_smfmac_f32_16x16x64_bf16 v[68:71], v[60:63], v[168:175], v104\n\t" \
  "ds_read_b128 v[56:59], v101 offset:11264\n\t" \
  "global_load_dword v119, v109, %[kc] sc1\n\t" \
  "v_smfmac_f32_16x16x64_bf16 v[72:75], v[60:63], v[176:183], v104\n\t" \
  "s_waitcnt lgkmcnt(3)\n\t" \
  "v_smfmac_f32_16x16x64_bf16 v[68:71], v[64:67], a[0:7], v104\n\t" \
  "ds_read_b128 v[60:63], v101 offset:14336\n\t" \
  "v_smfmac_f32_16x16x64_bf16 v[72:75], v[64:67], a[8:15], v104\n\t" \
  "s_waitcnt lgkmcnt(3)\n\t" \
  "v_smfmac_f32_16x16x64_bf16 v[68:71], v[48:51], a[16:23], v104\n\t" \
  "ds_read_b128 v[64:67], v101 offset:15360\n\t" \
  "ds_read_b128 v[16:19], v102 offset:16384\n\t" \
  "v_smfmac_f32_16x16x64_bf16 v[72:75], v[48:51], a[24:31], v104\n\t" \
  "s_waitcnt lgkmcnt(4)\n\t" \
  "v_smfmac_f32_16x16x64_bf16 v[68:71], v[52:55], v[84:91], v104\n\t" \
  "ds_read_b128 v[48:51], v101 offset:3072\n\t" \
  "ds_read_b128 v[20:23], v102 offset:17408\n\t" \
  "v_smfmac_f32_16x16x64_bf16 v[72:75], v[52:55], v[92:99], v104\n\t" \
  "s_waitcnt lgkmcnt(5)\n\t" \
  "v_smfmac_f32_16x16x64_bf16 v[68:71], v[56:59], v[120:127], v104\n\t" \
  "ds_read_b128 v[52:55], v101 offset:2048\n\t" \
  "ds_read_b128 v[24:27], v102 offset:18432\n\t" \
  "v_smfmac_f32_16x16x64_bf16 v[72:75], v[56:59], v[128:135], v104\n\t" \
  "ds_read_b128 v[28:31], v102 offset:19456\n\t" \
  "buffer_load_dwordx4 v[84:87], v107, %[ru], s94 offen offset:0 sc1\n\t" \
  "s_waitcnt lgkmcnt(7)\n\t" \
  "v_smfmac_f32_16x16x64_bf16 v[68:71], v[60:63], v[0:7], v104\n\t" \
  "v_smfmac_f32_16x16x64_bf16 v[72:75], v[60:63], v[8:15], v104\n\t" \
  "buffer_load_dwordx4 v[88:91], v107, %[ru], s94 offen offset:1024 sc1\n\t" \
  "s_waitcnt lgkmcnt(6)\n\t" \
  "v_smfmac_f32_16x16x64_bf16 v[68:71], v[64:67], v[216:223], v104\n\t" \
  "ds_read_b128 v[0:3], v103 offset:0\n\t" \
  "v_smfmac_f32_16x16x64_bf16 v[72:75], v[64:67], v[224:231], v104\n\t" \
  "ds_read_b128 v[4:7], v103 offset:1024\n\t" \
  "buffer_load_dwordx4 v[92:95], v107, %[ru], s94 offen offset:2048 sc1\n\t" \
  "s_waitcnt lgkmcnt(6)\n\t" \
  "v_smfmac_f32_16x16x64_bf16 v[68:71], v[48:51], a[64:71], v104\n\t" \
  "s_waitcnt lgkmcnt(4)\n\t" \
  "v_smfmac_f32_16x16x64_bf16 v[68:71], v[52:55], v[16:23], v104\n\t" \
  "ds_read_b128 v[8:11], v103 offset:2048\n\t" \
  "buffer_load_dwordx4 v[96:99], v107, %[ru], s94 offen offset:3072 sc1\n\t" \
  "v_smfmac_f32_16x16x64_bf16 v[72:75], v[48:51], a[72:79], v104\n\t" \
  "ds_read_b128 v[12:15], v103 offset:3072\n\t" \
  "s_waitcnt lgkmcnt(4)\n\t" \
  "v_smfmac_f32_16x16x64_bf16 v[72:75], v[52:55], v[24:31], v104\n\t" \
  "v_add_f32 v44, v68, v70\n\t" \
  "v_smfmac_f32_16x16x64_bf16 v[76:79], v[56:59], v[136:143], v104\n\t" \
  "v_add_f32 v45, v69, v71\n\t" \
  "v_cvt_pk_bf16_f32 v44, v44, v45\n\t" \
  "v_smfmac_f32_16x16x64_bf16 v[80:83], v[56:59], v[144:151], v104\n\t" \
  "v_add_f32 v46, v72, v74\n\t" \
  "v_add_f32 v47, v73, v75\n\t" \
  "v_smfmac_f32_16x16x64_bf16 v[76:79], v[60:63], v[200:207], v104\n\t" \
  "v_cvt_pk_bf16_f32 v46, v46, v47\n\t" \
  "ds_write2st64_b32 v105, v44, v46 offset1:1\n\t" \
  "v_smfmac_f32_16x16x64_bf16 v[80:83], v[60:63], v[208:215], v104\n\t" \
  "ds_read_b128 v[56:59], v101 offset:8192\n\t" \
  "v_smfmac_f32_16x16x64_bf16 v[76:79], v[64:67], v[232:239], v104\n\t" \
  "ds_read_b128 v[16:19], v103 offset:32768\n\t" \
  "v_smfmac_f32_16x16x64_bf16 v[80:83], v[64:67], v[240:247], v104\n\t" \
  "ds_read_b128 v[20:23], v103 offset:33792\n\t" \
  "v_smfmac_f32_16x16x64_bf16 v[76:79], v[48:51], a[80:87], v104\n\t" \
  "s_waitcnt lgkmcnt(3)\n\t" \
  "s_barrier\n\t" \
  "v_smfmac_f32_16x16x64_bf16 v[80:83], v[48:51], a[88:95], v104\n\t" \
  "v_smfmac_f32_16x16x64_bf16 v[76:79], v[52:55], a[48:55], v104\n\t" \
  "ds_read_b128 v[60:63], v101 offset:1024\n\t" \
  "ds_read_b128 v[24:27], v103 offset:34816\n\t" \
  "v_smfmac_f32_16x16x64_bf16 v[80:83], v[52:55], a[56:63], v104\n\t" \
  "ds_read_b128 v[64:67], v101 offset:10240\n\t" \
  "s_waitcnt lgkmcnt(5)\n\t" \
  "v_smfmac_f32_16x16x64_bf16 v[76:79], v[56:59], v[0:7], v104\n\t" \
  "ds_read_b128 v[48:51], v101 offset:12288\n\t" \
  "ds_read_b128 v[28:31], v103 offset:35840\n\t" \
  "v_smfmac_f32_16x16x64_bf16 v[80:83], v[56:59], v[8:15], v104\n\t" \
  "buffer_load_dwordx4 v[32:35], v100, %[ru], s47 offen offset:0 sc1\n\t" \
  "s_waitcnt lgkmcnt(4)\n\t" \
  "v_smfmac_f32_16x16x64_bf16 v[76:79], v[60:63], a[32:39], v104\n\t" \
  "ds_read_b128 v[52:55], v101 offset:9216\n\t" \
  "v_smfmac_f32_16x16x64_bf16 v[80:83], v[60:63], a[40:47], v104\n\t" \
  "ds_read_b128 v[0:3], v102 offset:0\n\t" \
  "s_waitcnt lgkmcnt(4)\n\t" \
  "v_smfmac_f32_16x16x64_bf16 v[76:79], v[64:67], a[240:247], v104\n\t" \
  "ds_read_b128 v[56:59], v101 offset:13312\n\t" \
  "buffer_load_dwordx4 v[36:39], v100, %[ru], s47 offen offset:1024 sc1\n\t" \
  "v_smfmac_f32_16x16x64_bf16 v[80:83], v[64:67], a[248:255], v104\n\t" \
  "ds_read_b128 v[60:63], v101 offset:0\n\t" \
  "ds_read_b128 v[4:7], v102 offset:1024\n\t" \
  "s_waitcnt lgkmcnt(6)\n\t" \
  "v_smfmac_f32_16x16x64_bf16 v[76:79], v[48:51], v[16:23], v104\n\t" \
  "ds_read_b128 v[8:11], v102 offset:2048\n\t" \
  "s_waitcnt lgkmcnt(6)\n\t" \
  "v_smfmac_f32_16x16x64_bf16 v[80:83], v[48:51], v[24:31], v104\n\t" \
  "ds_read_b128 v[12:15], v102 offset:3072\n\t" \
  "buffer_load_dwordx4 v[40:43], v100, %[ru], s47 offen offset:2048 sc1\n\t" \
  "s_waitcnt lgkmcnt(6)\n\t" \
  "v_smfmac_f32_16x16x64_bf16 v[76:79], v[52:55], a[224:231], v104\n\t" \
  "v_smfmac_f32_16x16x64_bf16 v[80:83], v[52:55], a[232:239], v104\n\t" \
  "s_waitcnt lgkmcnt(4)\n\t" \
  "v_smfmac_f32_16x16x64_bf16 v[76:79], v[56:59], v[184:191], v104\n\t" \
  "s_waitcnt lgkmcnt(2)\n\t" \
  "v_smfmac_f32_16x16x64_bf16 v[76:79], v[60:63], v[0:7], v104\n\t" \
  "v_smfmac_f32_16x16x64_bf16 v[80:83], v[56:59], v[192:199], v104\n\t" \
  "s_waitcnt lgkmcnt(0)\n\t" \
  "v_smfmac_f32_16x16x64_bf16 v[80:83], v[60:63], v[8:15], v104\n\t" \
  "v_add_f32 v20, v76, v78\n\t" \
  "v_add_f32 v21, v77, v79\n\t" \
  "v_cvt_pk_bf16_f32 v20, v20, v21\n\t" \
  "ds_write_b32 v106, v20\n\t" \
  "s_nop 3\n\t" \
  "v_add_f32 v23, v80, v82\n\t" \
  "v_add_f32 v21, v81, v83\n\t" \
  "v_cvt_pk_bf16_f32 v21, v23, v21\n\t" \
  "ds_write_b32 v106, v21 offset:256\n\t" \
  "s_waitcnt lgkmcnt(0)\n\t" \
  "s_barrier\n\t" \
  "v_bfe_i32 v64, v114, 0, 1\n\t" \
  "v_and_b32 v64, 0xff800000, v64\n\t" \
  "v_bfe_i32 v65, v114, 1, 1\n\t" \
  "v_and_b32 v65, 0xff800000, v65\n\t" \
  "v_bfe_i32 v66, v114, 2, 1\n\t" \
  "v_and_b32 v66, 0xff800000, v66\n\t" \
  "v_bfe_i32 v67, v114, 3, 1\n\t" \
  "v_and_b32 v67, 0xff800000, v67\n\t" \
  "v_bfe_i32 v68, v114, 4, 1\n\t" \
  "v_and_b32 v68, 0xff800000, v68\n\t" \
  "v_bfe_i32 v69, v114, 5, 1\n\t" \
  "v_and_b32 v69, 0xff800000, v69\n\t" \
  "v_bfe_i32 v70, v114, 6, 1\n\t" \
  "v_and_b32 v70, 0xff800000, v70\n\t" \
  "v_bfe_i32 v71, v114, 7, 1\n\t" \
  "v_and_b32 v71, 0xff800000, v71\n\t" \
  "s_lshl_b32 s94, s45, 4\n\t" \
  "s_and_b32 s96, s44, 2\n\t" \
  "s_lshl_b32 s96, s96, 13\n\t" \
  "s_and_b32 s76, s44, 1\n\t" \
  "s_lshl_b32 s76, s76, 12\n\t" \
  "s_add_i32 s96, s96, s76\n\t" \
  "s_mov_b32 s95, 1\n\t" \
  "s_sleep 1\n\t" \
  "ds_read_b128 v[0:3], v101 offset:20480\n\t" \
  "ds_read_b128 v[4:7], v101 offset:21504\n\t" \
  "ds_read_b128 v[8:11], v101 offset:22528\n\t" \
  "ds_read_b128 v[12:15], v101 offset:23552\n\t" \
  "ds_read_b128 v[16:19], v101 offset:24576\n\t" \
  "ds_read_b128 v[20:23], v101 offset:25600\n\t" \
  "ds_read_b128 v[24:27], v101 offset:26624\n\t" \
  "ds_read_b128 v[28:31], v101 offset:27648\n\t" \
  "ds_read_b64_tr_b16 v[48:49], v108 offset:0\n\t" \
  "ds_read_b64_tr_b16 v[50:51], v108 offset:64\n\t" \
  "ds_read_b64_tr_b16 v[52:53], v108 offset:512\n\t" \
  "ds_read_b64_tr_b16 v[54:55], v108 offset:576\n\t" \
  "ds_read_b64_tr_b16 v[56:57], v108 offset:1024\n\t" \
  "ds_read_b64_tr_b16 v[58:59], v108 offset:1088\n\t" \
  "ds_read_b64_tr_b16 v[60:61], v108 offset:1536\n\t" \
  "ds_read_b64_tr_b16 v[62:63], v108 offset:1600\n\t" \
  "s_waitcnt vmcnt(3) lgkmcnt(8)\n\t" \
  "v_mfma_f32_16x16x32_bf16 v[64:67], v[0:3], v[84:87], v[64:67]\n\t" \
  "v_mfma_f32_16x16x32_bf16 v[68:71], v[16:19], v[84:87], v[68:71]\n\t" \
  "v_mfma_f32_16x16x32_bf16 v[64:67], v[4:7], v[88:91], v[64:67]\n\t" \
  "v_mfma_f32_16x16x32_bf16 v[68:71], v[20:23], v[88:91], v[68:71]\n\t" \
  "v_mfma_f32_16x16x32_bf16 v[64:67], v[8:11], v[92:95], v[64:67]\n\t" \
  "v_mfma_f32_16x16x32_bf16 v[68:71], v[24:27], v[92:95], v[68:71]\n\t" \
  "v_mfma_f32_16x16x32_bf16 v[64:67], v[12:15], v[96:99], v[64:67]\n\t" \
  "v_mfma_f32_16x16x32_bf16 v[68:71], v[28:31], v[96:99], v[68:71]\n\t" \
  "s_waitcnt lgkmcnt(0)\n\t" \
  "v_mfma_f32_16x16x32_bf16 v[72:75], v[48:51], v[84:87], 0\n\t" \
  "v_mfma_f32_16x16x32_bf16 v[72:75], v[52:55], v[88:91], v[72:75]\n\t" \
  "v_mfma_f32_16x16x32_bf16 v[72:75], v[56:59], v[92:95], v[72:75]\n\t" \
  "v_mfma_f32_16x16x32_bf16 v[72:75], v[60:63], v[96:99], v[72:75]\n\t" \
  "s_barrier\n\t" \
  "v_max3_f32 v76, v64, v65, v66\n\t" \
  "v_max3_f32 v77, v67, v68, v69\n\t" \
  "v_max3_f32 v76, v76, v70, v71\n\t" \
  "s_nop 4\n\t" \
  "v_max3_f32 v77, v77, v72, v73\n\t" \
  "v_max3_f32 v76, v76, v74, v75\n\t" \
  "v_max_f32 v76, v76, v77\n\t" \
  "v_mov_b32 v77, v76\n\t" \
  "s_nop 1\n\t" \
  "v_permlane16_swap_b32 v76, v77\n\t" \
  "v_max_f32 v76, v76, v77\n\t" \
  "v_mov_b32 v77, v76\n\t" \
  "s_nop 1\n\t" \
  "v_permlane32_swap_b32 v76, v77\n\t" \
  "v_max_f32 v76, v76, v77\n\t" \
  "v_mul_f32 v76, %[cs], v76\n\t" \
  "v_fma_f32 v64, v64, %[cs], -v76\n\t" \
  "v_fma_f32 v65, v65, %[cs], -v76\n\t" \
  "v_fma_f32 v66, v66, %[cs], -v76\n\t" \
  "v_fma_f32 v67, v67, %[cs], -v76\n\t" \
  "v_fma_f32 v68, v68, %[cs], -v76\n\t" \
  "v_fma_f32 v69, v69, %[cs], -v76\n\t" \
  "v_fma_f32 v70, v70, %[cs], -v76\n\t" \
  "v_fma_f32 v71, v71, %[cs], -v76\n\t" \
  "v_fma_f32 v72, v72, %[cs], -v76\n\t" \
  "v_fma_f32 v73, v73, %[cs], -v76\n\t" \
  "v_fma_f32 v74, v74, %[cs], -v76\n\t" \
  "v_fma_f32 v75, v75, %[cs], -v76\n\t" \
  "v_exp_f32 v64, v64\n\t" \
  "v_exp_f32 v65, v65\n\t" \
  "v_exp_f32 v66, v66\n\t" \
  "v_exp_f32 v67, v67\n\t" \
  "v_exp_f32 v68, v68\n\t" \
  "v_exp_f32 v69, v69\n\t" \
  "v_exp_f32 v70, v70\n\t" \
  "v_exp_f32 v71, v71\n\t" \
  "v_exp_f32 v72, v72\n\t" \
  "v_exp_f32 v73, v73\n\t" \
  "v_exp_f32 v74, v74\n\t" \
  "v_exp_f32 v75, v75\n\t" \
  "v_add_f32 v76, v72, v73\n\t" \
  "v_add_f32 v77, v74, v75\n\t" \
  "v_add_f32 v78, v64, v65\n\t" \
  "v_add_f32 v79, v66, v67\n\t" \
  "v_add_f32 v76, v76, v77\n\t" \
  "v_add_f32 v78, v78, v68\n\t" \
  "v_add_f32 v79, v79, v69\n\t" \
  "v_add_f32 v78, v78, v70\n\t" \
  "v_add_f32 v79, v79, v71\n\t" \
  "v_fma_f32 v76, v76, 0.5, v78\n\t" \
  "v_add_f32 v76, v76, v79\n\t" \
  "v_mov_b32 v77, v76\n\t" \
  "s_nop 1\n\t" \
  "v_permlane16_swap_b32 v76, v77\n\t" \
  "v_add_f32 v76, v76, v77\n\t" \
  "v_mov_b32 v77, v76\n\t" \
  "s_nop 1\n\t" \
  "v_permlane32_swap_b32 v76, v77\n\t" \
  "v_add_f32 v76, v76, v77\n\t" \
  "v_rcp_f32 v76, v76\n\t" \
  "s_nop 0\n\t" \
  "v_mul_f32 v64, v64, v76\n\t" \
  "v_mul_f32 v65, v65, v76\n\t" \
  "v_mul_f32 v66, v66, v76\n\t" \
  "v_mul_f32 v67, v67, v76\n\t" \
  "v_mul_f32 v68, v68, v76\n\t" \
  "v_mul_f32 v69, v69, v76\n\t" \
  "v_mul_f32 v70, v70, v76\n\t" \
  "v_mul_f32 v71, v71, v76\n\t" \
  "v_mul_f32 v72, v72, v76\n\t" \
  "v_mul_f32 v73, v73, v76\n\t" \
  "v_mul_f32 v74, v74, v76\n\t" \
  "v_mul_f32 v75, v75, v76\n\t" \
  "v_cvt_pk_bf16_f32 v0, v64, v68\n\t" \
  "v_cvt_pk_bf16_f32 v1, v65, v69\n\t" \
  "v_cvt_pk_bf16_f32 v2, v66, v70\n\t" \
  "v_cvt_pk_bf16_f32 v3, v67, v71\n\t" \
  "v_cvt_pk_bf16_f32 v4, v72, v73\n\t" \
  "v_cvt_pk_bf16_f32 v5, v74, v75\n\t" \
  "v_permlane32_swap_b32 v0, v2\n\t" \
  "v_permlane32_swap_b32 v1, v3\n\t" \
  "v_mov_b64 v[6:7], v[4:5]\n\t" \
  "s_mov_b32 exec_hi, 0\n\t" \
  "buffer_load_dword v118, v112, %[rh], s96 offen sc1\n\t" \
  "s_mov_b64 exec, -1\n\t" \
  "buffer_store_dwordx4 v[0:3], v110, %[rh], s94 offen sc1\n\t" \
  "v_permlane16_swap_b32 v4, v6\n\t" \
  "v_permlane16_swap_b32 v5, v7\n\t" \
  "buffer_store_dwordx4 v[4:7], v111, %[rh], s94 offen sc1\n\t" \
  "s_waitcnt vmcnt(3)\n\t" \
  "ds_write_b128 v101, v[32:35] offset:20480\n\t" \
  "ds_write_b128 v101, v[36:39] offset:21504\n\t" \
  "ds_write_b128 v101, v[40:43] offset:22528\n\t" \
  "s_add_i32 s77, s44, 2\n\t" \
  "s_min_u32 s77, s77, 63\n\t" \
  "v_cmp_gt_i32 vcc, s77, v119\n\t" \
  "s_cmp_lg_u64 vcc, 0\n\t" \
  "s_cselect_b32 s42, 32, 0\n\t" \
  "s_cmp_eq_u32 s44, 0\n\t" \
  "s_cselect_b32 s42, 32, s42\n\t" \
  "s_add_i32 s41, s56, 1950\n\t" \
  "s_sub_i32 s76, s92, s51\n\t" \
  "s_sub_i32 s77, s93, s53\n\t" \
  "s_cmp_eq_u32 s76, 0\n\t" \
  "s_cbranch_scc1 21f\n\t" \
  "v_cvt_f32_u32 v117, s76\n\t" \
  "v_cvt_f32_u32 v116, s77\n\t" \
  "v_rcp_f32 v117, v117\n\t" \
  "s_mov_b32 s51, s92\n\t" \
  "v_mul_f32 v117, v117, v116\n\t" \
  "v_max_f32 v117, 0x3f028f5c, v117\n\t" \
  "v_min_f32 v117, 0x3ffeb852, v117\n\t" \
  "v_rcp_f32 v116, v117\n\t" \
  "v_mul_f32 v117, 0x4f000000, v117\n\t" \
  "v_mul_f32 v116, 0x47800000, v116\n\t" \
  "v_cvt_u32_f32 v117, v117\n\t" \
  "v_cvt_u32_f32 v116, v116\n\t" \
  "s_mov_b32 s53, s93\n\t" \
  "v_readfirstlane_b32 s46, v117\n\t" \
  "v_readfirstlane_b32 s52, v116\n\t" \
  "21:\n\t" \
  "s_add_i32 s44, s44, 1\n\t" \
  "s_and_b32 s58, s58, 4\n\t" \
  "s_and_b32 s76, s44, 64\n\t" \
  "s_or_b32 s42, s42, s76\n\t" \
  "s_cbranch_scc0 20b\n\t" \
  "s_branch 99f\n\t" \
  "14:\n\t" \
  "s_add_u32 s59, s59, 0x10000\n\t" \
  "s_cmp_lt_u32 s59, 0x100000\n\t" \
  "s_cbranch_scc1 13b\n\t" \
  "s_mov_b64 exec, -1\n\t" \
  "s_mov_b32 s42, 16\n\t" \
  "s_branch 99f\n\t" \
  "8:\n\t" \
  "s_bitcmp1_b32 s58, 1\n\t" \
  "s_cbranch_scc1 4b\n\t" \
  "s_bitcmp1_b32 s58, 2\n\t" \
  "s_cbranch_scc1 5b\n\t" \
  "v_max3_u32 v116, v32, v33, v34\n\t" \
  "v_max3_u32 v117, v35, v36, v37\n\t" \
  "v_max3_u32 v116, v116, v38, v39\n\t" \
  "v_max_u32 v116, v116, v117\n\t" \
  "v_cmp_eq_u32 vcc, -1, v116\n\t" \
  "s_cbranch_vccz 5b\n\t" \
  "s_branch 0f\n\t" \
  "9:\n\t" \
  "s_bitcmp1_b32 s58, 2\n\t" \
  "s_cbranch_scc1 6b\n\t" \
  "v_max3_u32 v116, v40, v41, v42\n\t" \
  "v_max3_u32 v117, v43, v44, v45\n\t" \
  "v_max3_u32 v116, v116, v46, v47\n\t" \
  "v_max_u32 v116, v116, v117\n\t" \
  "v_cmp_eq_u32 vcc, -1, v116\n\t" \
  "s_cbranch_vccz 6b\n\t" \
  "s_or_b32 s58, s58, 2\n\t" \
  "0:\n\t" \
  "s_or_b32 s58, s58, 1\n\t" \
  "11:\n\t" \
  "buffer_load_dwordx4 v[32:35], v100, %[rh], s40 offen sc1\n\t" \
  "buffer_load_dwordx4 v[36:39], v100, %[rh], s76 offen sc1\n\t" \
  "buffer_load_dwordx4 v[40:43], v100, %[rh], s77 offen sc1\n\t" \
  "buffer_load_dwordx4 v[44:47], v100, %[rh], s78 offen sc1\n\t" \
  "s_waitcnt lgkmcnt(0)\n\t" \
  "s_branch 10b\n\t" \
  "17:\n\t" \
  "s_add_u32 s59, s59, 1\n\t" \
  "s_bitcmp1_b32 s58, 2\n\t" \
  "s_cbranch_scc1 3b\n\t" \
  "s_waitcnt vmcnt(0)\n\t" \
  "v_max_u32 v116, v32, v33\n\t" \
  "v_max3_u32 v116, v116, v34, v35\n\t" \
  "v_max3_u32 v116, v116, v36, v37\n\t" \
  "v_max3_u32 v116, v116, v38, v39\n\t" \
  "v_max3_u32 v116, v116, v40, v41\n\t" \
  "v_max3_u32 v116, v116, v42, v43\n\t" \
  "v_max3_u32 v116, v116, v44, v45\n\t" \
  "v_max3_u32 v116, v116, v46, v47\n\t" \
  "v_cmp_eq_u32 vcc, -1, v116\n\t" \
  "s_cbranch_vccz 3b\n\t" \
  "s_bitcmp1_b32 s59, 6\n\t" \
  "s_cbranch_scc1 12f\n\t" \
  "buffer_load_dwordx4 v[32:35], v100, %[rh], s40 offen sc1\n\t" \
  "buffer_load_dwordx4 v[36:39], v100, %[rh], s76 offen sc1\n\t" \
  "buffer_load_dwordx4 v[40:43], v100, %[rh], s77 offen sc1\n\t" \
  "buffer_load_dwordx4 v[44:47], v100, %[rh], s78 offen sc1\n\t" \
  "s_branch 17b\n\t" \
  "12:\n\t" \
  "s_mov_b32 s42, s58\n\t" \
  "s_branch 99f\n\t" \
  "92:\n\t" \
  "s_mov_b32 s58, s42\n\t" \
  "s_mov_b32 s95, 0\n\t" \
  "20:\n\t" \
  "s_and_b32 s76, s44, 7\n\t" \
  "s_lshl_b32 s76, s76, 15\n\t" \
  "s_add_i32 s40, s54, s76\n\t" \
  "s_and_b32 s76, s44, 3\n\t" \
  "s_lshl_b32 s45, s76, 14\n\t" \
  "s_add_i32 s47, s44, 1\n\t" \
  "s_min_u32 s47, s47, 63\n\t" \
  "s_lshr_b32 s76, s47, 1\n\t" \
  "s_lshl_b32 s47, s47, 20\n\t" \
  "s_add_i32 s47, s47, s55\n\t" \
  "s_bitcmp1_b64 %[sel], s76\n\t" \
  "s_cselect_b32 s76, 0x2000, 0\n\t" \
  "s_add_i32 s47, s47, s76\n\t" \
  "s_lshr_b32 s76, s44, 1\n\t" \
  "s_lshl_b32 s94, s44, 20\n\t" \
  "s_bitcmp1_b64 %[sel], s76\n\t" \
  "s_cselect_b32 s76, 0x2000, 0\n\t" \
  "s_add_i32 s94, s94, s76\n\t" \
  "s_add_i32 s49, s44, -1\n\t" \
  "s_and_b32 s48, s49, 2\n\t" \
  "s_lshl_b32 s48, s48, 13\n\t" \
  "s_and_b32 s76, s49, 1\n\t" \
  "s_lshl_b32 s76, s76, 12\n\t" \
  "s_add_i32 s48, s48, s76\n\t" \
  "s_lshl_b32 s50, s53, 8\n\t" \
  "s_mov_b32 s59, 0\n\t" \
  "s_mov_b32 s92, s51\n\t" \
  "s_mov_b32 s93, s53\n\t" \
  "s_xor_b32 s76, s40, 0x400\n\t" \
  "s_xor_b32 s77, s40, 0x800\n\t" \
  "s_xor_b32 s78, s40, 0xc00\n\t" \
  "s_cmp_lg_u32 s58, 0\n\t" \
  "s_cbranch_scc1 11f\n\t" \
  "10:\n\t" \
  "ds_read_b128 v[48:51], v103 offset:0\n\t" \
  "ds_read_b128 v[52:55], v103 offset:1024\n\t" \
  "ds_read_b128 v[56:59], v103 offset:2048\n\t" \
  "ds_read_b128 v[60:63], v103 offset:3072\n\t" \
  "ds_read_b128 v[16:19], v103 offset:16384\n\t" \
  "ds_read_b128 v[20:23], v103 offset:17408\n\t" \
  "ds_read_b128 v[24:27], v103 offset:18432\n\t" \
  "ds_read_b128 v[28:31], v103 offset:19456\n\t" \
  "ds_read_b128 v[84:87], v103 offset:49152\n\t" \
  "ds_read_b128 v[88:91], v103 offset:50176\n\t" \
  "ds_read_b128 v[92:95], v103 offset:51200\n\t" \
  "ds_read_b128 v[96:99], v103 offset:52224\n\t" \
  "s_waitcnt lgkmcnt(4)\n\t" \
  "ds_read_b128 v[0:3], v102 offset:16384\n\t" \
  "ds_read_b128 v[4:7], v102 offset:17408\n\t" \
  "ds_read_b128 v[8:11], v102 offset:18432\n\t" \
  "ds_read_b128 v[12:15], v102 offset:19456\n\t" \
  "s_bitcmp1_b32 s58, 1\n\t" \
  "s_cselect_b64 s[80:81], -1, 0\n\t" \
  "s_cmp_lg_u32 s58, 0\n\t" \
  "s_cbranch_scc1 17f\n\t" \
  "s_mov_b32 exec_hi, 0\n\t" \
  "s_cmp_eq_u32 s95, 0\n\t" \
  "s_cbranch_scc1 16f\n\t" \
  "s_waitcnt vmcnt(2)\n\t" \
  "v_mov_b32 v116, v118\n\t" \
  "s_branch 15f\n\t" \
  "16:\n\t" \
  "s_memtime vcc\n\t" \
  "s_waitcnt lgkmcnt(0)\n\t" \
  "s_sub_i32 s79, s41, vcc_lo\n\t" \
  "s_cmp_gt_i32 s79, 64\n\t" \
  "s_cbranch_scc1 16b\n\t" \
  "13:\n\t" \
  "buffer_load_dword v116, v112, %[rh], s48 offen sc1\n\t" \
  "s_waitcnt vmcnt(0)\n\t" \
  "15:\n\t" \
  "v_mov_b32 v117, 20000\n\t" \
  "v_cmp_ne_u32_sdwa vcc, v116, s49 src0_sel:BYTE_0 src1_sel:DWORD\n\t" \
  "v_subrev_u32 v116, s50, v116\n\t" \
  "v_ashrrev_i32 v116, 8, v116\n\t" \
  "s_nop 1\n\t" \
  "v_max_i32_dpp v116, v116, v116 quad_perm:[1,0,3,2] row_mask:0xf bank_mask:0xf\n\t" \
  "s_cbranch_vccnz 14f\n\t" \
  "s_nop 0\n\t" \
  "v_max_i32_dpp v116, v116, v116 quad_perm:[2,3,0,1] row_mask:0xf bank_mask:0xf\n\t" \
  "s_nop 1\n\t" \
  "v_max_i32_dpp v116, v116, v116 row_half_mirror row_mask:0xf bank_mask:0xf\n\t" \
  "s_nop 1\n\t" \
  "v_max_i32_dpp v116, v116, v116 row_mirror row_mask:0xf bank_mask:0xf\n\t" \
  "v_med3_i32 v116, v116, 0, v117\n\t" \
  "v_mul_u32_u24 v116, s52, v116\n\t" \
  "v_lshrrev_b32 v116, 16, v116\n\t" \
  "s_mov_b64 exec, -1\n\t" \
  "v_readfirstlane_b32 s84, v116\n\t" \
  "v_readlane_b32 s85, v116, 16\n\t" \
  "s_max_u32 s84, s84, s85\n\t" \
  "s_add_i32 s84, s84, s51\n\t" \
  "1:\n\t" \
  "s_memtime vcc\n\t" \
  "s_waitcnt lgkmcnt(0)\n\t" \
  "s_sub_i32 s79, s84, vcc_lo\n\t" \
  "s_cmp_gt_i32 s79, 128\n\t" \
  "s_cbranch_scc1 1b\n\t" \
  ".p2align 4\n\t" \
  "2:\n\t" \
  "s_sub_i32 s79, s79, 33\n\t" \
  "s_cmp_gt_i32 s79, 56\n\t" \
  "s_cbranch_scc1 2b\n\t" \
  "buffer_load_dwordx4 v[32:35], v100, %[rh], s40 offen sc1\n\t" \
  "s_nop 12\n\t" \
  "buffer_load_dwordx4 v[36:39], v100, %[rh], s76 offen sc1\n\t" \
  "s_nop 12\n\t" \
  "buffer_load_dwordx4 v[40:43], v100, %[rh], s77 offen sc1\n\t" \
  "s_nop 12\n\t" \
  "buffer_load_dwordx4 v[44:47], v100, %[rh], s78 offen sc1\n\t" \
  "s_memtime vcc\n\t" \
  "s_memrealtime s[82:83]\n\t" \
  "s_nop 4\n\t" \
  "s_memrealtime s[84:85]\n\t" \
  "s_nop 4\n\t" \
  "s_memrealtime s[56:57]\n\t" \
  "s_nop 4\n\t" \
  "s_memrealtime s[86:87]\n\t" \
  "s_nop 4\n\t" \
  "s_memrealtime s[88:89]\n\t" \
  "s_nop 4\n\t" \
  "s_memrealtime s[90:91]\n\t" \
  "s_mov_b32 s92, 0\n\t" \
  "s_waitcnt lgkmcnt(0)\n\t" \
  "s_cmp_eq_u32 s82, s84\n\t" \
  "s_addc_u32 s92, s92, 0\n\t" \
  "s_cmp_eq_u32 s82, s56\n\t" \
  "s_addc_u32 s92, s92, 0\n\t" \
  "s_cmp_eq_u32 s82, s86\n\t" \
  "s_addc_u32 s92, s92, 0\n\t" \
  "s_cmp_eq_u32 s82, s88\n\t" \
  "s_addc_u32 s92, s92, 0\n\t" \
  "s_cmp_eq_u32 s82, s90\n\t" \
  "s_addc_u32 s92, s92, 0\n\t" \
  "s_mul_i32 s92, s92, 24\n\t" \
  "s_add_i32 s92, s92, vcc_lo\n\t" \
  "s_lshl4_add_u32 s93, s82, 64\n\t" \
  "3:\n\t" \
  "v_mov_b64 v[68:69], 0\n\t" \
  "v_mov_b64 v[70:71], 0\n\t" \
  "v_mov_b64 v[72:73], 0\n\t" \
  "v_mov_b64 v[74:75], 0\n\t" \
  "v_mov_b64 v[76:77], 0\n\t" \
  "v_mov_b64 v[78:79], 0\n\t" \
  "v_mov_b64 v[80:81], 0\n\t" \
  "v_mov_b64 v[82:83], 0\n\t" \
  "s_waitcnt lgkmcnt(0)\n\t" \
  "s_waitcnt vmcnt(3)\n\t" \
  "v_smfmac_f32_16x16x64_bf16 v[68:71], v[32:35], a[192:199], v104\n\t" \
  "ds_write_b128 v101, v[32:35] offset:8192\n\t" \
  "v_smfmac_f32_16x16x64_bf16 v[72:75], v[32:35], a[200:207], v104\n\t" \
  "v_smfmac_f32_16x16x64_bf16 v[76:79], v[32:35], v[48:55], v104\n\t" \
  "v_smfmac_f32_16x16x64_bf16 v[80:83], v[32:35], v[56:63], v104\n\t" \
  "s_waitcnt vmcnt(2)\n\t" \
  "v_smfmac_f32_16x16x64_bf16 v[68:71], v[36:39], a[208:215], v104\n\t" \
  "ds_write_b128 v101, v[36:39] offset:9216\n\t" \
  "v_smfmac_f32_16x16x64_bf16 v[72:75], v[36:39], a[216:223], v104\n\t" \
  "v_smfmac_f32_16x16x64_bf16 v[76:79], v[36:39], a[224:231], v104\n\t" \
  "v_cmp_u_f32 vcc, v68, v69\n\t" \
  "v_smfmac_f32_16x16x64_bf16 v[80:83], v[36:39], a[232:239], v104\n\t" \
  "v_cmp_u_f32 s[82:83], v70, v71\n\t" \
  "s_waitcnt vmcnt(1)\n\t" \
  "v_smfmac_f32_16x16x64_bf16 v[68:71], v[40:43], v[16:23], v104\n\t" \
  "ds_write_b128 v101, v[40:43] offset:10240\n\t" \
  "v_smfmac_f32_16x16x64_bf16 v[72:75], v[40:43], v[24:31], v104\n\t" \
  "s_or_b64 vcc, vcc, s[82:83]\n\t" \
  "s_or_b64 vcc, vcc, s[80:81]\n\t" \
  "s_waitcnt lgkmcnt(1)\n\t" \
  "v_smfmac_f32_16x16x64_bf16 v[76:79], v[40:43], a[240:247], v104\n\t" \
  "s_cbranch_scc1 8f\n\t" \
  "5:\n\t" \
  "s_barrier\n\t" \
  "4:\n\t" \
  "s_memtime s[56:57]\n\t" \
  "ds_read_b128 v[48:51], v101 offset:12288\n\t" \
  "v_smfmac_f32_16x16x64_bf16 v[80:83], v[40:43], a[248:255], v104\n\t" \
  "ds_read_b128 v[52:55], v101 offset:13312\n\t" \
  "ds_read_b128 v[56:59], v101 offset:0\n\t" \
  "s_waitcnt vmcnt(0)\n\t" \
  "v_smfmac_f32_16x16x64_bf16 v[68:71], v[44:47], v[120:127], v104\n\t" \
  "v_smfmac_f32_16x16x64_bf16 v[72:75], v[44:47], v[128:135], v104\n\t" \
  "ds_write_b128 v101, v[44:47] offset:11264\n\t" \
  "v_smfmac_f32_16x16x64_bf16 v[76:79], v[44:47], v[136:143], v104\n\t" \
  "v_cmp_u_f32 vcc, v68, v69\n\t" \
  "v_smfmac_f32_16x16x64_bf16 v[80:83], v[44:47], v[144:151], v104\n\t" \
  "v_cmp_u_f32 s[82:83], v70, v71\n\t" \
  "s_waitcnt lgkmcnt(3)\n\t" \
  "v_smfmac_f32_16x16x64_bf16 v[68:71], v[48:51], v[152:159], v104\n\t" \
  "ds_read_b128 v[60:63], v101 offset:1024\n\t" \
  "ds_read_b128 v[64:67], v101 offset:4096\n\t" \
  "v_smfmac_f32_16x16x64_bf16 v[72:75], v[48:51], v[160:167], v104\n\t" \
  "s_or_b64 vcc, vcc, s[82:83]\n\t" \
  "s_waitcnt lgkmcnt(2)\n\t" \
  "v_smfmac_f32_16x16x64_bf16 v[68:71], v[52:55], v[168:175], v104\n\t" \
  "s_cbranch_scc1 9f\n\t" \
  "6:\n\t" \
  "s_barrier\n\t" \
  "v_smfmac_f32_16x16x64_bf16 v[72:75], v[52:55], v[176:183], v104\n\t" \
  "ds_read_b128 v[48:51], v101 offset:5120\n\t" \
  "v_smfmac_f32_16x16x64_bf16 v[68:71], v[56:59], a[0:7], v104\n\t" \
  "ds_read_b128 v[52:55], v101 offset:14336\n\t" \
  "v_smfmac_f32_16x16x64_bf16 v[72:75], v[56:59], a[8:15], v104\n\t" \
  "s_waitcnt lgkmcnt(3)\n\t" \
  "v_smfmac_f32_16x16x64_bf16 v[68:71], v[60:63], a[16:23], v104\n\t" \
  "ds_read_b128 v[56:59], v101 offset:15360\n\t" \
  "v_smfmac_f32_16x16x64_bf16 v[72:75], v[60:63], a[24:31], v104\n\t" \
  "s_waitcnt lgkmcnt(3)\n\t" \
  "v_smfmac_f32_16x16x64_bf16 v[68:71], v[64:67], a[96:103], v104\n\t" \
  "ds_read_b128 v[60:63], v101 offset:2048\n\t" \
  "global_load_dword v119, v109, %[kc] sc1\n\t" \
  "v_smfmac_f32_16x16x64_bf16 v[72:75], v[64:67], a[104:111], v104\n\t" \
  "s_waitcnt lgkmcnt(3)\n\t" \
  "v_smfmac_f32_16x16x64_bf16 v[68:71], v[48:51], a[112:119], v104\n\t" \
  "ds_read_b128 v[64:67], v101 offset:3072\n\t" \
  "ds_read_b128 v[16:19], v102 offset:49152\n\t" \
  "v_smfmac_f32_16x16x64_bf16 v[72:75], v[48:51], a[120:127], v104\n\t" \
  "s_waitcnt lgkmcnt(4)\n\t" \
  "v_smfmac_f32_16x16x64_bf16 v[68:71], v[52:55], v[84:91], v104\n\t" \
  "ds_read_b128 v[48:51], v101 offset:7168\n\t" \
  "ds_read_b128 v[20:23], v102 offset:50176\n\t" \
  "v_smfmac_f32_16x16x64_bf16 v[72:75], v[52:55], v[92:99], v104\n\t" \
  "s_waitcnt lgkmcnt(5)\n\t" \
  "v_smfmac_f32_16x16x64_bf16 v[68:71], v[56:59], v[216:223], v104\n\t" \
  "ds_read_b128 v[52:55], v101 offset:6144\n\t" \
  "ds_read_b128 v[24:27], v102 offset:51200\n\t" \
  "v_smfmac_f32_16x16x64_bf16 v[72:75], v[56:59], v[224:231], v104\n\t" \
  "ds_read_b128 v[28:31], v102 offset:52224\n\t" \
  "s_waitcnt lgkmcnt(7)\n\t" \
  "v_smfmac_f32_16x16x64_bf16 v[68:71], v[60:63], v[0:7], v104\n\t" \
  "buffer_load_dwordx4 v[84:87], v107, %[ru], s94 offen offset:0 sc1\n\t" \
  "v_smfmac_f32_16x16x64_bf16 v[72:75], v[60:63], v[8:15], v104\n\t" \
  "s_waitcnt lgkmcnt(6)\n\t" \
  "v_smfmac_f32_16x16x64_bf16 v[68:71], v[64:67], a[64:71], v104\n\t" \
  "ds_read_b128 v[0:3], v103 offset:32768\n\t" \
  "buffer_load_dwordx4 v[88:91], v107, %[ru], s94 offen offset:1024 sc1\n\t" \
  "v_smfmac_f32_16x16x64_bf16 v[72:75], v[64:67], a[72:79], v104\n\t" \
  "ds_read_b128 v[4:7], v103 offset:33792\n\t" \
  "s_waitcnt lgkmcnt(6)\n\t" \
  "v_smfmac_f32_16x16x64_bf16 v[68:71], v[48:51], a[160:167], v104\n\t" \
  "buffer_load_dwordx4 v[92:95], v107, %[ru], s94 offen offset:2048 sc1\n\t" \
  "s_waitcnt lgkmcnt(4)\n\t" \
  "v_smfmac_f32_16x16x64_bf16 v[68:71], v[52:55], v[16:23], v104\n\t" \
  "ds_read_b128 v[8:11], v103 offset:34816\n\t" \
  "v_smfmac_f32_16x16x64_bf16 v[72:75], v[48:51], a[168:175], v104\n\t" \
  "ds_read_b128 v[12:15], v103 offset:35840\n\t" \
  "buffer_load_dwordx4 v[96:99], v107, %[ru], s94 offen offset:3072 sc1\n\t" \
  "s_waitcnt lgkmcnt(4)\n\t" \
  "v_smfmac_f32_16x16x64_bf16 v[72:75], v[52:55], v[24:31], v104\n\t" \
  "v_add_f32 v44, v68, v70\n\t" \
  "v_smfmac_f32_16x16x64_bf16 v[76:79], v[56:59], v[232:239], v104\n\t" \
  "v_add_f32 v45, v69, v71\n\t" \
  "v_cvt_pk_bf16_f32 v44, v44, v45\n\t" \
  "v_smfmac_f32_16x16x64_bf16 v[80:83], v[56:59], v[240:247], v104\n\t" \
  "v_add_f32 v46, v72, v74\n\t" \
  "v_add_f32 v47, v73, v75\n\t" \
  "v_smfmac_f32_16x16x64_bf16 v[76:79], v[60:63], a[48:55], v104\n\t" \
  "v_cvt_pk_bf16_f32 v46, v46, v47\n\t" \
  "ds_write2st64_b32 v105, v44, v46 offset1:1\n\t" \
  "v_smfmac_f32_16x16x64_bf16 v[80:83], v[60:63], a[56:63], v104\n\t" \
  "ds_read_b128 v[56:59], v101 offset:12288\n\t" \
  "v_smfmac_f32_16x16x64_bf16 v[76:79], v[64:67], a[80:87], v104\n\t" \
  "ds_read_b128 v[16:19], v102 offset:0\n\t" \
  "v_smfmac_f32_16x16x64_bf16 v[80:83], v[64:67], a[88:95], v104\n\t" \
  "ds_read_b128 v[20:23], v102 offset:1024\n\t" \
  "v_smfmac_f32_16x16x64_bf16 v[76:79], v[48:51], a[176:183], v104\n\t" \
  "s_waitcnt lgkmcnt(3)\n\t" \
  "s_barrier\n\t" \
  "v_smfmac_f32_16x16x64_bf16 v[80:83], v[48:51], a[184:191], v104\n\t" \
  "v_smfmac_f32_16x16x64_bf16 v[76:79], v[52:55], a[144:151], v104\n\t" \
  "ds_read_b128 v[60:63], v101 offset:5120\n\t" \
  "ds_read_b128 v[24:27], v102 offset:2048\n\t" \
  "v_smfmac_f32_16x16x64_bf16 v[80:83], v[52:55], a[152:159], v104\n\t" \
  "ds_read_b128 v[64:67], v101 offset:14336\n\t" \
  "s_waitcnt lgkmcnt(5)\n\t" \
  "v_smfmac_f32_16x16x64_bf16 v[76:79], v[56:59], v[0:7], v104\n\t" \
  "ds_read_b128 v[48:51], v101 offset:0\n\t" \
  "ds_read_b128 v[28:31], v102 offset:3072\n\t" \
  "v_smfmac_f32_16x16x64_bf16 v[80:83], v[56:59], v[8:15], v104\n\t" \
  "s_waitcnt lgkmcnt(4)\n\t" \
  "v_smfmac_f32_16x16x64_bf16 v[76:79], v[60:63], a[128:135], v104\n\t" \
  "ds_read_b128 v[52:55], v101 offset:13312\n\t" \
  "buffer_load_dwordx4 v[32:35], v100, %[ru], s47 offen offset:0 sc1\n\t" \
  "v_smfmac_f32_16x16x64_bf16 v[80:83], v[60:63], a[136:143], v104\n\t" \
  "ds_read_b128 v[0:3], v102 offset:32768\n\t" \
  "s_waitcnt lgkmcnt(4)\n\t" \
  "v_smfmac_f32_16x16x64_bf16 v[76:79], v[64:67], v[200:207], v104\n\t" \
  "ds_read_b128 v[56:59], v101 offset:1024\n\t" \
  "v_smfmac_f32_16x16x64_bf16 v[80:83], v[64:67], v[208:215], v104\n\t" \
  "ds_read_b128 v[60:63], v101 offset:4096\n\t" \
  "ds_read_b128 v[4:7], v102 offset:33792\n\t" \
  "buffer_load_dwordx4 v[36:39], v100, %[ru], s47 offen offset:1024 sc1\n\t" \
  "s_waitcnt lgkmcnt(6)\n\t" \
  "v_smfmac_f32_16x16x64_bf16 v[76:79], v[48:51], v[16:23], v104\n\t" \
  "ds_read_b128 v[8:11], v102 offset:34816\n\t" \
  "s_waitcnt lgkmcnt(6)\n\t" \
  "v_smfmac_f32_16x16x64_bf16 v[80:83], v[48:51], v[24:31], v104\n\t" \
  "ds_read_b128 v[12:15], v102 offset:35840\n\t" \
  "s_waitcnt lgkmcnt(6)\n\t" \
  "v_smfmac_f32_16x16x64_bf16 v[76:79], v[52:55], v[184:191], v104\n\t" \
  "buffer_load_dwordx4 v[40:43], v100, %[ru], s47 offen offset:2048 sc1\n\t" \
  "v_smfmac_f32_16x16x64_bf16 v[80:83], v[52:55], v[192:199], v104\n\t" \
  "s_waitcnt lgkmcnt(4)\n\t" \
  "v_smfmac_f32_16x16x64_bf16 v[76:79], v[56:59], a[32:39], v104\n\t" \
  "s_waitcnt lgkmcnt(2)\n\t" \
  "v_smfmac_f32_16x16x64_bf16 v[76:79], v[60:63], v[0:7], v104\n\t" \
  "v_smfmac_f32_16x16x64_bf16 v[80:83], v[56:59], a[40:47], v104\n\t" \
  "s_waitcnt lgkmcnt(0)\n\t" \
  "v_smfmac_f32_16x16x64_bf16 v[80:83], v[60:63], v[8:15], v104\n\t" \
  "v_add_f32 v20, v76, v78\n\t" \
  "v_add_f32 v21, v77, v79\n\t" \
  "v_cvt_pk_bf16_f32 v20, v20, v21\n\t" \
  "ds_write_b32 v106, v20\n\t" \
  "s_nop 3\n\t" \
  "v_add_f32 v23, v80, v82\n\t" \
  "v_add_f32 v21, v81, v83\n\t" \
  "v_cvt_pk_bf16_f32 v21, v23, v21\n\t" \
  "ds_write_b32 v106, v21 offset:256\n\t" \
  "s_waitcnt lgkmcnt(0)\n\t" \
  "s_barrier\n\t" \
  "v_bfe_i32 v64, v114, 0, 1\n\t" \
  "v_and_b32 v64, 0xff800000, v64\n\t" \
  "v_bfe_i32 v65, v114, 1, 1\n\t" \
  "v_and_b32 v65, 0xff800000, v65\n\t" \
  "v_bfe_i32 v66, v114, 2, 1\n\t" \
  "v_and_b32 v66, 0xff800000, v66\n\t" \
  "v_bfe_i32 v67, v114, 3, 1\n\t" \
  "v_and_b32 v67, 0xff800000, v67\n\t" \
  "v_bfe_i32 v68, v114, 4, 1\n\t" \
  "v_and_b32 v68, 0xff800000, v68\n\t" \
  "v_bfe_i32 v69, v114, 5, 1\n\t" \
  "v_and_b32 v69, 0xff800000, v69\n\t" \
  "v_bfe_i32 v70, v114, 6, 1\n\t" \
  "v_and_b32 v70, 0xff800000, v70\n\t" \
  "v_bfe_i32 v71, v114, 7, 1\n\t" \
  "v_and_b32 v71, 0xff800000, v71\n\t" \
  "s_lshl_b32 s94, s45, 4\n\t" \
  "s_and_b32 s96, s44, 2\n\t" \
  "s_lshl_b32 s96, s96, 13\n\t" \
  "s_and_b32 s76, s44, 1\n\t" \
  "s_lshl_b32 s76, s76, 12\n\t" \
  "s_add_i32 s96, s96, s76\n\t" \
  "s_mov_b32 s95, 1\n\t" \
  "s_sleep 1\n\t" \
  "ds_read_b128 v[0:3], v101 offset:20480\n\t" \
  "ds_read_b128 v[4:7], v101 offset:21504\n\t" \
  "ds_read_b128 v[8:11], v101 offset:22528\n\t" \
  "ds_read_b128 v[12:15], v101 offset:23552\n\t" \
  "ds_read_b128 v[16:19], v101 offset:24576\n\t" \
  "ds_read_b128 v[20:23], v101 offset:25600\n\t" \
  "ds_read_b128 v[24:27], v101 offset:26624\n\t" \
  "ds_read_b128 v[28:31], v101 offset:27648\n\t" \
  "ds_read_b64_tr_b16 v[48:49], v108 offset:0\n\t" \
  "ds_read_b64_tr_b16 v[50:51], v108 offset:64\n\t" \
  "ds_read_b64_tr_b16 v[52:53], v108 offset:512\n\t" \
  "ds_read_b64_tr_b16 v[54:55], v108 offset:576\n\t" \
  "ds_read_b64_tr_b16 v[56:57], v108 offset:1024\n\t" \
  "ds_read_b64_tr_b16 v[58:59], v108 offset:1088\n\t" \
  "ds_read_b64_tr_b16 v[60:61], v108 offset:1536\n\t" \
  "ds_read_b64_tr_b16 v[62:63], v108 offset:1600\n\t" \
  "s_waitcnt vmcnt(3) lgkmcnt(8)\n\t" \
  "v_mfma_f32_16x16x32_bf16 v[64:67], v[0:3], v[84:87], v[64:67]\n\t" \
  "v_mfma_f32_16x16x32_bf16 v[68:71], v[16:19], v[84:87], v[68:71]\n\t" \
  "v_mfma_f32_16x16x32_bf16 v[64:67], v[4:7], v[88:91], v[64:67]\n\t" \
  "v_mfma_f32_16x16x32_bf16 v[68:71], v[20:23], v[88:91], v[68:71]\n\t" \
  "v_mfma_f32_16x16x32_bf16 v[64:67], v[8:11], v[92:95], v[64:67]\n\t" \
  "v_mfma_f32_16x16x32_bf16 v[68:71], v[24:27], v[92:95], v[68:71]\n\t" \
  "v_mfma_f32_16x16x32_bf16 v[64:67], v[12:15], v[96:99], v[64:67]\n\t" \
  "v_mfma_f32_16x16x32_bf16 v[68:71], v[28:31], v[96:99], v[68:71]\n\t" \
  "s_waitcnt lgkmcnt(0)\n\t" \
  "v_mfma_f32_16x16x32_bf16 v[72:75], v[48:51], v[84:87], 0\n\t" \
  "v_mfma_f32_16x16x32_bf16 v[72:75], v[52:55], v[88:91], v[72:75]\n\t" \
  "v_mfma_f32_16x16x32_bf16 v[72:75], v[56:59], v[92:95], v[72:75]\n\t" \
  "v_mfma_f32_16x16x32_bf16 v[72:75], v[60:63], v[96:99], v[72:75]\n\t" \
  "s_barrier\n\t" \
  "v_max3_f32 v76, v64, v65, v66\n\t" \
  "v_max3_f32 v77, v67, v68, v69\n\t" \
  "v_max3_f32 v76, v76, v70, v71\n\t" \
  "s_nop 4\n\t" \
  "v_max3_f32 v77, v77, v72, v73\n\t" \
  "v_max3_f32 v76, v76, v74, v75\n\t" \
  "v_max_f32 v76, v76, v77\n\t" \
  "v_mov_b32 v77, v76\n\t" \
  "s_nop 1\n\t" \
  "v_permlane16_swap_b32 v76, v77\n\t" \
  "v_max_f32 v76, v76, v77\n\t" \
  "v_mov_b32 v77, v76\n\t" \
  "s_nop 1\n\t" \
  "v_permlane32_swap_b32 v76, v77\n\t" \
  "v_max_f32 v76, v76, v77\n\t" \
  "v_mul_f32 v76, %[cs], v76\n\t" \
  "v_fma_f32 v64, v64, %[cs], -v76\n\t" \
  "v_fma_f32 v65, v65, %[cs], -v76\n\t" \
  "v_fma_f32 v66, v66, %[cs], -v76\n\t" \
  "v_fma_f32 v67, v67, %[cs], -v76\n\t" \
  "v_fma_f32 v68, v68, %[cs], -v76\n\t" \
  "v_fma_f32 v69, v69, %[cs], -v76\n\t" \
  "v_fma_f32 v70, v70, %[cs], -v76\n\t" \
  "v_fma_f32 v71, v71, %[cs], -v76\n\t" \
  "v_fma_f32 v72, v72, %[cs], -v76\n\t" \
  "v_fma_f32 v73, v73, %[cs], -v76\n\t" \
  "v_fma_f32 v74, v74, %[cs], -v76\n\t" \
  "v_fma_f32 v75, v75, %[cs], -v76\n\t" \
  "v_exp_f32 v64, v64\n\t" \
  "v_exp_f32 v65, v65\n\t" \
  "v_exp_f32 v66, v66\n\t" \
  "v_exp_f32 v67, v67\n\t" \
  "v_exp_f32 v68, v68\n\t" \
  "v_exp_f32 v69, v69\n\t" \
  "v_exp_f32 v70, v70\n\t" \
  "v_exp_f32 v71, v71\n\t" \
  "v_exp_f32 v72, v72\n\t" \
  "v_exp_f32 v73, v73\n\t" \
  "v_exp_f32 v74, v74\n\t" \
  "v_exp_f32 v75, v75\n\t" \
  "v_add_f32 v76, v72, v73\n\t" \
  "v_add_f32 v77, v74, v75\n\t" \
  "v_add_f32 v78, v64, v65\n\t" \
  "v_add_f32 v79, v66, v67\n\t" \
  "v_add_f32 v76, v76, v77\n\t" \
  "v_add_f32 v78, v78, v68\n\t" \
  "v_add_f32 v79, v79, v69\n\t" \
  "v_add_f32 v78, v78, v70\n\t" \
  "v_add_f32 v79, v79, v71\n\t" \
  "v_fma_f32 v76, v76, 0.5, v78\n\t" \
  "v_add_f32 v76, v76, v79\n\t" \
  "v_mov_b32 v77, v76\n\t" \
  "s_nop 1\n\t" \
  "v_permlane16_swap_b32 v76, v77\n\t" \
  "v_add_f32 v76, v76, v77\n\t" \
  "v_mov_b32 v77, v76\n\t" \
  "s_nop 1\n\t" \
  "v_permlane32_swap_b32 v76, v77\n\t" \
  "v_add_f32 v76, v76, v77\n\t" \
  "v_rcp_f32 v76, v76\n\t" \
  "s_nop 0\n\t" \
  "v_mul_f32 v64, v64, v76\n\t" \
  "v_mul_f32 v65, v65, v76\n\t" \
  "v_mul_f32 v66, v66, v76\n\t" \
  "v_mul_f32 v67, v67, v76\n\t" \
  "v_mul_f32 v68, v68, v76\n\t" \
  "v_mul_f32 v69, v69, v76\n\t" \
  "v_mul_f32 v70, v70, v76\n\t" \
  "v_mul_f32 v71, v71, v76\n\t" \
  "v_mul_f32 v72, v72, v76\n\t" \
  "v_mul_f32 v73, v73, v76\n\t" \
  "v_mul_f32 v74, v74, v76\n\t" \
  "v_mul_f32 v75, v75, v76\n\t" \
  "v_cvt_pk_bf16_f32 v0, v64, v68\n\t" \
  "v_cvt_pk_bf16_f32 v1, v65, v69\n\t" \
  "v_cvt_pk_bf16_f32 v2, v66, v70\n\t" \
  "v_cvt_pk_bf16_f32 v3, v67, v71\n\t" \
  "v_cvt_pk_bf16_f32 v4, v72, v73\n\t" \
  "v_cvt_pk_bf16_f32 v5, v74, v75\n\t" \
  "v_permlane32_swap_b32 v0, v2\n\t" \
  "v_permlane32_swap_b32 v1, v3\n\t" \
  "v_mov_b64 v[6:7], v[4:5]\n\t" \
  "s_mov_b32 exec_hi, 0\n\t" \
  "buffer_load_dword v118, v112, %[rh], s96 offen sc1\n\t" \
  "s_mov_b64 exec, -1\n\t" \
  "buffer_store_dwordx4 v[0:3], v110, %[rh], s94 offen sc1\n\t" \
  "v_permlane16_swap_b32 v4, v6\n\t" \
  "v_permlane16_swap_b32 v5, v7\n\t" \
  "buffer_store_dwordx4 v[4:7], v111, %[rh], s94 offen sc1\n\t" \
  "s_waitcnt vmcnt(3)\n\t" \
  "ds_write_b128 v101, v[32:35] offset:23552\n\t" \
  "ds_write_b128 v101, v[36:39] offset:24576\n\t" \
  "ds_write_b128 v101, v[40:43] offset:25600\n\t" \
  "s_add_i32 s77, s44, 2\n\t" \
  "s_min_u32 s77, s77, 63\n\t" \
  "v_cmp_gt_i32 vcc, s77, v119\n\t" \
  "s_cmp_lg_u64 vcc, 0\n\t" \
  "s_cselect_b32 s42, 32, 0\n\t" \
  "s_cmp_eq_u32 s44, 0\n\t" \
  "s_cselect_b32 s42, 32, s42\n\t" \
  "s_add_i32 s41, s56, 1950\n\t" \
  "s_sub_i32 s76, s92, s51\n\t" \
  "s_sub_i32 s77, s93, s53\n\t" \
  "s_cmp_eq_u32 s76, 0\n\t" \
  "s_cbranch_scc1 21f\n\t" \
  "v_cvt_f32_u32 v117, s76\n\t" \
  "v_cvt_f32_u32 v116, s77\n\t" \
  "v_rcp_f32 v117, v117\n\t" \
  "s_mov_b32 s51, s92\n\t" \
  "v_mul_f32 v117, v117, v116\n\t" \
  "v_max_f32 v117, 0x3f028f5c, v117\n\t" \
  "v_min_f32 v117, 0x3ffeb852, v117\n\t" \
  "v_rcp_f32 v116, v117\n\t" \
  "v_mul_f32 v117, 0x4f000000, v117\n\t" \
  "v_mul_f32 v116, 0x47800000, v116\n\t" \
  "v_cvt_u32_f32 v117, v117\n\t" \
  "v_cvt_u32_f32 v116, v116\n\t" \
  "s_mov_b32 s53, s93\n\t" \
  "v_readfirstlane_b32 s46, v117\n\t" \
  "v_readfirstlane_b32 s52, v116\n\t" \
  "21:\n\t" \
  "s_add_i32 s44, s44, 1\n\t" \
  "s_and_b32 s58, s58, 4\n\t" \
  "s_and_b32 s76, s44, 64\n\t" \
  "s_or_b32 s42, s42, s76\n\t" \
  "s_cbranch_scc0 20b\n\t" \
  "s_branch 99f\n\t" \
  "14:\n\t" \
  "s_add_u32 s59, s59, 0x10000\n\t" \
  "s_cmp_lt_u32 s59, 0x100000\n\t" \
  "s_cbranch_scc1 13b\n\t" \
  "s_mov_b64 exec, -1\n\t" \
  "s_mov_b32 s42, 16\n\t" \
  "s_branch 99f\n\t" \
  "8:\n\t" \
  "s_bitcmp1_b32 s58, 1\n\t" \
  "s_cbranch_scc1 4b\n\t" \
  "s_bitcmp1_b32 s58, 2\n\t" \
  "s_cbranch_scc1 5b\n\t" \
  "v_max3_u32 v116, v32, v33, v34\n\t" \
  "v_max3_u32 v117, v35, v36, v37\n\t" \
  "v_max3_u32 v116, v116, v38, v39\n\t" \
  "v_max_u32 v116, v116, v117\n\t" \
  "v_cmp_eq_u32 vcc, -1, v116\n\t" \
  "s_cbranch_vccz 5b\n\t" \
  "s_branch 0f\n\t" \
  "9:\n\t" \
  "s_bitcmp1_b32 s58, 2\n\t" \
  "s_cbranch_scc1 6b\n\t" \
  "v_max3_u32 v116, v40, v41, v42\n\t" \
  "v_max3_u32 v117, v43, v44, v45\n\t" \
  "v_max3_u32 v116, v116, v46, v47\n\t" \
  "v_max_u32 v116, v116, v117\n\t" \
  "v_cmp_eq_u32 vcc, -1, v116\n\t" \
  "s_cbranch_vccz 6b\n\t" \
  "s_or_b32 s58, s58, 2\n\t" \
  "0:\n\t" \
  "s_or_b32 s58, s58, 1\n\t" \
  "11:\n\t" \
  "buffer_load_dwordx4 v[32:35], v100, %[rh], s40 offen sc1\n\t" \
  "buffer_load_dwordx4 v[36:39], v100, %[rh], s76 offen sc1\n\t" \
  "buffer_load_dwordx4 v[40:43], v100, %[rh], s77 offen sc1\n\t" \
  "buffer_load_dwordx4 v[44:47], v100, %[rh], s78 offen sc1\n\t" \
  "s_waitcnt lgkmcnt(0)\n\t" \
  "s_branch 10b\n\t" \
  "17:\n\t" \
  "s_add_u32 s59, s59, 1\n\t" \
  "s_bitcmp1_b32 s58, 2\n\t" \
  "s_cbranch_scc1 3b\n\t" \
  "s_waitcnt vmcnt(0)\n\t" \
  "v_max_u32 v116, v32, v33\n\t" \
  "v_max3_u32 v116, v116, v34, v35\n\t" \
  "v_max3_u32 v116, v116, v36, v37\n\t" \
  "v_max3_u32 v116, v116, v38, v39\n\t" \
  "v_max3_u32 v116, v116, v40, v41\n\t" \
  "v_max3_u32 v116, v116, v42, v43\n\t" \
  "v_max3_u32 v116, v116, v44, v45\n\t" \
  "v_max3_u32 v116, v116, v46, v47\n\t" \
  "v_cmp_eq_u32 vcc, -1, v116\n\t" \
  "s_cbranch_vccz 3b\n\t" \
  "s_bitcmp1_b32 s59, 6\n\t" \
  "s_cbranch_scc1 12f\n\t" \
  "buffer_load_dwordx4 v[32:35], v100, %[rh], s40 offen sc1\n\t" \
  "buffer_load_dwordx4 v[36:39], v100, %[rh], s76 offen sc1\n\t" \
  "buffer_load_dwordx4 v[40:43], v100, %[rh], s77 offen sc1\n\t" \
  "buffer_load_dwordx4 v[44:47], v100, %[rh], s78 offen sc1\n\t" \
  "s_branch 17b\n\t" \
  "12:\n\t" \
  "s_mov_b32 s42, s58\n\t" \
  "s_branch 99f\n\t" \
  "93:\n\t" \
  "s_mov_b32 s58, s42\n\t" \
  "20:\n\t" \
  "s_and_b32 s76, s44, 7\n\t" \
  "s_lshl_b32 s76, s76, 15\n\t" \
  "s_add_i32 s40, s54, s76\n\t" \
  "s_and_b32 s76, s44, 3\n\t" \
  "s_lshl_b32 s45, s76, 14\n\t" \
  "s_add_i32 s47, s44, 1\n\t" \
  "s_min_u32 s47, s47, 63\n\t" \
  "s_lshr_b32 s76, s47, 1\n\t" \
  "s_lshl_b32 s47, s47, 20\n\t" \
  "s_add_i32 s47, s47, s55\n\t" \
  "s_bitcmp1_b64 %[sel], s76\n\t" \
  "s_cselect_b32 s76, 0x2000, 0\n\t" \
  "s_add_i32 s47, s47, s76\n\t" \
  "s_add_i32 s49, s44, -1\n\t" \
  "s_and_b32 s48, s49, 2\n\t" \
  "s_lshl_b32 s48, s48, 13\n\t" \
  "s_and_b32 s76, s49, 1\n\t" \
  "s_lshl_b32 s76, s76, 12\n\t" \
  "s_add_i32 s48, s48, s76\n\t" \
  "s_lshl_b32 s50, s53, 8\n\t" \
  "s_mov_b32 s59, 0\n\t" \
  "s_mov_b32 s92, s51\n\t" \
  "s_mov_b32 s93, s53\n\t" \
  "s_xor_b32 s76, s40, 0x400\n\t" \
  "s_xor_b32 s77, s40, 0x800\n\t" \
  "s_xor_b32 s78, s40, 0xc00\n\t" \
  "s_cmp_lg_u32 s58, 0\n\t" \
  "s_cbranch_scc1 11f\n\t" \
  "10:\n\t" \
  "ds_read_b128 v[48:51], v103 offset:32768\n\t" \
  "ds_read_b128 v[52:55], v103 offset:33792\n\t" \
  "ds_read_b128 v[56:59], v103 offset:34816\n\t" \
  "ds_read_b128 v[60:63], v103 offset:35840\n\t" \
  "ds_read_b128 v[16:19], v103 offset:49152\n\t" \
  "ds_read_b128 v[20:23], v103 offset:50176\n\t" \
  "ds_read_b128 v[24:27], v103 offset:51200\n\t" \
  "ds_read_b128 v[28:31], v103 offset:52224\n\t" \
  "ds_read_b128 v[84:87], v102 offset:16384\n\t" \
  "ds_read_b128 v[88:91], v102 offset:17408\n\t" \
  "ds_read_b128 v[92:95], v102 offset:18432\n\t" \
  "ds_read_b128 v[96:99], v102 offset:19456\n\t" \
  "s_waitcnt lgkmcnt(4)\n\t" \
  "ds_read_b128 v[0:3], v102 offset:49152\n\t" \
  "ds_read_b128 v[4:7], v102 offset:50176\n\t" \
  "ds_read_b128 v[8:11], v102 offset:51200\n\t" \
  "ds_read_b128 v[12:15], v102 offset:52224\n\t" \
  "s_bitcmp1_b32 s58, 1\n\t" \
  "s_cselect_b64 s[80:81], -1, 0\n\t" \
  "s_cmp_lg_u32 s58, 0\n\t" \
  "s_cbranch_scc1 17f\n\t" \
  "s_mov_b32 exec_hi, 0\n\t" \
  "16:\n\t" \
  "s_memtime vcc\n\t" \
  "s_waitcnt lgkmcnt(0)\n\t" \
  "s_sub_i32 s79, s41, vcc_lo\n\t" \
  "s_cmp_gt_i32 s79, 64\n\t" \
  "s_cbranch_scc1 16b\n\t" \
  "13:\n\t" \
  "buffer_load_dword v116, v112, %[rh], s48 offen sc1\n\t" \
  "s_waitcnt vmcnt(0)\n\t" \
  "15:\n\t" \
  "v_mov_b32 v117, 20000\n\t" \
  "v_cmp_ne_u32_sdwa vcc, v116, s49 src0_sel:BYTE_0 src1_sel:DWORD\n\t" \
  "v_subrev_u32 v116, s50, v116\n\t" \
  "v_ashrrev_i32 v116, 8, v116\n\t" \
  "s_nop 1\n\t" \
  "v_max_i32_dpp v116, v116, v116 quad_perm:[1,0,3,2] row_mask:0xf bank_mask:0xf\n\t" \
  "s_cbranch_vccnz 14f\n\t" \
  "s_nop 0\n\t" \
  "v_max_i32_dpp v116, v116, v116 quad_perm:[2,3,0,1] row_mask:0xf bank_mask:0xf\n\t" \
  "s_nop 1\n\t" \
  "v_max_i32_dpp v116, v116, v116 row_half_mirror row_mask:0xf bank_mask:0xf\n\t" \
  "s_nop 1\n\t" \
  "v_max_i32_dpp v116, v116, v116 row_mirror row_mask:0xf bank_mask:0xf\n\t" \
  "v_med3_i32 v116, v116, 0, v117\n\t" \
  "v_mul_u32_u24 v116, s52, v116\n\t" \
  "v_lshrrev_b32 v116, 16, v116\n\t" \
  "s_mov_b64 exec, -1\n\t" \
  "v_readfirstlane_b32 s84, v116\n\t" \
  "v_readlane_b32 s85, v116, 16\n\t" \
  "s_max_u32 s84, s84, s85\n\t" \
  "s_add_i32 s84, s84, s51\n\t" \
  "1:\n\t" \
  "s_memtime vcc\n\t" \
  "s_waitcnt lgkmcnt(0)\n\t" \
  "s_sub_i32 s79, s84, vcc_lo\n\t" \
  "s_cmp_gt_i32 s79, 128\n\t" \
  "s_cbranch_scc1 1b\n\t" \
  ".p2align 4\n\t" \
  "2:\n\t" \
  "s_sub_i32 s79, s79, 33\n\t" \
  "s_cmp_gt_i32 s79, 56\n\t" \
  "s_cbranch_scc1 2b\n\t" \
  "buffer_load_dwordx4 v[32:35], v100, %[rh], s40 offen sc1\n\t" \
  "s_nop 12\n\t" \
  "buffer_load_dwordx4 v[36:39], v100, %[rh], s76 offen sc1\n\t" \
  "s_nop 12\n\t" \
  "buffer_load_dwordx4 v[40:43], v100, %[rh], s77 offen sc1\n\t" \
  "s_nop 12\n\t" \
  "buffer_load_dwordx4 v[44:47], v100, %[rh], s78 offen sc1\n\t" \
  "s_memtime vcc\n\t" \
  "s_memrealtime s[82:83]\n\t" \
  "s_nop 4\n\t" \
  "s_memrealtime s[84:85]\n\t" \
  "s_nop 4\n\t" \
  "s_memrealtime s[56:57]\n\t" \
  "s_nop 4\n\t" \
  "s_memrealtime s[86:87]\n\t" \
  "s_nop 4\n\t" \
  "s_memrealtime s[88:89]\n\t" \
  "s_nop 4\n\t" \
  "s_memrealtime s[90:91]\n\t" \
  "s_mov_b32 s92, 0\n\t" \
  "s_waitcnt lgkmcnt(0)\n\t" \
  "s_cmp_eq_u32 s82, s84\n\t" \
  "s_addc_u32 s92, s92, 0\n\t" \
  "s_cmp_eq_u32 s82, s56\n\t" \
  "s_addc_u32 s92, s92, 0\n\t" \
  "s_cmp_eq_u32 s82, s86\n\t" \
  "s_addc_u32 s92, s92, 0\n\t" \
  "s_cmp_eq_u32 s82, s88\n\t" \
  "s_addc_u32 s92, s92, 0\n\t" \
  "s_cmp_eq_u32 s82, s90\n\t" \
  "s_addc_u32 s92, s92, 0\n\t" \
  "s_mul_i32 s92, s92, 24\n\t" \
  "s_add_i32 s92, s92, vcc_lo\n\t" \
  "s_lshl4_add_u32 s93, s82, 64\n\t" \
  "3:\n\t" \
  "v_mov_b64 v[68:69], 0\n\t" \
  "v_mov_b64 v[70:71], 0\n\t" \
  "v_mov_b64 v[72:73], 0\n\t" \
  "v_mov_b64 v[74:75], 0\n\t" \
  "v_mov_b64 v[76:77], 0\n\t" \
  "v_mov_b64 v[78:79], 0\n\t" \
  "v_mov_b64 v[80:81], 0\n\t" \
  "v_mov_b64 v[82:83], 0\n\t" \
  "s_waitcnt lgkmcnt(0)\n\t" \
  "s_waitcnt vmcnt(3)\n\t" \
  "v_smfmac_f32_16x16x64_bf16 v[68:71], v[32:35], v[152:159], v104\n\t" \
  "ds_write_b128 v101, v[32:35] offset:12288\n\t" \
  "v_smfmac_f32_16x16x64_bf16 v[72:75], v[32:35], v[160:167], v104\n\t" \
  "v_smfmac_f32_16x16x64_bf16 v[76:79], v[32:35], v[48:55], v104\n\t" \
  "v_smfmac_f32_16x16x64_bf16 v[80:83], v[32:35], v[56:63], v104\n\t" \
  "s_waitcnt vmcnt(2)\n\t" \
  "v_smfmac_f32_16x16x64_bf16 v[68:71], v[36:39], v[168:175], v104\n\t" \
  "ds_write_b128 v101, v[36:39] offset:13312\n\t" \
  "v_smfmac_f32_16x16x64_bf16 v[72:75], v[36:39], v[176:183], v104\n\t" \
  "v_smfmac_f32_16x16x64_bf16 v[76:79], v[36:39], v[184:191], v104\n\t" \
  "v_cmp_u_f32 vcc, v68, v69\n\t" \
  "v_smfmac_f32_16x16x64_bf16 v[80:83], v[36:39], v[192:199], v104\n\t" \
  "v_cmp_u_f32 s[82:83], v70, v71\n\t" \
  "s_waitcnt vmcnt(1)\n\t" \
  "v_smfmac_f32_16x16x64_bf16 v[68:71], v[40:43], v[16:23], v104\n\t" \
  "ds_write_b128 v101, v[40:43] offset:14336\n\t" \
  "v_smfmac_f32_16x16x64_bf16 v[72:75], v[40:43], v[24:31], v104\n\t" \
  "s_or_b64 vcc, vcc, s[82:83]\n\t" \
  "s_or_b64 vcc, vcc, s[80:81]\n\t" \
  "s_waitcnt lgkmcnt(1)\n\t" \
  "v_smfmac_f32_16x16x64_bf16 v[76:79], v[40:43], v[200:207], v104\n\t" \
  "s_cbranch_scc1 8f\n\t" \
  "5:\n\t" \
  "s_barrier\n\t" \
  "4:\n\t" \
  "s_memtime s[56:57]\n\t" \
  "ds_read_b128 v[48:51], v101 offset:0\n\t" \
  "v_smfmac_f32_16x16x64_bf16 v[80:83], v[40:43], v[208:215], v104\n\t" \
  "ds_read_b128 v[52:55], v101 offset:1024\n\t" \
  "ds_read_b128 v[56:59], v101 offset:4096\n\t" \
  "s_waitcnt vmcnt(0)\n\t" \
  "v_smfmac_f32_16x16x64_bf16 v[68:71], v[44:47], v[216:223], v104\n\t" \
  "v_smfmac_f32_16x16x64_bf16 v[72:75], v[44:47], v[224:231], v104\n\t" \
  "ds_write_b128 v101, v[44:47] offset:15360\n\t" \
  "v_smfmac_f32_16x16x64_bf16 v[76:79], v[44:47], v[232:239], v104\n\t" \
  "v_cmp_u_f32 vcc, v68, v69\n\t" \
  "v_smfmac_f32_16x16x64_bf16 v[80:83], v[44:47], v[240:247], v104\n\t" \
  "v_cmp_u_f32 s[82:83], v70, v71\n\t" \
  "s_waitcnt lgkmcnt(3)\n\t" \
  "v_smfmac_f32_16x16x64_bf16 v[68:71], v[48:51], a[0:7], v104\n\t" \
  "ds_read_b128 v[60:63], v101 offset:5120\n\t" \
  "ds_read_b128 v[64:67], v101 offset:8192\n\t" \
  "v_smfmac_f32_16x16x64_bf16 v[72:75], v[48:51], a[8:15], v104\n\t" \
  "s_or_b64 vcc, vcc, s[82:83]\n\t" \
  "s_waitcnt lgkmcnt(2)\n\t" \
  "v_smfmac_f32_16x16x64_bf16 v[68:71], v[52:55], a[16:23], v104\n\t" \
  "s_cbranch_scc1 9f\n\t" \
  "6:\n\t" \
  "s_barrier\n\t" \
  "v_smfmac_f32_16x16x64_bf16 v[72:75], v[52:55], a[24:31], v104\n\t" \
  "ds_read_b128 v[48:51], v101 offset:9216\n\t" \
  "v_smfmac_f32_16x16x64_bf16 v[68:71], v[56:59], a[96:103], v104\n\t" \
  "ds_read_b128 v[52:55], v101 offset:2048\n\t" \
  "v_smfmac_f32_16x16x64_bf16 v[72:75], v[56:59], a[104:111], v104\n\t" \
  "s_waitcnt lgkmcnt(3)\n\t" \
  "v_smfmac_f32_16x16x64_bf16 v[68:71], v[60:63], a[112:119], v104\n\t" \
  "ds_read_b128 v[56:59], v101 offset:3072\n\t" \
  "v_smfmac_f32_16x16x64_bf16 v[72:75], v[60:63], a[120:127], v104\n\t" \
  "s_waitcnt lgkmcnt(3)\n\t" \
  "v_smfmac_f32_16x16x64_bf16 v[68:71], v[64:67], a[192:199], v104\n\t" \
  "ds_read_b128 v[60:63], v101 offset:6144\n\t" \
  "v_smfmac_f32_16x16x64_bf16 v[72:75], v[64:67], a[200:207], v104\n\t" \
  "s_waitcnt lgkmcnt(3)\n\t" \
  "v_smfmac_f32_16x16x64_bf16 v[68:71], v[48:51], a[208:215], v104\n\t" \
  "ds_read_b128 v[64:67], v101 offset:7168\n\t" \
  "ds_read_b128 v[16:19], v103 offset:16384\n\t" \
  "global_load_dword v119, v109, %[kc] sc1\n\t" \
  "v_smfmac_f32_16x16x64_bf16 v[72:75], v[48:51], a[216:223], v104\n\t" \
  "s_waitcnt lgkmcnt(4)\n\t" \
  "v_smfmac_f32_16x16x64_bf16 v[68:71], v[52:55], v[84:91], v104\n\t" \
  "ds_read_b128 v[48:51], v101 offset:11264\n\t" \
  "ds_read_b128 v[20:23], v103 offset:17408\n\t" \
  "v_smfmac_f32_16x16x64_bf16 v[72:75], v[52:55], v[92:99], v104\n\t" \
  "s_waitcnt lgkmcnt(5)\n\t" \
  "v_smfmac_f32_16x16x64_bf16 v[68:71], v[56:59], a[64:71], v104\n\t" \
  "ds_read_b128 v[52:55], v101 offset:10240\n\t" \
  "ds_read_b128 v[24:27], v103 offset:18432\n\t" \
  "v_smfmac_f32_16x16x64_bf16 v[72:75], v[56:59], a[72:79], v104\n\t" \
  "ds_read_b128 v[28:31], v103 offset:19456\n\t" \
  "s_waitcnt lgkmcnt(7)\n\t" \
  "v_smfmac_f32_16x16x64_bf16 v[68:71], v[60:63], v[0:7], v104\n\t" \
  "v_smfmac_f32_16x16x64_bf16 v[72:75], v[60:63], v[8:15], v104\n\t" \
  "s_waitcnt lgkmcnt(6)\n\t" \
  "v_smfmac_f32_16x16x64_bf16 v[68:71], v[64:67], a[160:167], v104\n\t" \
  "ds_read_b128 v[0:3], v102 offset:0\n\t" \
  "v_smfmac_f32_16x16x64_bf16 v[72:75], v[64:67], a[168:175], v104\n\t" \
  "ds_read_b128 v[4:7], v102 offset:1024\n\t" \
  "s_waitcnt lgkmcnt(6)\n\t" \
  "v_smfmac_f32_16x16x64_bf16 v[68:71], v[48:51], v[120:127], v104\n\t" \
  "s_waitcnt lgkmcnt(4)\n\t" \
  "v_smfmac_f32_16x16x64_bf16 v[68:71], v[52:55], v[16:23], v104\n\t" \
  "ds_read_b128 v[8:11], v102 offset:2048\n\t" \
  "v_smfmac_f32_16x16x64_bf16 v[72:75], v[48:51], v[128:135], v104\n\t" \
  "ds_read_b128 v[12:15], v102 offset:3072\n\t" \
  "s_waitcnt lgkmcnt(4)\n\t" \
  "v_smfmac_f32_16x16x64_bf16 v[72:75], v[52:55], v[24:31], v104\n\t" \
  "v_add_f32 v44, v68, v70\n\t" \
  "v_smfmac_f32_16x16x64_bf16 v[76:79], v[56:59], a[80:87], v104\n\t" \
  "v_add_f32 v45, v69, v71\n\t" \
  "v_cvt_pk_bf16_f32 v44, v44, v45\n\t" \
  "v_smfmac_f32_16x16x64_bf16 v[80:83], v[56:59], a[88:95], v104\n\t" \
  "v_add_f32 v46, v72, v74\n\t" \
  "v_add_f32 v47, v73, v75\n\t" \
  "v_smfmac_f32_16x16x64_bf16 v[76:79], v[60:63], a[144:151], v104\n\t" \
  "v_cvt_pk_bf16_f32 v46, v46, v47\n\t" \
  "ds_write2st64_b32 v105, v44, v46 offset1:1\n\t" \
  "v_smfmac_f32_16x16x64_bf16 v[80:83], v[60:63], a[152:159], v104\n\t" \
  "ds_read_b128 v[56:59], v101 offset:0\n\t" \
  "v_smfmac_f32_16x16x64_bf16 v[76:79], v[64:67], a[176:183], v104\n\t" \
  "ds_read_b128 v[16:19], v102 offset:32768\n\t" \
  "v_smfmac_f32_16x16x64_bf16 v[80:83], v[64:67], a[184:191], v104\n\t" \
  "ds_read_b128 v[20:23], v102 offset:33792\n\t" \
  "v_smfmac_f32_16x16x64_bf16 v[76:79], v[48:51], v[136:143], v104\n\t" \
  "s_waitcnt lgkmcnt(3)\n\t" \
  "s_barrier\n\t" \
  "v_smfmac_f32_16x16x64_bf16 v[80:83], v[48:51], v[144:151], v104\n\t" \
  "v_smfmac_f32_16x16x64_bf16 v[76:79], v[52:55], a[240:247], v104\n\t" \
  "ds_read_b128 v[60:63], v101 offset:9216\n\t" \
  "ds_read_b128 v[24:27], v102 offset:34816\n\t" \
  "v_smfmac_f32_16x16x64_bf16 v[80:83], v[52:55], a[248:255], v104\n\t" \
  "ds_read_b128 v[64:67], v101 offset:2048\n\t" \
  "s_waitcnt lgkmcnt(5)\n\t" \
  "v_smfmac_f32_16x16x64_bf16 v[76:79], v[56:59], v[0:7], v104\n\t" \
  "ds_read_b128 v[48:51], v101 offset:4096\n\t" \
  "ds_read_b128 v[28:31], v102 offset:35840\n\t" \
  "v_smfmac_f32_16x16x64_bf16 v[80:83], v[56:59], v[8:15], v104\n\t" \
  "s_waitcnt lgkmcnt(4)\n\t" \
  "v_smfmac_f32_16x16x64_bf16 v[76:79], v[60:63], a[224:231], v104\n\t" \
  "ds_read_b128 v[52:55], v101 offset:1024\n\t" \
  "v_smfmac_f32_16x16x64_bf16 v[80:83], v[60:63], a[232:239], v104\n\t" \
  "ds_read_b128 v[0:3], v103 offset:0\n\t" \
  "buffer_load_dwordx4 v[32:35], v100, %[ru], s47 offen offset:0 sc1\n\t" \
  "s_waitcnt lgkmcnt(4)\n\t" \
  "v_smfmac_f32_16x16x64_bf16 v[76:79], v[64:67], a[48:55], v104\n\t" \
  "ds_read_b128 v[56:59], v101 offset:5120\n\t" \
  "v_smfmac_f32_16x16x64_bf16 v[80:83], v[64:67], a[56:63], v104\n\t" \
  "ds_read_b128 v[60:63], v101 offset:8192\n\t" \
  "ds_read_b128 v[4:7], v103 offset:1024\n\t" \
  "s_waitcnt lgkmcnt(6)\n\t" \
  "v_smfmac_f32_16x16x64_bf16 v[76:79], v[48:51], v[16:23], v104\n\t" \
  "ds_read_b128 v[8:11], v103 offset:2048\n\t" \
  "buffer_load_dwordx4 v[36:39], v100, %[ru], s47 offen offset:1024 sc1\n\t" \
  "s_waitcnt lgkmcnt(6)\n\t" \
  "v_smfmac_f32_16x16x64_bf16 v[80:83], v[48:51], v[24:31], v104\n\t" \
  "ds_read_b128 v[12:15], v103 offset:3072\n\t" \
  "s_waitcnt lgkmcnt(6)\n\t" \
  "v_smfmac_f32_16x16x64_bf16 v[76:79], v[52:55], a[32:39], v104\n\t" \
  "v_smfmac_f32_16x16x64_bf16 v[80:83], v[52:55], a[40:47], v104\n\t" \
  "s_waitcnt lgkmcnt(4)\n\t" \
  "v_smfmac_f32_16x16x64_bf16 v[76:79], v[56:59], a[128:135], v104\n\t" \
  "s_waitcnt lgkmcnt(2)\n\t" \
  "v_smfmac_f32_16x16x64_bf16 v[76:79], v[60:63], v[0:7], v104\n\t" \
  "v_smfmac_f32_16x16x64_bf16 v[80:83], v[56:59], a[136:143], v104\n\t" \
  "s_waitcnt lgkmcnt(0)\n\t" \
  "v_smfmac_f32_16x16x64_bf16 v[80:83], v[60:63], v[8:15], v104\n\t" \
  "v_add_f32 v20, v76, v78\n\t" \
  "v_add_f32 v21, v77, v79\n\t" \
  "v_cvt_pk_bf16_f32 v20, v20, v21\n\t" \
  "ds_write_b32 v106, v20\n\t" \
  "s_nop 3\n\t" \
  "v_add_f32 v23, v80, v82\n\t" \
  "v_add_f32 v21, v81, v83\n\t" \
  "v_cvt_pk_bf16_f32 v21, v23, v21\n\t" \
  "ds_write_b32 v106, v21 offset:256\n\t" \
  "s_waitcnt lgkmcnt(0)\n\t" \
  "s_barrier\n\t" \
  "s_waitcnt vmcnt(0)\n\t" \
  "s_barrier\n\t" \
  "ds_write_b128 v101, v[32:35] offset:26624\n\t" \
  "ds_write_b128 v101, v[36:39] offset:27648\n\t" \
  "s_add_i32 s77, s44, 2\n\t" \
  "s_min_u32 s77, s77, 63\n\t" \
  "v_cmp_gt_i32 vcc, s77, v119\n\t" \
  "s_cmp_lg_u64 vcc, 0\n\t" \
  "s_cselect_b32 s42, 32, 0\n\t" \
  "s_cmp_eq_u32 s44, 0\n\t" \
  "s_cselect_b32 s42, 32, s42\n\t" \
  "s_add_i32 s41, s56, 1950\n\t" \
  "s_sub_i32 s76, s92, s51\n\t" \
  "s_sub_i32 s77, s93, s53\n\t" \
  "s_cmp_eq_u32 s76, 0\n\t" \
  "s_cbranch_scc1 21f\n\t" \
  "v_cvt_f32_u32 v117, s76\n\t" \
  "v_cvt_f32_u32 v116, s77\n\t" \
  "v_rcp_f32 v117, v117\n\t" \
  "s_mov_b32 s51, s92\n\t" \
  "v_mul_f32 v117, v117, v116\n\t" \
  "v_max_f32 v117, 0x3f028f5c, v117\n\t" \
  "v_min_f32 v117, 0x3ffeb852, v117\n\t" \
  "v_rcp_f32 v116, v117\n\t" \
  "v_mul_f32 v117, 0x4f000000, v117\n\t" \
  "v_mul_f32 v116, 0x47800000, v116\n\t" \
  "v_cvt_u32_f32 v117, v117\n\t" \
  "v_cvt_u32_f32 v116, v116\n\t" \
  "s_mov_b32 s53, s93\n\t" \
  "v_readfirstlane_b32 s46, v117\n\t" \
  "v_readfirstlane_b32 s52, v116\n\t" \
  "21:\n\t" \
  "v_mov_b32 v0, -1\n\t" \
  "v_mov_b32 v1, -1\n\t" \
  "v_mov_b32 v2, -1\n\t" \
  "v_mov_b32 v3, -1\n\t" \
  "s_and_b32 s76, s49, 3\n\t" \
  "s_mul_i32 s76, s76, 0xc000\n\t" \
  "s_add_i32 s76, s76, 0x40000\n\t" \
  "buffer_store_dwordx4 v[0:3], v110, %[rh], s76 offen sc1\n\t" \
  "s_add_i32 s76, s44, 1\n\t" \
  "s_and_b32 s76, s76, 3\n\t" \
  "s_lshl_b32 s76, s76, 18\n\t" \
  "s_add_i32 s77, s76, 0x10000\n\t" \
  "buffer_store_dwordx4 v[0:3], v107, %[rh], s76 offen sc1\n\t" \
  "buffer_store_dwordx4 v[0:3], v107, %[rh], s77 offen sc1\n\t" \
  "buffer_store_dwordx4 v[0:3], v111, %[rh], s76 offen sc1\n\t" \
  "s_add_i32 s44, s44, 1\n\t" \
  "s_and_b32 s58, s58, 4\n\t" \
  "s_and_b32 s76, s44, 64\n\t" \
  "s_or_b32 s42, s42, s76\n\t" \
  "s_cbranch_scc0 20b\n\t" \
  "s_branch 99f\n\t" \
  "14:\n\t" \
  "s_add_u32 s59, s59, 0x10000\n\t" \
  "s_cmp_lt_u32 s59, 0x100000\n\t" \
  "s_cbranch_scc1 13b\n\t" \
  "s_mov_b64 exec, -1\n\t" \
  "s_mov_b32 s42, 16\n\t" \
  "s_branch 99f\n\t" \
  "8:\n\t" \
  "s_bitcmp1_b32 s58, 1\n\t" \
  "s_cbranch_scc1 4b\n\t" \
  "s_bitcmp1_b32 s58, 2\n\t" \
  "s_cbranch_scc1 5b\n\t" \
  "v_max3_u32 v116, v32, v33, v34\n\t" \
  "v_max3_u32 v117, v35, v36, v37\n\t" \
  "v_max3_u32 v116, v116, v38, v39\n\t" \
  "v_max_u32 v116, v116, v117\n\t" \
  "v_cmp_eq_u32 vcc, -1, v116\n\t" \
  "s_cbranch_vccz 5b\n\t" \
  "s_branch 0f\n\t" \
  "9:\n\t" \
  "s_bitcmp1_b32 s58, 2\n\t" \
  "s_cbranch_scc1 6b\n\t" \
  "v_max3_u32 v116, v40, v41, v42\n\t" \
  "v_max3_u32 v117, v43, v44, v45\n\t" \
  "v_max3_u32 v116, v116, v46, v47\n\t" \
  "v_max_u32 v116, v116, v117\n\t" \
  "v_cmp_eq_u32 vcc, -1, v116\n\t" \
  "s_cbranch_vccz 6b\n\t" \
  "s_or_b32 s58, s58, 2\n\t" \
  "0:\n\t" \
  "s_or_b32 s58, s58, 1\n\t" \
  "11:\n\t" \
  "buffer_load_dwordx4 v[32:35], v100, %[rh], s40 offen sc1\n\t" \
  "buffer_load_dwordx4 v[36:39], v100, %[rh], s76 offen sc1\n\t" \
  "buffer_load_dwordx4 v[40:43], v100, %[rh], s77 offen sc1\n\t" \
  "buffer_load_dwordx4 v[44:47], v100, %[rh], s78 offen sc1\n\t" \
  "s_waitcnt lgkmcnt(0)\n\t" \
  "s_branch 10b\n\t" \
  "17:\n\t" \
  "s_add_u32 s59, s59, 1\n\t" \
  "s_bitcmp1_b32 s58, 2\n\t" \
  "s_cbranch_scc1 3b\n\t" \
  "s_waitcnt vmcnt(0)\n\t" \
  "v_max_u32 v116, v32, v33\n\t" \
  "v_max3_u32 v116, v116, v34, v35\n\t" \
  "v_max3_u32 v116, v116, v36, v37\n\t" \
  "v_max3_u32 v116, v116, v38, v39\n\t" \
  "v_max3_u32 v116, v116, v40, v41\n\t" \
  "v_max3_u32 v116, v116, v42, v43\n\t" \
  "v_max3_u32 v116, v116, v44, v45\n\t" \
  "v_max3_u32 v116, v116, v46, v47\n\t" \
  "v_cmp_eq_u32 vcc, -1, v116\n\t" \
  "s_cbranch_vccz 3b\n\t" \
  "s_bitcmp1_b32 s59, 6\n\t" \
  "s_cbranch_scc1 12f\n\t" \
  "buffer_load_dwordx4 v[32:35], v100, %[rh], s40 offen sc1\n\t" \
  "buffer_load_dwordx4 v[36:39], v100, %[rh], s76 offen sc1\n\t" \
  "buffer_load_dwordx4 v[40:43], v100, %[rh], s77 offen sc1\n\t" \
  "buffer_load_dwordx4 v[44:47], v100, %[rh], s78 offen sc1\n\t" \
  "s_branch 17b\n\t" \
  "12:\n\t" \
  "s_mov_b32 s42, s58\n\t" \
  "s_branch 99f\n\t" \
  "99:\n\t" \
  "s_waitcnt vmcnt(0) lgkmcnt(0)\n\t" \
  ""
#define VO_BODY \
  "s_cmp_eq_u32 %[wv], 1\n\t" \
  "s_cbranch_scc1 91f\n\t" \
  "s_cmp_eq_u32 %[wv], 2\n\t" \
  "s_cbranch_scc1 92f\n\t" \
  "s_cmp_eq_u32 %[wv], 3\n\t" \
  "s_cbranch_scc1 93f\n\t" \
  "90:\n\t" \
  "s_mov_b32 s58, s42\n\t" \
  "20:\n\t" \
  "s_and_b32 s76, s44, 7\n\t" \
  "s_lshl_b32 s76, s76, 15\n\t" \
  "s_add_i32 s40, s54, s76\n\t" \
  "s_and_b32 s76, s44, 3\n\t" \
  "s_lshl_b32 s45, s76, 14\n\t" \
  "s_lshl_b32 s47, s76, 20\n\t" \
  "s_add_i32 s47, s47, s55\n\t" \
  "s_lshr_b32 s76, s47, 21\n\t" \
  "s_bitcmp1_b64 %[sel], s76\n\t" \
  "s_cselect_b32 s76, 0x2000, 0\n\t" \
  "s_add_i32 s47, s47, s76\n\t" \
  "s_add_i32 s49, s44, -1\n\t" \
  "s_and_b32 s48, s49, 2\n\t" \
  "s_lshl_b32 s48, s48, 13\n\t" \
  "s_and_b32 s76, s49, 1\n\t" \
  "s_lshl_b32 s76, s76, 12\n\t" \
  "s_add_i32 s48, s48, s76\n\t" \
  "s_lshl_b32 s50, s53, 8\n\t" \
  "s_mov_b32 s59, 0\n\t" \
  "s_mov_b32 s92, s51\n\t" \
  "s_mov_b32 s93, s53\n\t" \
  "s_xor_b32 s76, s40, 0x400\n\t" \
  "s_xor_b32 s77, s40, 0x800\n\t" \
  "s_xor_b32 s78, s40, 0xc00\n\t" \
  "s_cmp_lg_u32 s58, 0\n\t" \
  "s_cbranch_scc1 11f\n\t" \
  "10:\n\t" \
  "ds_read_b128 v[48:51], v102 offset:0\n\t" \
  "ds_read_b128 v[52:55], v102 offset:1024\n\t" \
  "ds_read_b128 v[56:59], v102 offset:2048\n\t" \
  "ds_read_b128 v[60:63], v102 offset:3072\n\t" \
  "ds_read_b128 v[0:3], v102 offset:16384\n\t" \
  "ds_read_b128 v[4:7], v102 offset:17408\n\t" \
  "ds_read_b128 v[8:11], v102 offset:18432\n\t" \
  "ds_read_b128 v[12:15], v102 offset:19456\n\t" \
  "ds_read_b128 v[16:19], v102 offset:32768\n\t" \
  "ds_read_b128 v[20:23], v102 offset:33792\n\t" \
  "ds_read_b128 v[24:27], v102 offset:34816\n\t" \
  "ds_read_b128 v[28:31], v102 offset:35840\n\t" \
  "s_waitcnt lgkmcnt(4)\n\t" \
  "ds_read_b128 v[80:83], v102 offset:49152\n\t" \
  "ds_read_b128 v[84:87], v102 offset:50176\n\t" \
  "ds_read_b128 v[88:91], v102 offset:51200\n\t" \
  "ds_read_b128 v[92:95], v102 offset:52224\n\t" \
  "s_bitcmp1_b32 s58, 1\n\t" \
  "s_cselect_b64 s[80:81], -1, 0\n\t" \
  "s_cmp_lg_u32 s58, 0\n\t" \
  "s_cbranch_scc1 17f\n\t" \
  "s_mov_b32 exec_hi, 0\n\t" \
  "16:\n\t" \
  "s_memtime vcc\n\t" \
  "s_waitcnt lgkmcnt(0)\n\t" \
  "s_sub_i32 s79, s41, vcc_lo\n\t" \
  "s_cmp_gt_i32 s79, 64\n\t" \
  "s_cbranch_scc1 16b\n\t" \
  "13:\n\t" \
  "buffer_load_dword v116, v112, %[rh], s48 offen sc1\n\t" \
  "s_waitcnt vmcnt(0)\n\t" \
  "15:\n\t" \
  "v_mov_b32 v117, 20000\n\t" \
  "v_cmp_ne_u32_sdwa vcc, v116, s49 src0_sel:BYTE_0 src1_sel:DWORD\n\t" \
  "v_subrev_u32 v116, s50, v116\n\t" \
  "v_ashrrev_i32 v116, 8, v116\n\t" \
  "s_nop 1\n\t" \
  "v_max_i32_dpp v116, v116, v116 quad_perm:[1,0,3,2] row_mask:0xf bank_mask:0xf\n\t" \
  "s_cbranch_vccnz 14f\n\t" \
  "s_nop 0\n\t" \
  "v_max_i32_dpp v116, v116, v116 quad_perm:[2,3,0,1] row_mask:0xf bank_mask:0xf\n\t" \
  "s_nop 1\n\t" \
  "v_max_i32_dpp v116, v116, v116 row_half_mirror row_mask:0xf bank_mask:0xf\n\t" \
  "s_nop 1\n\t" \
  "v_max_i32_dpp v116, v116, v116 row_mirror row_mask:0xf bank_mask:0xf\n\t" \
  "v_med3_i32 v116, v116, 0, v117\n\t" \
  "v_mul_u32_u24 v116, s52, v116\n\t" \
  "v_lshrrev_b32 v116, 16, v116\n\t" \
  "s_mov_b64 exec, -1\n\t" \
  "v_readfirstlane_b32 s84, v116\n\t" \
  "v_readlane_b32 s85, v116, 16\n\t" \
  "s_max_u32 s84, s84, s85\n\t" \
  "s_add_i32 s84, s84, s51\n\t" \
  "1:\n\t" \
  "s_memtime vcc\n\t" \
  "s_waitcnt lgkmcnt(0)\n\t" \
  "s_sub_i32 s79, s84, vcc_lo\n\t" \
  "s_cmp_gt_i32 s79, 128\n\t" \
  "s_cbranch_scc1 1b\n\t" \
  ".p2align 4\n\t" \
  "2:\n\t" \
  "s_sub_i32 s79, s79, 33\n\t" \
  "s_cmp_gt_i32 s79, 56\n\t" \
  "s_cbranch_scc1 2b\n\t" \
  "buffer_load_dwordx4 v[32:35], v100, %[rh], s40 offen sc1\n\t" \
  "s_nop 12\n\t" \
  "buffer_load_dwordx4 v[36:39], v100, %[rh], s76 offen sc1\n\t" \
  "s_nop 12\n\t" \
  "buffer_load_dwordx4 v[40:43], v100, %[rh], s77 offen sc1\n\t" \
  "s_nop 12\n\t" \
  "buffer_load_dwordx4 v[44:47], v100, %[rh], s78 offen sc1\n\t" \
  "s_memtime vcc\n\t" \
  "s_memrealtime s[82:83]\n\t" \
  "s_nop 4\n\t" \
  "s_memrealtime s[84:85]\n\t" \
  "s_nop 4\n\t" \
  "s_memrealtime s[56:57]\n\t" \
  "s_nop 4\n\t" \
  "s_memrealtime s[86:87]\n\t" \
  "s_nop 4\n\t" \
  "s_memrealtime s[88:89]\n\t" \
  "s_nop 4\n\t" \
  "s_memrealtime s[90:91]\n\t" \
  "s_mov_b32 s92, 0\n\t" \
  "s_waitcnt lgkmcnt(0)\n\t" \
  "s_cmp_eq_u32 s82, s84\n\t" \
  "s_addc_u32 s92, s92, 0\n\t" \
  "s_cmp_eq_u32 s82, s56\n\t" \
  "s_addc_u32 s92, s92, 0\n\t" \
  "s_cmp_eq_u32 s82, s86\n\t" \
  "s_addc_u32 s92, s92, 0\n\t" \
  "s_cmp_eq_u32 s82, s88\n\t" \
  "s_addc_u32 s92, s92, 0\n\t" \
  "s_cmp_eq_u32 s82, s90\n\t" \
  "s_addc_u32 s92, s92, 0\n\t" \
  "s_mul_i32 s92, s92, 24\n\t" \
  "s_add_i32 s92, s92, vcc_lo\n\t" \
  "s_lshl4_add_u32 s93, s82, 64\n\t" \
  "3:\n\t" \
  "v_mov_b64 v[72:73], 0\n\t" \
  "v_mov_b64 v[74:75], 0\n\t" \
  "v_mov_b64 v[76:77], 0\n\t" \
  "v_mov_b64 v[78:79], 0\n\t" \
  "s_waitcnt lgkmcnt(0)\n\t" \
  "s_waitcnt vmcnt(3)\n\t" \
  "v_smfmac_f32_16x16x64_bf16 v[72:75], v[32:35], v[48:55], v104\n\t" \
  "ds_write_b128 v101, v[32:35] offset:0\n\t" \
  "v_smfmac_f32_16x16x64_bf16 v[76:79], v[32:35], v[56:63], v104\n\t" \
  "s_waitcnt vmcnt(2)\n\t" \
  "ds_write_b128 v101, v[36:39] offset:1024\n\t" \
  "v_smfmac_f32_16x16x64_bf16 v[72:75], v[36:39], a[0:7], v104\n\t" \
  "v_smfmac_f32_16x16x64_bf16 v[76:79], v[36:39], a[8:15], v104\n\t" \
  "s_nop 3\n\t" \
  "v_cmp_u_f32 vcc, v72, v73\n\t" \
  "v_cmp_u_f32 s[82:83], v74, v75\n\t" \
  "s_waitcnt vmcnt(1)\n\t" \
  "v_smfmac_f32_16x16x64_bf16 v[72:75], v[40:43], a[16:23], v104\n\t" \
  "s_or_b64 vcc, vcc, s[82:83]\n\t" \
  "s_or_b64 vcc, vcc, s[80:81]\n\t" \
  "v_smfmac_f32_16x16x64_bf16 v[76:79], v[40:43], a[24:31], v104\n\t" \
  "s_waitcnt lgkmcnt(0)\n\t" \
  "s_cbranch_scc1 8f\n\t" \
  "5:\n\t" \
  "s_barrier\n\t" \
  "4:\n\t" \
  "s_memtime s[56:57]\n\t" \
  "ds_read_b128 v[48:51], v101 offset:4096\n\t" \
  "ds_read_b128 v[52:55], v101 offset:5120\n\t" \
  "ds_read_b128 v[56:59], v101 offset:9216\n\t" \
  "s_waitcnt vmcnt(0)\n\t" \
  "v_smfmac_f32_16x16x64_bf16 v[72:75], v[44:47], a[32:39], v104\n\t" \
  "ds_read_b128 v[60:63], v101 offset:8192\n\t" \
  "ds_read_b128 v[64:67], v101 offset:13312\n\t" \
  "v_smfmac_f32_16x16x64_bf16 v[76:79], v[44:47], a[40:47], v104\n\t" \
  "ds_read_b128 v[68:71], v101 offset:12288\n\t" \
  "ds_write_b128 v101, v[40:43] offset:2048\n\t" \
  "s_nop 1\n\t" \
  "v_cmp_u_f32 vcc, v72, v73\n\t" \
  "v_cmp_u_f32 s[82:83], v74, v75\n\t" \
  "s_waitcnt lgkmcnt(6)\n\t" \
  "v_smfmac_f32_16x16x64_bf16 v[72:75], v[48:51], v[0:7], v104\n\t" \
  "s_or_b64 vcc, vcc, s[82:83]\n\t" \
  "v_smfmac_f32_16x16x64_bf16 v[76:79], v[48:51], v[8:15], v104\n\t" \
  "s_cbranch_scc1 9f\n\t" \
  "6:\n\t" \
  "s_waitcnt lgkmcnt(5)\n\t" \
  "v_smfmac_f32_16x16x64_bf16 v[72:75], v[52:55], a[48:55], v104\n\t" \
  "ds_write_b128 v101, v[44:47] offset:3072\n\t" \
  "v_smfmac_f32_16x16x64_bf16 v[76:79], v[52:55], a[56:63], v104\n\t" \
  "v_mov_b64 v[40:41], 0\n\t" \
  "s_waitcnt lgkmcnt(5)\n\t" \
  "v_smfmac_f32_16x16x64_bf16 v[72:75], v[56:59], a[96:103], v104\n\t" \
  "v_mov_b64 v[42:43], 0\n\t" \
  "v_smfmac_f32_16x16x64_bf16 v[76:79], v[56:59], a[104:111], v104\n\t" \
  "s_waitcnt lgkmcnt(4)\n\t" \
  "v_smfmac_f32_16x16x64_bf16 v[72:75], v[60:63], v[16:23], v104\n\t" \
  "s_waitcnt lgkmcnt(0)\n\t" \
  "s_barrier\n\t" \
  "v_smfmac_f32_16x16x64_bf16 v[76:79], v[60:63], v[24:31], v104\n\t" \
  "ds_read_b128 v[48:51], v101 offset:6144\n\t" \
  "ds_read_b128 v[52:55], v101 offset:10240\n\t" \
  "v_smfmac_f32_16x16x64_bf16 v[72:75], v[64:67], a[144:151], v104\n\t" \
  "ds_read_b128 v[56:59], v101 offset:14336\n\t" \
  "ds_read_b128 v[60:63], v101 offset:7168\n\t" \
  "v_smfmac_f32_16x16x64_bf16 v[76:79], v[64:67], a[152:159], v104\n\t" \
  "ds_read_b128 v[32:35], v101 offset:11264\n\t" \
  "ds_read_b128 v[36:39], v101 offset:15360\n\t" \
  "v_smfmac_f32_16x16x64_bf16 v[72:75], v[68:71], v[80:87], v104\n\t" \
  "ds_read_b128 v[0:3], v103 offset:0\n\t" \
  "v_mov_b64 v[44:45], 0\n\t" \
  "v_smfmac_f32_16x16x64_bf16 v[76:79], v[68:71], v[88:95], v104\n\t" \
  "v_mov_b64 v[46:47], 0\n\t" \
  "s_waitcnt lgkmcnt(5)\n\t" \
  "v_smfmac_f32_16x16x64_bf16 v[72:75], v[48:51], a[64:71], v104\n\t" \
  "ds_read_b128 v[4:7], v103 offset:1024\n\t" \
  "v_mov_b64 v[80:81], 0\n\t" \
  "v_smfmac_f32_16x16x64_bf16 v[72:75], v[52:55], a[112:119], v104\n\t" \
  "v_mov_b64 v[82:83], 0\n\t" \
  "s_waitcnt lgkmcnt(2)\n\t" \
  "v_smfmac_f32_16x16x64_bf16 v[72:75], v[56:59], a[160:167], v104\n\t" \
  "ds_read_b128 v[8:11], v103 offset:2048\n\t" \
  "v_mov_b64 v[84:85], 0\n\t" \
  "v_smfmac_f32_16x16x64_bf16 v[72:75], v[60:63], a[80:87], v104\n\t" \
  "ds_read_b128 v[12:15], v103 offset:3072\n\t" \
  "v_mov_b64 v[86:87], 0\n\t" \
  "v_smfmac_f32_16x16x64_bf16 v[72:75], v[32:35], a[128:135], v104\n\t" \
  "ds_read_b128 v[16:19], v103 offset:16384\n\t" \
  "v_mov_b64 v[88:89], 0\n\t" \
  "v_smfmac_f32_16x16x64_bf16 v[72:75], v[36:39], a[176:183], v104\n\t" \
  "ds_read_b128 v[20:23], v103 offset:17408\n\t" \
  "v_mov_b64 v[90:91], 0\n\t" \
  "v_smfmac_f32_16x16x64_bf16 v[76:79], v[48:51], a[72:79], v104\n\t" \
  "ds_read_b128 v[24:27], v103 offset:18432\n\t" \
  "v_mov_b64 v[92:93], 0\n\t" \
  "v_smfmac_f32_16x16x64_bf16 v[76:79], v[52:55], a[120:127], v104\n\t" \
  "v_add_f32 v96, v72, v74\n\t" \
  "v_add_f32 v97, v73, v75\n\t" \
  "v_cvt_pk_bf16_f32 v96, v96, v97\n\t" \
  "ds_write_b32 v105, v96\n\t" \
  "v_smfmac_f32_16x16x64_bf16 v[76:79], v[56:59], a[168:175], v104\n\t" \
  "ds_read_b128 v[28:31], v103 offset:19456\n\t" \
  "v_mov_b64 v[94:95], 0\n\t" \
  "v_smfmac_f32_16x16x64_bf16 v[76:79], v[60:63], a[88:95], v104\n\t" \
  "v_smfmac_f32_16x16x64_bf16 v[76:79], v[32:35], a[136:143], v104\n\t" \
  "v_smfmac_f32_16x16x64_bf16 v[76:79], v[36:39], a[184:191], v104\n\t" \
  "s_waitcnt lgkmcnt(1)\n\t" \
  "s_barrier\n\t" \
  "ds_read_b64_tr_b16 v[96:97], v107 offset:0\n\t" \
  "ds_read_b64_tr_b16 v[98:99], v107 offset:64\n\t" \
  "ds_read_b64_tr_b16 v[56:57], v107 offset:1024\n\t" \
  "ds_read_b64_tr_b16 v[58:59], v107 offset:1088\n\t" \
  "s_nop 1\n\t" \
  "v_add_f32 v116, v76, v78\n\t" \
  "v_add_f32 v117, v77, v79\n\t" \
  "v_cvt_pk_bf16_f32 v116, v116, v117\n\t" \
  "ds_write_b32 v105, v116 offset:256\n\t" \
  "s_waitcnt lgkmcnt(3)\n\t" \
  "v_smfmac_f32_16x16x64_bf16 v[40:43], v[96:99], a[192:199], v106\n\t" \
  "v_smfmac_f32_16x16x64_bf16 v[44:47], v[96:99], a[224:231], v106\n\t" \
  "s_waitcnt lgkmcnt(0)\n\t" \
  "s_barrier\n\t" \
  "ds_read_b64_tr_b16 v[52:53], v107 offset:256\n\t" \
  "ds_read_b64_tr_b16 v[54:55], v107 offset:320\n\t" \
  "v_smfmac_f32_16x16x64_bf16 v[40:43], v[56:59], a[208:215], v106\n\t" \
  "ds_read_b64_tr_b16 v[60:61], v107 offset:1280\n\t" \
  "ds_read_b64_tr_b16 v[62:63], v107 offset:1344\n\t" \
  "v_smfmac_f32_16x16x64_bf16 v[44:47], v[56:59], v[0:7], v106\n\t" \
  "v_smfmac_f32_16x16x64_bf16 v[80:83], v[96:99], a[240:247], v106\n\t" \
  "v_mov_b64 v[32:33], 0\n\t" \
  "v_smfmac_f32_16x16x64_bf16 v[84:87], v[96:99], v[136:143], v106\n\t" \
  "v_mov_b64 v[34:35], 0\n\t" \
  "v_smfmac_f32_16x16x64_bf16 v[80:83], v[56:59], v[120:127], v106\n\t" \
  "v_mov_b64 v[36:37], 0\n\t" \
  "v_smfmac_f32_16x16x64_bf16 v[84:87], v[56:59], v[16:23], v106\n\t" \
  "s_waitcnt lgkmcnt(2)\n\t" \
  "v_smfmac_f32_16x16x64_bf16 v[40:43], v[52:55], a[200:207], v106\n\t" \
  "v_smfmac_f32_16x16x64_bf16 v[44:47], v[52:55], a[232:239], v106\n\t" \
  "s_xor_b32 s76, s47, 0x40000\n\t" \
  "s_waitcnt lgkmcnt(0)\n\t" \
  "v_smfmac_f32_16x16x64_bf16 v[40:43], v[60:63], a[216:223], v106\n\t" \
  "v_mov_b64 v[38:39], 0\n\t" \
  "s_xor_b32 s77, s47, 0x80000\n\t" \
  "v_smfmac_f32_16x16x64_bf16 v[44:47], v[60:63], v[8:15], v106\n\t" \
  "s_xor_b32 s78, s47, 0xc0000\n\t" \
  "v_smfmac_f32_16x16x64_bf16 v[80:83], v[52:55], a[248:255], v106\n\t" \
  "ds_read_b128 v[0:3], v103 offset:32768\n\t" \
  "v_cvt_pk_bf16_f32 v64, v40, v41\n\t" \
  "v_smfmac_f32_16x16x64_bf16 v[84:87], v[52:55], v[144:151], v106\n\t" \
  "ds_read_b128 v[4:7], v103 offset:33792\n\t" \
  "v_cvt_pk_bf16_f32 v65, v42, v43\n\t" \
  "v_smfmac_f32_16x16x64_bf16 v[80:83], v[60:63], v[128:135], v106\n\t" \
  "ds_read_b128 v[8:11], v103 offset:34816\n\t" \
  "v_cvt_pk_bf16_f32 v66, v44, v45\n\t" \
  "v_smfmac_f32_16x16x64_bf16 v[84:87], v[60:63], v[24:31], v106\n\t" \
  "ds_read_b128 v[12:15], v103 offset:35840\n\t" \
  "v_cvt_pk_bf16_f32 v67, v46, v47\n\t" \
  "v_smfmac_f32_16x16x64_bf16 v[88:91], v[96:99], v[152:159], v106\n\t" \
  "v_permlane16_swap_b32 v64, v66\n\t" \
  "v_cvt_pk_bf16_f32 v68, v80, v81\n\t" \
  "v_smfmac_f32_16x16x64_bf16 v[92:95], v[96:99], v[184:191], v106\n\t" \
  "v_permlane16_swap_b32 v65, v67\n\t" \
  "buffer_store_dwordx4 v[64:67], v108, %[ru], s47 offen sc1\n\t" \
  "v_cvt_pk_bf16_f32 v69, v82, v83\n\t" \
  "v_smfmac_f32_16x16x64_bf16 v[88:91], v[52:55], v[160:167], v106\n\t" \
  "v_cvt_pk_bf16_f32 v70, v84, v85\n\t" \
  "v_smfmac_f32_16x16x64_bf16 v[92:95], v[52:55], v[192:199], v106\n\t" \
  "v_cvt_pk_bf16_f32 v71, v86, v87\n\t" \
  "v_smfmac_f32_16x16x64_bf16 v[88:91], v[56:59], v[168:175], v106\n\t" \
  "ds_read_b128 v[16:19], v103 offset:49152\n\t" \
  "v_permlane16_swap_b32 v68, v70\n\t" \
  "s_waitcnt lgkmcnt(3)\n\t" \
  "v_smfmac_f32_16x16x64_bf16 v[92:95], v[56:59], v[0:7], v106\n\t" \
  "ds_read_b128 v[20:23], v103 offset:50176\n\t" \
  "v_permlane16_swap_b32 v69, v71\n\t" \
  "buffer_store_dwordx4 v[68:71], v108, %[ru], s76 offen sc1\n\t" \
  "v_smfmac_f32_16x16x64_bf16 v[88:91], v[60:63], v[176:183], v106\n\t" \
  "ds_read_b128 v[24:27], v103 offset:51200\n\t" \
  "s_waitcnt lgkmcnt(3)\n\t" \
  "v_smfmac_f32_16x16x64_bf16 v[92:95], v[60:63], v[8:15], v106\n\t" \
  "ds_read_b128 v[28:31], v103 offset:52224\n\t" \
  "v_smfmac_f32_16x16x64_bf16 v[32:35], v[96:99], v[200:207], v106\n\t" \
  "v_cvt_pk_bf16_f32 v72, v88, v89\n\t" \
  "v_smfmac_f32_16x16x64_bf16 v[36:39], v[96:99], v[232:239], v106\n\t" \
  "v_cvt_pk_bf16_f32 v73, v90, v91\n\t" \
  "v_smfmac_f32_16x16x64_bf16 v[32:35], v[52:55], v[208:215], v106\n\t" \
  "v_cvt_pk_bf16_f32 v74, v92, v93\n\t" \
  "v_smfmac_f32_16x16x64_bf16 v[36:39], v[52:55], v[240:247], v106\n\t" \
  "v_cvt_pk_bf16_f32 v75, v94, v95\n\t" \
  "v_smfmac_f32_16x16x64_bf16 v[32:35], v[56:59], v[216:223], v106\n\t" \
  "v_permlane16_swap_b32 v72, v74\n\t" \
  "v_smfmac_f32_16x16x64_bf16 v[32:35], v[60:63], v[224:231], v106\n\t" \
  "v_permlane16_swap_b32 v73, v75\n\t" \
  "buffer_store_dwordx4 v[72:75], v108, %[ru], s77 offen sc1\n\t" \
  "s_waitcnt lgkmcnt(2)\n\t" \
  "v_smfmac_f32_16x16x64_bf16 v[36:39], v[56:59], v[16:23], v106\n\t" \
  "s_waitcnt lgkmcnt(0)\n\t" \
  "v_smfmac_f32_16x16x64_bf16 v[36:39], v[60:63], v[24:31], v106\n\t" \
  "v_cvt_pk_bf16_f32 v76, v32, v33\n\t" \
  "v_cvt_pk_bf16_f32 v77, v34, v35\n\t" \
  "s_nop 5\n\t" \
  "v_cvt_pk_bf16_f32 v78, v36, v37\n\t" \
  "v_cvt_pk_bf16_f32 v79, v38, v39\n\t" \
  "s_nop 0\n\t" \
  "v_permlane16_swap_b32 v76, v78\n\t" \
  "v_permlane16_swap_b32 v77, v79\n\t" \
  "buffer_store_dwordx4 v[76:79], v108, %[ru], s78 offen sc1\n\t" \
  "s_memtime s[76:77]\n\t" \
  "s_mov_b32 s42, 0\n\t" \
  "s_waitcnt lgkmcnt(0)\n\t" \
  "s_mov_b32 s57, s76\n\t" \
  "s_add_i32 s41, s56, 1950\n\t" \
  "s_sub_i32 s76, s92, s51\n\t" \
  "s_sub_i32 s77, s93, s53\n\t" \
  "s_cmp_eq_u32 s76, 0\n\t" \
  "s_cbranch_scc1 21f\n\t" \
  "v_cvt_f32_u32 v117, s76\n\t" \
  "v_cvt_f32_u32 v116, s77\n\t" \
  "v_rcp_f32 v117, v117\n\t" \
  "s_mov_b32 s51, s92\n\t" \
  "v_mul_f32 v117, v117, v116\n\t" \
  "v_max_f32 v117, 0x3f028f5c, v117\n\t" \
  "v_min_f32 v117, 0x3ffeb852, v117\n\t" \
  "v_rcp_f32 v116, v117\n\t" \
  "v_mul_f32 v117, 0x4f000000, v117\n\t" \
  "v_mul_f32 v116, 0x47800000, v116\n\t" \
  "v_cvt_u32_f32 v117, v117\n\t" \
  "v_cvt_u32_f32 v116, v116\n\t" \
  "s_mov_b32 s53, s93\n\t" \
  "v_readfirstlane_b32 s46, v117\n\t" \
  "v_readfirstlane_b32 s52, v116\n\t" \
  "21:\n\t" \
  "s_add_i32 s44, s44, 1\n\t" \
  "s_and_b32 s58, s58, 4\n\t" \
  "s_and_b32 s76, s44, 64\n\t" \
  "s_or_b32 s42, s42, s76\n\t" \
  "s_cbranch_scc0 20b\n\t" \
  "s_branch 99f\n\t" \
  "14:\n\t" \
  "s_add_u32 s59, s59, 0x10000\n\t" \
  "s_cmp_lt_u32 s59, 0x100000\n\t" \
  "s_cbranch_scc1 13b\n\t" \
  "s_mov_b64 exec, -1\n\t" \
  "s_mov_b32 s42, 16\n\t" \
  "s_branch 99f\n\t" \
  "8:\n\t" \
  "s_bitcmp1_b32 s58, 1\n\t" \
  "s_cbranch_scc1 4b\n\t" \
  "s_bitcmp1_b32 s58, 2\n\t" \
  "s_cbranch_scc1 5b\n\t" \
  "v_max3_u32 v116, v32, v33, v34\n\t" \
  "v_max3_u32 v117, v35, v36, v37\n\t" \
  "v_max3_u32 v116, v116, v38, v39\n\t" \
  "v_max_u32 v116, v116, v117\n\t" \
  "v_cmp_eq_u32 vcc, -1, v116\n\t" \
  "s_cbranch_vccz 5b\n\t" \
  "s_branch 0f\n\t" \
  "9:\n\t" \
  "s_bitcmp1_b32 s58, 2\n\t" \
  "s_cbranch_scc1 6b\n\t" \
  "v_max3_u32 v116, v40, v41, v42\n\t" \
  "v_max3_u32 v117, v43, v44, v45\n\t" \
  "v_max3_u32 v116, v116, v46, v47\n\t" \
  "v_max_u32 v116, v116, v117\n\t" \
  "v_cmp_eq_u32 vcc, -1, v116\n\t" \
  "s_cbranch_vccz 6b\n\t" \
  "s_or_b32 s58, s58, 2\n\t" \
  "0:\n\t" \
  "s_or_b32 s58, s58, 1\n\t" \
  "11:\n\t" \
  "buffer_load_dwordx4 v[32:35], v100, %[rh], s40 offen sc1\n\t" \
  "buffer_load_dwordx4 v[36:39], v100, %[rh], s76 offen sc1\n\t" \
  "buffer_load_dwordx4 v[40:43], v100, %[rh], s77 offen sc1\n\t" \
  "buffer_load_dwordx4 v[44:47], v100, %[rh], s78 offen sc1\n\t" \
  "s_waitcnt lgkmcnt(0)\n\t" \
  "s_branch 10b\n\t" \
  "17:\n\t" \
  "s_add_u32 s59, s59, 1\n\t" \
  "s_bitcmp1_b32 s58, 2\n\t" \
  "s_cbranch_scc1 3b\n\t" \
  "s_waitcnt vmcnt(0)\n\t" \
  "v_max_u32 v116, v32, v33\n\t" \
  "v_max3_u32 v116, v116, v34, v35\n\t" \
  "v_max3_u32 v116, v116, v36, v37\n\t" \
  "v_max3_u32 v116, v116, v38, v39\n\t" \
  "v_max3_u32 v116, v116, v40, v41\n\t" \
  "v_max3_u32 v116, v116, v42, v43\n\t" \
  "v_max3_u32 v116, v116, v44, v45\n\t" \
  "v_max3_u32 v116, v116, v46, v47\n\t" \
  "v_cmp_eq_u32 vcc, -1, v116\n\t" \
  "s_cbranch_vccz 3b\n\t" \
  "s_bitcmp1_b32 s59, 6\n\t" \
  "s_cbranch_scc1 12f\n\t" \
  "buffer_load_dwordx4 v[32:35], v100, %[rh], s40 offen sc1\n\t" \
  "buffer_load_dwordx4 v[36:39], v100, %[rh], s76 offen sc1\n\t" \
  "buffer_load_dwordx4 v[40:43], v100, %[rh], s77 offen sc1\n\t" \
  "buffer_load_dwordx4 v[44:47], v100, %[rh], s78 offen sc1\n\t" \
  "s_branch 17b\n\t" \
  "12:\n\t" \
  "s_mov_b32 s42, s58\n\t" \
  "s_branch 99f\n\t" \
  "91:\n\t" \
  "s_mov_b32 s58, s42\n\t" \
  "20:\n\t" \
  "s_and_b32 s76, s44, 7\n\t" \
  "s_lshl_b32 s76, s76, 15\n\t" \
  "s_add_i32 s40, s54, s76\n\t" \
  "s_and_b32 s76, s44, 3\n\t" \
  "s_lshl_b32 s45, s76, 14\n\t" \
  "s_lshl_b32 s47, s76, 20\n\t" \
  "s_add_i32 s47, s47, s55\n\t" \
  "s_lshr_b32 s76, s47, 21\n\t" \
  "s_bitcmp1_b64 %[sel], s76\n\t" \
  "s_cselect_b32 s76, 0x2000, 0\n\t" \
  "s_add_i32 s47, s47, s76\n\t" \
  "s_add_i32 s49, s44, -1\n\t" \
  "s_and_b32 s48, s49, 2\n\t" \
  "s_lshl_b32 s48, s48, 13\n\t" \
  "s_and_b32 s76, s49, 1\n\t" \
  "s_lshl_b32 s76, s76, 12\n\t" \
  "s_add_i32 s48, s48, s76\n\t" \
  "s_lshl_b32 s50, s53, 8\n\t" \
  "s_mov_b32 s59, 0\n\t" \
  "s_mov_b32 s92, s51\n\t" \
  "s_mov_b32 s93, s53\n\t" \
  "s_xor_b32 s76, s40, 0x400\n\t" \
  "s_xor_b32 s77, s40, 0x800\n\t" \
  "s_xor_b32 s78, s40, 0xc00\n\t" \
  "s_cmp_lg_u32 s58, 0\n\t" \
  "s_cbranch_scc1 11f\n\t" \
  "10:\n\t" \
  "ds_read_b128 v[48:51], v102 offset:16384\n\t" \
  "ds_read_b128 v[52:55], v102 offset:17408\n\t" \
  "ds_read_b128 v[56:59], v102 offset:18432\n\t" \
  "ds_read_b128 v[60:63], v102 offset:19456\n\t" \
  "ds_read_b128 v[0:3], v102 offset:32768\n\t" \
  "ds_read_b128 v[4:7], v102 offset:33792\n\t" \
  "ds_read_b128 v[8:11], v102 offset:34816\n\t" \
  "ds_read_b128 v[12:15], v102 offset:35840\n\t" \
  "ds_read_b128 v[16:19], v102 offset:49152\n\t" \
  "ds_read_b128 v[20:23], v102 offset:50176\n\t" \
  "ds_read_b128 v[24:27], v102 offset:51200\n\t" \
  "ds_read_b128 v[28:31], v102 offset:52224\n\t" \
  "s_waitcnt lgkmcnt(4)\n\t" \
  "ds_read_b128 v[80:83], v102 offset:0\n\t" \
  "ds_read_b128 v[84:87], v102 offset:1024\n\t" \
  "ds_read_b128 v[88:91], v102 offset:2048\n\t" \
  "ds_read_b128 v[92:95], v102 offset:3072\n\t" \
  "s_bitcmp1_b32 s58, 1\n\t" \
  "s_cselect_b64 s[80:81], -1, 0\n\t" \
  "s_cmp_lg_u32 s58, 0\n\t" \
  "s_cbranch_scc1 17f\n\t" \
  "s_mov_b32 exec_hi, 0\n\t" \
  "16:\n\t" \
  "s_memtime vcc\n\t" \
  "s_waitcnt lgkmcnt(0)\n\t" \
  "s_sub_i32 s79, s41, vcc_lo\n\t" \
  "s_cmp_gt_i32 s79, 64\n\t" \
  "s_cbranch_scc1 16b\n\t" \
  "13:\n\t" \
  "buffer_load_dword v116, v112, %[rh], s48 offen sc1\n\t" \
  "s_waitcnt vmcnt(0)\n\t" \
  "15:\n\t" \
  "v_mov_b32 v117, 20000\n\t" \
  "v_cmp_ne_u32_sdwa vcc, v116, s49 src0_sel:BYTE_0 src1_sel:DWORD\n\t" \
  "v_subrev_u32 v116, s50, v116\n\t" \
  "v_ashrrev_i32 v116, 8, v116\n\t" \
  "s_nop 1\n\t" \
  "v_max_i32_dpp v116, v116, v116 quad_perm:[1,0,3,2] row_mask:0xf bank_mask:0xf\n\t" \
  "s_cbranch_vccnz 14f\n\t" \
  "s_nop 0\n\t" \
  "v_max_i32_dpp v116, v116, v116 quad_perm:[2,3,0,1] row_mask:0xf bank_mask:0xf\n\t" \
  "s_nop 1\n\t" \
  "v_max_i32_dpp v116, v116, v116 row_half_mirror row_mask:0xf bank_mask:0xf\n\t" \
  "s_nop 1\n\t" \
  "v_max_i32_dpp v116, v116, v116 row_mirror row_mask:0xf bank_mask:0xf\n\t" \
  "v_med3_i32 v116, v116, 0, v117\n\t" \
  "v_mul_u32_u24 v116, s52, v116\n\t" \
  "v_lshrrev_b32 v116, 16, v116\n\t" \
  "s_mov_b64 exec, -1\n\t" \
  "v_readfirstlane_b32 s84, v116\n\t" \
  "v_readlane_b32 s85, v116, 16\n\t" \
  "s_max_u32 s84, s84, s85\n\t" \
  "s_add_i32 s84, s84, s51\n\t" \
  "1:\n\t" \
  "s_memtime vcc\n\t" \
  "s_waitcnt lgkmcnt(0)\n\t" \
  "s_sub_i32 s79, s84, vcc_lo\n\t" \
  "s_cmp_gt_i32 s79, 128\n\t" \
  "s_cbranch_scc1 1b\n\t" \
  ".p2align 4\n\t" \
  "2:\n\t" \
  "s_sub_i32 s79, s79, 33\n\t" \
  "s_cmp_gt_i32 s79, 56\n\t" \
  "s_cbranch_scc1 2b\n\t" \
  "buffer_load_dwordx4 v[32:35], v100, %[rh], s40 offen sc1\n\t" \
  "s_nop 12\n\t" \
  "buffer_load_dwordx4 v[36:39], v100, %[rh], s76 offen sc1\n\t" \
  "s_nop 12\n\t" \
  "buffer_load_dwordx4 v[40:43], v100, %[rh], s77 offen sc1\n\t" \
  "s_nop 12\n\t" \
  "buffer_load_dwordx4 v[44:47], v100, %[rh], s78 offen sc1\n\t" \
  "s_memtime vcc\n\t" \
  "s_memrealtime s[82:83]\n\t" \
  "s_nop 4\n\t" \
  "s_memrealtime s[84:85]\n\t" \
  "s_nop 4\n\t" \
  "s_memrealtime s[56:57]\n\t" \
  "s_nop 4\n\t" \
  "s_memrealtime s[86:87]\n\t" \
  "s_nop 4\n\t" \
  "s_memrealtime s[88:89]\n\t" \
  "s_nop 4\n\t" \
  "s_memrealtime s[90:91]\n\t" \
  "s_mov_b32 s92, 0\n\t" \
  "s_waitcnt lgkmcnt(0)\n\t" \
  "s_cmp_eq_u32 s82, s84\n\t" \
  "s_addc_u32 s92, s92, 0\n\t" \
  "s_cmp_eq_u32 s82, s56\n\t" \
  "s_addc_u32 s92, s92, 0\n\t" \
  "s_cmp_eq_u32 s82, s86\n\t" \
  "s_addc_u32 s92, s92, 0\n\t" \
  "s_cmp_eq_u32 s82, s88\n\t" \
  "s_addc_u32 s92, s92, 0\n\t" \
  "s_cmp_eq_u32 s82, s90\n\t" \
  "s_addc_u32 s92, s92, 0\n\t" \
  "s_mul_i32 s92, s92, 24\n\t" \
  "s_add_i32 s92, s92, vcc_lo\n\t" \
  "s_lshl4_add_u32 s93, s82, 64\n\t" \
  "3:\n\t" \
  "v_mov_b64 v[72:73], 0\n\t" \
  "v_mov_b64 v[74:75], 0\n\t" \
  "v_mov_b64 v[76:77], 0\n\t" \
  "v_mov_b64 v[78:79], 0\n\t" \
  "s_waitcnt lgkmcnt(0)\n\t" \
  "s_waitcnt vmcnt(3)\n\t" \
  "v_smfmac_f32_16x16x64_bf16 v[72:75], v[32:35], v[48:55], v104\n\t" \
  "ds_write_b128 v101, v[32:35] offset:4096\n\t" \
  "v_smfmac_f32_16x16x64_bf16 v[76:79], v[32:35], v[56:63], v104\n\t" \
  "s_waitcnt vmcnt(2)\n\t" \
  "ds_write_b128 v101, v[36:39] offset:5120\n\t" \
  "v_smfmac_f32_16x16x64_bf16 v[72:75], v[36:39], a[48:55], v104\n\t" \
  "v_smfmac_f32_16x16x64_bf16 v[76:79], v[36:39], a[56:63], v104\n\t" \
  "s_nop 3\n\t" \
  "v_cmp_u_f32 vcc, v72, v73\n\t" \
  "v_cmp_u_f32 s[82:83], v74, v75\n\t" \
  "s_waitcnt vmcnt(1)\n\t" \
  "v_smfmac_f32_16x16x64_bf16 v[72:75], v[40:43], a[64:71], v104\n\t" \
  "s_or_b64 vcc, vcc, s[82:83]\n\t" \
  "s_or_b64 vcc, vcc, s[80:81]\n\t" \
  "v_smfmac_f32_16x16x64_bf16 v[76:79], v[40:43], a[72:79], v104\n\t" \
  "s_waitcnt lgkmcnt(0)\n\t" \
  "s_cbranch_scc1 8f\n\t" \
  "5:\n\t" \
  "s_barrier\n\t" \
  "4:\n\t" \
  "s_memtime s[56:57]\n\t" \
  "ds_read_b128 v[48:51], v101 offset:8192\n\t" \
  "ds_read_b128 v[52:55], v101 offset:9216\n\t" \
  "ds_read_b128 v[56:59], v101 offset:13312\n\t" \
  "s_waitcnt vmcnt(0)\n\t" \
  "v_smfmac_f32_16x16x64_bf16 v[72:75], v[44:47], a[80:87], v104\n\t" \
  "ds_read_b128 v[60:63], v101 offset:12288\n\t" \
  "ds_read_b128 v[64:67], v101 offset:1024\n\t" \
  "v_smfmac_f32_16x16x64_bf16 v[76:79], v[44:47], a[88:95], v104\n\t" \
  "ds_read_b128 v[68:71], v101 offset:0\n\t" \
  "ds_write_b128 v101, v[40:43] offset:6144\n\t" \
  "s_nop 1\n\t" \
  "v_cmp_u_f32 vcc, v72, v73\n\t" \
  "v_cmp_u_f32 s[82:83], v74, v75\n\t" \
  "s_waitcnt lgkmcnt(6)\n\t" \
  "v_smfmac_f32_16x16x64_bf16 v[72:75], v[48:51], v[0:7], v104\n\t" \
  "s_or_b64 vcc, vcc, s[82:83]\n\t" \
  "v_smfmac_f32_16x16x64_bf16 v[76:79], v[48:51], v[8:15], v104\n\t" \
  "s_cbranch_scc1 9f\n\t" \
  "6:\n\t" \
  "s_waitcnt lgkmcnt(5)\n\t" \
  "v_smfmac_f32_16x16x64_bf16 v[72:75], v[52:55], a[96:103], v104\n\t" \
  "ds_write_b128 v101, v[44:47] offset:7168\n\t" \
  "v_smfmac_f32_16x16x64_bf16 v[76:79], v[52:55], a[104:111], v104\n\t" \
  "v_mov_b64 v[40:41], 0\n\t" \
  "s_waitcnt lgkmcnt(5)\n\t" \
  "v_smfmac_f32_16x16x64_bf16 v[72:75], v[56:59], a[144:151], v104\n\t" \
  "v_mov_b64 v[42:43], 0\n\t" \
  "v_smfmac_f32_16x16x64_bf16 v[76:79], v[56:59], a[152:159], v104\n\t" \
  "s_waitcnt lgkmcnt(4)\n\t" \
  "v_smfmac_f32_16x16x64_bf16 v[72:75], v[60:63], v[16:23], v104\n\t" \
  "s_waitcnt lgkmcnt(0)\n\t" \
  "s_barrier\n\t" \
  "v_smfmac_f32_16x16x64_bf16 v[76:79], v[60:63], v[24:31], v104\n\t" \
  "ds_read_b128 v[48:51], v101 offset:10240\n\t" \
  "ds_read_b128 v[52:55], v101 offset:14336\n\t" \
  "v_smfmac_f32_16x16x64_bf16 v[72:75], v[64:67], a[0:7], v104\n\t" \
  "ds_read_b128 v[56:59], v101 offset:2048\n\t" \
  "ds_read_b128 v[60:63], v101 offset:11264\n\t" \
  "v_smfmac_f32_16x16x64_bf16 v[76:79], v[64:67], a[8:15], v104\n\t" \
  "ds_read_b128 v[32:35], v101 offset:15360\n\t" \
  "ds_read_b128 v[36:39], v101 offset:3072\n\t" \
  "v_smfmac_f32_16x16x64_bf16 v[72:75], v[68:71], v[80:87], v104\n\t" \
  "ds_read_b128 v[0:3], v103 offset:0\n\t" \
  "v_mov_b64 v[44:45], 0\n\t" \
  "v_smfmac_f32_16x16x64_bf16 v[76:79], v[68:71], v[88:95], v104\n\t" \
  "v_mov_b64 v[46:47], 0\n\t" \
  "s_waitcnt lgkmcnt(5)\n\t" \
  "v_smfmac_f32_16x16x64_bf16 v[72:75], v[48:51], a[112:119], v104\n\t" \
  "ds_read_b128 v[4:7], v103 offset:1024\n\t" \
  "v_mov_b64 v[80:81], 0\n\t" \
  "v_smfmac_f32_16x16x64_bf16 v[72:75], v[52:55], a[160:167], v104\n\t" \
  "v_mov_b64 v[82:83], 0\n\t" \
  "s_waitcnt lgkmcnt(2)\n\t" \
  "v_smfmac_f32_16x16x64_bf16 v[72:75], v[56:59], a[16:23], v104\n\t" \
  "ds_read_b128 v[8:11], v103 offset:2048\n\t" \
  "v_mov_b64 v[84:85], 0\n\t" \
  "v_smfmac_f32_16x16x64_bf16 v[72:75], v[60:63], a[128:135], v104\n\t" \
  "ds_read_b128 v[12:15], v103 offset:3072\n\t" \
  "v_mov_b64 v[86:87], 0\n\t" \
  "v_smfmac_f32_16x16x64_bf16 v[72:75], v[32:35], a[176:183], v104\n\t" \
  "ds_read_b128 v[16:19], v103 offset:16384\n\t" \
  "v_mov_b64 v[88:89], 0\n\t" \
  "v_smfmac_f32_16x16x64_bf16 v[72:75], v[36:39], a[32:39], v104\n\t" \
  "ds_read_b128 v[20:23], v103 offset:17408\n\t" \
  "v_mov_b64 v[90:91], 0\n\t" \
  "v_smfmac_f32_16x16x64_bf16 v[76:79], v[48:51], a[120:127], v104\n\t" \
  "ds_read_b128 v[24:27], v103 offset:18432\n\t" \
  "v_mov_b64 v[92:93], 0\n\t" \
  "v_smfmac_f32_16x16x64_bf16 v[76:79], v[52:55], a[168:175], v104\n\t" \
  "v_add_f32 v96, v72, v74\n\t" \
  "v_add_f32 v97, v73, v75\n\t" \
  "v_cvt_pk_bf16_f32 v96, v96, v97\n\t" \
  "ds_write_b32 v105, v96\n\t" \
  "v_smfmac_f32_16x16x64_bf16 v[76:79], v[56:59], a[24:31], v104\n\t" \
  "ds_read_b128 v[28:31], v103 offset:19456\n\t" \
  "v_mov_b64 v[94:95], 0\n\t" \
  "v_smfmac_f32_16x16x64_bf16 v[76:79], v[60:63], a[136:143], v104\n\t" \
  "v_smfmac_f32_16x16x64_bf16 v[76:79], v[32:35], a[184:191], v104\n\t" \
  "v_smfmac_f32_16x16x64_bf16 v[76:79], v[36:39], a[40:47], v104\n\t" \
  "s_waitcnt lgkmcnt(1)\n\t" \
  "s_barrier\n\t" \
  "ds_read_b64_tr_b16 v[96:97], v107 offset:0\n\t" \
  "ds_read_b64_tr_b16 v[98:99], v107 offset:64\n\t" \
  "ds_read_b64_tr_b16 v[56:57], v107 offset:1024\n\t" \
  "ds_read_b64_tr_b16 v[58:59], v107 offset:1088\n\t" \
  "s_nop 1\n\t" \
  "v_add_f32 v116, v76, v78\n\t" \
  "v_add_f32 v117, v77, v79\n\t" \
  "v_cvt_pk_bf16_f32 v116, v116, v117\n\t" \
  "ds_write_b32 v105, v116 offset:256\n\t" \
  "s_waitcnt lgkmcnt(3)\n\t" \
  "v_smfmac_f32_16x16x64_bf16 v[40:43], v[96:99], a[192:199], v106\n\t" \
  "v_smfmac_f32_16x16x64_bf16 v[44:47], v[96:99], a[224:231], v106\n\t" \
  "s_waitcnt lgkmcnt(0)\n\t" \
  "s_barrier\n\t" \
  "ds_read_b64_tr_b16 v[52:53], v107 offset:256\n\t" \
  "ds_read_b64_tr_b16 v[54:55], v107 offset:320\n\t" \
  "v_smfmac_f32_16x16x64_bf16 v[40:43], v[56:59], a[208:215], v106\n\t" \
  "ds_read_b64_tr_b16 v[60:61], v107 offset:1280\n\t" \
  "ds_read_b64_tr_b16 v[62:63], v107 offset:1344\n\t" \
  "v_smfmac_f32_16x16x64_bf16 v[44:47], v[56:59], v[0:7], v106\n\t" \
  "v_smfmac_f32_16x16x64_bf16 v[80:83], v[96:99], a[240:247], v106\n\t" \
  "v_mov_b64 v[32:33], 0\n\t" \
  "v_smfmac_f32_16x16x64_bf16 v[84:87], v[96:99], v[136:143], v106\n\t" \
  "v_mov_b64 v[34:35], 0\n\t" \
  "v_smfmac_f32_16x16x64_bf16 v[80:83], v[56:59], v[120:127], v106\n\t" \
  "v_mov_b64 v[36:37], 0\n\t" \
  "v_smfmac_f32_16x16x64_bf16 v[84:87], v[56:59], v[16:23], v106\n\t" \
  "s_waitcnt lgkmcnt(2)\n\t" \
  "v_smfmac_f32_16x16x64_bf16 v[40:43], v[52:55], a[200:207], v106\n\t" \
  "v_smfmac_f32_16x16x64_bf16 v[44:47], v[52:55], a[232:239], v106\n\t" \
  "s_xor_b32 s76, s47, 0x40000\n\t" \
  "s_waitcnt lgkmcnt(0)\n\t" \
  "v_smfmac_f32_16x16x64_bf16 v[40:43], v[60:63], a[216:223], v106\n\t" \
  "v_mov_b64 v[38:39], 0\n\t" \
  "s_xor_b32 s77, s47, 0x80000\n\t" \
  "v_smfmac_f32_16x16x64_bf16 v[44:47], v[60:63], v[8:15], v106\n\t" \
  "s_xor_b32 s78, s47, 0xc0000\n\t" \
  "v_smfmac_f32_16x16x64_bf16 v[80:83], v[52:55], a[248:255], v106\n\t" \
  "ds_read_b128 v[0:3], v103 offset:32768\n\t" \
  "v_cvt_pk_bf16_f32 v64, v40, v41\n\t" \
  "v_smfmac_f32_16x16x64_bf16 v[84:87], v[52:55], v[144:151], v106\n\t" \
  "ds_read_b128 v[4:7], v103 offset:33792\n\t" \
  "v_cvt_pk_bf16_f32 v65, v42, v43\n\t" \
  "v_smfmac_f32_16x16x64_bf16 v[80:83], v[60:63], v[128:135], v106\n\t" \
  "ds_read_b128 v[8:11], v103 offset:34816\n\t" \
  "v_cvt_pk_bf16_f32 v66, v44, v45\n\t" \
  "v_smfmac_f32_16x16x64_bf16 v[84:87], v[60:63], v[24:31], v106\n\t" \
  "ds_read_b128 v[12:15], v103 offset:35840\n\t" \
  "v_cvt_pk_bf16_f32 v67, v46, v47\n\t" \
  "v_smfmac_f32_16x16x64_bf16 v[88:91], v[96:99], v[152:159], v106\n\t" \
  "v_permlane16_swap_b32 v64, v66\n\t" \
  "v_cvt_pk_bf16_f32 v68, v80, v81\n\t" \
  "v_smfmac_f32_16x16x64_bf16 v[92:95], v[96:99], v[184:191], v106\n\t" \
  "v_permlane16_swap_b32 v65, v67\n\t" \
  "v_cvt_pk_bf16_f32 v69, v82, v83\n\t" \
  "v_smfmac_f32_16x16x64_bf16 v[88:91], v[52:55], v[160:167], v106\n\t" \
  "buffer_store_dwordx4 v[64:67], v108, %[ru], s47 offen sc1\n\t" \
  "v_cvt_pk_bf16_f32 v70, v84, v85\n\t" \
  "v_smfmac_f32_16x16x64_bf16 v[92:95], v[52:55], v[192:199], v106\n\t" \
  "v_cvt_pk_bf16_f32 v71, v86, v87\n\t" \
  "v_smfmac_f32_16x16x64_bf16 v[88:91], v[56:59], v[168:175], v106\n\t" \
  "ds_read_b128 v[16:19], v103 offset:49152\n\t" \
  "v_permlane16_swap_b32 v68, v70\n\t" \
  "s_waitcnt lgkmcnt(3)\n\t" \
  "v_smfmac_f32_16x16x64_bf16 v[92:95], v[56:59], v[0:7], v106\n\t" \
  "ds_read_b128 v[20:23], v103 offset:50176\n\t" \
  "v_permlane16_swap_b32 v69, v71\n\t" \
  "v_smfmac_f32_16x16x64_bf16 v[88:91], v[60:63], v[176:183], v106\n\t" \
  "ds_read_b128 v[24:27], v103 offset:51200\n\t" \
  "buffer_store_dwordx4 v[68:71], v108, %[ru], s76 offen sc1\n\t" \
  "s_waitcnt lgkmcnt(3)\n\t" \
  "v_smfmac_f32_16x16x64_bf16 v[92:95], v[60:63], v[8:15], v106\n\t" \
  "ds_read_b128 v[28:31], v103 offset:52224\n\t" \
  "v_smfmac_f32_16x16x64_bf16 v[32:35], v[96:99], v[200:207], v106\n\t" \
  "v_cvt_pk_bf16_f32 v72, v88, v89\n\t" \
  "v_smfmac_f32_16x16x64_bf16 v[36:39], v[96:99], v[232:239], v106\n\t" \
  "v_cvt_pk_bf16_f32 v73, v90, v91\n\t" \
  "v_smfmac_f32_16x16x64_bf16 v[32:35], v[52:55], v[208:215], v106\n\t" \
  "v_cvt_pk_bf16_f32 v74, v92, v93\n\t" \
  "v_smfmac_f32_16x16x64_bf16 v[36:39], v[52:55], v[240:247], v106\n\t" \
  "v_cvt_pk_bf16_f32 v75, v94, v95\n\t" \
  "v_smfmac_f32_16x16x64_bf16 v[32:35], v[56:59], v[216:223], v106\n\t" \
  "v_permlane16_swap_b32 v72, v74\n\t" \
  "v_smfmac_f32_16x16x64_bf16 v[32:35], v[60:63], v[224:231], v106\n\t" \
  "v_permlane16_swap_b32 v73, v75\n\t" \
  "s_waitcnt lgkmcnt(2)\n\t" \
  "v_smfmac_f32_16x16x64_bf16 v[36:39], v[56:59], v[16:23], v106\n\t" \
  "buffer_store_dwordx4 v[72:75], v108, %[ru], s77 offen sc1\n\t" \
  "s_waitcnt lgkmcnt(0)\n\t" \
  "v_smfmac_f32_16x16x64_bf16 v[36:39], v[60:63], v[24:31], v106\n\t" \
  "v_cvt_pk_bf16_f32 v76, v32, v33\n\t" \
  "v_cvt_pk_bf16_f32 v77, v34, v35\n\t" \
  "s_nop 5\n\t" \
  "v_cvt_pk_bf16_f32 v78, v36, v37\n\t" \
  "v_cvt_pk_bf16_f32 v79, v38, v39\n\t" \
  "s_nop 0\n\t" \
  "v_permlane16_swap_b32 v76, v78\n\t" \
  "v_permlane16_swap_b32 v77, v79\n\t" \
  "buffer_store_dwordx4 v[76:79], v108, %[ru], s78 offen sc1\n\t" \
  "s_memtime s[76:77]\n\t" \
  "s_mov_b32 s42, 0\n\t" \
  "s_waitcnt lgkmcnt(0)\n\t" \
  "s_mov_b32 s57, s76\n\t" \
  "s_add_i32 s41, s56, 1950\n\t" \
  "s_sub_i32 s76, s92, s51\n\t" \
  "s_sub_i32 s77, s93, s53\n\t" \
  "s_cmp_eq_u32 s76, 0\n\t" \
  "s_cbranch_scc1 21f\n\t" \
  "v_cvt_f32_u32 v117, s76\n\t" \
  "v_cvt_f32_u32 v116, s77\n\t" \
  "v_rcp_f32 v117, v117\n\t" \
  "s_mov_b32 s51, s92\n\t" \
  "v_mul_f32 v117, v117, v116\n\t" \
  "v_max_f32 v117, 0x3f028f5c, v117\n\t" \
  "v_min_f32 v117, 0x3ffeb852, v117\n\t" \
  "v_rcp_f32 v116, v117\n\t" \
  "v_mul_f32 v117, 0x4f000000, v117\n\t" \
  "v_mul_f32 v116, 0x47800000, v116\n\t" \
  "v_cvt_u32_f32 v117, v117\n\t" \
  "v_cvt_u32_f32 v116, v116\n\t" \
  "s_mov_b32 s53, s93\n\t" \
  "v_readfirstlane_b32 s46, v117\n\t" \
  "v_readfirstlane_b32 s52, v116\n\t" \
  "21:\n\t" \
  "s_add_i32 s44, s44, 1\n\t" \
  "s_and_b32 s58, s58, 4\n\t" \
  "s_and_b32 s76, s44, 64\n\t" \
  "s_or_b32 s42, s42, s76\n\t" \
  "s_cbranch_scc0 20b\n\t" \
  "s_branch 99f\n\t" \
  "14:\n\t" \
  "s_add_u32 s59, s59, 0x10000\n\t" \
  "s_cmp_lt_u32 s59, 0x100000\n\t" \
  "s_cbranch_scc1 13b\n\t" \
  "s_mov_b64 exec, -1\n\t" \
  "s_mov_b32 s42, 16\n\t" \
  "s_branch 99f\n\t" \
  "8:\n\t" \
  "s_bitcmp1_b32 s58, 1\n\t" \
  "s_cbranch_scc1 4b\n\t" \
  "s_bitcmp1_b32 s58, 2\n\t" \
  "s_cbranch_scc1 5b\n\t" \
  "v_max3_u32 v116, v32, v33, v34\n\t" \
  "v_max3_u32 v117, v35, v36, v37\n\t" \
  "v_max3_u32 v116, v116, v38, v39\n\t" \
  "v_max_u32 v116, v116, v117\n\t" \
  "v_cmp_eq_u32 vcc, -1, v116\n\t" \
  "s_cbranch_vccz 5b\n\t" \
  "s_branch 0f\n\t" \
  "9:\n\t" \
  "s_bitcmp1_b32 s58, 2\n\t" \
  "s_cbranch_scc1 6b\n\t" \
  "v_max3_u32 v116, v40, v41, v42\n\t" \
  "v_max3_u32 v117, v43, v44, v45\n\t" \
  "v_max3_u32 v116, v116, v46, v47\n\t" \
  "v_max_u32 v116, v116, v117\n\t" \
  "v_cmp_eq_u32 vcc, -1, v116\n\t" \
  "s_cbranch_vccz 6b\n\t" \
  "s_or_b32 s58, s58, 2\n\t" \
  "0:\n\t" \
  "s_or_b32 s58, s58, 1\n\t" \
  "11:\n\t" \
  "buffer_load_dwordx4 v[32:35], v100, %[rh], s40 offen sc1\n\t" \
  "buffer_load_dwordx4 v[36:39], v100, %[rh], s76 offen sc1\n\t" \
  "buffer_load_dwordx4 v[40:43], v100, %[rh], s77 offen sc1\n\t" \
  "buffer_load_dwordx4 v[44:47], v100, %[rh], s78 offen sc1\n\t" \
  "s_waitcnt lgkmcnt(0)\n\t" \
  "s_branch 10b\n\t" \
  "17:\n\t" \
  "s_add_u32 s59, s59, 1\n\t" \
  "s_bitcmp1_b32 s58, 2\n\t" \
  "s_cbranch_scc1 3b\n\t" \
  "s_waitcnt vmcnt(0)\n\t" \
  "v_max_u32 v116, v32, v33\n\t" \
  "v_max3_u32 v116, v116, v34, v35\n\t" \
  "v_max3_u32 v116, v116, v36, v37\n\t" \
  "v_max3_u32 v116, v116, v38, v39\n\t" \
  "v_max3_u32 v116, v116, v40, v41\n\t" \
  "v_max3_u32 v116, v116, v42, v43\n\t" \
  "v_max3_u32 v116, v116, v44, v45\n\t" \
  "v_max3_u32 v116, v116, v46, v47\n\t" \
  "v_cmp_eq_u32 vcc, -1, v116\n\t" \
  "s_cbranch_vccz 3b\n\t" \
  "s_bitcmp1_b32 s59, 6\n\t" \
  "s_cbranch_scc1 12f\n\t" \
  "buffer_load_dwordx4 v[32:35], v100, %[rh], s40 offen sc1\n\t" \
  "buffer_load_dwordx4 v[36:39], v100, %[rh], s76 offen sc1\n\t" \
  "buffer_load_dwordx4 v[40:43], v100, %[rh], s77 offen sc1\n\t" \
  "buffer_load_dwordx4 v[44:47], v100, %[rh], s78 offen sc1\n\t" \
  "s_branch 17b\n\t" \
  "12:\n\t" \
  "s_mov_b32 s42, s58\n\t" \
  "s_branch 99f\n\t" \
  "92:\n\t" \
  "s_mov_b32 s58, s42\n\t" \
  "20:\n\t" \
  "s_and_b32 s76, s44, 7\n\t" \
  "s_lshl_b32 s76, s76, 15\n\t" \
  "s_add_i32 s40, s54, s76\n\t" \
  "s_and_b32 s76, s44, 3\n\t" \
  "s_lshl_b32 s45, s76, 14\n\t" \
  "s_lshl_b32 s47, s76, 20\n\t" \
  "s_add_i32 s47, s47, s55\n\t" \
  "s_lshr_b32 s76, s47, 21\n\t" \
  "s_bitcmp1_b64 %[sel], s76\n\t" \
  "s_cselect_b32 s76, 0x2000, 0\n\t" \
  "s_add_i32 s47, s47, s76\n\t" \
  "s_add_i32 s49, s44, -1\n\t" \
  "s_and_b32 s48, s49, 2\n\t" \
  "s_lshl_b32 s48, s48, 13\n\t" \
  "s_and_b32 s76, s49, 1\n\t" \
  "s_lshl_b32 s76, s76, 12\n\t" \
  "s_add_i32 s48, s48, s76\n\t" \
  "s_lshl_b32 s50, s53, 8\n\t" \
  "s_mov_b32 s59, 0\n\t" \
  "s_mov_b32 s92, s51\n\t" \
  "s_mov_b32 s93, s53\n\t" \
  "s_xor_b32 s76, s40, 0x400\n\t" \
  "s_xor_b32 s77, s40, 0x800\n\t" \
  "s_xor_b32 s78, s40, 0xc00\n\t" \
  "s_cmp_lg_u32 s58, 0\n\t" \
  "s_cbranch_scc1 11f\n\t" \
  "10:\n\t" \
  "ds_read_b128 v[48:51], v102 offset:32768\n\t" \
  "ds_read_b128 v[52:55], v102 offset:33792\n\t" \
  "ds_read_b128 v[56:59], v102 offset:34816\n\t" \
  "ds_read_b128 v[60:63], v102 offset:35840\n\t" \
  "ds_read_b128 v[0:3], v102 offset:49152\n\t" \
  "ds_read_b128 v[4:7], v102 offset:50176\n\t" \
  "ds_read_b128 v[8:11], v102 offset:51200\n\t" \
  "ds_read_b128 v[12:15], v102 offset:52224\n\t" \
  "ds_read_b128 v[16:19], v102 offset:0\n\t" \
  "ds_read_b128 v[20:23], v102 offset:1024\n\t" \
  "ds_read_b128 v[24:27], v102 offset:2048\n\t" \
  "ds_read_b128 v[28:31], v102 offset:3072\n\t" \
  "s_waitcnt lgkmcnt(4)\n\t" \
  "ds_read_b128 v[80:83], v102 offset:16384\n\t" \
  "ds_read_b128 v[84:87], v102 offset:17408\n\t" \
  "ds_read_b128 v[88:91], v102 offset:18432\n\t" \
  "ds_read_b128 v[92:95], v102 offset:19456\n\t" \
  "s_bitcmp1_b32 s58, 1\n\t" \
  "s_cselect_b64 s[80:81], -1, 0\n\t" \
  "s_cmp_lg_u32 s58, 0\n\t" \
  "s_cbranch_scc1 17f\n\t" \
  "s_mov_b32 exec_hi, 0\n\t" \
  "16:\n\t" \
  "s_memtime vcc\n\t" \
  "s_waitcnt lgkmcnt(0)\n\t" \
  "s_sub_i32 s79, s41, vcc_lo\n\t" \
  "s_cmp_gt_i32 s79, 64\n\t" \
  "s_cbranch_scc1 16b\n\t" \
  "13:\n\t" \
  "buffer_load_dword v116, v112, %[rh], s48 offen sc1\n\t" \
  "s_waitcnt vmcnt(0)\n\t" \
  "15:\n\t" \
  "v_mov_b32 v117, 20000\n\t" \
  "v_cmp_ne_u32_sdwa vcc, v116, s49 src0_sel:BYTE_0 src1_sel:DWORD\n\t" \
  "v_subrev_u32 v116, s50, v116\n\t" \
  "v_ashrrev_i32 v116, 8, v116\n\t" \
  "s_nop 1\n\t" \
  "v_max_i32_dpp v116, v116, v116 quad_perm:[1,0,3,2] row_mask:0xf bank_mask:0xf\n\t" \
  "s_cbranch_vccnz 14f\n\t" \
  "s_nop 0\n\t" \
  "v_max_i32_dpp v116, v116, v116 quad_perm:[2,3,0,1] row_mask:0xf bank_mask:0xf\n\t" \
  "s_nop 1\n\t" \
  "v_max_i32_dpp v116, v116, v116 row_half_mirror row_mask:0xf bank_mask:0xf\n\t" \
  "s_nop 1\n\t" \
  "v_max_i32_dpp v116, v116, v116 row_mirror row_mask:0xf bank_mask:0xf\n\t" \
  "v_med3_i32 v116, v116, 0, v117\n\t" \
  "v_mul_u32_u24 v116, s52, v116\n\t" \
  "v_lshrrev_b32 v116, 16, v116\n\t" \
  "s_mov_b64 exec, -1\n\t" \
  "v_readfirstlane_b32 s84, v116\n\t" \
  "v_readlane_b32 s85, v116, 16\n\t" \
  "s_max_u32 s84, s84, s85\n\t" \
  "s_add_i32 s84, s84, s51\n\t" \
  "1:\n\t" \
  "s_memtime vcc\n\t" \
  "s_waitcnt lgkmcnt(0)\n\t" \
  "s_sub_i32 s79, s84, vcc_lo\n\t" \
  "s_cmp_gt_i32 s79, 128\n\t" \
  "s_cbranch_scc1 1b\n\t" \
  ".p2align 4\n\t" \
  "2:\n\t" \
  "s_sub_i32 s79, s79, 33\n\t" \
  "s_cmp_gt_i32 s79, 56\n\t" \
  "s_cbranch_scc1 2b\n\t" \
  "buffer_load_dwordx4 v[32:35], v100, %[rh], s40 offen sc1\n\t" \
  "s_nop 12\n\t" \
  "buffer_load_dwordx4 v[36:39], v100, %[rh], s76 offen sc1\n\t" \
  "s_nop 12\n\t" \
  "buffer_load_dwordx4 v[40:43], v100, %[rh], s77 offen sc1\n\t" \
  "s_nop 12\n\t" \
  "buffer_load_dwordx4 v[44:47], v100, %[rh], s78 offen sc1\n\t" \
  "s_memtime vcc\n\t" \
  "s_memrealtime s[82:83]\n\t" \
  "s_nop 4\n\t" \
  "s_memrealtime s[84:85]\n\t" \
  "s_nop 4\n\t" \
  "s_memrealtime s[56:57]\n\t" \
  "s_nop 4\n\t" \
  "s_memrealtime s[86:87]\n\t" \
  "s_nop 4\n\t" \
  "s_memrealtime s[88:89]\n\t" \
  "s_nop 4\n\t" \
  "s_memrealtime s[90:91]\n\t" \
  "s_mov_b32 s92, 0\n\t" \
  "s_waitcnt lgkmcnt(0)\n\t" \
  "s_cmp_eq_u32 s82, s84\n\t" \
  "s_addc_u32 s92, s92, 0\n\t" \
  "s_cmp_eq_u32 s82, s56\n\t" \
  "s_addc_u32 s92, s92, 0\n\t" \
  "s_cmp_eq_u32 s82, s86\n\t" \
  "s_addc_u32 s92, s92, 0\n\t" \
  "s_cmp_eq_u32 s82, s88\n\t" \
  "s_addc_u32 s92, s92, 0\n\t" \
  "s_cmp_eq_u32 s82, s90\n\t" \
  "s_addc_u32 s92, s92, 0\n\t" \
  "s_mul_i32 s92, s92, 24\n\t" \
  "s_add_i32 s92, s92, vcc_lo\n\t" \
  "s_lshl4_add_u32 s93, s82, 64\n\t" \
  "3:\n\t" \
  "v_mov_b64 v[72:73], 0\n\t" \
  "v_mov_b64 v[74:75], 0\n\t" \
  "v_mov_b64 v[76:77], 0\n\t" \
  "v_mov_b64 v[78:79], 0\n\t" \
  "s_waitcnt lgkmcnt(0)\n\t" \
  "s_waitcnt vmcnt(3)\n\t" \
  "v_smfmac_f32_16x16x64_bf16 v[72:75], v[32:35], v[48:55], v104\n\t" \
  "ds_write_b128 v101, v[32:35] offset:8192\n\t" \
  "v_smfmac_f32_16x16x64_bf16 v[76:79], v[32:35], v[56:63], v104\n\t" \
  "s_waitcnt vmcnt(2)\n\t" \
  "ds_write_b128 v101, v[36:39] offset:9216\n\t" \
  "v_smfmac_f32_16x16x64_bf16 v[72:75], v[36:39], a[96:103], v104\n\t" \
  "v_smfmac_f32_16x16x64_bf16 v[76:79], v[36:39], a[104:111], v104\n\t" \
  "s_nop 3\n\t" \
  "v_cmp_u_f32 vcc, v72, v73\n\t" \
  "v_cmp_u_f32 s[82:83], v74, v75\n\t" \
  "s_waitcnt vmcnt(1)\n\t" \
  "v_smfmac_f32_16x16x64_bf16 v[72:75], v[40:43], a[112:119], v104\n\t" \
  "s_or_b64 vcc, vcc, s[82:83]\n\t" \
  "s_or_b64 vcc, vcc, s[80:81]\n\t" \
  "v_smfmac_f32_16x16x64_bf16 v[76:79], v[40:43], a[120:127], v104\n\t" \
  "s_waitcnt lgkmcnt(0)\n\t" \
  "s_cbranch_scc1 8f\n\t" \
  "5:\n\t" \
  "s_barrier\n\t" \
  "4:\n\t" \
  "s_memtime s[56:57]\n\t" \
  "ds_read_b128 v[48:51], v101 offset:12288\n\t" \
  "ds_read_b128 v[52:55], v101 offset:13312\n\t" \
  "ds_read_b128 v[56:59], v101 offset:1024\n\t" \
  "s_waitcnt vmcnt(0)\n\t" \
  "v_smfmac_f32_16x16x64_bf16 v[72:75], v[44:47], a[128:135], v104\n\t" \
  "ds_read_b128 v[60:63], v101 offset:0\n\t" \
  "ds_read_b128 v[64:67], v101 offset:5120\n\t" \
  "v_smfmac_f32_16x16x64_bf16 v[76:79], v[44:47], a[136:143], v104\n\t" \
  "ds_read_b128 v[68:71], v101 offset:4096\n\t" \
  "ds_write_b128 v101, v[40:43] offset:10240\n\t" \
  "s_nop 1\n\t" \
  "v_cmp_u_f32 vcc, v72, v73\n\t" \
  "v_cmp_u_f32 s[82:83], v74, v75\n\t" \
  "s_waitcnt lgkmcnt(6)\n\t" \
  "v_smfmac_f32_16x16x64_bf16 v[72:75], v[48:51], v[0:7], v104\n\t" \
  "s_or_b64 vcc, vcc, s[82:83]\n\t" \
  "v_smfmac_f32_16x16x64_bf16 v[76:79], v[48:51], v[8:15], v104\n\t" \
  "s_cbranch_scc1 9f\n\t" \
  "6:\n\t" \
  "s_waitcnt lgkmcnt(5)\n\t" \
  "v_smfmac_f32_16x16x64_bf16 v[72:75], v[52:55], a[144:151], v104\n\t" \
  "ds_write_b128 v101, v[44:47] offset:11264\n\t" \
  "v_smfmac_f32_16x16x64_bf16 v[76:79], v[52:55], a[152:159], v104\n\t" \
  "v_mov_b64 v[40:41], 0\n\t" \
  "s_waitcnt lgkmcnt(5)\n\t" \
  "v_smfmac_f32_16x16x64_bf16 v[72:75], v[56:59], a[0:7], v104\n\t" \
  "v_mov_b64 v[42:43], 0\n\t" \
  "v_smfmac_f32_16x16x64_bf16 v[76:79], v[56:59], a[8:15], v104\n\t" \
  "s_waitcnt lgkmcnt(4)\n\t" \
  "v_smfmac_f32_16x16x64_bf16 v[72:75], v[60:63], v[16:23], v104\n\t" \
  "s_waitcnt lgkmcnt(0)\n\t" \
  "s_barrier\n\t" \
  "v_smfmac_f32_16x16x64_bf16 v[76:79], v[60:63], v[24:31], v104\n\t" \
  "ds_read_b128 v[48:51], v101 offset:14336\n\t" \
  "ds_read_b128 v[52:55], v101 offset:2048\n\t" \
  "v_smfmac_f32_16x16x64_bf16 v[72:75], v[64:67], a[48:55], v104\n\t" \
  "ds_read_b128 v[56:59], v101 offset:6144\n\t" \
  "ds_read_b128 v[60:63], v101 offset:15360\n\t" \
  "v_smfmac_f32_16x16x64_bf16 v[76:79], v[64:67], a[56:63], v104\n\t" \
  "ds_read_b128 v[32:35], v101 offset:3072\n\t" \
  "ds_read_b128 v[36:39], v101 offset:7168\n\t" \
  "v_smfmac_f32_16x16x64_bf16 v[72:75], v[68:71], v[80:87], v104\n\t" \
  "ds_read_b128 v[0:3], v103 offset:0\n\t" \
  "v_mov_b64 v[44:45], 0\n\t" \
  "v_smfmac_f32_16x16x64_bf16 v[76:79], v[68:71], v[88:95], v104\n\t" \
  "v_mov_b64 v[46:47], 0\n\t" \
  "s_waitcnt lgkmcnt(5)\n\t" \
  "v_smfmac_f32_16x16x64_bf16 v[72:75], v[48:51], a[160:167], v104\n\t" \
  "ds_read_b128 v[4:7], v103 offset:1024\n\t" \
  "v_mov_b64 v[80:81], 0\n\t" \
  "v_smfmac_f32_16x16x64_bf16 v[72:75], v[52:55], a[16:23], v104\n\t" \
  "v_mov_b64 v[82:83], 0\n\t" \
  "s_waitcnt lgkmcnt(2)\n\t" \
  "v_smfmac_f32_16x16x64_bf16 v[72:75], v[56:59], a[64:71], v104\n\t" \
  "ds_read_b128 v[8:11], v103 offset:2048\n\t" \
  "v_mov_b64 v[84:85], 0\n\t" \
  "v_smfmac_f32_16x16x64_bf16 v[72:75], v[60:63], a[176:183], v104\n\t" \
  "ds_read_b128 v[12:15], v103 offset:3072\n\t" \
  "v_mov_b64 v[86:87], 0\n\t" \
  "v_smfmac_f32_16x16x64_bf16 v[72:75], v[32:35], a[32:39], v104\n\t" \
  "ds_read_b128 v[16:19], v103 offset:16384\n\t" \
  "v_mov_b64 v[88:89], 0\n\t" \
  "v_smfmac_f32_16x16x64_bf16 v[72:75], v[36:39], a[80:87], v104\n\t" \
  "ds_read_b128 v[20:23], v103 offset:17408\n\t" \
  "v_mov_b64 v[90:91], 0\n\t" \
  "v_smfmac_f32_16x16x64_bf16 v[76:79], v[48:51], a[168:175], v104\n\t" \
  "ds_read_b128 v[24:27], v103 offset:18432\n\t" \
  "v_mov_b64 v[92:93], 0\n\t" \
  "v_smfmac_f32_16x16x64_bf16 v[76:79], v[52:55], a[24:31], v104\n\t" \
  "v_add_f32 v96, v72, v74\n\t" \
  "v_add_f32 v97, v73, v75\n\t" \
  "v_cvt_pk_bf16_f32 v96, v96, v97\n\t" \
  "ds_write_b32 v105, v96\n\t" \
  "v_smfmac_f32_16x16x64_bf16 v[76:79], v[56:59], a[72:79], v104\n\t" \
  "ds_read_b128 v[28:31], v103 offset:19456\n\t" \
  "v_mov_b64 v[94:95], 0\n\t" \
  "v_smfmac_f32_16x16x64_bf16 v[76:79], v[60:63], a[184:191], v104\n\t" \
  "v_smfmac_f32_16x16x64_bf16 v[76:79], v[32:35], a[40:47], v104\n\t" \
  "v_smfmac_f32_16x16x64_bf16 v[76:79], v[36:39], a[88:95], v104\n\t" \
  "s_waitcnt lgkmcnt(1)\n\t" \
  "s_barrier\n\t" \
  "ds_read_b64_tr_b16 v[96:97], v107 offset:0\n\t" \
  "ds_read_b64_tr_b16 v[98:99], v107 offset:64\n\t" \
  "ds_read_b64_tr_b16 v[56:57], v107 offset:1024\n\t" \
  "ds_read_b64_tr_b16 v[58:59], v107 offset:1088\n\t" \
  "s_nop 1\n\t" \
  "v_add_f32 v116, v76, v78\n\t" \
  "v_add_f32 v117, v77, v79\n\t" \
  "v_cvt_pk_bf16_f32 v116, v116, v117\n\t" \
  "ds_write_b32 v105, v116 offset:256\n\t" \
  "s_waitcnt lgkmcnt(3)\n\t" \
  "v_smfmac_f32_16x16x64_bf16 v[40:43], v[96:99], a[192:199], v106\n\t" \
  "v_smfmac_f32_16x16x64_bf16 v[44:47], v[96:99], a[224:231], v106\n\t" \
  "s_waitcnt lgkmcnt(0)\n\t" \
  "s_barrier\n\t" \
  "ds_read_b64_tr_b16 v[52:53], v107 offset:256\n\t" \
  "ds_read_b64_tr_b16 v[54:55], v107 offset:320\n\t" \
  "v_smfmac_f32_16x16x64_bf16 v[40:43], v[56:59], a[208:215], v106\n\t" \
  "ds_read_b64_tr_b16 v[60:61], v107 offset:1280\n\t" \
  "ds_read_b64_tr_b16 v[62:63], v107 offset:1344\n\t" \
  "v_smfmac_f32_16x16x64_bf16 v[44:47], v[56:59], v[0:7], v106\n\t" \
  "v_smfmac_f32_16x16x64_bf16 v[80:83], v[96:99], a[240:247], v106\n\t" \
  "v_mov_b64 v[32:33], 0\n\t" \
  "v_smfmac_f32_16x16x64_bf16 v[84:87], v[96:99], v[136:143], v106\n\t" \
  "v_mov_b64 v[34:35], 0\n\t" \
  "v_smfmac_f32_16x16x64_bf16 v[80:83], v[56:59], v[120:127], v106\n\t" \
  "v_mov_b64 v[36:37], 0\n\t" \
  "v_smfmac_f32_16x16x64_bf16 v[84:87], v[56:59], v[16:23], v106\n\t" \
  "s_waitcnt lgkmcnt(2)\n\t" \
  "v_smfmac_f32_16x16x64_bf16 v[40:43], v[52:55], a[200:207], v106\n\t" \
  "v_smfmac_f32_16x16x64_bf16 v[44:47], v[52:55], a[232:239], v106\n\t" \
  "s_xor_b32 s76, s47, 0x40000\n\t" \
  "s_waitcnt lgkmcnt(0)\n\t" \
  "v_smfmac_f32_16x16x64_bf16 v[40:43], v[60:63], a[216:223], v106\n\t" \
  "v_mov_b64 v[38:39], 0\n\t" \
  "s_xor_b32 s77, s47, 0x80000\n\t" \
  "v_smfmac_f32_16x16x64_bf16 v[44:47], v[60:63], v[8:15], v106\n\t" \
  "s_xor_b32 s78, s47, 0xc0000\n\t" \
  "v_smfmac_f32_16x16x64_bf16 v[80:83], v[52:55], a[248:255], v106\n\t" \
  "ds_read_b128 v[0:3], v103 offset:32768\n\t" \
  "v_cvt_pk_bf16_f32 v64, v40, v41\n\t" \
  "v_smfmac_f32_16x16x64_bf16 v[84:87], v[52:55], v[144:151], v106\n\t" \
  "ds_read_b128 v[4:7], v103 offset:33792\n\t" \
  "v_cvt_pk_bf16_f32 v65, v42, v43\n\t" \
  "v_smfmac_f32_16x16x64_bf16 v[80:83], v[60:63], v[128:135], v106\n\t" \
  "ds_read_b128 v[8:11], v103 offset:34816\n\t" \
  "v_cvt_pk_bf16_f32 v66, v44, v45\n\t" \
  "v_smfmac_f32_16x16x64_bf16 v[84:87], v[60:63], v[24:31], v106\n\t" \
  "ds_read_b128 v[12:15], v103 offset:35840\n\t" \
  "v_cvt_pk_bf16_f32 v67, v46, v47\n\t" \
  "v_smfmac_f32_16x16x64_bf16 v[88:91], v[96:99], v[152:159], v106\n\t" \
  "v_permlane16_swap_b32 v64, v66\n\t" \
  "v_cvt_pk_bf16_f32 v68, v80, v81\n\t" \
  "v_smfmac_f32_16x16x64_bf16 v[92:95], v[96:99], v[184:191], v106\n\t" \
  "v_permlane16_swap_b32 v65, v67\n\t" \
  "v_cvt_pk_bf16_f32 v69, v82, v83\n\t" \
  "v_smfmac_f32_16x16x64_bf16 v[88:91], v[52:55], v[160:167], v106\n\t" \
  "v_cvt_pk_bf16_f32 v70, v84, v85\n\t" \
  "v_smfmac_f32_16x16x64_bf16 v[92:95], v[52:55], v[192:199], v106\n\t" \
  "buffer_store_dwordx4 v[64:67], v108, %[ru], s47 offen sc1\n\t" \
  "v_cvt_pk_bf16_f32 v71, v86, v87\n\t" \
  "v_smfmac_f32_16x16x64_bf16 v[88:91], v[56:59], v[168:175], v106\n\t" \
  "ds_read_b128 v[16:19], v103 offset:49152\n\t" \
  "v_permlane16_swap_b32 v68, v70\n\t" \
  "s_waitcnt lgkmcnt(3)\n\t" \
  "v_smfmac_f32_16x16x64_bf16 v[92:95], v[56:59], v[0:7], v106\n\t" \
  "ds_read_b128 v[20:23], v103 offset:50176\n\t" \
  "v_permlane16_swap_b32 v69, v71\n\t" \
  "v_smfmac_f32_16x16x64_bf16 v[88:91], v[60:63], v[176:183], v106\n\t" \
  "ds_read_b128 v[24:27], v103 offset:51200\n\t" \
  "s_waitcnt lgkmcnt(3)\n\t" \
  "v_smfmac_f32_16x16x64_bf16 v[92:95], v[60:63], v[8:15], v106\n\t" \
  "ds_read_b128 v[28:31], v103 offset:52224\n\t" \
  "buffer_store_dwordx4 v[68:71], v108, %[ru], s76 offen sc1\n\t" \
  "v_smfmac_f32_16x16x64_bf16 v[32:35], v[96:99], v[200:207], v106\n\t" \
  "v_cvt_pk_bf16_f32 v72, v88, v89\n\t" \
  "v_smfmac_f32_16x16x64_bf16 v[36:39], v[96:99], v[232:239], v106\n\t" \
  "v_cvt_pk_bf16_f32 v73, v90, v91\n\t" \
  "v_smfmac_f32_16x16x64_bf16 v[32:35], v[52:55], v[208:215], v106\n\t" \
  "v_cvt_pk_bf16_f32 v74, v92, v93\n\t" \
  "v_smfmac_f32_16x16x64_bf16 v[36:39], v[52:55], v[240:247], v106\n\t" \
  "v_cvt_pk_bf16_f32 v75, v94, v95\n\t" \
  "v_smfmac_f32_16x16x64_bf16 v[32:35], v[56:59], v[216:223], v106\n\t" \
  "v_permlane16_swap_b32 v72, v74\n\t" \
  "v_smfmac_f32_16x16x64_bf16 v[32:35], v[60:63], v[224:231], v106\n\t" \
  "v_permlane16_swap_b32 v73, v75\n\t" \
  "s_waitcnt lgkmcnt(2)\n\t" \
  "v_smfmac_f32_16x16x64_bf16 v[36:39], v[56:59], v[16:23], v106\n\t" \
  "s_waitcnt lgkmcnt(0)\n\t" \
  "v_smfmac_f32_16x16x64_bf16 v[36:39], v[60:63], v[24:31], v106\n\t" \
  "buffer_store_dwordx4 v[72:75], v108, %[ru], s77 offen sc1\n\t" \
  "v_cvt_pk_bf16_f32 v76, v32, v33\n\t" \
  "v_cvt_pk_bf16_f32 v77, v34, v35\n\t" \
  "s_nop 5\n\t" \
  "v_cvt_pk_bf16_f32 v78, v36, v37\n\t" \
  "v_cvt_pk_bf16_f32 v79, v38, v39\n\t" \
  "s_nop 0\n\t" \
  "v_permlane16_swap_b32 v76, v78\n\t" \
  "v_permlane16_swap_b32 v77, v79\n\t" \
  "buffer_store_dwordx4 v[76:79], v108, %[ru], s78 offen sc1\n\t" \
  "s_memtime s[76:77]\n\t" \
  "s_mov_b32 s42, 0\n\t" \
  "s_waitcnt lgkmcnt(0)\n\t" \
  "s_mov_b32 s57, s76\n\t" \
  "s_add_i32 s41, s56, 1950\n\t" \
  "s_sub_i32 s76, s92, s51\n\t" \
  "s_sub_i32 s77, s93, s53\n\t" \
  "s_cmp_eq_u32 s76, 0\n\t" \
  "s_cbranch_scc1 21f\n\t" \
  "v_cvt_f32_u32 v117, s76\n\t" \
  "v_cvt_f32_u32 v116, s77\n\t" \
  "v_rcp_f32 v117, v117\n\t" \
  "s_mov_b32 s51, s92\n\t" \
  "v_mul_f32 v117, v117, v116\n\t" \
  "v_max_f32 v117, 0x3f028f5c, v117\n\t" \
  "v_min_f32 v117, 0x3ffeb852, v117\n\t" \
  "v_rcp_f32 v116, v117\n\t" \
  "v_mul_f32 v117, 0x4f000000, v117\n\t" \
  "v_mul_f32 v116, 0x47800000, v116\n\t" \
  "v_cvt_u32_f32 v117, v117\n\t" \
  "v_cvt_u32_f32 v116, v116\n\t" \
  "s_mov_b32 s53, s93\n\t" \
  "v_readfirstlane_b32 s46, v117\n\t" \
  "v_readfirstlane_b32 s52, v116\n\t" \
  "21:\n\t" \
  "s_add_i32 s44, s44, 1\n\t" \
  "s_and_b32 s58, s58, 4\n\t" \
  "s_and_b32 s76, s44, 64\n\t" \
  "s_or_b32 s42, s42, s76\n\t" \
  "s_cbranch_scc0 20b\n\t" \
  "s_branch 99f\n\t" \
  "14:\n\t" \
  "s_add_u32 s59, s59, 0x10000\n\t" \
  "s_cmp_lt_u32 s59, 0x100000\n\t" \
  "s_cbranch_scc1 13b\n\t" \
  "s_mov_b64 exec, -1\n\t" \
  "s_mov_b32 s42, 16\n\t" \
  "s_branch 99f\n\t" \
  "8:\n\t" \
  "s_bitcmp1_b32 s58, 1\n\t" \
  "s_cbranch_scc1 4b\n\t" \
  "s_bitcmp1_b32 s58, 2\n\t" \
  "s_cbranch_scc1 5b\n\t" \
  "v_max3_u32 v116, v32, v33, v34\n\t" \
  "v_max3_u32 v117, v35, v36, v37\n\t" \
  "v_max3_u32 v116, v116, v38, v39\n\t" \
  "v_max_u32 v116, v116, v117\n\t" \
  "v_cmp_eq_u32 vcc, -1, v116\n\t" \
  "s_cbranch_vccz 5b\n\t" \
  "s_branch 0f\n\t" \
  "9:\n\t" \
  "s_bitcmp1_b32 s58, 2\n\t" \
  "s_cbranch_scc1 6b\n\t" \
  "v_max3_u32 v116, v40, v41, v42\n\t" \
  "v_max3_u32 v117, v43, v44, v45\n\t" \
  "v_max3_u32 v116, v116, v46, v47\n\t" \
  "v_max_u32 v116, v116, v117\n\t" \
  "v_cmp_eq_u32 vcc, -1, v116\n\t" \
  "s_cbranch_vccz 6b\n\t" \
  "s_or_b32 s58, s58, 2\n\t" \
  "0:\n\t" \
  "s_or_b32 s58, s58, 1\n\t" \
  "11:\n\t" \
  "buffer_load_dwordx4 v[32:35], v100, %[rh], s40 offen sc1\n\t" \
  "buffer_load_dwordx4 v[36:39], v100, %[rh], s76 offen sc1\n\t" \
  "buffer_load_dwordx4 v[40:43], v100, %[rh], s77 offen sc1\n\t" \
  "buffer_load_dwordx4 v[44:47], v100, %[rh], s78 offen sc1\n\t" \
  "s_waitcnt lgkmcnt(0)\n\t" \
  "s_branch 10b\n\t" \
  "17:\n\t" \
  "s_add_u32 s59, s59, 1\n\t" \
  "s_bitcmp1_b32 s58, 2\n\t" \
  "s_cbranch_scc1 3b\n\t" \
  "s_waitcnt vmcnt(0)\n\t" \
  "v_max_u32 v116, v32, v33\n\t" \
  "v_max3_u32 v116, v116, v34, v35\n\t" \
  "v_max3_u32 v116, v116, v36, v37\n\t" \
  "v_max3_u32 v116, v116, v38, v39\n\t" \
  "v_max3_u32 v116, v116, v40, v41\n\t" \
  "v_max3_u32 v116, v116, v42, v43\n\t" \
  "v_max3_u32 v116, v116, v44, v45\n\t" \
  "v_max3_u32 v116, v116, v46, v47\n\t" \
  "v_cmp_eq_u32 vcc, -1, v116\n\t" \
  "s_cbranch_vccz 3b\n\t" \
  "s_bitcmp1_b32 s59, 6\n\t" \
  "s_cbranch_scc1 12f\n\t" \
  "buffer_load_dwordx4 v[32:35], v100, %[rh], s40 offen sc1\n\t" \
  "buffer_load_dwordx4 v[36:39], v100, %[rh], s76 offen sc1\n\t" \
  "buffer_load_dwordx4 v[40:43], v100, %[rh], s77 offen sc1\n\t" \
  "buffer_load_dwordx4 v[44:47], v100, %[rh], s78 offen sc1\n\t" \
  "s_branch 17b\n\t" \
  "12:\n\t" \
  "s_mov_b32 s42, s58\n\t" \
  "s_branch 99f\n\t" \
  "93:\n\t" \
  "s_mov_b32 s58, s42\n\t" \
  "20:\n\t" \
  "s_and_b32 s76, s44, 7\n\t" \
  "s_lshl_b32 s76, s76, 15\n\t" \
  "s_add_i32 s40, s54, s76\n\t" \
  "s_and_b32 s76, s44, 3\n\t" \
  "s_lshl_b32 s45, s76, 14\n\t" \
  "s_lshl_b32 s47, s76, 20\n\t" \
  "s_add_i32 s47, s47, s55\n\t" \
  "s_lshr_b32 s76, s47, 21\n\t" \
  "s_bitcmp1_b64 %[sel], s76\n\t" \
  "s_cselect_b32 s76, 0x2000, 0\n\t" \
  "s_add_i32 s47, s47, s76\n\t" \
  "s_add_i32 s49, s44, -1\n\t" \
  "s_and_b32 s48, s49, 2\n\t" \
  "s_lshl_b32 s48, s48, 13\n\t" \
  "s_and_b32 s76, s49, 1\n\t" \
  "s_lshl_b32 s76, s76, 12\n\t" \
  "s_add_i32 s48, s48, s76\n\t" \
  "s_lshl_b32 s50, s53, 8\n\t" \
  "s_mov_b32 s59, 0\n\t" \
  "s_mov_b32 s92, s51\n\t" \
  "s_mov_b32 s93, s53\n\t" \
  "s_xor_b32 s76, s40, 0x400\n\t" \
  "s_xor_b32 s77, s40, 0x800\n\t" \
  "s_xor_b32 s78, s40, 0xc00\n\t" \
  "s_cmp_lg_u32 s58, 0\n\t" \
  "s_cbranch_scc1 11f\n\t" \
  "10:\n\t" \
  "ds_read_b128 v[48:51], v102 offset:49152\n\t" \
  "ds_read_b128 v[52:55], v102 offset:50176\n\t" \
  "ds_read_b128 v[56:59], v102 offset:51200\n\t" \
  "ds_read_b128 v[60:63], v102 offset:52224\n\t" \
  "ds_read_b128 v[0:3], v102 offset:0\n\t" \
  "ds_read_b128 v[4:7], v102 offset:1024\n\t" \
  "ds_read_b128 v[8:11], v102 offset:2048\n\t" \
  "ds_read_b128 v[12:15], v102 offset:3072\n\t" \
  "ds_read_b128 v[16:19], v102 offset:16384\n\t" \
  "ds_read_b128 v[20:23], v102 offset:17408\n\t" \
  "ds_read_b128 v[24:27], v102 offset:18432\n\t" \
  "ds_read_b128 v[28:31], v102 offset:19456\n\t" \
  "s_waitcnt lgkmcnt(4)\n\t" \
  "ds_read_b128 v[80:83], v102 offset:32768\n\t" \
  "ds_read_b128 v[84:87], v102 offset:33792\n\t" \
  "ds_read_b128 v[88:91], v102 offset:34816\n\t" \
  "ds_read_b128 v[92:95], v102 offset:35840\n\t" \
  "s_bitcmp1_b32 s58, 1\n\t" \
  "s_cselect_b64 s[80:81], -1, 0\n\t" \
  "s_cmp_lg_u32 s58, 0\n\t" \
  "s_cbranch_scc1 17f\n\t" \
  "s_mov_b32 exec_hi, 0\n\t" \
  "16:\n\t" \
  "s_memtime vcc\n\t" \
  "s_waitcnt lgkmcnt(0)\n\t" \
  "s_sub_i32 s79, s41, vcc_lo\n\t" \
  "s_cmp_gt_i32 s79, 64\n\t" \
  "s_cbranch_scc1 16b\n\t" \
  "13:\n\t" \
  "buffer_load_dword v116, v112, %[rh], s48 offen sc1\n\t" \
  "s_waitcnt vmcnt(0)\n\t" \
  "15:\n\t" \
  "v_mov_b32 v117, 20000\n\t" \
  "v_cmp_ne_u32_sdwa vcc, v116, s49 src0_sel:BYTE_0 src1_sel:DWORD\n\t" \
  "v_subrev_u32 v116, s50, v116\n\t" \
  "v_ashrrev_i32 v116, 8, v116\n\t" \
  "s_nop 1\n\t" \
  "v_max_i32_dpp v116, v116, v116 quad_perm:[1,0,3,2] row_mask:0xf bank_mask:0xf\n\t" \
  "s_cbranch_vccnz 14f\n\t" \
  "s_nop 0\n\t" \
  "v_max_i32_dpp v116, v116, v116 quad_perm:[2,3,0,1] row_mask:0xf bank_mask:0xf\n\t" \
  "s_nop 1\n\t" \
  "v_max_i32_dpp v116, v116, v116 row_half_mirror row_mask:0xf bank_mask:0xf\n\t" \
  "s_nop 1\n\t" \
  "v_max_i32_dpp v116, v116, v116 row_mirror row_mask:0xf bank_mask:0xf\n\t" \
  "v_med3_i32 v116, v116, 0, v117\n\t" \
  "v_mul_u32_u24 v116, s52, v116\n\t" \
  "v_lshrrev_b32 v116, 16, v116\n\t" \
  "s_mov_b64 exec, -1\n\t" \
  "v_readfirstlane_b32 s84, v116\n\t" \
  "v_readlane_b32 s85, v116, 16\n\t" \
  "s_max_u32 s84, s84, s85\n\t" \
  "s_add_i32 s84, s84, s51\n\t" \
  "1:\n\t" \
  "s_memtime vcc\n\t" \
  "s_waitcnt lgkmcnt(0)\n\t" \
  "s_sub_i32 s79, s84, vcc_lo\n\t" \
  "s_cmp_gt_i32 s79, 128\n\t" \
  "s_cbranch_scc1 1b\n\t" \
  ".p2align 4\n\t" \
  "2:\n\t" \
  "s_sub_i32 s79, s79, 33\n\t" \
  "s_cmp_gt_i32 s79, 56\n\t" \
  "s_cbranch_scc1 2b\n\t" \
  "buffer_load_dwordx4 v[32:35], v100, %[rh], s40 offen sc1\n\t" \
  "s_nop 12\n\t" \
  "buffer_load_dwordx4 v[36:39], v100, %[rh], s76 offen sc1\n\t" \
  "s_nop 12\n\t" \
  "buffer_load_dwordx4 v[40:43], v100, %[rh], s77 offen sc1\n\t" \
  "s_nop 12\n\t" \
  "buffer_load_dwordx4 v[44:47], v100, %[rh], s78 offen sc1\n\t" \
  "s_memtime vcc\n\t" \
  "s_memrealtime s[82:83]\n\t" \
  "s_nop 4\n\t" \
  "s_memrealtime s[84:85]\n\t" \
  "s_nop 4\n\t" \
  "s_memrealtime s[56:57]\n\t" \
  "s_nop 4\n\t" \
  "s_memrealtime s[86:87]\n\t" \
  "s_nop 4\n\t" \
  "s_memrealtime s[88:89]\n\t" \
  "s_nop 4\n\t" \
  "s_memrealtime s[90:91]\n\t" \
  "s_mov_b32 s92, 0\n\t" \
  "s_waitcnt lgkmcnt(0)\n\t" \
  "s_cmp_eq_u32 s82, s84\n\t" \
  "s_addc_u32 s92, s92, 0\n\t" \
  "s_cmp_eq_u32 s82, s56\n\t" \
  "s_addc_u32 s92, s92, 0\n\t" \
  "s_cmp_eq_u32 s82, s86\n\t" \
  "s_addc_u32 s92, s92, 0\n\t" \
  "s_cmp_eq_u32 s82, s88\n\t" \
  "s_addc_u32 s92, s92, 0\n\t" \
  "s_cmp_eq_u32 s82, s90\n\t" \
  "s_addc_u32 s92, s92, 0\n\t" \
  "s_mul_i32 s92, s92, 24\n\t" \
  "s_add_i32 s92, s92, vcc_lo\n\t" \
  "s_lshl4_add_u32 s93, s82, 64\n\t" \
  "3:\n\t" \
  "v_mov_b64 v[72:73], 0\n\t" \
  "v_mov_b64 v[74:75], 0\n\t" \
  "v_mov_b64 v[76:77], 0\n\t" \
  "v_mov_b64 v[78:79], 0\n\t" \
  "s_waitcnt lgkmcnt(0)\n\t" \
  "s_waitcnt vmcnt(3)\n\t" \
  "v_smfmac_f32_16x16x64_bf16 v[72:75], v[32:35], v[48:55], v104\n\t" \
  "ds_write_b128 v101, v[32:35] offset:12288\n\t" \
  "v_smfmac_f32_16x16x64_bf16 v[76:79], v[32:35], v[56:63], v104\n\t" \
  "s_waitcnt vmcnt(2)\n\t" \
  "ds_write_b128 v101, v[36:39] offset:13312\n\t" \
  "v_smfmac_f32_16x16x64_bf16 v[72:75], v[36:39], a[144:151], v104\n\t" \
  "v_smfmac_f32_16x16x64_bf16 v[76:79], v[36:39], a[152:159], v104\n\t" \
  "s_nop 3\n\t" \
  "v_cmp_u_f32 vcc, v72, v73\n\t" \
  "v_cmp_u_f32 s[82:83], v74, v75\n\t" \
  "s_waitcnt vmcnt(1)\n\t" \
  "v_smfmac_f32_16x16x64_bf16 v[72:75], v[40:43], a[160:167], v104\n\t" \
  "s_or_b64 vcc, vcc, s[82:83]\n\t" \
  "s_or_b64 vcc, vcc, s[80:81]\n\t" \
  "v_smfmac_f32_16x16x64_bf16 v[76:79], v[40:43], a[168:175], v104\n\t" \
  "s_waitcnt lgkmcnt(0)\n\t" \
  "s_cbranch_scc1 8f\n\t" \
  "5:\n\t" \
  "s_barrier\n\t" \
  "4:\n\t" \
  "s_memtime s[56:57]\n\t" \
  "ds_read_b128 v[48:51], v101 offset:0\n\t" \
  "ds_read_b128 v[52:55], v101 offset:1024\n\t" \
  "ds_read_b128 v[56:59], v101 offset:5120\n\t" \
  "s_waitcnt vmcnt(0)\n\t" \
  "v_smfmac_f32_16x16x64_bf16 v[72:75], v[44:47], a[176:183], v104\n\t" \
  "ds_read_b128 v[60:63], v101 offset:4096\n\t" \
  "ds_read_b128 v[64:67], v101 offset:9216\n\t" \
  "v_smfmac_f32_16x16x64_bf16 v[76:79], v[44:47], a[184:191], v104\n\t" \
  "ds_read_b128 v[68:71], v101 offset:8192\n\t" \
  "ds_write_b128 v101, v[40:43] offset:14336\n\t" \
  "s_nop 1\n\t" \
  "v_cmp_u_f32 vcc, v72, v73\n\t" \
  "v_cmp_u_f32 s[82:83], v74, v75\n\t" \
  "s_waitcnt lgkmcnt(6)\n\t" \
  "v_smfmac_f32_16x16x64_bf16 v[72:75], v[48:51], v[0:7], v104\n\t" \
  "s_or_b64 vcc, vcc, s[82:83]\n\t" \
  "v_smfmac_f32_16x16x64_bf16 v[76:79], v[48:51], v[8:15], v104\n\t" \
  "s_cbranch_scc1 9f\n\t" \
  "6:\n\t" \
  "s_waitcnt lgkmcnt(5)\n\t" \
  "v_smfmac_f32_16x16x64_bf16 v[72:75], v[52:55], a[0:7], v104\n\t" \
  "ds_write_b128 v101, v[44:47] offset:15360\n\t" \
  "v_smfmac_f32_16x16x64_bf16 v[76:79], v[52:55], a[8:15], v104\n\t" \
  "v_mov_b64 v[40:41], 0\n\t" \
  "s_waitcnt lgkmcnt(5)\n\t" \
  "v_smfmac_f32_16x16x64_bf16 v[72:75], v[56:59], a[48:55], v104\n\t" \
  "v_mov_b64 v[42:43], 0\n\t" \
  "v_smfmac_f32_16x16x64_bf16 v[76:79], v[56:59], a[56:63], v104\n\t" \
  "s_waitcnt lgkmcnt(4)\n\t" \
  "v_smfmac_f32_16x16x64_bf16 v[72:75], v[60:63], v[16:23], v104\n\t" \
  "s_waitcnt lgkmcnt(0)\n\t" \
  "s_barrier\n\t" \
  "v_smfmac_f32_16x16x64_bf16 v[76:79], v[60:63], v[24:31], v104\n\t" \
  "ds_read_b128 v[48:51], v101 offset:2048\n\t" \
  "ds_read_b128 v[52:55], v101 offset:6144\n\t" \
  "v_smfmac_f32_16x16x64_bf16 v[72:75], v[64:67], a[96:103], v104\n\t" \
  "ds_read_b128 v[56:59], v101 offset:10240\n\t" \
  "ds_read_b128 v[60:63], v101 offset:3072\n\t" \
  "v_smfmac_f32_16x16x64_bf16 v[76:79], v[64:67], a[104:111], v104\n\t" \
  "ds_read_b128 v[32:35], v101 offset:7168\n\t" \
  "ds_read_b128 v[36:39], v101 offset:11264\n\t" \
  "v_smfmac_f32_16x16x64_bf16 v[72:75], v[68:71], v[80:87], v104\n\t" \
  "ds_read_b128 v[0:3], v103 offset:0\n\t" \
  "v_mov_b64 v[44:45], 0\n\t" \
  "v_smfmac_f32_16x16x64_bf16 v[76:79], v[68:71], v[88:95], v104\n\t" \
  "v_mov_b64 v[46:47], 0\n\t" \
  "s_waitcnt lgkmcnt(5)\n\t" \
  "v_smfmac_f32_16x16x64_bf16 v[72:75], v[48:51], a[16:23], v104\n\t" \
  "ds_read_b128 v[4:7], v103 offset:1024\n\t" \
  "v_mov_b64 v[80:81], 0\n\t" \
  "v_smfmac_f32_16x16x64_bf16 v[72:75], v[52:55], a[64:71], v104\n\t" \
  "v_mov_b64 v[82:83], 0\n\t" \
  "s_waitcnt lgkmcnt(2)\n\t" \
  "v_smfmac_f32_16x16x64_bf16 v[72:75], v[56:59], a[112:119], v104\n\t" \
  "ds_read_b128 v[8:11], v103 offset:2048\n\t" \
  "v_mov_b64 v[84:85], 0\n\t" \
  "v_smfmac_f32_16x16x64_bf16 v[72:75], v[60:63], a[32:39], v104\n\t" \
  "ds_read_b128 v[12:15], v103 offset:3072\n\t" \
  "v_mov_b64 v[86:87], 0\n\t" \
  "v_smfmac_f32_16x16x64_bf16 v[72:75], v[32:35], a[80:87], v104\n\t" \
  "ds_read_b128 v[16:19], v103 offset:16384\n\t" \
  "v_mov_b64 v[88:89], 0\n\t" \
  "v_smfmac_f32_16x16x64_bf16 v[72:75], v[36:39], a[128:135], v104\n\t" \
  "ds_read_b128 v[20:23], v103 offset:17408\n\t" \
  "v_mov_b64 v[90:91], 0\n\t" \
  "v_smfmac_f32_16x16x64_bf16 v[76:79], v[48:51], a[24:31], v104\n\t" \
  "ds_read_b128 v[24:27], v103 offset:18432\n\t" \
  "v_mov_b64 v[92:93], 0\n\t" \
  "v_smfmac_f32_16x16x64_bf16 v[76:79], v[52:55], a[72:79], v104\n\t" \
  "v_add_f32 v96, v72, v74\n\t" \
  "v_add_f32 v97, v73, v75\n\t" \
  "v_cvt_pk_bf16_f32 v96, v96, v97\n\t" \
  "ds_write_b32 v105, v96\n\t" \
  "v_smfmac_f32_16x16x64_bf16 v[76:79], v[56:59], a[120:127], v104\n\t" \
  "ds_read_b128 v[28:31], v103 offset:19456\n\t" \
  "v_mov_b64 v[94:95], 0\n\t" \
  "v_smfmac_f32_16x16x64_bf16 v[76:79], v[60:63], a[40:47], v104\n\t" \
  "v_smfmac_f32_16x16x64_bf16 v[76:79], v[32:35], a[88:95], v104\n\t" \
  "v_smfmac_f32_16x16x64_bf16 v[76:79], v[36:39], a[136:143], v104\n\t" \
  "s_waitcnt lgkmcnt(1)\n\t" \
  "s_barrier\n\t" \
  "ds_read_b64_tr_b16 v[96:97], v107 offset:0\n\t" \
  "ds_read_b64_tr_b16 v[98:99], v107 offset:64\n\t" \
  "ds_read_b64_tr_b16 v[56:57], v107 offset:1024\n\t" \
  "ds_read_b64_tr_b16 v[58:59], v107 offset:1088\n\t" \
  "s_nop 1\n\t" \
  "v_add_f32 v116, v76, v78\n\t" \
  "v_add_f32 v117, v77, v79\n\t" \
  "v_cvt_pk_bf16_f32 v116, v116, v117\n\t" \
  "ds_write_b32 v105, v116 offset:256\n\t" \
  "s_waitcnt lgkmcnt(3)\n\t" \
  "v_smfmac_f32_16x16x64_bf16 v[40:43], v[96:99], a[192:199], v106\n\t" \
  "v_smfmac_f32_16x16x64_bf16 v[44:47], v[96:99], a[224:231], v106\n\t" \
  "s_waitcnt lgkmcnt(0)\n\t" \
  "s_barrier\n\t" \
  "ds_read_b64_tr_b16 v[52:53], v107 offset:256\n\t" \
  "ds_read_b64_tr_b16 v[54:55], v107 offset:320\n\t" \
  "v_smfmac_f32_16x16x64_bf16 v[40:43], v[56:59], a[208:215], v106\n\t" \
  "ds_read_b64_tr_b16 v[60:61], v107 offset:1280\n\t" \
  "ds_read_b64_tr_b16 v[62:63], v107 offset:1344\n\t" \
  "v_smfmac_f32_16x16x64_bf16 v[44:47], v[56:59], v[0:7], v106\n\t" \
  "v_smfmac_f32_16x16x64_bf16 v[80:83], v[96:99], a[240:247], v106\n\t" \
  "v_mov_b64 v[32:33], 0\n\t" \
  "v_smfmac_f32_16x16x64_bf16 v[84:87], v[96:99], v[136:143], v106\n\t" \
  "v_mov_b64 v[34:35], 0\n\t" \
  "v_smfmac_f32_16x16x64_bf16 v[80:83], v[56:59], v[120:127], v106\n\t" \
  "v_mov_b64 v[36:37], 0\n\t" \
  "v_smfmac_f32_16x16x64_bf16 v[84:87], v[56:59], v[16:23], v106\n\t" \
  "s_waitcnt lgkmcnt(2)\n\t" \
  "v_smfmac_f32_16x16x64_bf16 v[40:43], v[52:55], a[200:207], v106\n\t" \
  "v_smfmac_f32_16x16x64_bf16 v[44:47], v[52:55], a[232:239], v106\n\t" \
  "s_xor_b32 s76, s47, 0x40000\n\t" \
  "s_waitcnt lgkmcnt(0)\n\t" \
  "v_smfmac_f32_16x16x64_bf16 v[40:43], v[60:63], a[216:223], v106\n\t" \
  "v_mov_b64 v[38:39], 0\n\t" \
  "s_xor_b32 s77, s47, 0x80000\n\t" \
  "v_smfmac_f32_16x16x64_bf16 v[44:47], v[60:63], v[8:15], v106\n\t" \
  "s_xor_b32 s78, s47, 0xc0000\n\t" \
  "v_smfmac_f32_16x16x64_bf16 v[80:83], v[52:55], a[248:255], v106\n\t" \
  "ds_read_b128 v[0:3], v103 offset:32768\n\t" \
  "v_cvt_pk_bf16_f32 v64, v40, v41\n\t" \
  "v_smfmac_f32_16x16x64_bf16 v[84:87], v[52:55], v[144:151], v106\n\t" \
  "ds_read_b128 v[4:7], v103 offset:33792\n\t" \
  "v_cvt_pk_bf16_f32 v65, v42, v43\n\t" \
  "v_smfmac_f32_16x16x64_bf16 v[80:83], v[60:63], v[128:135], v106\n\t" \
  "ds_read_b128 v[8:11], v103 offset:34816\n\t" \
  "v_cvt_pk_bf16_f32 v66, v44, v45\n\t" \
  "v_smfmac_f32_16x16x64_bf16 v[84:87], v[60:63], v[24:31], v106\n\t" \
  "ds_read_b128 v[12:15], v103 offset:35840\n\t" \
  "v_cvt_pk_bf16_f32 v67, v46, v47\n\t" \
  "v_smfmac_f32_16x16x64_bf16 v[88:91], v[96:99], v[152:159], v106\n\t" \
  "v_permlane16_swap_b32 v64, v66\n\t" \
  "v_cvt_pk_bf16_f32 v68, v80, v81\n\t" \
  "v_smfmac_f32_16x16x64_bf16 v[92:95], v[96:99], v[184:191], v106\n\t" \
  "v_permlane16_swap_b32 v65, v67\n\t" \
  "v_cvt_pk_bf16_f32 v69, v82, v83\n\t" \
  "v_smfmac_f32_16x16x64_bf16 v[88:91], v[52:55], v[160:167], v106\n\t" \
  "v_cvt_pk_bf16_f32 v70, v84, v85\n\t" \
  "v_smfmac_f32_16x16x64_bf16 v[92:95], v[52:55], v[192:199], v106\n\t" \
  "v_cvt_pk_bf16_f32 v71, v86, v87\n\t" \
  "v_smfmac_f32_16x16x64_bf16 v[88:91], v[56:59], v[168:175], v106\n\t" \
  "ds_read_b128 v[16:19], v103 offset:49152\n\t" \
  "buffer_store_dwordx4 v[64:67], v108, %[ru], s47 offen sc1\n\t" \
  "v_permlane16_swap_b32 v68, v70\n\t" \
  "s_waitcnt lgkmcnt(3)\n\t" \
  "v_smfmac_f32_16x16x64_bf16 v[92:95], v[56:59], v[0:7], v106\n\t" \
  "ds_read_b128 v[20:23], v103 offset:50176\n\t" \
  "v_permlane16_swap_b32 v69, v71\n\t" \
  "v_smfmac_f32_16x16x64_bf16 v[88:91], v[60:63], v[176:183], v106\n\t" \
  "ds_read_b128 v[24:27], v103 offset:51200\n\t" \
  "s_waitcnt lgkmcnt(3)\n\t" \
  "v_smfmac_f32_16x16x64_bf16 v[92:95], v[60:63], v[8:15], v106\n\t" \
  "ds_read_b128 v[28:31], v103 offset:52224\n\t" \
  "v_smfmac_f32_16x16x64_bf16 v[32:35], v[96:99], v[200:207], v106\n\t" \
  "buffer_store_dwordx4 v[68:71], v108, %[ru], s76 offen sc1\n\t" \
  "v_cvt_pk_bf16_f32 v72, v88, v89\n\t" \
  "v_smfmac_f32_16x16x64_bf16 v[36:39], v[96:99], v[232:239], v106\n\t" \
  "v_cvt_pk_bf16_f32 v73, v90, v91\n\t" \
  "v_smfmac_f32_16x16x64_bf16 v[32:35], v[52:55], v[208:215], v106\n\t" \
  "v_cvt_pk_bf16_f32 v74, v92, v93\n\t" \
  "v_smfmac_f32_16x16x64_bf16 v[36:39], v[52:55], v[240:247], v106\n\t" \
  "v_cvt_pk_bf16_f32 v75, v94, v95\n\t" \
  "v_smfmac_f32_16x16x64_bf16 v[32:35], v[56:59], v[216:223], v106\n\t" \
  "v_permlane16_swap_b32 v72, v74\n\t" \
  "v_smfmac_f32_16x16x64_bf16 v[32:35], v[60:63], v[224:231], v106\n\t" \
  "v_permlane16_swap_b32 v73, v75\n\t" \
  "s_waitcnt lgkmcnt(2)\n\t" \
  "v_smfmac_f32_16x16x64_bf16 v[36:39], v[56:59], v[16:23], v106\n\t" \
  "s_waitcnt lgkmcnt(0)\n\t" \
  "v_smfmac_f32_16x16x64_bf16 v[36:39], v[60:63], v[24:31], v106\n\t" \
  "buffer_store_dwordx4 v[72:75], v108, %[ru], s77 offen sc1\n\t" \
  "v_cvt_pk_bf16_f32 v76, v32, v33\n\t" \
  "v_cvt_pk_bf16_f32 v77, v34, v35\n\t" \
  "s_nop 5\n\t" \
  "v_cvt_pk_bf16_f32 v78, v36, v37\n\t" \
  "v_cvt_pk_bf16_f32 v79, v38, v39\n\t" \
  "s_nop 0\n\t" \
  "v_permlane16_swap_b32 v76, v78\n\t" \
  "v_permlane16_swap_b32 v77, v79\n\t" \
  "buffer_store_dwordx4 v[76:79], v108, %[ru], s78 offen sc1\n\t" \
  "s_memtime s[76:77]\n\t" \
  "s_mov_b32 s42, 0\n\t" \
  "s_waitcnt lgkmcnt(0)\n\t" \
  "s_mov_b32 s57, s76\n\t" \
  "s_add_i32 s41, s56, 1950\n\t" \
  "s_sub_i32 s76, s92, s51\n\t" \
  "s_sub_i32 s77, s93, s53\n\t" \
  "s_cmp_eq_u32 s76, 0\n\t" \
  "s_cbranch_scc1 21f\n\t" \
  "v_cvt_f32_u32 v117, s76\n\t" \
  "v_cvt_f32_u32 v116, s77\n\t" \
  "v_rcp_f32 v117, v117\n\t" \
  "s_mov_b32 s51, s92\n\t" \
  "v_mul_f32 v117, v117, v116\n\t" \
  "v_max_f32 v117, 0x3f028f5c, v117\n\t" \
  "v_min_f32 v117, 0x3ffeb852, v117\n\t" \
  "v_rcp_f32 v116, v117\n\t" \
  "v_mul_f32 v117, 0x4f000000, v117\n\t" \
  "v_mul_f32 v116, 0x47800000, v116\n\t" \
  "v_cvt_u32_f32 v117, v117\n\t" \
  "v_cvt_u32_f32 v116, v116\n\t" \
  "s_mov_b32 s53, s93\n\t" \
  "v_readfirstlane_b32 s46, v117\n\t" \
  "v_readfirstlane_b32 s52, v116\n\t" \
  "21:\n\t" \
  "s_add_i32 s44, s44, 1\n\t" \
  "s_and_b32 s58, s58, 4\n\t" \
  "s_and_b32 s76, s44, 64\n\t" \
  "s_or_b32 s42, s42, s76\n\t" \
  "s_cbranch_scc0 20b\n\t" \
  "s_branch 99f\n\t" \
  "14:\n\t" \
  "s_add_u32 s59, s59, 0x10000\n\t" \
  "s_cmp_lt_u32 s59, 0x100000\n\t" \
  "s_cbranch_scc1 13b\n\t" \
  "s_mov_b64 exec, -1\n\t" \
  "s_mov_b32 s42, 16\n\t" \
  "s_branch 99f\n\t" \
  "8:\n\t" \
  "s_bitcmp1_b32 s58, 1\n\t" \
  "s_cbranch_scc1 4b\n\t" \
  "s_bitcmp1_b32 s58, 2\n\t" \
  "s_cbranch_scc1 5b\n\t" \
  "v_max3_u32 v116, v32, v33, v34\n\t" \
  "v_max3_u32 v117, v35, v36, v37\n\t" \
  "v_max3_u32 v116, v116, v38, v39\n\t" \
  "v_max_u32 v116, v116, v117\n\t" \
  "v_cmp_eq_u32 vcc, -1, v116\n\t" \
  "s_cbranch_vccz 5b\n\t" \
  "s_branch 0f\n\t" \
  "9:\n\t" \
  "s_bitcmp1_b32 s58, 2\n\t" \
  "s_cbranch_scc1 6b\n\t" \
  "v_max3_u32 v116, v40, v41, v42\n\t" \
  "v_max3_u32 v117, v43, v44, v45\n\t" \
  "v_max3_u32 v116, v116, v46, v47\n\t" \
  "v_max_u32 v116, v116, v117\n\t" \
  "v_cmp_eq_u32 vcc, -1, v116\n\t" \
  "s_cbranch_vccz 6b\n\t" \
  "s_or_b32 s58, s58, 2\n\t" \
  "0:\n\t" \
  "s_or_b32 s58, s58, 1\n\t" \
  "11:\n\t" \
  "buffer_load_dwordx4 v[32:35], v100, %[rh], s40 offen sc1\n\t" \
  "buffer_load_dwordx4 v[36:39], v100, %[rh], s76 offen sc1\n\t" \
  "buffer_load_dwordx4 v[40:43], v100, %[rh], s77 offen sc1\n\t" \
  "buffer_load_dwordx4 v[44:47], v100, %[rh], s78 offen sc1\n\t" \
  "s_waitcnt lgkmcnt(0)\n\t" \
  "s_branch 10b\n\t" \
  "17:\n\t" \
  "s_add_u32 s59, s59, 1\n\t" \
  "s_bitcmp1_b32 s58, 2\n\t" \
  "s_cbranch_scc1 3b\n\t" \
  "s_waitcnt vmcnt(0)\n\t" \
  "v_max_u32 v116, v32, v33\n\t" \
  "v_max3_u32 v116, v116, v34, v35\n\t" \
  "v_max3_u32 v116, v116, v36, v37\n\t" \
  "v_max3_u32 v116, v116, v38, v39\n\t" \
  "v_max3_u32 v116, v116, v40, v41\n\t" \
  "v_max3_u32 v116, v116, v42, v43\n\t" \
  "v_max3_u32 v116, v116, v44, v45\n\t" \
  "v_max3_u32 v116, v116, v46, v47\n\t" \
  "v_cmp_eq_u32 vcc, -1, v116\n\t" \
  "s_cbranch_vccz 3b\n\t" \
  "s_bitcmp1_b32 s59, 6\n\t" \
  "s_cbranch_scc1 12f\n\t" \
  "buffer_load_dwordx4 v[32:35], v100, %[rh], s40 offen sc1\n\t" \
  "buffer_load_dwordx4 v[36:39], v100, %[rh], s76 offen sc1\n\t" \
  "buffer_load_dwordx4 v[40:43], v100, %[rh], s77 offen sc1\n\t" \
  "buffer_load_dwordx4 v[44:47], v100, %[rh], s78 offen sc1\n\t" \
  "s_branch 17b\n\t" \
  "12:\n\t" \
  "s_mov_b32 s42, s58\n\t" \
  "s_branch 99f\n\t" \
  "99:\n\t" \
  "s_waitcnt vmcnt(0) lgkmcnt(0)\n\t" \
  ".Lchain_last:\n\t" \
  ""
#define CB_BODY \
  "s_cmp_gt_u32 %[wv], 1\n\t" \
  "s_cbranch_scc1 91f\n\t" \
  "s_mov_b32 s58, s42\n\t" \
  "s_mov_b32 s59, 0\n\t" \
  "s_bitcmp1_b32 s58, 4\n\t" \
  "s_cbranch_scc1 26f\n\t" \
  "s_bitcmp1_b32 s58, 3\n\t" \
  "s_cbranch_scc1 34f\n\t" \
  "s_bitcmp1_b32 s58, 1\n\t" \
  "s_cbranch_scc1 18f\n\t" \
  "19:\n\t" \
  "s_add_i32 s85, s40, -1\n\t" \
  "s_and_b32 s82, s85, 2\n\t" \
  "s_lshl_b32 s82, s82, 13\n\t" \
  "s_and_b32 s80, s85, 1\n\t" \
  "s_lshl_b32 s80, s80, 12\n\t" \
  "s_add_i32 s82, s82, s80\n\t" \
  "s_add_i32 s82, s82, 0x84000\n\t" \
  "s_mov_b32 exec_hi, 0\n\t" \
  "buffer_load_dword v184, v200, %[rh], s82 offen sc1\n\t" \
  "buffer_load_dword v186, v203, %[ru], 0 offen sc1\n\t" \
  "s_mov_b64 exec, -1\n\t" \
  "20:\n\t" \
  "s_and_b32 s80, s40, 3\n\t" \
  "s_mul_i32 s77, s80, 0xc000\n\t" \
  "s_add_i32 s77, s77, 0x40000\n\t" \
  "s_lshl_b32 s78, s80, 14\n\t" \
  "s_add_i32 s78, s78, 0x70000\n\t" \
  "s_mov_b32 s57, 0\n\t" \
  "s_add_i32 s96, s77, 0x1000\n\t" \
  "s_lshl_b32 s88, s80, 20\n\t" \
  "s_add_i32 s88, s88, s48\n\t" \
  "s_lshr_b32 s80, s88, 21\n\t" \
  "s_bitcmp1_b64 %[sel], s80\n\t" \
  "s_cselect_b32 s80, 0x2000, 0\n\t" \
  "s_add_i32 s88, s88, s80\n\t" \
  "s_xor_b32 s89, s88, 0x800\n\t" \
  "s_xor_b32 s90, s88, 0x1000\n\t" \
  "s_xor_b32 s91, s88, 0x1800\n\t" \
  "s_add_i32 s92, s77, s50\n\t" \
  "s_xor_b32 s93, s92, 0x100\n\t" \
  "s_xor_b32 s94, s92, 0x200\n\t" \
  "s_xor_b32 s95, s92, 0x300\n\t" \
  "s_add_i32 s97, s40, 1\n\t" \
  "s_and_b32 s97, s97, 7\n\t" \
  "s_lshl_b32 s97, s97, 15\n\t" \
  "s_add_i32 s97, s97, s49\n\t" \
  "s_lshl_b32 s83, s43, 8\n\t" \
  "s_mov_b32 s84, 700\n\t" \
  "s_mov_b32 exec_hi, 0\n\t" \
  "s_waitcnt vmcnt(0)\n\t" \
  "v_cmp_ne_u32_sdwa vcc, v184, s85 src0_sel:BYTE_0 src1_sel:DWORD\n\t" \
  "v_subrev_u32 v184, s83, v184\n\t" \
  "v_ashrrev_i32 v184, 8, v184\n\t" \
  "s_cbranch_vccnz 22f\n\t" \
  "s_nop 0\n\t" \
  "v_max_i32_dpp v184, v184, v184 quad_perm:[1,0,3,2] row_mask:0xf bank_mask:0xf\n\t" \
  "s_nop 1\n\t" \
  "v_max_i32_dpp v184, v184, v184 quad_perm:[2,3,0,1] row_mask:0xf bank_mask:0xf\n\t" \
  "s_nop 1\n\t" \
  "v_max_i32_dpp v184, v184, v184 row_half_mirror row_mask:0xf bank_mask:0xf\n\t" \
  "s_nop 1\n\t" \
  "v_max_i32_dpp v184, v184, v184 row_mirror row_mask:0xf bank_mask:0xf\n\t" \
  "s_nop 0\n\t" \
  "v_readlane_b32 s81, v184, 0\n\t" \
  "v_readlane_b32 s82, v184, 16\n\t" \
  "s_max_i32 s81, s81, s82\n\t" \
  "s_add_i32 s81, s81, 1056\n\t" \
  "s_max_i32 s81, s81, 0\n\t" \
  "s_min_i32 s81, s81, 20000\n\t" \
  "s_mul_i32 s81, s81, s44\n\t" \
  "s_lshr_b32 s81, s81, 16\n\t" \
  "24:\n\t" \
  "s_add_i32 s81, s81, s41\n\t" \
  "s_mov_b64 exec, -1\n\t" \
  "s_add_i32 s82, s40, 2\n\t" \
  "s_and_b32 s82, s82, 3\n\t" \
  "s_lshl_b32 s82, s82, 20\n\t" \
  "s_add_i32 s82, s82, s48\n\t" \
  "s_andn2_b32 s82, s82, 0x1800\n\t" \
  "s_lshr_b32 s80, s82, 21\n\t" \
  "s_bitcmp1_b64 %[sel], s80\n\t" \
  "s_cselect_b32 s80, 0x2000, 0\n\t" \
  "s_add_i32 s82, s82, s80\n\t" \
  "s_add_i32 s80, s82, 0x1000\n\t" \
  "v_mov_b64 v[0:1], -1\n\t" \
  "v_mov_b64 v[2:3], -1\n\t" \
  "s_add_i32 s86, s81, -700\n\t" \
  "16:\n\t" \
  "s_memtime vcc\n\t" \
  "s_waitcnt lgkmcnt(0)\n\t" \
  "s_sub_i32 s79, s86, vcc_lo\n\t" \
  "s_cmp_gt_i32 s79, 128\n\t" \
  "s_cbranch_scc1 16b\n\t" \
  "buffer_store_dwordx4 v[0:3], v192, %[ru], s82 offen offset:0 sc1\n\t" \
  "buffer_store_dwordx4 v[0:3], v192, %[ru], s82 offen offset:1024 sc1\n\t" \
  "buffer_store_dwordx4 v[0:3], v192, %[ru], s82 offen offset:2048 sc1\n\t" \
  "buffer_store_dwordx4 v[0:3], v192, %[ru], s82 offen offset:3072 sc1\n\t" \
  "buffer_store_dwordx4 v[0:3], v192, %[ru], s80 offen offset:0 sc1\n\t" \
  "buffer_store_dwordx4 v[0:3], v192, %[ru], s80 offen offset:1024 sc1\n\t" \
  "buffer_store_dwordx4 v[0:3], v192, %[ru], s80 offen offset:2048 sc1\n\t" \
  "buffer_store_dwordx4 v[0:3], v192, %[ru], s80 offen offset:3072 sc1\n\t" \
  "s_mov_b32 exec_hi, 0\n\t" \
  "1:\n\t" \
  "s_memtime vcc\n\t" \
  "s_waitcnt lgkmcnt(0)\n\t" \
  "s_sub_i32 s79, s81, vcc_lo\n\t" \
  "s_cmp_gt_i32 s79, 128\n\t" \
  "s_cbranch_scc1 1b\n\t" \
  ".p2align 4\n\t" \
  "2:\n\t" \
  "s_sub_i32 s79, s79, 33\n\t" \
  "s_cmp_gt_i32 s79, 56\n\t" \
  "s_cbranch_scc1 2b\n\t" \
  "13:\n\t" \
  "buffer_load_dword v184, v200, %[rh], s78 offen sc1\n\t" \
  "s_waitcnt vmcnt(0)\n\t" \
  "s_memtime s[86:87]\n\t" \
  "v_cmp_ne_u32_sdwa vcc, v184, s40 src0_sel:BYTE_0 src1_sel:DWORD\n\t" \
  "v_subrev_u32 v184, s83, v184\n\t" \
  "v_ashrrev_i32 v184, 8, v184\n\t" \
  "s_nop 1\n\t" \
  "v_max_i32_dpp v184, v184, v184 quad_perm:[1,0,3,2] row_mask:0xf bank_mask:0xf\n\t" \
  "s_nop 1\n\t" \
  "v_max_i32_dpp v184, v184, v184 quad_perm:[2,3,0,1] row_mask:0xf bank_mask:0xf\n\t" \
  "s_cbranch_vccnz 14f\n\t" \
  "15:\n\t" \
  "s_nop 0\n\t" \
  "v_max_i32_dpp v184, v184, v184 row_half_mirror row_mask:0xf bank_mask:0xf\n\t" \
  "s_nop 1\n\t" \
  "v_max_i32_dpp v184, v184, v184 row_mirror row_mask:0xf bank_mask:0xf\n\t" \
  "s_mov_b64 exec, -1\n\t" \
  "v_readlane_b32 s57, v184, 0\n\t" \
  "v_readlane_b32 s82, v184, 16\n\t" \
  "s_waitcnt lgkmcnt(0)\n\t" \
  "s_sub_i32 s79, s41, s86\n\t" \
  "s_max_i32 s57, s57, s82\n\t" \
  "s_add_i32 s81, s57, 255\n\t" \
  "s_max_i32 s81, s81, 0\n\t" \
  "s_min_i32 s81, s81, 20000\n\t" \
  "s_mul_i32 s81, s81, s44\n\t" \
  "s_lshr_b32 s81, s81, 16\n\t" \
  "s_add_i32 s79, s79, s81\n\t" \
  "s_add_i32 s79, s79, -280\n\t" \
  ".p2align 4\n\t" \
  "3:\n\t" \
  "s_sub_i32 s79, s79, 33\n\t" \
  "s_cmp_gt_i32 s79, 56\n\t" \
  "s_cbranch_scc1 3b\n\t" \
  "buffer_load_dwordx4 v[72:75], v192, %[ru], s88 offen offset:0 sc1\n\t" \
  "s_nop 4\n\t" \
  "buffer_load_dwordx4 v[76:79], v192, %[ru], s88 offen offset:1024 sc1\n\t" \
  "s_nop 4\n\t" \
  "s_add_i32 s79, s79, 2\n\t" \
  ".p2align 4\n\t" \
  "4:\n\t" \
  "s_sub_i32 s79, s79, 33\n\t" \
  "s_cmp_gt_i32 s79, 56\n\t" \
  "s_cbranch_scc1 4b\n\t" \
  "buffer_load_dwordx4 v[80:83], v192, %[ru], s89 offen offset:0 sc1\n\t" \
  "s_nop 4\n\t" \
  "buffer_load_dwordx4 v[84:87], v192, %[ru], s89 offen offset:1024 sc1\n\t" \
  "s_nop 4\n\t" \
  "s_add_i32 s79, s79, 62\n\t" \
  ".p2align 4\n\t" \
  "5:\n\t" \
  "s_sub_i32 s79, s79, 33\n\t" \
  "s_cmp_gt_i32 s79, 56\n\t" \
  "s_cbranch_scc1 5b\n\t" \
  "buffer_load_dwordx4 v[88:91], v192, %[ru], s90 offen offset:0 sc1\n\t" \
  "buffer_load_dwordx4 v[92:95], v192, %[ru], s90 offen offset:1024 sc1\n\t" \
  "s_add_i32 s79, s79, 32\n\t" \
  ".p2align 4\n\t" \
  "9:\n\t" \
  "s_sub_i32 s79, s79, 33\n\t" \
  "s_cmp_gt_i32 s79, 56\n\t" \
  "s_cbranch_scc1 9b\n\t" \
  "buffer_load_dwordx4 v[104:107], v194, %[rh], s92 offen sc1\n\t" \
  "s_nop 4\n\t" \
  "buffer_load_dwordx4 v[108:111], v194, %[rh], s93 offen sc1\n\t" \
  "s_nop 4\n\t" \
  "buffer_load_dwordx4 v[112:115], v194, %[rh], s94 offen sc1\n\t" \
  "s_nop 4\n\t" \
  "buffer_load_dwordx4 v[116:119], v194, %[rh], s95 offen sc1\n\t" \
  "s_nop 4\n\t" \
  "buffer_load_dwordx4 v[96:99], v192, %[ru], s91 offen offset:0 sc1\n\t" \
  "s_nop 4\n\t" \
  "buffer_load_dwordx4 v[100:103], v192, %[ru], s91 offen offset:1024 sc1\n\t" \
  "s_nop 4\n\t" \
  "10:\n\t" \
  "s_waitcnt vmcnt(5)\n\t" \
  "v_mfma_f32_16x16x32_bf16 v[136:139], v[104:107], v[72:75], 0\n\t" \
  "v_mfma_f32_16x16x32_bf16 v[140:143], v[104:107], v[76:79], 0\n\t" \
  "s_waitcnt vmcnt(4)\n\t" \
  "v_mfma_f32_16x16x32_bf16 v[136:139], v[108:111], v[80:83], v[136:139]\n\t" \
  "v_mfma_f32_16x16x32_bf16 v[140:143], v[108:111], v[84:87], v[140:143]\n\t" \
  "s_waitcnt vmcnt(3)\n\t" \
  "v_mfma_f32_16x16x32_bf16 v[136:139], v[112:115], v[88:91], v[136:139]\n\t" \
  "v_mfma_f32_16x16x32_bf16 v[140:143], v[112:115], v[92:95], v[140:143]\n\t" \
  "s_waitcnt vmcnt(1)\n\t" \
  "v_mfma_f32_16x16x32_bf16 v[136:139], v[116:119], v[96:99], v[136:139]\n\t" \
  "s_waitcnt vmcnt(0)\n\t" \
  "v_mfma_f32_16x16x32_bf16 v[140:143], v[116:119], v[100:103], v[140:143]\n\t" \
  "s_barrier\n\t" \
  "ds_read2st64_b64 v[144:147], v195 offset1:1\n\t" \
  "s_nop 1\n\t" \
  "v_permlane32_swap_b32 v136, v138\n\t" \
  "v_permlane32_swap_b32 v137, v139\n\t" \
  "s_nop 1\n\t" \
  "v_permlane32_swap_b32 v140, v142\n\t" \
  "v_permlane32_swap_b32 v141, v143\n\t" \
  "v_add_f32 v136, v136, v142\n\t" \
  "v_add_f32 v137, v137, v143\n\t" \
  "s_waitcnt lgkmcnt(0)\n\t" \
  "v_add_f32 v136, v136, v144\n\t" \
  "v_add_f32 v137, v137, v145\n\t" \
  "v_add_f32 v136, v136, v146\n\t" \
  "v_add_f32 v137, v137, v147\n\t" \
  "v_cvt_pk_bf16_f32 v148, v136, v137\n\t" \
  "s_nop 1\n\t" \
  "v_mov_b32_dpp v149, v148 row_half_mirror row_mask:0xf bank_mask:0xf\n\t" \
  "v_perm_b32 v150, v148, v149, v205\n\t" \
  "v_cmpx_o_f16_sdwa vcc, v150, v150 src0_sel:WORD_0 src1_sel:WORD_1\n\t" \
  "6:\n\t" \
  "buffer_store_dword v150, v201, %[rh], s97 offen sc1\n\t" \
  "s_memtime s[82:83]\n\t" \
  "s_mov_b64 exec, -1\n\t" \
  "s_cmp_eq_u64 vcc, -1\n\t" \
  "s_cbranch_scc0 8f\n\t" \
  "v_mov_b32 v151, 0\n\t" \
  "5:\n\t" \
  "ds_write_b32 v206, v151\n\t" \
  "s_waitcnt lgkmcnt(0)\n\t" \
  "s_mov_b32 s56, s82\n\t" \
  "s_barrier\n\t" \
  "ds_read2_b32 v[148:149], v207 offset1:8\n\t" \
  "s_waitcnt lgkmcnt(0)\n\t" \
  "v_or_b32 v148, v148, v149\n\t" \
  "v_cmp_ne_u32 vcc, 0, v148\n\t" \
  "s_cbranch_vccnz 12f\n\t" \
  "s_and_b32 s58, s58, 4\n\t" \
  "25:\n\t" \
  "s_add_i32 s82, s40, 2\n\t" \
  "s_min_i32 s83, s82, 63\n\t" \
  "s_mov_b32 exec_hi, 0\n\t" \
  "v_cmp_gt_i32 vcc, s83, v186\n\t" \
  "s_mov_b64 exec, -1\n\t" \
  "s_and_b32 s81, s82, 15\n\t" \
  "s_lshl_b32 s81, s81, 22\n\t" \
  "s_add_i32 s81, s81, s51\n\t" \
  "s_lshr_b32 s80, s81, 21\n\t" \
  "s_bitcmp0_b64 %[sel], s80\n\t" \
  "s_cselect_b32 s80, 0x2000, 0\n\t" \
  "s_add_i32 s81, s81, s80\n\t" \
  "s_cbranch_vccnz 27f\n\t" \
  "28:\n\t" \
  "s_cmp_lt_i32 s40, 62\n\t" \
  "s_cselect_b32 s81, s81, 0x70000000\n\t" \
  "buffer_load_dwordx4 v[0:3], v210, %[ru], s81 offen offset:0 sc1\n\t" \
  "buffer_load_dwordx4 v[4:7], v210, %[ru], s81 offen offset:1024 sc1\n\t" \
  "s_add_i32 s81, s81, 0x20000\n\t" \
  "buffer_load_dwordx4 v[8:11], v210, %[ru], s81 offen offset:0 sc1\n\t" \
  "buffer_load_dwordx4 v[12:15], v210, %[ru], s81 offen offset:1024 sc1\n\t" \
  "s_add_i32 s81, s81, 0x20000\n\t" \
  "buffer_load_dwordx4 v[16:19], v210, %[ru], s81 offen offset:0 sc1\n\t" \
  "buffer_load_dwordx4 v[20:23], v210, %[ru], s81 offen offset:1024 sc1\n\t" \
  "s_add_i32 s81, s81, 0x20000\n\t" \
  "buffer_load_dwordx4 v[24:27], v210, %[ru], s81 offen offset:0 sc1\n\t" \
  "buffer_load_dwordx4 v[28:31], v210, %[ru], s81 offen offset:1024 sc1\n\t" \
  "s_add_i32 s81, s81, 0x20000\n\t" \
  "s_bitcmp1_b32 s58, 4\n\t" \
  "s_cbranch_scc1 29f\n\t" \
  "s_sub_i32 s80, s56, s41\n\t" \
  "s_mul_hi_u32 s80, s80, s45\n\t" \
  "s_lshl_b32 s80, s80, 1\n\t" \
  "s_sub_i32 s80, s80, s57\n\t" \
  "s_min_u32 s47, s47, s46\n\t" \
  "s_min_u32 s47, s47, s80\n\t" \
  "v_mov_b32 v188, s47\n\t" \
  "s_mov_b32 s47, s46\n\t" \
  "s_mov_b32 s46, s80\n\t" \
  "buffer_store_dword v188, v202, %[rh], s52 offen sc1\n\t" \
  "v_mov_b64 v[188:189], -1\n\t" \
  "v_mov_b64 v[190:191], -1\n\t" \
  "v_mov_b32 v187, s40\n\t" \
  "s_add_i32 s80, s40, -1\n\t" \
  "s_and_b32 s80, s80, 7\n\t" \
  "s_lshl_b32 s80, s80, 15\n\t" \
  "s_add_i32 s80, s80, s49\n\t" \
  "buffer_store_dwordx4 v[188:191], v208, %[rh], s80 offen sc1\n\t" \
  "buffer_store_dword v187, v209, %[ru], 0 offen sc1\n\t" \
  "ds_write_b128 v197, v[72:75] offset:16384\n\t" \
  "ds_write_b128 v197, v[76:79] offset:17408\n\t" \
  "ds_write_b128 v197, v[80:83] offset:18432\n\t" \
  "ds_write_b128 v197, v[84:87] offset:19456\n\t" \
  "ds_write_b128 v197, v[88:91] offset:20480\n\t" \
  "ds_write_b128 v197, v[92:95] offset:21504\n\t" \
  "ds_write_b128 v197, v[96:99] offset:22528\n\t" \
  "ds_write_b128 v197, v[100:103] offset:23552\n\t" \
  "29:\n\t" \
  "s_and_b32 s58, s58, 36\n\t" \
  "buffer_load_dwordx4 v[32:35], v210, %[ru], s81 offen offset:0 sc1\n\t" \
  "buffer_load_dwordx4 v[36:39], v210, %[ru], s81 offen offset:1024 sc1\n\t" \
  "s_add_i32 s81, s81, 0x20000\n\t" \
  "buffer_load_dwordx4 v[40:43], v210, %[ru], s81 offen offset:0 sc1\n\t" \
  "buffer_load_dwordx4 v[44:47], v210, %[ru], s81 offen offset:1024 sc1\n\t" \
  "s_add_i32 s81, s81, 0x20000\n\t" \
  "buffer_load_dwordx4 v[48:51], v210, %[ru], s81 offen offset:0 sc1\n\t" \
  "buffer_load_dwordx4 v[52:55], v210, %[ru], s81 offen offset:1024 sc1\n\t" \
  "s_add_i32 s81, s81, 0x20000\n\t" \
  "buffer_load_dwordx4 v[56:59], v210, %[ru], s81 offen offset:0 sc1\n\t" \
  "buffer_load_dwordx4 v[60:63], v210, %[ru], s81 offen offset:1024 sc1\n\t" \
  "s_add_i32 s81, s81, 0x20000\n\t" \
  "buffer_load_dwordx4 v[64:67], v210, %[ru], s81 offen offset:0 sc1\n\t" \
  "buffer_load_dwordx4 v[68:71], v210, %[ru], s81 offen offset:1024 sc1\n\t" \
  "s_add_i32 s81, s81, 0x20000\n\t" \
  "buffer_load_dwordx4 v[104:107], v210, %[ru], s81 offen offset:0 sc1\n\t" \
  "buffer_load_dwordx4 v[108:111], v210, %[ru], s81 offen offset:1024 sc1\n\t" \
  "s_add_i32 s81, s81, 0x20000\n\t" \
  "ds_read_b128 v[160:163], v193 offset:0\n\t" \
  "ds_read_b128 v[164:167], v193 offset:256\n\t" \
  "ds_read_b128 v[168:171], v193 offset:2048\n\t" \
  "ds_read_b128 v[172:175], v193 offset:2304\n\t" \
  "ds_read_b128 v[176:179], v193 offset:4096\n\t" \
  "ds_read_b128 v[180:183], v193 offset:4352\n\t" \
  "s_waitcnt lgkmcnt(2)\n\t" \
  "v_pk_add_f32 v[160:161], v[160:161], v[168:169]\n\t" \
  "v_pk_add_f32 v[162:163], v[162:163], v[170:171]\n\t" \
  "v_pk_add_f32 v[164:165], v[164:165], v[172:173]\n\t" \
  "v_pk_add_f32 v[166:167], v[166:167], v[174:175]\n\t" \
  "s_waitcnt lgkmcnt(0)\n\t" \
  "v_pk_add_f32 v[160:161], v[160:161], v[176:177]\n\t" \
  "v_pk_add_f32 v[162:163], v[162:163], v[178:179]\n\t" \
  "v_pk_add_f32 v[164:165], v[164:165], v[180:181]\n\t" \
  "v_pk_add_f32 v[166:167], v[166:167], v[182:183]\n\t" \
  "v_cvt_pk_bf16_f32 v160, v160, v161\n\t" \
  "v_cvt_pk_bf16_f32 v161, v162, v163\n\t" \
  "v_cvt_pk_bf16_f32 v162, v164, v165\n\t" \
  "v_cvt_pk_bf16_f32 v163, v166, v167\n\t" \
  "s_memtime vcc\n\t" \
  "s_memrealtime s[82:83]\n\t" \
  "s_nop 4\n\t" \
  "s_memrealtime s[84:85]\n\t" \
  "s_nop 4\n\t" \
  "s_memrealtime s[86:87]\n\t" \
  "s_nop 4\n\t" \
  "s_memrealtime s[88:89]\n\t" \
  "s_nop 4\n\t" \
  "s_memrealtime s[90:91]\n\t" \
  "s_nop 4\n\t" \
  "s_memrealtime s[92:93]\n\t" \
  "s_mov_b32 s94, 0\n\t" \
  "s_waitcnt lgkmcnt(0)\n\t" \
  "s_cmp_eq_u32 s82, s84\n\t" \
  "s_addc_u32 s94, s94, 0\n\t" \
  "s_cmp_eq_u32 s82, s86\n\t" \
  "s_addc_u32 s94, s94, 0\n\t" \
  "s_cmp_eq_u32 s82, s88\n\t" \
  "s_addc_u32 s94, s94, 0\n\t" \
  "s_cmp_eq_u32 s82, s90\n\t" \
  "s_addc_u32 s94, s94, 0\n\t" \
  "s_cmp_eq_u32 s82, s92\n\t" \
  "s_addc_u32 s94, s94, 0\n\t" \
  "s_mul_i32 s94, s94, 24\n\t" \
  "s_add_i32 s94, s94, vcc_lo\n\t" \
  "s_lshl4_add_u32 s95, s82, 64\n\t" \
  "s_sub_i32 s82, s94, s41\n\t" \
  "s_sub_i32 s83, s95, s43\n\t" \
  "s_cmp_lt_u32 s82, 4000\n\t" \
  "s_cbranch_scc1 21f\n\t" \
  "v_cvt_f32_u32 v165, s82\n\t" \
  "v_cvt_f32_u32 v164, s83\n\t" \
  "v_rcp_f32 v165, v165\n\t" \
  "s_mov_b32 s41, s94\n\t" \
  "v_mul_f32 v165, v165, v164\n\t" \
  "v_max_f32 v165, 0x3f028f5c, v165\n\t" \
  "v_min_f32 v165, 0x3ffeb852, v165\n\t" \
  "v_rcp_f32 v164, v165\n\t" \
  "v_mul_f32 v165, 0x4f000000, v165\n\t" \
  "v_mul_f32 v164, 0x47800000, v164\n\t" \
  "v_cvt_u32_f32 v165, v165\n\t" \
  "v_cvt_u32_f32 v164, v164\n\t" \
  "s_mov_b32 s43, s95\n\t" \
  "v_readfirstlane_b32 s45, v165\n\t" \
  "v_readfirstlane_b32 s44, v164\n\t" \
  "21:\n\t" \
  "v_mov_b32 v164, s41\n\t" \
  "v_mov_b32 v165, s43\n\t" \
  "v_mov_b32 v166, s44\n\t" \
  "v_mov_b32 v167, s45\n\t" \
  "ds_write_b128 v206, v[164:167] offset:16\n\t" \
  "buffer_load_dwordx4 v[112:115], v210, %[ru], s81 offen offset:0 sc1\n\t" \
  "buffer_load_dwordx4 v[116:119], v210, %[ru], s81 offen offset:1024 sc1\n\t" \
  "s_add_i32 s81, s81, 0x20000\n\t" \
  "buffer_load_dwordx4 v[120:123], v210, %[ru], s81 offen offset:0 sc1\n\t" \
  "buffer_load_dwordx4 v[124:127], v210, %[ru], s81 offen offset:1024 sc1\n\t" \
  "s_add_i32 s81, s81, 0x20000\n\t" \
  "s_waitcnt vmcnt(20)\n\t" \
  "v_swap_b32 v1, v8\n\t" \
  "v_swap_b32 v3, v10\n\t" \
  "v_swap_b32 v5, v12\n\t" \
  "v_swap_b32 v7, v14\n\t" \
  "ds_write_b128 v204, v[0:3] offset:0\n\t" \
  "ds_write_b128 v204, v[8:11] offset:1024\n\t" \
  "ds_write_b128 v204, v[4:7] offset:2048\n\t" \
  "ds_write_b128 v204, v[12:15] offset:3072\n\t" \
  "buffer_load_dwordx4 v[128:131], v210, %[ru], s81 offen offset:0 sc1\n\t" \
  "buffer_load_dwordx4 v[132:135], v210, %[ru], s81 offen offset:1024 sc1\n\t" \
  "s_add_i32 s81, s81, 0x20000\n\t" \
  "buffer_load_dwordx4 v[136:139], v210, %[ru], s81 offen offset:0 sc1\n\t" \
  "buffer_load_dwordx4 v[140:143], v210, %[ru], s81 offen offset:1024 sc1\n\t" \
  "s_add_i32 s81, s81, 0x20000\n\t" \
  "s_waitcnt vmcnt(20)\n\t" \
  "v_swap_b32 v17, v24\n\t" \
  "v_swap_b32 v19, v26\n\t" \
  "v_swap_b32 v21, v28\n\t" \
  "v_swap_b32 v23, v30\n\t" \
  "ds_write_b128 v204, v[16:19] offset:4096\n\t" \
  "ds_write_b128 v204, v[24:27] offset:5120\n\t" \
  "ds_write_b128 v204, v[20:23] offset:6144\n\t" \
  "ds_write_b128 v204, v[28:31] offset:7168\n\t" \
  "buffer_load_dwordx4 v[144:147], v210, %[ru], s81 offen offset:0 sc1\n\t" \
  "buffer_load_dwordx4 v[148:151], v210, %[ru], s81 offen offset:1024 sc1\n\t" \
  "s_add_i32 s81, s81, 0x20000\n\t" \
  "buffer_load_dwordx4 v[152:155], v210, %[ru], s81 offen offset:0 sc1\n\t" \
  "buffer_load_dwordx4 v[156:159], v210, %[ru], s81 offen offset:1024 sc1\n\t" \
  "s_add_i32 s81, s81, 0x20000\n\t" \
  "s_barrier\n\t" \
  "s_waitcnt vmcnt(20)\n\t" \
  "v_swap_b32 v33, v40\n\t" \
  "v_swap_b32 v35, v42\n\t" \
  "v_swap_b32 v37, v44\n\t" \
  "v_swap_b32 v39, v46\n\t" \
  "ds_write_b128 v204, v[32:35] offset:8192\n\t" \
  "ds_write_b128 v204, v[40:43] offset:9216\n\t" \
  "ds_write_b128 v204, v[36:39] offset:10240\n\t" \
  "ds_write_b128 v204, v[44:47] offset:11264\n\t" \
  "s_waitcnt vmcnt(16)\n\t" \
  "v_swap_b32 v49, v56\n\t" \
  "v_swap_b32 v51, v58\n\t" \
  "v_swap_b32 v53, v60\n\t" \
  "v_swap_b32 v55, v62\n\t" \
  "ds_write_b128 v204, v[48:51] offset:12288\n\t" \
  "ds_write_b128 v204, v[56:59] offset:13312\n\t" \
  "ds_write_b128 v204, v[52:55] offset:14336\n\t" \
  "ds_write_b128 v204, v[60:63] offset:15360\n\t" \
  "s_waitcnt vmcnt(12)\n\t" \
  "v_swap_b32 v65, v104\n\t" \
  "v_swap_b32 v67, v106\n\t" \
  "v_swap_b32 v69, v108\n\t" \
  "v_swap_b32 v71, v110\n\t" \
  "ds_write_b128 v204, v[64:67] offset:16384\n\t" \
  "ds_write_b128 v204, v[104:107] offset:17408\n\t" \
  "ds_write_b128 v204, v[68:71] offset:18432\n\t" \
  "ds_write_b128 v204, v[108:111] offset:19456\n\t" \
  "s_waitcnt vmcnt(8)\n\t" \
  "v_swap_b32 v113, v120\n\t" \
  "v_swap_b32 v115, v122\n\t" \
  "v_swap_b32 v117, v124\n\t" \
  "v_swap_b32 v119, v126\n\t" \
  "ds_write_b128 v204, v[112:115] offset:20480\n\t" \
  "ds_write_b128 v204, v[120:123] offset:21504\n\t" \
  "ds_write_b128 v204, v[116:119] offset:22528\n\t" \
  "ds_write_b128 v204, v[124:127] offset:23552\n\t" \
  "s_waitcnt vmcnt(4)\n\t" \
  "v_swap_b32 v129, v136\n\t" \
  "v_swap_b32 v131, v138\n\t" \
  "v_swap_b32 v133, v140\n\t" \
  "v_swap_b32 v135, v142\n\t" \
  "ds_write_b128 v204, v[128:131] offset:24576\n\t" \
  "ds_write_b128 v204, v[136:139] offset:25600\n\t" \
  "ds_write_b128 v204, v[132:135] offset:26624\n\t" \
  "ds_write_b128 v204, v[140:143] offset:27648\n\t" \
  "s_waitcnt vmcnt(0)\n\t" \
  "v_swap_b32 v145, v152\n\t" \
  "v_swap_b32 v147, v154\n\t" \
  "v_swap_b32 v149, v156\n\t" \
  "v_swap_b32 v151, v158\n\t" \
  "ds_write_b128 v204, v[144:147] offset:28672\n\t" \
  "ds_write_b128 v204, v[152:155] offset:29696\n\t" \
  "ds_write_b128 v204, v[148:151] offset:30720\n\t" \
  "ds_write_b128 v204, v[156:159] offset:31744\n\t" \
  "s_waitcnt lgkmcnt(0)\n\t" \
  "s_barrier\n\t" \
  "s_add_i32 s85, s40, 1\n\t" \
  "s_and_b32 s76, s40, 3\n\t" \
  "s_lshl_b32 s76, s76, 18\n\t" \
  "buffer_load_dwordx4 a[0:3], v211, %[rh], s76 offen offset:0 sc1\n\t" \
  "buffer_load_dwordx4 a[4:7], v211, %[rh], s76 offen offset:1024 sc1\n\t" \
  "buffer_load_dwordx4 a[8:11], v212, %[rh], s76 offen offset:0 sc1\n\t" \
  "buffer_load_dwordx4 a[12:15], v212, %[rh], s76 offen offset:1024 sc1\n\t" \
  "buffer_load_dwordx4 a[16:19], v213, %[rh], s76 offen offset:0 sc1\n\t" \
  "buffer_load_dwordx4 a[20:23], v213, %[rh], s76 offen offset:1024 sc1\n\t" \
  "buffer_load_dwordx4 a[24:27], v214, %[rh], s76 offen offset:0 sc1\n\t" \
  "buffer_load_dwordx4 a[28:31], v214, %[rh], s76 offen offset:1024 sc1\n\t" \
  "s_and_b32 s82, s85, 2\n\t" \
  "s_lshl_b32 s82, s82, 13\n\t" \
  "s_and_b32 s80, s85, 1\n\t" \
  "s_lshl_b32 s80, s80, 12\n\t" \
  "s_add_i32 s82, s82, s80\n\t" \
  "s_add_i32 s82, s82, 0x84000\n\t" \
  "s_mov_b32 exec_hi, 0\n\t" \
  "buffer_load_dword v184, v200, %[rh], s82 offen sc1\n\t" \
  "buffer_load_dword v186, v203, %[ru], 0 offen sc1\n\t" \
  "s_mov_b64 exec, -1\n\t" \
  "s_add_i32 s80, s40, -2\n\t" \
  "s_lshl_b32 s80, s80, 17\n\t" \
  "s_min_u32 s80, s80, 0x70000000\n\t" \
  "buffer_store_dwordx4 v[160:163], v196, %[ro], s80 offen nt\n\t" \
  "ds_read_b128 a[32:35], v198 offset:0\n\t" \
  "ds_read_b128 a[36:39], v198 offset:1024\n\t" \
  "ds_read_b128 a[40:43], v198 offset:2048\n\t" \
  "ds_read_b128 a[44:47], v198 offset:3072\n\t" \
  "ds_read_b128 a[48:51], v198 offset:4096\n\t" \
  "ds_read_b128 a[52:55], v198 offset:5120\n\t" \
  "ds_read_b128 a[56:59], v198 offset:6144\n\t" \
  "ds_read_b128 a[60:63], v198 offset:7168\n\t" \
  "ds_read_b128 a[64:67], v198 offset:8192\n\t" \
  "ds_read_b128 a[68:71], v198 offset:9216\n\t" \
  "ds_read_b128 a[72:75], v198 offset:10240\n\t" \
  "ds_read_b128 a[76:79], v198 offset:11264\n\t" \
  "ds_read_b128 a[80:83], v198 offset:12288\n\t" \
  "ds_read_b128 a[84:87], v198 offset:13312\n\t" \
  "ds_read_b128 a[88:91], v198 offset:14336\n\t" \
  "ds_read_b128 a[92:95], v198 offset:15360\n\t" \
  "s_waitcnt vmcnt(3) lgkmcnt(0)\n\t" \
  "31:\n\t" \
  "v_mfma_f32_16x16x32_bf16 v[168:171], a[32:35], a[0:3], 0\n\t" \
  "v_mfma_f32_16x16x32_bf16 v[172:175], a[64:67], a[0:3], 0\n\t" \
  "v_mfma_f32_16x16x32_bf16 v[168:171], a[36:39], a[4:7], v[168:171]\n\t" \
  "v_mfma_f32_16x16x32_bf16 v[172:175], a[68:71], a[4:7], v[172:175]\n\t" \
  "v_mfma_f32_16x16x32_bf16 v[168:171], a[40:43], a[8:11], v[168:171]\n\t" \
  "v_mfma_f32_16x16x32_bf16 v[172:175], a[72:75], a[8:11], v[172:175]\n\t" \
  "v_mfma_f32_16x16x32_bf16 v[168:171], a[44:47], a[12:15], v[168:171]\n\t" \
  "v_mfma_f32_16x16x32_bf16 v[172:175], a[76:79], a[12:15], v[172:175]\n\t" \
  "v_mfma_f32_16x16x32_bf16 v[168:171], a[48:51], a[16:19], v[168:171]\n\t" \
  "v_mfma_f32_16x16x32_bf16 v[172:175], a[80:83], a[16:19], v[172:175]\n\t" \
  "v_mfma_f32_16x16x32_bf16 v[168:171], a[52:55], a[20:23], v[168:171]\n\t" \
  "v_mfma_f32_16x16x32_bf16 v[172:175], a[84:87], a[20:23], v[172:175]\n\t" \
  "v_mfma_f32_16x16x32_bf16 v[168:171], a[56:59], a[24:27], v[168:171]\n\t" \
  "v_mfma_f32_16x16x32_bf16 v[172:175], a[88:91], a[24:27], v[172:175]\n\t" \
  "v_mfma_f32_16x16x32_bf16 v[168:171], a[60:63], a[28:31], v[168:171]\n\t" \
  "v_mfma_f32_16x16x32_bf16 v[172:175], a[92:95], a[28:31], v[172:175]\n\t" \
  "s_nop 7\n\t" \
  "s_add_i32 s40, s40, 2\n\t" \
  "v_cmp_u_f32 vcc, v168, v168\n\t" \
  "s_cbranch_vccnz 32f\n\t" \
  "33:\n\t" \
  "ds_write_b128 v199, v[168:171] offset:0\n\t" \
  "ds_write_b128 v199, v[172:175] offset:1024\n\t" \
  "s_mov_b32 s59, 0\n\t" \
  "s_and_b32 s58, s58, 4\n\t" \
  "s_cmp_lt_i32 s40, 64\n\t" \
  "s_cbranch_scc1 20b\n\t" \
  "s_waitcnt lgkmcnt(0)\n\t" \
  "s_barrier\n\t" \
  "ds_read_b128 v[160:163], v193 offset:0\n\t" \
  "ds_read_b128 v[164:167], v193 offset:256\n\t" \
  "ds_read_b128 v[168:171], v193 offset:2048\n\t" \
  "ds_read_b128 v[172:175], v193 offset:2304\n\t" \
  "ds_read_b128 v[176:179], v193 offset:4096\n\t" \
  "ds_read_b128 v[180:183], v193 offset:4352\n\t" \
  "s_waitcnt lgkmcnt(2)\n\t" \
  "v_pk_add_f32 v[160:161], v[160:161], v[168:169]\n\t" \
  "v_pk_add_f32 v[162:163], v[162:163], v[170:171]\n\t" \
  "v_pk_add_f32 v[164:165], v[164:165], v[172:173]\n\t" \
  "v_pk_add_f32 v[166:167], v[166:167], v[174:175]\n\t" \
  "s_waitcnt lgkmcnt(0)\n\t" \
  "v_pk_add_f32 v[160:161], v[160:161], v[176:177]\n\t" \
  "v_pk_add_f32 v[162:163], v[162:163], v[178:179]\n\t" \
  "v_pk_add_f32 v[164:165], v[164:165], v[180:181]\n\t" \
  "v_pk_add_f32 v[166:167], v[166:167], v[182:183]\n\t" \
  "v_cvt_pk_bf16_f32 v160, v160, v161\n\t" \
  "v_cvt_pk_bf16_f32 v161, v162, v163\n\t" \
  "v_cvt_pk_bf16_f32 v162, v164, v165\n\t" \
  "v_cvt_pk_bf16_f32 v163, v166, v167\n\t" \
  "s_add_i32 s80, s40, -2\n\t" \
  "s_lshl_b32 s80, s80, 17\n\t" \
  "s_min_u32 s80, s80, 0x70000000\n\t" \
  "buffer_store_dwordx4 v[160:163], v196, %[ro], s80 offen nt\n\t" \
  "s_mov_b32 s58, 64\n\t" \
  "s_branch 7f\n\t" \
  "22:\n\t" \
  "s_mov_b32 s81, 0\n\t" \
  "s_cmp_eq_u32 s40, 0\n\t" \
  "s_cbranch_scc1 24b\n\t" \
  "s_bitcmp1_b32 s58, 2\n\t" \
  "s_cbranch_scc1 24b\n\t" \
  "s_mov_b64 exec, -1\n\t" \
  "s_add_u32 s59, s59, 0x10000\n\t" \
  "s_cmp_lt_u32 s59, 0x100000\n\t" \
  "s_cbranch_scc1 19b\n\t" \
  "s_or_b32 s58, s58, 1\n\t" \
  "s_branch 7f\n\t" \
  "14:\n\t" \
  "s_bitcmp1_b32 s58, 2\n\t" \
  "s_cbranch_scc1 15b\n\t" \
  "s_mov_b32 s84, 1000\n\t" \
  "s_add_u32 s59, s59, 0x10000\n\t" \
  "s_cmp_lt_u32 s59, 0x100000\n\t" \
  "s_cbranch_scc1 13b\n\t" \
  "s_mov_b64 exec, -1\n\t" \
  "s_or_b32 s58, s58, 1\n\t" \
  "s_branch 7f\n\t" \
  "27:\n\t" \
  "s_bitcmp1_b32 s58, 2\n\t" \
  "s_cbranch_scc1 28b\n\t" \
  "s_bitcmp1_b32 s58, 4\n\t" \
  "s_cbranch_scc1 23f\n\t" \
  "s_sub_i32 s80, s56, s41\n\t" \
  "s_mul_hi_u32 s80, s80, s45\n\t" \
  "s_lshl_b32 s80, s80, 1\n\t" \
  "s_sub_i32 s80, s80, s57\n\t" \
  "s_min_u32 s47, s47, s46\n\t" \
  "s_min_u32 s47, s47, s80\n\t" \
  "v_mov_b32 v188, s47\n\t" \
  "s_mov_b32 s47, s46\n\t" \
  "s_mov_b32 s46, s80\n\t" \
  "buffer_store_dword v188, v202, %[rh], s52 offen sc1\n\t" \
  "v_mov_b64 v[188:189], -1\n\t" \
  "v_mov_b64 v[190:191], -1\n\t" \
  "v_mov_b32 v187, s40\n\t" \
  "s_add_i32 s80, s40, -1\n\t" \
  "s_and_b32 s80, s80, 7\n\t" \
  "s_lshl_b32 s80, s80, 15\n\t" \
  "s_add_i32 s80, s80, s49\n\t" \
  "buffer_store_dwordx4 v[188:191], v208, %[rh], s80 offen sc1\n\t" \
  "buffer_store_dword v187, v209, %[ru], 0 offen sc1\n\t" \
  "ds_write_b128 v197, v[72:75] offset:16384\n\t" \
  "ds_write_b128 v197, v[76:79] offset:17408\n\t" \
  "ds_write_b128 v197, v[80:83] offset:18432\n\t" \
  "ds_write_b128 v197, v[84:87] offset:19456\n\t" \
  "ds_write_b128 v197, v[88:91] offset:20480\n\t" \
  "ds_write_b128 v197, v[92:95] offset:21504\n\t" \
  "ds_write_b128 v197, v[96:99] offset:22528\n\t" \
  "ds_write_b128 v197, v[100:103] offset:23552\n\t" \
  "s_or_b32 s58, s58, 16\n\t" \
  "23:\n\t" \
  "s_add_u32 s59, s59, 0x1000000\n\t" \
  "s_cmp_lt_u32 s59, 0x10000000\n\t" \
  "s_cbranch_scc1 26f\n\t" \
  "s_or_b32 s58, s58, 1\n\t" \
  "s_branch 7f\n\t" \
  "26:\n\t" \
  "buffer_load_dword v186, v203, %[ru], 0 offen sc1\n\t" \
  "s_waitcnt vmcnt(0)\n\t" \
  "s_branch 25b\n\t" \
  "8:\n\t" \
  "s_mov_b64 vcc, -1\n\t" \
  "s_and_b32 s80, s58, 6\n\t" \
  "s_cbranch_scc1 6b\n\t" \
  "v_mov_b32 v151, 1\n\t" \
  "s_branch 5b\n\t" \
  "32:\n\t" \
  "s_and_b32 s80, s58, 36\n\t" \
  "s_cbranch_scc1 33b\n\t" \
  "s_add_i32 s40, s40, -2\n\t" \
  "34:\n\t" \
  "s_or_b32 s59, s59, 128\n\t" \
  "s_and_b32 s76, s40, 3\n\t" \
  "s_lshl_b32 s76, s76, 18\n\t" \
  "35:\n\t" \
  "buffer_load_dwordx4 a[0:3], v211, %[rh], s76 offen offset:0 sc1\n\t" \
  "buffer_load_dwordx4 a[4:7], v211, %[rh], s76 offen offset:1024 sc1\n\t" \
  "buffer_load_dwordx4 a[8:11], v212, %[rh], s76 offen offset:0 sc1\n\t" \
  "buffer_load_dwordx4 a[12:15], v212, %[rh], s76 offen offset:1024 sc1\n\t" \
  "buffer_load_dwordx4 a[16:19], v213, %[rh], s76 offen offset:0 sc1\n\t" \
  "buffer_load_dwordx4 a[20:23], v213, %[rh], s76 offen offset:1024 sc1\n\t" \
  "buffer_load_dwordx4 a[24:27], v214, %[rh], s76 offen offset:0 sc1\n\t" \
  "buffer_load_dwordx4 a[28:31], v214, %[rh], s76 offen offset:1024 sc1\n\t" \
  "s_add_i32 s85, s40, 1\n\t" \
  "s_and_b32 s82, s85, 2\n\t" \
  "s_lshl_b32 s82, s82, 13\n\t" \
  "s_and_b32 s80, s85, 1\n\t" \
  "s_lshl_b32 s80, s80, 12\n\t" \
  "s_add_i32 s82, s82, s80\n\t" \
  "s_add_i32 s82, s82, 0x84000\n\t" \
  "s_mov_b32 exec_hi, 0\n\t" \
  "buffer_load_dword v184, v200, %[rh], s82 offen sc1\n\t" \
  "buffer_load_dword v186, v203, %[ru], 0 offen sc1\n\t" \
  "s_mov_b64 exec, -1\n\t" \
  "s_waitcnt vmcnt(0)\n\t" \
  "s_bitcmp1_b32 s58, 2\n\t" \
  "s_cbranch_scc1 36f\n\t" \
  "v_accvgpr_read_b32 v0, a0\n\t" \
  "v_accvgpr_read_b32 v1, a1\n\t" \
  "v_accvgpr_read_b32 v2, a2\n\t" \
  "v_accvgpr_read_b32 v3, a3\n\t" \
  "v_accvgpr_read_b32 v4, a4\n\t" \
  "v_accvgpr_read_b32 v5, a5\n\t" \
  "v_accvgpr_read_b32 v6, a6\n\t" \
  "v_accvgpr_read_b32 v7, a7\n\t" \
  "v_accvgpr_read_b32 v8, a8\n\t" \
  "v_accvgpr_read_b32 v9, a9\n\t" \
  "v_accvgpr_read_b32 v10, a10\n\t" \
  "v_accvgpr_read_b32 v11, a11\n\t" \
  "v_accvgpr_read_b32 v12, a12\n\t" \
  "v_accvgpr_read_b32 v13, a13\n\t" \
  "v_accvgpr_read_b32 v14, a14\n\t" \
  "v_accvgpr_read_b32 v15, a15\n\t" \
  "v_accvgpr_read_b32 v16, a16\n\t" \
  "v_accvgpr_read_b32 v17, a17\n\t" \
  "v_accvgpr_read_b32 v18, a18\n\t" \
  "v_accvgpr_read_b32 v19, a19\n\t" \
  "v_accvgpr_read_b32 v20, a20\n\t" \
  "v_accvgpr_read_b32 v21, a21\n\t" \
  "v_accvgpr_read_b32 v22, a22\n\t" \
  "v_accvgpr_read_b32 v23, a23\n\t" \
  "v_accvgpr_read_b32 v24, a24\n\t" \
  "v_accvgpr_read_b32 v25, a25\n\t" \
  "v_accvgpr_read_b32 v26, a26\n\t" \
  "v_accvgpr_read_b32 v27, a27\n\t" \
  "v_accvgpr_read_b32 v28, a28\n\t" \
  "v_accvgpr_read_b32 v29, a29\n\t" \
  "v_accvgpr_read_b32 v30, a30\n\t" \
  "v_accvgpr_read_b32 v31, a31\n\t" \
  "v_max_u32 v148, v0, v1\n\t" \
  "v_max3_u32 v148, v148, v2, v3\n\t" \
  "v_max3_u32 v148, v148, v4, v5\n\t" \
  "v_max3_u32 v148, v148, v6, v7\n\t" \
  "v_max3_u32 v148, v148, v8, v9\n\t" \
  "v_max3_u32 v148, v148, v10, v11\n\t" \
  "v_max3_u32 v148, v148, v12, v13\n\t" \
  "v_max3_u32 v148, v148, v14, v15\n\t" \
  "v_max3_u32 v148, v148, v16, v17\n\t" \
  "v_max3_u32 v148, v148, v18, v19\n\t" \
  "v_max3_u32 v148, v148, v20, v21\n\t" \
  "v_max3_u32 v148, v148, v22, v23\n\t" \
  "v_max3_u32 v148, v148, v24, v25\n\t" \
  "v_max3_u32 v148, v148, v26, v27\n\t" \
  "v_max3_u32 v148, v148, v28, v29\n\t" \
  "v_max3_u32 v148, v148, v30, v31\n\t" \
  "v_cmp_eq_u32 vcc, -1, v148\n\t" \
  "s_cbranch_vccz 36f\n\t" \
  "s_add_u32 s59, s59, 0x1000000\n\t" \
  "s_cmp_lt_u32 s59, 0x10000000\n\t" \
  "s_cbranch_scc1 35b\n\t" \
  "s_or_b32 s58, s58, 9\n\t" \
  "s_branch 7f\n\t" \
  "36:\n\t" \
  "s_and_b32 s58, s58, 4\n\t" \
  "s_or_b32 s58, s58, 32\n\t" \
  "s_branch 31b\n\t" \
  "18:\n\t" \
  "v_mov_b32 v186, -1\n\t" \
  "s_and_b32 s80, s40, 3\n\t" \
  "s_mul_i32 s77, s80, 0xc000\n\t" \
  "s_add_i32 s77, s77, 0x40000\n\t" \
  "s_lshl_b32 s78, s80, 14\n\t" \
  "s_add_i32 s78, s78, 0x70000\n\t" \
  "s_mov_b32 s57, 0\n\t" \
  "s_add_i32 s96, s77, 0x1000\n\t" \
  "s_lshl_b32 s88, s80, 20\n\t" \
  "s_add_i32 s88, s88, s48\n\t" \
  "s_lshr_b32 s80, s88, 21\n\t" \
  "s_bitcmp1_b64 %[sel], s80\n\t" \
  "s_cselect_b32 s80, 0x2000, 0\n\t" \
  "s_add_i32 s88, s88, s80\n\t" \
  "s_xor_b32 s89, s88, 0x800\n\t" \
  "s_xor_b32 s90, s88, 0x1000\n\t" \
  "s_xor_b32 s91, s88, 0x1800\n\t" \
  "s_add_i32 s92, s77, s50\n\t" \
  "s_xor_b32 s93, s92, 0x100\n\t" \
  "s_xor_b32 s94, s92, 0x200\n\t" \
  "s_xor_b32 s95, s92, 0x300\n\t" \
  "s_add_i32 s97, s40, 1\n\t" \
  "s_and_b32 s97, s97, 7\n\t" \
  "s_lshl_b32 s97, s97, 15\n\t" \
  "s_add_i32 s97, s97, s49\n\t" \
  "12:\n\t" \
  "s_or_b32 s58, s58, 3\n\t" \
  "11:\n\t" \
  "buffer_load_dwordx4 v[72:75], v192, %[ru], s88 offen offset:0 sc1\n\t" \
  "buffer_load_dwordx4 v[76:79], v192, %[ru], s88 offen offset:1024 sc1\n\t" \
  "buffer_load_dwordx4 v[80:83], v192, %[ru], s89 offen offset:0 sc1\n\t" \
  "buffer_load_dwordx4 v[84:87], v192, %[ru], s89 offen offset:1024 sc1\n\t" \
  "buffer_load_dwordx4 v[88:91], v192, %[ru], s90 offen offset:0 sc1\n\t" \
  "buffer_load_dwordx4 v[92:95], v192, %[ru], s90 offen offset:1024 sc1\n\t" \
  "buffer_load_dwordx4 v[96:99], v192, %[ru], s91 offen offset:0 sc1\n\t" \
  "buffer_load_dwordx4 v[100:103], v192, %[ru], s91 offen offset:1024 sc1\n\t" \
  "buffer_load_dwordx4 v[104:107], v194, %[rh], s92 offen sc1\n\t" \
  "buffer_load_dwordx4 v[108:111], v194, %[rh], s93 offen sc1\n\t" \
  "buffer_load_dwordx4 v[112:115], v194, %[rh], s94 offen sc1\n\t" \
  "buffer_load_dwordx4 v[116:119], v194, %[rh], s95 offen sc1\n\t" \
  "s_waitcnt vmcnt(0)\n\t" \
  "s_add_u32 s59, s59, 1\n\t" \
  "s_bitcmp1_b32 s58, 2\n\t" \
  "s_cbranch_scc1 10b\n\t" \
  "v_max_u32 v148, v72, v73\n\t" \
  "v_max3_u32 v148, v148, v74, v75\n\t" \
  "v_max3_u32 v148, v148, v76, v77\n\t" \
  "v_max3_u32 v148, v148, v78, v79\n\t" \
  "v_max3_u32 v148, v148, v80, v81\n\t" \
  "v_max3_u32 v148, v148, v82, v83\n\t" \
  "v_max3_u32 v148, v148, v84, v85\n\t" \
  "v_max3_u32 v148, v148, v86, v87\n\t" \
  "v_max3_u32 v148, v148, v88, v89\n\t" \
  "v_max3_u32 v148, v148, v90, v91\n\t" \
  "v_max3_u32 v148, v148, v92, v93\n\t" \
  "v_max3_u32 v148, v148, v94, v95\n\t" \
  "v_max3_u32 v148, v148, v96, v97\n\t" \
  "v_max3_u32 v148, v148, v98, v99\n\t" \
  "v_max3_u32 v148, v148, v100, v101\n\t" \
  "v_max3_u32 v148, v148, v102, v103\n\t" \
  "v_max3_u32 v148, v148, v104, v105\n\t" \
  "v_max3_u32 v148, v148, v106, v107\n\t" \
  "v_max3_u32 v148, v148, v108, v109\n\t" \
  "v_max3_u32 v148, v148, v110, v111\n\t" \
  "v_max3_u32 v148, v148, v112, v113\n\t" \
  "v_max3_u32 v148, v148, v114, v115\n\t" \
  "v_max3_u32 v148, v148, v116, v117\n\t" \
  "v_max3_u32 v148, v148, v118, v119\n\t" \
  "v_cmp_eq_u32 vcc, -1, v148\n\t" \
  "s_cbranch_vccz 10b\n\t" \
  "s_bitcmp1_b32 s59, 6\n\t" \
  "s_cbranch_scc0 11b\n\t" \
  "7:\n\t" \
  "s_branch 92f\n\t" \
  "91:\n\t" \
  "s_mov_b32 s58, s42\n\t" \
  "s_mov_b32 s59, 0\n\t" \
  "s_bitcmp1_b32 s58, 4\n\t" \
  "s_cbranch_scc1 26f\n\t" \
  "s_bitcmp1_b32 s58, 3\n\t" \
  "s_cbranch_scc1 34f\n\t" \
  "s_bitcmp1_b32 s58, 1\n\t" \
  "s_cbranch_scc1 18f\n\t" \
  "v_mov_b64 v[40:41], 0\n\t" \
  "v_mov_b64 v[42:43], 0\n\t" \
  "v_mov_b64 v[44:45], 0\n\t" \
  "v_mov_b64 v[46:47], 0\n\t" \
  "v_mov_b64 v[48:49], 0\n\t" \
  "v_mov_b64 v[50:51], 0\n\t" \
  "v_mov_b64 v[52:53], 0\n\t" \
  "v_mov_b64 v[54:55], 0\n\t" \
  "v_mov_b64 v[56:57], 0\n\t" \
  "v_mov_b64 v[58:59], 0\n\t" \
  "v_mov_b64 v[60:61], 0\n\t" \
  "v_mov_b64 v[62:63], 0\n\t" \
  "v_mov_b64 v[64:65], 0\n\t" \
  "v_mov_b64 v[66:67], 0\n\t" \
  "v_mov_b64 v[68:69], 0\n\t" \
  "v_mov_b64 v[70:71], 0\n\t" \
  "v_mov_b64 v[72:73], 0\n\t" \
  "v_mov_b64 v[74:75], 0\n\t" \
  "v_mov_b64 v[76:77], 0\n\t" \
  "v_mov_b64 v[78:79], 0\n\t" \
  "v_mov_b64 v[80:81], 0\n\t" \
  "v_mov_b64 v[82:83], 0\n\t" \
  "v_mov_b64 v[84:85], 0\n\t" \
  "v_mov_b64 v[86:87], 0\n\t" \
  "v_mov_b64 v[88:89], 0\n\t" \
  "v_mov_b64 v[90:91], 0\n\t" \
  "v_mov_b64 v[92:93], 0\n\t" \
  "v_mov_b64 v[94:95], 0\n\t" \
  "v_mov_b64 v[96:97], 0\n\t" \
  "v_mov_b64 v[98:99], 0\n\t" \
  "v_mov_b64 v[100:101], 0\n\t" \
  "v_mov_b64 v[102:103], 0\n\t" \
  "v_mov_b64 v[136:137], 0\n\t" \
  "v_mov_b64 v[138:139], 0\n\t" \
  "v_mov_b64 v[140:141], 0\n\t" \
  "v_mov_b64 v[142:143], 0\n\t" \
  "19:\n\t" \
  "s_add_i32 s85, s40, -1\n\t" \
  "s_and_b32 s82, s85, 2\n\t" \
  "s_lshl_b32 s82, s82, 13\n\t" \
  "s_and_b32 s80, s85, 1\n\t" \
  "s_lshl_b32 s80, s80, 12\n\t" \
  "s_add_i32 s82, s82, s80\n\t" \
  "s_add_i32 s82, s82, 0x84000\n\t" \
  "s_mov_b32 exec_hi, 0\n\t" \
  "buffer_load_dword v184, v200, %[rh], s82 offen sc1\n\t" \
  "buffer_load_dword v185, v200, %[rh], s52 offen sc1\n\t" \
  "s_mov_b64 exec, -1\n\t" \
  "20:\n\t" \
  "s_and_b32 s80, s40, 3\n\t" \
  "s_mul_i32 s77, s80, 0xc000\n\t" \
  "s_add_i32 s77, s77, 0x40000\n\t" \
  "s_lshl_b32 s78, s80, 14\n\t" \
  "s_add_i32 s78, s78, 0x70000\n\t" \
  "s_mov_b32 s57, 0\n\t" \
  "s_add_i32 s96, s77, 0x1000\n\t" \
  "s_and_b32 s98, s40, 2\n\t" \
  "s_lshl_b32 s98, s98, 13\n\t" \
  "s_and_b32 s80, s40, 1\n\t" \
  "s_lshl_b32 s80, s80, 12\n\t" \
  "s_add_i32 s98, s98, s80\n\t" \
  "s_add_i32 s98, s98, 0x84000\n\t" \
  "s_lshl_b32 s83, s43, 8\n\t" \
  "s_mov_b32 s84, 700\n\t" \
  "s_mov_b32 exec_hi, 0\n\t" \
  "s_waitcnt vmcnt(0)\n\t" \
  "v_cmp_ne_u32_sdwa vcc, v184, s85 src0_sel:BYTE_0 src1_sel:DWORD\n\t" \
  "v_subrev_u32 v184, s83, v184\n\t" \
  "v_ashrrev_i32 v184, 8, v184\n\t" \
  "s_cbranch_vccnz 22f\n\t" \
  "s_nop 0\n\t" \
  "v_max_i32_dpp v184, v184, v184 quad_perm:[1,0,3,2] row_mask:0xf bank_mask:0xf\n\t" \
  "s_nop 1\n\t" \
  "v_max_i32_dpp v184, v184, v184 quad_perm:[2,3,0,1] row_mask:0xf bank_mask:0xf\n\t" \
  "s_nop 1\n\t" \
  "v_max_i32_dpp v184, v184, v184 row_half_mirror row_mask:0xf bank_mask:0xf\n\t" \
  "s_nop 1\n\t" \
  "v_max_i32_dpp v184, v184, v184 row_mirror row_mask:0xf bank_mask:0xf\n\t" \
  "s_nop 0\n\t" \
  "v_readlane_b32 s81, v184, 0\n\t" \
  "v_readlane_b32 s82, v184, 16\n\t" \
  "s_max_i32 s81, s81, s82\n\t" \
  "s_add_i32 s81, s81, 1056\n\t" \
  "s_max_i32 s81, s81, 0\n\t" \
  "s_min_i32 s81, s81, 20000\n\t" \
  "s_mul_i32 s81, s81, s44\n\t" \
  "s_lshr_b32 s81, s81, 16\n\t" \
  "24:\n\t" \
  "s_add_i32 s81, s81, s41\n\t" \
  "v_min_u32 v185, 1760, v185\n\t" \
  "s_nop 1\n\t" \
  "v_max_i32_dpp v185, v185, v185 quad_perm:[1,0,3,2] row_mask:0xf bank_mask:0xf\n\t" \
  "s_nop 1\n\t" \
  "v_max_i32_dpp v185, v185, v185 quad_perm:[2,3,0,1] row_mask:0xf bank_mask:0xf\n\t" \
  "s_nop 1\n\t" \
  "v_max_i32_dpp v185, v185, v185 row_half_mirror row_mask:0xf bank_mask:0xf\n\t" \
  "s_nop 1\n\t" \
  "v_max_i32_dpp v185, v185, v185 row_mirror row_mask:0xf bank_mask:0xf\n\t" \
  "s_nop 0\n\t" \
  "v_readlane_b32 s54, v185, 0\n\t" \
  "v_readlane_b32 s82, v185, 16\n\t" \
  "s_max_i32 s54, s54, s82\n\t" \
  "s_add_i32 s54, s54, 330\n\t" \
  "s_add_i32 s54, s54, s43\n\t" \
  "1:\n\t" \
  "s_memtime vcc\n\t" \
  "s_waitcnt lgkmcnt(0)\n\t" \
  "s_sub_i32 s79, s81, vcc_lo\n\t" \
  "s_cmp_gt_i32 s79, 128\n\t" \
  "s_cbranch_scc1 1b\n\t" \
  ".p2align 4\n\t" \
  "2:\n\t" \
  "s_sub_i32 s79, s79, 33\n\t" \
  "s_cmp_gt_i32 s79, 56\n\t" \
  "s_cbranch_scc1 2b\n\t" \
  "13:\n\t" \
  "buffer_load_dword v184, v200, %[rh], s78 offen sc1\n\t" \
  "s_waitcnt vmcnt(0)\n\t" \
  "s_memtime s[86:87]\n\t" \
  "v_cmp_ne_u32_sdwa vcc, v184, s40 src0_sel:BYTE_0 src1_sel:DWORD\n\t" \
  "v_subrev_u32 v184, s83, v184\n\t" \
  "v_ashrrev_i32 v184, 8, v184\n\t" \
  "s_nop 1\n\t" \
  "v_max_i32_dpp v184, v184, v184 quad_perm:[1,0,3,2] row_mask:0xf bank_mask:0xf\n\t" \
  "s_nop 1\n\t" \
  "v_max_i32_dpp v184, v184, v184 quad_perm:[2,3,0,1] row_mask:0xf bank_mask:0xf\n\t" \
  "s_cbranch_vccnz 14f\n\t" \
  "15:\n\t" \
  "s_nop 0\n\t" \
  "v_max_i32_dpp v184, v184, v184 row_half_mirror row_mask:0xf bank_mask:0xf\n\t" \
  "s_nop 1\n\t" \
  "v_max_i32_dpp v184, v184, v184 row_mirror row_mask:0xf bank_mask:0xf\n\t" \
  "s_mov_b64 exec, -1\n\t" \
  "v_readlane_b32 s57, v184, 0\n\t" \
  "v_readlane_b32 s82, v184, 16\n\t" \
  "s_waitcnt lgkmcnt(0)\n\t" \
  "s_sub_i32 s79, s41, s86\n\t" \
  "s_max_i32 s57, s57, s82\n\t" \
  "s_add_i32 s81, s57, 255\n\t" \
  "s_max_i32 s81, s81, 0\n\t" \
  "s_min_i32 s81, s81, 20000\n\t" \
  "s_mul_i32 s81, s81, s44\n\t" \
  "s_lshr_b32 s81, s81, 16\n\t" \
  "s_add_i32 s79, s79, s81\n\t" \
  "s_sub_i32 s85, s84, s79\n\t" \
  "s_max_i32 s85, s85, 0\n\t" \
  "s_mul_hi_u32 s85, s85, s45\n\t" \
  "s_lshl1_add_u32 s85, s85, s57\n\t" \
  "s_add_i32 s85, s85, s54\n\t" \
  "s_lshl_b32 s85, s85, 8\n\t" \
  "s_or_b32 s85, s85, s40\n\t" \
  "v_mov_b32 v184, s85\n\t" \
  "buffer_store_dword v184, v202, %[rh], s98 offen sc1\n\t" \
  "s_add_i32 s79, s79, -570\n\t" \
  ".p2align 4\n\t" \
  "3:\n\t" \
  "s_sub_i32 s79, s79, 33\n\t" \
  "s_cmp_gt_i32 s79, 56\n\t" \
  "s_cbranch_scc1 3b\n\t" \
  "buffer_load_dwordx4 v[0:3], v193, %[rh], s77 offen offset:0 sc1\n\t" \
  "s_nop 4\n\t" \
  "buffer_load_dwordx4 v[4:7], v193, %[rh], s77 offen offset:1024 sc1\n\t" \
  "s_nop 4\n\t" \
  "buffer_load_dwordx4 v[8:11], v193, %[rh], s77 offen offset:2048 sc1\n\t" \
  "s_nop 4\n\t" \
  "buffer_load_dwordx4 v[12:15], v193, %[rh], s77 offen offset:3072 sc1\n\t" \
  "s_nop 4\n\t" \
  "buffer_load_dwordx4 v[16:19], v193, %[rh], s96 offen offset:0 sc1\n\t" \
  "s_nop 4\n\t" \
  "buffer_load_dwordx4 v[20:23], v193, %[rh], s96 offen offset:1024 sc1\n\t" \
  "s_nop 4\n\t" \
  "buffer_load_dwordx4 v[24:27], v193, %[rh], s96 offen offset:2048 sc1\n\t" \
  "s_nop 4\n\t" \
  "buffer_load_dwordx4 v[28:31], v193, %[rh], s96 offen offset:3072 sc1\n\t" \
  "s_add_i32 s79, s79, 172\n\t" \
  ".p2align 4\n\t" \
  "4:\n\t" \
  "s_sub_i32 s79, s79, 33\n\t" \
  "s_cmp_gt_i32 s79, 56\n\t" \
  "s_cbranch_scc1 4b\n\t" \
  "buffer_load_dwordx4 v[32:35], v195, %[rh], s77 offen offset:0 sc1\n\t" \
  "buffer_load_dwordx4 v[36:39], v195, %[rh], s77 offen offset:2048 sc1\n\t" \
  "10:\n\t" \
  "s_waitcnt vmcnt(9)\n\t" \
  "v_smfmac_f32_16x16x64_bf16 v[40:43], v[0:3], a[0:7], v203\n\t" \
  "v_smfmac_f32_16x16x64_bf16 v[56:59], v[0:3], a[8:15], v203\n\t" \
  "s_waitcnt vmcnt(8)\n\t" \
  "v_smfmac_f32_16x16x64_bf16 v[44:47], v[4:7], a[16:23], v203\n\t" \
  "v_cvt_pk_bf16_f32 v40, v40, v41\n\t" \
  "v_cvt_pk_bf16_f32 v41, v42, v43\n\t" \
  "v_smfmac_f32_16x16x64_bf16 v[60:63], v[4:7], a[24:31], v203\n\t" \
  "v_cvt_pk_bf16_f32 v56, v56, v57\n\t" \
  "v_cvt_pk_bf16_f32 v57, v58, v59\n\t" \
  "s_waitcnt vmcnt(7)\n\t" \
  "v_smfmac_f32_16x16x64_bf16 v[48:51], v[8:11], a[32:39], v203\n\t" \
  "v_cvt_pk_bf16_f32 v42, v44, v45\n\t" \
  "v_cvt_pk_bf16_f32 v43, v46, v47\n\t" \
  "v_smfmac_f32_16x16x64_bf16 v[64:67], v[8:11], a[40:47], v203\n\t" \
  "v_cvt_pk_bf16_f32 v58, v60, v61\n\t" \
  "v_cvt_pk_bf16_f32 v59, v62, v63\n\t" \
  "s_waitcnt vmcnt(6)\n\t" \
  "v_smfmac_f32_16x16x64_bf16 v[52:55], v[12:15], a[48:55], v203\n\t" \
  "v_cvt_pk_bf16_f32 v44, v48, v49\n\t" \
  "v_cvt_pk_bf16_f32 v45, v50, v51\n\t" \
  "v_smfmac_f32_16x16x64_bf16 v[68:71], v[12:15], a[56:63], v203\n\t" \
  "v_cvt_pk_bf16_f32 v60, v64, v65\n\t" \
  "v_cvt_pk_bf16_f32 v61, v66, v67\n\t" \
  "s_waitcnt vmcnt(5)\n\t" \
  "v_smfmac_f32_16x16x64_bf16 v[72:75], v[16:19], a[64:71], v203\n\t" \
  "v_cvt_pk_bf16_f32 v46, v52, v53\n\t" \
  "v_cvt_pk_bf16_f32 v47, v54, v55\n\t" \
  "v_smfmac_f32_16x16x64_bf16 v[88:91], v[16:19], a[72:79], v203\n\t" \
  "v_cvt_pk_bf16_f32 v62, v68, v69\n\t" \
  "v_cvt_pk_bf16_f32 v63, v70, v71\n\t" \
  "s_waitcnt vmcnt(4)\n\t" \
  "v_smfmac_f32_16x16x64_bf16 v[76:79], v[20:23], a[80:87], v203\n\t" \
  "v_cvt_pk_bf16_f32 v72, v72, v73\n\t" \
  "v_cvt_pk_bf16_f32 v73, v74, v75\n\t" \
  "v_smfmac_f32_16x16x64_bf16 v[92:95], v[20:23], a[88:95], v203\n\t" \
  "v_cvt_pk_bf16_f32 v88, v88, v89\n\t" \
  "v_cvt_pk_bf16_f32 v89, v90, v91\n\t" \
  "s_waitcnt vmcnt(3)\n\t" \
  "v_smfmac_f32_16x16x64_bf16 v[80:83], v[24:27], a[96:103], v203\n\t" \
  "v_cvt_pk_bf16_f32 v74, v76, v77\n\t" \
  "v_cvt_pk_bf16_f32 v75, v78, v79\n\t" \
  "v_smfmac_f32_16x16x64_bf16 v[96:99], v[24:27], a[104:111], v203\n\t" \
  "v_cvt_pk_bf16_f32 v90, v92, v93\n\t" \
  "v_cvt_pk_bf16_f32 v91, v94, v95\n\t" \
  "s_waitcnt vmcnt(2)\n\t" \
  "v_smfmac_f32_16x16x64_bf16 v[84:87], v[28:31], a[112:119], v203\n\t" \
  "v_cvt_pk_bf16_f32 v76, v80, v81\n\t" \
  "v_cvt_pk_bf16_f32 v77, v82, v83\n\t" \
  "v_smfmac_f32_16x16x64_bf16 v[100:103], v[28:31], a[120:127], v203\n\t" \
  "v_cvt_pk_bf16_f32 v92, v96, v97\n\t" \
  "v_cvt_pk_bf16_f32 v93, v98, v99\n\t" \
  "s_waitcnt vmcnt(1)\n\t" \
  "v_smfmac_f32_16x16x64_bf16 v[136:139], v[32:35], v[40:47], v204\n\t" \
  "v_cvt_pk_bf16_f32 v78, v84, v85\n\t" \
  "v_cvt_pk_bf16_f32 v79, v86, v87\n\t" \
  "v_smfmac_f32_16x16x64_bf16 v[140:143], v[32:35], v[56:63], v204\n\t" \
  "v_cvt_pk_bf16_f32 v94, v100, v101\n\t" \
  "v_cvt_pk_bf16_f32 v95, v102, v103\n\t" \
  "s_waitcnt vmcnt(0)\n\t" \
  "v_smfmac_f32_16x16x64_bf16 v[136:139], v[36:39], v[72:79], v204\n\t" \
  "v_smfmac_f32_16x16x64_bf16 v[140:143], v[36:39], v[88:95], v204\n\t" \
  "s_nop 3\n\t" \
  "v_permlane32_swap_b32 v136, v138\n\t" \
  "v_permlane32_swap_b32 v137, v139\n\t" \
  "v_add_f32 v136, v136, v138\n\t" \
  "v_add_f32 v137, v137, v139\n\t" \
  "v_permlane32_swap_b32 v140, v142\n\t" \
  "v_permlane32_swap_b32 v141, v143\n\t" \
  "v_add_f32 v138, v140, v142\n\t" \
  "v_add_f32 v139, v141, v143\n\t" \
  "ds_write2st64_b64 v197, v[136:137], v[138:139] offset1:2\n\t" \
  "s_waitcnt lgkmcnt(0)\n\t" \
  "s_barrier\n\t" \
  "s_barrier\n\t" \
  "ds_read2_b32 v[148:149], v207 offset1:8\n\t" \
  "s_waitcnt lgkmcnt(0)\n\t" \
  "v_or_b32 v148, v148, v149\n\t" \
  "v_cmp_ne_u32 vcc, 0, v148\n\t" \
  "s_cbranch_vccnz 12f\n\t" \
  "s_and_b32 s58, s58, 4\n\t" \
  "26:\n\t" \
  "s_and_b32 s58, s58, 36\n\t" \
  "s_and_b32 s76, s40, 3\n\t" \
  "s_lshl_b32 s76, s76, 18\n\t" \
  "s_add_i32 s77, s76, 0x10000\n\t" \
  "s_add_i32 s78, s76, 0x1000\n\t" \
  "s_add_i32 s79, s76, 0x11000\n\t" \
  "s_add_i32 s85, s40, 1\n\t" \
  "s_barrier\n\t" \
  "buffer_load_dwordx4 v[0:3], v210, %[rh], s76 offen offset:0 sc1\n\t" \
  "buffer_load_dwordx4 v[4:7], v210, %[rh], s76 offen offset:1024 sc1\n\t" \
  "buffer_load_dwordx4 v[8:11], v210, %[rh], s77 offen offset:0 sc1\n\t" \
  "buffer_load_dwordx4 v[12:15], v210, %[rh], s77 offen offset:1024 sc1\n\t" \
  "buffer_load_dwordx4 v[16:19], v210, %[rh], s76 offen offset:2048 sc1\n\t" \
  "buffer_load_dwordx4 v[20:23], v210, %[rh], s76 offen offset:3072 sc1\n\t" \
  "buffer_load_dwordx4 v[24:27], v210, %[rh], s77 offen offset:2048 sc1\n\t" \
  "buffer_load_dwordx4 v[28:31], v210, %[rh], s77 offen offset:3072 sc1\n\t" \
  "buffer_load_dwordx4 v[32:35], v210, %[rh], s78 offen offset:0 sc1\n\t" \
  "buffer_load_dwordx4 v[36:39], v210, %[rh], s78 offen offset:1024 sc1\n\t" \
  "buffer_load_dwordx4 v[40:43], v210, %[rh], s79 offen offset:0 sc1\n\t" \
  "buffer_load_dwordx4 v[44:47], v210, %[rh], s79 offen offset:1024 sc1\n\t" \
  "buffer_load_dwordx4 v[48:51], v210, %[rh], s78 offen offset:2048 sc1\n\t" \
  "buffer_load_dwordx4 v[52:55], v210, %[rh], s78 offen offset:3072 sc1\n\t" \
  "buffer_load_dwordx4 v[56:59], v210, %[rh], s79 offen offset:2048 sc1\n\t" \
  "buffer_load_dwordx4 v[60:63], v210, %[rh], s79 offen offset:3072 sc1\n\t" \
  "buffer_load_dwordx4 v[64:67], v211, %[rh], s76 offen offset:0 sc1\n\t" \
  "buffer_load_dwordx4 v[68:71], v211, %[rh], s76 offen offset:1024 sc1\n\t" \
  "buffer_load_dwordx4 v[72:75], v211, %[rh], s77 offen offset:0 sc1\n\t" \
  "buffer_load_dwordx4 v[76:79], v211, %[rh], s77 offen offset:1024 sc1\n\t" \
  "buffer_load_dwordx4 v[80:83], v211, %[rh], s76 offen offset:2048 sc1\n\t" \
  "buffer_load_dwordx4 v[84:87], v211, %[rh], s76 offen offset:3072 sc1\n\t" \
  "buffer_load_dwordx4 v[88:91], v211, %[rh], s77 offen offset:2048 sc1\n\t" \
  "buffer_load_dwordx4 v[92:95], v211, %[rh], s77 offen offset:3072 sc1\n\t" \
  "buffer_load_dwordx4 v[96:99], v211, %[rh], s78 offen offset:0 sc1\n\t" \
  "buffer_load_dwordx4 v[100:103], v211, %[rh], s78 offen offset:1024 sc1\n\t" \
  "buffer_load_dwordx4 v[104:107], v211, %[rh], s79 offen offset:0 sc1\n\t" \
  "buffer_load_dwordx4 v[108:111], v211, %[rh], s79 offen offset:1024 sc1\n\t" \
  "buffer_load_dwordx4 v[112:115], v211, %[rh], s78 offen offset:2048 sc1\n\t" \
  "buffer_load_dwordx4 v[116:119], v211, %[rh], s78 offen offset:3072 sc1\n\t" \
  "buffer_load_dwordx4 v[120:123], v211, %[rh], s79 offen offset:2048 sc1\n\t" \
  "buffer_load_dwordx4 v[124:127], v211, %[rh], s79 offen offset:3072 sc1\n\t" \
  "s_and_b32 s82, s85, 2\n\t" \
  "s_lshl_b32 s82, s82, 13\n\t" \
  "s_and_b32 s80, s85, 1\n\t" \
  "s_lshl_b32 s80, s80, 12\n\t" \
  "s_add_i32 s82, s82, s80\n\t" \
  "s_add_i32 s82, s82, 0x84000\n\t" \
  "s_mov_b32 exec_hi, 0\n\t" \
  "buffer_load_dword v184, v200, %[rh], s82 offen sc1\n\t" \
  "buffer_load_dword v185, v200, %[rh], s52 offen sc1\n\t" \
  "s_mov_b64 exec, -1\n\t" \
  "s_barrier\n\t" \
  "ds_read_b128 v[164:167], v206 offset:16\n\t" \
  "s_waitcnt lgkmcnt(0)\n\t" \
  "v_readfirstlane_b32 s41, v164\n\t" \
  "v_readfirstlane_b32 s43, v165\n\t" \
  "v_readfirstlane_b32 s44, v166\n\t" \
  "v_readfirstlane_b32 s45, v167\n\t" \
  "31:\n\t" \
  "v_mov_b64 v[136:137], 0\n\t" \
  "v_mov_b64 v[138:139], 0\n\t" \
  "v_mov_b64 v[140:141], 0\n\t" \
  "v_mov_b64 v[142:143], 0\n\t" \
  "s_waitcnt vmcnt(32)\n\t" \
  "v_swap_b32 v1, v4\n\t" \
  "v_swap_b32 v3, v6\n\t" \
  "s_nop 1\n\t" \
  "v_mfma_f32_16x16x32_bf16 v[168:171], a[0:3], v[0:3], 0\n\t" \
  "v_mfma_f32_16x16x32_bf16 v[172:175], a[8:11], v[0:3], 0\n\t" \
  "s_waitcnt vmcnt(30)\n\t" \
  "v_swap_b32 v9, v12\n\t" \
  "v_mfma_f32_16x16x32_bf16 v[168:171], a[4:7], v[4:7], v[168:171]\n\t" \
  "v_swap_b32 v11, v14\n\t" \
  "v_mfma_f32_16x16x32_bf16 v[172:175], a[12:15], v[4:7], v[172:175]\n\t" \
  "s_nop 0\n\t" \
  "v_mfma_f32_16x16x32_bf16 v[176:179], a[0:3], v[8:11], 0\n\t" \
  "ds_read_b128 a[0:3], v194 offset:0\n\t" \
  "v_mfma_f32_16x16x32_bf16 v[180:183], a[8:11], v[8:11], 0\n\t" \
  "ds_read_b128 a[8:11], v194 offset:2048\n\t" \
  "s_waitcnt vmcnt(28)\n\t" \
  "v_swap_b32 v17, v20\n\t" \
  "v_mfma_f32_16x16x32_bf16 v[176:179], a[4:7], v[12:15], v[176:179]\n\t" \
  "ds_read_b128 a[4:7], v194 offset:1024\n\t" \
  "v_swap_b32 v19, v22\n\t" \
  "v_mfma_f32_16x16x32_bf16 v[180:183], a[12:15], v[12:15], v[180:183]\n\t" \
  "ds_read_b128 a[12:15], v194 offset:3072\n\t" \
  "v_mfma_f32_16x16x32_bf16 v[168:171], a[16:19], v[16:19], v[168:171]\n\t" \
  "v_mfma_f32_16x16x32_bf16 v[172:175], a[24:27], v[16:19], v[172:175]\n\t" \
  "s_waitcnt vmcnt(26)\n\t" \
  "v_swap_b32 v25, v28\n\t" \
  "v_mfma_f32_16x16x32_bf16 v[168:171], a[20:23], v[20:23], v[168:171]\n\t" \
  "v_swap_b32 v27, v30\n\t" \
  "v_mfma_f32_16x16x32_bf16 v[172:175], a[28:31], v[20:23], v[172:175]\n\t" \
  "s_nop 0\n\t" \
  "v_mfma_f32_16x16x32_bf16 v[176:179], a[16:19], v[24:27], v[176:179]\n\t" \
  "ds_read_b128 a[16:19], v194 offset:4096\n\t" \
  "v_mfma_f32_16x16x32_bf16 v[180:183], a[24:27], v[24:27], v[180:183]\n\t" \
  "ds_read_b128 a[24:27], v194 offset:6144\n\t" \
  "s_waitcnt vmcnt(24)\n\t" \
  "v_swap_b32 v33, v36\n\t" \
  "v_mfma_f32_16x16x32_bf16 v[176:179], a[20:23], v[28:31], v[176:179]\n\t" \
  "ds_read_b128 a[20:23], v194 offset:5120\n\t" \
  "v_swap_b32 v35, v38\n\t" \
  "v_mfma_f32_16x16x32_bf16 v[180:183], a[28:31], v[28:31], v[180:183]\n\t" \
  "ds_read_b128 a[28:31], v194 offset:7168\n\t" \
  "v_mfma_f32_16x16x32_bf16 v[168:171], a[32:35], v[32:35], v[168:171]\n\t" \
  "v_mfma_f32_16x16x32_bf16 v[172:175], a[40:43], v[32:35], v[172:175]\n\t" \
  "s_waitcnt vmcnt(22)\n\t" \
  "v_swap_b32 v41, v44\n\t" \
  "v_mfma_f32_16x16x32_bf16 v[168:171], a[36:39], v[36:39], v[168:171]\n\t" \
  "v_swap_b32 v43, v46\n\t" \
  "v_mfma_f32_16x16x32_bf16 v[172:175], a[44:47], v[36:39], v[172:175]\n\t" \
  "s_nop 0\n\t" \
  "v_mfma_f32_16x16x32_bf16 v[176:179], a[32:35], v[40:43], v[176:179]\n\t" \
  "ds_read_b128 a[32:35], v194 offset:8192\n\t" \
  "v_mfma_f32_16x16x32_bf16 v[180:183], a[40:43], v[40:43], v[180:183]\n\t" \
  "ds_read_b128 a[40:43], v194 offset:10240\n\t" \
  "s_waitcnt vmcnt(20)\n\t" \
  "v_swap_b32 v49, v52\n\t" \
  "v_mfma_f32_16x16x32_bf16 v[176:179], a[36:39], v[44:47], v[176:179]\n\t" \
  "ds_read_b128 a[36:39], v194 offset:9216\n\t" \
  "v_swap_b32 v51, v54\n\t" \
  "v_mfma_f32_16x16x32_bf16 v[180:183], a[44:47], v[44:47], v[180:183]\n\t" \
  "ds_read_b128 a[44:47], v194 offset:11264\n\t" \
  "v_mov_b64 v[40:41], 0\n\t" \
  "v_mov_b64 v[42:43], 0\n\t" \
  "v_mfma_f32_16x16x32_bf16 v[168:171], a[48:51], v[48:51], v[168:171]\n\t" \
  "v_mov_b64 v[44:45], 0\n\t" \
  "v_mov_b64 v[46:47], 0\n\t" \
  "v_mfma_f32_16x16x32_bf16 v[172:175], a[56:59], v[48:51], v[172:175]\n\t" \
  "s_waitcnt vmcnt(18)\n\t" \
  "v_swap_b32 v57, v60\n\t" \
  "v_mfma_f32_16x16x32_bf16 v[168:171], a[52:55], v[52:55], v[168:171]\n\t" \
  "v_swap_b32 v59, v62\n\t" \
  "v_mfma_f32_16x16x32_bf16 v[172:175], a[60:63], v[52:55], v[172:175]\n\t" \
  "v_mov_b64 v[48:49], 0\n\t" \
  "v_mov_b64 v[50:51], 0\n\t" \
  "v_mfma_f32_16x16x32_bf16 v[176:179], a[48:51], v[56:59], v[176:179]\n\t" \
  "ds_read_b128 a[48:51], v194 offset:12288\n\t" \
  "v_mov_b64 v[52:53], 0\n\t" \
  "v_mov_b64 v[54:55], 0\n\t" \
  "v_mfma_f32_16x16x32_bf16 v[180:183], a[56:59], v[56:59], v[180:183]\n\t" \
  "ds_read_b128 a[56:59], v194 offset:14336\n\t" \
  "s_waitcnt vmcnt(16)\n\t" \
  "v_swap_b32 v65, v68\n\t" \
  "v_mfma_f32_16x16x32_bf16 v[176:179], a[52:55], v[60:63], v[176:179]\n\t" \
  "ds_read_b128 a[52:55], v194 offset:13312\n\t" \
  "v_swap_b32 v67, v70\n\t" \
  "v_mfma_f32_16x16x32_bf16 v[180:183], a[60:63], v[60:63], v[180:183]\n\t" \
  "ds_read_b128 a[60:63], v194 offset:15360\n\t" \
  "v_mov_b64 v[56:57], 0\n\t" \
  "v_mov_b64 v[58:59], 0\n\t" \
  "v_mfma_f32_16x16x32_bf16 v[168:171], a[64:67], v[64:67], v[168:171]\n\t" \
  "v_mov_b64 v[60:61], 0\n\t" \
  "v_mov_b64 v[62:63], 0\n\t" \
  "v_mfma_f32_16x16x32_bf16 v[172:175], a[72:75], v[64:67], v[172:175]\n\t" \
  "s_waitcnt vmcnt(14)\n\t" \
  "v_swap_b32 v73, v76\n\t" \
  "v_mfma_f32_16x16x32_bf16 v[168:171], a[68:71], v[68:71], v[168:171]\n\t" \
  "v_swap_b32 v75, v78\n\t" \
  "v_mfma_f32_16x16x32_bf16 v[172:175], a[76:79], v[68:71], v[172:175]\n\t" \
  "v_mov_b64 v[64:65], 0\n\t" \
  "v_mov_b64 v[66:67], 0\n\t" \
  "v_mfma_f32_16x16x32_bf16 v[176:179], a[64:67], v[72:75], v[176:179]\n\t" \
  "ds_read_b128 a[64:67], v194 offset:16384\n\t" \
  "v_mov_b64 v[68:69], 0\n\t" \
  "v_mov_b64 v[70:71], 0\n\t" \
  "v_mfma_f32_16x16x32_bf16 v[180:183], a[72:75], v[72:75], v[180:183]\n\t" \
  "ds_read_b128 a[72:75], v194 offset:18432\n\t" \
  "s_waitcnt vmcnt(12)\n\t" \
  "v_swap_b32 v81, v84\n\t" \
  "v_mfma_f32_16x16x32_bf16 v[176:179], a[68:71], v[76:79], v[176:179]\n\t" \
  "ds_read_b128 a[68:71], v194 offset:17408\n\t" \
  "v_swap_b32 v83, v86\n\t" \
  "v_mfma_f32_16x16x32_bf16 v[180:183], a[76:79], v[76:79], v[180:183]\n\t" \
  "ds_read_b128 a[76:79], v194 offset:19456\n\t" \
  "v_mov_b64 v[72:73], 0\n\t" \
  "v_mov_b64 v[74:75], 0\n\t" \
  "v_mfma_f32_16x16x32_bf16 v[168:171], a[80:83], v[80:83], v[168:171]\n\t" \
  "v_mov_b64 v[76:77], 0\n\t" \
  "v_mov_b64 v[78:79], 0\n\t" \
  "v_mfma_f32_16x16x32_bf16 v[172:175], a[88:91], v[80:83], v[172:175]\n\t" \
  "s_waitcnt vmcnt(10)\n\t" \
  "v_swap_b32 v89, v92\n\t" \
  "v_mfma_f32_16x16x32_bf16 v[168:171], a[84:87], v[84:87], v[168:171]\n\t" \
  "v_swap_b32 v91, v94\n\t" \
  "v_mfma_f32_16x16x32_bf16 v[172:175], a[92:95], v[84:87], v[172:175]\n\t" \
  "v_mov_b64 v[80:81], 0\n\t" \
  "v_mov_b64 v[82:83], 0\n\t" \
  "v_mfma_f32_16x16x32_bf16 v[176:179], a[80:83], v[88:91], v[176:179]\n\t" \
  "ds_read_b128 a[80:83], v194 offset:20480\n\t" \
  "v_mov_b64 v[84:85], 0\n\t" \
  "v_mov_b64 v[86:87], 0\n\t" \
  "v_mfma_f32_16x16x32_bf16 v[180:183], a[88:91], v[88:91], v[180:183]\n\t" \
  "ds_read_b128 a[88:91], v194 offset:22528\n\t" \
  "s_waitcnt vmcnt(8)\n\t" \
  "v_swap_b32 v97, v100\n\t" \
  "v_mfma_f32_16x16x32_bf16 v[176:179], a[84:87], v[92:95], v[176:179]\n\t" \
  "ds_read_b128 a[84:87], v194 offset:21504\n\t" \
  "v_swap_b32 v99, v102\n\t" \
  "v_mfma_f32_16x16x32_bf16 v[180:183], a[92:95], v[92:95], v[180:183]\n\t" \
  "ds_read_b128 a[92:95], v194 offset:23552\n\t" \
  "v_mov_b64 v[88:89], 0\n\t" \
  "v_mov_b64 v[90:91], 0\n\t" \
  "v_mfma_f32_16x16x32_bf16 v[168:171], a[96:99], v[96:99], v[168:171]\n\t" \
  "v_mov_b64 v[92:93], 0\n\t" \
  "v_mov_b64 v[94:95], 0\n\t" \
  "v_mfma_f32_16x16x32_bf16 v[172:175], a[104:107], v[96:99], v[172:175]\n\t" \
  "s_waitcnt vmcnt(6)\n\t" \
  "v_swap_b32 v105, v108\n\t" \
  "v_mfma_f32_16x16x32_bf16 v[168:171], a[100:103], v[100:103], v[168:171]\n\t" \
  "v_swap_b32 v107, v110\n\t" \
  "v_mfma_f32_16x16x32_bf16 v[172:175], a[108:111], v[100:103], v[172:175]\n\t" \
  "v_mov_b64 v[96:97], 0\n\t" \
  "v_mov_b64 v[98:99], 0\n\t" \
  "v_mfma_f32_16x16x32_bf16 v[176:179], a[96:99], v[104:107], v[176:179]\n\t" \
  "ds_read_b128 a[96:99], v194 offset:24576\n\t" \
  "v_mov_b64 v[100:101], 0\n\t" \
  "v_mov_b64 v[102:103], 0\n\t" \
  "v_mfma_f32_16x16x32_bf16 v[180:183], a[104:107], v[104:107], v[180:183]\n\t" \
  "ds_read_b128 a[104:107], v194 offset:26624\n\t" \
  "s_waitcnt vmcnt(4)\n\t" \
  "v_swap_b32 v113, v116\n\t" \
  "v_mfma_f32_16x16x32_bf16 v[176:179], a[100:103], v[108:111], v[176:179]\n\t" \
  "ds_read_b128 a[100:103], v194 offset:25600\n\t" \
  "v_swap_b32 v115, v118\n\t" \
  "v_mfma_f32_16x16x32_bf16 v[180:183], a[108:111], v[108:111], v[180:183]\n\t" \
  "ds_read_b128 a[108:111], v194 offset:27648\n\t" \
  "v_mfma_f32_16x16x32_bf16 v[168:171], a[112:115], v[112:115], v[168:171]\n\t" \
  "v_mfma_f32_16x16x32_bf16 v[172:175], a[120:123], v[112:115], v[172:175]\n\t" \
  "s_waitcnt vmcnt(2)\n\t" \
  "v_swap_b32 v121, v124\n\t" \
  "v_mfma_f32_16x16x32_bf16 v[168:171], a[116:119], v[116:119], v[168:171]\n\t" \
  "v_swap_b32 v123, v126\n\t" \
  "v_mfma_f32_16x16x32_bf16 v[172:175], a[124:127], v[116:119], v[172:175]\n\t" \
  "s_nop 0\n\t" \
  "v_mfma_f32_16x16x32_bf16 v[176:179], a[112:115], v[120:123], v[176:179]\n\t" \
  "ds_read_b128 a[112:115], v194 offset:28672\n\t" \
  "v_mfma_f32_16x16x32_bf16 v[180:183], a[120:123], v[120:123], v[180:183]\n\t" \
  "ds_read_b128 a[120:123], v194 offset:30720\n\t" \
  "v_mfma_f32_16x16x32_bf16 v[176:179], a[116:119], v[124:127], v[176:179]\n\t" \
  "ds_read_b128 a[116:119], v194 offset:29696\n\t" \
  "v_mfma_f32_16x16x32_bf16 v[180:183], a[124:127], v[124:127], v[180:183]\n\t" \
  "ds_read_b128 a[124:127], v194 offset:31744\n\t" \
  "s_nop 6\n\t" \
  "s_add_i32 s40, s40, 2\n\t" \
  "v_cmp_u_f32 vcc, v168, v176\n\t" \
  "s_cbranch_vccnz 32f\n\t" \
  "33:\n\t" \
  "ds_write_b128 v199, v[168:171] offset:0\n\t" \
  "ds_write_b128 v199, v[172:175] offset:1024\n\t" \
  "ds_write_b128 v199, v[176:179] offset:8192\n\t" \
  "ds_write_b128 v199, v[180:183] offset:9216\n\t" \
  "s_mov_b32 s59, 0\n\t" \
  "s_and_b32 s58, s58, 4\n\t" \
  "s_cmp_lt_i32 s40, 64\n\t" \
  "s_cbranch_scc1 20b\n\t" \
  "s_waitcnt lgkmcnt(0)\n\t" \
  "s_barrier\n\t" \
  "s_mov_b32 s58, 64\n\t" \
  "s_branch 7f\n\t" \
  "22:\n\t" \
  "s_mov_b32 s81, 0\n\t" \
  "s_cmp_eq_u32 s40, 0\n\t" \
  "s_cbranch_scc1 24b\n\t" \
  "s_bitcmp1_b32 s58, 2\n\t" \
  "s_cbranch_scc1 24b\n\t" \
  "s_mov_b64 exec, -1\n\t" \
  "s_add_u32 s59, s59, 0x10000\n\t" \
  "s_cmp_lt_u32 s59, 0x100000\n\t" \
  "s_cbranch_scc1 19b\n\t" \
  "s_or_b32 s58, s58, 1\n\t" \
  "s_branch 7f\n\t" \
  "14:\n\t" \
  "s_bitcmp1_b32 s58, 2\n\t" \
  "s_cbranch_scc1 15b\n\t" \
  "s_mov_b32 s84, 1000\n\t" \
  "s_add_u32 s59, s59, 0x10000\n\t" \
  "s_cmp_lt_u32 s59, 0x100000\n\t" \
  "s_cbranch_scc1 13b\n\t" \
  "s_mov_b64 exec, -1\n\t" \
  "s_or_b32 s58, s58, 1\n\t" \
  "s_branch 7f\n\t" \
  "32:\n\t" \
  "s_and_b32 s80, s58, 36\n\t" \
  "s_cbranch_scc1 33b\n\t" \
  "s_add_i32 s40, s40, -2\n\t" \
  "34:\n\t" \
  "s_or_b32 s59, s59, 128\n\t" \
  "s_and_b32 s76, s40, 3\n\t" \
  "s_lshl_b32 s76, s76, 18\n\t" \
  "s_add_i32 s77, s76, 0x10000\n\t" \
  "s_add_i32 s78, s76, 0x1000\n\t" \
  "s_add_i32 s79, s76, 0x11000\n\t" \
  "35:\n\t" \
  "buffer_load_dwordx4 v[0:3], v210, %[rh], s76 offen offset:0 sc1\n\t" \
  "buffer_load_dwordx4 v[4:7], v210, %[rh], s76 offen offset:1024 sc1\n\t" \
  "buffer_load_dwordx4 v[8:11], v210, %[rh], s77 offen offset:0 sc1\n\t" \
  "buffer_load_dwordx4 v[12:15], v210, %[rh], s77 offen offset:1024 sc1\n\t" \
  "buffer_load_dwordx4 v[16:19], v210, %[rh], s76 offen offset:2048 sc1\n\t" \
  "buffer_load_dwordx4 v[20:23], v210, %[rh], s76 offen offset:3072 sc1\n\t" \
  "buffer_load_dwordx4 v[24:27], v210, %[rh], s77 offen offset:2048 sc1\n\t" \
  "buffer_load_dwordx4 v[28:31], v210, %[rh], s77 offen offset:3072 sc1\n\t" \
  "buffer_load_dwordx4 v[32:35], v210, %[rh], s78 offen offset:0 sc1\n\t" \
  "buffer_load_dwordx4 v[36:39], v210, %[rh], s78 offen offset:1024 sc1\n\t" \
  "buffer_load_dwordx4 v[40:43], v210, %[rh], s79 offen offset:0 sc1\n\t" \
  "buffer_load_dwordx4 v[44:47], v210, %[rh], s79 offen offset:1024 sc1\n\t" \
  "buffer_load_dwordx4 v[48:51], v210, %[rh], s78 offen offset:2048 sc1\n\t" \
  "buffer_load_dwordx4 v[52:55], v210, %[rh], s78 offen offset:3072 sc1\n\t" \
  "buffer_load_dwordx4 v[56:59], v210, %[rh], s79 offen offset:2048 sc1\n\t" \
  "buffer_load_dwordx4 v[60:63], v210, %[rh], s79 offen offset:3072 sc1\n\t" \
  "buffer_load_dwordx4 v[64:67], v211, %[rh], s76 offen offset:0 sc1\n\t" \
  "buffer_load_dwordx4 v[68:71], v211, %[rh], s76 offen offset:1024 sc1\n\t" \
  "buffer_load_dwordx4 v[72:75], v211, %[rh], s77 offen offset:0 sc1\n\t" \
  "buffer_load_dwordx4 v[76:79], v211, %[rh], s77 offen offset:1024 sc1\n\t" \
  "buffer_load_dwordx4 v[80:83], v211, %[rh], s76 offen offset:2048 sc1\n\t" \
  "buffer_load_dwordx4 v[84:87], v211, %[rh], s76 offen offset:3072 sc1\n\t" \
  "buffer_load_dwordx4 v[88:91], v211, %[rh], s77 offen offset:2048 sc1\n\t" \
  "buffer_load_dwordx4 v[92:95], v211, %[rh], s77 offen offset:3072 sc1\n\t" \
  "buffer_load_dwordx4 v[96:99], v211, %[rh], s78 offen offset:0 sc1\n\t" \
  "buffer_load_dwordx4 v[100:103], v211, %[rh], s78 offen offset:1024 sc1\n\t" \
  "buffer_load_dwordx4 v[104:107], v211, %[rh], s79 offen offset:0 sc1\n\t" \
  "buffer_load_dwordx4 v[108:111], v211, %[rh], s79 offen offset:1024 sc1\n\t" \
  "buffer_load_dwordx4 v[112:115], v211, %[rh], s78 offen offset:2048 sc1\n\t" \
  "buffer_load_dwordx4 v[116:119], v211, %[rh], s78 offen offset:3072 sc1\n\t" \
  "buffer_load_dwordx4 v[120:123], v211, %[rh], s79 offen offset:2048 sc1\n\t" \
  "buffer_load_dwordx4 v[124:127], v211, %[rh], s79 offen offset:3072 sc1\n\t" \
  "s_add_i32 s85, s40, 1\n\t" \
  "s_and_b32 s82, s85, 2\n\t" \
  "s_lshl_b32 s82, s82, 13\n\t" \
  "s_and_b32 s80, s85, 1\n\t" \
  "s_lshl_b32 s80, s80, 12\n\t" \
  "s_add_i32 s82, s82, s80\n\t" \
  "s_add_i32 s82, s82, 0x84000\n\t" \
  "s_mov_b32 exec_hi, 0\n\t" \
  "buffer_load_dword v184, v200, %[rh], s82 offen sc1\n\t" \
  "buffer_load_dword v185, v200, %[rh], s52 offen sc1\n\t" \
  "s_mov_b64 exec, -1\n\t" \
  "s_waitcnt vmcnt(0)\n\t" \
  "s_bitcmp1_b32 s58, 2\n\t" \
  "s_cbranch_scc1 36f\n\t" \
  "v_max_u32 v148, v0, v1\n\t" \
  "v_max3_u32 v148, v148, v2, v3\n\t" \
  "v_max3_u32 v148, v148, v4, v5\n\t" \
  "v_max3_u32 v148, v148, v6, v7\n\t" \
  "v_max3_u32 v148, v148, v8, v9\n\t" \
  "v_max3_u32 v148, v148, v10, v11\n\t" \
  "v_max3_u32 v148, v148, v12, v13\n\t" \
  "v_max3_u32 v148, v148, v14, v15\n\t" \
  "v_max3_u32 v148, v148, v16, v17\n\t" \
  "v_max3_u32 v148, v148, v18, v19\n\t" \
  "v_max3_u32 v148, v148, v20, v21\n\t" \
  "v_max3_u32 v148, v148, v22, v23\n\t" \
  "v_max3_u32 v148, v148, v24, v25\n\t" \
  "v_max3_u32 v148, v148, v26, v27\n\t" \
  "v_max3_u32 v148, v148, v28, v29\n\t" \
  "v_max3_u32 v148, v148, v30, v31\n\t" \
  "v_max3_u32 v148, v148, v32, v33\n\t" \
  "v_max3_u32 v148, v148, v34, v35\n\t" \
  "v_max3_u32 v148, v148, v36, v37\n\t" \
  "v_max3_u32 v148, v148, v38, v39\n\t" \
  "v_max3_u32 v148, v148, v40, v41\n\t" \
  "v_max3_u32 v148, v148, v42, v43\n\t" \
  "v_max3_u32 v148, v148, v44, v45\n\t" \
  "v_max3_u32 v148, v148, v46, v47\n\t" \
  "v_max3_u32 v148, v148, v48, v49\n\t" \
  "v_max3_u32 v148, v148, v50, v51\n\t" \
  "v_max3_u32 v148, v148, v52, v53\n\t" \
  "v_max3_u32 v148, v148, v54, v55\n\t" \
  "v_max3_u32 v148, v148, v56, v57\n\t" \
  "v_max3_u32 v148, v148, v58, v59\n\t" \
  "v_max3_u32 v148, v148, v60, v61\n\t" \
  "v_max3_u32 v148, v148, v62, v63\n\t" \
  "v_max3_u32 v148, v148, v64, v65\n\t" \
  "v_max3_u32 v148, v148, v66, v67\n\t" \
  "v_max3_u32 v148, v148, v68, v69\n\t" \
  "v_max3_u32 v148, v148, v70, v71\n\t" \
  "v_max3_u32 v148, v148, v72, v73\n\t" \
  "v_max3_u32 v148, v148, v74, v75\n\t" \
  "v_max3_u32 v148, v148, v76, v77\n\t" \
  "v_max3_u32 v148, v148, v78, v79\n\t" \
  "v_max3_u32 v148, v148, v80, v81\n\t" \
  "v_max3_u32 v148, v148, v82, v83\n\t" \
  "v_max3_u32 v148, v148, v84, v85\n\t" \
  "v_max3_u32 v148, v148, v86, v87\n\t" \
  "v_max3_u32 v148, v148, v88, v89\n\t" \
  "v_max3_u32 v148, v148, v90, v91\n\t" \
  "v_max3_u32 v148, v148, v92, v93\n\t" \
  "v_max3_u32 v148, v148, v94, v95\n\t" \
  "v_max3_u32 v148, v148, v96, v97\n\t" \
  "v_max3_u32 v148, v148, v98, v99\n\t" \
  "v_max3_u32 v148, v148, v100, v101\n\t" \
  "v_max3_u32 v148, v148, v102, v103\n\t" \
  "v_max3_u32 v148, v148, v104, v105\n\t" \
  "v_max3_u32 v148, v148, v106, v107\n\t" \
  "v_max3_u32 v148, v148, v108, v109\n\t" \
  "v_max3_u32 v148, v148, v110, v111\n\t" \
  "v_max3_u32 v148, v148, v112, v113\n\t" \
  "v_max3_u32 v148, v148, v114, v115\n\t" \
  "v_max3_u32 v148, v148, v116, v117\n\t" \
  "v_max3_u32 v148, v148, v118, v119\n\t" \
  "v_max3_u32 v148, v148, v120, v121\n\t" \
  "v_max3_u32 v148, v148, v122, v123\n\t" \
  "v_max3_u32 v148, v148, v124, v125\n\t" \
  "v_max3_u32 v148, v148, v126, v127\n\t" \
  "v_cmp_eq_u32 vcc, -1, v148\n\t" \
  "s_cbranch_vccz 36f\n\t" \
  "s_add_u32 s59, s59, 0x1000000\n\t" \
  "s_cmp_lt_u32 s59, 0x10000000\n\t" \
  "s_cbranch_scc1 35b\n\t" \
  "s_or_b32 s58, s58, 9\n\t" \
  "s_branch 7f\n\t" \
  "36:\n\t" \
  "s_and_b32 s81, s40, 15\n\t" \
  "s_lshl_b32 s81, s81, 22\n\t" \
  "s_add_i32 s81, s81, s51\n\t" \
  "s_lshr_b32 s80, s81, 21\n\t" \
  "s_bitcmp0_b64 %[sel], s80\n\t" \
  "s_cselect_b32 s80, 0x2000, 0\n\t" \
  "s_add_i32 s81, s81, s80\n\t" \
  "buffer_load_dwordx4 a[128:131], v208, %[ru], s81 offen offset:0 sc1\n\t" \
  "buffer_load_dwordx4 a[132:135], v208, %[ru], s81 offen offset:1024 sc1\n\t" \
  "s_add_i32 s81, s81, 0x20000\n\t" \
  "buffer_load_dwordx4 a[136:139], v208, %[ru], s81 offen offset:0 sc1\n\t" \
  "buffer_load_dwordx4 a[140:143], v208, %[ru], s81 offen offset:1024 sc1\n\t" \
  "s_add_i32 s81, s81, 0x20000\n\t" \
  "buffer_load_dwordx4 a[144:147], v208, %[ru], s81 offen offset:0 sc1\n\t" \
  "buffer_load_dwordx4 a[148:151], v208, %[ru], s81 offen offset:1024 sc1\n\t" \
  "s_add_i32 s81, s81, 0x20000\n\t" \
  "buffer_load_dwordx4 a[152:155], v208, %[ru], s81 offen offset:0 sc1\n\t" \
  "buffer_load_dwordx4 a[156:159], v208, %[ru], s81 offen offset:1024 sc1\n\t" \
  "s_add_i32 s81, s81, 0x20000\n\t" \
  "buffer_load_dwordx4 a[160:163], v208, %[ru], s81 offen offset:0 sc1\n\t" \
  "buffer_load_dwordx4 a[164:167], v208, %[ru], s81 offen offset:1024 sc1\n\t" \
  "s_add_i32 s81, s81, 0x20000\n\t" \
  "buffer_load_dwordx4 a[168:171], v208, %[ru], s81 offen offset:0 sc1\n\t" \
  "buffer_load_dwordx4 a[172:175], v208, %[ru], s81 offen offset:1024 sc1\n\t" \
  "s_add_i32 s81, s81, 0x20000\n\t" \
  "buffer_load_dwordx4 a[176:179], v208, %[ru], s81 offen offset:0 sc1\n\t" \
  "buffer_load_dwordx4 a[180:183], v208, %[ru], s81 offen offset:1024 sc1\n\t" \
  "s_add_i32 s81, s81, 0x20000\n\t" \
  "buffer_load_dwordx4 a[184:187], v208, %[ru], s81 offen offset:0 sc1\n\t" \
  "buffer_load_dwordx4 a[188:191], v208, %[ru], s81 offen offset:1024 sc1\n\t" \
  "s_add_i32 s81, s81, 0x20000\n\t" \
  "buffer_load_dwordx4 a[192:195], v208, %[ru], s81 offen offset:0 sc1\n\t" \
  "buffer_load_dwordx4 a[196:199], v208, %[ru], s81 offen offset:1024 sc1\n\t" \
  "s_add_i32 s81, s81, 0x20000\n\t" \
  "buffer_load_dwordx4 a[200:203], v208, %[ru], s81 offen offset:0 sc1\n\t" \
  "buffer_load_dwordx4 a[204:207], v208, %[ru], s81 offen offset:1024 sc1\n\t" \
  "s_add_i32 s81, s81, 0x20000\n\t" \
  "buffer_load_dwordx4 a[208:211], v208, %[ru], s81 offen offset:0 sc1\n\t" \
  "buffer_load_dwordx4 a[212:215], v208, %[ru], s81 offen offset:1024 sc1\n\t" \
  "s_add_i32 s81, s81, 0x20000\n\t" \
  "buffer_load_dwordx4 a[216:219], v208, %[ru], s81 offen offset:0 sc1\n\t" \
  "buffer_load_dwordx4 a[220:223], v208, %[ru], s81 offen offset:1024 sc1\n\t" \
  "s_add_i32 s81, s81, 0x20000\n\t" \
  "buffer_load_dwordx4 a[224:227], v208, %[ru], s81 offen offset:0 sc1\n\t" \
  "buffer_load_dwordx4 a[228:231], v208, %[ru], s81 offen offset:1024 sc1\n\t" \
  "s_add_i32 s81, s81, 0x20000\n\t" \
  "buffer_load_dwordx4 a[232:235], v208, %[ru], s81 offen offset:0 sc1\n\t" \
  "buffer_load_dwordx4 a[236:239], v208, %[ru], s81 offen offset:1024 sc1\n\t" \
  "s_add_i32 s81, s81, 0x20000\n\t" \
  "buffer_load_dwordx4 a[240:243], v208, %[ru], s81 offen offset:0 sc1\n\t" \
  "buffer_load_dwordx4 a[244:247], v208, %[ru], s81 offen offset:1024 sc1\n\t" \
  "s_add_i32 s81, s81, 0x20000\n\t" \
  "buffer_load_dwordx4 a[248:251], v208, %[ru], s81 offen offset:0 sc1\n\t" \
  "buffer_load_dwordx4 a[252:255], v208, %[ru], s81 offen offset:1024 sc1\n\t" \
  "s_add_i32 s81, s81, 0x20000\n\t" \
  "s_waitcnt vmcnt(0) lgkmcnt(0)\n\t" \
  "v_accvgpr_mov_b32 a0, a128\n\t" \
  "v_accvgpr_mov_b32 a1, a136\n\t" \
  "v_accvgpr_mov_b32 a2, a130\n\t" \
  "v_accvgpr_mov_b32 a3, a138\n\t" \
  "v_accvgpr_mov_b32 a4, a129\n\t" \
  "v_accvgpr_mov_b32 a5, a137\n\t" \
  "v_accvgpr_mov_b32 a6, a131\n\t" \
  "v_accvgpr_mov_b32 a7, a139\n\t" \
  "v_accvgpr_mov_b32 a8, a132\n\t" \
  "v_accvgpr_mov_b32 a9, a140\n\t" \
  "v_accvgpr_mov_b32 a10, a134\n\t" \
  "v_accvgpr_mov_b32 a11, a142\n\t" \
  "v_accvgpr_mov_b32 a12, a133\n\t" \
  "v_accvgpr_mov_b32 a13, a141\n\t" \
  "v_accvgpr_mov_b32 a14, a135\n\t" \
  "v_accvgpr_mov_b32 a15, a143\n\t" \
  "v_accvgpr_mov_b32 a16, a144\n\t" \
  "v_accvgpr_mov_b32 a17, a152\n\t" \
  "v_accvgpr_mov_b32 a18, a146\n\t" \
  "v_accvgpr_mov_b32 a19, a154\n\t" \
  "v_accvgpr_mov_b32 a20, a145\n\t" \
  "v_accvgpr_mov_b32 a21, a153\n\t" \
  "v_accvgpr_mov_b32 a22, a147\n\t" \
  "v_accvgpr_mov_b32 a23, a155\n\t" \
  "v_accvgpr_mov_b32 a24, a148\n\t" \
  "v_accvgpr_mov_b32 a25, a156\n\t" \
  "v_accvgpr_mov_b32 a26, a150\n\t" \
  "v_accvgpr_mov_b32 a27, a158\n\t" \
  "v_accvgpr_mov_b32 a28, a149\n\t" \
  "v_accvgpr_mov_b32 a29, a157\n\t" \
  "v_accvgpr_mov_b32 a30, a151\n\t" \
  "v_accvgpr_mov_b32 a31, a159\n\t" \
  "v_accvgpr_mov_b32 a32, a160\n\t" \
  "v_accvgpr_mov_b32 a33, a168\n\t" \
  "v_accvgpr_mov_b32 a34, a162\n\t" \
  "v_accvgpr_mov_b32 a35, a170\n\t" \
  "v_accvgpr_mov_b32 a36, a161\n\t" \
  "v_accvgpr_mov_b32 a37, a169\n\t" \
  "v_accvgpr_mov_b32 a38, a163\n\t" \
  "v_accvgpr_mov_b32 a39, a171\n\t" \
  "v_accvgpr_mov_b32 a40, a164\n\t" \
  "v_accvgpr_mov_b32 a41, a172\n\t" \
  "v_accvgpr_mov_b32 a42, a166\n\t" \
  "v_accvgpr_mov_b32 a43, a174\n\t" \
  "v_accvgpr_mov_b32 a44, a165\n\t" \
  "v_accvgpr_mov_b32 a45, a173\n\t" \
  "v_accvgpr_mov_b32 a46, a167\n\t" \
  "v_accvgpr_mov_b32 a47, a175\n\t" \
  "v_accvgpr_mov_b32 a48, a176\n\t" \
  "v_accvgpr_mov_b32 a49, a184\n\t" \
  "v_accvgpr_mov_b32 a50, a178\n\t" \
  "v_accvgpr_mov_b32 a51, a186\n\t" \
  "v_accvgpr_mov_b32 a52, a177\n\t" \
  "v_accvgpr_mov_b32 a53, a185\n\t" \
  "v_accvgpr_mov_b32 a54, a179\n\t" \
  "v_accvgpr_mov_b32 a55, a187\n\t" \
  "v_accvgpr_mov_b32 a56, a180\n\t" \
  "v_accvgpr_mov_b32 a57, a188\n\t" \
  "v_accvgpr_mov_b32 a58, a182\n\t" \
  "v_accvgpr_mov_b32 a59, a190\n\t" \
  "v_accvgpr_mov_b32 a60, a181\n\t" \
  "v_accvgpr_mov_b32 a61, a189\n\t" \
  "v_accvgpr_mov_b32 a62, a183\n\t" \
  "v_accvgpr_mov_b32 a63, a191\n\t" \
  "v_accvgpr_mov_b32 a64, a192\n\t" \
  "v_accvgpr_mov_b32 a65, a200\n\t" \
  "v_accvgpr_mov_b32 a66, a194\n\t" \
  "v_accvgpr_mov_b32 a67, a202\n\t" \
  "v_accvgpr_mov_b32 a68, a193\n\t" \
  "v_accvgpr_mov_b32 a69, a201\n\t" \
  "v_accvgpr_mov_b32 a70, a195\n\t" \
  "v_accvgpr_mov_b32 a71, a203\n\t" \
  "v_accvgpr_mov_b32 a72, a196\n\t" \
  "v_accvgpr_mov_b32 a73, a204\n\t" \
  "v_accvgpr_mov_b32 a74, a198\n\t" \
  "v_accvgpr_mov_b32 a75, a206\n\t" \
  "v_accvgpr_mov_b32 a76, a197\n\t" \
  "v_accvgpr_mov_b32 a77, a205\n\t" \
  "v_accvgpr_mov_b32 a78, a199\n\t" \
  "v_accvgpr_mov_b32 a79, a207\n\t" \
  "v_accvgpr_mov_b32 a80, a208\n\t" \
  "v_accvgpr_mov_b32 a81, a216\n\t" \
  "v_accvgpr_mov_b32 a82, a210\n\t" \
  "v_accvgpr_mov_b32 a83, a218\n\t" \
  "v_accvgpr_mov_b32 a84, a209\n\t" \
  "v_accvgpr_mov_b32 a85, a217\n\t" \
  "v_accvgpr_mov_b32 a86, a211\n\t" \
  "v_accvgpr_mov_b32 a87, a219\n\t" \
  "v_accvgpr_mov_b32 a88, a212\n\t" \
  "v_accvgpr_mov_b32 a89, a220\n\t" \
  "v_accvgpr_mov_b32 a90, a214\n\t" \
  "v_accvgpr_mov_b32 a91, a222\n\t" \
  "v_accvgpr_mov_b32 a92, a213\n\t" \
  "v_accvgpr_mov_b32 a93, a221\n\t" \
  "v_accvgpr_mov_b32 a94, a215\n\t" \
  "v_accvgpr_mov_b32 a95, a223\n\t" \
  "v_accvgpr_mov_b32 a96, a224\n\t" \
  "v_accvgpr_mov_b32 a97, a232\n\t" \
  "v_accvgpr_mov_b32 a98, a226\n\t" \
  "v_accvgpr_mov_b32 a99, a234\n\t" \
  "v_accvgpr_mov_b32 a100, a225\n\t" \
  "v_accvgpr_mov_b32 a101, a233\n\t" \
  "v_accvgpr_mov_b32 a102, a227\n\t" \
  "v_accvgpr_mov_b32 a103, a235\n\t" \
  "v_accvgpr_mov_b32 a104, a228\n\t" \
  "v_accvgpr_mov_b32 a105, a236\n\t" \
  "v_accvgpr_mov_b32 a106, a230\n\t" \
  "v_accvgpr_mov_b32 a107, a238\n\t" \
  "v_accvgpr_mov_b32 a108, a229\n\t" \
  "v_accvgpr_mov_b32 a109, a237\n\t" \
  "v_accvgpr_mov_b32 a110, a231\n\t" \
  "v_accvgpr_mov_b32 a111, a239\n\t" \
  "v_accvgpr_mov_b32 a112, a240\n\t" \
  "v_accvgpr_mov_b32 a113, a248\n\t" \
  "v_accvgpr_mov_b32 a114, a242\n\t" \
  "v_accvgpr_mov_b32 a115, a250\n\t" \
  "v_accvgpr_mov_b32 a116, a241\n\t" \
  "v_accvgpr_mov_b32 a117, a249\n\t" \
  "v_accvgpr_mov_b32 a118, a243\n\t" \
  "v_accvgpr_mov_b32 a119, a251\n\t" \
  "v_accvgpr_mov_b32 a120, a244\n\t" \
  "v_accvgpr_mov_b32 a121, a252\n\t" \
  "v_accvgpr_mov_b32 a122, a246\n\t" \
  "v_accvgpr_mov_b32 a123, a254\n\t" \
  "v_accvgpr_mov_b32 a124, a245\n\t" \
  "v_accvgpr_mov_b32 a125, a253\n\t" \
  "v_accvgpr_mov_b32 a126, a247\n\t" \
  "v_accvgpr_mov_b32 a127, a255\n\t" \
  "s_and_b32 s58, s58, 4\n\t" \
  "s_or_b32 s58, s58, 32\n\t" \
  "s_branch 31b\n\t" \
  "18:\n\t" \
  "v_mov_b32 v186, -1\n\t" \
  "s_and_b32 s80, s40, 3\n\t" \
  "s_mul_i32 s77, s80, 0xc000\n\t" \
  "s_add_i32 s77, s77, 0x40000\n\t" \
  "s_lshl_b32 s78, s80, 14\n\t" \
  "s_add_i32 s78, s78, 0x70000\n\t" \
  "s_mov_b32 s57, 0\n\t" \
  "s_add_i32 s96, s77, 0x1000\n\t" \
  "s_and_b32 s98, s40, 2\n\t" \
  "s_lshl_b32 s98, s98, 13\n\t" \
  "s_and_b32 s80, s40, 1\n\t" \
  "s_lshl_b32 s80, s80, 12\n\t" \
  "s_add_i32 s98, s98, s80\n\t" \
  "s_add_i32 s98, s98, 0x84000\n\t" \
  "12:\n\t" \
  "s_or_b32 s58, s58, 3\n\t" \
  "11:\n\t" \
  "buffer_load_dwordx4 v[0:3], v193, %[rh], s77 offen offset:0 sc1\n\t" \
  "buffer_load_dwordx4 v[4:7], v193, %[rh], s77 offen offset:1024 sc1\n\t" \
  "buffer_load_dwordx4 v[8:11], v193, %[rh], s77 offen offset:2048 sc1\n\t" \
  "buffer_load_dwordx4 v[12:15], v193, %[rh], s77 offen offset:3072 sc1\n\t" \
  "buffer_load_dwordx4 v[16:19], v193, %[rh], s96 offen offset:0 sc1\n\t" \
  "buffer_load_dwordx4 v[20:23], v193, %[rh], s96 offen offset:1024 sc1\n\t" \
  "buffer_load_dwordx4 v[24:27], v193, %[rh], s96 offen offset:2048 sc1\n\t" \
  "buffer_load_dwordx4 v[28:31], v193, %[rh], s96 offen offset:3072 sc1\n\t" \
  "buffer_load_dwordx4 v[32:35], v195, %[rh], s77 offen offset:0 sc1\n\t" \
  "buffer_load_dwordx4 v[36:39], v195, %[rh], s77 offen offset:2048 sc1\n\t" \
  "v_mov_b64 v[40:41], 0\n\t" \
  "v_mov_b64 v[42:43], 0\n\t" \
  "v_mov_b64 v[44:45], 0\n\t" \
  "v_mov_b64 v[46:47], 0\n\t" \
  "v_mov_b64 v[48:49], 0\n\t" \
  "v_mov_b64 v[50:51], 0\n\t" \
  "v_mov_b64 v[52:53], 0\n\t" \
  "v_mov_b64 v[54:55], 0\n\t" \
  "v_mov_b64 v[56:57], 0\n\t" \
  "v_mov_b64 v[58:59], 0\n\t" \
  "v_mov_b64 v[60:61], 0\n\t" \
  "v_mov_b64 v[62:63], 0\n\t" \
  "v_mov_b64 v[64:65], 0\n\t" \
  "v_mov_b64 v[66:67], 0\n\t" \
  "v_mov_b64 v[68:69], 0\n\t" \
  "v_mov_b64 v[70:71], 0\n\t" \
  "v_mov_b64 v[72:73], 0\n\t" \
  "v_mov_b64 v[74:75], 0\n\t" \
  "v_mov_b64 v[76:77], 0\n\t" \
  "v_mov_b64 v[78:79], 0\n\t" \
  "v_mov_b64 v[80:81], 0\n\t" \
  "v_mov_b64 v[82:83], 0\n\t" \
  "v_mov_b64 v[84:85], 0\n\t" \
  "v_mov_b64 v[86:87], 0\n\t" \
  "v_mov_b64 v[88:89], 0\n\t" \
  "v_mov_b64 v[90:91], 0\n\t" \
  "v_mov_b64 v[92:93], 0\n\t" \
  "v_mov_b64 v[94:95], 0\n\t" \
  "v_mov_b64 v[96:97], 0\n\t" \
  "v_mov_b64 v[98:99], 0\n\t" \
  "v_mov_b64 v[100:101], 0\n\t" \
  "v_mov_b64 v[102:103], 0\n\t" \
  "v_mov_b64 v[136:137], 0\n\t" \
  "v_mov_b64 v[138:139], 0\n\t" \
  "v_mov_b64 v[140:141], 0\n\t" \
  "v_mov_b64 v[142:143], 0\n\t" \
  "s_waitcnt vmcnt(0)\n\t" \
  "s_add_u32 s59, s59, 1\n\t" \
  "s_bitcmp1_b32 s58, 2\n\t" \
  "s_cbranch_scc1 10b\n\t" \
  "v_max_u32 v148, v0, v1\n\t" \
  "v_max3_u32 v148, v148, v2, v3\n\t" \
  "v_max3_u32 v148, v148, v4, v5\n\t" \
  "v_max3_u32 v148, v148, v6, v7\n\t" \
  "v_max3_u32 v148, v148, v8, v9\n\t" \
  "v_max3_u32 v148, v148, v10, v11\n\t" \
  "v_max3_u32 v148, v148, v12, v13\n\t" \
  "v_max3_u32 v148, v148, v14, v15\n\t" \
  "v_max3_u32 v148, v148, v16, v17\n\t" \
  "v_max3_u32 v148, v148, v18, v19\n\t" \
  "v_max3_u32 v148, v148, v20, v21\n\t" \
  "v_max3_u32 v148, v148, v22, v23\n\t" \
  "v_max3_u32 v148, v148, v24, v25\n\t" \
  "v_max3_u32 v148, v148, v26, v27\n\t" \
  "v_max3_u32 v148, v148, v28, v29\n\t" \
  "v_max3_u32 v148, v148, v30, v31\n\t" \
  "v_max3_u32 v148, v148, v32, v33\n\t" \
  "v_max3_u32 v148, v148, v34, v35\n\t" \
  "v_max3_u32 v148, v148, v36, v37\n\t" \
  "v_max3_u32 v148, v148, v38, v39\n\t" \
  "v_cmp_eq_u32 vcc, -1, v148\n\t" \
  "s_cbranch_vccz 10b\n\t" \
  "s_bitcmp1_b32 s59, 6\n\t" \
  "s_cbranch_scc0 11b\n\t" \
  "7:\n\t" \
  "92:\n\t" \
  "s_mov_b32 s42, s58\n\t" \
  "s_waitcnt vmcnt(0) lgkmcnt(0)\n\t" \
  ""

#define V53 "v0", "v1", "v2", "v3", "v4", "v5", "v6", "v7", "v8", "v9", "v10", "v11", "v12", "v13", "v14", "v15", "v16", "v17", \
  "v18", "v19", "v20", "v21", "v22", "v23", "v24", "v25", "v26", "v27", "v28", "v29", "v30", "v31", "v32", "v33", "v34", "v35", \
  "v36", "v37", "v38", "v39", "v40", "v41", "v42", "v43", "v44", "v45", "v46", "v47", "v48", "v49", "v50", "v51", "v52"
#define V100 V53, "v53", "v54", "v55", "v56", "v57", "v58", "v59", "v60", "v61", "v62", "v63", "v64", "v65", "v66", "v67", "v68", "v69", "v70", \
  "v71", "v72", "v73", "v74", "v75", "v76", "v77", "v78", "v79", "v80", "v81", "v82", "v83", "v84", "v85", "v86", "v87", "v88", \
  "v89", "v90", "v91", "v116", "v117", "v118", "v119"
#define STEP_PV
#define STEPS(BODY, ...) asm volatile(BODY : "=&{v[92:99]}"(X), "+{s[40:55]}"(P) STEP_PV \
  : "{v[120:151]}"(W.v[0]), "{v[152:183]}"(W.v[1]), "{v[184:215]}"(W.v[2]), "{v[216:247]}"(W.v[3]), "{v[100:115]}"(C), [rh] "s"(rh), [wv] "s"(w), [ru] "s"(rum), [sel] "s"(sel) __VA_ARGS__ \
  : "memory", "vcc", "scc", "s56", "s57", "s58", "s59", "s76", "s77", "s78", "s79", "s80", "s81", "s82", "s83", "s84", "s85", "s86", "s87", "s88", "s89", \
    "s90", "s91", "s92", "s93", "s94", "s95", "s96", "s97", V100)
__device__ __forceinline__ void keep_a(const Wts& W) {
#pragma unroll
  for (int i = 0; i < 32; ++i) asm volatile("" :: "a"(W.a[i]));
}
__device__ __forceinline__ char* lds_base(char* smem, int off) { asm volatile("" : "+v"(off)); return smem + off; }
__device__ __forceinline__ u32x2 tr4(unsigned p) { return __builtin_bit_cast(u32x2, __builtin_amdgcn_ds_read_tr16_b64_v4i16((LAS bf16x4*)(uintptr_t)p)); }
__device__ __forceinline__ float rb1(float x) { return __uint_as_float(pk2(0.f, x)); }

__device__ __forceinline__ void steps_init(u32x16& P, int h, int w, unsigned place) {
#pragma unroll
  for (int i = 0; i < 16; ++i) P[i] = 0;
  P[2] = 1; P[3] = 1490; P[14] = H_MEM + (w >> 1) * CHK + (w & 1) * 4096 + (h & RHO) * 1024; P[15] = place;
}
__device__ __forceinline__ void steps_enter(u32x16& P, Clk& ck, bool dead) {
  ck.sync();
  P[2] = (P[2] & 3u) | (dead ? 4u : 0u); P[6] = ck.c2t << 15; P[11] = ck.ce; P[12] = ck.t2c; P[13] = ck.te;
#pragma unroll
  for (int i = 0; i < 16; ++i) P[i] = __builtin_amdgcn_readfirstlane(P[i]);
}

__device__ __forceinline__ void role_qk(const ChainArgs& a, int h, char* smem, const int w, Gate& G) {
  int lane = opaque_tid(0);
  const int b = h; (void)b;
  u64* prof = a.prof; (void)prof;
  PDECL(L_PRF, 4);
  const float CS = 0.08838834764831845f * 1.4426950408889634f;
  GAS unsigned* abortw = (GAS unsigned*)(a.sync + SY_ABORT);
  bool dead = false;
  Wts W;
  Clk ck; ck.init();
  load_weights<0>(a, h, w, lane, smem, W);
  if (!G.pass(a.sync)) return;
  PZERO;
  const Sel sel = G.sel;
  start_wait(a.sync, abortw, lane, dead, CT_K + h, CT_Q + h);
  const __amdgpu_buffer_rsrc_t rh = rsrc_hot(a.sync, sel), rum = rsrc_u(a.sync);
#pragma unroll
  for (int i = 0; i < 2; ++i) *(u32x4*)(smem + L_KX + (2 * w + i) * 1024 + lane * 16) = ld16(rum, x_slot(1, 0, h, sel) + (2 * w + i) * 1024 + lane * 16);
  auto pm_o = [&](int l) { return 2 * CHK + h * 256 + (l & 1) * 128 + (l >> 3) * 16 + (l & 6) * 2; };
  auto fac = [&](int l, int hf) { return ((l & 7) >> 1) == 2 * (l >> 5) + ((h >> 1) & 1) && ((l >> 3) & 1) == hf ? 1.f : 0.f; };
  u32x16 C, P;
  {
    const int r = lane & 15, g = lane >> 4;
    const unsigned l0 = (unsigned)(uintptr_t)(LAS char*)smem;
    C[0] = lane * 16; C[1] = l0 + L_MEM + lane * 16; C[2] = l0 + L_WTS + w * 4096 + lane * 16; C[3] = C[2] + 65536;
    C[4] = (lane & 2) ? 0xEEEE : 0x4444;
    C[5] = l0 + L_XCH + (16 * w + 2 * (r >> 2) + (r & 1)) * 32 + g * 8 + ((r >> 1) & 1) * 4;
    C[6] = l0 + L_XK + (32 * w + r) * 16 + g * 4;
    C[7] = w == 3 ? H_PX + (h >> 3) * CHK + (h & 7) * 1024 + lane * 16 : w ? (2 * h + 1) * CHK + (w - 1) * 4096 + lane * 16 : l0 + L_XCH + (8 * g + (r >> 2)) * 32 + (r & 3) * 8;
    C[8] = l0 + L_XK + (8 * g + (r >> 2)) * 16 + (r & 1) * 8;
    C[9] = w ? (lane & 1) * 4096 : (lane == 0 ? H_ANN + h * 128 : 0x7fffffffu);
    C[10] = w == 0 ? (h >> 4) * CHK + (h & 15) * 512 + ((lane >> 1) & 1) * 256 + (lane >> 5) * 128 + (lane & 1) * 64 + ((lane >> 3) & 3) * 16 + ((lane >> 2) & 1) * 8 :
            w < 3 ? H_PX + ((w - 1) * 4 + (h >> 3)) * CHK + (h & 7) * 1024 + lane * 16 :
            lane < 32 ? (h >> 4) * CHK + (h & 15) * 512 + lane * 16 : (lane < 48 ? 2 * CHK + h * 256 + (lane - 32) * 16 : 0x7fffffffu);
    C[11] = w == 0 ? pm_o(lane) : (lane < (w == 3 ? 32 : 16) ? H_PX + (w == 3 ? 8 + g : 7 + w) * CHK + hpos(h) * 256 + r * 16 : 0x7fffffffu);
    C[12] = H_TR + (lane & 31) * 128; C[13] = l0 + L_PRF + w * 1024 + (lane >> 6);
    unsigned mk = 0;
#pragma unroll
    for (int i = 0; i < 8; ++i) mk |= (16 * ((i >> 1) & 1) + 8 * (g & 1) + 4 * (g >> 1) + 2 * (i & 1) + (i >> 2) > 16 * (w - 1) + r ? 1u : 0u) << i;
    C[14] = w ? mk : __float_as_uint(fac(lane, 0)); C[15] = __float_as_uint(fac(lane, 1));
    asm volatile("" : "+v"(C));
  }
  SSTAMP(96 + h);
  steps_init(P, h, w, 2 * h * CHK + (3 * w - 3) * 1024);
  GAS unsigned* kcp = ctr(a, CT_Q + h);
  unsigned spins = 0;
  while (true) {
    u32x8 X; u32x16 pv; (void)pv;
    steps_enter(P, ck, dead);
    STEPS(QK_BODY, , [cs] "s"(CS), [kc] "s"(kcp));
    keep_a(W);
    const unsigned res = __builtin_amdgcn_readfirstlane(P[2]), s = __builtin_amdgcn_readfirstlane(P[4]);
    lane = opaque_tid(0);
    if (res & 8u) {
      auto sw = [](float v, int k) { return __int_as_float(k == 0 ? __builtin_amdgcn_mov_dpp(__float_as_int(v), 0xB1, 0xf, 0xf, false) :
                                                           k == 1 ? __builtin_amdgcn_mov_dpp(__float_as_int(v), 0x4E, 0xf, 0xf, false) :
                                                                    __builtin_amdgcn_mov_dpp(__float_as_int(v), 0x141, 0xf, 0xf, false)); };
      float x[5], d = fmaxf(__uint_as_float(X[4]), 0.f);
      d = fmaxf(d, sw(d, 0)); d = fmaxf(d, sw(d, 1)); d = fmaxf(d, sw(d, 2));
#pragma unroll
      for (int i = 0; i < 5; ++i) x[i] = __builtin_amdgcn_exp2f(__uint_as_float(X[i]) - d);
      float z = ((x[0] + x[1]) + (x[2] + x[3])) + x[4];
      z += sw(z, 0); z += sw(z, 1); z += sw(z, 2);
      z = __builtin_amdgcn_rcpf(z);
      const float y = sw(x[4], 0), f = __builtin_amdgcn_exp2f(-d) * z;
      const unsigned pm = (lane & 1) ? pk2(fac(lane, 0) * f, fac(lane, 1) * f) : pk2(x[4] * z, y * z);
      if (!(__uint_as_float(X[5]) < 1.0e18f))
        __builtin_amdgcn_raw_buffer_store_b32(nosent(pm), rh, pm_o(lane), H_P + ((s - 1) & 3) * 3 * CHK, ST_AUX);
    }
    if (res & 64u) break;
    if (res & 32u) { ctr_wait(kcp, min(s + 2, (unsigned)NSEG), abortw, lane, dead); ctr_wait(kcp + 1024, min(s + 2, (unsigned)NSEG), abortw, lane, dead); }
    if ((res & 32u) && s == 1) {
      for (int i = 3 * w - 3; i < min(3 * w, 8); ++i) *(u32x4*)(smem + L_KX + i * 1024 + lane * 16) = ld16(rum, x_slot(1, 1, h, sel) + i * 1024 + lane * 16);
    }
    if (res & 17u) {
      spins |= 63u;
      if (dead || poll_fail(spins, abortw, lane)) dead = true;
    }
  }
  PDUMP(0);
}

__device__ __forceinline__ void role_vo(const ChainArgs& a, int h, char* smem, const int w, Gate& G) {
  int lane = opaque_tid(0);
  const int b = 32 + h; (void)b;
  u64* prof = a.prof; (void)prof;
  PDECL(L_PRF, 4);
  GAS unsigned* abortw = (GAS unsigned*)(a.sync + SY_ABORT);
  bool dead = false;
  Wts W;
  Clk ck; ck.init();
  load_weights<1>(a, h, w, lane, smem, W);
  if (!G.pass(a.sync)) return;
  PZERO;
  const Sel sel = G.sel;
  start_wait(a.sync, abortw, lane, dead, CT_INIT, CT_INIT);
  const __amdgpu_buffer_rsrc_t rh = rsrc_hot(a.sync, sel), rum = rsrc_u(a.sync);
  u32x16 C, P;
  {
    const int r = lane & 15, g = lane >> 4;
    const unsigned l0 = (unsigned)(uintptr_t)(LAS char*)smem;
#pragma unroll
    for (int i = 0; i < 16; ++i) C[i] = H_TR + (lane & 31) * 128;
    C[0] = lane * 16; C[1] = l0 + L_MEM + lane * 16; C[2] = l0 + L_WTS + w * 4096 + lane * 16; C[3] = C[2] + 65536;
    C[4] = (lane & 2) ? 0xEEEE : 0x4444;
    C[5] = l0 + L_XCH + (32 * w + r) * 16 + g * 4;
    C[6] = (lane & 8) ? 0xEEEE : 0x4444;
    C[7] = l0 + L_XCH + (32 * (g >> 1) + 8 * (g & 1) + (r >> 2)) * 16 + (r & 1) * 8;
    C[8] = ((g & 1) * 8 + (g >> 1)) * CHK + r * 16;
    C[13] = l0 + L_PRF + w * 1024 + (lane >> 6);
    asm volatile("" : "+v"(C));
  }
  SSTAMP(128 + h);
  steps_init(P, h, w, SY_UM + (16 * (h & 3) + 2 * w) * CHK + hpos(h) * 256);
  unsigned spins = 0;
  while (true) {
    u32x8 X; u32x16 pv; (void)pv;
    steps_enter(P, ck, dead);
    STEPS(VO_BODY);
    keep_a(W);
    lane = opaque_tid(0);
    if (__builtin_amdgcn_readfirstlane(P[2]) & 64u) break;
    spins |= 63u;
    if (dead || poll_fail(spins, abortw, lane)) dead = true;
  }
  PDUMP(0);
}

__device__ __forceinline__ void calibrate(const ChainArgs& a, int cal, int slot, u64 entry) {
  const u32x4 rs = own_rsrc(a.sync, SY_BYTES);
  unsigned best[2] = {1u << 30, 1u << 30};
  for (int i = 0;; ++i) {
    if (i >= CALN && ((min(best[0], best[1]) < CALT && max(best[0], best[1]) - min(best[0], best[1]) > 120u) || __builtin_amdgcn_s_memrealtime() - entry > CALM)) break;
#pragma unroll
    for (int hf = 0; hf < 2; ++hf) {
      u64 c0, c1;
      unsigned z = 0;
      asm volatile("s_mov_b64 exec, 1\n\ts_memtime %0\n\ts_waitcnt lgkmcnt(0)\n\tbuffer_atomic_or %2, off, %3, %4 sc0 sc1\n\ts_waitcnt vmcnt(0)\n\ts_memtime %1\n\ts_mov_b64 exec, -1\n\ts_waitcnt lgkmcnt(0)"
                   : "=&s"(c0), "=&s"(c1), "+v"(z) : "s"(rs), "s"(cal * PAGE + (slot >> 1) * CHK + hf * 8192 + (slot & 1) * 4096) : "memory");
      best[hf] = min(best[hf], (unsigned)(c1 - c0));
    }
  }
  const u64 hs = (__builtin_amdgcn_s_memrealtime() & ~1ull) | ((best[1] < best[0] ? 1u : 0u) ^ die_half());
  if (opaque_tid(0) == 0) __hip_atomic_store((GAS u64*)ctr(a, CT_CAL + (slot >> 1)) + (slot & 1), hs, __ATOMIC_RELAXED, __HIP_MEMORY_SCOPE_AGENT);
}
__device__ __forceinline__ u32x4 pk8(f32x4 a, f32x4 b) { return (u32x4){pk2(a[0], a[1]), pk2(a[2], a[3]), pk2(b[0], b[1]), pk2(b[2], b[3])}; }
__device__ __forceinline__ unsigned pk2v(float a, float b) { unsigned r; asm volatile("v_cvt_pk_bf16_f32 %0, %1, %2" : "=v"(r) : "v"(a), "v"(b)); return r; }
__device__ __forceinline__ u32x4 pk8v(f32x4 a, f32x4 b) { asm volatile("s_nop 7"); return (u32x4){pk2v(a[0], a[1]), pk2v(a[2], a[3]), pk2v(b[0], b[1]), pk2v(b[2], b[3])}; }
__device__ __forceinline__ void st16g(u16* p, u32x4 v) { asm volatile("global_store_dwordx4 %0, %1, off " OUTP "\n\ts_nop 1" :: "v"(p), "v"(v) : "memory"); }
__device__ __forceinline__ void role_cb(const ChainArgs& a, int cbi, char* smem, const int w, Gate& G) {
  int lane = opaque_tid(0);
  const int b = 64 + cbi; (void)b;
  const int par = cbi >> 5, ci = cbi & 31, J = ci >> 3;
  u64* prof = a.prof; (void)prof;
  PDECL(65536, 8); PZERO;
  GAS unsigned* abortw = (GAS unsigned*)(a.sync + SY_ABORT);
  bool dead = false;
  auto pf = [&](int n0, int n1) {
    for (int n = n0; n < n1; ++n) {
      const int blk = m_block(par, n & 1, ((par & n ? W_HELP2 : W_HELP) >> (4 * (n >> 1))) & 15);
      const bool wo = par && blk >= 16;
      const u16* base = wo ? a.Wo + (128 * ci + 64 * (blk & 1)) * DM + 128 * (((blk - 16) >> 1) ^ (2 * (ci & 3)))
                           : (par ? a.Wv : (blk & 1 ? a.Wk : a.Wq)) + 64 * ((par ? blk : blk >> 1) ^ (ci & RHO)) * HD + 128 * ci;
#pragma unroll
      for (int j = 0; j < 4; ++j)
        __builtin_amdgcn_global_load_lds((const GAS void*)(base + ((w * 4 + j) * 4 + (lane >> 4)) * (wo ? DM : HD) + (lane & 15) * 8), (LAS void*)(smem + 32768 + (w * 4 + j) * 1024), 16, 0, (W_NT & 4) / 2);
      wait_vm<24>();
    }
    wait_vm<0>();
  };
  auto sweep = [&](auto n0, auto n1) {
    const __amdgpu_buffer_rsrc_t rx = mk_rsrc((void*)a.X, 4u << 20);
    u32x4 v[16];
#pragma unroll
    for (int i = n0; i < n1; ++i) v[i] = __builtin_amdgcn_raw_buffer_load_b128(rx, (unsigned)(cbi * 4096 + opaque_tid(w) * 16), (unsigned)(i * 262144), 0);
#pragma unroll
    for (int i = n0; i < n1; ++i) asm volatile("" :: "v"(v[i]));
  };
  pf(0, PF1);
  SSTAMP(224 + ci);
  if (!G.pass(a.sync)) return;
  const Sel sel = G.sel;
  SSTAMP(64 + ci);
  const __amdgpu_buffer_rsrc_t rh = rsrc_hot(a.sync, sel), rum = rsrc_u(a.sync);
  const int tw = 2 * ci + (w & 1);
  const unsigned mo = (tw >> 5) * CHK + (tw & 31) * 256;
  {
    const unsigned s1v = ~(unsigned)(lane >> 6);
    const u32x4 sv = {s1v, s1v, s1v, s1v};
#pragma unroll
    for (int i = 0; i < 2; ++i) {
      const unsigned o = w * 2048 + i * 1024 + lane * 16;
#pragma unroll
      for (int k = 0; k < 4; ++k) st16(rum, near_half(SY_UM + (64 * k + cbi) * CHK, sel) + o, sv);
      if (cbi >= 2 && cbi < 35) st16(rh, cbi * CHK + o, sv);
      if ((cbi & 15) < 10) st16(rh, H_PX + cbi * CHK + o, sv);
    }
    if (par == 0 && w < 2) {
      const u16* m = a.mem0 + (2 * (lane >> 4) + ((lane >> 2) & 1)) * DM + 16 * tw + (lane & 8);
      st4(rh, H_MEM + mo + lane * 4, nosent(m[lane & 3] | (unsigned)m[7 - (lane & 3)] << 16));
    }
    asm volatile("s_waitcnt vmcnt(0)" ::: "memory");
    lds_barrier();
    if (w == 0 && lane == 0) ctr_add(a, CT_INIT, 1u);
    SSTAMP(256 + ci);
  }
  sweep(std::integral_constant<int, 0>{}, std::integral_constant<int, 2>{});
  pf(PF1, PF1 + PF2);
  sweep(std::integral_constant<int, 2>{}, std::integral_constant<int, 16>{});
  Wts W;
#pragma unroll
  for (int i = 0; i < 32; ++i) asm volatile("" : "=a"(W.a[i]));
  u32x16 C, P; u32x8 C2;
  {
    const unsigned l0 = (unsigned)(uintptr_t)(LAS char*)smem, oor = 0x7fffffffu;
    const int r = lane & 7, t = (lane >> 3) & 1, g = lane >> 4, e = w & 1;
    const bool L = w < 2;
    C[0] = lane * 16;
    C[1] = L ? l0 + 32768 + e * 8192 + (lane & 2) * 512 + ((lane & 1) * 32 + (lane >> 2)) * 16 : e * CHK + t * 512 + (g & 1) * 256 + (r >> 2) * 128 + (g >> 1) * 64 + (r & 3) * 16;
    C[2] = L ? (4 * t + g) * 1024 + r * 16 : l0 + 81920 + e * 32768 + lane * 16;
    C[3] = L ? l0 + e * 1024 + lane * 8 : 2 * CHK + e * 4096 + (4 * (g >> 1) + 2 * t + (g & 1)) * 256 + 128 + r * 16;
    C[4] = ((16 * e + (lane >> 2)) * OUTW + DM + 32 * ci + 8 * (lane & 3)) * 2;
    C[5] = l0 + (L ? e * 8192 + lane * 16 : e * 512 + lane * 8); C[6] = l0 + 16384 + lane * 16; C[7] = l0 + 32768 + (L ? e * 8192 + 4096 : e * 2048) + lane * 16;
    C[8] = (lane & 31) * 128;
    C[9] = (8 * (g & 1) + 4 * (g >> 1) + 2 * t + (r >> 2)) * 16 + 4 * (r < 4 ? r : 7 - r);
    C[10] = (lane | e) ? oor : ci * 128;
    C[11] = L ? SY_CT + (CT_U + 16 * e + (lane & 15)) * 128 : (t ? 0xEEEE : 0x4444); C[12] = L ? l0 + 81920 + e * 32768 + lane * 16 : ((lane & 2) ? 0xEEEE : 0x4444); C[13] = r < 4 ? 0x01000504u : 0x07060302u;
    C[14] = l0 + 49152 + 32 * e + (lane >> 6); C[15] = l0 + 49152 + (lane >> 6);
    C2[0] = L ? (lane < 16 ? lane * 16 : oor) : ux_tile(2 * ci) + lane * 16;
    C2[1] = lane == 0 && e && ci < 8 ? SY_CT + (CT_DONE + 8 * par + ci) * 128 : oor;
    C2[2] = (L ? ux_tile(2 * ci) : H_PX + 2 * e * CHK) + lane * 16;
#pragma unroll
    for (int n = 0; n < 4; ++n) C2[3 + n] = L ? H_PX + (8 + e) * CHK + (J ^ n) * 2048 + lane * 16 : C2[2] + CHK;
    C2[7] = l0 + 65536 + w * 2048 - 64 + (lane >> 6);
    asm volatile("" : "+v"(C), "+v"(C2));
#pragma unroll
    for (int i = 0; i < 16; ++i) P[i] = par - 2;
    P[2] = 48; P[6] = P[7] = K_DEF * TU; P[8] = SY_UM + tw * CHK + J * 2048; P[9] = H_MEM + mo; P[10] = 2 * CHK + J * 256; P[11] = e * 16 * UXH; P[12] = H_DLT + par * 4096;
  }
  const __amdgpu_buffer_rsrc_t ra = mk_rsrc(a.sync, SY_BYTES), ro = mk_rsrc(a.out, S_LEN * OUTW * 2);
  Clk ck; ck.init();
  SSTAMP(0 + ci);
  ctr_wait(ctr(a, CT_INIT), 64u, abortw, lane, dead);
  unsigned spins = 0;
  while (true) {
    u32x16 pv; (void)pv;
    const unsigned res = __builtin_amdgcn_readfirstlane(P[2]);
    if (res & 64u) break;
    if (res & 1u) {
      spins |= 63u;
      if (dead || poll_fail(spins, abortw, opaque_tid(0))) dead = true;
    }
    ck.sync();
    P[2] = (res & 58u) | (dead ? 4u : 0u); P[1] = ck.ce; P[3] = ck.te; P[4] = ck.t2c; P[5] = ck.c2t << 15;
#pragma unroll
    for (int i = 0; i < 16; ++i) P[i] = __builtin_amdgcn_readfirstlane(P[i]);
    asm volatile(CB_BODY : "+{s[40:55]}"(P) STEP_PV
                 : "{v[192:207]}"(C), "{v[208:215]}"(C2), [rh] "s"(rh), [ru] "s"(ra), [ro] "s"(ro), [wv] "s"(w), [sel] "s"(sel)
                 : "memory", "vcc", "scc", "s56", "s57", "s58", "s59", "s76", "s77", "s78", "s79", "s80", "s81", "s82", "s83", "s84", "s85", "s86", "s87", "s88", "s89", "s90", "s91", "s92", "s93", "s94", "s95", "s96", "s97", "s98", "s99", "s100", "s101", V53, "v53", "v54", "v55", "v56", "v57", "v58", "v59", "v60", "v61", "v62", "v63", "v64", "v65", "v66", "v67", "v68", "v69", "v70", "v71", "v72", "v73", "v74", "v75", "v76", "v77", "v78", "v79", "v80", "v81", "v82", "v83", "v84", "v85", "v86", "v87", "v88", "v89", "v90", "v91", "v92", "v93", "v94", "v95", "v96", "v97", "v98", "v99", "v100", "v101", "v102", "v103", "v104", "v105", "v106", "v107", "v108", "v109", "v110", "v111", "v112", "v113", "v114", "v115", "v116", "v117", "v118", "v119", "v120", "v121", "v122", "v123", "v124", "v125", "v126", "v127", "v128", "v129", "v130", "v131", "v132", "v133", "v134", "v135", "v136", "v137", "v138", "v139", "v140", "v141", "v142", "v143", "v144", "v145", "v146", "v147", "v148", "v149", "v150", "v151", "v152", "v153", "v154", "v155", "v156", "v157", "v158", "v159", "v160", "v161", "v162", "v163", "v164", "v165", "v166", "v167", "v168", "v169", "v170", "v171", "v172", "v173", "v174", "v175", "v176", "v177", "v178", "v179", "v180", "v181", "v182", "v183", "v184", "v185", "v186", "v187", "v188", "v189", "v190", "v191");
    keep_a(W);
  }
  PDUMP(2);
}

__device__ __forceinline__ void bg_drain() {
  asm volatile("s_waitcnt vmcnt(0)" ::: "memory");
  __builtin_amdgcn_s_barrier();
  asm volatile("" ::: "memory");
}
#define BG_SLP 64
#define A2N 2
constexpr int A2_NCH = A2N;
enum { K_U, K_W, K_NONE, K_A1, K_A2, K_A3 };
struct Pick {
  static constexpr unsigned A4 = 4 * A2_NCH, N4 = 8 * A4 + 128;
  const ChainArgs& a; int x, wv;
  unsigned v, o; int kind, j;
  __device__ __forceinline__ void look() {
    const int lane = opaque_tid(0);
    v = ld4(mk_rsrc(a.sync + SY_CT, SY_BYTES - SY_CT), wv || lane > 23 ? 0x7fffffffu : ((lane < 8 ? CT_QUEUE : (lane < 16 ? CT_A2 : CT_TK)) + ((x + lane) & 7)) * 128);
  }
  __device__ __forceinline__ bool claim() {
    const int lane = opaque_tid(0);
    const unsigned r = (unsigned)__builtin_amdgcn_ballot_w64(v + 1u >= (lane < 8 ? N4 : (lane < 16 ? 128u : 8u)));
    const unsigned m1 = r >> 8 & ~(r >> 16) & 255u, m0 = ~r & 255u;
    kind = m1 ? 1 : (m0 ? 0 : ((r >> 16 & 255u) == 255u ? -2 : -1));
    j = (x + __builtin_ctz((m1 ? m1 : m0) | 128u)) & 7;
    const __amdgpu_buffer_rsrc_t rc = mk_rsrc(a.sync + SY_CT, SY_BYTES - SY_CT);
    unsigned qo = ((kind ? CT_TK : CT_QUEUE) + j) * 128;
    o = kind ? 1u : A4;
    asm volatile("" : "+v"(qo), "+v"(o));
    if ((wv | lane) == 0 && kind >= 0) o = __builtin_amdgcn_raw_ptr_buffer_atomic_add_i32(o, rc, qo, 0, 0);
    return kind != -2;
  }
  __device__ __forceinline__ unsigned next() {
    unsigned spins = 0;
    while (kind != -2) {
      if (kind >= 0) {
        const unsigned e = __builtin_amdgcn_readfirstlane(o) + 1u;
        const int k = kind;
        kind = -1;
        if (e < (k ? 8u : N4)) return e | k << 16 | j << 20;
      } else if (spins) __builtin_amdgcn_s_sleep(Q_SLP);
      look();
      if (!claim() || poll_fail(spins, (GAS unsigned*)(a.sync + SY_ABORT), opaque_tid(0))) break;
    }
    return SENT;
  }
};

#define WAH 4
#define DSP 4
#define SIGQ 2
__device__ __forceinline__ void task_w(const ChainArgs& a, int kind, int h, char* smem, int wv, Gate& G) {
  const int lane = opaque_tid(0), w = wv, r = lane & 15, g = lane >> 4;
  const __amdgpu_buffer_rsrc_t rw = mk_rsrc(kind == 0 ? a.Wq : (kind == 1 ? a.Wk : a.Wv), 8u << 20), rx = mk_rsrc(a.X, 4u << 20);
  const unsigned lds0 = (unsigned)(uintptr_t)(LAS char*)smem + w * 4096;
  unsigned vx[2], vw[4];
#pragma unroll
  for (int j = 0; j < 2; ++j) {
    const int rr = 8 * j + (lane >> 3);
    const int tb = kind == 0 ? rr : (kind == 1 ? 16 * ((rr >> 1) & 1) + 8 * ((rr >> 2) & 1) + 4 * (rr >> 3) + 2 * (rr & 1) : 8 * (rr >> 2) + (rr & 3));
    vx[j] = tb * DM * 2 + ((lane & 7) ^ (rr >> 1)) * 16;
  }
#pragma unroll
  for (int j = 0; j < 4; ++j) { const int row = (w * 4 + j) * 4 + g; vw[j] = row * HD * 2 + (r ^ swz_b(row)) * 16; }
  const u64 wo = kind ? bg_order(1) : bg_order(0);
  auto xo = [&](int q) { return ((int)((wo >> (16 * q + 4 * w)) & 15) ^ (h & RHO)) * 128; };
  unsigned dT = (kind == 0 ? 16 : (kind == 1 ? 1 : 4)) * DM * 2;
  asm volatile("s_nop 4" : "+s"(dT));
  const unsigned vx0 = ((w & 1) ? vx[1] : vx[0]) + (w >> 1) * dT;
  auto st_w = [&](int n) {
#pragma unroll
    for (int j = 0; j < 4; ++j) dma16(rw, vw[j], (64 * ((int)((wo >> (4 * n)) & 15) ^ (h & RHO)) * HD + 128 * h) * 2, lds0 + (n & 7) * 16384 + j * 1024, W_NT & 16);
    dma16(rx, vx0, ((int)((wo >> (4 * n)) & 15) ^ (h & RHO)) * 128, lds0 - 3072 * w + 131072 + (n & 7) * 4096);
  };
  auto st_x = [&](int s, int b, int q, int j, bool pad) { dma16(rx, vx[j & 1], s * 65536 + (j >> 1) * dT + xo(q), lds0 + (b * 4 + q) * 16384 + j * 1024, false, pad); };
  if (KQ_D && kind < 2) while (__builtin_amdgcn_s_memrealtime() - G.entry < KQ_D) __builtin_amdgcn_s_sleep(8);
  __builtin_amdgcn_s_waitcnt(0x0F70);
  u32x4 W[32][2], gv;
  char* xb[3][2];
#pragma unroll
  for (int i = 0; i < 6; ++i) xb[i >> 1][i & 1] = lds_base(smem, (i >> 1) * 65536 + (r >> 3) * 1024 + (r & 7) * 128 + ((4 * (i & 1) + g) ^ (r >> 1)) * 16);
  f32x4 acc[2][4];
#pragma unroll
  for (int i = 0; i < 4; ++i) acc[1][i] = vzero();
#pragma unroll
  for (int n = 0; n < WAH; ++n) st_w(n);
#pragma unroll
  for (int n = 0; n < 16; ++n) {
    asm volatile("s_waitcnt vmcnt(%0)" :: "n"(4 * WAH - 4 + (n < 16 - WAH ? WAH - 1 : 15 - n)) : "memory");
    lds_barrier();
    u32x4 f[4], y[4];
#pragma unroll
    for (int i = 0; i < 4; ++i) {
#pragma unroll
      for (int q = 0; q < 2; ++q) {
        const int row = (i & 1) * 32 + 8 * g + 4 * q + (r >> 2);
        const u32x2 u = __builtin_bit_cast(u32x2, __builtin_amdgcn_ds_read_tr16_b64_v4i16((LAS bf16x4*)(smem + (n & 7) * 16384 + row * 256 + (((4 * w + (r & 3)) ^ swz_b(row)) * 16) + 8 * (i >> 1))));
        f[i][2 * q] = u[0]; f[i][2 * q + 1] = u[1];
      }
      y[i] = *(const u32x4*)(xb[2][i & 1] + (n & 7) * 4096 + (i >> 1) * 2048);
    }
    SB0;
    if (n == 16 - WAH) G.ask(a.sync, gv);
    if (n == 15) asm volatile("" : "+v"(gv));
    if (n + WAH < 16) st_w(n + WAH);
    else {
#pragma unroll
      for (int j = 0; j < 4; ++j) st_x(1 + ((n + WAH - 16) >> 2), (n + WAH - 16) >> 2, (n + WAH) & 3, j, true);
    }
#pragma unroll
    for (int i = 0; i < 8; ++i)
      asm volatile("s_nop 1\n\tv_mfma_f32_16x16x32_bf16 %0, %1, %2, %0" : "+v"(acc[1][i & 3]) : "v"(f[2 * (i & 1) + (i >> 2)]), "v"(y[(i & 2) + (i >> 2)]));
#pragma unroll
    for (int i = 0; i < 4; ++i) { asm volatile("" : "+a"(f[i])); W[2 * n + (i & 1)][i >> 1] = f[i]; }
  }
  if (!G.pass(a.sync, gv)) { bg_drain(); return; }
  SSTAMP(160 + h);
  const Sel sel = G.sel;
  const __amdgpu_buffer_rsrc_t ru = mk_rsrc(a.sync, SY_BYTES);
  const unsigned sgo = (w | lane) ? 0x7fffffffu : SY_CT + (CT_Q + 32 * kind + h) * 128;
  const u32x4 rx2 = own_rsrc(a.X, 4u << 20);
  unsigned vq[4][4];
#pragma unroll
  for (int i = 0; i < 16; ++i) { vq[i >> 2][i & 3] = vx[i & 1] + ((i >> 1) & 1) * dT + xo(i >> 2); asm volatile("" : "+v"(vq[i >> 2][i & 3])); }
  u32x4 xf[4][2], o;
  auto rd = [&](int b, int k, int T) { xf[k & 3][T] = *(const u32x4*)(xb[b][k & 1] + (k >> 1) * 4096 + T * 2048); };
  auto signal = [&](int s) {
    st4(ru, sgo, s);
    if (w == 0 && lane == 0 && s >= 0 && (s < 5 || (s & 7) == 0 || (s & 15) == 2 || s > 61)) {
    }
  };
#pragma unroll
  for (int T = 0; T < 2; ++T) st16s(pk8v(acc[1][2 * T], acc[1][2 * T + 1]), ru, lane * 16 + T * 4096, x_slot(kind, 0, h, sel) + w * 1024);
  bg_drain();
  signal(0);
#pragma unroll
  for (int i = 4 * WAH; i < 28; ++i) st_x(1 + (i >> 4), i >> 4, (i >> 2) & 3, i & 3, true);
#pragma unroll
  for (int i = 0; i < 6; ++i) rd(0, i >> 1, i & 1);
  WST(kind == 2, 18);
  auto seg = [&](int s, int b) {
    const unsigned so = x_slot(kind, s - 1, h, sel) + w * 1024;
    WST(kind == 2 && s > 0 && s < 4, 18 + s);
#pragma unroll
    for (int k = 0; k < 32; ++k)
#pragma unroll
      for (int i = 0; i < 4; ++i) {
        if (k == 0) asm volatile("v_mfma_f32_16x16x32_bf16 %0, %1, %2, 0" : "=&v"(acc[b][i]) : "a"(W[k][i & 1]), "v"(xf[k & 3][i >> 1]));
        else asm volatile("v_mfma_f32_16x16x32_bf16 %0, %1, %2, %0" : "+v"(acc[b][i]) : "a"(W[k][i & 1]), "v"(xf[k & 3][i >> 1]));
        SB0;
        const int q = k >> 3, n = 4 * k + i;
        if ((k & 7) == 5 && i == 0) {
          if (q == SIGQ) wait_vm<4 * SIGQ>(); else wait_vm<20>();
          if (q == 1 && __builtin_expect(s - 2u < W_NOW, 0)) { wait_vm<4>(); __builtin_amdgcn_s_barrier(); signal(s - 1); }
          else __builtin_amdgcn_s_barrier();
          asm volatile("" ::: "memory");
        }
        if (i == 1 || i == 2) rd(k + 3 < 32 ? b : b ^ 1, (k + 3) & 31, i - 1);
        {
          const int m = (n + 128 - 23) & 127, j = (m & 31) / DSP, q0 = m >> 5;
          if (j < 4 && (m & 31) % DSP == 0) asm volatile("s_add_i32 m0, %0, %1" :: "s"(lds0), "n"(((b ^ (q0 == 0) ^ (n < 23)) * 4 + ((q0 + 3) & 3)) * 16384 + j * 1024) : "m0", "scc");
          if (j < 4 && (m & 31) % DSP == 1)
            asm volatile("buffer_load_dwordx4 %0, %1, %2 offen lds" :: "v"(vq[(q0 + 3) & 3][j & 3]), "s"(rx2), "s"((s + 2 - (q0 == 0) - (n < 23)) * 65536) : "memory");
        }
        if (k == 8 * SIGQ + 5 && i == 2) signal(s - 1);
        if (k >= 1 && k < 5 && (i == 0 || i == 3)) { const f32x4 x = acc[b ^ 1][k - 1]; o[2 * (~k & 1) + (i & 1)] = pk2v(x[2 * (i & 1)], x[2 * (i & 1) + 1]); }
        if ((k == 2 || k == 4) && i == 3) { __builtin_amdgcn_raw_buffer_store_b128(o, ru, (unsigned)(lane * 16 + (k >> 2) * 4096), so, ST_AUX); asm volatile("s_nop 0" :: "v"(o)); }
        SB0;
      }
  };
  for (int s = 1;; s += 2) {
    seg(s, 0);
    if (s == NSEG - 1) break;
    seg(s + 1, 1);
  }
#pragma unroll
  for (int T = 0; T < 2; ++T) st16s(pk8v(acc[0][2 * T], acc[0][2 * T + 1]), ru, lane * 16 + T * 4096, x_slot(kind, NSEG - 1, h, sel) + w * 1024);
  bg_drain();
  signal(NSEG - 1);
}
__device__ __forceinline__ float blo(unsigned d) { return __uint_as_float(d << 16); }
__device__ __forceinline__ float bhi(unsigned d) { return __uint_as_float(d & 0xffff0000u); }
constexpr int A2_CSTR = 4160;
constexpr int A2_UOFF = 8 * A2_CSTR + 16 * 80;
constexpr int A2_WLDS = A2_UOFF + 64 * 80;
static_assert(4 * A2_WLDS + 256 <= CHAIN_LDS - 16, "lds");
template <class F>
__device__ __forceinline__ void a2_wave(const ChainArgs& a, int d0, int d1, char* smem, int w, F hook) {
  const int lane = opaque_tid(0);
  char* Wb = smem + w * A2_WLDS;
  char* U = Wb + A2_UOFF;
  const __amdgpu_buffer_rsrc_t rf = mk_rsrc((void*)a.Assm, 4u << 20);
  const __amdgpu_buffer_rsrc_t rkv = mk_rsrc((void*)a.kvT, 4u << 20);
  u32x4 hi[4], lo[4], uv[4];
  auto ld_h = [&](int d) {
#pragma unroll
    for (int i = 0; i < 4; ++i) {
      hi[i] = __builtin_amdgcn_raw_buffer_load_b128(rf, (unsigned)((255 - 64 * i - lane) * 16), (unsigned)(d * 4096), 2);
      lo[i] = __builtin_amdgcn_raw_buffer_load_b128(rf, (unsigned)(max(254 - 64 * i - lane, 0) * 16), (unsigned)(d * 4096), 2);
    }
  };
  auto ld_u = [&](int d) {
#pragma unroll
    for (int i = 0; i < 4; ++i) uv[i] = __builtin_amdgcn_raw_buffer_load_b128(rkv, (unsigned)((i * 64 + lane) * 16), (unsigned)(d * 4096), LD_AUX);
  };
  {
    GAS unsigned* abortw = (GAS unsigned*)(a.sync + SY_ABORT);
    bool dead = false;
    ctr_wait<BG_SLP>(ctr(a, CT_A1 + (min(d0, D_A - 1) >> 7)), d0 < d1 ? 8u : 0u, abortw, lane, dead);
  }
  ld_h(d0); ld_u(d0);
  if (lane < 24) *(f32x4*)(Wb + (lane / 3) * A2_CSTR + (256 + lane % 3) * 16) = vzero();
#pragma unroll
  for (int i = 0; i < 2; ++i) if (64 * i + lane < 80) *(f32x4*)(U - 16 * 80 + (64 * i + lane) * 16) = vzero();
  const int r = lane & 15, g = lane >> 4;
#pragma unroll 1
  for (int d = d0; d < d1; ++d) {
#pragma unroll
    for (int i = 0; i < 4; ++i) {
      u32x4 l = lo[i];
      if (i == 3 && lane == 63) l = (u32x4){0u, 0u, 0u, 0u};
      const unsigned src[8] = {l[0], l[1], l[2], l[3], hi[i][0], hi[i][1], hi[i][2], hi[i][3]};
      unsigned f[15];
#pragma unroll
      for (int x = 0; x < 15; ++x) {
        if ((x & 1) == 0) f[x] = __builtin_amdgcn_alignbit(src[7 - x / 2], src[7 - x / 2], 16);
        else f[x] = __builtin_amdgcn_perm(src[7 - (x + 1) / 2], src[7 - (x - 1) / 2], 0x07060100u);
      }
#pragma unroll
      for (int m = 0; m < 8; ++m) *(u32x4*)(Wb + m * A2_CSTR + (64 * i + lane) * 16) = (u32x4){f[7 - m], f[9 - m], f[11 - m], f[13 - m]};
    }
#pragma unroll
    for (int i = 0; i < 4; ++i) {
      const int ch = i * 64 + lane;
      *(u32x4*)(U + (ch >> 2) * 80 + (ch & 3) * 16) = uv[i];
    }
    const float Dd = bf2f(a.AD[d]);
    asm volatile("" ::: "memory");
    ld_h(min(d + 1, d1 - 1)); ld_u(min(d + 1, d1 - 1));
    hook(0, d + 1 == d1);
    f32x4 acc[4][2];
#pragma unroll
    for (int i = 0; i < 8; ++i) acc[i >> 1][i & 1] = vzero();
    const char* pa = Wb + (r & 7) * A2_CSTR + 2 * (2040 - (r & 8) + 8 * g) - 3 * 1024 - 32 - 64;
    const char* pb = U + (r - 1) * 80 + 16 * g;
    bf16x8 fa[2][8], fb[2][4];
    auto rd = [&](int b, int n, int k) {
      if (n < 4) fb[b][n] = *(const bf16x8*)(pb + 80 * (16 * n + 1 - k));
      else fa[b][n - 4] = *(const bf16x8*)(pa + 1024 * (3 - ((n - 4) >> 1)) + 32 * (1 - (n & 1)) + 64 * (1 - k));
    };
#pragma unroll
    for (int n = 0; n < 12; ++n) rd(0, n, 0);
#pragma unroll 1
    for (int k = 0; k < 16; k += 2) {
#pragma unroll
      for (int b = 0; b < 2; ++b) {
        int q = 0;
#pragma unroll
        for (int j = 0; j < 4; ++j)
#pragma unroll
          for (int i = j; i < 4; ++i)
#pragma unroll
            for (int mt = 0; mt < 2; ++mt) {
              asm volatile("v_mfma_f32_16x16x32_bf16 %0, %1, %2, %0" : "+v"(acc[i][mt]) : "v"(fa[b][2 * j + mt]), "v"(fb[b][i - j]));
              SB0;
              if (q < 12) rd(b ^ 1, q, 1);
              if (q == 12) { pa -= 64; pb -= 80; }
              ++q;
              SB0;
            }
      }
    }
    asm volatile("s_nop 7" ::: "memory");
#pragma unroll
    for (int i = 0; i < 8; ++i) asm volatile("" : "+v"(acc[i >> 1][i & 1]));
    hook(1, d + 1 == d1);
#pragma unroll
    for (int i = 0; i < 8; ++i) {
      char* pz = U + (16 * (i >> 1) + r) * 80 + 32 * (i & 1) + 8 * g;
      const u32x2 kvv = *(const u32x2*)pz;
      const f32x4 y = acc[i >> 1][i & 1];
      *(u32x2*)pz = (u32x2){pk2(fmaf(blo(kvv[0]), Dd, y[0]), fmaf(bhi(kvv[0]), Dd, y[1])), pk2(fmaf(blo(kvv[1]), Dd, y[2]), fmaf(bhi(kvv[1]), Dd, y[3]))};
    }
#pragma unroll
    for (int i = 0; i < 4; ++i) {
      const int ch = i * 64 + lane;
      st16s(*(const u32x4*)(U + (ch >> 2) * 80 + (ch & 3) * 16), rkv, (unsigned)(ch * 16), (unsigned)(d * 4096));
    }
  }
  wait_vm<0>();
  if (lane == 0 && d0 < d1) ctr_add(a, CT_A2 + (d0 >> 7), d1 - d0);
}
__device__ __forceinline__ void task_u(const ChainArgs& a, int h, char* smem, int wv, Gate& G) {
  const int lane = opaque_tid(0);
  const int w = wv;
  GAS unsigned* abortw = (GAS unsigned*)(a.sync + SY_ABORT);
  bool dead = false;
  char* sb = smem + w * 32768;
  const __amdgpu_buffer_rsrc_t rwo = mk_rsrc(a.Wo, 8u << 20);
  const unsigned lds0 = (unsigned)(uintptr_t)(LAS char*)sb;
  unsigned wo4[4];
#pragma unroll
  for (int i = 0; i < 4; ++i) { const int row = i * 4 + (lane >> 4); wo4[i] = row * DM * 2 + (((lane & 15) ^ swz_b(row)) * 16); }
  asm volatile("s_nop 4");
  auto stage = [&](int n) {
    const int p = (U_ORD >> (4 * n)) & 3;
#pragma unroll
    for (int i = 0; i < 16; ++i)
      dma16(rwo, wo4[i & 3], (unsigned)(((128 * h + 64 * (p & 1) + 16 * (i >> 2)) * DM + 256 * w + 128 * (p >> 1)) * 2), lds0 + (n & 1) * 16384 + i * 1024, W_NT & 2);
  };
  __builtin_amdgcn_s_waitcnt(0x0F70);
  u32x4 W[16][4];
  stage(0); stage(1);
#pragma unroll
  for (int n = 0; n < 4; ++n) {
    const int p = (U_ORD >> (4 * n)) & 3;
    if (n < 3) wait_vm<16>(); else wait_vm<0>();
#pragma unroll
    for (int t = 0; t < 8; ++t) {
      const u32x8 f = tr_frag(sb + (n & 1) * 16384, t, lane);
      u32x4 f0 = {f[0], f[1], f[2], f[3]}, f1 = {f[4], f[5], f[6], f[7]};
      asm volatile("" : "+a"(f0), "+a"(f1));
      W[8 * (p >> 1) + t][2 * (p & 1)] = f0; W[8 * (p >> 1) + t][2 * (p & 1) + 1] = f1;
    }
    asm volatile("s_waitcnt lgkmcnt(0)" ::: "memory");
    if (n + 2 < 4) stage(n + 2);
  }
  lds_barrier();
  if (!G.pass(a.sync)) return;
  SSTAMP(192 + h);
  const Sel sel = G.sel;
  const u32x4 ru = own_rsrc(a.sync, SY_BYTES);
  u32x4 V[2][8], pc[2], o[2];
  f32x4 c[2][2];
  unsigned cv, cw, vo[4], lk[2], sgo = (w | lane) ? 0x7fffffffu : SY_CT + (CT_U + h) * 128, nd = (lane & 1) ? 5 - URING : 2;
#pragma unroll
  for (int i = 0; i < 4; ++i) { vo[i] = lane * 16 + ux_tile(16 * w + 4 * i); asm volatile("" : "+v"(vo[i])); }
#pragma unroll
  for (int i = 0; i < 2; ++i) lk[i] = SY_CT + ((lane & 1) ? CT_DONE + 8 * i + (h & 7) : CT_V + h) * 128;
  char* vl = lds_base(smem, lane * 16);
  const unsigned vl2 = lane * 16 + w * 2048;
  auto ld_v = [&](int s) {
    asm volatile("buffer_load_dwordx4 %0, %2, %3, %4 offen sc1\n\tbuffer_load_dwordx4 %1, %2, %3, %4 offen offset:1024 sc1" : "=&v"(pc[0]), "=&v"(pc[1]) : "v"(vl2), "s"(ru), "s"(x_slot(2, s, h, sel)) : "memory");
  };
  auto signal = [&](int s) {
    asm volatile("buffer_store_dword %0, %1, %2, 0 offen sc1" :: "v"(s), "v"(sgo), "s"(ru) : "memory");
  };
  auto st_u = [&](int n, unsigned ub) { asm volatile("buffer_store_dwordx4 %0, %1, %2, %3 offen offset:%4 sc1" :: "v"(o[n & 1]), "v"(vo[n >> 2]), "s"(ru), "s"(ub), "n"((n & 3) * 1024) : "memory"); };
  auto fetch = [&](int s) {
    ctr_wait<2>(ctr(a, CT_V + h), s + 1, abortw, lane, dead);
    WST(s == 0, 24);
    ld_v(s);
    asm volatile("s_waitcnt vmcnt(0)" : "+v"(pc[0]), "+v"(pc[1]) :: "memory");
#pragma unroll
    for (int i = 0; i < 2; ++i) *(u32x4*)(vl + 16384 + w * 2048 + i * 1024) = pc[i];
    lds_barrier();
#pragma unroll
    for (int i = 0; i < 8; ++i) V[s][i] = *(const u32x4*)(vl + 16384 + i * 1024);
  };
  fetch(0);
  u64 bad = 0;
  unsigned up = 0x7f000000u;
  c[1][0] = c[1][1] = vzero();
  auto seg = [&](int s, int b) {
    const unsigned ub = ux_base(s, h, sel);
    WST(s < 4, 25 + s);
#pragma unroll
    for (int n = 0; n < 16; ++n)
#pragma unroll
      for (int q = 0; q < 8; ++q) {
        const int kp = q >> 1, m = q & 1;
        if (kp == 0) asm volatile("v_mfma_f32_16x16x32_bf16 %0, %1, %2, 0" : "=&v"(c[n & 1][m]) : "v"(V[b][4 * m + kp]), "a"(W[n][kp]));
        else asm volatile("v_mfma_f32_16x16x32_bf16 %0, %1, %2, %0" : "+v"(c[n & 1][m]) : "v"(V[b][4 * m + kp]), "a"(W[n][kp]));
        SB0;
        if (q >= 2 && q < 6) { const f32x4 x = c[~n & 1][(q - 2) >> 1]; o[~n & 1][q - 2] = pk2v(x[2 * (q & 1)], x[2 * (q & 1) + 1]); }
        if (q == 6) st_u((n - 1) & 15, n ? ub : up);
        if (q == 5) asm volatile("" :: "v"(o[n & 1]));
        if (n == U_LD && q == 0) {
          if (__builtin_expect(bad != 0, 0)) {
            if (s <= U_NOW) {
              asm volatile("s_waitcnt vmcnt(%1)" : "+v"(cw) : "n"(U_LD - 2) : "memory");
              __builtin_amdgcn_s_barrier();
              signal(s - 1);
              bad = (int)__builtin_amdgcn_readfirstlane(cw) <= s;
            }
            if (bad & 1) ctr_wait<2>(ctr(a, CT_V + h), min(s + 2, NSEG), abortw, lane, dead);
            if (bad & 2) ctr_wait<8>(ctr(a, CT_DONE + 8 * b + (h & 7)), max(s + 5 - URING, 0), abortw, lane, dead);
          }
          ld_v(min(s + 1, NSEG - 1));
        }
        if (n == 1 && q == 7) asm volatile("buffer_load_dword %0, %1, %2, 0 offen sc1" : "=&v"(cw) : "v"(s <= U_NOW ? lk[0] : 0x7fffffffu), "s"(ru) : "memory");
        if (n == U_LD && q == 7) asm volatile("buffer_load_dword %0, %1, %2, 0 offen sc1" : "=&v"(cv) : "v"(lk[b ^ 1]), "s"(ru) : "memory");
        if (n == U_SIG2 && q == 0) asm volatile("s_waitcnt vmcnt(%4)" : "+v"(pc[0]), "+v"(pc[1]), "+v"(cv), "+v"(cw) : "n"(U_SIG2 - U_LD - 1) : "memory");
        if (n == U_SIG2 && (q == 1 || q == 7)) *(u32x4*)(vl + (b ^ 1) * 8192 + w * 2048 + (q >> 2) * 1024) = pc[q >> 2];
        if (n == U_SIG2 + 1 && q == 0) { bad = __builtin_amdgcn_ballot_w64((int)cv < (s < U_NOW && lane > 1 ? NSEG : min(s + (int)nd, NSEG - 1))); asm volatile("" : "+s"(bad)); }
        if (n == U_SIG2 + 1 && q == 7) lds_barrier();
        if (n == U_SIG2 + 2 && q == 0) signal(s - 1);
        if ((n == U_SIG2 + 2 || n == U_SIG2 + 3) && (q & 1)) V[b ^ 1][4 * (n - U_SIG2 - 2) + (q >> 1)] = *(const u32x4*)(vl + (b ^ 1) * 8192 + (4 * (n - U_SIG2 - 2) + (q >> 1)) * 1024);
        SB0;
      }
    up = ub;
  };
  for (int s = 0; s < NSEG; s += 2) {
    seg(s, 0);
    if (__builtin_expect(s == 0, 0)) {
      o[1] = pk8v(c[1][0], c[1][1]);
      st_u(15, up);
      bg_drain();
      signal(0);
      fetch(1);
    }
    seg(s + 1, 1);
  }
  o[1] = pk8v(c[1][0], c[1][1]);
  st_u(15, up);
  bg_drain();
  signal(NSEG - 1);
}

__device__ __forceinline__ void task_a1(const ChainArgs& a, int dp, int tq, char* smem, int wv, Pick& pk) {
  const int lane = opaque_tid(0);
  const int dg = 2 * dp + (wv & 1), t0 = tq * 256 + (wv >> 1) * 128;
  const int d = dg * 64 + 8 * (lane & 7);
  const int tb = t0 + 8 * (lane >> 3);
  char* T = smem + wv * 16384;
  u32x4 kk[2][11], vv[2][8], sh[4];
#pragma unroll
  for (int i = 0; i < 4; ++i) sh[i] = *(const u32x4*)(a.Ashift + d * 4 + 8 * i);
  SB0;
#pragma unroll
  for (int gq = 0; gq < 2; ++gq) {
#pragma unroll
    for (int i = 0; i < 11; ++i) {
      const int t = tb + 64 * gq - 3 + i;
      kk[gq][i] = LDNT((const u32x4*)(a.Ak + (long)(t < 0 ? 0 : t) * D_A + d));
    }
#pragma unroll
    for (int i = 0; i < 8; ++i) vv[gq][i] = LDNT((const u32x4*)(a.Av + (long)(tb + 64 * gq + i) * D_A + d));
    SB0;
  }
  pk.look();
#pragma unroll
  for (int i = 0; i < 3; ++i) if (tb == 0) kk[0][i] = (u32x4){0u, 0u, 0u, 0u};
#pragma unroll
  for (int gq = 0; gq < 2; ++gq) {
#pragma unroll
    for (int c = 0; c < 8; ++c) {
      const unsigned t01 = sh[c >> 1][2 * (c & 1)], t23 = sh[c >> 1][2 * (c & 1) + 1];
      const float s0 = blo(t01), s1 = bhi(t01), s2 = blo(t23), s3 = bhi(t23);
      float kf[11];
#pragma unroll
      for (int i = 0; i < 11; ++i) kf[i] = (c & 1) ? bhi(kk[gq][i][c >> 1]) : blo(kk[gq][i][c >> 1]);
      u32x4 o;
#pragma unroll
      for (int i = 0; i < 8; i += 2) {
        float r[2];
#pragma unroll
        for (int j = 0; j < 2; ++j)
          r[j] = fmaf(kf[i + j + 3], s0, fmaf(kf[i + j + 2], s1, fmaf(kf[i + j + 1], s2, kf[i + j] * s3))) * ((c & 1) ? bhi(vv[gq][i + j][c >> 1]) : blo(vv[gq][i + j][c >> 1]));
        o[i >> 1] = pk2(r[0], r[1]);
      }
      *(u32x4*)(T + (8 * (lane & 7) + c) * 256 + (((gq * 8 + (lane >> 3)) ^ (lane & 7)) * 16)) = o;
    }
    SB0;
  }
  pk.claim();
  const __amdgpu_buffer_rsrc_t rkv = mk_rsrc((void*)a.kvT, 4u << 20);
#pragma unroll
  for (int i = 0; i < 16; ++i) {
    const int row = 4 * i + (lane >> 4);
    const u32x4 o = *(const u32x4*)(T + row * 256 + (((lane & 15) ^ (row >> 3)) * 16));
    st16(rkv, (unsigned)(((dg * 64 + row) * S_LEN + t0 + (lane & 15) * 8) * 2), o);
  }
  bg_drain();
  if (opaque_tid(wv) == 0) ctr_add(a, CT_A1 + dp, 1u);
}

__device__ __forceinline__ void task_a3(const ChainArgs& a, int dp, int tq, char* smem, int wv, Pick& pk) {
  const int lane = opaque_tid(0);
  const int dg = 2 * dp + (wv & 1), t0 = tq * 256 + (wv >> 1) * 128;
  const int d = dg * 64 + 8 * (lane & 7);
  const int tb = t0 + 8 * (lane >> 3);
  const __amdgpu_buffer_rsrc_t rz = mk_rsrc((void*)a.kvT, 4u << 20);
  u32x4 qq[16], zz[8][2];
#pragma unroll
  for (int i = 0; i < 16; ++i) qq[i] = LDNT((const u32x4*)(a.Aq + (long)(tb + 64 * (i >> 3) + (i & 7)) * D_A + d));
  char* T = smem + wv * 16384;
  {
    u32x4 zr[16];
    pk.look();
#pragma unroll
    for (int i = 0; i < 16; ++i) zr[i] = ld16(rz, (unsigned)(((dg * 64 + 4 * i + (lane >> 4)) * S_LEN + t0 + (lane & 15) * 8) * 2));
#pragma unroll
    for (int i = 0; i < 16; ++i) {
      const int row = 4 * i + (lane >> 4);
      *(u32x4*)(T + row * 256 + (((lane & 15) ^ (row >> 3)) * 16)) = zr[i];
    }
  }
  pk.claim();
#pragma unroll
  for (int c = 0; c < 8; ++c)
#pragma unroll
    for (int gq = 0; gq < 2; ++gq) zz[c][gq] = *(const u32x4*)(T + (8 * (lane & 7) + c) * 256 + (((gq * 8 + (lane >> 3)) ^ (lane & 7)) * 16));
#pragma unroll
  for (int i = 0; i < 16; ++i) {
    u32x4 o;
#pragma unroll
    for (int c = 0; c < 8; c += 2) {
      const unsigned z0 = zz[c][i >> 3][(i >> 1) & 3], z1 = zz[c + 1][i >> 3][(i >> 1) & 3];
      o[c >> 1] = pk2(blo(qq[i][c >> 1]) * ((i & 1) ? bhi(z0) : blo(z0)), bhi(qq[i][c >> 1]) * ((i & 1) ? bhi(z1) : blo(z1)));
    }
    st16g(a.out + (long)(tb + 64 * (i >> 3) + (i & 7)) * OUTW + d, o);
  }
}

__device__ __forceinline__ void role_bg(const ChainArgs& a, char* smem, const int x_, int wv, Gate& G) {
  Pick pk{a, x_, wv, 0u, 0u, -1, 0};
  while (true) {
    asm volatile("" : "+s"(wv));
    pk.wv = wv;
    int toff = CHAIN_LDS - 16;
    asm volatile("" : "+v"(toff));
    volatile LAS int* tslot = (volatile LAS int*)(smem + toff);
    if (wv == 0) *tslot = (int)pk.next();
    __syncthreads();
    int t = __builtin_amdgcn_readfirstlane(*tslot);
    __syncthreads();
    if (t < 0) break;
    const int x = (t >> 20) & 7, list = (t >> 16) & 7;
    t &= 0xffff;
    if (list) task_a3(a, x, t, smem, wv, pk);
    else if (t < 8 * (int)Pick::A4) task_a1(a, x, t / Pick::A4, smem, wv, pk);
    else {
      const int arg = t - 8 * Pick::A4;
      pk.v = pk.o = 0;
      a2_wave(a, 128 * x + arg + A2_NCH * wv, 128 * x + min(arg + A2_NCH * (wv + 1), 128), smem, wv, [&](int k, bool last) { if (k == 0) pk.look(); else { pk.o = 0; if (last) pk.claim(); } });
    }
  }
}

__global__ __launch_bounds__(256, 1) void chain_kernel(ChainArgs a) {
  extern __shared__ __attribute__((aligned(16))) char smem[];
  const int b = blockIdx.x;
  {
    u32x4 rc;
    asm volatile("s_getpc_b64 s[60:61]\n\t.Lchain_here:\n\ts_sub_u32 s60, s60, .Lchain_here-_Z12chain_kernel9ChainArgs\n\ts_subb_u32 s61, s61, 0\n\ts_and_b32 s61, s61, 0xffff\n\t"
                 "s_mov_b32 s62, .Lchain_end-_Z12chain_kernel9ChainArgs\n\ts_mov_b32 s63, 0x20000\n\t"
                 "buffer_load_dwordx4 a[252:255], %1, s[60:63], 0 offen\n\tbuffer_load_dwordx4 a[252:255], %1, s[60:63], s63 offen"
                 : "={s[60:63]}"(rc) : "v"((b >> 3) * 4096 + threadIdx.x * 16) : "scc", "a252", "a253", "a254", "a255");
  }
  int w = __builtin_amdgcn_readfirstlane(threadIdx.x >> 6);
  Gate G;
  G.entry = __builtin_amdgcn_s_memrealtime(); G.open = false; G.arr = w == 0 ? b & 7 : -1;
  G.rot = ((unsigned)__builtin_amdgcn_s_getreg(20 | (3 << 11)) - b + WRONG) & 7u;
  {
    volatile LAS unsigned* t = (volatile LAS unsigned*)smem;
    if (w == 0) { t[0] = (unsigned)G.entry; t[1] = (unsigned)(G.entry >> 32); }
    __syncthreads();
    G.entry = (u64)(unsigned)__builtin_amdgcn_readfirstlane(t[0]) | ((u64)(unsigned)__builtin_amdgcn_readfirstlane(t[1]) << 32);
    __syncthreads();
  }
  if (((b + G.rot) & 4) == 0 && b < 128) {
    const int c = (b >> 6) * 32 + 8 * ((b + G.rot) & 3) + ((b >> 3) & 7);
    if (w < 2) calibrate(a, w ? SY_HOT / PAGE : c, 2 * c + w, G.entry);
    __builtin_amdgcn_s_barrier();
  }
  if (b == 0) {
    const int lane = opaque_tid(0);
    const __amdgpu_buffer_rsrc_t rs = mk_rsrc(a.sync + SY_ABORT, SY_CT - SY_ABORT + CT_CAL * 128);
    st16(rs, w * 8192 + lane * 128, (u32x4){SENT, SENT, SENT, SENT});
    static_assert(SY_CT - SY_ABORT + CT_CAL * 128 <= 32768, "one store per wave");
    if (w == 3) {
      u32x4 v;
      bool ok;
      do {
        v = ld16(mk_rsrc(a.sync + SY_CT + CT_CAL * 128, 8192), lane * 128);
        ok = ((u64)v[1] << 32 | v[0]) - G.entry + 2000u < (1ull << 31) && ((u64)v[3] << 32 | v[2]) - G.entry + 2000u < (1ull << 31);
      } while (!__all(ok) && __builtin_amdgcn_s_memrealtime() - G.entry < 2 * CALM);
      if (lane == SY_HOT / PAGE) v[0] = 0;
      const u64 m = __builtin_amdgcn_ballot_w64(ok && (v[0] & 1u)) | (u64)(__builtin_popcountll(__builtin_amdgcn_ballot_w64(ok && (v[2] & 1u))) > 32) << (SY_HOT / PAGE);
      if (lane == 0) __hip_atomic_store((GAS u64*)(a.sync + SY_SEL), m, __ATOMIC_RELAXED, __HIP_MEMORY_SCOPE_AGENT);
    }
    bg_drain();
    if (w == 0 && lane == 0) __hip_atomic_store((GAS u64*)(a.sync + SY_EPOCH), (__builtin_amdgcn_s_memrealtime() & ~7ull) | G.rot, __ATOMIC_RELAXED, __HIP_MEMORY_SCOPE_AGENT);
  }
  while (true) {
    const unsigned rot = G.rot;
    asm volatile("" : "+s"(w));
    const int x = (b + rot) & 7, j = b >> 3, h = 8 * (x & 3) + (j & 7), role = (x & 4) + (j >> 3);
    if ((role == 0 || role == 7) && w >= 2) {
      const int n = opaque_tid(w - 2);
      const unsigned x0 = __builtin_amdgcn_raw_buffer_load_b32(mk_rsrc(a.sync, 134u << 20), n * PAGE + 4096 + (j & 31) * 128, 0, 2);
      const unsigned x1 = __builtin_amdgcn_raw_buffer_load_b32(mk_rsrc(a.out, 8u << 20), n * PAGE + (j & 31) * 128, 0, 2);
      asm volatile("" :: "v"(x0), "v"(x1));
    }
    if (role < 2) role_cb(a, 32 * role + h, smem, w, G);
    else if (role == 2) role_qk(a, h, smem, w, G);
    else if (role == 3) role_vo(a, h, smem, w, G);
    else if (role == 4) task_u(a, h, smem, w, G);
    else task_w(a, 7 - role, h, smem, w, G);
    if (G.rot == rot) role_bg(a, smem, 2 * (x & 3) + (j & 1), w, G);
    if (G.rot == rot) break;
    __syncthreads();
  }
  asm volatile(".Lchain_end:");
}

extern "C" void kernel_launch(void* const* d_in, const int* in_sizes, int n_in,
                              void* d_out, int out_size, void* d_ws, size_t ws_size,
                              hipStream_t stream) {
  const u16* A_q = (const u16*)d_in[0];
  const u16* A_k = (const u16*)d_in[1];
  const u16* A_v = (const u16*)d_in[2];
  const u16* A_shift = (const u16*)d_in[3];
  const u16* A_ssmk = (const u16*)d_in[4];
  const u16* A_D = (const u16*)d_in[5];
  const u16* B_x = (const u16*)d_in[6];
  const u16* B_mem0 = (const u16*)d_in[7];
  const u16* B_Wq = (const u16*)d_in[8];
  const u16* B_Wk = (const u16*)d_in[9];
  const u16* B_Wv = (const u16*)d_in[10];
  const u16* B_Wo = (const u16*)d_in[11];
  u16* out = (u16*)d_out;
  char* ws = (char*)(((uintptr_t)d_ws + (2u << 20) - 1) & ~(uintptr_t)((2u << 20) - 1));
  u16* kvT = (u16*)(ws + OFF_KVT);
  hipFuncSetAttribute((const void*)chain_kernel, hipFuncAttributeMaxDynamicSharedMemorySize, CHAIN_LDS);
  ChainArgs ca;
  ca.prof = (u64*)((char*)d_ws + PROF_AT);
  ca.mem0 = B_mem0; ca.Wq = B_Wq; ca.Wk = B_Wk; ca.Wv = B_Wv; ca.Wo = B_Wo; ca.X = B_x;
  ca.out = out; ca.sync = ws;
  ca.Aq = A_q; ca.Ak = A_k; ca.Av = A_v; ca.Ashift = A_shift; ca.Assm = A_ssmk; ca.AD = A_D; ca.kvT = kvT;
  chain_kernel<<<256, 256, CHAIN_LDS, stream>>>(ca);
}
```

```cpp
#include <hip/hip_runtime.h>
#include <hip/hip_bf16.h>
#include <stdint.h>
#include <type_traits>

constexpr int S_LEN = 2048;
constexpr int D_A   = 1024;
constexpr int DM    = 1024;
constexpr int NH    = 32;
constexpr int DH    = 128;
constexpr int HD    = NH * DH;
constexpr int MEMR  = 8;
constexpr int SEG   = 32;
constexpr int NSEG  = S_LEN / SEG;
constexpr int OUTW  = 2048;

typedef unsigned short u16;
typedef unsigned long long u64;
typedef short bf16x8 __attribute__((ext_vector_type(8)));
typedef short bf16x4 __attribute__((ext_vector_type(4)));
typedef float f32x4 __attribute__((ext_vector_type(4)));
typedef float f32x16 __attribute__((ext_vector_type(16)));
typedef unsigned u32x4 __attribute__((ext_vector_type(4)));
typedef unsigned u32x2 __attribute__((ext_vector_type(2)));
#define LAS __attribute__((address_space(3)))
#define GAS __attribute__((address_space(1)))

constexpr size_t MB = 1u << 20;
constexpr size_t OFF_KVT = 130 * MB;
constexpr size_t PROF_AT = 0;

__device__ __forceinline__ float bf2f(u16 b) { return __uint_as_float(((unsigned)b) << 16); }
__device__ __forceinline__ u16 f2bf(float f) { __bf16 b = (__bf16)f; return __builtin_bit_cast(u16, b); }
__device__ __forceinline__ float rbf(float f) { return bf2f(f2bf(f)); }
__device__ __forceinline__ unsigned pk2(float a, float b) {
  typedef float f2 __attribute__((ext_vector_type(2)));
  typedef __bf16 b2 __attribute__((ext_vector_type(2)));
  f2 v = {a, b}; b2 r = __builtin_convertvector(v, b2); return __builtin_bit_cast(unsigned, r);
}

__device__ __forceinline__ int opaque_tid(int wv) {
  int l;
  asm volatile("v_mbcnt_lo_u32_b32 %0, -1, 0\n\tv_mbcnt_hi_u32_b32 %0, -1, %0" : "=v"(l));
  return wv * 64 + l;
}
__device__ __forceinline__ int swz_b(int row) { return ((row & 3) << 2) | ((row >> 2) & 3); }

#define KNOB(name, v) constexpr int name = v
KNOB(K_DEF, 72);
KNOB(W_STEP, 33); KNOB(W_OFF, 56);
#define RHOV 3
KNOB(RHO, RHOV);
#define CDUR 1600
#define DRY 1
#define U_SLP 8
#define U_STG 0
#define U_ST0 0
#define WSOFF 118
#define CALN 3
#define CALM 600u
#define CALT 700u
#define KQ_D 500
#define W_NOW 4
#define PF1 0
#define PF2 0
#define U_SIG2 11
#define U_LD 5
#define U_NOW 6
#define WRONG 0
#define OUTP "nt"
#define LDNT(p) __builtin_nontemporal_load(p)
constexpr int CHK      = 16384;
constexpr int PAGE     = 2 << 20;
constexpr int SY_UM    = 34 * PAGE;
constexpr int SY_HOT   = 32 * PAGE;
constexpr int H_MEM    = 0;
constexpr int H_P      = 16 * CHK;
constexpr int H_ANN    = 28 * CHK;
constexpr int H_DLT    = 32 * CHK;
constexpr int H_TR     = 33 * CHK;
constexpr int H_PX     = 64 * CHK;
constexpr int H_BYTES  = 128 * CHK;
constexpr int URING    = 16;
constexpr int SY_SET   = 64 * PAGE;
constexpr int SY_EPOCH = SY_SET + 128;
constexpr int SY_ABORT = SY_SET + 256;
constexpr int SY_SEL   = SY_EPOCH + 8;
constexpr int SY_CT    = SY_ABORT + 128;
constexpr int CT_ARR = 0, CT_INIT = 8, CT_TK = 9, CT_A1 = 13, CT_DONE = 17, CT_A2 = 33;
constexpr int CT_Q = 49, CT_K = CT_Q + 32, CT_V = CT_K + 32, CT_U = CT_V + 32, CT_CAL = CT_U + 32;
constexpr int SY_BYTES = SY_CT + (CT_CAL + 64) * 128;
constexpr unsigned SENT = 0xFFFFFFFFu;
constexpr unsigned SPIN_CAP = 1u << 19;
constexpr int NLF = 16;
constexpr int L_WTS = 0;
constexpr int L_MEM = L_WTS + 4 * NLF * 2048;
constexpr int L_XCH = L_MEM + 16384;
constexpr int L_XK  = L_XCH + 2048;
constexpr int L_KX  = L_XCH + 4096;
constexpr int L_PRF = L_KX + 8192;
constexpr int XRS = 40;
constexpr int CHAIN_LDS = 160 * 1024;
static_assert(L_XCH + 128 * XRS <= L_PRF && L_PRF + 4096 <= CHAIN_LDS, "lds");

typedef __bf16 bfv8 __attribute__((ext_vector_type(8)));
typedef __bf16 bfv16 __attribute__((ext_vector_type(16)));
typedef unsigned u32x8 __attribute__((ext_vector_type(8)));
typedef unsigned u32x12 __attribute__((ext_vector_type(12)));
typedef unsigned u32x16 __attribute__((ext_vector_type(16)));
typedef unsigned u32x32 __attribute__((ext_vector_type(32)));
struct Wts { u32x8 a[32]; u32x32 v[4]; };

#define STAMPL(i) do {} while (0)
#define PVAL(i, v) do {} while (0)
#define PDECL(base, str) do {} while (0)
#define PZERO do {} while (0)
#define PDUMP(mask) do {} while (0)
#define TA(x) do {} while (0)
#define TB(x) TS(x)
#define TS(x) do {} while (0)
#define SSTAMP(i) do {} while (0)
#define WST(c, i) do {} while (0)
#define AST(i) do {} while (0)
struct ChainArgs {
  u64* prof;
  const u16* mem0; const u16* Wq; const u16* Wk; const u16* Wv; const u16* Wo; const u16* X;
  u16* out; char* sync;
  const u16* Aq; const u16* Ak; const u16* Av; const u16* Ashift; const u16* Assm; const u16* AD; u16* kvT;
};
__device__ __forceinline__ GAS unsigned* ctr(const ChainArgs& a, int i) { return (GAS unsigned*)(a.sync + SY_CT + i * 128); }
__device__ __forceinline__ void ctr_add(const ChainArgs& a, int i, unsigned v) {
  __hip_atomic_fetch_add(ctr(a, i), v, __ATOMIC_RELAXED, __HIP_MEMORY_SCOPE_AGENT);
}

__device__ __forceinline__ __amdgpu_buffer_rsrc_t mk_rsrc(const void* p, unsigned bytes) {
  const u64 v = (u64)p;
  const unsigned lo = (unsigned)__builtin_amdgcn_readfirstlane((unsigned)v), hi = (unsigned)__builtin_amdgcn_readfirstlane((unsigned)(v >> 32));
  const u64 u = (u64)lo | ((u64)hi << 32);
  return __builtin_amdgcn_make_buffer_rsrc((void*)u, 0, bytes, 0x00020000);
}
__device__ __forceinline__ u32x4 own_rsrc(const void* p, unsigned bytes) {
  u32x4 r = {(unsigned)__builtin_amdgcn_readfirstlane((unsigned)(u64)p), (unsigned)__builtin_amdgcn_readfirstlane((unsigned)((u64)p >> 32)) & 0xffffu, bytes, 0x00020000u};
  asm volatile("s_nop 4" : "+s"(r));
  return r;
}
constexpr int LD_AUX = 16, ST_AUX = 16;
__device__ __forceinline__ u32x4 ld16(__amdgpu_buffer_rsrc_t rs, unsigned off) {
  return __builtin_amdgcn_raw_buffer_load_b128(rs, off, 0, LD_AUX);
}
__device__ __forceinline__ void st16(__amdgpu_buffer_rsrc_t rs, unsigned off, u32x4 v) {
  __builtin_amdgcn_raw_buffer_store_b128(v, rs, off, 0, ST_AUX);
}
__device__ __forceinline__ void st16s(u32x4 v, __amdgpu_buffer_rsrc_t rs, unsigned voff, unsigned soff) {
  __builtin_amdgcn_raw_buffer_store_b128(v, rs, voff, soff, ST_AUX);
  asm volatile("s_nop 1" :: "v"(v) : "memory");
}
__device__ __forceinline__ void st8(__amdgpu_buffer_rsrc_t rs, unsigned off, u32x2 v) {
  __builtin_amdgcn_raw_buffer_store_b64(v, rs, off, 0, ST_AUX);
}
__device__ __forceinline__ void st4(__amdgpu_buffer_rsrc_t rs, unsigned off, unsigned v) {
  __builtin_amdgcn_raw_buffer_store_b32(v, rs, off, 0, ST_AUX);
}
__device__ __forceinline__ unsigned ld4(__amdgpu_buffer_rsrc_t rs, unsigned off) {
  return __builtin_amdgcn_raw_buffer_load_b32(rs, off, 0, LD_AUX);
}
constexpr int TU = 16;
__device__ __forceinline__ unsigned cyc32() { return (unsigned)__builtin_amdgcn_s_memtime(); }
__device__ __forceinline__ unsigned to_sgpr(unsigned x) {
  unsigned r;
  asm volatile("s_nop 0\n\tv_readfirstlane_b32 %0, %1\n\ts_nop 4" : "=s"(r) : "v"(x));
  return r;
}
struct Clk {
  unsigned ce, te;
  unsigned c2t, t2c;
  __device__ __forceinline__ void edge(unsigned& c, unsigned& t) {
    u64 c0, r0, r1, r2, r3, r4, r5;
    asm volatile("s_memtime %0\n\ts_memrealtime %1\n\ts_nop 4\n\ts_memrealtime %2\n\ts_nop 4\n\ts_memrealtime %3\n\ts_nop 4\n\t"
                 "s_memrealtime %4\n\ts_nop 4\n\ts_memrealtime %5\n\ts_nop 4\n\ts_memrealtime %6\n\ts_waitcnt lgkmcnt(0)"
                 : "=&s"(c0), "=&s"(r0), "=&s"(r1), "=&s"(r2), "=&s"(r3), "=&s"(r4), "=&s"(r5) :: "memory");
    const unsigned x = (unsigned)r0;
    const unsigned n = ((unsigned)r1 == x) + ((unsigned)r2 == x) + ((unsigned)r3 == x) + ((unsigned)r4 == x) + ((unsigned)r5 == x);
    c = to_sgpr((unsigned)c0 + 24u * n); t = (x + 4u) * TU;
  }
  __device__ __forceinline__ void init() { edge(ce, te); c2t = 44000u; t2c = 97600u; }
  __device__ __forceinline__ void sync() {
    unsigned c, t;
    edge(c, t);
    set(c, t);
  }
  __device__ __forceinline__ void set(unsigned c, unsigned t) {
    if (c == ce) return;
    const float q = (float)(t - te) * __builtin_amdgcn_rcpf((float)(c - ce));
    const float qc = fminf(fmaxf(q, 0.51f), 1.99f);
    c2t = to_sgpr((unsigned)(qc * 65536.f));
    t2c = to_sgpr((unsigned)(__builtin_amdgcn_rcpf(qc) * 65536.f));
    ce = c; te = t;
  }
  __device__ __forceinline__ unsigned time(unsigned c) const { return te + ((min(c - ce, 30000u) * c2t) >> 16); }
  __device__ __forceinline__ unsigned now() const { return time(cyc32()); }
  __device__ __forceinline__ unsigned target(unsigned t) const {
    return to_sgpr(ce + (((unsigned)min(max((int)(t - te), 0), 20000) * t2c) >> 16));
  }
  __device__ __forceinline__ void wait(unsigned t) const { wait_c(target(t)); }
  __device__ __forceinline__ void wait_c(unsigned c) const {
    unsigned y;
    asm volatile("1:\n\ts_memtime vcc\n\ts_waitcnt lgkmcnt(0)\n\ts_sub_i32 %0, %1, vcc_lo\n\ts_cmp_gt_i32 %0, 128\n\ts_cbranch_scc1 1b\n\t"
                 "2:\n\ts_sub_i32 %0, %0, %2\n\ts_cmp_gt_i32 %0, %3\n\ts_cbranch_scc1 2b"
                 : "=&s"(y) : "s"(c), "n"(W_STEP), "n"(W_OFF) : "memory", "scc", "vcc");
  }
};
__device__ __forceinline__ unsigned nosent(unsigned d) { return min(d, SENT - 1u); }
__device__ __forceinline__ int arow(int row, int half) { return 4 * (row >> 1) + 2 * half + (row & 1); }
__device__ __forceinline__ unsigned umax4(u32x4 v) { return max(max(v[0], v[1]), max(v[2], v[3])); }
__device__ __forceinline__ void stamp_again(GAS u64* p) {
  __hip_atomic_store(p, (__builtin_amdgcn_s_memrealtime() & ~7ull) | (__hip_atomic_load(p, __ATOMIC_RELAXED, __HIP_MEMORY_SCOPE_AGENT) & 7ull),
                     __ATOMIC_RELAXED, __HIP_MEMORY_SCOPE_AGENT);
}
__device__ __forceinline__ bool poll_fail(unsigned& spins, GAS unsigned* abortw, int lane) {
  ++spins;
  if (spins > SPIN_CAP) {
    if (lane == 0) __hip_atomic_store(abortw, 1u, __ATOMIC_RELAXED, __HIP_MEMORY_SCOPE_AGENT);
    return true;
  }
  if ((spins & 511u) == 0u && lane == 0) {
    unsigned n = 0;
    for (int i = 0; i < 8; ++i) n += __hip_atomic_load(abortw + (SY_CT - SY_ABORT + (CT_ARR + i) * 128) / 4, __ATOMIC_RELAXED, __HIP_MEMORY_SCOPE_AGENT) + 1u;
    if (n < 256u) stamp_again((GAS u64*)(abortw - 32));
  }
  if ((spins & 63u) == 0u)
    return __builtin_amdgcn_readfirstlane(__hip_atomic_load(abortw, __ATOMIC_RELAXED, __HIP_MEMORY_SCOPE_AGENT)) != SENT;
  return false;
}
struct Gate {
  u64 entry, sel; int arr; unsigned rot; bool open;
  __device__ __forceinline__ bool look(char* sync, u32x4 v) {
    if (open) return true;
    for (unsigned spins = 1;; ++spins) {
#pragma unroll
      for (int i = 0; i < 4; ++i) v[i] = __builtin_amdgcn_readfirstlane(v[i]);
      if (((u64)v[1] << 32 | v[0]) - entry + 2000u < (1ull << 31) || spins > 4 * SPIN_CAP) break;
      if (spins > 1) __builtin_amdgcn_s_sleep(8);
      v = ld16(mk_rsrc(sync + SY_EPOCH, 16), 0);
    }
    asm volatile("" ::: "memory");
    sel = (u64)v[3] << 32 | v[2];
    const bool same = rot == (v[0] & 7u);
    rot = v[0] & 7u; open = true;
    return same;
  }
  __device__ __forceinline__ void ask(char* sync, u32x4& v) { asm volatile("buffer_load_dwordx4 %0, off, %1, 0 sc1" : "=v"(v) : "s"(own_rsrc(sync + SY_EPOCH, 16)) : "memory"); }
  __device__ __forceinline__ void arrive(char* sync) {
    if (arr >= 0 && opaque_tid(0) == 0) __hip_atomic_fetch_add((GAS unsigned*)(sync + SY_CT + (CT_ARR + arr) * 128), 1u, __ATOMIC_RELAXED, __HIP_MEMORY_SCOPE_AGENT);
    arr = -1;
  }
  __device__ __forceinline__ bool pass(char* sync, u32x4 v = {0u, 0u, 0u, 0u}) { const bool r = look(sync, v); arrive(sync); return r; }
};
__device__ __forceinline__ unsigned ann_val(unsigned t, int s) { return (t << 8) | (unsigned)s; }
__device__ __forceinline__ unsigned tr_place(int s) { return (s & 2) * (CHK / 2) + (s & 1) * 4096; }
__device__ __forceinline__ unsigned die_half() { return ((unsigned)__builtin_amdgcn_s_getreg(20 | (3 << 11)) >> 2) & 1u; }
typedef u64 Sel;
__device__ __forceinline__ void start_wait(char* sync, GAS unsigned* abortw, int lane, bool& dead, int ci, int cj) {
  unsigned spins = 0;
  GAS unsigned* p = (GAS unsigned*)(sync + SY_CT + (lane == 0 ? CT_INIT : (lane == 1 ? ci : cj)) * 128);
  while (!__all(__hip_atomic_load(p, __ATOMIC_RELAXED, __HIP_MEMORY_SCOPE_AGENT) + 1u >= (lane == 0 ? 64u : 1u))) {
    __builtin_amdgcn_s_sleep(4);
    if (dead || poll_fail(spins, abortw, lane)) { dead = true; break; }
  }
}
__device__ __forceinline__ __amdgpu_buffer_rsrc_t rsrc_u(char* sync) { return mk_rsrc((void*)sync, SY_SET); }
__device__ __forceinline__ unsigned near_half(unsigned off, u64 sel) { return off + ((unsigned)(sel >> ((off >> 21) & 63)) & 1u) * 8192; }
__device__ __forceinline__ unsigned x_slot(int kind, int s, int h, Sel sel) {
  return kind == 2 ? near_half(32 * PAGE + (s * 32 + h) * CHK, ~sel) : near_half((s * 64 + 2 * h + 1 - kind) * CHK, sel);
}
__device__ __forceinline__ int hpos(int h) { return (h & 3) * 8 + (h >> 2); }
constexpr unsigned UXH = 8 * CHK;
__device__ __forceinline__ unsigned ux_base(int s, int h, Sel sel) { return near_half(((s & (URING - 1)) * 32 + h) * UXH, ~sel); }
__device__ __forceinline__ constexpr unsigned ux_tile(int t) { return (t >> 3) * CHK + (t & 7) * 1024; }
__device__ __forceinline__ __amdgpu_buffer_rsrc_t rsrc_hot(char* sync, Sel sel) {
  return mk_rsrc((void*)(sync + near_half(SY_HOT, sel)), H_BYTES - 8192);
}
template <int SLP = 4>
__device__ __forceinline__ void ctr_wait(GAS unsigned* c, unsigned need, GAS unsigned* abortw, int lane, bool& dead) {
  unsigned spins = 0;
  while (true) {
    unsigned v = __builtin_amdgcn_readfirstlane(__hip_atomic_load(c, __ATOMIC_RELAXED, __HIP_MEMORY_SCOPE_AGENT)) + 1u;
    if (v >= need) break;
    __builtin_amdgcn_s_sleep(SLP);
    if (dead || poll_fail(spins, abortw, lane)) { dead = true; break; }
  }
}
template <int N> __device__ __forceinline__ void wait_vm() { asm volatile("s_waitcnt vmcnt(%0)" :: "n"(N) : "memory"); }
__device__ __forceinline__ void lds_barrier() {
  asm volatile("s_waitcnt lgkmcnt(0)" ::: "memory");
  __builtin_amdgcn_s_barrier();
  asm volatile("" ::: "memory");
}
__device__ __forceinline__ f32x4 vzero() { float z; asm volatile("v_mov_b32 %0, 0" : "=v"(z)); f32x4 r = {z, z, z, z}; asm volatile("" : "+v"(r)); return r; }
__device__ __forceinline__ f32x4 smf(u32x4 a, u32x8 b, f32x4 c, int idx) {
  return __builtin_amdgcn_smfmac_f32_16x16x64_bf16(__builtin_bit_cast(bfv8, a), __builtin_bit_cast(bfv16, b), c, idx, 0, 0);
}
__device__ __forceinline__ float red16max(float x) { auto r = __builtin_amdgcn_permlane16_swap(__float_as_uint(x), __float_as_uint(x), false, false); return fmaxf(__uint_as_float(r[0]), __uint_as_float(r[1])); }
__device__ __forceinline__ float red32max(float x) { auto r = __builtin_amdgcn_permlane32_swap(__float_as_uint(x), __float_as_uint(x), false, false); return fmaxf(__uint_as_float(r[0]), __uint_as_float(r[1])); }
__device__ __forceinline__ float red16add(float x) { auto r = __builtin_amdgcn_permlane16_swap(__float_as_uint(x), __float_as_uint(x), false, false); return __uint_as_float(r[0]) + __uint_as_float(r[1]); }
__device__ __forceinline__ float red32add(float x) { auto r = __builtin_amdgcn_permlane32_swap(__float_as_uint(x), __float_as_uint(x), false, false); return __uint_as_float(r[0]) + __uint_as_float(r[1]); }
constexpr bool blk_in_lds(int R, int b) {
  return R == 0 ? ((b & 7) == 1 || (b & 7) == 4) : (b < 16 ? (b & 3) == 0 : (b & 3) == 3);
}
constexpr int lds_index(int R, int b) { int n = 0; for (int i = 0; i < b; ++i) if (blk_in_lds(R, i)) ++n; return n; }
constexpr bool frag_in_lds(int R, int f) { return blk_in_lds(R, f >> 1); }
constexpr int frag_reg_slot(int R, int f) { int n = 0; for (int i = 0; i < f; ++i) if (!frag_in_lds(R, i)) ++n; return n; }

constexpr int m_block(int R, int m, int i) {
  return R == 0 ? 2 * (i < 12 ? (m ? i + i / 3 + 1 : i + (i + 1) / 3) : 4 * (i - 12) + 2 - 2 * m) + m : m ? 16 + 4 * (i & 3) + (i >> 2) : (i < 12 ? i + i / 3 + 1 : 4 * (i - 12));
}
#define VO_ORD 1
constexpr int w_block(int R, int i) { return R && VO_ORD ? m_block(R, i < 12, i % 12) : m_block(R, i & 1, i >> 1); }
#define W_BG 0xfedcba9876543210ull
#define W_HELP 0xfea74ull
#define W_NT 0
#define W_HELP2 0xdef32ull
#define U_ORD 0x3210
constexpr u64 bg_order(int m) { u64 r = 0; for (int n = 0; n < 16; ++n) r |= (u64)(m_block(0, m, (W_BG >> (4 * n)) & 15) >> 1) << (4 * n); return r; }
struct WSched { int ord[32]; int slot[32]; int issue[32]; int out[32]; int reg[32]; };
constexpr WSched make_wsched(int R) {
  WSched S{};
  int n = 0;
  for (int i = 0; i < 24; ++i) S.ord[n++] = w_block(R, i);
  for (int i = 0; i < n; ++i) S.reg[i] = frag_reg_slot(R, 2 * S.ord[i]);
  const int npure = n;
  for (int b = 0; b < 32; ++b) if (blk_in_lds(R, b)) S.ord[n++] = b;
  int seq[32] = {};
  int nissued = 0, next = 0;
  for (int i = 0; i < 10; ++i) { S.slot[i] = (i + 10 - npure % 10) % 10; S.issue[i] = -1; seq[i] = nissued++; }
  next = 10;
  S.out[0] = nissued - 1;
  for (int c = 0; c <= npure; ++c) {
    if (c >= 1) {
      const int k = S.slot[c - 1];
      if (next < npure) { S.slot[next] = k; S.issue[next] = c; seq[next] = nissued++; ++next; }
      else if (k < NLF / 2) { S.slot[npure + k] = k; S.issue[npure + k] = c; seq[npure + k] = nissued++; }
    }
    if (c + 1 < npure) S.out[c + 1] = nissued - 1 - seq[c + 1];
  }
  return S;
}
__device__ __forceinline__ int kcol(int k) { return 8 * ((k >> 1) & 1) + ((k & 1) ? 7 - (k >> 2) : k >> 2); }
template <bool PERM = false>
__device__ __forceinline__ u32x8 tr_frag(const char* sb, int tile, int lane) {
  u32x8 f;
#pragma unroll
  for (int q = 0; q < 4; ++q) {
    int row = (q >> 1) * 32 + 8 * (lane >> 4) + 4 * (q & 1) + ((lane & 15) >> 2);
    if (PERM) row = (row & ~15) + kcol(row & 15);
    int p = lane & 3;
    int lc = 2 * tile + (p >> 1);
    int addr = row * 256 + ((lc ^ swz_b(row)) * 16) + 8 * (p & 1);
    bf16x4 v = __builtin_amdgcn_ds_read_tr16_b64_v4i16((LAS bf16x4*)(sb + addr));
    u32x2 u = __builtin_bit_cast(u32x2, v);
    f[2 * q] = u[0]; f[2 * q + 1] = u[1];
  }
  return f;
}
__device__ __forceinline__ void dma16(__amdgpu_buffer_rsrc_t rs, unsigned voff, unsigned soff, unsigned lds, bool nt = false, bool pad = true) {
  if (nt) asm volatile("s_mov_b32 m0, %0\n\ts_nop 3\n\tbuffer_load_dwordx4 %1, %2, %3 offen nt lds" :: "s"(lds), "v"(voff), "s"(rs), "s"(soff) : "m0", "memory");
  else if (pad) asm volatile("s_mov_b32 m0, %0\n\ts_nop 3\n\tbuffer_load_dwordx4 %1, %2, %3 offen lds" :: "s"(lds), "v"(voff), "s"(rs), "s"(soff) : "m0", "memory");
  else asm volatile("s_mov_b32 m0, %0\n\ts_nop 0\n\tbuffer_load_dwordx4 %1, %2, %3 offen lds" :: "s"(lds), "v"(voff), "s"(rs), "s"(soff) : "m0", "memory");
}
__device__ __forceinline__ u32x8 tr_frag2(const char* sb, int tile, int f, int lane) {
  u32x8 r;
#pragma unroll
  for (int c = 0; c < 2; ++c)
#pragma unroll
    for (int t = 0; t < 2; ++t) {
      const int row = 32 * c + 16 * f + 4 * (lane >> 4) + ((lane & 15) >> 2);
      const int p = lane & 3, lc = 2 * (tile + t) + (p >> 1);
      const u32x2 u = __builtin_bit_cast(u32x2, __builtin_amdgcn_ds_read_tr16_b64_v4i16((LAS bf16x4*)(sb + row * 256 + ((lc ^ swz_b(row)) * 16) + 8 * (p & 1))));
      r[4 * c + t] = u[0]; r[4 * c + 2 + t] = u[1];
    }
  return r;
}
template <int ROLE>
__device__ __forceinline__ void load_weights(const ChainArgs& a, int h, int w, int lane, char* smem, Wts& W) {
#pragma unroll
  for (int i = 0; i < 4; ++i) asm volatile("" : "=v"(W.v[i]));
#pragma unroll
  for (int i = 0; i < 32; ++i) asm volatile("" : "=a"(W.a[i]));
  constexpr WSched S = make_wsched(ROLE);
  constexpr int NP = 32 - NLF / 2;
  const __amdgpu_buffer_rsrc_t r0 = mk_rsrc(ROLE == 0 ? a.Wq : a.Wv, 8u << 20), r1 = mk_rsrc(ROLE == 0 ? a.Wk : a.Wo, 8u << 20);
  const unsigned lds0 = (unsigned)(uintptr_t)(LAS char*)smem;
  unsigned vo[2][4];
#pragma unroll
  for (int j = 0; j < 4; ++j) {
    const int row = (w * 4 + j) * 4 + (lane >> 4), lc = (lane & 15) ^ swz_b(row);
    vo[0][j] = row * HD * 2 + lc * 16; vo[1][j] = row * DM * 2 + lc * 16;
  }
  asm volatile("s_nop 4");
  auto stage = [&](int i) {
    const int blk = S.ord[i];
    const bool second = ROLE == 0 ? (blk & 1) : blk >= 16, wo = ROLE == 1 && second;
    const unsigned so = wo ? (unsigned)(((128 * h + 64 * (blk & 1)) * DM + 128 * (((blk - 16) >> 1) ^ (2 * (h & 3)))) * 2)
                           : (unsigned)((64 * ((ROLE == 0 ? blk >> 1 : blk) ^ (h & RHO)) * HD + 128 * h) * 2);
#pragma unroll
    for (int j = 0; j < 4; ++j) dma16(second ? r1 : r0, vo[wo][j], so, lds0 + 16384 * S.slot[i] + (w * 4 + j) * 1024, W_NT & (ROLE ? 8 : 1));
  };
#pragma unroll
  for (int i = 0; i < 32; ++i) if (S.issue[i] == -1) stage(i);
#pragma unroll
  for (int c = 0; c < NP; ++c) {
    asm volatile("s_waitcnt vmcnt(%0)" :: "n"(4 * S.out[c]) : "memory");
    lds_barrier();
#pragma unroll
    for (int i = 0; i < 32; ++i) if (S.issue[i] == c) stage(i);
    const char* sb = smem + 16384 * S.slot[c];
#pragma unroll
    for (int t = 0; t < 2; ++t) {
      u32x8 f = (ROLE == 1 && S.ord[c] >= 16) ? tr_frag2(sb, 2 * w, t, lane) : tr_frag<true>(sb, 2 * w + t, lane);
      if (S.reg[c] < 32) {
#pragma unroll
        for (int i = 0; i < 8; ++i) asm volatile("v_accvgpr_write_b32 a[%1], %0" :: "v"(f[i]), "n"(8 * ((S.reg[c] + t) & 31) + i));
      }
      else {
#pragma unroll
        for (int i = 0; i < 8; ++i) W.v[(S.reg[c] >> 2) & 3][8 * ((S.reg[c] + t) & 3) + i] = f[i];
      }
    }
    if (S.reg[c] >= 32) asm volatile("" : "+v"(W.v[(S.reg[c] >> 2) & 3]));
  }
  static_assert(S.issue[31] < NP && S.issue[NP] < S.issue[31], "resident blocks are issued in slot order inside the loop");
#pragma unroll
  for (int j = 0; j < NLF / 2; ++j) {
    asm volatile("s_waitcnt vmcnt(%0)" :: "n"(4 * (NLF / 2 - 1 - j)) : "memory");
    lds_barrier();
    const bool two = ROLE == 1 && S.ord[NP + j] >= 16;
    u32x8 f0 = two ? tr_frag2(smem + 16384 * j, 2 * w, 0, lane) : tr_frag<true>(smem + 16384 * j, 2 * w, lane);
    u32x8 f1 = two ? tr_frag2(smem + 16384 * j, 2 * w, 1, lane) : tr_frag<true>(smem + 16384 * j, 2 * w + 1, lane);
    lds_barrier();
    char* d = smem + 16384 * j + w * 4096 + lane * 16;
    *(u32x4*)d = (u32x4){f0[0], f0[1], f0[2], f0[3]};
    *(u32x4*)(d + 1024) = (u32x4){f0[4], f0[5], f0[6], f0[7]};
    *(u32x4*)(d + 2048) = (u32x4){f1[0], f1[1], f1[2], f1[3]};
    *(u32x4*)(d + 3072) = (u32x4){f1[4], f1[5], f1[6], f1[7]};
  }
  lds_barrier();
}
template <int... Is> struct iseq {};
template <int N, int... Is> struct mkseq : mkseq<N - 1, N - 1, Is...> {};
template <int... Is> struct mkseq<0, Is...> { typedef iseq<Is...> type; };

#define SB0 __builtin_amdgcn_sched_barrier(0)
#define CAT8(x, y) ((u32x8){x[0], x[1], x[2], x[3], y[0], y[1], y[2], y[3]})
template <class F, int... I>
__device__ __forceinline__ void for_slots(iseq<I...>, F f) { (f(std::integral_constant<int, I>{}), ...); }
#define QK_BODY \
  "s_cmp_eq_u32 %[wv], 1\n\t" \
  "s_cbranch_scc1 91f\n\t" \
  "s_cmp_eq_u32 %[wv], 2\n\t" \
  "s_cbranch_scc1 92f\n\t" \
  "s_cmp_eq_u32 %[wv], 3\n\t" \
  "s_cbranch_scc1 93f\n\t" \
  "90:\n\t" \
  "s_mov_b32 s58, s42\n\t" \
  "20:\n\t" \
  "s_and_b32 s76, s44, 7\n\t" \
  "s_lshl_b32 s76, s76, 15\n\t" \
  "s_add_i32 s40, s54, s76\n\t" \
  "s_and_b32 s76, s44, 3\n\t" \
  "s_lshl_b32 s45, s76, 14\n\t" \
  "s_mul_i32 s47, s76, 0xc000\n\t" \
  "s_add_i32 s47, s47, 0x40000\n\t" \
  "s_add_i32 s49, s44, -1\n\t" \
  "s_and_b32 s48, s49, 2\n\t" \
  "s_lshl_b32 s48, s48, 13\n\t" \
  "s_and_b32 s76, s49, 1\n\t" \
  "s_lshl_b32 s76, s76, 12\n\t" \
  "s_add_i32 s48, s48, s76\n\t" \
  "s_lshl_b32 s50, s53, 8\n\t" \
  "s_mov_b32 s59, 0\n\t" \
  "s_mov_b32 s92, s51\n\t" \
  "s_mov_b32 s93, s53\n\t" \
  "s_xor_b32 s76, s40, 0x400\n\t" \
  "s_xor_b32 s77, s40, 0x800\n\t" \
  "s_xor_b32 s78, s40, 0xc00\n\t" \
  "s_cmp_lg_u32 s58, 0\n\t" \
  "s_cbranch_scc1 11f\n\t" \
  "10:\n\t" \
  "ds_read_b128 v[48:51], v102 offset:0\n\t" \
  "ds_read_b128 v[52:55], v102 offset:1024\n\t" \
  "ds_read_b128 v[56:59], v102 offset:2048\n\t" \
  "ds_read_b128 v[60:63], v102 offset:3072\n\t" \
  "ds_read_b128 v[16:19], v102 offset:16384\n\t" \
  "ds_read_b128 v[20:23], v102 offset:17408\n\t" \
  "ds_read_b128 v[24:27], v102 offset:18432\n\t" \
  "ds_read_b128 v[28:31], v102 offset:19456\n\t" \
  "ds_read_b128 v[84:87], v102 offset:49152\n\t" \
  "ds_read_b128 v[88:91], v102 offset:50176\n\t" \
  "ds_read_b128 v[92:95], v102 offset:51200\n\t" \
  "ds_read_b128 v[96:99], v102 offset:52224\n\t" \
  "s_waitcnt lgkmcnt(4)\n\t" \
  "ds_read_b128 v[0:3], v103 offset:16384\n\t" \
  "ds_read_b128 v[4:7], v103 offset:17408\n\t" \
  "ds_read_b128 v[8:11], v103 offset:18432\n\t" \
  "ds_read_b128 v[12:15], v103 offset:19456\n\t" \
  "s_bitcmp1_b32 s58, 1\n\t" \
  "s_cselect_b64 s[80:81], -1, 0\n\t" \
  "s_cmp_lg_u32 s58, 0\n\t" \
  "s_cbranch_scc1 17f\n\t" \
  "s_mov_b32 exec_hi, 0\n\t" \
  "16:\n\t" \
  "s_memtime vcc\n\t" \
  "s_waitcnt lgkmcnt(0)\n\t" \
  "s_sub_i32 s79, s41, vcc_lo\n\t" \
  "s_cmp_gt_i32 s79, 64\n\t" \
  "s_cbranch_scc1 16b\n\t" \
  "13:\n\t" \
  "buffer_load_dword v116, v112, %[rh], s48 offen sc1\n\t" \
  "s_waitcnt vmcnt(0)\n\t" \
  "15:\n\t" \
  "v_mov_b32 v117, 20000\n\t" \
  "v_cmp_ne_u32_sdwa vcc, v116, s49 src0_sel:BYTE_0 src1_sel:DWORD\n\t" \
  "v_subrev_u32 v116, s50, v116\n\t" \
  "v_ashrrev_i32 v116, 8, v116\n\t" \
  "s_nop 1\n\t" \
  "v_max_i32_dpp v116, v116, v116 quad_perm:[1,0,3,2] row_mask:0xf bank_mask:0xf\n\t" \
  "s_cbranch_vccnz 14f\n\t" \
  "s_nop 0\n\t" \
  "v_max_i32_dpp v116, v116, v116 quad_perm:[2,3,0,1] row_mask:0xf bank_mask:0xf\n\t" \
  "s_nop 1\n\t" \
  "v_max_i32_dpp v116, v116, v116 row_half_mirror row_mask:0xf bank_mask:0xf\n\t" \
  "s_nop 1\n\t" \
  "v_max_i32_dpp v116, v116, v116 row_mirror row_mask:0xf bank_mask:0xf\n\t" \
  "v_med3_i32 v116, v116, 0, v117\n\t" \
  "v_mul_u32_u24 v116, s52, v116\n\t" \
  "v_lshrrev_b32 v116, 16, v116\n\t" \
  "s_mov_b64 exec, -1\n\t" \
  "v_readfirstlane_b32 s84, v116\n\t" \
  "v_readlane_b32 s85, v116, 16\n\t" \
  "s_max_u32 s84, s84, s85\n\t" \
  "s_add_i32 s84, s84, s51\n\t" \
  "1:\n\t" \
  "s_memtime vcc\n\t" \
  "s_waitcnt lgkmcnt(0)\n\t" \
  "s_sub_i32 s79, s84, vcc_lo\n\t" \
  "s_cmp_gt_i32 s79, 128\n\t" \
  "s_cbranch_scc1 1b\n\t" \
  ".p2align 4\n\t" \
  "2:\n\t" \
  "s_sub_i32 s79, s79, 33\n\t" \
  "s_cmp_gt_i32 s79, 56\n\t" \
  "s_cbranch_scc1 2b\n\t" \
  "buffer_load_dwordx4 v[32:35], v100, %[rh], s40 offen sc1\n\t" \
  "s_nop 12\n\t" \
  "buffer_load_dwordx4 v[36:39], v100, %[rh], s76 offen sc1\n\t" \
  "s_nop 12\n\t" \
  "buffer_load_dwordx4 v[40:43], v100, %[rh], s77 offen sc1\n\t" \
  "s_nop 12\n\t" \
  "buffer_load_dwordx4 v[44:47], v100, %[rh], s78 offen sc1\n\t" \
  "s_memtime vcc\n\t" \
  "s_memrealtime s[82:83]\n\t" \
  "s_nop 4\n\t" \
  "s_memrealtime s[84:85]\n\t" \
  "s_nop 4\n\t" \
  "s_memrealtime s[56:57]\n\t" \
  "s_nop 4\n\t" \
  "s_memrealtime s[86:87]\n\t" \
  "s_nop 4\n\t" \
  "s_memrealtime s[88:89]\n\t" \
  "s_nop 4\n\t" \
  "s_memrealtime s[90:91]\n\t" \
  "s_mov_b32 s92, 0\n\t" \
  "s_waitcnt lgkmcnt(0)\n\t" \
  "s_cmp_eq_u32 s82, s84\n\t" \
  "s_addc_u32 s92, s92, 0\n\t" \
  "s_cmp_eq_u32 s82, s56\n\t" \
  "s_addc_u32 s92, s92, 0\n\t" \
  "s_cmp_eq_u32 s82, s86\n\t" \
  "s_addc_u32 s92, s92, 0\n\t" \
  "s_cmp_eq_u32 s82, s88\n\t" \
  "s_addc_u32 s92, s92, 0\n\t" \
  "s_cmp_eq_u32 s82, s90\n\t" \
  "s_addc_u32 s92, s92, 0\n\t" \
  "s_mul_i32 s92, s92, 24\n\t" \
  "s_add_i32 s92, s92, vcc_lo\n\t" \
  "s_lshl4_add_u32 s93, s82, 64\n\t" \
  "3:\n\t" \
  "s_sub_i32 s84, s92, s43\n\t" \
  "s_lshl_b32 s85, s93, 8\n\t" \
  "s_or_b32 s85, s85, s44\n\t" \
  "v_mov_b32 v118, s85\n\t" \
  "v_mov_b64 v[68:69], 0\n\t" \
  "v_mov_b64 v[70:71], 0\n\t" \
  "v_mov_b64 v[72:73], 0\n\t" \
  "v_mov_b64 v[74:75], 0\n\t" \
  "v_mov_b64 v[76:77], 0\n\t" \
  "v_mov_b64 v[78:79], 0\n\t" \
  "v_mov_b64 v[80:81], 0\n\t" \
  "v_mov_b64 v[82:83], 0\n\t" \
  "s_waitcnt lgkmcnt(0)\n\t" \
  "s_waitcnt vmcnt(3)\n\t" \
  "v_smfmac_f32_16x16x64_bf16 v[68:71], v[32:35], a[0:7], v104\n\t" \
  "ds_write_b128 v101, v[32:35] offset:0\n\t" \
  "v_smfmac_f32_16x16x64_bf16 v[72:75], v[32:35], a[8:15], v104\n\t" \
  "v_smfmac_f32_16x16x64_bf16 v[76:79], v[32:35], v[48:55], v104\n\t" \
  "v_smfmac_f32_16x16x64_bf16 v[80:83], v[32:35], v[56:63], v104\n\t" \
  "s_waitcnt vmcnt(2)\n\t" \
  "v_smfmac_f32_16x16x64_bf16 v[68:71], v[36:39], a[16:23], v104\n\t" \
  "ds_write_b128 v101, v[36:39] offset:1024\n\t" \
  "v_smfmac_f32_16x16x64_bf16 v[72:75], v[36:39], a[24:31], v104\n\t" \
  "v_smfmac_f32_16x16x64_bf16 v[76:79], v[36:39], a[32:39], v104\n\t" \
  "v_cmp_u_f32 vcc, v68, v69\n\t" \
  "v_smfmac_f32_16x16x64_bf16 v[80:83], v[36:39], a[40:47], v104\n\t" \
  "v_cmp_u_f32 s[82:83], v70, v71\n\t" \
  "s_waitcnt vmcnt(1)\n\t" \
  "v_smfmac_f32_16x16x64_bf16 v[68:71], v[40:43], v[16:23], v104\n\t" \
  "ds_write_b128 v101, v[40:43] offset:2048\n\t" \
  "v_smfmac_f32_16x16x64_bf16 v[72:75], v[40:43], v[24:31], v104\n\t" \
  "s_or_b64 vcc, vcc, s[82:83]\n\t" \
  "s_or_b64 vcc, vcc, s[80:81]\n\t" \
  "s_waitcnt lgkmcnt(1)\n\t" \
  "v_smfmac_f32_16x16x64_bf16 v[76:79], v[40:43], a[48:55], v104\n\t" \
  "s_cbranch_scc1 8f\n\t" \
  "5:\n\t" \
  "s_barrier\n\t" \
  "4:\n\t" \
  "s_memtime s[56:57]\n\t" \
  "ds_read_b128 v[48:51], v101 offset:4096\n\t" \
  "v_smfmac_f32_16x16x64_bf16 v[80:83], v[40:43], a[56:63], v104\n\t" \
  "ds_read_b128 v[52:55], v101 offset:5120\n\t" \
  "ds_read_b128 v[56:59], v101 offset:8192\n\t" \
  "s_waitcnt vmcnt(0)\n\t" \
  "v_smfmac_f32_16x16x64_bf16 v[68:71], v[44:47], a[64:71], v104\n\t" \
  "ds_write_b128 v101, v[44:47] offset:3072\n\t" \
  "v_smfmac_f32_16x16x64_bf16 v[72:75], v[44:47], a[72:79], v104\n\t" \
  "v_smfmac_f32_16x16x64_bf16 v[76:79], v[44:47], a[80:87], v104\n\t" \
  "v_cmp_u_f32 vcc, v68, v69\n\t" \
  "v_smfmac_f32_16x16x64_bf16 v[80:83], v[44:47], a[88:95], v104\n\t" \
  "v_cmp_u_f32 s[82:83], v70, v71\n\t" \
  "s_waitcnt lgkmcnt(3)\n\t" \
  "v_smfmac_f32_16x16x64_bf16 v[68:71], v[48:51], a[96:103], v104\n\t" \
  "ds_read_b128 v[60:63], v101 offset:9216\n\t" \
  "ds_read_b128 v[64:67], v101 offset:12288\n\t" \
  "v_smfmac_f32_16x16x64_bf16 v[72:75], v[48:51], a[104:111], v104\n\t" \
  "s_or_b64 vcc, vcc, s[82:83]\n\t" \
  "s_waitcnt lgkmcnt(2)\n\t" \
  "v_smfmac_f32_16x16x64_bf16 v[68:71], v[52:55], a[112:119], v104\n\t" \
  "s_cbranch_scc1 9f\n\t" \
  "6:\n\t" \
  "s_barrier\n\t" \
  "v_smfmac_f32_16x16x64_bf16 v[72:75], v[52:55], a[120:127], v104\n\t" \
  "s_waitcnt lgkmcnt(0)\n\t" \
  "ds_read_b128 v[48:51], v101 offset:13312\n\t" \
  "v_smfmac_f32_16x16x64_bf16 v[68:71], v[56:59], a[192:199], v104\n\t" \
  "ds_read_b128 v[52:55], v101 offset:6144\n\t" \
  "s_sub_i32 s79, s56, s84\n\t" \
  "s_mul_hi_u32 s79, s79, s46\n\t" \
  "v_smfmac_f32_16x16x64_bf16 v[72:75], v[56:59], a[200:207], v104\n\t" \
  "v_lshl_add_u32 v116, s79, 9, v118\n\t" \
  "buffer_store_dword v116, v109, %[rh], s45 offen sc1\n\t" \
  "v_smfmac_f32_16x16x64_bf16 v[68:71], v[60:63], a[208:215], v104\n\t" \
  "ds_read_b128 v[56:59], v101 offset:7168\n\t" \
  "v_smfmac_f32_16x16x64_bf16 v[72:75], v[60:63], a[216:223], v104\n\t" \
  "v_smfmac_f32_16x16x64_bf16 v[68:71], v[64:67], v[152:159], v104\n\t" \
  "ds_read_b128 v[60:63], v101 offset:10240\n\t" \
  "v_smfmac_f32_16x16x64_bf16 v[72:75], v[64:67], v[160:167], v104\n\t" \
  "s_waitcnt lgkmcnt(3)\n\t" \
  "v_smfmac_f32_16x16x64_bf16 v[68:71], v[48:51], v[168:175], v104\n\t" \
  "ds_read_b128 v[64:67], v101 offset:11264\n\t" \
  "ds_read_b128 v[16:19], v103 offset:49152\n\t" \
  "v_smfmac_f32_16x16x64_bf16 v[72:75], v[48:51], v[176:183], v104\n\t" \
  "s_waitcnt lgkmcnt(4)\n\t" \
  "v_smfmac_f32_16x16x64_bf16 v[68:71], v[52:55], v[84:91], v104\n\t" \
  "ds_read_b128 v[48:51], v101 offset:15360\n\t" \
  "ds_read_b128 v[20:23], v103 offset:50176\n\t" \
  "v_smfmac_f32_16x16x64_bf16 v[72:75], v[52:55], v[92:99], v104\n\t" \
  "s_waitcnt lgkmcnt(5)\n\t" \
  "v_smfmac_f32_16x16x64_bf16 v[68:71], v[56:59], a[160:167], v104\n\t" \
  "ds_read_b128 v[52:55], v101 offset:14336\n\t" \
  "ds_read_b128 v[24:27], v103 offset:51200\n\t" \
  "v_smfmac_f32_16x16x64_bf16 v[72:75], v[56:59], a[168:175], v104\n\t" \
  "ds_read_b128 v[28:31], v103 offset:52224\n\t" \
  "s_waitcnt lgkmcnt(7)\n\t" \
  "v_smfmac_f32_16x16x64_bf16 v[68:71], v[60:63], v[0:7], v104\n\t" \
  "ds_read_b128 v[32:35], v101 offset:20480\n\t" \
  "v_smfmac_f32_16x16x64_bf16 v[72:75], v[60:63], v[8:15], v104\n\t" \
  "ds_read_b128 v[36:39], v101 offset:21504\n\t" \
  "v_mov_b64 v[92:93], 0\n\t" \
  "s_waitcnt lgkmcnt(8)\n\t" \
  "v_smfmac_f32_16x16x64_bf16 v[68:71], v[64:67], v[120:127], v104\n\t" \
  "ds_read_b128 v[0:3], v102 offset:32768\n\t" \
  "ds_read_b128 v[40:43], v101 offset:22528\n\t" \
  "v_smfmac_f32_16x16x64_bf16 v[72:75], v[64:67], v[128:135], v104\n\t" \
  "ds_read_b128 v[4:7], v102 offset:33792\n\t" \
  "s_waitcnt lgkmcnt(9)\n\t" \
  "v_smfmac_f32_16x16x64_bf16 v[68:71], v[48:51], v[216:223], v104\n\t" \
  "ds_read_b128 v[44:47], v101 offset:23552\n\t" \
  "v_mov_b64 v[94:95], 0\n\t" \
  "s_waitcnt lgkmcnt(8)\n\t" \
  "v_smfmac_f32_16x16x64_bf16 v[68:71], v[52:55], v[16:23], v104\n\t" \
  "ds_read_b128 v[8:11], v102 offset:34816\n\t" \
  "v_smfmac_f32_16x16x64_bf16 v[72:75], v[48:51], v[224:231], v104\n\t" \
  "ds_read_b128 v[12:15], v102 offset:35840\n\t" \
  "s_waitcnt lgkmcnt(8)\n\t" \
  "v_smfmac_f32_16x16x64_bf16 v[72:75], v[52:55], v[24:31], v104\n\t" \
  "v_add_f32 v96, v68, v70\n\t" \
  "v_smfmac_f32_16x16x64_bf16 v[76:79], v[56:59], a[176:183], v104\n\t" \
  "v_add_f32 v97, v69, v71\n\t" \
  "v_cvt_pk_bf16_f32 v96, v96, v97\n\t" \
  "v_smfmac_f32_16x16x64_bf16 v[80:83], v[56:59], a[184:191], v104\n\t" \
  "v_add_f32 v98, v72, v74\n\t" \
  "v_add_f32 v99, v73, v75\n\t" \
  "v_smfmac_f32_16x16x64_bf16 v[76:79], v[60:63], a[240:247], v104\n\t" \
  "v_cvt_pk_bf16_f32 v98, v98, v99\n\t" \
  "ds_write2st64_b32 v105, v96, v98 offset1:1\n\t" \
  "v_smfmac_f32_16x16x64_bf16 v[80:83], v[60:63], a[248:255], v104\n\t" \
  "ds_read_b128 v[56:59], v101 offset:4096\n\t" \
  "ds_read_b128 v[16:19], v101 offset:24576\n\t" \
  "v_mov_b64 v[68:69], 0\n\t" \
  "v_smfmac_f32_16x16x64_bf16 v[76:79], v[64:67], v[136:143], v104\n\t" \
  "ds_read_b128 v[20:23], v101 offset:25600\n\t" \
  "v_mov_b64 v[70:71], 0\n\t" \
  "v_smfmac_f32_16x16x64_bf16 v[80:83], v[64:67], v[144:151], v104\n\t" \
  "ds_read_b128 v[24:27], v101 offset:26624\n\t" \
  "v_smfmac_f32_16x16x64_bf16 v[76:79], v[48:51], v[232:239], v104\n\t" \
  "s_waitcnt lgkmcnt(4)\n\t" \
  "s_barrier\n\t" \
  "ds_read_b64_tr_b16 v[84:85], v107 offset:0\n\t" \
  "v_smfmac_f32_16x16x64_bf16 v[80:83], v[48:51], v[240:247], v104\n\t" \
  "ds_read_b64_tr_b16 v[86:87], v107 offset:128\n\t" \
  "ds_read_b64_tr_b16 v[88:89], v107 offset:1024\n\t" \
  "v_smfmac_f32_16x16x64_bf16 v[76:79], v[52:55], v[200:207], v104\n\t" \
  "ds_read_b128 v[60:63], v101 offset:13312\n\t" \
  "ds_read_b64_tr_b16 v[90:91], v107 offset:1152\n\t" \
  "v_smfmac_f32_16x16x64_bf16 v[80:83], v[52:55], v[208:215], v104\n\t" \
  "ds_read_b128 v[64:67], v101 offset:6144\n\t" \
  "ds_read_b128 v[28:31], v101 offset:27648\n\t" \
  "s_waitcnt lgkmcnt(10)\n\t" \
  "v_smfmac_f32_16x16x64_bf16 v[76:79], v[56:59], v[0:7], v104\n\t" \
  "ds_read_b128 v[48:51], v101 offset:8192\n\t" \
  "s_waitcnt lgkmcnt(6)\n\t" \
  "v_smfmac_f32_16x16x64_bf16 v[92:95], v[84:87], v[32:39], v104\n\t" \
  "ds_read_b128 v[0:3], v103 offset:0\n\t" \
  "s_waitcnt lgkmcnt(4)\n\t" \
  "v_smfmac_f32_16x16x64_bf16 v[92:95], v[88:91], v[40:47], v104\n\t" \
  "ds_read_b128 v[4:7], v103 offset:1024\n\t" \
  "v_smfmac_f32_16x16x64_bf16 v[68:71], v[84:87], v[16:23], v104\n\t" \
  "s_waitcnt lgkmcnt(3)\n\t" \
  "v_smfmac_f32_16x16x64_bf16 v[68:71], v[88:91], v[24:31], v104\n\t" \
  "v_add_f32 v92, v92, v94\n\t" \
  "v_add_f32 v94, v93, v95\n\t" \
  "v_smfmac_f32_16x16x64_bf16 v[80:83], v[56:59], v[8:15], v104\n\t" \
  "ds_read_b128 v[8:11], v103 offset:2048\n\t" \
  "v_mov_b32 v97, v94\n\t" \
  "v_mov_b32_dpp v94, v92 row_ror:8 row_mask:0xf bank_mask:0x3\n\t" \
  "s_nop 0\n\t" \
  "v_mov_b32_dpp v92, v97 row_ror:8 row_mask:0xf bank_mask:0xc\n\t" \
  "v_smfmac_f32_16x16x64_bf16 v[76:79], v[60:63], v[184:191], v104\n\t" \
  "ds_read_b128 v[52:55], v101 offset:5120\n\t" \
  "ds_read_b128 v[12:15], v103 offset:3072\n\t" \
  "v_add_f32 v93, v68, v70\n\t" \
  "v_add_f32 v95, v69, v71\n\t" \
  "v_max_f32 v98, v92, v94\n\t" \
  "v_mov_b32 v97, v95\n\t" \
  "v_mov_b32_dpp v95, v93 row_ror:8 row_mask:0xf bank_mask:0x3\n\t" \
  "v_smfmac_f32_16x16x64_bf16 v[80:83], v[60:63], v[192:199], v104\n\t" \
  "v_mov_b32_dpp v93, v97 row_ror:8 row_mask:0xf bank_mask:0xc\n\t" \
  "v_max3_f32 v98, v98, v93, v95\n\t" \
  "v_smfmac_f32_16x16x64_bf16 v[76:79], v[64:67], a[144:151], v104\n\t" \
  "ds_read_b128 v[56:59], v101 offset:9216\n\t" \
  "ds_read_b128 v[32:35], v103 offset:32768\n\t" \
  "v_max_f32_dpp v98, v98, v98 quad_perm:[1,0,3,2] row_mask:0xf bank_mask:0xf\n\t" \
  "v_smfmac_f32_16x16x64_bf16 v[80:83], v[64:67], a[152:159], v104\n\t" \
  "ds_read_b128 v[60:63], v101 offset:12288\n\t" \
  "ds_read_b128 v[36:39], v103 offset:33792\n\t" \
  "v_max_f32_dpp v98, v98, v98 quad_perm:[2,3,0,1] row_mask:0xf bank_mask:0xf\n\t" \
  "s_waitcnt lgkmcnt(7)\n\t" \
  "v_smfmac_f32_16x16x64_bf16 v[76:79], v[48:51], v[0:7], v104\n\t" \
  "ds_read_b128 v[40:43], v103 offset:34816\n\t" \
  "v_max_f32_dpp v98, v98, v98 row_half_mirror row_mask:0xf bank_mask:0xf\n\t" \
  "v_mul_f32 v98, %[cs], v98\n\t" \
  "v_fma_f32 v92, v92, %[cs], -v98\n\t" \
  "v_fma_f32 v93, v93, %[cs], -v98\n\t" \
  "v_fma_f32 v94, v94, %[cs], -v98\n\t" \
  "s_waitcnt lgkmcnt(5)\n\t" \
  "v_smfmac_f32_16x16x64_bf16 v[80:83], v[48:51], v[8:15], v104\n\t" \
  "ds_read_b128 v[44:47], v103 offset:35840\n\t" \
  "v_fma_f32 v95, v95, %[cs], -v98\n\t" \
  "v_exp_f32 v72, v92\n\t" \
  "v_exp_f32 v73, v93\n\t" \
  "v_exp_f32 v74, v94\n\t" \
  "v_exp_f32 v75, v95\n\t" \
  "v_cvt_pk_bf16_f32 v68, v72, v73\n\t" \
  "v_cvt_pk_bf16_f32 v69, v74, v75\n\t" \
  "buffer_store_dwordx2 v[68:69], v110, %[rh], s47 offen sc1\n\t" \
  "v_smfmac_f32_16x16x64_bf16 v[76:79], v[52:55], a[128:135], v104\n\t" \
  "v_smfmac_f32_16x16x64_bf16 v[80:83], v[52:55], a[136:143], v104\n\t" \
  "s_waitcnt lgkmcnt(5)\n\t" \
  "v_smfmac_f32_16x16x64_bf16 v[76:79], v[56:59], a[224:231], v104\n\t" \
  "s_waitcnt lgkmcnt(2)\n\t" \
  "v_smfmac_f32_16x16x64_bf16 v[76:79], v[60:63], v[32:39], v104\n\t" \
  "v_smfmac_f32_16x16x64_bf16 v[80:83], v[56:59], a[232:239], v104\n\t" \
  "s_waitcnt lgkmcnt(0)\n\t" \
  "v_smfmac_f32_16x16x64_bf16 v[80:83], v[60:63], v[40:47], v104\n\t" \
  "v_add_f32 v20, v76, v78\n\t" \
  "v_add_f32 v21, v77, v79\n\t" \
  "v_cvt_pk_bf16_f32 v20, v20, v21\n\t" \
  "ds_write_b32 v106, v20\n\t" \
  "s_nop 0\n\t" \
  "s_nop 0\n\t" \
  "s_nop 0\n\t" \
  "s_nop 0\n\t" \
  "v_add_f32 v23, v80, v82\n\t" \
  "v_add_f32 v21, v81, v83\n\t" \
  "v_cvt_pk_bf16_f32 v21, v23, v21\n\t" \
  "ds_write_b32 v106, v21 offset:256\n\t" \
  "s_waitcnt lgkmcnt(0)\n\t" \
  "s_barrier\n\t" \
  "ds_read_b64_tr_b16 v[4:5], v108\n\t" \
  "ds_read_b64_tr_b16 v[6:7], v108 offset:64\n\t" \
  "ds_read_b64_tr_b16 v[8:9], v108 offset:512\n\t" \
  "ds_read_b64_tr_b16 v[10:11], v108 offset:576\n\t" \
  "ds_read_b64_tr_b16 v[12:13], v108 offset:1024\n\t" \
  "ds_read_b64_tr_b16 v[14:15], v108 offset:1088\n\t" \
  "ds_read_b64_tr_b16 v[16:17], v108 offset:1536\n\t" \
  "ds_read_b64_tr_b16 v[18:19], v108 offset:1600\n\t" \
  "v_mov_b64 v[0:1], 0\n\t" \
  "v_mov_b64 v[2:3], 0\n\t" \
  "v_add_f32 v29, v72, v73\n\t" \
  "v_add_f32 v29, v29, v74\n\t" \
  "s_waitcnt lgkmcnt(4)\n\t" \
  "v_smfmac_f32_16x16x64_bf16 v[0:3], v[84:87], v[4:11], v104\n\t" \
  "s_waitcnt lgkmcnt(0)\n\t" \
  "v_smfmac_f32_16x16x64_bf16 v[0:3], v[88:91], v[12:19], v104\n\t" \
  "v_add_f32 v29, v29, v75\n\t" \
  "s_mov_b32 s76, 0xaaaaaaaa\n\t" \
  "s_mov_b32 s77, s76\n\t" \
  "s_nop 4\n\t" \
  "v_add_f32 v96, v0, v2\n\t" \
  "v_add_f32_dpp v96, v1, v3 quad_perm:[0,1,2,3] row_mask:0xf bank_mask:0xc\n\t" \
  "v_fma_f32 v96, v96, %[cs], -v98\n\t" \
  "v_exp_f32 v24, v96\n\t" \
  "s_nop 0\n\t" \
  "v_add_f32 v29, v29, v24\n\t" \
  "s_nop 0\n\t" \
  "v_mov_b32_dpp v25, v24 quad_perm:[1,0,3,2] row_mask:0xf bank_mask:0xf\n\t" \
  "v_add_f32_dpp v29, v29, v29 quad_perm:[1,0,3,2] row_mask:0xf bank_mask:0xf\n\t" \
  "v_cndmask_b32_e64 v24, v24, v114, s[76:77]\n\t" \
  "v_cndmask_b32_e64 v25, v25, v115, s[76:77]\n\t" \
  "v_add_f32_dpp v29, v29, v29 quad_perm:[2,3,0,1] row_mask:0xf bank_mask:0xf\n\t" \
  "s_nop 1\n\t" \
  "v_add_f32_dpp v29, v29, v29 row_half_mirror row_mask:0xf bank_mask:0xf\n\t" \
  "v_rcp_f32 v26, v29\n\t" \
  "v_cmpx_gt_f32 vcc, 0x5d5e0b6b, v29\n\t" \
  "v_mul_f32 v24, v24, v26\n\t" \
  "v_mul_f32 v25, v25, v26\n\t" \
  "v_cvt_pk_bf16_f32 v24, v24, v25\n\t" \
  "buffer_store_dword v24, v111, %[rh], s47 offen sc1\n\t" \
  "s_memtime s[76:77]\n\t" \
  "s_mov_b64 exec, -1\n\t" \
  "v_mov_b32 v97, v29\n\t" \
  "s_cmp_eq_u64 vcc, -1\n\t" \
  "s_cselect_b32 s42, 0, 8\n\t" \
  "s_waitcnt lgkmcnt(0)\n\t" \
  "s_mov_b32 s57, s76\n\t" \
  "s_barrier\n\t" \
  "s_add_i32 s41, s56, 1950\n\t" \
  "s_sub_i32 s76, s57, s56\n\t" \
  "s_add_i32 s77, s43, 8\n\t" \
  "s_min_u32 s76, s76, s77\n\t" \
  "s_sub_i32 s43, s43, 4\n\t" \
  "s_max_u32 s43, s43, s76\n\t" \
  "s_add_i32 s76, s45, 0x4000\n\t" \
  "s_and_b32 s76, s76, 0xc000\n\t" \
  "v_mov_b32 v116, -1\n\t" \
  "buffer_store_dword v116, v109, %[rh], s76 offen sc1\n\t" \
  "s_sub_i32 s76, s92, s51\n\t" \
  "s_sub_i32 s77, s93, s53\n\t" \
  "s_cmp_eq_u32 s76, 0\n\t" \
  "s_cbranch_scc1 21f\n\t" \
  "v_cvt_f32_u32 v117, s76\n\t" \
  "v_cvt_f32_u32 v116, s77\n\t" \
  "v_rcp_f32 v117, v117\n\t" \
  "s_mov_b32 s51, s92\n\t" \
  "v_mul_f32 v117, v117, v116\n\t" \
  "v_max_f32 v117, 0x3f028f5c, v117\n\t" \
  "v_min_f32 v117, 0x3ffeb852, v117\n\t" \
  "v_rcp_f32 v116, v117\n\t" \
  "v_mul_f32 v117, 0x4f000000, v117\n\t" \
  "v_mul_f32 v116, 0x47800000, v116\n\t" \
  "v_cvt_u32_f32 v117, v117\n\t" \
  "v_cvt_u32_f32 v116, v116\n\t" \
  "s_mov_b32 s53, s93\n\t" \
  "v_readfirstlane_b32 s46, v117\n\t" \
  "v_readfirstlane_b32 s52, v116\n\t" \
  "21:\n\t" \
  "s_add_i32 s44, s44, 1\n\t" \
  "s_and_b32 s58, s58, 4\n\t" \
  "s_and_b32 s76, s44, 64\n\t" \
  "s_or_b32 s42, s42, s76\n\t" \
  "s_cbranch_scc0 20b\n\t" \
  "s_branch 99f\n\t" \
  "14:\n\t" \
  "s_sleep 3\n\t" \
  "s_add_u32 s59, s59, 0x10000\n\t" \
  "s_cmp_lt_u32 s59, 0x100000\n\t" \
  "s_cbranch_scc1 13b\n\t" \
  "s_mov_b64 exec, -1\n\t" \
  "s_mov_b32 s42, 16\n\t" \
  "s_branch 99f\n\t" \
  "8:\n\t" \
  "s_bitcmp1_b32 s58, 1\n\t" \
  "s_cbranch_scc1 4b\n\t" \
  "s_bitcmp1_b32 s58, 2\n\t" \
  "s_cbranch_scc1 5b\n\t" \
  "v_max3_u32 v116, v32, v33, v34\n\t" \
  "v_max3_u32 v117, v35, v36, v37\n\t" \
  "v_max3_u32 v116, v116, v38, v39\n\t" \
  "v_max_u32 v116, v116, v117\n\t" \
  "v_cmp_eq_u32 vcc, -1, v116\n\t" \
  "s_cbranch_vccz 5b\n\t" \
  "s_branch 0f\n\t" \
  "9:\n\t" \
  "s_bitcmp1_b32 s58, 2\n\t" \
  "s_cbranch_scc1 6b\n\t" \
  "v_max3_u32 v116, v40, v41, v42\n\t" \
  "v_max3_u32 v117, v43, v44, v45\n\t" \
  "v_max3_u32 v116, v116, v46, v47\n\t" \
  "v_max_u32 v116, v116, v117\n\t" \
  "v_cmp_eq_u32 vcc, -1, v116\n\t" \
  "s_cbranch_vccz 6b\n\t" \
  "s_or_b32 s58, s58, 2\n\t" \
  "0:\n\t" \
  "s_or_b32 s58, s58, 1\n\t" \
  "11:\n\t" \
  "buffer_load_dwordx4 v[32:35], v100, %[rh], s40 offen sc1\n\t" \
  "buffer_load_dwordx4 v[36:39], v100, %[rh], s76 offen sc1\n\t" \
  "buffer_load_dwordx4 v[40:43], v100, %[rh], s77 offen sc1\n\t" \
  "buffer_load_dwordx4 v[44:47], v100, %[rh], s78 offen sc1\n\t" \
  "s_waitcnt lgkmcnt(0)\n\t" \
  "s_branch 10b\n\t" \
  "17:\n\t" \
  "s_add_u32 s59, s59, 1\n\t" \
  "s_bitcmp1_b32 s58, 2\n\t" \
  "s_cbranch_scc1 3b\n\t" \
  "s_waitcnt vmcnt(0)\n\t" \
  "v_max_u32 v116, v32, v33\n\t" \
  "v_max3_u32 v116, v116, v34, v35\n\t" \
  "v_max3_u32 v116, v116, v36, v37\n\t" \
  "v_max3_u32 v116, v116, v38, v39\n\t" \
  "v_max3_u32 v116, v116, v40, v41\n\t" \
  "v_max3_u32 v116, v116, v42, v43\n\t" \
  "v_max3_u32 v116, v116, v44, v45\n\t" \
  "v_max3_u32 v116, v116, v46, v47\n\t" \
  "v_cmp_eq_u32 vcc, -1, v116\n\t" \
  "s_cbranch_vccz 3b\n\t" \
  "s_bitcmp1_b32 s59, 6\n\t" \
  "s_cbranch_scc1 12f\n\t" \
  "buffer_load_dwordx4 v[32:35], v100, %[rh], s40 offen sc1\n\t" \
  "buffer_load_dwordx4 v[36:39], v100, %[rh], s76 offen sc1\n\t" \
  "buffer_load_dwordx4 v[40:43], v100, %[rh], s77 offen sc1\n\t" \
  "buffer_load_dwordx4 v[44:47], v100, %[rh], s78 offen sc1\n\t" \
  "s_branch 17b\n\t" \
  "12:\n\t" \
  "s_mov_b32 s42, s58\n\t" \
  "s_branch 99f\n\t" \
  "91:\n\t" \
  "s_mov_b32 s58, s42\n\t" \
  "s_mov_b32 s95, 0\n\t" \
  "20:\n\t" \
  "s_and_b32 s76, s44, 7\n\t" \
  "s_lshl_b32 s76, s76, 15\n\t" \
  "s_add_i32 s40, s54, s76\n\t" \
  "s_and_b32 s76, s44, 3\n\t" \
  "s_lshl_b32 s45, s76, 14\n\t" \
  "s_add_i32 s47, s44, 1\n\t" \
  "s_min_u32 s47, s47, 63\n\t" \
  "s_lshr_b32 s76, s47, 1\n\t" \
  "s_lshl_b32 s47, s47, 20\n\t" \
  "s_add_i32 s47, s47, s55\n\t" \
  "s_bitcmp1_b64 %[sel], s76\n\t" \
  "s_cselect_b32 s76, 0x2000, 0\n\t" \
  "s_add_i32 s47, s47, s76\n\t" \
  "s_lshr_b32 s76, s44, 1\n\t" \
  "s_lshl_b32 s94, s44, 20\n\t" \
  "s_bitcmp1_b64 %[sel], s76\n\t" \
  "s_cselect_b32 s76, 0x2000, 0\n\t" \
  "s_add_i32 s94, s94, s76\n\t" \
  "s_add_i32 s49, s44, -1\n\t" \
  "s_and_b32 s48, s49, 2\n\t" \
  "s_lshl_b32 s48, s48, 13\n\t" \
  "s_and_b32 s76, s49, 1\n\t" \
  "s_lshl_b32 s76, s76, 12\n\t" \
  "s_add_i32 s48, s48, s76\n\t" \
  "s_lshl_b32 s50, s53, 8\n\t" \
  "s_mov_b32 s59, 0\n\t" \
  "s_mov_b32 s92, s51\n\t" \
  "s_mov_b32 s93, s53\n\t" \
  "s_xor_b32 s76, s40, 0x400\n\t" \
  "s_xor_b32 s77, s40, 0x800\n\t" \
  "s_xor_b32 s78, s40, 0xc00\n\t" \
  "s_cmp_lg_u32 s58, 0\n\t" \
  "s_cbranch_scc1 11f\n\t" \
  "10:\n\t" \
  "ds_read_b128 v[48:51], v102 offset:32768\n\t" \
  "ds_read_b128 v[52:55], v102 offset:33792\n\t" \
  "ds_read_b128 v[56:59], v102 offset:34816\n\t" \
  "ds_read_b128 v[60:63], v102 offset:35840\n\t" \
  "ds_read_b128 v[16:19], v102 offset:49152\n\t" \
  "ds_read_b128 v[20:23], v102 offset:50176\n\t" \
  "ds_read_b128 v[24:27], v102 offset:51200\n\t" \
  "ds_read_b128 v[28:31], v102 offset:52224\n\t" \
  "ds_read_b128 v[84:87], v103 offset:16384\n\t" \
  "ds_read_b128 v[88:91], v103 offset:17408\n\t" \
  "ds_read_b128 v[92:95], v103 offset:18432\n\t" \
  "ds_read_b128 v[96:99], v103 offset:19456\n\t" \
  "s_waitcnt lgkmcnt(4)\n\t" \
  "ds_read_b128 v[0:3], v103 offset:49152\n\t" \
  "ds_read_b128 v[4:7], v103 offset:50176\n\t" \
  "ds_read_b128 v[8:11], v103 offset:51200\n\t" \
  "ds_read_b128 v[12:15], v103 offset:52224\n\t" \
  "s_bitcmp1_b32 s58, 1\n\t" \
  "s_cselect_b64 s[80:81], -1, 0\n\t" \
  "s_cmp_lg_u32 s58, 0\n\t" \
  "s_cbranch_scc1 17f\n\t" \
  "s_mov_b32 exec_hi, 0\n\t" \
  "s_cmp_eq_u32 s95, 0\n\t" \
  "s_cbranch_scc1 16f\n\t" \
  "s_waitcnt vmcnt(2)\n\t" \
  "v_mov_b32 v116, v118\n\t" \
  "s_branch 15f\n\t" \
  "16:\n\t" \
  "s_memtime vcc\n\t" \
  "s_waitcnt lgkmcnt(0)\n\t" \
  "s_sub_i32 s79, s41, vcc_lo\n\t" \
  "s_cmp_gt_i32 s79, 64\n\t" \
  "s_cbranch_scc1 16b\n\t" \
  "13:\n\t" \
  "buffer_load_dword v116, v112, %[rh], s48 offen sc1\n\t" \
  "s_waitcnt vmcnt(0)\n\t" \
  "15:\n\t" \
  "v_mov_b32 v117, 20000\n\t" \
  "v_cmp_ne_u32_sdwa vcc, v116, s49 src0_sel:BYTE_0 src1_sel:DWORD\n\t" \
  "v_subrev_u32 v116, s50, v116\n\t" \
  "v_ashrrev_i32 v116, 8, v116\n\t" \
  "s_nop 1\n\t" \
  "v_max_i32_dpp v116, v116, v116 quad_perm:[1,0,3,2] row_mask:0xf bank_mask:0xf\n\t" \
  "s_cbranch_vccnz 14f\n\t" \
  "s_nop 0\n\t" \
  "v_max_i32_dpp v116, v116, v116 quad_perm:[2,3,0,1] row_mask:0xf bank_mask:0xf\n\t" \
  "s_nop 1\n\t" \
  "v_max_i32_dpp v116, v116, v116 row_half_mirror row_mask:0xf bank_mask:0xf\n\t" \
  "s_nop 1\n\t" \
  "v_max_i32_dpp v116, v116, v116 row_mirror row_mask:0xf bank_mask:0xf\n\t" \
  "v_med3_i32 v116, v116, 0, v117\n\t" \
  "v_mul_u32_u24 v116, s52, v116\n\t" \
  "v_lshrrev_b32 v116, 16, v116\n\t" \
  "s_mov_b64 exec, -1\n\t" \
  "v_readfirstlane_b32 s84, v116\n\t" \
  "v_readlane_b32 s85, v116, 16\n\t" \
  "s_max_u32 s84, s84, s85\n\t" \
  "s_add_i32 s84, s84, s51\n\t" \
  "1:\n\t" \
  "s_memtime vcc\n\t" \
  "s_waitcnt lgkmcnt(0)\n\t" \
  "s_sub_i32 s79, s84, vcc_lo\n\t" \
  "s_cmp_gt_i32 s79, 128\n\t" \
  "s_cbranch_scc1 1b\n\t" \
  ".p2align 4\n\t" \
  "2:\n\t" \
  "s_sub_i32 s79, s79, 33\n\t" \
  "s_cmp_gt_i32 s79, 56\n\t" \
  "s_cbranch_scc1 2b\n\t" \
  "buffer_load_dwordx4 v[32:35], v100, %[rh], s40 offen sc1\n\t" \
  "s_nop 12\n\t" \
  "buffer_load_dwordx4 v[36:39], v100, %[rh], s76 offen sc1\n\t" \
  "s_nop 12\n\t" \
  "buffer_load_dwordx4 v[40:43], v100, %[rh], s77 offen sc1\n\t" \
  "s_nop 12\n\t" \
  "buffer_load_dwordx4 v[44:47], v100, %[rh], s78 offen sc1\n\t" \
  "s_memtime vcc\n\t" \
  "s_memrealtime s[82:83]\n\t" \
  "s_nop 4\n\t" \
  "s_memrealtime s[84:85]\n\t" \
  "s_nop 4\n\t" \
  "s_memrealtime s[56:57]\n\t" \
  "s_nop 4\n\t" \
  "s_memrealtime s[86:87]\n\t" \
  "s_nop 4\n\t" \
  "s_memrealtime s[88:89]\n\t" \
  "s_nop 4\n\t" \
  "s_memrealtime s[90:91]\n\t" \
  "s_mov_b32 s92, 0\n\t" \
  "s_waitcnt lgkmcnt(0)\n\t" \
  "s_cmp_eq_u32 s82, s84\n\t" \
  "s_addc_u32 s92, s92, 0\n\t" \
  "s_cmp_eq_u32 s82, s56\n\t" \
  "s_addc_u32 s92, s92, 0\n\t" \
  "s_cmp_eq_u32 s82, s86\n\t" \
  "s_addc_u32 s92, s92, 0\n\t" \
  "s_cmp_eq_u32 s82, s88\n\t" \
  "s_addc_u32 s92, s92, 0\n\t" \
  "s_cmp_eq_u32 s82, s90\n\t" \
  "s_addc_u32 s92, s92, 0\n\t" \
  "s_mul_i32 s92, s92, 24\n\t" \
  "s_add_i32 s92, s92, vcc_lo\n\t" \
  "s_lshl4_add_u32 s93, s82, 64\n\t" \
  "3:\n\t" \
  "v_mov_b64 v[68:69], 0\n\t" \
  "v_mov_b64 v[70:71], 0\n\t" \
  "v_mov_b64 v[72:73], 0\n\t" \
  "v_mov_b64 v[74:75], 0\n\t" \
  "v_mov_b64 v[76:77], 0\n\t" \
  "v_mov_b64 v[78:79], 0\n\t" \
  "v_mov_b64 v[80:81], 0\n\t" \
  "v_mov_b64 v[82:83], 0\n\t" \
  "s_waitcnt lgkmcnt(0)\n\t" \
  "s_waitcnt vmcnt(3)\n\t" \
  "v_smfmac_f32_16x16x64_bf16 v[68:71], v[32:35], a[96:103], v104\n\t" \
  "ds_write_b128 v101, v[32:35] offset:4096\n\t" \
  "v_smfmac_f32_16x16x64_bf16 v[72:75], v[32:35], a[104:111], v104\n\t" \
  "v_smfmac_f32_16x16x64_bf16 v[76:79], v[32:35], v[48:55], v104\n\t" \
  "v_smfmac_f32_16x16x64_bf16 v[80:83], v[32:35], v[56:63], v104\n\t" \
  "s_waitcnt vmcnt(2)\n\t" \
  "v_smfmac_f32_16x16x64_bf16 v[68:71], v[36:39], a[112:119], v104\n\t" \
  "ds_write_b128 v101, v[36:39] offset:5120\n\t" \
  "v_smfmac_f32_16x16x64_bf16 v[72:75], v[36:39], a[120:127], v104\n\t" \
  "v_smfmac_f32_16x16x64_bf16 v[76:79], v[36:39], a[128:135], v104\n\t" \
  "v_cmp_u_f32 vcc, v68, v69\n\t" \
  "v_smfmac_f32_16x16x64_bf16 v[80:83], v[36:39], a[136:143], v104\n\t" \
  "v_cmp_u_f32 s[82:83], v70, v71\n\t" \
  "s_waitcnt vmcnt(1)\n\t" \
  "v_smfmac_f32_16x16x64_bf16 v[68:71], v[40:43], v[16:23], v104\n\t" \
  "ds_write_b128 v101, v[40:43] offset:6144\n\t" \
  "v_smfmac_f32_16x16x64_bf16 v[72:75], v[40:43], v[24:31], v104\n\t" \
  "s_or_b64 vcc, vcc, s[82:83]\n\t" \
  "s_or_b64 vcc, vcc, s[80:81]\n\t" \
  "s_waitcnt lgkmcnt(1)\n\t" \
  "v_smfmac_f32_16x16x64_bf16 v[76:79], v[40:43], a[144:151], v104\n\t" \
  "s_cbranch_scc1 8f\n\t" \
  "5:\n\t" \
  "s_barrier\n\t" \
  "4:\n\t" \
  "s_memtime s[56:57]\n\t" \
  "ds_read_b128 v[48:51], v101 offset:8192\n\t" \
  "v_smfmac_f32_16x16x64_bf16 v[80:83], v[40:43], a[152:159], v104\n\t" \
  "ds_read_b128 v[52:55], v101 offset:9216\n\t" \
  "ds_read_b128 v[56:59], v101 offset:12288\n\t" \
  "s_waitcnt vmcnt(0)\n\t" \
  "v_smfmac_f32_16x16x64_bf16 v[68:71], v[44:47], a[160:167], v104\n\t" \
  "ds_write_b128 v101, v[44:47] offset:7168\n\t" \
  "v_smfmac_f32_16x16x64_bf16 v[72:75], v[44:47], a[168:175], v104\n\t" \
  "v_smfmac_f32_16x16x64_bf16 v[76:79], v[44:47], a[176:183], v104\n\t" \
  "v_cmp_u_f32 vcc, v68, v69\n\t" \
  "v_smfmac_f32_16x16x64_bf16 v[80:83], v[44:47], a[184:191], v104\n\t" \
  "v_cmp_u_f32 s[82:83], v70, v71\n\t" \
  "s_waitcnt lgkmcnt(3)\n\t" \
  "v_smfmac_f32_16x16x64_bf16 v[68:71], v[48:51], a[192:199], v104\n\t" \
  "ds_read_b128 v[60:63], v101 offset:13312\n\t" \
  "ds_read_b128 v[64:67], v101 offset:0\n\t" \
  "v_smfmac_f32_16x16x64_bf16 v[72:75], v[48:51], a[200:207], v104\n\t" \
  "s_or_b64 vcc, vcc, s[82:83]\n\t" \
  "s_waitcnt lgkmcnt(2)\n\t" \
  "v_smfmac_f32_16x16x64_bf16 v[68:71], v[52:55], a[208:215], v104\n\t" \
  "s_cbranch_scc1 9f\n\t" \
  "6:\n\t" \
  "s_barrier\n\t" \
  "v_smfmac_f32_16x16x64_bf16 v[72:75], v[52:55], a[216:223], v104\n\t" \
  "ds_read_b128 v[48:51], v101 offset:1024\n\t" \
  "v_smfmac_f32_16x16x64_bf16 v[68:71], v[56:59], v[152:159], v104\n\t" \
  "ds_read_b128 v[52:55], v101 offset:10240\n\t" \
  "v_smfmac_f32_16x16x64_bf16 v[72:75], v[56:59], v[160:167], v104\n\t" \
  "s_waitcnt lgkmcnt(3)\n\t" \
  "v_smfmac_f32_16x16x64_bf16 v[68:71], v[60:63], v[168:175], v104\n\t" \
  "ds_read_b128 v[56:59], v101 offset:11264\n\t" \
  "global_load_dword v119, v109, %[kc] sc1\n\t" \
  "v_smfmac_f32_16x16x64_bf16 v[72:75], v[60:63], v[176:183], v104\n\t" \
  "s_waitcnt lgkmcnt(3)\n\t" \
  "v_smfmac_f32_16x16x64_bf16 v[68:71], v[64:67], a[0:7], v104\n\t" \
  "ds_read_b128 v[60:63], v101 offset:14336\n\t" \
  "v_smfmac_f32_16x16x64_bf16 v[72:75], v[64:67], a[8:15], v104\n\t" \
  "s_waitcnt lgkmcnt(3)\n\t" \
  "v_smfmac_f32_16x16x64_bf16 v[68:71], v[48:51], a[16:23], v104\n\t" \
  "ds_read_b128 v[64:67], v101 offset:15360\n\t" \
  "ds_read_b128 v[16:19], v102 offset:16384\n\t" \
  "v_smfmac_f32_16x16x64_bf16 v[72:75], v[48:51], a[24:31], v104\n\t" \
  "s_waitcnt lgkmcnt(4)\n\t" \
  "v_smfmac_f32_16x16x64_bf16 v[68:71], v[52:55], v[84:91], v104\n\t" \
  "ds_read_b128 v[48:51], v101 offset:3072\n\t" \
  "ds_read_b128 v[20:23], v102 offset:17408\n\t" \
  "v_smfmac_f32_16x16x64_bf16 v[72:75], v[52:55], v[92:99], v104\n\t" \
  "s_waitcnt lgkmcnt(5)\n\t" \
  "v_smfmac_f32_16x16x64_bf16 v[68:71], v[56:59], v[120:127], v104\n\t" \
  "ds_read_b128 v[52:55], v101 offset:2048\n\t" \
  "ds_read_b128 v[24:27], v102 offset:18432\n\t" \
  "v_smfmac_f32_16x16x64_bf16 v[72:75], v[56:59], v[128:135], v104\n\t" \
  "ds_read_b128 v[28:31], v102 offset:19456\n\t" \
  "buffer_load_dwordx4 v[84:87], v107, %[ru], s94 offen offset:0 sc1\n\t" \
  "s_waitcnt lgkmcnt(7)\n\t" \
  "v_smfmac_f32_16x16x64_bf16 v[68:71], v[60:63], v[0:7], v104\n\t" \
  "v_smfmac_f32_16x16x64_bf16 v[72:75], v[60:63], v[8:15], v104\n\t" \
  "buffer_load_dwordx4 v[88:91], v107, %[ru], s94 offen offset:1024 sc1\n\t" \
  "s_waitcnt lgkmcnt(6)\n\t" \
  "v_smfmac_f32_16x16x64_bf16 v[68:71], v[64:67], v[216:223], v104\n\t" \
  "ds_read_b128 v[0:3], v103 offset:0\n\t" \
  "v_smfmac_f32_16x16x64_bf16 v[72:75], v[64:67], v[224:231], v104\n\t" \
  "ds_read_b128 v[4:7], v103 offset:1024\n\t" \
  "buffer_load_dwordx4 v[92:95], v107, %[ru], s94 offen offset:2048 sc1\n\t" \
  "s_waitcnt lgkmcnt(6)\n\t" \
  "v_smfmac_f32_16x16x64_bf16 v[68:71], v[48:51], a[64:71], v104\n\t" \
  "s_waitcnt lgkmcnt(4)\n\t" \
  "v_smfmac_f32_16x16x64_bf16 v[68:71], v[52:55], v[16:23], v104\n\t" \
  "ds_read_b128 v[8:11], v103 offset:2048\n\t" \
  "buffer_load_dwordx4 v[96:99], v107, %[ru], s94 offen offset:3072 sc1\n\t" \
  "v_smfmac_f32_16x16x64_bf16 v[72:75], v[48:51], a[72:79], v104\n\t" \
  "ds_read_b128 v[12:15], v103 offset:3072\n\t" \
  "s_waitcnt lgkmcnt(4)\n\t" \
  "v_smfmac_f32_16x16x64_bf16 v[72:75], v[52:55], v[24:31], v104\n\t" \
  "v_add_f32 v44, v68, v70\n\t" \
  "v_smfmac_f32_16x16x64_bf16 v[76:79], v[56:59], v[136:143], v104\n\t" \
  "v_add_f32 v45, v69, v71\n\t" \
  "v_cvt_pk_bf16_f32 v44, v44, v45\n\t" \
  "v_smfmac_f32_16x16x64_bf16 v[80:83], v[56:59], v[144:151], v104\n\t" \
  "v_add_f32 v46, v72, v74\n\t" \
  "v_add_f32 v47, v73, v75\n\t" \
  "v_smfmac_f32_16x16x64_bf16 v[76:79], v[60:63], v[200:207], v104\n\t" \
  "v_cvt_pk_bf16_f32 v46, v46, v47\n\t" \
  "ds_write2st64_b32 v105, v44, v46 offset1:1\n\t" \
  "v_smfmac_f32_16x16x64_bf16 v[80:83], v[60:63], v[208:215], v104\n\t" \
  "ds_read_b128 v[56:59], v101 offset:8192\n\t" \
  "v_smfmac_f32_16x16x64_bf16 v[76:79], v[64:67], v[232:239], v104\n\t" \
  "ds_read_b128 v[16:19], v103 offset:32768\n\t" \
  "v_smfmac_f32_16x16x64_bf16 v[80:83], v[64:67], v[240:247], v104\n\t" \
  "ds_read_b128 v[20:23], v103 offset:33792\n\t" \
  "v_smfmac_f32_16x16x64_bf16 v[76:79], v[48:51], a[80:87], v104\n\t" \
  "s_waitcnt lgkmcnt(3)\n\t" \
  "s_barrier\n\t" \
  "v_smfmac_f32_16x16x64_bf16 v[80:83], v[48:51], a[88:95], v104\n\t" \
  "v_smfmac_f32_16x16x64_bf16 v[76:79], v[52:55], a[48:55], v104\n\t" \
  "ds_read_b128 v[60:63], v101 offset:1024\n\t" \
  "ds_read_b128 v[24:27], v103 offset:34816\n\t" \
  "v_smfmac_f32_16x16x64_bf16 v[80:83], v[52:55], a[56:63], v104\n\t" \
  "ds_read_b128 v[64:67], v101 offset:10240\n\t" \
  "s_waitcnt lgkmcnt(5)\n\t" \
  "v_smfmac_f32_16x16x64_bf16 v[76:79], v[56:59], v[0:7], v104\n\t" \
  "ds_read_b128 v[48:51], v101 offset:12288\n\t" \
  "ds_read_b128 v[28:31], v103 offset:35840\n\t" \
  "v_smfmac_f32_16x16x64_bf16 v[80:83], v[56:59], v[8:15], v104\n\t" \
  "buffer_load_dwordx4 v[32:35], v100, %[ru], s47 offen offset:0 sc1\n\t" \
  "s_waitcnt lgkmcnt(4)\n\t" \
  "v_smfmac_f32_16x16x64_bf16 v[76:79], v[60:63], a[32:39], v104\n\t" \
  "ds_read_b128 v[52:55], v101 offset:9216\n\t" \
  "v_smfmac_f32_16x16x64_bf16 v[80:83], v[60:63], a[40:47], v104\n\t" \
  "ds_read_b128 v[0:3], v102 offset:0\n\t" \
  "s_waitcnt lgkmcnt(4)\n\t" \
  "v_smfmac_f32_16x16x64_bf16 v[76:79], v[64:67], a[240:247], v104\n\t" \
  "ds_read_b128 v[56:59], v101 offset:13312\n\t" \
  "buffer_load_dwordx4 v[36:39], v100, %[ru], s47 offen offset:1024 sc1\n\t" \
  "v_smfmac_f32_16x16x64_bf16 v[80:83], v[64:67], a[248:255], v104\n\t" \
  "ds_read_b128 v[60:63], v101 offset:0\n\t" \
  "ds_read_b128 v[4:7], v102 offset:1024\n\t" \
  "s_waitcnt lgkmcnt(6)\n\t" \
  "v_smfmac_f32_16x16x64_bf16 v[76:79], v[48:51], v[16:23], v104\n\t" \
  "ds_read_b128 v[8:11], v102 offset:2048\n\t" \
  "s_waitcnt lgkmcnt(6)\n\t" \
  "v_smfmac_f32_16x16x64_bf16 v[80:83], v[48:51], v[24:31], v104\n\t" \
  "ds_read_b128 v[12:15], v102 offset:3072\n\t" \
  "buffer_load_dwordx4 v[40:43], v100, %[ru], s47 offen offset:2048 sc1\n\t" \
  "s_waitcnt lgkmcnt(6)\n\t" \
  "v_smfmac_f32_16x16x64_bf16 v[76:79], v[52:55], a[224:231], v104\n\t" \
  "v_smfmac_f32_16x16x64_bf16 v[80:83], v[52:55], a[232:239], v104\n\t" \
  "s_waitcnt lgkmcnt(4)\n\t" \
  "v_smfmac_f32_16x16x64_bf16 v[76:79], v[56:59], v[184:191], v104\n\t" \
  "s_waitcnt lgkmcnt(2)\n\t" \
  "v_smfmac_f32_16x16x64_bf16 v[76:79], v[60:63], v[0:7], v104\n\t" \
  "v_smfmac_f32_16x16x64_bf16 v[80:83], v[56:59], v[192:199], v104\n\t" \
  "s_waitcnt lgkmcnt(0)\n\t" \
  "v_smfmac_f32_16x16x64_bf16 v[80:83], v[60:63], v[8:15], v104\n\t" \
  "v_add_f32 v20, v76, v78\n\t" \
  "v_add_f32 v21, v77, v79\n\t" \
  "v_cvt_pk_bf16_f32 v20, v20, v21\n\t" \
  "ds_write_b32 v106, v20\n\t" \
  "s_nop 3\n\t" \
  "v_add_f32 v23, v80, v82\n\t" \
  "v_add_f32 v21, v81, v83\n\t" \
  "v_cvt_pk_bf16_f32 v21, v23, v21\n\t" \
  "ds_write_b32 v106, v21 offset:256\n\t" \
  "s_waitcnt lgkmcnt(0)\n\t" \
  "s_barrier\n\t" \
  "v_bfe_i32 v64, v114, 0, 1\n\t" \
  "v_and_b32 v64, 0xff800000, v64\n\t" \
  "v_bfe_i32 v65, v114, 1, 1\n\t" \
  "v_and_b32 v65, 0xff800000, v65\n\t" \
  "v_bfe_i32 v66, v114, 2, 1\n\t" \
  "v_and_b32 v66, 0xff800000, v66\n\t" \
  "v_bfe_i32 v67, v114, 3, 1\n\t" \
  "v_and_b32 v67, 0xff800000, v67\n\t" \
  "v_bfe_i32 v68, v114, 4, 1\n\t" \
  "v_and_b32 v68, 0xff800000, v68\n\t" \
  "v_bfe_i32 v69, v114, 5, 1\n\t" \
  "v_and_b32 v69, 0xff800000, v69\n\t" \
  "v_bfe_i32 v70, v114, 6, 1\n\t" \
  "v_and_b32 v70, 0xff800000, v70\n\t" \
  "v_bfe_i32 v71, v114, 7, 1\n\t" \
  "v_and_b32 v71, 0xff800000, v71\n\t" \
  "s_lshl_b32 s94, s45, 4\n\t" \
  "s_and_b32 s96, s44, 2\n\t" \
  "s_lshl_b32 s96, s96, 13\n\t" \
  "s_and_b32 s76, s44, 1\n\t" \
  "s_lshl_b32 s76, s76, 12\n\t" \
  "s_add_i32 s96, s96, s76\n\t" \
  "s_mov_b32 s95, 1\n\t" \
  "s_sleep 1\n\t" \
  "ds_read_b128 v[0:3], v101 offset:20480\n\t" \
  "ds_read_b128 v[4:7], v101 offset:21504\n\t" \
  "ds_read_b128 v[8:11], v101 offset:22528\n\t" \
  "ds_read_b128 v[12:15], v101 offset:23552\n\t" \
  "ds_read_b128 v[16:19], v101 offset:24576\n\t" \
  "ds_read_b128 v[20:23], v101 offset:25600\n\t" \
  "ds_read_b128 v[24:27], v101 offset:26624\n\t" \
  "ds_read_b128 v[28:31], v101 offset:27648\n\t" \
  "ds_read_b64_tr_b16 v[48:49], v108 offset:0\n\t" \
  "ds_read_b64_tr_b16 v[50:51], v108 offset:64\n\t" \
  "ds_read_b64_tr_b16 v[52:53], v108 offset:512\n\t" \
  "ds_read_b64_tr_b16 v[54:55], v108 offset:576\n\t" \
  "ds_read_b64_tr_b16 v[56:57], v108 offset:1024\n\t" \
  "ds_read_b64_tr_b16 v[58:59], v108 offset:1088\n\t" \
  "ds_read_b64_tr_b16 v[60:61], v108 offset:1536\n\t" \
  "ds_read_b64_tr_b16 v[62:63], v108 offset:1600\n\t" \
  "s_waitcnt vmcnt(3) lgkmcnt(8)\n\t" \
  "v_mfma_f32_16x16x32_bf16 v[64:67], v[0:3], v[84:87], v[64:67]\n\t" \
  "v_mfma_f32_16x16x32_bf16 v[68:71], v[16:19], v[84:87], v[68:71]\n\t" \
  "v_mfma_f32_16x16x32_bf16 v[64:67], v[4:7], v[88:91], v[64:67]\n\t" \
  "v_mfma_f32_16x16x32_bf16 v[68:71], v[20:23], v[88:91], v[68:71]\n\t" \
  "v_mfma_f32_16x16x32_bf16 v[64:67], v[8:11], v[92:95], v[64:67]\n\t" \
  "v_mfma_f32_16x16x32_bf16 v[68:71], v[24:27], v[92:95], v[68:71]\n\t" \
  "v_mfma_f32_16x16x32_bf16 v[64:67], v[12:15], v[96:99], v[64:67]\n\t" \
  "v_mfma_f32_16x16x32_bf16 v[68:71], v[28:31], v[96:99], v[68:71]\n\t" \
  "s_waitcnt lgkmcnt(0)\n\t" \
  "v_mfma_f32_16x16x32_bf16 v[72:75], v[48:51], v[84:87], 0\n\t" \
  "v_mfma_f32_16x16x32_bf16 v[72:75], v[52:55], v[88:91], v[72:75]\n\t" \
  "v_mfma_f32_16x16x32_bf16 v[72:75], v[56:59], v[92:95], v[72:75]\n\t" \
  "v_mfma_f32_16x16x32_bf16 v[72:75], v[60:63], v[96:99], v[72:75]\n\t" \
  "s_barrier\n\t" \
  "v_max3_f32 v76, v64, v65, v66\n\t" \
  "v_max3_f32 v77, v67, v68, v69\n\t" \
  "v_max3_f32 v76, v76, v70, v71\n\t" \
  "s_nop 4\n\t" \
  "v_max3_f32 v77, v77, v72, v73\n\t" \
  "v_max3_f32 v76, v76, v74, v75\n\t" \
  "v_max_f32 v76, v76, v77\n\t" \
  "v_mov_b32 v77, v76\n\t" \
  "s_nop 1\n\t" \
  "v_permlane16_swap_b32 v76, v77\n\t" \
  "v_max_f32 v76, v76, v77\n\t" \
  "v_mov_b32 v77, v76\n\t" \
  "s_nop 1\n\t" \
  "v_permlane32_swap_b32 v76, v77\n\t" \
  "v_max_f32 v76, v76, v77\n\t" \
  "v_mul_f32 v76, %[cs], v76\n\t" \
  "v_fma_f32 v64, v64, %[cs], -v76\n\t" \
  "v_fma_f32 v65, v65, %[cs], -v76\n\t" \
  "v_fma_f32 v66, v66, %[cs], -v76\n\t" \
  "v_fma_f32 v67, v67, %[cs], -v76\n\t" \
  "v_fma_f32 v68, v68, %[cs], -v76\n\t" \
  "v_fma_f32 v69, v69, %[cs], -v76\n\t" \
  "v_fma_f32 v70, v70, %[cs], -v76\n\t" \
  "v_fma_f32 v71, v71, %[cs], -v76\n\t" \
  "v_fma_f32 v72, v72, %[cs], -v76\n\t" \
  "v_fma_f32 v73, v73, %[cs], -v76\n\t" \
  "v_fma_f32 v74, v74, %[cs], -v76\n\t" \
  "v_fma_f32 v75, v75, %[cs], -v76\n\t" \
  "v_exp_f32 v64, v64\n\t" \
  "v_exp_f32 v65, v65\n\t" \
  "v_exp_f32 v66, v66\n\t" \
  "v_exp_f32 v67, v67\n\t" \
  "v_exp_f32 v68, v68\n\t" \
  "v_exp_f32 v69, v69\n\t" \
  "v_exp_f32 v70, v70\n\t" \
  "v_exp_f32 v71, v71\n\t" \
  "v_exp_f32 v72, v72\n\t" \
  "v_exp_f32 v73, v73\n\t" \
  "v_exp_f32 v74, v74\n\t" \
  "v_exp_f32 v75, v75\n\t" \
  "v_add_f32 v76, v72, v73\n\t" \
  "v_add_f32 v77, v74, v75\n\t" \
  "v_add_f32 v78, v64, v65\n\t" \
  "v_add_f32 v79, v66, v67\n\t" \
  "v_add_f32 v76, v76, v77\n\t" \
  "v_add_f32 v78, v78, v68\n\t" \
  "v_add_f32 v79, v79, v69\n\t" \
  "v_add_f32 v78, v78, v70\n\t" \
  "v_add_f32 v79, v79, v71\n\t" \
  "v_fma_f32 v76, v76, 0.5, v78\n\t" \
  "v_add_f32 v76, v76, v79\n\t" \
  "v_mov_b32 v77, v76\n\t" \
  "s_nop 1\n\t" \
  "v_permlane16_swap_b32 v76, v77\n\t" \
  "v_add_f32 v76, v76, v77\n\t" \
  "v_mov_b32 v77, v76\n\t" \
  "s_nop 1\n\t" \
  "v_permlane32_swap_b32 v76, v77\n\t" \
  "v_add_f32 v76, v76, v77\n\t" \
  "v_rcp_f32 v76, v76\n\t" \
  "s_nop 0\n\t" \
  "v_mul_f32 v64, v64, v76\n\t" \
  "v_mul_f32 v65, v65, v76\n\t" \
  "v_mul_f32 v66, v66, v76\n\t" \
  "v_mul_f32 v67, v67, v76\n\t" \
  "v_mul_f32 v68, v68, v76\n\t" \
  "v_mul_f32 v69, v69, v76\n\t" \
  "v_mul_f32 v70, v70, v76\n\t" \
  "v_mul_f32 v71, v71, v76\n\t" \
  "v_mul_f32 v72, v72, v76\n\t" \
  "v_mul_f32 v73, v73, v76\n\t" \
  "v_mul_f32 v74, v74, v76\n\t" \
  "v_mul_f32 v75, v75, v76\n\t" \
  "v_cvt_pk_bf16_f32 v0, v64, v68\n\t" \
  "v_cvt_pk_bf16_f32 v1, v65, v69\n\t" \
  "v_cvt_pk_bf16_f32 v2, v66, v70\n\t" \
  "v_cvt_pk_bf16_f32 v3, v67, v71\n\t" \
  "v_cvt_pk_bf16_f32 v4, v72, v73\n\t" \
  "v_cvt_pk_bf16_f32 v5, v74, v75\n\t" \
  "v_permlane32_swap_b32 v0, v2\n\t" \
  "v_permlane32_swap_b32 v1, v3\n\t" \
  "v_mov_b64 v[6:7], v[4:5]\n\t" \
  "s_mov_b32 exec_hi, 0\n\t" \
  "buffer_load_dword v118, v112, %[rh], s96 offen sc1\n\t" \
  "s_mov_b64 exec, -1\n\t" \
  "buffer_store_dwordx4 v[0:3], v110, %[rh], s94 offen sc1\n\t" \
  "v_permlane16_swap_b32 v4, v6\n\t" \
  "v_permlane16_swap_b32 v5, v7\n\t" \
  "buffer_store_dwordx4 v[4:7], v111, %[rh], s94 offen sc1\n\t" \
  "s_waitcnt vmcnt(3)\n\t" \
  "ds_write_b128 v101, v[32:35] offset:20480\n\t" \
  "ds_write_b128 v101, v[36:39] offset:21504\n\t" \
  "ds_write_b128 v101, v[40:43] offset:22528\n\t" \
  "s_add_i32 s77, s44, 2\n\t" \
  "s_min_u32 s77, s77, 63\n\t" \
  "v_cmp_gt_i32 vcc, s77, v119\n\t" \
  "s_cmp_lg_u64 vcc, 0\n\t" \
  "s_cselect_b32 s42, 32, 0\n\t" \
  "s_cmp_eq_u32 s44, 0\n\t" \
  "s_cselect_b32 s42, 32, s42\n\t" \
  "s_add_i32 s41, s56, 1950\n\t" \
  "s_sub_i32 s76, s92, s51\n\t" \
  "s_sub_i32 s77, s93, s53\n\t" \
  "s_cmp_eq_u32 s76, 0\n\t" \
  "s_cbranch_scc1 21f\n\t" \
  "v_cvt_f32_u32 v117, s76\n\t" \
  "v_cvt_f32_u32 v116, s77\n\t" \
  "v_rcp_f32 v117, v117\n\t" \
  "s_mov_b32 s51, s92\n\t" \
  "v_mul_f32 v117, v117, v116\n\t" \
  "v_max_f32 v117, 0x3f028f5c, v117\n\t" \
  "v_min_f32 v117, 0x3ffeb852, v117\n\t" \
  "v_rcp_f32 v116, v117\n\t" \
  "v_mul_f32 v117, 0x4f000000, v117\n\t" \
  "v_mul_f32 v116, 0x47800000, v116\n\t" \
  "v_cvt_u32_f32 v117, v117\n\t" \
  "v_cvt_u32_f32 v116, v116\n\t" \
  "s_mov_b32 s53, s93\n\t" \
  "v_readfirstlane_b32 s46, v117\n\t" \
  "v_readfirstlane_b32 s52, v116\n\t" \
  "21:\n\t" \
  "s_add_i32 s44, s44, 1\n\t" \
  "s_and_b32 s58, s58, 4\n\t" \
  "s_and_b32 s76, s44, 64\n\t" \
  "s_or_b32 s42, s42, s76\n\t" \
  "s_cbranch_scc0 20b\n\t" \
  "s_branch 99f\n\t" \
  "14:\n\t" \
  "s_sleep 3\n\t" \
  "s_add_u32 s59, s59, 0x10000\n\t" \
  "s_cmp_lt_u32 s59, 0x100000\n\t" \
  "s_cbranch_scc1 13b\n\t" \
  "s_mov_b64 exec, -1\n\t" \
  "s_mov_b32 s42, 16\n\t" \
  "s_branch 99f\n\t" \
  "8:\n\t" \
  "s_bitcmp1_b32 s58, 1\n\t" \
  "s_cbranch_scc1 4b\n\t" \
  "s_bitcmp1_b32 s58, 2\n\t" \
  "s_cbranch_scc1 5b\n\t" \
  "v_max3_u32 v116, v32, v33, v34\n\t" \
  "v_max3_u32 v117, v35, v36, v37\n\t" \
  "v_max3_u32 v116, v116, v38, v39\n\t" \
  "v_max_u32 v116, v116, v117\n\t" \
  "v_cmp_eq_u32 vcc, -1, v116\n\t" \
  "s_cbranch_vccz 5b\n\t" \
  "s_branch 0f\n\t" \
  "9:\n\t" \
  "s_bitcmp1_b32 s58, 2\n\t" \
  "s_cbranch_scc1 6b\n\t" \
  "v_max3_u32 v116, v40, v41, v42\n\t" \
  "v_max3_u32 v117, v43, v44, v45\n\t" \
  "v_max3_u32 v116, v116, v46, v47\n\t" \
  "v_max_u32 v116, v116, v117\n\t" \
  "v_cmp_eq_u32 vcc, -1, v116\n\t" \
  "s_cbranch_vccz 6b\n\t" \
  "s_or_b32 s58, s58, 2\n\t" \
  "0:\n\t" \
  "s_or_b32 s58, s58, 1\n\t" \
  "11:\n\t" \
  "buffer_load_dwordx4 v[32:35], v100, %[rh], s40 offen sc1\n\t" \
  "buffer_load_dwordx4 v[36:39], v100, %[rh], s76 offen sc1\n\t" \
  "buffer_load_dwordx4 v[40:43], v100, %[rh], s77 offen sc1\n\t" \
  "buffer_load_dwordx4 v[44:47], v100, %[rh], s78 offen sc1\n\t" \
  "s_waitcnt lgkmcnt(0)\n\t" \
  "s_branch 10b\n\t" \
  "17:\n\t" \
  "s_add_u32 s59, s59, 1\n\t" \
  "s_bitcmp1_b32 s58, 2\n\t" \
  "s_cbranch_scc1 3b\n\t" \
  "s_waitcnt vmcnt(0)\n\t" \
  "v_max_u32 v116, v32, v33\n\t" \
  "v_max3_u32 v116, v116, v34, v35\n\t" \
  "v_max3_u32 v116, v116, v36, v37\n\t" \
  "v_max3_u32 v116, v116, v38, v39\n\t" \
  "v_max3_u32 v116, v116, v40, v41\n\t" \
  "v_max3_u32 v116, v116, v42, v43\n\t" \
  "v_max3_u32 v116, v116, v44, v45\n\t" \
  "v_max3_u32 v116, v116, v46, v47\n\t" \
  "v_cmp_eq_u32 vcc, -1, v116\n\t" \
  "s_cbranch_vccz 3b\n\t" \
  "s_bitcmp1_b32 s59, 6\n\t" \
  "s_cbranch_scc1 12f\n\t" \
  "buffer_load_dwordx4 v[32:35], v100, %[rh], s40 offen sc1\n\t" \
  "buffer_load_dwordx4 v[36:39], v100, %[rh], s76 offen sc1\n\t" \
  "buffer_load_dwordx4 v[40:43], v100, %[rh], s77 offen sc1\n\t" \
  "buffer_load_dwordx4 v[44:47], v100, %[rh], s78 offen sc1\n\t" \
  "s_branch 17b\n\t" \
  "12:\n\t" \
  "s_mov_b32 s42, s58\n\t" \
  "s_branch 99f\n\t" \
  "92:\n\t" \
  "s_mov_b32 s58, s42\n\t" \
  "s_mov_b32 s95, 0\n\t" \
  "20:\n\t" \
  "s_and_b32 s76, s44, 7\n\t" \
  "s_lshl_b32 s76, s76, 15\n\t" \
  "s_add_i32 s40, s54, s76\n\t" \
  "s_and_b32 s76, s44, 3\n\t" \
  "s_lshl_b32 s45, s76, 14\n\t" \
  "s_add_i32 s47, s44, 1\n\t" \
  "s_min_u32 s47, s47, 63\n\t" \
  "s_lshr_b32 s76, s47, 1\n\t" \
  "s_lshl_b32 s47, s47, 20\n\t" \
  "s_add_i32 s47, s47, s55\n\t" \
  "s_bitcmp1_b64 %[sel], s76\n\t" \
  "s_cselect_b32 s76, 0x2000, 0\n\t" \
  "s_add_i32 s47, s47, s76\n\t" \
  "s_lshr_b32 s76, s44, 1\n\t" \
  "s_lshl_b32 s94, s44, 20\n\t" \
  "s_bitcmp1_b64 %[sel], s76\n\t" \
  "s_cselect_b32 s76, 0x2000, 0\n\t" \
  "s_add_i32 s94, s94, s76\n\t" \
  "s_add_i32 s49, s44, -1\n\t" \
  "s_and_b32 s48, s49, 2\n\t" \
  "s_lshl_b32 s48, s48, 13\n\t" \
  "s_and_b32 s76, s49, 1\n\t" \
  "s_lshl_b32 s76, s76, 12\n\t" \
  "s_add_i32 s48, s48, s76\n\t" \
  "s_lshl_b32 s50, s53, 8\n\t" \
  "s_mov_b32 s59, 0\n\t" \
  "s_mov_b32 s92, s51\n\t" \
  "s_mov_b32 s93, s53\n\t" \
  "s_xor_b32 s76, s40, 0x400\n\t" \
  "s_xor_b32 s77, s40, 0x800\n\t" \
  "s_xor_b32 s78, s40, 0xc00\n\t" \
  "s_cmp_lg_u32 s58, 0\n\t" \
  "s_cbranch_scc1 11f\n\t" \
  "10:\n\t" \
  "ds_read_b128 v[48:51], v103 offset:0\n\t" \
  "ds_read_b128 v[52:55], v103 offset:1024\n\t" \
  "ds_read_b128 v[56:59], v103 offset:2048\n\t" \
  "ds_read_b128 v[60:63], v103 offset:3072\n\t" \
  "ds_read_b128 v[16:19], v103 offset:16384\n\t" \
  "ds_read_b128 v[20:23], v103 offset:17408\n\t" \
  "ds_read_b128 v[24:27], v103 offset:18432\n\t" \
  "ds_read_b128 v[28:31], v103 offset:19456\n\t" \
  "ds_read_b128 v[84:87], v103 offset:49152\n\t" \
  "ds_read_b128 v[88:91], v103 offset:50176\n\t" \
  "ds_read_b128 v[92:95], v103 offset:51200\n\t" \
  "ds_read_b128 v[96:99], v103 offset:52224\n\t" \
  "s_waitcnt lgkmcnt(4)\n\t" \
  "ds_read_b128 v[0:3], v102 offset:16384\n\t" \
  "ds_read_b128 v[4:7], v102 offset:17408\n\t" \
  "ds_read_b128 v[8:11], v102 offset:18432\n\t" \
  "ds_read_b128 v[12:15], v102 offset:19456\n\t" \
  "s_bitcmp1_b32 s58, 1\n\t" \
  "s_cselect_b64 s[80:81], -1, 0\n\t" \
  "s_cmp_lg_u32 s58, 0\n\t" \
  "s_cbranch_scc1 17f\n\t" \
  "s_mov_b32 exec_hi, 0\n\t" \
  "s_cmp_eq_u32 s95, 0\n\t" \
  "s_cbranch_scc1 16f\n\t" \
  "s_waitcnt vmcnt(2)\n\t" \
  "v_mov_b32 v116, v118\n\t" \
  "s_branch 15f\n\t" \
  "16:\n\t" \
  "s_memtime vcc\n\t" \
  "s_waitcnt lgkmcnt(0)\n\t" \
  "s_sub_i32 s79, s41, vcc_lo\n\t" \
  "s_cmp_gt_i32 s79, 64\n\t" \
  "s_cbranch_scc1 16b\n\t" \
  "13:\n\t" \
  "buffer_load_dword v116, v112, %[rh], s48 offen sc1\n\t" \
  "s_waitcnt vmcnt(0)\n\t" \
  "15:\n\t" \
  "v_mov_b32 v117, 20000\n\t" \
  "v_cmp_ne_u32_sdwa vcc, v116, s49 src0_sel:BYTE_0 src1_sel:DWORD\n\t" \
  "v_subrev_u32 v116, s50, v116\n\t" \
  "v_ashrrev_i32 v116, 8, v116\n\t" \
  "s_nop 1\n\t" \
  "v_max_i32_dpp v116, v116, v116 quad_perm:[1,0,3,2] row_mask:0xf bank_mask:0xf\n\t" \
  "s_cbranch_vccnz 14f\n\t" \
  "s_nop 0\n\t" \
  "v_max_i32_dpp v116, v116, v116 quad_perm:[2,3,0,1] row_mask:0xf bank_mask:0xf\n\t" \
  "s_nop 1\n\t" \
  "v_max_i32_dpp v116, v116, v116 row_half_mirror row_mask:0xf bank_mask:0xf\n\t" \
  "s_nop 1\n\t" \
  "v_max_i32_dpp v116, v116, v116 row_mirror row_mask:0xf bank_mask:0xf\n\t" \
  "v_med3_i32 v116, v116, 0, v117\n\t" \
  "v_mul_u32_u24 v116, s52, v116\n\t" \
  "v_lshrrev_b32 v116, 16, v116\n\t" \
  "s_mov_b64 exec, -1\n\t" \
  "v_readfirstlane_b32 s84, v116\n\t" \
  "v_readlane_b32 s85, v116, 16\n\t" \
  "s_max_u32 s84, s84, s85\n\t" \
  "s_add_i32 s84, s84, s51\n\t" \
  "1:\n\t" \
  "s_memtime vcc\n\t" \
  "s_waitcnt lgkmcnt(0)\n\t" \
  "s_sub_i32 s79, s84, vcc_lo\n\t" \
  "s_cmp_gt_i32 s79, 128\n\t" \
  "s_cbranch_scc1 1b\n\t" \
  ".p2align 4\n\t" \
  "2:\n\t" \
  "s_sub_i32 s79, s79, 33\n\t" \
  "s_cmp_gt_i32 s79, 56\n\t" \
  "s_cbranch_scc1 2b\n\t" \
  "buffer_load_dwordx4 v[32:35], v100, %[rh], s40 offen sc1\n\t" \
  "s_nop 12\n\t" \
  "buffer_load_dwordx4 v[36:39], v100, %[rh], s76 offen sc1\n\t" \
  "s_nop 12\n\t" \
  "buffer_load_dwordx4 v[40:43], v100, %[rh], s77 offen sc1\n\t" \
  "s_nop 12\n\t" \
  "buffer_load_dwordx4 v[44:47], v100, %[rh], s78 offen sc1\n\t" \
  "s_memtime vcc\n\t" \
  "s_memrealtime s[82:83]\n\t" \
  "s_nop 4\n\t" \
  "s_memrealtime s[84:85]\n\t" \
  "s_nop 4\n\t" \
  "s_memrealtime s[56:57]\n\t" \
  "s_nop 4\n\t" \
  "s_memrealtime s[86:87]\n\t" \
  "s_nop 4\n\t" \
  "s_memrealtime s[88:89]\n\t" \
  "s_nop 4\n\t" \
  "s_memrealtime s[90:91]\n\t" \
  "s_mov_b32 s92, 0\n\t" \
  "s_waitcnt lgkmcnt(0)\n\t" \
  "s_cmp_eq_u32 s82, s84\n\t" \
  "s_addc_u32 s92, s92, 0\n\t" \
  "s_cmp_eq_u32 s82, s56\n\t" \
  "s_addc_u32 s92, s92, 0\n\t" \
  "s_cmp_eq_u32 s82, s86\n\t" \
  "s_addc_u32 s92, s92, 0\n\t" \
  "s_cmp_eq_u32 s82, s88\n\t" \
  "s_addc_u32 s92, s92, 0\n\t" \
  "s_cmp_eq_u32 s82, s90\n\t" \
  "s_addc_u32 s92, s92, 0\n\t" \
  "s_mul_i32 s92, s92, 24\n\t" \
  "s_add_i32 s92, s92, vcc_lo\n\t" \
  "s_lshl4_add_u32 s93, s82, 64\n\t" \
  "3:\n\t" \
  "v_mov_b64 v[68:69], 0\n\t" \
  "v_mov_b64 v[70:71], 0\n\t" \
  "v_mov_b64 v[72:73], 0\n\t" \
  "v_mov_b64 v[74:75], 0\n\t" \
  "v_mov_b64 v[76:77], 0\n\t" \
  "v_mov_b64 v[78:79], 0\n\t" \
  "v_mov_b64 v[80:81], 0\n\t" \
  "v_mov_b64 v[82:83], 0\n\t" \
  "s_waitcnt lgkmcnt(0)\n\t" \
  "s_waitcnt vmcnt(3)\n\t" \
  "v_smfmac_f32_16x16x64_bf16 v[68:71], v[32:35], a[192:199], v104\n\t" \
  "ds_write_b128 v101, v[32:35] offset:8192\n\t" \
  "v_smfmac_f32_16x16x64_bf16 v[72:75], v[32:35], a[200:207], v104\n\t" \
  "v_smfmac_f32_16x16x64_bf16 v[76:79], v[32:35], v[48:55], v104\n\t" \
  "v_smfmac_f32_16x16x64_bf16 v[80:83], v[32:35], v[56:63], v104\n\t" \
  "s_waitcnt vmcnt(2)\n\t" \
  "v_smfmac_f32_16x16x64_bf16 v[68:71], v[36:39], a[208:215], v104\n\t" \
  "ds_write_b128 v101, v[36:39] offset:9216\n\t" \
  "v_smfmac_f32_16x16x64_bf16 v[72:75], v[36:39], a[216:223], v104\n\t" \
  "v_smfmac_f32_16x16x64_bf16 v[76:79], v[36:39], a[224:231], v104\n\t" \
  "v_cmp_u_f32 vcc, v68, v69\n\t" \
  "v_smfmac_f32_16x16x64_bf16 v[80:83], v[36:39], a[232:239], v104\n\t" \
  "v_cmp_u_f32 s[82:83], v70, v71\n\t" \
  "s_waitcnt vmcnt(1)\n\t" \
  "v_smfmac_f32_16x16x64_bf16 v[68:71], v[40:43], v[16:23], v104\n\t" \
  "ds_write_b128 v101, v[40:43] offset:10240\n\t" \
  "v_smfmac_f32_16x16x64_bf16 v[72:75], v[40:43], v[24:31], v104\n\t" \
  "s_or_b64 vcc, vcc, s[82:83]\n\t" \
  "s_or_b64 vcc, vcc, s[80:81]\n\t" \
  "s_waitcnt lgkmcnt(1)\n\t" \
  "v_smfmac_f32_16x16x64_bf16 v[76:79], v[40:43], a[240:247], v104\n\t" \
  "s_cbranch_scc1 8f\n\t" \
  "5:\n\t" \
  "s_barrier\n\t" \
  "4:\n\t" \
  "s_memtime s[56:57]\n\t" \
  "ds_read_b128 v[48:51], v101 offset:12288\n\t" \
  "v_smfmac_f32_16x16x64_bf16 v[80:83], v[40:43], a[248:255], v104\n\t" \
  "ds_read_b128 v[52:55], v101 offset:13312\n\t" \
  "ds_read_b128 v[56:59], v101 offset:0\n\t" \
  "s_waitcnt vmcnt(0)\n\t" \
  "v_smfmac_f32_16x16x64_bf16 v[68:71], v[44:47], v[120:127], v104\n\t" \
  "v_smfmac_f32_16x16x64_bf16 v[72:75], v[44:47], v[128:135], v104\n\t" \
  "ds_write_b128 v101, v[44:47] offset:11264\n\t" \
  "v_smfmac_f32_16x16x64_bf16 v[76:79], v[44:47], v[136:143], v104\n\t" \
  "v_cmp_u_f32 vcc, v68, v69\n\t" \
  "v_smfmac_f32_16x16x64_bf16 v[80:83], v[44:47], v[144:151], v104\n\t" \
  "v_cmp_u_f32 s[82:83], v70, v71\n\t" \
  "s_waitcnt lgkmcnt(3)\n\t" \
  "v_smfmac_f32_16x16x64_bf16 v[68:71], v[48:51], v[152:159], v104\n\t" \
  "ds_read_b128 v[60:63], v101 offset:1024\n\t" \
  "ds_read_b128 v[64:67], v101 offset:4096\n\t" \
  "v_smfmac_f32_16x16x64_bf16 v[72:75], v[48:51], v[160:167], v104\n\t" \
  "s_or_b64 vcc, vcc, s[82:83]\n\t" \
  "s_waitcnt lgkmcnt(2)\n\t" \
  "v_smfmac_f32_16x16x64_bf16 v[68:71], v[52:55], v[168:175], v104\n\t" \
  "s_cbranch_scc1 9f\n\t" \
  "6:\n\t" \
  "s_barrier\n\t" \
  "v_smfmac_f32_16x16x64_bf16 v[72:75], v[52:55], v[176:183], v104\n\t" \
  "ds_read_b128 v[48:51], v101 offset:5120\n\t" \
  "v_smfmac_f32_16x16x64_bf16 v[68:71], v[56:59], a[0:7], v104\n\t" \
  "ds_read_b128 v[52:55], v101 offset:14336\n\t" \
  "v_smfmac_f32_16x16x64_bf16 v[72:75], v[56:59], a[8:15], v104\n\t" \
  "s_waitcnt lgkmcnt(3)\n\t" \
  "v_smfmac_f32_16x16x64_bf16 v[68:71], v[60:63], a[16:23], v104\n\t" \
  "ds_read_b128 v[56:59], v101 offset:15360\n\t" \
  "v_smfmac_f32_16x16x64_bf16 v[72:75], v[60:63], a[24:31], v104\n\t" \
  "s_waitcnt lgkmcnt(3)\n\t" \
  "v_smfmac_f32_16x16x64_bf16 v[68:71], v[64:67], a[96:103], v104\n\t" \
  "ds_read_b128 v[60:63], v101 offset:2048\n\t" \
  "global_load_dword v119, v109, %[kc] sc1\n\t" \
  "v_smfmac_f32_16x16x64_bf16 v[72:75], v[64:67], a[104:111], v104\n\t" \
  "s_waitcnt lgkmcnt(3)\n\t" \
  "v_smfmac_f32_16x16x64_bf16 v[68:71], v[48:51], a[112:119], v104\n\t" \
  "ds_read_b128 v[64:67], v101 offset:3072\n\t" \
  "ds_read_b128 v[16:19], v102 offset:49152\n\t" \
  "v_smfmac_f32_16x16x64_bf16 v[72:75], v[48:51], a[120:127], v104\n\t" \
  "s_waitcnt lgkmcnt(4)\n\t" \
  "v_smfmac_f32_16x16x64_bf16 v[68:71], v[52:55], v[84:91], v104\n\t" \
  "ds_read_b128 v[48:51], v101 offset:7168\n\t" \
  "ds_read_b128 v[20:23], v102 offset:50176\n\t" \
  "v_smfmac_f32_16x16x64_bf16 v[72:75], v[52:55], v[92:99], v104\n\t" \
  "s_waitcnt lgkmcnt(5)\n\t" \
  "v_smfmac_f32_16x16x64_bf16 v[68:71], v[56:59], v[216:223], v104\n\t" \
  "ds_read_b128 v[52:55], v101 offset:6144\n\t" \
  "ds_read_b128 v[24:27], v102 offset:51200\n\t" \
  "v_smfmac_f32_16x16x64_bf16 v[72:75], v[56:59], v[224:231], v104\n\t" \
  "ds_read_b128 v[28:31], v102 offset:52224\n\t" \
  "s_waitcnt lgkmcnt(7)\n\t" \
  "v_smfmac_f32_16x16x64_bf16 v[68:71], v[60:63], v[0:7], v104\n\t" \
  "buffer_load_dwordx4 v[84:87], v107, %[ru], s94 offen offset:0 sc1\n\t" \
  "v_smfmac_f32_16x16x64_bf16 v[72:75], v[60:63], v[8:15], v104\n\t" \
  "s_waitcnt lgkmcnt(6)\n\t" \
  "v_smfmac_f32_16x16x64_bf16 v[68:71], v[64:67], a[64:71], v104\n\t" \
  "ds_read_b128 v[0:3], v103 offset:32768\n\t" \
  "buffer_load_dwordx4 v[88:91], v107, %[ru], s94 offen offset:1024 sc1\n\t" \
  "v_smfmac_f32_16x16x64_bf16 v[72:75], v[64:67], a[72:79], v104\n\t" \
  "ds_read_b128 v[4:7], v103 offset:33792\n\t" \
  "s_waitcnt lgkmcnt(6)\n\t" \
  "v_smfmac_f32_16x16x64_bf16 v[68:71], v[48:51], a[160:167], v104\n\t" \
  "buffer_load_dwordx4 v[92:95], v107, %[ru], s94 offen offset:2048 sc1\n\t" \
  "s_waitcnt lgkmcnt(4)\n\t" \
  "v_smfmac_f32_16x16x64_bf16 v[68:71], v[52:55], v[16:23], v104\n\t" \
  "ds_read_b128 v[8:11], v103 offset:34816\n\t" \
  "v_smfmac_f32_16x16x64_bf16 v[72:75], v[48:51], a[168:175], v104\n\t" \
  "ds_read_b128 v[12:15], v103 offset:35840\n\t" \
  "buffer_load_dwordx4 v[96:99], v107, %[ru], s94 offen offset:3072 sc1\n\t" \
  "s_waitcnt lgkmcnt(4)\n\t" \
  "v_smfmac_f32_16x16x64_bf16 v[72:75], v[52:55], v[24:31], v104\n\t" \
  "v_add_f32 v44, v68, v70\n\t" \
  "v_smfmac_f32_16x16x64_bf16 v[76:79], v[56:59], v[232:239], v104\n\t" \
  "v_add_f32 v45, v69, v71\n\t" \
  "v_cvt_pk_bf16_f32 v44, v44, v45\n\t" \
  "v_smfmac_f32_16x16x64_bf16 v[80:83], v[56:59], v[240:247], v104\n\t" \
  "v_add_f32 v46, v72, v74\n\t" \
  "v_add_f32 v47, v73, v75\n\t" \
  "v_smfmac_f32_16x16x64_bf16 v[76:79], v[60:63], a[48:55], v104\n\t" \
  "v_cvt_pk_bf16_f32 v46, v46, v47\n\t" \
  "ds_write2st64_b32 v105, v44, v46 offset1:1\n\t" \
  "v_smfmac_f32_16x16x64_bf16 v[80:83], v[60:63], a[56:63], v104\n\t" \
  "ds_read_b128 v[56:59], v101 offset:12288\n\t" \
  "v_smfmac_f32_16x16x64_bf16 v[76:79], v[64:67], a[80:87], v104\n\t" \
  "ds_read_b128 v[16:19], v102 offset:0\n\t" \
  "v_smfmac_f32_16x16x64_bf16 v[80:83], v[64:67], a[88:95], v104\n\t" \
  "ds_read_b128 v[20:23], v102 offset:1024\n\t" \
  "v_smfmac_f32_16x16x64_bf16 v[76:79], v[48:51], a[176:183], v104\n\t" \
  "s_waitcnt lgkmcnt(3)\n\t" \
  "s_barrier\n\t" \
  "v_smfmac_f32_16x16x64_bf16 v[80:83], v[48:51], a[184:191], v104\n\t" \
  "v_smfmac_f32_16x16x64_bf16 v[76:79], v[52:55], a[144:151], v104\n\t" \
  "ds_read_b128 v[60:63], v101 offset:5120\n\t" \
  "ds_read_b128 v[24:27], v102 offset:2048\n\t" \
  "v_smfmac_f32_16x16x64_bf16 v[80:83], v[52:55], a[152:159], v104\n\t" \
  "ds_read_b128 v[64:67], v101 offset:14336\n\t" \
  "s_waitcnt lgkmcnt(5)\n\t" \
  "v_smfmac_f32_16x16x64_bf16 v[76:79], v[56:59], v[0:7], v104\n\t" \
  "ds_read_b128 v[48:51], v101 offset:0\n\t" \
  "ds_read_b128 v[28:31], v102 offset:3072\n\t" \
  "v_smfmac_f32_16x16x64_bf16 v[80:83], v[56:59], v[8:15], v104\n\t" \
  "s_waitcnt lgkmcnt(4)\n\t" \
  "v_smfmac_f32_16x16x64_bf16 v[76:79], v[60:63], a[128:135], v104\n\t" \
  "ds_read_b128 v[52:55], v101 offset:13312\n\t" \
  "buffer_load_dwordx4 v[32:35], v100, %[ru], s47 offen offset:0 sc1\n\t" \
  "v_smfmac_f32_16x16x64_bf16 v[80:83], v[60:63], a[136:143], v104\n\t" \
  "ds_read_b128 v[0:3], v102 offset:32768\n\t" \
  "s_waitcnt lgkmcnt(4)\n\t" \
  "v_smfmac_f32_16x16x64_bf16 v[76:79], v[64:67], v[200:207], v104\n\t" \
  "ds_read_b128 v[56:59], v101 offset:1024\n\t" \
  "v_smfmac_f32_16x16x64_bf16 v[80:83], v[64:67], v[208:215], v104\n\t" \
  "ds_read_b128 v[60:63], v101 offset:4096\n\t" \
  "ds_read_b128 v[4:7], v102 offset:33792\n\t" \
  "buffer_load_dwordx4 v[36:39], v100, %[ru], s47 offen offset:1024 sc1\n\t" \
  "s_waitcnt lgkmcnt(6)\n\t" \
  "v_smfmac_f32_16x16x64_bf16 v[76:79], v[48:51], v[16:23], v104\n\t" \
  "ds_read_b128 v[8:11], v102 offset:34816\n\t" \
  "s_waitcnt lgkmcnt(6)\n\t" \
  "v_smfmac_f32_16x16x64_bf16 v[80:83], v[48:51], v[24:31], v104\n\t" \
  "ds_read_b128 v[12:15], v102 offset:35840\n\t" \
  "s_waitcnt lgkmcnt(6)\n\t" \
  "v_smfmac_f32_16x16x64_bf16 v[76:79], v[52:55], v[184:191], v104\n\t" \
  "buffer_load_dwordx4 v[40:43], v100, %[ru], s47 offen offset:2048 sc1\n\t" \
  "v_smfmac_f32_16x16x64_bf16 v[80:83], v[52:55], v[192:199], v104\n\t" \
  "s_waitcnt lgkmcnt(4)\n\t" \
  "v_smfmac_f32_16x16x64_bf16 v[76:79], v[56:59], a[32:39], v104\n\t" \
  "s_waitcnt lgkmcnt(2)\n\t" \
  "v_smfmac_f32_16x16x64_bf16 v[76:79], v[60:63], v[0:7], v104\n\t" \
  "v_smfmac_f32_16x16x64_bf16 v[80:83], v[56:59], a[40:47], v104\n\t" \
  "s_waitcnt lgkmcnt(0)\n\t" \
  "v_smfmac_f32_16x16x64_bf16 v[80:83], v[60:63], v[8:15], v104\n\t" \
  "v_add_f32 v20, v76, v78\n\t" \
  "v_add_f32 v21, v77, v79\n\t" \
  "v_cvt_pk_bf16_f32 v20, v20, v21\n\t" \
  "ds_write_b32 v106, v20\n\t" \
  "s_nop 3\n\t" \
  "v_add_f32 v23, v80, v82\n\t" \
  "v_add_f32 v21, v81, v83\n\t" \
  "v_cvt_pk_bf16_f32 v21, v23, v21\n\t" \
  "ds_write_b32 v106, v21 offset:256\n\t" \
  "s_waitcnt lgkmcnt(0)\n\t" \
  "s_barrier\n\t" \
  "v_bfe_i32 v64, v114, 0, 1\n\t" \
  "v_and_b32 v64, 0xff800000, v64\n\t" \
  "v_bfe_i32 v65, v114, 1, 1\n\t" \
  "v_and_b32 v65, 0xff800000, v65\n\t" \
  "v_bfe_i32 v66, v114, 2, 1\n\t" \
  "v_and_b32 v66, 0xff800000, v66\n\t" \
  "v_bfe_i32 v67, v114, 3, 1\n\t" \
  "v_and_b32 v67, 0xff800000, v67\n\t" \
  "v_bfe_i32 v68, v114, 4, 1\n\t" \
  "v_and_b32 v68, 0xff800000, v68\n\t" \
  "v_bfe_i32 v69, v114, 5, 1\n\t" \
  "v_and_b32 v69, 0xff800000, v69\n\t" \
  "v_bfe_i32 v70, v114, 6, 1\n\t" \
  "v_and_b32 v70, 0xff800000, v70\n\t" \
  "v_bfe_i32 v71, v114, 7, 1\n\t" \
  "v_and_b32 v71, 0xff800000, v71\n\t" \
  "s_lshl_b32 s94, s45, 4\n\t" \
  "s_and_b32 s96, s44, 2\n\t" \
  "s_lshl_b32 s96, s96, 13\n\t" \
  "s_and_b32 s76, s44, 1\n\t" \
  "s_lshl_b32 s76, s76, 12\n\t" \
  "s_add_i32 s96, s96, s76\n\t" \
  "s_mov_b32 s95, 1\n\t" \
  "s_sleep 1\n\t" \
  "ds_read_b128 v[0:3], v101 offset:20480\n\t" \
  "ds_read_b128 v[4:7], v101 offset:21504\n\t" \
  "ds_read_b128 v[8:11], v101 offset:22528\n\t" \
  "ds_read_b128 v[12:15], v101 offset:23552\n\t" \
  "ds_read_b128 v[16:19], v101 offset:24576\n\t" \
  "ds_read_b128 v[20:23], v101 offset:25600\n\t" \
  "ds_read_b128 v[24:27], v101 offset:26624\n\t" \
  "ds_read_b128 v[28:31], v101 offset:27648\n\t" \
  "ds_read_b64_tr_b16 v[48:49], v108 offset:0\n\t" \
  "ds_read_b64_tr_b16 v[50:51], v108 offset:64\n\t" \
  "ds_read_b64_tr_b16 v[52:53], v108 offset:512\n\t" \
  "ds_read_b64_tr_b16 v[54:55], v108 offset:576\n\t" \
  "ds_read_b64_tr_b16 v[56:57], v108 offset:1024\n\t" \
  "ds_read_b64_tr_b16 v[58:59], v108 offset:1088\n\t" \
  "ds_read_b64_tr_b16 v[60:61], v108 offset:1536\n\t" \
  "ds_read_b64_tr_b16 v[62:63], v108 offset:1600\n\t" \
  "s_waitcnt vmcnt(3) lgkmcnt(8)\n\t" \
  "v_mfma_f32_16x16x32_bf16 v[64:67], v[0:3], v[84:87], v[64:67]\n\t" \
  "v_mfma_f32_16x16x32_bf16 v[68:71], v[16:19], v[84:87], v[68:71]\n\t" \
  "v_mfma_f32_16x16x32_bf16 v[64:67], v[4:7], v[88:91], v[64:67]\n\t" \
  "v_mfma_f32_16x16x32_bf16 v[68:71], v[20:23], v[88:91], v[68:71]\n\t" \
  "v_mfma_f32_16x16x32_bf16 v[64:67], v[8:11], v[92:95], v[64:67]\n\t" \
  "v_mfma_f32_16x16x32_bf16 v[68:71], v[24:27], v[92:95], v[68:71]\n\t" \
  "v_mfma_f32_16x16x32_bf16 v[64:67], v[12:15], v[96:99], v[64:67]\n\t" \
  "v_mfma_f32_16x16x32_bf16 v[68:71], v[28:31], v[96:99], v[68:71]\n\t" \
  "s_waitcnt lgkmcnt(0)\n\t" \
  "v_mfma_f32_16x16x32_bf16 v[72:75], v[48:51], v[84:87], 0\n\t" \
  "v_mfma_f32_16x16x32_bf16 v[72:75], v[52:55], v[88:91], v[72:75]\n\t" \
  "v_mfma_f32_16x16x32_bf16 v[72:75], v[56:59], v[92:95], v[72:75]\n\t" \
  "v_mfma_f32_16x16x32_bf16 v[72:75], v[60:63], v[96:99], v[72:75]\n\t" \
  "s_barrier\n\t" \
  "v_max3_f32 v76, v64, v65, v66\n\t" \
  "v_max3_f32 v77, v67, v68, v69\n\t" \
  "v_max3_f32 v76, v76, v70, v71\n\t" \
  "s_nop 4\n\t" \
  "v_max3_f32 v77, v77, v72, v73\n\t" \
  "v_max3_f32 v76, v76, v74, v75\n\t" \
  "v_max_f32 v76, v76, v77\n\t" \
  "v_mov_b32 v77, v76\n\t" \
  "s_nop 1\n\t" \
  "v_permlane16_swap_b32 v76, v77\n\t" \
  "v_max_f32 v76, v76, v77\n\t" \
  "v_mov_b32 v77, v76\n\t" \
  "s_nop 1\n\t" \
  "v_permlane32_swap_b32 v76, v77\n\t" \
  "v_max_f32 v76, v76, v77\n\t" \
  "v_mul_f32 v76, %[cs], v76\n\t" \
  "v_fma_f32 v64, v64, %[cs], -v76\n\t" \
  "v_fma_f32 v65, v65, %[cs], -v76\n\t" \
  "v_fma_f32 v66, v66, %[cs], -v76\n\t" \
  "v_fma_f32 v67, v67, %[cs], -v76\n\t" \
  "v_fma_f32 v68, v68, %[cs], -v76\n\t" \
  "v_fma_f32 v69, v69, %[cs], -v76\n\t" \
  "v_fma_f32 v70, v70, %[cs], -v76\n\t" \
  "v_fma_f32 v71, v71, %[cs], -v76\n\t" \
  "v_fma_f32 v72, v72, %[cs], -v76\n\t" \
  "v_fma_f32 v73, v73, %[cs], -v76\n\t" \
  "v_fma_f32 v74, v74, %[cs], -v76\n\t" \
  "v_fma_f32 v75, v75, %[cs], -v76\n\t" \
  "v_exp_f32 v64, v64\n\t" \
  "v_exp_f32 v65, v65\n\t" \
  "v_exp_f32 v66, v66\n\t" \
  "v_exp_f32 v67, v67\n\t" \
  "v_exp_f32 v68, v68\n\t" \
  "v_exp_f32 v69, v69\n\t" \
  "v_exp_f32 v70, v70\n\t" \
  "v_exp_f32 v71, v71\n\t" \
  "v_exp_f32 v72, v72\n\t" \
  "v_exp_f32 v73, v73\n\t" \
  "v_exp_f32 v74, v74\n\t" \
  "v_exp_f32 v75, v75\n\t" \
  "v_add_f32 v76, v72, v73\n\t" \
  "v_add_f32 v77, v74, v75\n\t" \
  "v_add_f32 v78, v64, v65\n\t" \
  "v_add_f32 v79, v66, v67\n\t" \
  "v_add_f32 v76, v76, v77\n\t" \
  "v_add_f32 v78, v78, v68\n\t" \
  "v_add_f32 v79, v79, v69\n\t" \
  "v_add_f32 v78, v78, v70\n\t" \
  "v_add_f32 v79, v79, v71\n\t" \
  "v_fma_f32 v76, v76, 0.5, v78\n\t" \
  "v_add_f32 v76, v76, v79\n\t" \
  "v_mov_b32 v77, v76\n\t" \
  "s_nop 1\n\t" \
  "v_permlane16_swap_b32 v76, v77\n\t" \
  "v_add_f32 v76, v76, v77\n\t" \
  "v_mov_b32 v77, v76\n\t" \
  "s_nop 1\n\t" \
  "v_permlane32_swap_b32 v76, v77\n\t" \
  "v_add_f32 v76, v76, v77\n\t" \
  "v_rcp_f32 v76, v76\n\t" \
  "s_nop 0\n\t" \
  "v_mul_f32 v64, v64, v76\n\t" \
  "v_mul_f32 v65, v65, v76\n\t" \
  "v_mul_f32 v66, v66, v76\n\t" \
  "v_mul_f32 v67, v67, v76\n\t" \
  "v_mul_f32 v68, v68, v76\n\t" \
  "v_mul_f32 v69, v69, v76\n\t" \
  "v_mul_f32 v70, v70, v76\n\t" \
  "v_mul_f32 v71, v71, v76\n\t" \
  "v_mul_f32 v72, v72, v76\n\t" \
  "v_mul_f32 v73, v73, v76\n\t" \
  "v_mul_f32 v74, v74, v76\n\t" \
  "v_mul_f32 v75, v75, v76\n\t" \
  "v_cvt_pk_bf16_f32 v0, v64, v68\n\t" \
  "v_cvt_pk_bf16_f32 v1, v65, v69\n\t" \
  "v_cvt_pk_bf16_f32 v2, v66, v70\n\t" \
  "v_cvt_pk_bf16_f32 v3, v67, v71\n\t" \
  "v_cvt_pk_bf16_f32 v4, v72, v73\n\t" \
  "v_cvt_pk_bf16_f32 v5, v74, v75\n\t" \
  "v_permlane32_swap_b32 v0, v2\n\t" \
  "v_permlane32_swap_b32 v1, v3\n\t" \
  "v_mov_b64 v[6:7], v[4:5]\n\t" \
  "s_mov_b32 exec_hi, 0\n\t" \
  "buffer_load_dword v118, v112, %[rh], s96 offen sc1\n\t" \
  "s_mov_b64 exec, -1\n\t" \
  "buffer_store_dwordx4 v[0:3], v110, %[rh], s94 offen sc1\n\t" \
  "v_permlane16_swap_b32 v4, v6\n\t" \
  "v_permlane16_swap_b32 v5, v7\n\t" \
  "buffer_store_dwordx4 v[4:7], v111, %[rh], s94 offen sc1\n\t" \
  "s_waitcnt vmcnt(3)\n\t" \
  "ds_write_b128 v101, v[32:35] offset:23552\n\t" \
  "ds_write_b128 v101, v[36:39] offset:24576\n\t" \
  "ds_write_b128 v101, v[40:43] offset:25600\n\t" \
  "s_add_i32 s77, s44, 2\n\t" \
  "s_min_u32 s77, s77, 63\n\t" \
  "v_cmp_gt_i32 vcc, s77, v119\n\t" \
  "s_cmp_lg_u64 vcc, 0\n\t" \
  "s_cselect_b32 s42, 32, 0\n\t" \
  "s_cmp_eq_u32 s44, 0\n\t" \
  "s_cselect_b32 s42, 32, s42\n\t" \
  "s_add_i32 s41, s56, 1950\n\t" \
  "s_sub_i32 s76, s92, s51\n\t" \
  "s_sub_i32 s77, s93, s53\n\t" \
  "s_cmp_eq_u32 s76, 0\n\t" \
  "s_cbranch_scc1 21f\n\t" \
  "v_cvt_f32_u32 v117, s76\n\t" \
  "v_cvt_f32_u32 v116, s77\n\t" \
  "v_rcp_f32 v117, v117\n\t" \
  "s_mov_b32 s51, s92\n\t" \
  "v_mul_f32 v117, v117, v116\n\t" \
  "v_max_f32 v117, 0x3f028f5c, v117\n\t" \
  "v_min_f32 v117, 0x3ffeb852, v117\n\t" \
  "v_rcp_f32 v116, v117\n\t" \
  "v_mul_f32 v117, 0x4f000000, v117\n\t" \
  "v_mul_f32 v116, 0x47800000, v116\n\t" \
  "v_cvt_u32_f32 v117, v117\n\t" \
  "v_cvt_u32_f32 v116, v116\n\t" \
  "s_mov_b32 s53, s93\n\t" \
  "v_readfirstlane_b32 s46, v117\n\t" \
  "v_readfirstlane_b32 s52, v116\n\t" \
  "21:\n\t" \
  "s_add_i32 s44, s44, 1\n\t" \
  "s_and_b32 s58, s58, 4\n\t" \
  "s_and_b32 s76, s44, 64\n\t" \
  "s_or_b32 s42, s42, s76\n\t" \
  "s_cbranch_scc0 20b\n\t" \
  "s_branch 99f\n\t" \
  "14:\n\t" \
  "s_sleep 3\n\t" \
  "s_add_u32 s59, s59, 0x10000\n\t" \
  "s_cmp_lt_u32 s59, 0x100000\n\t" \
  "s_cbranch_scc1 13b\n\t" \
  "s_mov_b64 exec, -1\n\t" \
  "s_mov_b32 s42, 16\n\t" \
  "s_branch 99f\n\t" \
  "8:\n\t" \
  "s_bitcmp1_b32 s58, 1\n\t" \
  "s_cbranch_scc1 4b\n\t" \
  "s_bitcmp1_b32 s58, 2\n\t" \
  "s_cbranch_scc1 5b\n\t" \
  "v_max3_u32 v116, v32, v33, v34\n\t" \
  "v_max3_u32 v117, v35, v36, v37\n\t" \
  "v_max3_u32 v116, v116, v38, v39\n\t" \
  "v_max_u32 v116, v116, v117\n\t" \
  "v_cmp_eq_u32 vcc, -1, v116\n\t" \
  "s_cbranch_vccz 5b\n\t" \
  "s_branch 0f\n\t" \
  "9:\n\t" \
  "s_bitcmp1_b32 s58, 2\n\t" \
  "s_cbranch_scc1 6b\n\t" \
  "v_max3_u32 v116, v40, v41, v42\n\t" \
  "v_max3_u32 v117, v43, v44, v45\n\t" \
  "v_max3_u32 v116, v116, v46, v47\n\t" \
  "v_max_u32 v116, v116, v117\n\t" \
  "v_cmp_eq_u32 vcc, -1, v116\n\t" \
  "s_cbranch_vccz 6b\n\t" \
  "s_or_b32 s58, s58, 2\n\t" \
  "0:\n\t" \
  "s_or_b32 s58, s58, 1\n\t" \
  "11:\n\t" \
  "buffer_load_dwordx4 v[32:35], v100, %[rh], s40 offen sc1\n\t" \
  "buffer_load_dwordx4 v[36:39], v100, %[rh], s76 offen sc1\n\t" \
  "buffer_load_dwordx4 v[40:43], v100, %[rh], s77 offen sc1\n\t" \
  "buffer_load_dwordx4 v[44:47], v100, %[rh], s78 offen sc1\n\t" \
  "s_waitcnt lgkmcnt(0)\n\t" \
  "s_branch 10b\n\t" \
  "17:\n\t" \
  "s_add_u32 s59, s59, 1\n\t" \
  "s_bitcmp1_b32 s58, 2\n\t" \
  "s_cbranch_scc1 3b\n\t" \
  "s_waitcnt vmcnt(0)\n\t" \
  "v_max_u32 v116, v32, v33\n\t" \
  "v_max3_u32 v116, v116, v34, v35\n\t" \
  "v_max3_u32 v116, v116, v36, v37\n\t" \
  "v_max3_u32 v116, v116, v38, v39\n\t" \
  "v_max3_u32 v116, v116, v40, v41\n\t" \
  "v_max3_u32 v116, v116, v42, v43\n\t" \
  "v_max3_u32 v116, v116, v44, v45\n\t" \
  "v_max3_u32 v116, v116, v46, v47\n\t" \
  "v_cmp_eq_u32 vcc, -1, v116\n\t" \
  "s_cbranch_vccz 3b\n\t" \
  "s_bitcmp1_b32 s59, 6\n\t" \
  "s_cbranch_scc1 12f\n\t" \
  "buffer_load_dwordx4 v[32:35], v100, %[rh], s40 offen sc1\n\t" \
  "buffer_load_dwordx4 v[36:39], v100, %[rh], s76 offen sc1\n\t" \
  "buffer_load_dwordx4 v[40:43], v100, %[rh], s77 offen sc1\n\t" \
  "buffer_load_dwordx4 v[44:47], v100, %[rh], s78 offen sc1\n\t" \
  "s_branch 17b\n\t" \
  "12:\n\t" \
  "s_mov_b32 s42, s58\n\t" \
  "s_branch 99f\n\t" \
  "93:\n\t" \
  "s_mov_b32 s58, s42\n\t" \
  "20:\n\t" \
  "s_and_b32 s76, s44, 7\n\t" \
  "s_lshl_b32 s76, s76, 15\n\t" \
  "s_add_i32 s40, s54, s76\n\t" \
  "s_and_b32 s76, s44, 3\n\t" \
  "s_lshl_b32 s45, s76, 14\n\t" \
  "s_add_i32 s47, s44, 1\n\t" \
  "s_min_u32 s47, s47, 63\n\t" \
  "s_lshr_b32 s76, s47, 1\n\t" \
  "s_lshl_b32 s47, s47, 20\n\t" \
  "s_add_i32 s47, s47, s55\n\t" \
  "s_bitcmp1_b64 %[sel], s76\n\t" \
  "s_cselect_b32 s76, 0x2000, 0\n\t" \
  "s_add_i32 s47, s47, s76\n\t" \
  "s_add_i32 s49, s44, -1\n\t" \
  "s_and_b32 s48, s49, 2\n\t" \
  "s_lshl_b32 s48, s48, 13\n\t" \
  "s_and_b32 s76, s49, 1\n\t" \
  "s_lshl_b32 s76, s76, 12\n\t" \
  "s_add_i32 s48, s48, s76\n\t" \
  "s_lshl_b32 s50, s53, 8\n\t" \
  "s_mov_b32 s59, 0\n\t" \
  "s_mov_b32 s92, s51\n\t" \
  "s_mov_b32 s93, s53\n\t" \
  "s_xor_b32 s76, s40, 0x400\n\t" \
  "s_xor_b32 s77, s40, 0x800\n\t" \
  "s_xor_b32 s78, s40, 0xc00\n\t" \
  "s_cmp_lg_u32 s58, 0\n\t" \
  "s_cbranch_scc1 11f\n\t" \
  "10:\n\t" \
  "ds_read_b128 v[48:51], v103 offset:32768\n\t" \
  "ds_read_b128 v[52:55], v103 offset:33792\n\t" \
  "ds_read_b128 v[56:59], v103 offset:34816\n\t" \
  "ds_read_b128 v[60:63], v103 offset:35840\n\t" \
  "ds_read_b128 v[16:19], v103 offset:49152\n\t" \
  "ds_read_b128 v[20:23], v103 offset:50176\n\t" \
  "ds_read_b128 v[24:27], v103 offset:51200\n\t" \
  "ds_read_b128 v[28:31], v103 offset:52224\n\t" \
  "ds_read_b128 v[84:87], v102 offset:16384\n\t" \
  "ds_read_b128 v[88:91], v102 offset:17408\n\t" \
  "ds_read_b128 v[92:95], v102 offset:18432\n\t" \
  "ds_read_b128 v[96:99], v102 offset:19456\n\t" \
  "s_waitcnt lgkmcnt(4)\n\t" \
  "ds_read_b128 v[0:3], v102 offset:49152\n\t" \
  "ds_read_b128 v[4:7], v102 offset:50176\n\t" \
  "ds_read_b128 v[8:11], v102 offset:51200\n\t" \
  "ds_read_b128 v[12:15], v102 offset:52224\n\t" \
  "s_bitcmp1_b32 s58, 1\n\t" \
  "s_cselect_b64 s[80:81], -1, 0\n\t" \
  "s_cmp_lg_u32 s58, 0\n\t" \
  "s_cbranch_scc1 17f\n\t" \
  "s_mov_b32 exec_hi, 0\n\t" \
  "16:\n\t" \
  "s_memtime vcc\n\t" \
  "s_waitcnt lgkmcnt(0)\n\t" \
  "s_sub_i32 s79, s41, vcc_lo\n\t" \
  "s_cmp_gt_i32 s79, 64\n\t" \
  "s_cbranch_scc1 16b\n\t" \
  "13:\n\t" \
  "buffer_load_dword v116, v112, %[rh], s48 offen sc1\n\t" \
  "s_waitcnt vmcnt(0)\n\t" \
  "15:\n\t" \
  "v_mov_b32 v117, 20000\n\t" \
  "v_cmp_ne_u32_sdwa vcc, v116, s49 src0_sel:BYTE_0 src1_sel:DWORD\n\t" \
  "v_subrev_u32 v116, s50, v116\n\t" \
  "v_ashrrev_i32 v116, 8, v116\n\t" \
  "s_nop 1\n\t" \
  "v_max_i32_dpp v116, v116, v116 quad_perm:[1,0,3,2] row_mask:0xf bank_mask:0xf\n\t" \
  "s_cbranch_vccnz 14f\n\t" \
  "s_nop 0\n\t" \
  "v_max_i32_dpp v116, v116, v116 quad_perm:[2,3,0,1] row_mask:0xf bank_mask:0xf\n\t" \
  "s_nop 1\n\t" \
  "v_max_i32_dpp v116, v116, v116 row_half_mirror row_mask:0xf bank_mask:0xf\n\t" \
  "s_nop 1\n\t" \
  "v_max_i32_dpp v116, v116, v116 row_mirror row_mask:0xf bank_mask:0xf\n\t" \
  "v_med3_i32 v116, v116, 0, v117\n\t" \
  "v_mul_u32_u24 v116, s52, v116\n\t" \
  "v_lshrrev_b32 v116, 16, v116\n\t" \
  "s_mov_b64 exec, -1\n\t" \
  "v_readfirstlane_b32 s84, v116\n\t" \
  "v_readlane_b32 s85, v116, 16\n\t" \
  "s_max_u32 s84, s84, s85\n\t" \
  "s_add_i32 s84, s84, s51\n\t" \
  "1:\n\t" \
  "s_memtime vcc\n\t" \
  "s_waitcnt lgkmcnt(0)\n\t" \
  "s_sub_i32 s79, s84, vcc_lo\n\t" \
  "s_cmp_gt_i32 s79, 128\n\t" \
  "s_cbranch_scc1 1b\n\t" \
  ".p2align 4\n\t" \
  "2:\n\t" \
  "s_sub_i32 s79, s79, 33\n\t" \
  "s_cmp_gt_i32 s79, 56\n\t" \
  "s_cbranch_scc1 2b\n\t" \
  "buffer_load_dwordx4 v[32:35], v100, %[rh], s40 offen sc1\n\t" \
  "s_nop 12\n\t" \
  "buffer_load_dwordx4 v[36:39], v100, %[rh], s76 offen sc1\n\t" \
  "s_nop 12\n\t" \
  "buffer_load_dwordx4 v[40:43], v100, %[rh], s77 offen sc1\n\t" \
  "s_nop 12\n\t" \
  "buffer_load_dwordx4 v[44:47], v100, %[rh], s78 offen sc1\n\t" \
  "s_memtime vcc\n\t" \
  "s_memrealtime s[82:83]\n\t" \
  "s_nop 4\n\t" \
  "s_memrealtime s[84:85]\n\t" \
  "s_nop 4\n\t" \
  "s_memrealtime s[56:57]\n\t" \
  "s_nop 4\n\t" \
  "s_memrealtime s[86:87]\n\t" \
  "s_nop 4\n\t" \
  "s_memrealtime s[88:89]\n\t" \
  "s_nop 4\n\t" \
  "s_memrealtime s[90:91]\n\t" \
  "s_mov_b32 s92, 0\n\t" \
  "s_waitcnt lgkmcnt(0)\n\t" \
  "s_cmp_eq_u32 s82, s84\n\t" \
  "s_addc_u32 s92, s92, 0\n\t" \
  "s_cmp_eq_u32 s82, s56\n\t" \
  "s_addc_u32 s92, s92, 0\n\t" \
  "s_cmp_eq_u32 s82, s86\n\t" \
  "s_addc_u32 s92, s92, 0\n\t" \
  "s_cmp_eq_u32 s82, s88\n\t" \
  "s_addc_u32 s92, s92, 0\n\t" \
  "s_cmp_eq_u32 s82, s90\n\t" \
  "s_addc_u32 s92, s92, 0\n\t" \
  "s_mul_i32 s92, s92, 24\n\t" \
  "s_add_i32 s92, s92, vcc_lo\n\t" \
  "s_lshl4_add_u32 s93, s82, 64\n\t" \
  "3:\n\t" \
  "v_mov_b64 v[68:69], 0\n\t" \
  "v_mov_b64 v[70:71], 0\n\t" \
  "v_mov_b64 v[72:73], 0\n\t" \
  "v_mov_b64 v[74:75], 0\n\t" \
  "v_mov_b64 v[76:77], 0\n\t" \
  "v_mov_b64 v[78:79], 0\n\t" \
  "v_mov_b64 v[80:81], 0\n\t" \
  "v_mov_b64 v[82:83], 0\n\t" \
  "s_waitcnt lgkmcnt(0)\n\t" \
  "s_waitcnt vmcnt(3)\n\t" \
  "v_smfmac_f32_16x16x64_bf16 v[68:71], v[32:35], v[152:159], v104\n\t" \
  "ds_write_b128 v101, v[32:35] offset:12288\n\t" \
  "v_smfmac_f32_16x16x64_bf16 v[72:75], v[32:35], v[160:167], v104\n\t" \
  "v_smfmac_f32_16x16x64_bf16 v[76:79], v[32:35], v[48:55], v104\n\t" \
  "v_smfmac_f32_16x16x64_bf16 v[80:83], v[32:35], v[56:63], v104\n\t" \
  "s_waitcnt vmcnt(2)\n\t" \
  "v_smfmac_f32_16x16x64_bf16 v[68:71], v[36:39], v[168:175], v104\n\t" \
  "ds_write_b128 v101, v[36:39] offset:13312\n\t" \
  "v_smfmac_f32_16x16x64_bf16 v[72:75], v[36:39], v[176:183], v104\n\t" \
  "v_smfmac_f32_16x16x64_bf16 v[76:79], v[36:39], v[184:191], v104\n\t" \
  "v_cmp_u_f32 vcc, v68, v69\n\t" \
  "v_smfmac_f32_16x16x64_bf16 v[80:83], v[36:39], v[192:199], v104\n\t" \
  "v_cmp_u_f32 s[82:83], v70, v71\n\t" \
  "s_waitcnt vmcnt(1)\n\t" \
  "v_smfmac_f32_16x16x64_bf16 v[68:71], v[40:43], v[16:23], v104\n\t" \
  "ds_write_b128 v101, v[40:43] offset:14336\n\t" \
  "v_smfmac_f32_16x16x64_bf16 v[72:75], v[40:43], v[24:31], v104\n\t" \
  "s_or_b64 vcc, vcc, s[82:83]\n\t" \
  "s_or_b64 vcc, vcc, s[80:81]\n\t" \
  "s_waitcnt lgkmcnt(1)\n\t" \
  "v_smfmac_f32_16x16x64_bf16 v[76:79], v[40:43], v[200:207], v104\n\t" \
  "s_cbranch_scc1 8f\n\t" \
  "5:\n\t" \
  "s_barrier\n\t" \
  "4:\n\t" \
  "s_memtime s[56:57]\n\t" \
  "ds_read_b128 v[48:51], v101 offset:0\n\t" \
  "v_smfmac_f32_16x16x64_bf16 v[80:83], v[40:43], v[208:215], v104\n\t" \
  "ds_read_b128 v[52:55], v101 offset:1024\n\t" \
  "ds_read_b128 v[56:59], v101 offset:4096\n\t" \
  "s_waitcnt vmcnt(0)\n\t" \
  "v_smfmac_f32_16x16x64_bf16 v[68:71], v[44:47], v[216:223], v104\n\t" \
  "v_smfmac_f32_16x16x64_bf16 v[72:75], v[44:47], v[224:231], v104\n\t" \
  "ds_write_b128 v101, v[44:47] offset:15360\n\t" \
  "v_smfmac_f32_16x16x64_bf16 v[76:79], v[44:47], v[232:239], v104\n\t" \
  "v_cmp_u_f32 vcc, v68, v69\n\t" \
  "v_smfmac_f32_16x16x64_bf16 v[80:83], v[44:47], v[240:247], v104\n\t" \
  "v_cmp_u_f32 s[82:83], v70, v71\n\t" \
  "s_waitcnt lgkmcnt(3)\n\t" \
  "v_smfmac_f32_16x16x64_bf16 v[68:71], v[48:51], a[0:7], v104\n\t" \
  "ds_read_b128 v[60:63], v101 offset:5120\n\t" \
  "ds_read_b128 v[64:67], v101 offset:8192\n\t" \
  "v_smfmac_f32_16x16x64_bf16 v[72:75], v[48:51], a[8:15], v104\n\t" \
  "s_or_b64 vcc, vcc, s[82:83]\n\t" \
  "s_waitcnt lgkmcnt(2)\n\t" \
  "v_smfmac_f32_16x16x64_bf16 v[68:71], v[52:55], a[16:23], v104\n\t" \
  "s_cbranch_scc1 9f\n\t" \
  "6:\n\t" \
  "s_barrier\n\t" \
  "v_smfmac_f32_16x16x64_bf16 v[72:75], v[52:55], a[24:31], v104\n\t" \
  "ds_read_b128 v[48:51], v101 offset:9216\n\t" \
  "v_smfmac_f32_16x16x64_bf16 v[68:71], v[56:59], a[96:103], v104\n\t" \
  "ds_read_b128 v[52:55], v101 offset:2048\n\t" \
  "v_smfmac_f32_16x16x64_bf16 v[72:75], v[56:59], a[104:111], v104\n\t" \
  "s_waitcnt lgkmcnt(3)\n\t" \
  "v_smfmac_f32_16x16x64_bf16 v[68:71], v[60:63], a[112:119], v104\n\t" \
  "ds_read_b128 v[56:59], v101 offset:3072\n\t" \
  "v_smfmac_f32_16x16x64_bf16 v[72:75], v[60:63], a[120:127], v104\n\t" \
  "s_waitcnt lgkmcnt(3)\n\t" \
  "v_smfmac_f32_16x16x64_bf16 v[68:71], v[64:67], a[192:199], v104\n\t" \
  "ds_read_b128 v[60:63], v101 offset:6144\n\t" \
  "v_smfmac_f32_16x16x64_bf16 v[72:75], v[64:67], a[200:207], v104\n\t" \
  "s_waitcnt lgkmcnt(3)\n\t" \
  "v_smfmac_f32_16x16x64_bf16 v[68:71], v[48:51], a[208:215], v104\n\t" \
  "ds_read_b128 v[64:67], v101 offset:7168\n\t" \
  "ds_read_b128 v[16:19], v103 offset:16384\n\t" \
  "global_load_dword v119, v109, %[kc] sc1\n\t" \
  "v_smfmac_f32_16x16x64_bf16 v[72:75], v[48:51], a[216:223], v104\n\t" \
  "s_waitcnt lgkmcnt(4)\n\t" \
  "v_smfmac_f32_16x16x64_bf16 v[68:71], v[52:55], v[84:91], v104\n\t" \
  "ds_read_b128 v[48:51], v101 offset:11264\n\t" \
  "ds_read_b128 v[20:23], v103 offset:17408\n\t" \
  "v_smfmac_f32_16x16x64_bf16 v[72:75], v[52:55], v[92:99], v104\n\t" \
  "s_waitcnt lgkmcnt(5)\n\t" \
  "v_smfmac_f32_16x16x64_bf16 v[68:71], v[56:59], a[64:71], v104\n\t" \
  "ds_read_b128 v[52:55], v101 offset:10240\n\t" \
  "ds_read_b128 v[24:27], v103 offset:18432\n\t" \
  "v_smfmac_f32_16x16x64_bf16 v[72:75], v[56:59], a[72:79], v104\n\t" \
  "ds_read_b128 v[28:31], v103 offset:19456\n\t" \
  "s_waitcnt lgkmcnt(7)\n\t" \
  "v_smfmac_f32_16x16x64_bf16 v[68:71], v[60:63], v[0:7], v104\n\t" \
  "v_smfmac_f32_16x16x64_bf16 v[72:75], v[60:63], v[8:15], v104\n\t" \
  "s_waitcnt lgkmcnt(6)\n\t" \
  "v_smfmac_f32_16x16x64_bf16 v[68:71], v[64:67], a[160:167], v104\n\t" \
  "ds_read_b128 v[0:3], v102 offset:0\n\t" \
  "v_smfmac_f32_16x16x64_bf16 v[72:75], v[64:67], a[168:175], v104\n\t" \
  "ds_read_b128 v[4:7], v102 offset:1024\n\t" \
  "s_waitcnt lgkmcnt(6)\n\t" \
  "v_smfmac_f32_16x16x64_bf16 v[68:71], v[48:51], v[120:127], v104\n\t" \
  "s_waitcnt lgkmcnt(4)\n\t" \
  "v_smfmac_f32_16x16x64_bf16 v[68:71], v[52:55], v[16:23], v104\n\t" \
  "ds_read_b128 v[8:11], v102 offset:2048\n\t" \
  "v_smfmac_f32_16x16x64_bf16 v[72:75], v[48:51], v[128:135], v104\n\t" \
  "ds_read_b128 v[12:15], v102 offset:3072\n\t" \
  "s_waitcnt lgkmcnt(4)\n\t" \
  "v_smfmac_f32_16x16x64_bf16 v[72:75], v[52:55], v[24:31], v104\n\t" \
  "v_add_f32 v44, v68, v70\n\t" \
  "v_smfmac_f32_16x16x64_bf16 v[76:79], v[56:59], a[80:87], v104\n\t" \
  "v_add_f32 v45, v69, v71\n\t" \
  "v_cvt_pk_bf16_f32 v44, v44, v45\n\t" \
  "v_smfmac_f32_16x16x64_bf16 v[80:83], v[56:59], a[88:95], v104\n\t" \
  "v_add_f32 v46, v72, v74\n\t" \
  "v_add_f32 v47, v73, v75\n\t" \
  "v_smfmac_f32_16x16x64_bf16 v[76:79], v[60:63], a[144:151], v104\n\t" \
  "v_cvt_pk_bf16_f32 v46, v46, v47\n\t" \
  "ds_write2st64_b32 v105, v44, v46 offset1:1\n\t" \
  "v_smfmac_f32_16x16x64_bf16 v[80:83], v[60:63], a[152:159], v104\n\t" \
  "ds_read_b128 v[56:59], v101 offset:0\n\t" \
  "v_smfmac_f32_16x16x64_bf16 v[76:79], v[64:67], a[176:183], v104\n\t" \
  "ds_read_b128 v[16:19], v102 offset:32768\n\t" \
  "v_smfmac_f32_16x16x64_bf16 v[80:83], v[64:67], a[184:191], v104\n\t" \
  "ds_read_b128 v[20:23], v102 offset:33792\n\t" \
  "v_smfmac_f32_16x16x64_bf16 v[76:79], v[48:51], v[136:143], v104\n\t" \
  "s_waitcnt lgkmcnt(3)\n\t" \
  "s_barrier\n\t" \
  "v_smfmac_f32_16x16x64_bf16 v[80:83], v[48:51], v[144:151], v104\n\t" \
  "v_smfmac_f32_16x16x64_bf16 v[76:79], v[52:55], a[240:247], v104\n\t" \
  "ds_read_b128 v[60:63], v101 offset:9216\n\t" \
  "ds_read_b128 v[24:27], v102 offset:34816\n\t" \
  "v_smfmac_f32_16x16x64_bf16 v[80:83], v[52:55], a[248:255], v104\n\t" \
  "ds_read_b128 v[64:67], v101 offset:2048\n\t" \
  "s_waitcnt lgkmcnt(5)\n\t" \
  "v_smfmac_f32_16x16x64_bf16 v[76:79], v[56:59], v[0:7], v104\n\t" \
  "ds_read_b128 v[48:51], v101 offset:4096\n\t" \
  "ds_read_b128 v[28:31], v102 offset:35840\n\t" \
  "v_smfmac_f32_16x16x64_bf16 v[80:83], v[56:59], v[8:15], v104\n\t" \
  "s_waitcnt lgkmcnt(4)\n\t" \
  "v_smfmac_f32_16x16x64_bf16 v[76:79], v[60:63], a[224:231], v104\n\t" \
  "ds_read_b128 v[52:55], v101 offset:1024\n\t" \
  "v_smfmac_f32_16x16x64_bf16 v[80:83], v[60:63], a[232:239], v104\n\t" \
  "ds_read_b128 v[0:3], v103 offset:0\n\t" \
  "buffer_load_dwordx4 v[32:35], v100, %[ru], s47 offen offset:0 sc1\n\t" \
  "s_waitcnt lgkmcnt(4)\n\t" \
  "v_smfmac_f32_16x16x64_bf16 v[76:79], v[64:67], a[48:55], v104\n\t" \
  "ds_read_b128 v[56:59], v101 offset:5120\n\t" \
  "v_smfmac_f32_16x16x64_bf16 v[80:83], v[64:67], a[56:63], v104\n\t" \
  "ds_read_b128 v[60:63], v101 offset:8192\n\t" \
  "ds_read_b128 v[4:7], v103 offset:1024\n\t" \
  "s_waitcnt lgkmcnt(6)\n\t" \
  "v_smfmac_f32_16x16x64_bf16 v[76:79], v[48:51], v[16:23], v104\n\t" \
  "ds_read_b128 v[8:11], v103 offset:2048\n\t" \
  "buffer_load_dwordx4 v[36:39], v100, %[ru], s47 offen offset:1024 sc1\n\t" \
  "s_waitcnt lgkmcnt(6)\n\t" \
  "v_smfmac_f32_16x16x64_bf16 v[80:83], v[48:51], v[24:31], v104\n\t" \
  "ds_read_b128 v[12:15], v103 offset:3072\n\t" \
  "s_waitcnt lgkmcnt(6)\n\t" \
  "v_smfmac_f32_16x16x64_bf16 v[76:79], v[52:55], a[32:39], v104\n\t" \
  "v_smfmac_f32_16x16x64_bf16 v[80:83], v[52:55], a[40:47], v104\n\t" \
  "s_waitcnt lgkmcnt(4)\n\t" \
  "v_smfmac_f32_16x16x64_bf16 v[76:79], v[56:59], a[128:135], v104\n\t" \
  "s_waitcnt lgkmcnt(2)\n\t" \
  "v_smfmac_f32_16x16x64_bf16 v[76:79], v[60:63], v[0:7], v104\n\t" \
  "v_smfmac_f32_16x16x64_bf16 v[80:83], v[56:59], a[136:143], v104\n\t" \
  "s_waitcnt lgkmcnt(0)\n\t" \
  "v_smfmac_f32_16x16x64_bf16 v[80:83], v[60:63], v[8:15], v104\n\t" \
  "v_add_f32 v20, v76, v78\n\t" \
  "v_add_f32 v21, v77, v79\n\t" \
  "v_cvt_pk_bf16_f32 v20, v20, v21\n\t" \
  "ds_write_b32 v106, v20\n\t" \
  "s_nop 3\n\t" \
  "v_add_f32 v23, v80, v82\n\t" \
  "v_add_f32 v21, v81, v83\n\t" \
  "v_cvt_pk_bf16_f32 v21, v23, v21\n\t" \
  "ds_write_b32 v106, v21 offset:256\n\t" \
  "s_waitcnt lgkmcnt(0)\n\t" \
  "s_barrier\n\t" \
  "s_waitcnt vmcnt(0)\n\t" \
  "s_barrier\n\t" \
  "ds_write_b128 v101, v[32:35] offset:26624\n\t" \
  "ds_write_b128 v101, v[36:39] offset:27648\n\t" \
  "s_add_i32 s77, s44, 2\n\t" \
  "s_min_u32 s77, s77, 63\n\t" \
  "v_cmp_gt_i32 vcc, s77, v119\n\t" \
  "s_cmp_lg_u64 vcc, 0\n\t" \
  "s_cselect_b32 s42, 32, 0\n\t" \
  "s_cmp_eq_u32 s44, 0\n\t" \
  "s_cselect_b32 s42, 32, s42\n\t" \
  "s_add_i32 s41, s56, 1950\n\t" \
  "s_sub_i32 s76, s92, s51\n\t" \
  "s_sub_i32 s77, s93, s53\n\t" \
  "s_cmp_eq_u32 s76, 0\n\t" \
  "s_cbranch_scc1 21f\n\t" \
  "v_cvt_f32_u32 v117, s76\n\t" \
  "v_cvt_f32_u32 v116, s77\n\t" \
  "v_rcp_f32 v117, v117\n\t" \
  "s_mov_b32 s51, s92\n\t" \
  "v_mul_f32 v117, v117, v116\n\t" \
  "v_max_f32 v117, 0x3f028f5c, v117\n\t" \
  "v_min_f32 v117, 0x3ffeb852, v117\n\t" \
  "v_rcp_f32 v116, v117\n\t" \
  "v_mul_f32 v117, 0x4f000000, v117\n\t" \
  "v_mul_f32 v116, 0x47800000, v116\n\t" \
  "v_cvt_u32_f32 v117, v117\n\t" \
  "v_cvt_u32_f32 v116, v116\n\t" \
  "s_mov_b32 s53, s93\n\t" \
  "v_readfirstlane_b32 s46, v117\n\t" \
  "v_readfirstlane_b32 s52, v116\n\t" \
  "21:\n\t" \
  "v_mov_b32 v0, -1\n\t" \
  "v_mov_b32 v1, -1\n\t" \
  "v_mov_b32 v2, -1\n\t" \
  "v_mov_b32 v3, -1\n\t" \
  "s_and_b32 s76, s49, 3\n\t" \
  "s_mul_i32 s76, s76, 0xc000\n\t" \
  "s_add_i32 s76, s76, 0x40000\n\t" \
  "buffer_store_dwordx4 v[0:3], v110, %[rh], s76 offen sc1\n\t" \
  "s_add_i32 s76, s44, 1\n\t" \
  "s_and_b32 s76, s76, 3\n\t" \
  "s_lshl_b32 s76, s76, 18\n\t" \
  "s_add_i32 s77, s76, 0x10000\n\t" \
  "buffer_store_dwordx4 v[0:3], v107, %[rh], s76 offen sc1\n\t" \
  "buffer_store_dwordx4 v[0:3], v107, %[rh], s77 offen sc1\n\t" \
  "buffer_store_dwordx4 v[0:3], v111, %[rh], s76 offen sc1\n\t" \
  "s_add_i32 s44, s44, 1\n\t" \
  "s_and_b32 s58, s58, 4\n\t" \
  "s_and_b32 s76, s44, 64\n\t" \
  "s_or_b32 s42, s42, s76\n\t" \
  "s_cbranch_scc0 20b\n\t" \
  "s_branch 99f\n\t" \
  "14:\n\t" \
  "s_sleep 3\n\t" \
  "s_add_u32 s59, s59, 0x10000\n\t" \
  "s_cmp_lt_u32 s59, 0x100000\n\t" \
  "s_cbranch_scc1 13b\n\t" \
  "s_mov_b64 exec, -1\n\t" \
  "s_mov_b32 s42, 16\n\t" \
  "s_branch 99f\n\t" \
  "8:\n\t" \
  "s_bitcmp1_b32 s58, 1\n\t" \
  "s_cbranch_scc1 4b\n\t" \
  "s_bitcmp1_b32 s58, 2\n\t" \
  "s_cbranch_scc1 5b\n\t" \
  "v_max3_u32 v116, v32, v33, v34\n\t" \
  "v_max3_u32 v117, v35, v36, v37\n\t" \
  "v_max3_u32 v116, v116, v38, v39\n\t" \
  "v_max_u32 v116, v116, v117\n\t" \
  "v_cmp_eq_u32 vcc, -1, v116\n\t" \
  "s_cbranch_vccz 5b\n\t" \
  "s_branch 0f\n\t" \
  "9:\n\t" \
  "s_bitcmp1_b32 s58, 2\n\t" \
  "s_cbranch_scc1 6b\n\t" \
  "v_max3_u32 v116, v40, v41, v42\n\t" \
  "v_max3_u32 v117, v43, v44, v45\n\t" \
  "v_max3_u32 v116, v116, v46, v47\n\t" \
  "v_max_u32 v116, v116, v117\n\t" \
  "v_cmp_eq_u32 vcc, -1, v116\n\t" \
  "s_cbranch_vccz 6b\n\t" \
  "s_or_b32 s58, s58, 2\n\t" \
  "0:\n\t" \
  "s_or_b32 s58, s58, 1\n\t" \
  "11:\n\t" \
  "buffer_load_dwordx4 v[32:35], v100, %[rh], s40 offen sc1\n\t" \
  "buffer_load_dwordx4 v[36:39], v100, %[rh], s76 offen sc1\n\t" \
  "buffer_load_dwordx4 v[40:43], v100, %[rh], s77 offen sc1\n\t" \
  "buffer_load_dwordx4 v[44:47], v100, %[rh], s78 offen sc1\n\t" \
  "s_waitcnt lgkmcnt(0)\n\t" \
  "s_branch 10b\n\t" \
  "17:\n\t" \
  "s_add_u32 s59, s59, 1\n\t" \
  "s_bitcmp1_b32 s58, 2\n\t" \
  "s_cbranch_scc1 3b\n\t" \
  "s_waitcnt vmcnt(0)\n\t" \
  "v_max_u32 v116, v32, v33\n\t" \
  "v_max3_u32 v116, v116, v34, v35\n\t" \
  "v_max3_u32 v116, v116, v36, v37\n\t" \
  "v_max3_u32 v116, v116, v38, v39\n\t" \
  "v_max3_u32 v116, v116, v40, v41\n\t" \
  "v_max3_u32 v116, v116, v42, v43\n\t" \
  "v_max3_u32 v116, v116, v44, v45\n\t" \
  "v_max3_u32 v116, v116, v46, v47\n\t" \
  "v_cmp_eq_u32 vcc, -1, v116\n\t" \
  "s_cbranch_vccz 3b\n\t" \
  "s_bitcmp1_b32 s59, 6\n\t" \
  "s_cbranch_scc1 12f\n\t" \
  "buffer_load_dwordx4 v[32:35], v100, %[rh], s40 offen sc1\n\t" \
  "buffer_load_dwordx4 v[36:39], v100, %[rh], s76 offen sc1\n\t" \
  "buffer_load_dwordx4 v[40:43], v100, %[rh], s77 offen sc1\n\t" \
  "buffer_load_dwordx4 v[44:47], v100, %[rh], s78 offen sc1\n\t" \
  "s_branch 17b\n\t" \
  "12:\n\t" \
  "s_mov_b32 s42, s58\n\t" \
  "s_branch 99f\n\t" \
  "99:\n\t" \
  "s_waitcnt vmcnt(0) lgkmcnt(0)\n\t" \
  ""
#define VO_BODY \
  "s_cmp_eq_u32 %[wv], 1\n\t" \
  "s_cbranch_scc1 91f\n\t" \
  "s_cmp_eq_u32 %[wv], 2\n\t" \
  "s_cbranch_scc1 92f\n\t" \
  "s_cmp_eq_u32 %[wv], 3\n\t" \
  "s_cbranch_scc1 93f\n\t" \
  "90:\n\t" \
  "s_mov_b32 s58, s42\n\t" \
  "20:\n\t" \
  "s_and_b32 s76, s44, 7\n\t" \
  "s_lshl_b32 s76, s76, 15\n\t" \
  "s_add_i32 s40, s54, s76\n\t" \
  "s_and_b32 s76, s44, 3\n\t" \
  "s_lshl_b32 s45, s76, 14\n\t" \
  "s_lshl_b32 s47, s76, 20\n\t" \
  "s_add_i32 s47, s47, s55\n\t" \
  "s_lshr_b32 s76, s47, 21\n\t" \
  "s_bitcmp1_b64 %[sel], s76\n\t" \
  "s_cselect_b32 s76, 0x2000, 0\n\t" \
  "s_add_i32 s47, s47, s76\n\t" \
  "s_add_i32 s49, s44, -1\n\t" \
  "s_and_b32 s48, s49, 2\n\t" \
  "s_lshl_b32 s48, s48, 13\n\t" \
  "s_and_b32 s76, s49, 1\n\t" \
  "s_lshl_b32 s76, s76, 12\n\t" \
  "s_add_i32 s48, s48, s76\n\t" \
  "s_lshl_b32 s50, s53, 8\n\t" \
  "s_mov_b32 s59, 0\n\t" \
  "s_mov_b32 s92, s51\n\t" \
  "s_mov_b32 s93, s53\n\t" \
  "s_xor_b32 s76, s40, 0x400\n\t" \
  "s_xor_b32 s77, s40, 0x800\n\t" \
  "s_xor_b32 s78, s40, 0xc00\n\t" \
  "s_cmp_lg_u32 s58, 0\n\t" \
  "s_cbranch_scc1 11f\n\t" \
  "10:\n\t" \
  "ds_read_b128 v[48:51], v102 offset:0\n\t" \
  "ds_read_b128 v[52:55], v102 offset:1024\n\t" \
  "ds_read_b128 v[56:59], v102 offset:2048\n\t" \
  "ds_read_b128 v[60:63], v102 offset:3072\n\t" \
  "ds_read_b128 v[0:3], v102 offset:16384\n\t" \
  "ds_read_b128 v[4:7], v102 offset:17408\n\t" \
  "ds_read_b128 v[8:11], v102 offset:18432\n\t" \
  "ds_read_b128 v[12:15], v102 offset:19456\n\t" \
  "ds_read_b128 v[16:19], v102 offset:32768\n\t" \
  "ds_read_b128 v[20:23], v102 offset:33792\n\t" \
  "ds_read_b128 v[24:27], v102 offset:34816\n\t" \
  "ds_read_b128 v[28:31], v102 offset:35840\n\t" \
  "s_waitcnt lgkmcnt(4)\n\t" \
  "ds_read_b128 v[80:83], v102 offset:49152\n\t" \
  "ds_read_b128 v[84:87], v102 offset:50176\n\t" \
  "ds_read_b128 v[88:91], v102 offset:51200\n\t" \
  "ds_read_b128 v[92:95], v102 offset:52224\n\t" \
  "s_bitcmp1_b32 s58, 1\n\t" \
  "s_cselect_b64 s[80:81], -1, 0\n\t" \
  "s_cmp_lg_u32 s58, 0\n\t" \
  "s_cbranch_scc1 17f\n\t" \
  "s_mov_b32 exec_hi, 0\n\t" \
  "16:\n\t" \
  "s_memtime vcc\n\t" \
  "s_waitcnt lgkmcnt(0)\n\t" \
  "s_sub_i32 s79, s41, vcc_lo\n\t" \
  "s_cmp_gt_i32 s79, 64\n\t" \
  "s_cbranch_scc1 16b\n\t" \
  "13:\n\t" \
  "buffer_load_dword v116, v112, %[rh], s48 offen sc1\n\t" \
  "s_waitcnt vmcnt(0)\n\t" \
  "15:\n\t" \
  "v_mov_b32 v117, 20000\n\t" \
  "v_cmp_ne_u32_sdwa vcc, v116, s49 src0_sel:BYTE_0 src1_sel:DWORD\n\t" \
  "v_subrev_u32 v116, s50, v116\n\t" \
  "v_ashrrev_i32 v116, 8, v116\n\t" \
  "s_nop 1\n\t" \
  "v_max_i32_dpp v116, v116, v116 quad_perm:[1,0,3,2] row_mask:0xf bank_mask:0xf\n\t" \
  "s_cbranch_vccnz 14f\n\t" \
  "s_nop 0\n\t" \
  "v_max_i32_dpp v116, v116, v116 quad_perm:[2,3,0,1] row_mask:0xf bank_mask:0xf\n\t" \
  "s_nop 1\n\t" \
  "v_max_i32_dpp v116, v116, v116 row_half_mirror row_mask:0xf bank_mask:0xf\n\t" \
  "s_nop 1\n\t" \
  "v_max_i32_dpp v116, v116, v116 row_mirror row_mask:0xf bank_mask:0xf\n\t" \
  "v_med3_i32 v116, v116, 0, v117\n\t" \
  "v_mul_u32_u24 v116, s52, v116\n\t" \
  "v_lshrrev_b32 v116, 16, v116\n\t" \
  "s_mov_b64 exec, -1\n\t" \
  "v_readfirstlane_b32 s84, v116\n\t" \
  "v_readlane_b32 s85, v116, 16\n\t" \
  "s_max_u32 s84, s84, s85\n\t" \
  "s_add_i32 s84, s84, s51\n\t" \
  "1:\n\t" \
  "s_memtime vcc\n\t" \
  "s_waitcnt lgkmcnt(0)\n\t" \
  "s_sub_i32 s79, s84, vcc_lo\n\t" \
  "s_cmp_gt_i32 s79, 128\n\t" \
  "s_cbranch_scc1 1b\n\t" \
  ".p2align 4\n\t" \
  "2:\n\t" \
  "s_sub_i32 s79, s79, 33\n\t" \
  "s_cmp_gt_i32 s79, 56\n\t" \
  "s_cbranch_scc1 2b\n\t" \
  "buffer_load_dwordx4 v[32:35], v100, %[rh], s40 offen sc1\n\t" \
  "s_nop 12\n\t" \
  "buffer_load_dwordx4 v[36:39], v100, %[rh], s76 offen sc1\n\t" \
  "s_nop 12\n\t" \
  "buffer_load_dwordx4 v[40:43], v100, %[rh], s77 offen sc1\n\t" \
  "s_nop 12\n\t" \
  "buffer_load_dwordx4 v[44:47], v100, %[rh], s78 offen sc1\n\t" \
  "s_memtime vcc\n\t" \
  "s_memrealtime s[82:83]\n\t" \
  "s_nop 4\n\t" \
  "s_memrealtime s[84:85]\n\t" \
  "s_nop 4\n\t" \
  "s_memrealtime s[56:57]\n\t" \
  "s_nop 4\n\t" \
  "s_memrealtime s[86:87]\n\t" \
  "s_nop 4\n\t" \
  "s_memrealtime s[88:89]\n\t" \
  "s_nop 4\n\t" \
  "s_memrealtime s[90:91]\n\t" \
  "s_mov_b32 s92, 0\n\t" \
  "s_waitcnt lgkmcnt(0)\n\t" \
  "s_cmp_eq_u32 s82, s84\n\t" \
  "s_addc_u32 s92, s92, 0\n\t" \
  "s_cmp_eq_u32 s82, s56\n\t" \
  "s_addc_u32 s92, s92, 0\n\t" \
  "s_cmp_eq_u32 s82, s86\n\t" \
  "s_addc_u32 s92, s92, 0\n\t" \
  "s_cmp_eq_u32 s82, s88\n\t" \
  "s_addc_u32 s92, s92, 0\n\t" \
  "s_cmp_eq_u32 s82, s90\n\t" \
  "s_addc_u32 s92, s92, 0\n\t" \
  "s_mul_i32 s92, s92, 24\n\t" \
  "s_add_i32 s92, s92, vcc_lo\n\t" \
  "s_lshl4_add_u32 s93, s82, 64\n\t" \
  "3:\n\t" \
  "v_mov_b64 v[72:73], 0\n\t" \
  "v_mov_b64 v[74:75], 0\n\t" \
  "v_mov_b64 v[76:77], 0\n\t" \
  "v_mov_b64 v[78:79], 0\n\t" \
  "s_waitcnt lgkmcnt(0)\n\t" \
  "s_waitcnt vmcnt(3)\n\t" \
  "v_smfmac_f32_16x16x64_bf16 v[72:75], v[32:35], v[48:55], v104\n\t" \
  "ds_write_b128 v101, v[32:35] offset:0\n\t" \
  "v_smfmac_f32_16x16x64_bf16 v[76:79], v[32:35], v[56:63], v104\n\t" \
  "s_waitcnt vmcnt(2)\n\t" \
  "ds_write_b128 v101, v[36:39] offset:1024\n\t" \
  "v_smfmac_f32_16x16x64_bf16 v[72:75], v[36:39], a[0:7], v104\n\t" \
  "v_smfmac_f32_16x16x64_bf16 v[76:79], v[36:39], a[8:15], v104\n\t" \
  "s_nop 3\n\t" \
  "v_cmp_u_f32 vcc, v72, v73\n\t" \
  "v_cmp_u_f32 s[82:83], v74, v75\n\t" \
  "s_waitcnt vmcnt(1)\n\t" \
  "v_smfmac_f32_16x16x64_bf16 v[72:75], v[40:43], a[16:23], v104\n\t" \
  "s_or_b64 vcc, vcc, s[82:83]\n\t" \
  "s_or_b64 vcc, vcc, s[80:81]\n\t" \
  "v_smfmac_f32_16x16x64_bf16 v[76:79], v[40:43], a[24:31], v104\n\t" \
  "s_waitcnt lgkmcnt(0)\n\t" \
  "s_cbranch_scc1 8f\n\t" \
  "5:\n\t" \
  "s_barrier\n\t" \
  "4:\n\t" \
  "s_memtime s[56:57]\n\t" \
  "ds_read_b128 v[48:51], v101 offset:4096\n\t" \
  "ds_read_b128 v[52:55], v101 offset:5120\n\t" \
  "ds_read_b128 v[56:59], v101 offset:9216\n\t" \
  "s_waitcnt vmcnt(0)\n\t" \
  "v_smfmac_f32_16x16x64_bf16 v[72:75], v[44:47], a[32:39], v104\n\t" \
  "ds_read_b128 v[60:63], v101 offset:8192\n\t" \
  "ds_read_b128 v[64:67], v101 offset:13312\n\t" \
  "v_smfmac_f32_16x16x64_bf16 v[76:79], v[44:47], a[40:47], v104\n\t" \
  "ds_read_b128 v[68:71], v101 offset:12288\n\t" \
  "ds_write_b128 v101, v[40:43] offset:2048\n\t" \
  "s_nop 1\n\t" \
  "v_cmp_u_f32 vcc, v72, v73\n\t" \
  "v_cmp_u_f32 s[82:83], v74, v75\n\t" \
  "s_waitcnt lgkmcnt(6)\n\t" \
  "v_smfmac_f32_16x16x64_bf16 v[72:75], v[48:51], v[0:7], v104\n\t" \
  "s_or_b64 vcc, vcc, s[82:83]\n\t" \
  "v_smfmac_f32_16x16x64_bf16 v[76:79], v[48:51], v[8:15], v104\n\t" \
  "s_cbranch_scc1 9f\n\t" \
  "6:\n\t" \
  "s_waitcnt lgkmcnt(5)\n\t" \
  "v_smfmac_f32_16x16x64_bf16 v[72:75], v[52:55], a[48:55], v104\n\t" \
  "ds_write_b128 v101, v[44:47] offset:3072\n\t" \
  "v_smfmac_f32_16x16x64_bf16 v[76:79], v[52:55], a[56:63], v104\n\t" \
  "v_mov_b64 v[40:41], 0\n\t" \
  "s_waitcnt lgkmcnt(5)\n\t" \
  "v_smfmac_f32_16x16x64_bf16 v[72:75], v[56:59], a[96:103], v104\n\t" \
  "v_mov_b64 v[42:43], 0\n\t" \
  "v_smfmac_f32_16x16x64_bf16 v[76:79], v[56:59], a[104:111], v104\n\t" \
  "s_waitcnt lgkmcnt(4)\n\t" \
  "v_smfmac_f32_16x16x64_bf16 v[72:75], v[60:63], v[16:23], v104\n\t" \
  "s_waitcnt lgkmcnt(0)\n\t" \
  "s_barrier\n\t" \
  "v_smfmac_f32_16x16x64_bf16 v[76:79], v[60:63], v[24:31], v104\n\t" \
  "ds_read_b128 v[48:51], v101 offset:6144\n\t" \
  "ds_read_b128 v[52:55], v101 offset:10240\n\t" \
  "v_smfmac_f32_16x16x64_bf16 v[72:75], v[64:67], a[144:151], v104\n\t" \
  "ds_read_b128 v[56:59], v101 offset:14336\n\t" \
  "ds_read_b128 v[60:63], v101 offset:7168\n\t" \
  "v_smfmac_f32_16x16x64_bf16 v[76:79], v[64:67], a[152:159], v104\n\t" \
  "ds_read_b128 v[32:35], v101 offset:11264\n\t" \
  "ds_read_b128 v[36:39], v101 offset:15360\n\t" \
  "v_smfmac_f32_16x16x64_bf16 v[72:75], v[68:71], v[80:87], v104\n\t" \
  "ds_read_b128 v[0:3], v103 offset:0\n\t" \
  "v_mov_b64 v[44:45], 0\n\t" \
  "v_smfmac_f32_16x16x64_bf16 v[76:79], v[68:71], v[88:95], v104\n\t" \
  "v_mov_b64 v[46:47], 0\n\t" \
  "s_waitcnt lgkmcnt(5)\n\t" \
  "v_smfmac_f32_16x16x64_bf16 v[72:75], v[48:51], a[64:71], v104\n\t" \
  "ds_read_b128 v[4:7], v103 offset:1024\n\t" \
  "v_mov_b64 v[80:81], 0\n\t" \
  "v_smfmac_f32_16x16x64_bf16 v[72:75], v[52:55], a[112:119], v104\n\t" \
  "v_mov_b64 v[82:83], 0\n\t" \
  "s_waitcnt lgkmcnt(2)\n\t" \
  "v_smfmac_f32_16x16x64_bf16 v[72:75], v[56:59], a[160:167], v104\n\t" \
  "ds_read_b128 v[8:11], v103 offset:2048\n\t" \
  "v_mov_b64 v[84:85], 0\n\t" \
  "v_smfmac_f32_16x16x64_bf16 v[72:75], v[60:63], a[80:87], v104\n\t" \
  "ds_read_b128 v[12:15], v103 offset:3072\n\t" \
  "v_mov_b64 v[86:87], 0\n\t" \
  "v_smfmac_f32_16x16x64_bf16 v[72:75], v[32:35], a[128:135], v104\n\t" \
  "ds_read_b128 v[16:19], v103 offset:16384\n\t" \
  "v_mov_b64 v[88:89], 0\n\t" \
  "v_smfmac_f32_16x16x64_bf16 v[72:75], v[36:39], a[176:183], v104\n\t" \
  "ds_read_b128 v[20:23], v103 offset:17408\n\t" \
  "v_mov_b64 v[90:91], 0\n\t" \
  "v_smfmac_f32_16x16x64_bf16 v[76:79], v[48:51], a[72:79], v104\n\t" \
  "ds_read_b128 v[24:27], v103 offset:18432\n\t" \
  "v_mov_b64 v[92:93], 0\n\t" \
  "v_smfmac_f32_16x16x64_bf16 v[76:79], v[52:55], a[120:127], v104\n\t" \
  "v_add_f32 v96, v72, v74\n\t" \
  "v_add_f32 v97, v73, v75\n\t" \
  "v_cvt_pk_bf16_f32 v96, v96, v97\n\t" \
  "ds_write_b32 v105, v96\n\t" \
  "v_smfmac_f32_16x16x64_bf16 v[76:79], v[56:59], a[168:175], v104\n\t" \
  "ds_read_b128 v[28:31], v103 offset:19456\n\t" \
  "v_mov_b64 v[94:95], 0\n\t" \
  "v_smfmac_f32_16x16x64_bf16 v[76:79], v[60:63], a[88:95], v104\n\t" \
  "v_smfmac_f32_16x16x64_bf16 v[76:79], v[32:35], a[136:143], v104\n\t" \
  "v_smfmac_f32_16x16x64_bf16 v[76:79], v[36:39], a[184:191], v104\n\t" \
  "s_waitcnt lgkmcnt(1)\n\t" \
  "s_barrier\n\t" \
  "ds_read_b64_tr_b16 v[96:97], v107 offset:0\n\t" \
  "ds_read_b64_tr_b16 v[98:99], v107 offset:64\n\t" \
  "ds_read_b64_tr_b16 v[56:57], v107 offset:1024\n\t" \
  "ds_read_b64_tr_b16 v[58:59], v107 offset:1088\n\t" \
  "s_nop 1\n\t" \
  "v_add_f32 v116, v76, v78\n\t" \
  "v_add_f32 v117, v77, v79\n\t" \
  "v_cvt_pk_bf16_f32 v116, v116, v117\n\t" \
  "ds_write_b32 v105, v116 offset:256\n\t" \
  "s_waitcnt lgkmcnt(3)\n\t" \
  "v_smfmac_f32_16x16x64_bf16 v[40:43], v[96:99], a[192:199], v106\n\t" \
  "v_smfmac_f32_16x16x64_bf16 v[44:47], v[96:99], a[224:231], v106\n\t" \
  "s_waitcnt lgkmcnt(0)\n\t" \
  "s_barrier\n\t" \
  "ds_read_b64_tr_b16 v[52:53], v107 offset:256\n\t" \
  "ds_read_b64_tr_b16 v[54:55], v107 offset:320\n\t" \
  "v_smfmac_f32_16x16x64_bf16 v[40:43], v[56:59], a[208:215], v106\n\t" \
  "ds_read_b64_tr_b16 v[60:61], v107 offset:1280\n\t" \
  "ds_read_b64_tr_b16 v[62:63], v107 offset:1344\n\t" \
  "v_smfmac_f32_16x16x64_bf16 v[44:47], v[56:59], v[0:7], v106\n\t" \
  "v_smfmac_f32_16x16x64_bf16 v[80:83], v[96:99], a[240:247], v106\n\t" \
  "v_mov_b64 v[32:33], 0\n\t" \
  "v_smfmac_f32_16x16x64_bf16 v[84:87], v[96:99], v[136:143], v106\n\t" \
  "v_mov_b64 v[34:35], 0\n\t" \
  "v_smfmac_f32_16x16x64_bf16 v[80:83], v[56:59], v[120:127], v106\n\t" \
  "v_mov_b64 v[36:37], 0\n\t" \
  "v_smfmac_f32_16x16x64_bf16 v[84:87], v[56:59], v[16:23], v106\n\t" \
  "s_waitcnt lgkmcnt(2)\n\t" \
  "v_smfmac_f32_16x16x64_bf16 v[40:43], v[52:55], a[200:207], v106\n\t" \
  "v_smfmac_f32_16x16x64_bf16 v[44:47], v[52:55], a[232:239], v106\n\t" \
  "s_xor_b32 s76, s47, 0x40000\n\t" \
  "s_waitcnt lgkmcnt(0)\n\t" \
  "v_smfmac_f32_16x16x64_bf16 v[40:43], v[60:63], a[216:223], v106\n\t" \
  "v_mov_b64 v[38:39], 0\n\t" \
  "s_xor_b32 s77, s47, 0x80000\n\t" \
  "v_smfmac_f32_16x16x64_bf16 v[44:47], v[60:63], v[8:15], v106\n\t" \
  "s_xor_b32 s78, s47, 0xc0000\n\t" \
  "v_smfmac_f32_16x16x64_bf16 v[80:83], v[52:55], a[248:255], v106\n\t" \
  "ds_read_b128 v[0:3], v103 offset:32768\n\t" \
  "v_cvt_pk_bf16_f32 v64, v40, v41\n\t" \
  "v_smfmac_f32_16x16x64_bf16 v[84:87], v[52:55], v[144:151], v106\n\t" \
  "ds_read_b128 v[4:7], v103 offset:33792\n\t" \
  "v_cvt_pk_bf16_f32 v65, v42, v43\n\t" \
  "v_smfmac_f32_16x16x64_bf16 v[80:83], v[60:63], v[128:135], v106\n\t" \
  "ds_read_b128 v[8:11], v103 offset:34816\n\t" \
  "v_cvt_pk_bf16_f32 v66, v44, v45\n\t" \
  "v_smfmac_f32_16x16x64_bf16 v[84:87], v[60:63], v[24:31], v106\n\t" \
  "ds_read_b128 v[12:15], v103 offset:35840\n\t" \
  "v_cvt_pk_bf16_f32 v67, v46, v47\n\t" \
  "v_smfmac_f32_16x16x64_bf16 v[88:91], v[96:99], v[152:159], v106\n\t" \
  "v_permlane16_swap_b32 v64, v66\n\t" \
  "v_cvt_pk_bf16_f32 v68, v80, v81\n\t" \
  "v_smfmac_f32_16x16x64_bf16 v[92:95], v[96:99], v[184:191], v106\n\t" \
  "v_permlane16_swap_b32 v65, v67\n\t" \
  "buffer_store_dwordx4 v[64:67], v108, %[ru], s47 offen sc1\n\t" \
  "v_cvt_pk_bf16_f32 v69, v82, v83\n\t" \
  "v_smfmac_f32_16x16x64_bf16 v[88:91], v[52:55], v[160:167], v106\n\t" \
  "v_cvt_pk_bf16_f32 v70, v84, v85\n\t" \
  "v_smfmac_f32_16x16x64_bf16 v[92:95], v[52:55], v[192:199], v106\n\t" \
  "v_cvt_pk_bf16_f32 v71, v86, v87\n\t" \
  "v_smfmac_f32_16x16x64_bf16 v[88:91], v[56:59], v[168:175], v106\n\t" \
  "ds_read_b128 v[16:19], v103 offset:49152\n\t" \
  "v_permlane16_swap_b32 v68, v70\n\t" \
  "s_waitcnt lgkmcnt(3)\n\t" \
  "v_smfmac_f32_16x16x64_bf16 v[92:95], v[56:59], v[0:7], v106\n\t" \
  "ds_read_b128 v[20:23], v103 offset:50176\n\t" \
  "v_permlane16_swap_b32 v69, v71\n\t" \
  "buffer_store_dwordx4 v[68:71], v108, %[ru], s76 offen sc1\n\t" \
  "v_smfmac_f32_16x16x64_bf16 v[88:91], v[60:63], v[176:183], v106\n\t" \
  "ds_read_b128 v[24:27], v103 offset:51200\n\t" \
  "s_waitcnt lgkmcnt(3)\n\t" \
  "v_smfmac_f32_16x16x64_bf16 v[92:95], v[60:63], v[8:15], v106\n\t" \
  "ds_read_b128 v[28:31], v103 offset:52224\n\t" \
  "v_smfmac_f32_16x16x64_bf16 v[32:35], v[96:99], v[200:207], v106\n\t" \
  "v_cvt_pk_bf16_f32 v72, v88, v89\n\t" \
  "v_smfmac_f32_16x16x64_bf16 v[36:39], v[96:99], v[232:239], v106\n\t" \
  "v_cvt_pk_bf16_f32 v73, v90, v91\n\t" \
  "v_smfmac_f32_16x16x64_bf16 v[32:35], v[52:55], v[208:215], v106\n\t" \
  "v_cvt_pk_bf16_f32 v74, v92, v93\n\t" \
  "v_smfmac_f32_16x16x64_bf16 v[36:39], v[52:55], v[240:247], v106\n\t" \
  "v_cvt_pk_bf16_f32 v75, v94, v95\n\t" \
  "v_smfmac_f32_16x16x64_bf16 v[32:35], v[56:59], v[216:223], v106\n\t" \
  "v_permlane16_swap_b32 v72, v74\n\t" \
  "v_smfmac_f32_16x16x64_bf16 v[32:35], v[60:63], v[224:231], v106\n\t" \
  "v_permlane16_swap_b32 v73, v75\n\t" \
  "buffer_store_dwordx4 v[72:75], v108, %[ru], s77 offen sc1\n\t" \
  "s_waitcnt lgkmcnt(2)\n\t" \
  "v_smfmac_f32_16x16x64_bf16 v[36:39], v[56:59], v[16:23], v106\n\t" \
  "s_waitcnt lgkmcnt(0)\n\t" \
  "v_smfmac_f32_16x16x64_bf16 v[36:39], v[60:63], v[24:31], v106\n\t" \
  "v_cvt_pk_bf16_f32 v76, v32, v33\n\t" \
  "v_cvt_pk_bf16_f32 v77, v34, v35\n\t" \
  "s_nop 5\n\t" \
  "v_cvt_pk_bf16_f32 v78, v36, v37\n\t" \
  "v_cvt_pk_bf16_f32 v79, v38, v39\n\t" \
  "s_nop 0\n\t" \
  "v_permlane16_swap_b32 v76, v78\n\t" \
  "v_permlane16_swap_b32 v77, v79\n\t" \
  "buffer_store_dwordx4 v[76:79], v108, %[ru], s78 offen sc1\n\t" \
  "s_memtime s[76:77]\n\t" \
  "s_mov_b32 s42, 0\n\t" \
  "s_waitcnt lgkmcnt(0)\n\t" \
  "s_mov_b32 s57, s76\n\t" \
  "s_add_i32 s41, s56, 1950\n\t" \
  "s_sub_i32 s76, s92, s51\n\t" \
  "s_sub_i32 s77, s93, s53\n\t" \
  "s_cmp_eq_u32 s76, 0\n\t" \
  "s_cbranch_scc1 21f\n\t" \
  "v_cvt_f32_u32 v117, s76\n\t" \
  "v_cvt_f32_u32 v116, s77\n\t" \
  "v_rcp_f32 v117, v117\n\t" \
  "s_mov_b32 s51, s92\n\t" \
  "v_mul_f32 v117, v117, v116\n\t" \
  "v_max_f32 v117, 0x3f028f5c, v117\n\t" \
  "v_min_f32 v117, 0x3ffeb852, v117\n\t" \
  "v_rcp_f32 v116, v117\n\t" \
  "v_mul_f32 v117, 0x4f000000, v117\n\t" \
  "v_mul_f32 v116, 0x47800000, v116\n\t" \
  "v_cvt_u32_f32 v117, v117\n\t" \
  "v_cvt_u32_f32 v116, v116\n\t" \
  "s_mov_b32 s53, s93\n\t" \
  "v_readfirstlane_b32 s46, v117\n\t" \
  "v_readfirstlane_b32 s52, v116\n\t" \
  "21:\n\t" \
  "s_add_i32 s44, s44, 1\n\t" \
  "s_and_b32 s58, s58, 4\n\t" \
  "s_and_b32 s76, s44, 64\n\t" \
  "s_or_b32 s42, s42, s76\n\t" \
  "s_cbranch_scc0 20b\n\t" \
  "s_branch 99f\n\t" \
  "14:\n\t" \
  "s_sleep 3\n\t" \
  "s_add_u32 s59, s59, 0x10000\n\t" \
  "s_cmp_lt_u32 s59, 0x100000\n\t" \
  "s_cbranch_scc1 13b\n\t" \
  "s_mov_b64 exec, -1\n\t" \
  "s_mov_b32 s42, 16\n\t" \
  "s_branch 99f\n\t" \
  "8:\n\t" \
  "s_bitcmp1_b32 s58, 1\n\t" \
  "s_cbranch_scc1 4b\n\t" \
  "s_bitcmp1_b32 s58, 2\n\t" \
  "s_cbranch_scc1 5b\n\t" \
  "v_max3_u32 v116, v32, v33, v34\n\t" \
  "v_max3_u32 v117, v35, v36, v37\n\t" \
  "v_max3_u32 v116, v116, v38, v39\n\t" \
  "v_max_u32 v116, v116, v117\n\t" \
  "v_cmp_eq_u32 vcc, -1, v116\n\t" \
  "s_cbranch_vccz 5b\n\t" \
  "s_branch 0f\n\t" \
  "9:\n\t" \
  "s_bitcmp1_b32 s58, 2\n\t" \
  "s_cbranch_scc1 6b\n\t" \
  "v_max3_u32 v116, v40, v41, v42\n\t" \
  "v_max3_u32 v117, v43, v44, v45\n\t" \
  "v_max3_u32 v116, v116, v46, v47\n\t" \
  "v_max_u32 v116, v116, v117\n\t" \
  "v_cmp_eq_u32 vcc, -1, v116\n\t" \
  "s_cbranch_vccz 6b\n\t" \
  "s_or_b32 s58, s58, 2\n\t" \
  "0:\n\t" \
  "s_or_b32 s58, s58, 1\n\t" \
  "11:\n\t" \
  "buffer_load_dwordx4 v[32:35], v100, %[rh], s40 offen sc1\n\t" \
  "buffer_load_dwordx4 v[36:39], v100, %[rh], s76 offen sc1\n\t" \
  "buffer_load_dwordx4 v[40:43], v100, %[rh], s77 offen sc1\n\t" \
  "buffer_load_dwordx4 v[44:47], v100, %[rh], s78 offen sc1\n\t" \
  "s_waitcnt lgkmcnt(0)\n\t" \
  "s_branch 10b\n\t" \
  "17:\n\t" \
  "s_add_u32 s59, s59, 1\n\t" \
  "s_bitcmp1_b32 s58, 2\n\t" \
  "s_cbranch_scc1 3b\n\t" \
  "s_waitcnt vmcnt(0)\n\t" \
  "v_max_u32 v116, v32, v33\n\t" \
  "v_max3_u32 v116, v116, v34, v35\n\t" \
  "v_max3_u32 v116, v116, v36, v37\n\t" \
  "v_max3_u32 v116, v116, v38, v39\n\t" \
  "v_max3_u32 v116, v116, v40, v41\n\t" \
  "v_max3_u32 v116, v116, v42, v43\n\t" \
  "v_max3_u32 v116, v116, v44, v45\n\t" \
  "v_max3_u32 v116, v116, v46, v47\n\t" \
  "v_cmp_eq_u32 vcc, -1, v116\n\t" \
  "s_cbranch_vccz 3b\n\t" \
  "s_bitcmp1_b32 s59, 6\n\t" \
  "s_cbranch_scc1 12f\n\t" \
  "buffer_load_dwordx4 v[32:35], v100, %[rh], s40 offen sc1\n\t" \
  "buffer_load_dwordx4 v[36:39], v100, %[rh], s76 offen sc1\n\t" \
  "buffer_load_dwordx4 v[40:43], v100, %[rh], s77 offen sc1\n\t" \
  "buffer_load_dwordx4 v[44:47], v100, %[rh], s78 offen sc1\n\t" \
  "s_branch 17b\n\t" \
  "12:\n\t" \
  "s_mov_b32 s42, s58\n\t" \
  "s_branch 99f\n\t" \
  "91:\n\t" \
  "s_mov_b32 s58, s42\n\t" \
  "20:\n\t" \
  "s_and_b32 s76, s44, 7\n\t" \
  "s_lshl_b32 s76, s76, 15\n\t" \
  "s_add_i32 s40, s54, s76\n\t" \
  "s_and_b32 s76, s44, 3\n\t" \
  "s_lshl_b32 s45, s76, 14\n\t" \
  "s_lshl_b32 s47, s76, 20\n\t" \
  "s_add_i32 s47, s47, s55\n\t" \
  "s_lshr_b32 s76, s47, 21\n\t" \
  "s_bitcmp1_b64 %[sel], s76\n\t" \
  "s_cselect_b32 s76, 0x2000, 0\n\t" \
  "s_add_i32 s47, s47, s76\n\t" \
  "s_add_i32 s49, s44, -1\n\t" \
  "s_and_b32 s48, s49, 2\n\t" \
  "s_lshl_b32 s48, s48, 13\n\t" \
  "s_and_b32 s76, s49, 1\n\t" \
  "s_lshl_b32 s76, s76, 12\n\t" \
  "s_add_i32 s48, s48, s76\n\t" \
  "s_lshl_b32 s50, s53, 8\n\t" \
  "s_mov_b32 s59, 0\n\t" \
  "s_mov_b32 s92, s51\n\t" \
  "s_mov_b32 s93, s53\n\t" \
  "s_xor_b32 s76, s40, 0x400\n\t" \
  "s_xor_b32 s77, s40, 0x800\n\t" \
  "s_xor_b32 s78, s40, 0xc00\n\t" \
  "s_cmp_lg_u32 s58, 0\n\t" \
  "s_cbranch_scc1 11f\n\t" \
  "10:\n\t" \
  "ds_read_b128 v[48:51], v102 offset:16384\n\t" \
  "ds_read_b128 v[52:55], v102 offset:17408\n\t" \
  "ds_read_b128 v[56:59], v102 offset:18432\n\t" \
  "ds_read_b128 v[60:63], v102 offset:19456\n\t" \
  "ds_read_b128 v[0:3], v102 offset:32768\n\t" \
  "ds_read_b128 v[4:7], v102 offset:33792\n\t" \
  "ds_read_b128 v[8:11], v102 offset:34816\n\t" \
  "ds_read_b128 v[12:15], v102 offset:35840\n\t" \
  "ds_read_b128 v[16:19], v102 offset:49152\n\t" \
  "ds_read_b128 v[20:23], v102 offset:50176\n\t" \
  "ds_read_b128 v[24:27], v102 offset:51200\n\t" \
  "ds_read_b128 v[28:31], v102 offset:52224\n\t" \
  "s_waitcnt lgkmcnt(4)\n\t" \
  "ds_read_b128 v[80:83], v102 offset:0\n\t" \
  "ds_read_b128 v[84:87], v102 offset:1024\n\t" \
  "ds_read_b128 v[88:91], v102 offset:2048\n\t" \
  "ds_read_b128 v[92:95], v102 offset:3072\n\t" \
  "s_bitcmp1_b32 s58, 1\n\t" \
  "s_cselect_b64 s[80:81], -1, 0\n\t" \
  "s_cmp_lg_u32 s58, 0\n\t" \
  "s_cbranch_scc1 17f\n\t" \
  "s_mov_b32 exec_hi, 0\n\t" \
  "16:\n\t" \
  "s_memtime vcc\n\t" \
  "s_waitcnt lgkmcnt(0)\n\t" \
  "s_sub_i32 s79, s41, vcc_lo\n\t" \
  "s_cmp_gt_i32 s79, 64\n\t" \
  "s_cbranch_scc1 16b\n\t" \
  "13:\n\t" \
  "buffer_load_dword v116, v112, %[rh], s48 offen sc1\n\t" \
  "s_waitcnt vmcnt(0)\n\t" \
  "15:\n\t" \
  "v_mov_b32 v117, 20000\n\t" \
  "v_cmp_ne_u32_sdwa vcc, v116, s49 src0_sel:BYTE_0 src1_sel:DWORD\n\t" \
  "v_subrev_u32 v116, s50, v116\n\t" \
  "v_ashrrev_i32 v116, 8, v116\n\t" \
  "s_nop 1\n\t" \
  "v_max_i32_dpp v116, v116, v116 quad_perm:[1,0,3,2] row_mask:0xf bank_mask:0xf\n\t" \
  "s_cbranch_vccnz 14f\n\t" \
  "s_nop 0\n\t" \
  "v_max_i32_dpp v116, v116, v116 quad_perm:[2,3,0,1] row_mask:0xf bank_mask:0xf\n\t" \
  "s_nop 1\n\t" \
  "v_max_i32_dpp v116, v116, v116 row_half_mirror row_mask:0xf bank_mask:0xf\n\t" \
  "s_nop 1\n\t" \
  "v_max_i32_dpp v116, v116, v116 row_mirror row_mask:0xf bank_mask:0xf\n\t" \
  "v_med3_i32 v116, v116, 0, v117\n\t" \
  "v_mul_u32_u24 v116, s52, v116\n\t" \
  "v_lshrrev_b32 v116, 16, v116\n\t" \
  "s_mov_b64 exec, -1\n\t" \
  "v_readfirstlane_b32 s84, v116\n\t" \
  "v_readlane_b32 s85, v116, 16\n\t" \
  "s_max_u32 s84, s84, s85\n\t" \
  "s_add_i32 s84, s84, s51\n\t" \
  "1:\n\t" \
  "s_memtime vcc\n\t" \
  "s_waitcnt lgkmcnt(0)\n\t" \
  "s_sub_i32 s79, s84, vcc_lo\n\t" \
  "s_cmp_gt_i32 s79, 128\n\t" \
  "s_cbranch_scc1 1b\n\t" \
  ".p2align 4\n\t" \
  "2:\n\t" \
  "s_sub_i32 s79, s79, 33\n\t" \
  "s_cmp_gt_i32 s79, 56\n\t" \
  "s_cbranch_scc1 2b\n\t" \
  "buffer_load_dwordx4 v[32:35], v100, %[rh], s40 offen sc1\n\t" \
  "s_nop 12\n\t" \
  "buffer_load_dwordx4 v[36:39], v100, %[rh], s76 offen sc1\n\t" \
  "s_nop 12\n\t" \
  "buffer_load_dwordx4 v[40:43], v100, %[rh], s77 offen sc1\n\t" \
  "s_nop 12\n\t" \
  "buffer_load_dwordx4 v[44:47], v100, %[rh], s78 offen sc1\n\t" \
  "s_memtime vcc\n\t" \
  "s_memrealtime s[82:83]\n\t" \
  "s_nop 4\n\t" \
  "s_memrealtime s[84:85]\n\t" \
  "s_nop 4\n\t" \
  "s_memrealtime s[56:57]\n\t" \
  "s_nop 4\n\t" \
  "s_memrealtime s[86:87]\n\t" \
  "s_nop 4\n\t" \
  "s_memrealtime s[88:89]\n\t" \
  "s_nop 4\n\t" \
  "s_memrealtime s[90:91]\n\t" \
  "s_mov_b32 s92, 0\n\t" \
  "s_waitcnt lgkmcnt(0)\n\t" \
  "s_cmp_eq_u32 s82, s84\n\t" \
  "s_addc_u32 s92, s92, 0\n\t" \
  "s_cmp_eq_u32 s82, s56\n\t" \
  "s_addc_u32 s92, s92, 0\n\t" \
  "s_cmp_eq_u32 s82, s86\n\t" \
  "s_addc_u32 s92, s92, 0\n\t" \
  "s_cmp_eq_u32 s82, s88\n\t" \
  "s_addc_u32 s92, s92, 0\n\t" \
  "s_cmp_eq_u32 s82, s90\n\t" \
  "s_addc_u32 s92, s92, 0\n\t" \
  "s_mul_i32 s92, s92, 24\n\t" \
  "s_add_i32 s92, s92, vcc_lo\n\t" \
  "s_lshl4_add_u32 s93, s82, 64\n\t" \
  "3:\n\t" \
  "v_mov_b64 v[72:73], 0\n\t" \
  "v_mov_b64 v[74:75], 0\n\t" \
  "v_mov_b64 v[76:77], 0\n\t" \
  "v_mov_b64 v[78:79], 0\n\t" \
  "s_waitcnt lgkmcnt(0)\n\t" \
  "s_waitcnt vmcnt(3)\n\t" \
  "v_smfmac_f32_16x16x64_bf16 v[72:75], v[32:35], v[48:55], v104\n\t" \
  "ds_write_b128 v101, v[32:35] offset:4096\n\t" \
  "v_smfmac_f32_16x16x64_bf16 v[76:79], v[32:35], v[56:63], v104\n\t" \
  "s_waitcnt vmcnt(2)\n\t" \
  "ds_write_b128 v101, v[36:39] offset:5120\n\t" \
  "v_smfmac_f32_16x16x64_bf16 v[72:75], v[36:39], a[48:55], v104\n\t" \
  "v_smfmac_f32_16x16x64_bf16 v[76:79], v[36:39], a[56:63], v104\n\t" \
  "s_nop 3\n\t" \
  "v_cmp_u_f32 vcc, v72, v73\n\t" \
  "v_cmp_u_f32 s[82:83], v74, v75\n\t" \
  "s_waitcnt vmcnt(1)\n\t" \
  "v_smfmac_f32_16x16x64_bf16 v[72:75], v[40:43], a[64:71], v104\n\t" \
  "s_or_b64 vcc, vcc, s[82:83]\n\t" \
  "s_or_b64 vcc, vcc, s[80:81]\n\t" \
  "v_smfmac_f32_16x16x64_bf16 v[76:79], v[40:43], a[72:79], v104\n\t" \
  "s_waitcnt lgkmcnt(0)\n\t" \
  "s_cbranch_scc1 8f\n\t" \
  "5:\n\t" \
  "s_barrier\n\t" \
  "4:\n\t" \
  "s_memtime s[56:57]\n\t" \
  "ds_read_b128 v[48:51], v101 offset:8192\n\t" \
  "ds_read_b128 v[52:55], v101 offset:9216\n\t" \
  "ds_read_b128 v[56:59], v101 offset:13312\n\t" \
  "s_waitcnt vmcnt(0)\n\t" \
  "v_smfmac_f32_16x16x64_bf16 v[72:75], v[44:47], a[80:87], v104\n\t" \
  "ds_read_b128 v[60:63], v101 offset:12288\n\t" \
  "ds_read_b128 v[64:67], v101 offset:1024\n\t" \
  "v_smfmac_f32_16x16x64_bf16 v[76:79], v[44:47], a[88:95], v104\n\t" \
  "ds_read_b128 v[68:71], v101 offset:0\n\t" \
  "ds_write_b128 v101, v[40:43] offset:6144\n\t" \
  "s_nop 1\n\t" \
  "v_cmp_u_f32 vcc, v72, v73\n\t" \
  "v_cmp_u_f32 s[82:83], v74, v75\n\t" \
  "s_waitcnt lgkmcnt(6)\n\t" \
  "v_smfmac_f32_16x16x64_bf16 v[72:75], v[48:51], v[0:7], v104\n\t" \
  "s_or_b64 vcc, vcc, s[82:83]\n\t" \
  "v_smfmac_f32_16x16x64_bf16 v[76:79], v[48:51], v[8:15], v104\n\t" \
  "s_cbranch_scc1 9f\n\t" \
  "6:\n\t" \
  "s_waitcnt lgkmcnt(5)\n\t" \
  "v_smfmac_f32_16x16x64_bf16 v[72:75], v[52:55], a[96:103], v104\n\t" \
  "ds_write_b128 v101, v[44:47] offset:7168\n\t" \
  "v_smfmac_f32_16x16x64_bf16 v[76:79], v[52:55], a[104:111], v104\n\t" \
  "v_mov_b64 v[40:41], 0\n\t" \
  "s_waitcnt lgkmcnt(5)\n\t" \
  "v_smfmac_f32_16x16x64_bf16 v[72:75], v[56:59], a[144:151], v104\n\t" \
  "v_mov_b64 v[42:43], 0\n\t" \
  "v_smfmac_f32_16x16x64_bf16 v[76:79], v[56:59], a[152:159], v104\n\t" \
  "s_waitcnt lgkmcnt(4)\n\t" \
  "v_smfmac_f32_16x16x64_bf16 v[72:75], v[60:63], v[16:23], v104\n\t" \
  "s_waitcnt lgkmcnt(0)\n\t" \
  "s_barrier\n\t" \
  "v_smfmac_f32_16x16x64_bf16 v[76:79], v[60:63], v[24:31], v104\n\t" \
  "ds_read_b128 v[48:51], v101 offset:10240\n\t" \
  "ds_read_b128 v[52:55], v101 offset:14336\n\t" \
  "v_smfmac_f32_16x16x64_bf16 v[72:75], v[64:67], a[0:7], v104\n\t" \
  "ds_read_b128 v[56:59], v101 offset:2048\n\t" \
  "ds_read_b128 v[60:63], v101 offset:11264\n\t" \
  "v_smfmac_f32_16x16x64_bf16 v[76:79], v[64:67], a[8:15], v104\n\t" \
  "ds_read_b128 v[32:35], v101 offset:15360\n\t" \
  "ds_read_b128 v[36:39], v101 offset:3072\n\t" \
  "v_smfmac_f32_16x16x64_bf16 v[72:75], v[68:71], v[80:87], v104\n\t" \
  "ds_read_b128 v[0:3], v103 offset:0\n\t" \
  "v_mov_b64 v[44:45], 0\n\t" \
  "v_smfmac_f32_16x16x64_bf16 v[76:79], v[68:71], v[88:95], v104\n\t" \
  "v_mov_b64 v[46:47], 0\n\t" \
  "s_waitcnt lgkmcnt(5)\n\t" \
  "v_smfmac_f32_16x16x64_bf16 v[72:75], v[48:51], a[112:119], v104\n\t" \
  "ds_read_b128 v[4:7], v103 offset:1024\n\t" \
  "v_mov_b64 v[80:81], 0\n\t" \
  "v_smfmac_f32_16x16x64_bf16 v[72:75], v[52:55], a[160:167], v104\n\t" \
  "v_mov_b64 v[82:83], 0\n\t" \
  "s_waitcnt lgkmcnt(2)\n\t" \
  "v_smfmac_f32_16x16x64_bf16 v[72:75], v[56:59], a[16:23], v104\n\t" \
  "ds_read_b128 v[8:11], v103 offset:2048\n\t" \
  "v_mov_b64 v[84:85], 0\n\t" \
  "v_smfmac_f32_16x16x64_bf16 v[72:75], v[60:63], a[128:135], v104\n\t" \
  "ds_read_b128 v[12:15], v103 offset:3072\n\t" \
  "v_mov_b64 v[86:87], 0\n\t" \
  "v_smfmac_f32_16x16x64_bf16 v[72:75], v[32:35], a[176:183], v104\n\t" \
  "ds_read_b128 v[16:19], v103 offset:16384\n\t" \
  "v_mov_b64 v[88:89], 0\n\t" \
  "v_smfmac_f32_16x16x64_bf16 v[72:75], v[36:39], a[32:39], v104\n\t" \
  "ds_read_b128 v[20:23], v103 offset:17408\n\t" \
  "v_mov_b64 v[90:91], 0\n\t" \
  "v_smfmac_f32_16x16x64_bf16 v[76:79], v[48:51], a[120:127], v104\n\t" \
  "ds_read_b128 v[24:27], v103 offset:18432\n\t" \
  "v_mov_b64 v[92:93], 0\n\t" \
  "v_smfmac_f32_16x16x64_bf16 v[76:79], v[52:55], a[168:175], v104\n\t" \
  "v_add_f32 v96, v72, v74\n\t" \
  "v_add_f32 v97, v73, v75\n\t" \
  "v_cvt_pk_bf16_f32 v96, v96, v97\n\t" \
  "ds_write_b32 v105, v96\n\t" \
  "v_smfmac_f32_16x16x64_bf16 v[76:79], v[56:59], a[24:31], v104\n\t" \
  "ds_read_b128 v[28:31], v103 offset:19456\n\t" \
  "v_mov_b64 v[94:95], 0\n\t" \
  "v_smfmac_f32_16x16x64_bf16 v[76:79], v[60:63], a[136:143], v104\n\t" \
  "v_smfmac_f32_16x16x64_bf16 v[76:79], v[32:35], a[184:191], v104\n\t" \
  "v_smfmac_f32_16x16x64_bf16 v[76:79], v[36:39], a[40:47], v104\n\t" \
  "s_waitcnt lgkmcnt(1)\n\t" \
  "s_barrier\n\t" \
  "ds_read_b64_tr_b16 v[96:97], v107 offset:0\n\t" \
  "ds_read_b64_tr_b16 v[98:99], v107 offset:64\n\t" \
  "ds_read_b64_tr_b16 v[56:57], v107 offset:1024\n\t" \
  "ds_read_b64_tr_b16 v[58:59], v107 offset:1088\n\t" \
  "s_nop 1\n\t" \
  "v_add_f32 v116, v76, v78\n\t" \
  "v_add_f32 v117, v77, v79\n\t" \
  "v_cvt_pk_bf16_f32 v116, v116, v117\n\t" \
  "ds_write_b32 v105, v116 offset:256\n\t" \
  "s_waitcnt lgkmcnt(3)\n\t" \
  "v_smfmac_f32_16x16x64_bf16 v[40:43], v[96:99], a[192:199], v106\n\t" \
  "v_smfmac_f32_16x16x64_bf16 v[44:47], v[96:99], a[224:231], v106\n\t" \
  "s_waitcnt lgkmcnt(0)\n\t" \
  "s_barrier\n\t" \
  "ds_read_b64_tr_b16 v[52:53], v107 offset:256\n\t" \
  "ds_read_b64_tr_b16 v[54:55], v107 offset:320\n\t" \
  "v_smfmac_f32_16x16x64_bf16 v[40:43], v[56:59], a[208:215], v106\n\t" \
  "ds_read_b64_tr_b16 v[60:61], v107 offset:1280\n\t" \
  "ds_read_b64_tr_b16 v[62:63], v107 offset:1344\n\t" \
  "v_smfmac_f32_16x16x64_bf16 v[44:47], v[56:59], v[0:7], v106\n\t" \
  "v_smfmac_f32_16x16x64_bf16 v[80:83], v[96:99], a[240:247], v106\n\t" \
  "v_mov_b64 v[32:33], 0\n\t" \
  "v_smfmac_f32_16x16x64_bf16 v[84:87], v[96:99], v[136:143], v106\n\t" \
  "v_mov_b64 v[34:35], 0\n\t" \
  "v_smfmac_f32_16x16x64_bf16 v[80:83], v[56:59], v[120:127], v106\n\t" \
  "v_mov_b64 v[36:37], 0\n\t" \
  "v_smfmac_f32_16x16x64_bf16 v[84:87], v[56:59], v[16:23], v106\n\t" \
  "s_waitcnt lgkmcnt(2)\n\t" \
  "v_smfmac_f32_16x16x64_bf16 v[40:43], v[52:55], a[200:207], v106\n\t" \
  "v_smfmac_f32_16x16x64_bf16 v[44:47], v[52:55], a[232:239], v106\n\t" \
  "s_xor_b32 s76, s47, 0x40000\n\t" \
  "s_waitcnt lgkmcnt(0)\n\t" \
  "v_smfmac_f32_16x16x64_bf16 v[40:43], v[60:63], a[216:223], v106\n\t" \
  "v_mov_b64 v[38:39], 0\n\t" \
  "s_xor_b32 s77, s47, 0x80000\n\t" \
  "v_smfmac_f32_16x16x64_bf16 v[44:47], v[60:63], v[8:15], v106\n\t" \
  "s_xor_b32 s78, s47, 0xc0000\n\t" \
  "v_smfmac_f32_16x16x64_bf16 v[80:83], v[52:55], a[248:255], v106\n\t" \
  "ds_read_b128 v[0:3], v103 offset:32768\n\t" \
  "v_cvt_pk_bf16_f32 v64, v40, v41\n\t" \
  "v_smfmac_f32_16x16x64_bf16 v[84:87], v[52:55], v[144:151], v106\n\t" \
  "ds_read_b128 v[4:7], v103 offset:33792\n\t" \
  "v_cvt_pk_bf16_f32 v65, v42, v43\n\t" \
  "v_smfmac_f32_16x16x64_bf16 v[80:83], v[60:63], v[128:135], v106\n\t" \
  "ds_read_b128 v[8:11], v103 offset:34816\n\t" \
  "v_cvt_pk_bf16_f32 v66, v44, v45\n\t" \
  "v_smfmac_f32_16x16x64_bf16 v[84:87], v[60:63], v[24:31], v106\n\t" \
  "ds_read_b128 v[12:15], v103 offset:35840\n\t" \
  "v_cvt_pk_bf16_f32 v67, v46, v47\n\t" \
  "v_smfmac_f32_16x16x64_bf16 v[88:91], v[96:99], v[152:159], v106\n\t" \
  "v_permlane16_swap_b32 v64, v66\n\t" \
  "v_cvt_pk_bf16_f32 v68, v80, v81\n\t" \
  "v_smfmac_f32_16x16x64_bf16 v[92:95], v[96:99], v[184:191], v106\n\t" \
  "v_permlane16_swap_b32 v65, v67\n\t" \
  "v_cvt_pk_bf16_f32 v69, v82, v83\n\t" \
  "v_smfmac_f32_16x16x64_bf16 v[88:91], v[52:55], v[160:167], v106\n\t" \
  "buffer_store_dwordx4 v[64:67], v108, %[ru], s47 offen sc1\n\t" \
  "v_cvt_pk_bf16_f32 v70, v84, v85\n\t" \
  "v_smfmac_f32_16x16x64_bf16 v[92:95], v[52:55], v[192:199], v106\n\t" \
  "v_cvt_pk_bf16_f32 v71, v86, v87\n\t" \
  "v_smfmac_f32_16x16x64_bf16 v[88:91], v[56:59], v[168:175], v106\n\t" \
  "ds_read_b128 v[16:19], v103 offset:49152\n\t" \
  "v_permlane16_swap_b32 v68, v70\n\t" \
  "s_waitcnt lgkmcnt(3)\n\t" \
  "v_smfmac_f32_16x16x64_bf16 v[92:95], v[56:59], v[0:7], v106\n\t" \
  "ds_read_b128 v[20:23], v103 offset:50176\n\t" \
  "v_permlane16_swap_b32 v69, v71\n\t" \
  "v_smfmac_f32_16x16x64_bf16 v[88:91], v[60:63], v[176:183], v106\n\t" \
  "ds_read_b128 v[24:27], v103 offset:51200\n\t" \
  "buffer_store_dwordx4 v[68:71], v108, %[ru], s76 offen sc1\n\t" \
  "s_waitcnt lgkmcnt(3)\n\t" \
  "v_smfmac_f32_16x16x64_bf16 v[92:95], v[60:63], v[8:15], v106\n\t" \
  "ds_read_b128 v[28:31], v103 offset:52224\n\t" \
  "v_smfmac_f32_16x16x64_bf16 v[32:35], v[96:99], v[200:207], v106\n\t" \
  "v_cvt_pk_bf16_f32 v72, v88, v89\n\t" \
  "v_smfmac_f32_16x16x64_bf16 v[36:39], v[96:99], v[232:239], v106\n\t" \
  "v_cvt_pk_bf16_f32 v73, v90, v91\n\t" \
  "v_smfmac_f32_16x16x64_bf16 v[32:35], v[52:55], v[208:215], v106\n\t" \
  "v_cvt_pk_bf16_f32 v74, v92, v93\n\t" \
  "v_smfmac_f32_16x16x64_bf16 v[36:39], v[52:55], v[240:247], v106\n\t" \
  "v_cvt_pk_bf16_f32 v75, v94, v95\n\t" \
  "v_smfmac_f32_16x16x64_bf16 v[32:35], v[56:59], v[216:223], v106\n\t" \
  "v_permlane16_swap_b32 v72, v74\n\t" \
  "v_smfmac_f32_16x16x64_bf16 v[32:35], v[60:63], v[224:231], v106\n\t" \
  "v_permlane16_swap_b32 v73, v75\n\t" \
  "s_waitcnt lgkmcnt(2)\n\t" \
  "v_smfmac_f32_16x16x64_bf16 v[36:39], v[56:59], v[16:23], v106\n\t" \
  "buffer_store_dwordx4 v[72:75], v108, %[ru], s77 offen sc1\n\t" \
  "s_waitcnt lgkmcnt(0)\n\t" \
  "v_smfmac_f32_16x16x64_bf16 v[36:39], v[60:63], v[24:31], v106\n\t" \
  "v_cvt_pk_bf16_f32 v76, v32, v33\n\t" \
  "v_cvt_pk_bf16_f32 v77, v34, v35\n\t" \
  "s_nop 5\n\t" \
  "v_cvt_pk_bf16_f32 v78, v36, v37\n\t" \
  "v_cvt_pk_bf16_f32 v79, v38, v39\n\t" \
  "s_nop 0\n\t" \
  "v_permlane16_swap_b32 v76, v78\n\t" \
  "v_permlane16_swap_b32 v77, v79\n\t" \
  "buffer_store_dwordx4 v[76:79], v108, %[ru], s78 offen sc1\n\t" \
  "s_memtime s[76:77]\n\t" \
  "s_mov_b32 s42, 0\n\t" \
  "s_waitcnt lgkmcnt(0)\n\t" \
  "s_mov_b32 s57, s76\n\t" \
  "s_add_i32 s41, s56, 1950\n\t" \
  "s_sub_i32 s76, s92, s51\n\t" \
  "s_sub_i32 s77, s93, s53\n\t" \
  "s_cmp_eq_u32 s76, 0\n\t" \
  "s_cbranch_scc1 21f\n\t" \
  "v_cvt_f32_u32 v117, s76\n\t" \
  "v_cvt_f32_u32 v116, s77\n\t" \
  "v_rcp_f32 v117, v117\n\t" \
  "s_mov_b32 s51, s92\n\t" \
  "v_mul_f32 v117, v117, v116\n\t" \
  "v_max_f32 v117, 0x3f028f5c, v117\n\t" \
  "v_min_f32 v117, 0x3ffeb852, v117\n\t" \
  "v_rcp_f32 v116, v117\n\t" \
  "v_mul_f32 v117, 0x4f000000, v117\n\t" \
  "v_mul_f32 v116, 0x47800000, v116\n\t" \
  "v_cvt_u32_f32 v117, v117\n\t" \
  "v_cvt_u32_f32 v116, v116\n\t" \
  "s_mov_b32 s53, s93\n\t" \
  "v_readfirstlane_b32 s46, v117\n\t" \
  "v_readfirstlane_b32 s52, v116\n\t" \
  "21:\n\t" \
  "s_add_i32 s44, s44, 1\n\t" \
  "s_and_b32 s58, s58, 4\n\t" \
  "s_and_b32 s76, s44, 64\n\t" \
  "s_or_b32 s42, s42, s76\n\t" \
  "s_cbranch_scc0 20b\n\t" \
  "s_branch 99f\n\t" \
  "14:\n\t" \
  "s_sleep 3\n\t" \
  "s_add_u32 s59, s59, 0x10000\n\t" \
  "s_cmp_lt_u32 s59, 0x100000\n\t" \
  "s_cbranch_scc1 13b\n\t" \
  "s_mov_b64 exec, -1\n\t" \
  "s_mov_b32 s42, 16\n\t" \
  "s_branch 99f\n\t" \
  "8:\n\t" \
  "s_bitcmp1_b32 s58, 1\n\t" \
  "s_cbranch_scc1 4b\n\t" \
  "s_bitcmp1_b32 s58, 2\n\t" \
  "s_cbranch_scc1 5b\n\t" \
  "v_max3_u32 v116, v32, v33, v34\n\t" \
  "v_max3_u32 v117, v35, v36, v37\n\t" \
  "v_max3_u32 v116, v116, v38, v39\n\t" \
  "v_max_u32 v116, v116, v117\n\t" \
  "v_cmp_eq_u32 vcc, -1, v116\n\t" \
  "s_cbranch_vccz 5b\n\t" \
  "s_branch 0f\n\t" \
  "9:\n\t" \
  "s_bitcmp1_b32 s58, 2\n\t" \
  "s_cbranch_scc1 6b\n\t" \
  "v_max3_u32 v116, v40, v41, v42\n\t" \
  "v_max3_u32 v117, v43, v44, v45\n\t" \
  "v_max3_u32 v116, v116, v46, v47\n\t" \
  "v_max_u32 v116, v116, v117\n\t" \
  "v_cmp_eq_u32 vcc, -1, v116\n\t" \
  "s_cbranch_vccz 6b\n\t" \
  "s_or_b32 s58, s58, 2\n\t" \
  "0:\n\t" \
  "s_or_b32 s58, s58, 1\n\t" \
  "11:\n\t" \
  "buffer_load_dwordx4 v[32:35], v100, %[rh], s40 offen sc1\n\t" \
  "buffer_load_dwordx4 v[36:39], v100, %[rh], s76 offen sc1\n\t" \
  "buffer_load_dwordx4 v[40:43], v100, %[rh], s77 offen sc1\n\t" \
  "buffer_load_dwordx4 v[44:47], v100, %[rh], s78 offen sc1\n\t" \
  "s_waitcnt lgkmcnt(0)\n\t" \
  "s_branch 10b\n\t" \
  "17:\n\t" \
  "s_add_u32 s59, s59, 1\n\t" \
  "s_bitcmp1_b32 s58, 2\n\t" \
  "s_cbranch_scc1 3b\n\t" \
  "s_waitcnt vmcnt(0)\n\t" \
  "v_max_u32 v116, v32, v33\n\t" \
  "v_max3_u32 v116, v116, v34, v35\n\t" \
  "v_max3_u32 v116, v116, v36, v37\n\t" \
  "v_max3_u32 v116, v116, v38, v39\n\t" \
  "v_max3_u32 v116, v116, v40, v41\n\t" \
  "v_max3_u32 v116, v116, v42, v43\n\t" \
  "v_max3_u32 v116, v116, v44, v45\n\t" \
  "v_max3_u32 v116, v116, v46, v47\n\t" \
  "v_cmp_eq_u32 vcc, -1, v116\n\t" \
  "s_cbranch_vccz 3b\n\t" \
  "s_bitcmp1_b32 s59, 6\n\t" \
  "s_cbranch_scc1 12f\n\t" \
  "buffer_load_dwordx4 v[32:35], v100, %[rh], s40 offen sc1\n\t" \
  "buffer_load_dwordx4 v[36:39], v100, %[rh], s76 offen sc1\n\t" \
  "buffer_load_dwordx4 v[40:43], v100, %[rh], s77 offen sc1\n\t" \
  "buffer_load_dwordx4 v[44:47], v100, %[rh], s78 offen sc1\n\t" \
  "s_branch 17b\n\t" \
  "12:\n\t" \
  "s_mov_b32 s42, s58\n\t" \
  "s_branch 99f\n\t" \
  "92:\n\t" \
  "s_mov_b32 s58, s42\n\t" \
  "20:\n\t" \
  "s_and_b32 s76, s44, 7\n\t" \
  "s_lshl_b32 s76, s76, 15\n\t" \
  "s_add_i32 s40, s54, s76\n\t" \
  "s_and_b32 s76, s44, 3\n\t" \
  "s_lshl_b32 s45, s76, 14\n\t" \
  "s_lshl_b32 s47, s76, 20\n\t" \
  "s_add_i32 s47, s47, s55\n\t" \
  "s_lshr_b32 s76, s47, 21\n\t" \
  "s_bitcmp1_b64 %[sel], s76\n\t" \
  "s_cselect_b32 s76, 0x2000, 0\n\t" \
  "s_add_i32 s47, s47, s76\n\t" \
  "s_add_i32 s49, s44, -1\n\t" \
  "s_and_b32 s48, s49, 2\n\t" \
  "s_lshl_b32 s48, s48, 13\n\t" \
  "s_and_b32 s76, s49, 1\n\t" \
  "s_lshl_b32 s76, s76, 12\n\t" \
  "s_add_i32 s48, s48, s76\n\t" \
  "s_lshl_b32 s50, s53, 8\n\t" \
  "s_mov_b32 s59, 0\n\t" \
  "s_mov_b32 s92, s51\n\t" \
  "s_mov_b32 s93, s53\n\t" \
  "s_xor_b32 s76, s40, 0x400\n\t" \
  "s_xor_b32 s77, s40, 0x800\n\t" \
  "s_xor_b32 s78, s40, 0xc00\n\t" \
  "s_cmp_lg_u32 s58, 0\n\t" \
  "s_cbranch_scc1 11f\n\t" \
  "10:\n\t" \
  "ds_read_b128 v[48:51], v102 offset:32768\n\t" \
  "ds_read_b128 v[52:55], v102 offset:33792\n\t" \
  "ds_read_b128 v[56:59], v102 offset:34816\n\t" \
  "ds_read_b128 v[60:63], v102 offset:35840\n\t" \
  "ds_read_b128 v[0:3], v102 offset:49152\n\t" \
  "ds_read_b128 v[4:7], v102 offset:50176\n\t" \
  "ds_read_b128 v[8:11], v102 offset:51200\n\t" \
  "ds_read_b128 v[12:15], v102 offset:52224\n\t" \
  "ds_read_b128 v[16:19], v102 offset:0\n\t" \
  "ds_read_b128 v[20:23], v102 offset:1024\n\t" \
  "ds_read_b128 v[24:27], v102 offset:2048\n\t" \
  "ds_read_b128 v[28:31], v102 offset:3072\n\t" \
  "s_waitcnt lgkmcnt(4)\n\t" \
  "ds_read_b128 v[80:83], v102 offset:16384\n\t" \
  "ds_read_b128 v[84:87], v102 offset:17408\n\t" \
  "ds_read_b128 v[88:91], v102 offset:18432\n\t" \
  "ds_read_b128 v[92:95], v102 offset:19456\n\t" \
  "s_bitcmp1_b32 s58, 1\n\t" \
  "s_cselect_b64 s[80:81], -1, 0\n\t" \
  "s_cmp_lg_u32 s58, 0\n\t" \
  "s_cbranch_scc1 17f\n\t" \
  "s_mov_b32 exec_hi, 0\n\t" \
  "16:\n\t" \
  "s_memtime vcc\n\t" \
  "s_waitcnt lgkmcnt(0)\n\t" \
  "s_sub_i32 s79, s41, vcc_lo\n\t" \
  "s_cmp_gt_i32 s79, 64\n\t" \
  "s_cbranch_scc1 16b\n\t" \
  "13:\n\t" \
  "buffer_load_dword v116, v112, %[rh], s48 offen sc1\n\t" \
  "s_waitcnt vmcnt(0)\n\t" \
  "15:\n\t" \
  "v_mov_b32 v117, 20000\n\t" \
  "v_cmp_ne_u32_sdwa vcc, v116, s49 src0_sel:BYTE_0 src1_sel:DWORD\n\t" \
  "v_subrev_u32 v116, s50, v116\n\t" \
  "v_ashrrev_i32 v116, 8, v116\n\t" \
  "s_nop 1\n\t" \
  "v_max_i32_dpp v116, v116, v116 quad_perm:[1,0,3,2] row_mask:0xf bank_mask:0xf\n\t" \
  "s_cbranch_vccnz 14f\n\t" \
  "s_nop 0\n\t" \
  "v_max_i32_dpp v116, v116, v116 quad_perm:[2,3,0,1] row_mask:0xf bank_mask:0xf\n\t" \
  "s_nop 1\n\t" \
  "v_max_i32_dpp v116, v116, v116 row_half_mirror row_mask:0xf bank_mask:0xf\n\t" \
  "s_nop 1\n\t" \
  "v_max_i32_dpp v116, v116, v116 row_mirror row_mask:0xf bank_mask:0xf\n\t" \
  "v_med3_i32 v116, v116, 0, v117\n\t" \
  "v_mul_u32_u24 v116, s52, v116\n\t" \
  "v_lshrrev_b32 v116, 16, v116\n\t" \
  "s_mov_b64 exec, -1\n\t" \
  "v_readfirstlane_b32 s84, v116\n\t" \
  "v_readlane_b32 s85, v116, 16\n\t" \
  "s_max_u32 s84, s84, s85\n\t" \
  "s_add_i32 s84, s84, s51\n\t" \
  "1:\n\t" \
  "s_memtime vcc\n\t" \
  "s_waitcnt lgkmcnt(0)\n\t" \
  "s_sub_i32 s79, s84, vcc_lo\n\t" \
  "s_cmp_gt_i32 s79, 128\n\t" \
  "s_cbranch_scc1 1b\n\t" \
  ".p2align 4\n\t" \
  "2:\n\t" \
  "s_sub_i32 s79, s79, 33\n\t" \
  "s_cmp_gt_i32 s79, 56\n\t" \
  "s_cbranch_scc1 2b\n\t" \
  "buffer_load_dwordx4 v[32:35], v100, %[rh], s40 offen sc1\n\t" \
  "s_nop 12\n\t" \
  "buffer_load_dwordx4 v[36:39], v100, %[rh], s76 offen sc1\n\t" \
  "s_nop 12\n\t" \
  "buffer_load_dwordx4 v[40:43], v100, %[rh], s77 offen sc1\n\t" \
  "s_nop 12\n\t" \
  "buffer_load_dwordx4 v[44:47], v100, %[rh], s78 offen sc1\n\t" \
  "s_memtime vcc\n\t" \
  "s_memrealtime s[82:83]\n\t" \
  "s_nop 4\n\t" \
  "s_memrealtime s[84:85]\n\t" \
  "s_nop 4\n\t" \
  "s_memrealtime s[56:57]\n\t" \
  "s_nop 4\n\t" \
  "s_memrealtime s[86:87]\n\t" \
  "s_nop 4\n\t" \
  "s_memrealtime s[88:89]\n\t" \
  "s_nop 4\n\t" \
  "s_memrealtime s[90:91]\n\t" \
  "s_mov_b32 s92, 0\n\t" \
  "s_waitcnt lgkmcnt(0)\n\t" \
  "s_cmp_eq_u32 s82, s84\n\t" \
  "s_addc_u32 s92, s92, 0\n\t" \
  "s_cmp_eq_u32 s82, s56\n\t" \
  "s_addc_u32 s92, s92, 0\n\t" \
  "s_cmp_eq_u32 s82, s86\n\t" \
  "s_addc_u32 s92, s92, 0\n\t" \
  "s_cmp_eq_u32 s82, s88\n\t" \
  "s_addc_u32 s92, s92, 0\n\t" \
  "s_cmp_eq_u32 s82, s90\n\t" \
  "s_addc_u32 s92, s92, 0\n\t" \
  "s_mul_i32 s92, s92, 24\n\t" \
  "s_add_i32 s92, s92, vcc_lo\n\t" \
  "s_lshl4_add_u32 s93, s82, 64\n\t" \
  "3:\n\t" \
  "v_mov_b64 v[72:73], 0\n\t" \
  "v_mov_b64 v[74:75], 0\n\t" \
  "v_mov_b64 v[76:77], 0\n\t" \
  "v_mov_b64 v[78:79], 0\n\t" \
  "s_waitcnt lgkmcnt(0)\n\t" \
  "s_waitcnt vmcnt(3)\n\t" \
  "v_smfmac_f32_16x16x64_bf16 v[72:75], v[32:35], v[48:55], v104\n\t" \
  "ds_write_b128 v101, v[32:35] offset:8192\n\t" \
  "v_smfmac_f32_16x16x64_bf16 v[76:79], v[32:35], v[56:63], v104\n\t" \
  "s_waitcnt vmcnt(2)\n\t" \
  "ds_write_b128 v101, v[36:39] offset:9216\n\t" \
  "v_smfmac_f32_16x16x64_bf16 v[72:75], v[36:39], a[96:103], v104\n\t" \
  "v_smfmac_f32_16x16x64_bf16 v[76:79], v[36:39], a[104:111], v104\n\t" \
  "s_nop 3\n\t" \
  "v_cmp_u_f32 vcc, v72, v73\n\t" \
  "v_cmp_u_f32 s[82:83], v74, v75\n\t" \
  "s_waitcnt vmcnt(1)\n\t" \
  "v_smfmac_f32_16x16x64_bf16 v[72:75], v[40:43], a[112:119], v104\n\t" \
  "s_or_b64 vcc, vcc, s[82:83]\n\t" \
  "s_or_b64 vcc, vcc, s[80:81]\n\t" \
  "v_smfmac_f32_16x16x64_bf16 v[76:79], v[40:43], a[120:127], v104\n\t" \
  "s_waitcnt lgkmcnt(0)\n\t" \
  "s_cbranch_scc1 8f\n\t" \
  "5:\n\t" \
  "s_barrier\n\t" \
  "4:\n\t" \
  "s_memtime s[56:57]\n\t" \
  "ds_read_b128 v[48:51], v101 offset:12288\n\t" \
  "ds_read_b128 v[52:55], v101 offset:13312\n\t" \
  "ds_read_b128 v[56:59], v101 offset:1024\n\t" \
  "s_waitcnt vmcnt(0)\n\t" \
  "v_smfmac_f32_16x16x64_bf16 v[72:75], v[44:47], a[128:135], v104\n\t" \
  "ds_read_b128 v[60:63], v101 offset:0\n\t" \
  "ds_read_b128 v[64:67], v101 offset:5120\n\t" \
  "v_smfmac_f32_16x16x64_bf16 v[76:79], v[44:47], a[136:143], v104\n\t" \
  "ds_read_b128 v[68:71], v101 offset:4096\n\t" \
  "ds_write_b128 v101, v[40:43] offset:10240\n\t" \
  "s_nop 1\n\t" \
  "v_cmp_u_f32 vcc, v72, v73\n\t" \
  "v_cmp_u_f32 s[82:83], v74, v75\n\t" \
  "s_waitcnt lgkmcnt(6)\n\t" \
  "v_smfmac_f32_16x16x64_bf16 v[72:75], v[48:51], v[0:7], v104\n\t" \
  "s_or_b64 vcc, vcc, s[82:83]\n\t" \
  "v_smfmac_f32_16x16x64_bf16 v[76:79], v[48:51], v[8:15], v104\n\t" \
  "s_cbranch_scc1 9f\n\t" \
  "6:\n\t" \
  "s_waitcnt lgkmcnt(5)\n\t" \
  "v_smfmac_f32_16x16x64_bf16 v[72:75], v[52:55], a[144:151], v104\n\t" \
  "ds_write_b128 v101, v[44:47] offset:11264\n\t" \
  "v_smfmac_f32_16x16x64_bf16 v[76:79], v[52:55], a[152:159], v104\n\t" \
  "v_mov_b64 v[40:41], 0\n\t" \
  "s_waitcnt lgkmcnt(5)\n\t" \
  "v_smfmac_f32_16x16x64_bf16 v[72:75], v[56:59], a[0:7], v104\n\t" \
  "v_mov_b64 v[42:43], 0\n\t" \
  "v_smfmac_f32_16x16x64_bf16 v[76:79], v[56:59], a[8:15], v104\n\t" \
  "s_waitcnt lgkmcnt(4)\n\t" \
  "v_smfmac_f32_16x16x64_bf16 v[72:75], v[60:63], v[16:23], v104\n\t" \
  "s_waitcnt lgkmcnt(0)\n\t" \
  "s_barrier\n\t" \
  "v_smfmac_f32_16x16x64_bf16 v[76:79], v[60:63], v[24:31], v104\n\t" \
  "ds_read_b128 v[48:51], v101 offset:14336\n\t" \
  "ds_read_b128 v[52:55], v101 offset:2048\n\t" \
  "v_smfmac_f32_16x16x64_bf16 v[72:75], v[64:67], a[48:55], v104\n\t" \
  "ds_read_b128 v[56:59], v101 offset:6144\n\t" \
  "ds_read_b128 v[60:63], v101 offset:15360\n\t" \
  "v_smfmac_f32_16x16x64_bf16 v[76:79], v[64:67], a[56:63], v104\n\t" \
  "ds_read_b128 v[32:35], v101 offset:3072\n\t" \
  "ds_read_b128 v[36:39], v101 offset:7168\n\t" \
  "v_smfmac_f32_16x16x64_bf16 v[72:75], v[68:71], v[80:87], v104\n\t" \
  "ds_read_b128 v[0:3], v103 offset:0\n\t" \
  "v_mov_b64 v[44:45], 0\n\t" \
  "v_smfmac_f32_16x16x64_bf16 v[76:79], v[68:71], v[88:95], v104\n\t" \
  "v_mov_b64 v[46:47], 0\n\t" \
  "s_waitcnt lgkmcnt(5)\n\t" \
  "v_smfmac_f32_16x16x64_bf16 v[72:75], v[48:51], a[160:167], v104\n\t" \
  "ds_read_b128 v[4:7], v103 offset:1024\n\t" \
  "v_mov_b64 v[80:81], 0\n\t" \
  "v_smfmac_f32_16x16x64_bf16 v[72:75], v[52:55], a[16:23], v104\n\t" \
  "v_mov_b64 v[82:83], 0\n\t" \
  "s_waitcnt lgkmcnt(2)\n\t" \
  "v_smfmac_f32_16x16x64_bf16 v[72:75], v[56:59], a[64:71], v104\n\t" \
  "ds_read_b128 v[8:11], v103 offset:2048\n\t" \
  "v_mov_b64 v[84:85], 0\n\t" \
  "v_smfmac_f32_16x16x64_bf16 v[72:75], v[60:63], a[176:183], v104\n\t" \
  "ds_read_b128 v[12:15], v103 offset:3072\n\t" \
  "v_mov_b64 v[86:87], 0\n\t" \
  "v_smfmac_f32_16x16x64_bf16 v[72:75], v[32:35], a[32:39], v104\n\t" \
  "ds_read_b128 v[16:19], v103 offset:16384\n\t" \
  "v_mov_b64 v[88:89], 0\n\t" \
  "v_smfmac_f32_16x16x64_bf16 v[72:75], v[36:39], a[80:87], v104\n\t" \
  "ds_read_b128 v[20:23], v103 offset:17408\n\t" \
  "v_mov_b64 v[90:91], 0\n\t" \
  "v_smfmac_f32_16x16x64_bf16 v[76:79], v[48:51], a[168:175], v104\n\t" \
  "ds_read_b128 v[24:27], v103 offset:18432\n\t" \
  "v_mov_b64 v[92:93], 0\n\t" \
  "v_smfmac_f32_16x16x64_bf16 v[76:79], v[52:55], a[24:31], v104\n\t" \
  "v_add_f32 v96, v72, v74\n\t" \
  "v_add_f32 v97, v73, v75\n\t" \
  "v_cvt_pk_bf16_f32 v96, v96, v97\n\t" \
  "ds_write_b32 v105, v96\n\t" \
  "v_smfmac_f32_16x16x64_bf16 v[76:79], v[56:59], a[72:79], v104\n\t" \
  "ds_read_b128 v[28:31], v103 offset:19456\n\t" \
  "v_mov_b64 v[94:95], 0\n\t" \
  "v_smfmac_f32_16x16x64_bf16 v[76:79], v[60:63], a[184:191], v104\n\t" \
  "v_smfmac_f32_16x16x64_bf16 v[76:79], v[32:35], a[40:47], v104\n\t" \
  "v_smfmac_f32_16x16x64_bf16 v[76:79], v[36:39], a[88:95], v104\n\t" \
  "s_waitcnt lgkmcnt(1)\n\t" \
  "s_barrier\n\t" \
  "ds_read_b64_tr_b16 v[96:97], v107 offset:0\n\t" \
  "ds_read_b64_tr_b16 v[98:99], v107 offset:64\n\t" \
  "ds_read_b64_tr_b16 v[56:57], v107 offset:1024\n\t" \
  "ds_read_b64_tr_b16 v[58:59], v107 offset:1088\n\t" \
  "s_nop 1\n\t" \
  "v_add_f32 v116, v76, v78\n\t" \
  "v_add_f32 v117, v77, v79\n\t" \
  "v_cvt_pk_bf16_f32 v116, v116, v117\n\t" \
  "ds_write_b32 v105, v116 offset:256\n\t" \
  "s_waitcnt lgkmcnt(3)\n\t" \
  "v_smfmac_f32_16x16x64_bf16 v[40:43], v[96:99], a[192:199], v106\n\t" \
  "v_smfmac_f32_16x16x64_bf16 v[44:47], v[96:99], a[224:231], v106\n\t" \
  "s_waitcnt lgkmcnt(0)\n\t" \
  "s_barrier\n\t" \
  "ds_read_b64_tr_b16 v[52:53], v107 offset:256\n\t" \
  "ds_read_b64_tr_b16 v[54:55], v107 offset:320\n\t" \
  "v_smfmac_f32_16x16x64_bf16 v[40:43], v[56:59], a[208:215], v106\n\t" \
  "ds_read_b64_tr_b16 v[60:61], v107 offset:1280\n\t" \
  "ds_read_b64_tr_b16 v[62:63], v107 offset:1344\n\t" \
  "v_smfmac_f32_16x16x64_bf16 v[44:47], v[56:59], v[0:7], v106\n\t" \
  "v_smfmac_f32_16x16x64_bf16 v[80:83], v[96:99], a[240:247], v106\n\t" \
  "v_mov_b64 v[32:33], 0\n\t" \
  "v_smfmac_f32_16x16x64_bf16 v[84:87], v[96:99], v[136:143], v106\n\t" \
  "v_mov_b64 v[34:35], 0\n\t" \
  "v_smfmac_f32_16x16x64_bf16 v[80:83], v[56:59], v[120:127], v106\n\t" \
  "v_mov_b64 v[36:37], 0\n\t" \
  "v_smfmac_f32_16x16x64_bf16 v[84:87], v[56:59], v[16:23], v106\n\t" \
  "s_waitcnt lgkmcnt(2)\n\t" \
  "v_smfmac_f32_16x16x64_bf16 v[40:43], v[52:55], a[200:207], v106\n\t" \
  "v_smfmac_f32_16x16x64_bf16 v[44:47], v[52:55], a[232:239], v106\n\t" \
  "s_xor_b32 s76, s47, 0x40000\n\t" \
  "s_waitcnt lgkmcnt(0)\n\t" \
  "v_smfmac_f32_16x16x64_bf16 v[40:43], v[60:63], a[216:223], v106\n\t" \
  "v_mov_b64 v[38:39], 0\n\t" \
  "s_xor_b32 s77, s47, 0x80000\n\t" \
  "v_smfmac_f32_16x16x64_bf16 v[44:47], v[60:63], v[8:15], v106\n\t" \
  "s_xor_b32 s78, s47, 0xc0000\n\t" \
  "v_smfmac_f32_16x16x64_bf16 v[80:83], v[52:55], a[248:255], v106\n\t" \
  "ds_read_b128 v[0:3], v103 offset:32768\n\t" \
  "v_cvt_pk_bf16_f32 v64, v40, v41\n\t" \
  "v_smfmac_f32_16x16x64_bf16 v[84:87], v[52:55], v[144:151], v106\n\t" \
  "ds_read_b128 v[4:7], v103 offset:33792\n\t" \
  "v_cvt_pk_bf16_f32 v65, v42, v43\n\t" \
  "v_smfmac_f32_16x16x64_bf16 v[80:83], v[60:63], v[128:135], v106\n\t" \
  "ds_read_b128 v[8:11], v103 offset:34816\n\t" \
  "v_cvt_pk_bf16_f32 v66, v44, v45\n\t" \
  "v_smfmac_f32_16x16x64_bf16 v[84:87], v[60:63], v[24:31], v106\n\t" \
  "ds_read_b128 v[12:15], v103 offset:35840\n\t" \
  "v_cvt_pk_bf16_f32 v67, v46, v47\n\t" \
  "v_smfmac_f32_16x16x64_bf16 v[88:91], v[96:99], v[152:159], v106\n\t" \
  "v_permlane16_swap_b32 v64, v66\n\t" \
  "v_cvt_pk_bf16_f32 v68, v80, v81\n\t" \
  "v_smfmac_f32_16x16x64_bf16 v[92:95], v[96:99], v[184:191], v106\n\t" \
  "v_permlane16_swap_b32 v65, v67\n\t" \
  "v_cvt_pk_bf16_f32 v69, v82, v83\n\t" \
  "v_smfmac_f32_16x16x64_bf16 v[88:91], v[52:55], v[160:167], v106\n\t" \
  "v_cvt_pk_bf16_f32 v70, v84, v85\n\t" \
  "v_smfmac_f32_16x16x64_bf16 v[92:95], v[52:55], v[192:199], v106\n\t" \
  "buffer_store_dwordx4 v[64:67], v108, %[ru], s47 offen sc1\n\t" \
  "v_cvt_pk_bf16_f32 v71, v86, v87\n\t" \
  "v_smfmac_f32_16x16x64_bf16 v[88:91], v[56:59], v[168:175], v106\n\t" \
  "ds_read_b128 v[16:19], v103 offset:49152\n\t" \
  "v_permlane16_swap_b32 v68, v70\n\t" \
  "s_waitcnt lgkmcnt(3)\n\t" \
  "v_smfmac_f32_16x16x64_bf16 v[92:95], v[56:59], v[0:7], v106\n\t" \
  "ds_read_b128 v[20:23], v103 offset:50176\n\t" \
  "v_permlane16_swap_b32 v69, v71\n\t" \
  "v_smfmac_f32_16x16x64_bf16 v[88:91], v[60:63], v[176:183], v106\n\t" \
  "ds_read_b128 v[24:27], v103 offset:51200\n\t" \
  "s_waitcnt lgkmcnt(3)\n\t" \
  "v_smfmac_f32_16x16x64_bf16 v[92:95], v[60:63], v[8:15], v106\n\t" \
  "ds_read_b128 v[28:31], v103 offset:52224\n\t" \
  "buffer_store_dwordx4 v[68:71], v108, %[ru], s76 offen sc1\n\t" \
  "v_smfmac_f32_16x16x64_bf16 v[32:35], v[96:99], v[200:207], v106\n\t" \
  "v_cvt_pk_bf16_f32 v72, v88, v89\n\t" \
  "v_smfmac_f32_16x16x64_bf16 v[36:39], v[96:99], v[232:239], v106\n\t" \
  "v_cvt_pk_bf16_f32 v73, v90, v91\n\t" \
  "v_smfmac_f32_16x16x64_bf16 v[32:35], v[52:55], v[208:215], v106\n\t" \
  "v_cvt_pk_bf16_f32 v74, v92, v93\n\t" \
  "v_smfmac_f32_16x16x64_bf16 v[36:39], v[52:55], v[240:247], v106\n\t" \
  "v_cvt_pk_bf16_f32 v75, v94, v95\n\t" \
  "v_smfmac_f32_16x16x64_bf16 v[32:35], v[56:59], v[216:223], v106\n\t" \
  "v_permlane16_swap_b32 v72, v74\n\t" \
  "v_smfmac_f32_16x16x64_bf16 v[32:35], v[60:63], v[224:231], v106\n\t" \
  "v_permlane16_swap_b32 v73, v75\n\t" \
  "s_waitcnt lgkmcnt(2)\n\t" \
  "v_smfmac_f32_16x16x64_bf16 v[36:39], v[56:59], v[16:23], v106\n\t" \
  "s_waitcnt lgkmcnt(0)\n\t" \
  "v_smfmac_f32_16x16x64_bf16 v[36:39], v[60:63], v[24:31], v106\n\t" \
  "buffer_store_dwordx4 v[72:75], v108, %[ru], s77 offen sc1\n\t" \
  "v_cvt_pk_bf16_f32 v76, v32, v33\n\t" \
  "v_cvt_pk_bf16_f32 v77, v34, v35\n\t" \
  "s_nop 5\n\t" \
  "v_cvt_pk_bf16_f32 v78, v36, v37\n\t" \
  "v_cvt_pk_bf16_f32 v79, v38, v39\n\t" \
  "s_nop 0\n\t" \
  "v_permlane16_swap_b32 v76, v78\n\t" \
  "v_permlane16_swap_b32 v77, v79\n\t" \
  "buffer_store_dwordx4 v[76:79], v108, %[ru], s78 offen sc1\n\t" \
  "s_memtime s[76:77]\n\t" \
  "s_mov_b32 s42, 0\n\t" \
  "s_waitcnt lgkmcnt(0)\n\t" \
  "s_mov_b32 s57, s76\n\t" \
  "s_add_i32 s41, s56, 1950\n\t" \
  "s_sub_i32 s76, s92, s51\n\t" \
  "s_sub_i32 s77, s93, s53\n\t" \
  "s_cmp_eq_u32 s76, 0\n\t" \
  "s_cbranch_scc1 21f\n\t" \
  "v_cvt_f32_u32 v117, s76\n\t" \
  "v_cvt_f32_u32 v116, s77\n\t" \
  "v_rcp_f32 v117, v117\n\t" \
  "s_mov_b32 s51, s92\n\t" \
  "v_mul_f32 v117, v117, v116\n\t" \
  "v_max_f32 v117, 0x3f028f5c, v117\n\t" \
  "v_min_f32 v117, 0x3ffeb852, v117\n\t" \
  "v_rcp_f32 v116, v117\n\t" \
  "v_mul_f32 v117, 0x4f000000, v117\n\t" \
  "v_mul_f32 v116, 0x47800000, v116\n\t" \
  "v_cvt_u32_f32 v117, v117\n\t" \
  "v_cvt_u32_f32 v116, v116\n\t" \
  "s_mov_b32 s53, s93\n\t" \
  "v_readfirstlane_b32 s46, v117\n\t" \
  "v_readfirstlane_b32 s52, v116\n\t" \
  "21:\n\t" \
  "s_add_i32 s44, s44, 1\n\t" \
  "s_and_b32 s58, s58, 4\n\t" \
  "s_and_b32 s76, s44, 64\n\t" \
  "s_or_b32 s42, s42, s76\n\t" \
  "s_cbranch_scc0 20b\n\t" \
  "s_branch 99f\n\t" \
  "14:\n\t" \
  "s_sleep 3\n\t" \
  "s_add_u32 s59, s59, 0x10000\n\t" \
  "s_cmp_lt_u32 s59, 0x100000\n\t" \
  "s_cbranch_scc1 13b\n\t" \
  "s_mov_b64 exec, -1\n\t" \
  "s_mov_b32 s42, 16\n\t" \
  "s_branch 99f\n\t" \
  "8:\n\t" \
  "s_bitcmp1_b32 s58, 1\n\t" \
  "s_cbranch_scc1 4b\n\t" \
  "s_bitcmp1_b32 s58, 2\n\t" \
  "s_cbranch_scc1 5b\n\t" \
  "v_max3_u32 v116, v32, v33, v34\n\t" \
  "v_max3_u32 v117, v35, v36, v37\n\t" \
  "v_max3_u32 v116, v116, v38, v39\n\t" \
  "v_max_u32 v116, v116, v117\n\t" \
  "v_cmp_eq_u32 vcc, -1, v116\n\t" \
  "s_cbranch_vccz 5b\n\t" \
  "s_branch 0f\n\t" \
  "9:\n\t" \
  "s_bitcmp1_b32 s58, 2\n\t" \
  "s_cbranch_scc1 6b\n\t" \
  "v_max3_u32 v116, v40, v41, v42\n\t" \
  "v_max3_u32 v117, v43, v44, v45\n\t" \
  "v_max3_u32 v116, v116, v46, v47\n\t" \
  "v_max_u32 v116, v116, v117\n\t" \
  "v_cmp_eq_u32 vcc, -1, v116\n\t" \
  "s_cbranch_vccz 6b\n\t" \
  "s_or_b32 s58, s58, 2\n\t" \
  "0:\n\t" \
  "s_or_b32 s58, s58, 1\n\t" \
  "11:\n\t" \
  "buffer_load_dwordx4 v[32:35], v100, %[rh], s40 offen sc1\n\t" \
  "buffer_load_dwordx4 v[36:39], v100, %[rh], s76 offen sc1\n\t" \
  "buffer_load_dwordx4 v[40:43], v100, %[rh], s77 offen sc1\n\t" \
  "buffer_load_dwordx4 v[44:47], v100, %[rh], s78 offen sc1\n\t" \
  "s_waitcnt lgkmcnt(0)\n\t" \
  "s_branch 10b\n\t" \
  "17:\n\t" \
  "s_add_u32 s59, s59, 1\n\t" \
  "s_bitcmp1_b32 s58, 2\n\t" \
  "s_cbranch_scc1 3b\n\t" \
  "s_waitcnt vmcnt(0)\n\t" \
  "v_max_u32 v116, v32, v33\n\t" \
  "v_max3_u32 v116, v116, v34, v35\n\t" \
  "v_max3_u32 v116, v116, v36, v37\n\t" \
  "v_max3_u32 v116, v116, v38, v39\n\t" \
  "v_max3_u32 v116, v116, v40, v41\n\t" \
  "v_max3_u32 v116, v116, v42, v43\n\t" \
  "v_max3_u32 v116, v116, v44, v45\n\t" \
  "v_max3_u32 v116, v116, v46, v47\n\t" \
  "v_cmp_eq_u32 vcc, -1, v116\n\t" \
  "s_cbranch_vccz 3b\n\t" \
  "s_bitcmp1_b32 s59, 6\n\t" \
  "s_cbranch_scc1 12f\n\t" \
  "buffer_load_dwordx4 v[32:35], v100, %[rh], s40 offen sc1\n\t" \
  "buffer_load_dwordx4 v[36:39], v100, %[rh], s76 offen sc1\n\t" \
  "buffer_load_dwordx4 v[40:43], v100, %[rh], s77 offen sc1\n\t" \
  "buffer_load_dwordx4 v[44:47], v100, %[rh], s78 offen sc1\n\t" \
  "s_branch 17b\n\t" \
  "12:\n\t" \
  "s_mov_b32 s42, s58\n\t" \
  "s_branch 99f\n\t" \
  "93:\n\t" \
  "s_mov_b32 s58, s42\n\t" \
  "20:\n\t" \
  "s_and_b32 s76, s44, 7\n\t" \
  "s_lshl_b32 s76, s76, 15\n\t" \
  "s_add_i32 s40, s54, s76\n\t" \
  "s_and_b32 s76, s44, 3\n\t" \
  "s_lshl_b32 s45, s76, 14\n\t" \
  "s_lshl_b32 s47, s76, 20\n\t" \
  "s_add_i32 s47, s47, s55\n\t" \
  "s_lshr_b32 s76, s47, 21\n\t" \
  "s_bitcmp1_b64 %[sel], s76\n\t" \
  "s_cselect_b32 s76, 0x2000, 0\n\t" \
  "s_add_i32 s47, s47, s76\n\t" \
  "s_add_i32 s49, s44, -1\n\t" \
  "s_and_b32 s48, s49, 2\n\t" \
  "s_lshl_b32 s48, s48, 13\n\t" \
  "s_and_b32 s76, s49, 1\n\t" \
  "s_lshl_b32 s76, s76, 12\n\t" \
  "s_add_i32 s48, s48, s76\n\t" \
  "s_lshl_b32 s50, s53, 8\n\t" \
  "s_mov_b32 s59, 0\n\t" \
  "s_mov_b32 s92, s51\n\t" \
  "s_mov_b32 s93, s53\n\t" \
  "s_xor_b32 s76, s40, 0x400\n\t" \
  "s_xor_b32 s77, s40, 0x800\n\t" \
  "s_xor_b32 s78, s40, 0xc00\n\t" \
  "s_cmp_lg_u32 s58, 0\n\t" \
  "s_cbranch_scc1 11f\n\t" \
  "10:\n\t" \
  "ds_read_b128 v[48:51], v102 offset:49152\n\t" \
  "ds_read_b128 v[52:55], v102 offset:50176\n\t" \
  "ds_read_b128 v[56:59], v102 offset:51200\n\t" \
  "ds_read_b128 v[60:63], v102 offset:52224\n\t" \
  "ds_read_b128 v[0:3], v102 offset:0\n\t" \
  "ds_read_b128 v[4:7], v102 offset:1024\n\t" \
  "ds_read_b128 v[8:11], v102 offset:2048\n\t" \
  "ds_read_b128 v[12:15], v102 offset:3072\n\t" \
  "ds_read_b128 v[16:19], v102 offset:16384\n\t" \
  "ds_read_b128 v[20:23], v102 offset:17408\n\t" \
  "ds_read_b128 v[24:27], v102 offset:18432\n\t" \
  "ds_read_b128 v[28:31], v102 offset:19456\n\t" \
  "s_waitcnt lgkmcnt(4)\n\t" \
  "ds_read_b128 v[80:83], v102 offset:32768\n\t" \
  "ds_read_b128 v[84:87], v102 offset:33792\n\t" \
  "ds_read_b128 v[88:91], v102 offset:34816\n\t" \
  "ds_read_b128 v[92:95], v102 offset:35840\n\t" \
  "s_bitcmp1_b32 s58, 1\n\t" \
  "s_cselect_b64 s[80:81], -1, 0\n\t" \
  "s_cmp_lg_u32 s58, 0\n\t" \
  "s_cbranch_scc1 17f\n\t" \
  "s_mov_b32 exec_hi, 0\n\t" \
  "16:\n\t" \
  "s_memtime vcc\n\t" \
  "s_waitcnt lgkmcnt(0)\n\t" \
  "s_sub_i32 s79, s41, vcc_lo\n\t" \
  "s_cmp_gt_i32 s79, 64\n\t" \
  "s_cbranch_scc1 16b\n\t" \
  "13:\n\t" \
  "buffer_load_dword v116, v112, %[rh], s48 offen sc1\n\t" \
  "s_waitcnt vmcnt(0)\n\t" \
  "15:\n\t" \
  "v_mov_b32 v117, 20000\n\t" \
  "v_cmp_ne_u32_sdwa vcc, v116, s49 src0_sel:BYTE_0 src1_sel:DWORD\n\t" \
  "v_subrev_u32 v116, s50, v116\n\t" \
  "v_ashrrev_i32 v116, 8, v116\n\t" \
  "s_nop 1\n\t" \
  "v_max_i32_dpp v116, v116, v116 quad_perm:[1,0,3,2] row_mask:0xf bank_mask:0xf\n\t" \
  "s_cbranch_vccnz 14f\n\t" \
  "s_nop 0\n\t" \
  "v_max_i32_dpp v116, v116, v116 quad_perm:[2,3,0,1] row_mask:0xf bank_mask:0xf\n\t" \
  "s_nop 1\n\t" \
  "v_max_i32_dpp v116, v116, v116 row_half_mirror row_mask:0xf bank_mask:0xf\n\t" \
  "s_nop 1\n\t" \
  "v_max_i32_dpp v116, v116, v116 row_mirror row_mask:0xf bank_mask:0xf\n\t" \
  "v_med3_i32 v116, v116, 0, v117\n\t" \
  "v_mul_u32_u24 v116, s52, v116\n\t" \
  "v_lshrrev_b32 v116, 16, v116\n\t" \
  "s_mov_b64 exec, -1\n\t" \
  "v_readfirstlane_b32 s84, v116\n\t" \
  "v_readlane_b32 s85, v116, 16\n\t" \
  "s_max_u32 s84, s84, s85\n\t" \
  "s_add_i32 s84, s84, s51\n\t" \
  "1:\n\t" \
  "s_memtime vcc\n\t" \
  "s_waitcnt lgkmcnt(0)\n\t" \
  "s_sub_i32 s79, s84, vcc_lo\n\t" \
  "s_cmp_gt_i32 s79, 128\n\t" \
  "s_cbranch_scc1 1b\n\t" \
  ".p2align 4\n\t" \
  "2:\n\t" \
  "s_sub_i32 s79, s79, 33\n\t" \
  "s_cmp_gt_i32 s79, 56\n\t" \
  "s_cbranch_scc1 2b\n\t" \
  "buffer_load_dwordx4 v[32:35], v100, %[rh], s40 offen sc1\n\t" \
  "s_nop 12\n\t" \
  "buffer_load_dwordx4 v[36:39], v100, %[rh], s76 offen sc1\n\t" \
  "s_nop 12\n\t" \
  "buffer_load_dwordx4 v[40:43], v100, %[rh], s77 offen sc1\n\t" \
  "s_nop 12\n\t" \
  "buffer_load_dwordx4 v[44:47], v100, %[rh], s78 offen sc1\n\t" \
  "s_memtime vcc\n\t" \
  "s_memrealtime s[82:83]\n\t" \
  "s_nop 4\n\t" \
  "s_memrealtime s[84:85]\n\t" \
  "s_nop 4\n\t" \
  "s_memrealtime s[56:57]\n\t" \
  "s_nop 4\n\t" \
  "s_memrealtime s[86:87]\n\t" \
  "s_nop 4\n\t" \
  "s_memrealtime s[88:89]\n\t" \
  "s_nop 4\n\t" \
  "s_memrealtime s[90:91]\n\t" \
  "s_mov_b32 s92, 0\n\t" \
  "s_waitcnt lgkmcnt(0)\n\t" \
  "s_cmp_eq_u32 s82, s84\n\t" \
  "s_addc_u32 s92, s92, 0\n\t" \
  "s_cmp_eq_u32 s82, s56\n\t" \
  "s_addc_u32 s92, s92, 0\n\t" \
  "s_cmp_eq_u32 s82, s86\n\t" \
  "s_addc_u32 s92, s92, 0\n\t" \
  "s_cmp_eq_u32 s82, s88\n\t" \
  "s_addc_u32 s92, s92, 0\n\t" \
  "s_cmp_eq_u32 s82, s90\n\t" \
  "s_addc_u32 s92, s92, 0\n\t" \
  "s_mul_i32 s92, s92, 24\n\t" \
  "s_add_i32 s92, s92, vcc_lo\n\t" \
  "s_lshl4_add_u32 s93, s82, 64\n\t" \
  "3:\n\t" \
  "v_mov_b64 v[72:73], 0\n\t" \
  "v_mov_b64 v[74:75], 0\n\t" \
  "v_mov_b64 v[76:77], 0\n\t" \
  "v_mov_b64 v[78:79], 0\n\t" \
  "s_waitcnt lgkmcnt(0)\n\t" \
  "s_waitcnt vmcnt(3)\n\t" \
  "v_smfmac_f32_16x16x64_bf16 v[72:75], v[32:35], v[48:55], v104\n\t" \
  "ds_write_b128 v101, v[32:35] offset:12288\n\t" \
  "v_smfmac_f32_16x16x64_bf16 v[76:79], v[32:35], v[56:63], v104\n\t" \
  "s_waitcnt vmcnt(2)\n\t" \
  "ds_write_b128 v101, v[36:39] offset:13312\n\t" \
  "v_smfmac_f32_16x16x64_bf16 v[72:75], v[36:39], a[144:151], v104\n\t" \
  "v_smfmac_f32_16x16x64_bf16 v[76:79], v[36:39], a[152:159], v104\n\t" \
  "s_nop 3\n\t" \
  "v_cmp_u_f32 vcc, v72, v73\n\t" \
  "v_cmp_u_f32 s[82:83], v74, v75\n\t" \
  "s_waitcnt vmcnt(1)\n\t" \
  "v_smfmac_f32_16x16x64_bf16 v[72:75], v[40:43], a[160:167], v104\n\t" \
  "s_or_b64 vcc, vcc, s[82:83]\n\t" \
  "s_or_b64 vcc, vcc, s[80:81]\n\t" \
  "v_smfmac_f32_16x16x64_bf16 v[76:79], v[40:43], a[168:175], v104\n\t" \
  "s_waitcnt lgkmcnt(0)\n\t" \
  "s_cbranch_scc1 8f\n\t" \
  "5:\n\t" \
  "s_barrier\n\t" \
  "4:\n\t" \
  "s_memtime s[56:57]\n\t" \
  "ds_read_b128 v[48:51], v101 offset:0\n\t" \
  "ds_read_b128 v[52:55], v101 offset:1024\n\t" \
  "ds_read_b128 v[56:59], v101 offset:5120\n\t" \
  "s_waitcnt vmcnt(0)\n\t" \
  "v_smfmac_f32_16x16x64_bf16 v[72:75], v[44:47], a[176:183], v104\n\t" \
  "ds_read_b128 v[60:63], v101 offset:4096\n\t" \
  "ds_read_b128 v[64:67], v101 offset:9216\n\t" \
  "v_smfmac_f32_16x16x64_bf16 v[76:79], v[44:47], a[184:191], v104\n\t" \
  "ds_read_b128 v[68:71], v101 offset:8192\n\t" \
  "ds_write_b128 v101, v[40:43] offset:14336\n\t" \
  "s_nop 1\n\t" \
  "v_cmp_u_f32 vcc, v72, v73\n\t" \
  "v_cmp_u_f32 s[82:83], v74, v75\n\t" \
  "s_waitcnt lgkmcnt(6)\n\t" \
  "v_smfmac_f32_16x16x64_bf16 v[72:75], v[48:51], v[0:7], v104\n\t" \
  "s_or_b64 vcc, vcc, s[82:83]\n\t" \
  "v_smfmac_f32_16x16x64_bf16 v[76:79], v[48:51], v[8:15], v104\n\t" \
  "s_cbranch_scc1 9f\n\t" \
  "6:\n\t" \
  "s_waitcnt lgkmcnt(5)\n\t" \
  "v_smfmac_f32_16x16x64_bf16 v[72:75], v[52:55], a[0:7], v104\n\t" \
  "ds_write_b128 v101, v[44:47] offset:15360\n\t" \
  "v_smfmac_f32_16x16x64_bf16 v[76:79], v[52:55], a[8:15], v104\n\t" \
  "v_mov_b64 v[40:41], 0\n\t" \
  "s_waitcnt lgkmcnt(5)\n\t" \
  "v_smfmac_f32_16x16x64_bf16 v[72:75], v[56:59], a[48:55], v104\n\t" \
  "v_mov_b64 v[42:43], 0\n\t" \
  "v_smfmac_f32_16x16x64_bf16 v[76:79], v[56:59], a[56:63], v104\n\t" \
  "s_waitcnt lgkmcnt(4)\n\t" \
  "v_smfmac_f32_16x16x64_bf16 v[72:75], v[60:63], v[16:23], v104\n\t" \
  "s_waitcnt lgkmcnt(0)\n\t" \
  "s_barrier\n\t" \
  "v_smfmac_f32_16x16x64_bf16 v[76:79], v[60:63], v[24:31], v104\n\t" \
  "ds_read_b128 v[48:51], v101 offset:2048\n\t" \
  "ds_read_b128 v[52:55], v101 offset:6144\n\t" \
  "v_smfmac_f32_16x16x64_bf16 v[72:75], v[64:67], a[96:103], v104\n\t" \
  "ds_read_b128 v[56:59], v101 offset:10240\n\t" \
  "ds_read_b128 v[60:63], v101 offset:3072\n\t" \
  "v_smfmac_f32_16x16x64_bf16 v[76:79], v[64:67], a[104:111], v104\n\t" \
  "ds_read_b128 v[32:35], v101 offset:7168\n\t" \
  "ds_read_b128 v[36:39], v101 offset:11264\n\t" \
  "v_smfmac_f32_16x16x64_bf16 v[72:75], v[68:71], v[80:87], v104\n\t" \
  "ds_read_b128 v[0:3], v103 offset:0\n\t" \
  "v_mov_b64 v[44:45], 0\n\t" \
  "v_smfmac_f32_16x16x64_bf16 v[76:79], v[68:71], v[88:95], v104\n\t" \
  "v_mov_b64 v[46:47], 0\n\t" \
  "s_waitcnt lgkmcnt(5)\n\t" \
  "v_smfmac_f32_16x16x64_bf16 v[72:75], v[48:51], a[16:23], v104\n\t" \
  "ds_read_b128 v[4:7], v103 offset:1024\n\t" \
  "v_mov_b64 v[80:81], 0\n\t" \
  "v_smfmac_f32_16x16x64_bf16 v[72:75], v[52:55], a[64:71], v104\n\t" \
  "v_mov_b64 v[82:83], 0\n\t" \
  "s_waitcnt lgkmcnt(2)\n\t" \
  "v_smfmac_f32_16x16x64_bf16 v[72:75], v[56:59], a[112:119], v104\n\t" \
  "ds_read_b128 v[8:11], v103 offset:2048\n\t" \
  "v_mov_b64 v[84:85], 0\n\t" \
  "v_smfmac_f32_16x16x64_bf16 v[72:75], v[60:63], a[32:39], v104\n\t" \
  "ds_read_b128 v[12:15], v103 offset:3072\n\t" \
  "v_mov_b64 v[86:87], 0\n\t" \
  "v_smfmac_f32_16x16x64_bf16 v[72:75], v[32:35], a[80:87], v104\n\t" \
  "ds_read_b128 v[16:19], v103 offset:16384\n\t" \
  "v_mov_b64 v[88:89], 0\n\t" \
  "v_smfmac_f32_16x16x64_bf16 v[72:75], v[36:39], a[128:135], v104\n\t" \
  "ds_read_b128 v[20:23], v103 offset:17408\n\t" \
  "v_mov_b64 v[90:91], 0\n\t" \
  "v_smfmac_f32_16x16x64_bf16 v[76:79], v[48:51], a[24:31], v104\n\t" \
  "ds_read_b128 v[24:27], v103 offset:18432\n\t" \
  "v_mov_b64 v[92:93], 0\n\t" \
  "v_smfmac_f32_16x16x64_bf16 v[76:79], v[52:55], a[72:79], v104\n\t" \
  "v_add_f32 v96, v72, v74\n\t" \
  "v_add_f32 v97, v73, v75\n\t" \
  "v_cvt_pk_bf16_f32 v96, v96, v97\n\t" \
  "ds_write_b32 v105, v96\n\t" \
  "v_smfmac_f32_16x16x64_bf16 v[76:79], v[56:59], a[120:127], v104\n\t" \
  "ds_read_b128 v[28:31], v103 offset:19456\n\t" \
  "v_mov_b64 v[94:95], 0\n\t" \
  "v_smfmac_f32_16x16x64_bf16 v[76:79], v[60:63], a[40:47], v104\n\t" \
  "v_smfmac_f32_16x16x64_bf16 v[76:79], v[32:35], a[88:95], v104\n\t" \
  "v_smfmac_f32_16x16x64_bf16 v[76:79], v[36:39], a[136:143], v104\n\t" \
  "s_waitcnt lgkmcnt(1)\n\t" \
  "s_barrier\n\t" \
  "ds_read_b64_tr_b16 v[96:97], v107 offset:0\n\t" \
  "ds_read_b64_tr_b16 v[98:99], v107 offset:64\n\t" \
  "ds_read_b64_tr_b16 v[56:57], v107 offset:1024\n\t" \
  "ds_read_b64_tr_b16 v[58:59], v107 offset:1088\n\t" \
  "s_nop 1\n\t" \
  "v_add_f32 v116, v76, v78\n\t" \
  "v_add_f32 v117, v77, v79\n\t" \
  "v_cvt_pk_bf16_f32 v116, v116, v117\n\t" \
  "ds_write_b32 v105, v116 offset:256\n\t" \
  "s_waitcnt lgkmcnt(3)\n\t" \
  "v_smfmac_f32_16x16x64_bf16 v[40:43], v[96:99], a[192:199], v106\n\t" \
  "v_smfmac_f32_16x16x64_bf16 v[44:47], v[96:99], a[224:231], v106\n\t" \
  "s_waitcnt lgkmcnt(0)\n\t" \
  "s_barrier\n\t" \
  "ds_read_b64_tr_b16 v[52:53], v107 offset:256\n\t" \
  "ds_read_b64_tr_b16 v[54:55], v107 offset:320\n\t" \
  "v_smfmac_f32_16x16x64_bf16 v[40:43], v[56:59], a[208:215], v106\n\t" \
  "ds_read_b64_tr_b16 v[60:61], v107 offset:1280\n\t" \
  "ds_read_b64_tr_b16 v[62:63], v107 offset:1344\n\t" \
  "v_smfmac_f32_16x16x64_bf16 v[44:47], v[56:59], v[0:7], v106\n\t" \
  "v_smfmac_f32_16x16x64_bf16 v[80:83], v[96:99], a[240:247], v106\n\t" \
  "v_mov_b64 v[32:33], 0\n\t" \
  "v_smfmac_f32_16x16x64_bf16 v[84:87], v[96:99], v[136:143], v106\n\t" \
  "v_mov_b64 v[34:35], 0\n\t" \
  "v_smfmac_f32_16x16x64_bf16 v[80:83], v[56:59], v[120:127], v106\n\t" \
  "v_mov_b64 v[36:37], 0\n\t" \
  "v_smfmac_f32_16x16x64_bf16 v[84:87], v[56:59], v[16:23], v106\n\t" \
  "s_waitcnt lgkmcnt(2)\n\t" \
  "v_smfmac_f32_16x16x64_bf16 v[40:43], v[52:55], a[200:207], v106\n\t" \
  "v_smfmac_f32_16x16x64_bf16 v[44:47], v[52:55], a[232:239], v106\n\t" \
  "s_xor_b32 s76, s47, 0x40000\n\t" \
  "s_waitcnt lgkmcnt(0)\n\t" \
  "v_smfmac_f32_16x16x64_bf16 v[40:43], v[60:63], a[216:223], v106\n\t" \
  "v_mov_b64 v[38:39], 0\n\t" \
  "s_xor_b32 s77, s47, 0x80000\n\t" \
  "v_smfmac_f32_16x16x64_bf16 v[44:47], v[60:63], v[8:15], v106\n\t" \
  "s_xor_b32 s78, s47, 0xc0000\n\t" \
  "v_smfmac_f32_16x16x64_bf16 v[80:83], v[52:55], a[248:255], v106\n\t" \
  "ds_read_b128 v[0:3], v103 offset:32768\n\t" \
  "v_cvt_pk_bf16_f32 v64, v40, v41\n\t" \
  "v_smfmac_f32_16x16x64_bf16 v[84:87], v[52:55], v[144:151], v106\n\t" \
  "ds_read_b128 v[4:7], v103 offset:33792\n\t" \
  "v_cvt_pk_bf16_f32 v65, v42, v43\n\t" \
  "v_smfmac_f32_16x16x64_bf16 v[80:83], v[60:63], v[128:135], v106\n\t" \
  "ds_read_b128 v[8:11], v103 offset:34816\n\t" \
  "v_cvt_pk_bf16_f32 v66, v44, v45\n\t" \
  "v_smfmac_f32_16x16x64_bf16 v[84:87], v[60:63], v[24:31], v106\n\t" \
  "ds_read_b128 v[12:15], v103 offset:35840\n\t" \
  "v_cvt_pk_bf16_f32 v67, v46, v47\n\t" \
  "v_smfmac_f32_16x16x64_bf16 v[88:91], v[96:99], v[152:159], v106\n\t" \
  "v_permlane16_swap_b32 v64, v66\n\t" \
  "v_cvt_pk_bf16_f32 v68, v80, v81\n\t" \
  "v_smfmac_f32_16x16x64_bf16 v[92:95], v[96:99], v[184:191], v106\n\t" \
  "v_permlane16_swap_b32 v65, v67\n\t" \
  "v_cvt_pk_bf16_f32 v69, v82, v83\n\t" \
  "v_smfmac_f32_16x16x64_bf16 v[88:91], v[52:55], v[160:167], v106\n\t" \
  "v_cvt_pk_bf16_f32 v70, v84, v85\n\t" \
  "v_smfmac_f32_16x16x64_bf16 v[92:95], v[52:55], v[192:199], v106\n\t" \
  "v_cvt_pk_bf16_f32 v71, v86, v87\n\t" \
  "v_smfmac_f32_16x16x64_bf16 v[88:91], v[56:59], v[168:175], v106\n\t" \
  "ds_read_b128 v[16:19], v103 offset:49152\n\t" \
  "buffer_store_dwordx4 v[64:67], v108, %[ru], s47 offen sc1\n\t" \
  "v_permlane16_swap_b32 v68, v70\n\t" \
  "s_waitcnt lgkmcnt(3)\n\t" \
  "v_smfmac_f32_16x16x64_bf16 v[92:95], v[56:59], v[0:7], v106\n\t" \
  "ds_read_b128 v[20:23], v103 offset:50176\n\t" \
  "v_permlane16_swap_b32 v69, v71\n\t" \
  "v_smfmac_f32_16x16x64_bf16 v[88:91], v[60:63], v[176:183], v106\n\t" \
  "ds_read_b128 v[24:27], v103 offset:51200\n\t" \
  "s_waitcnt lgkmcnt(3)\n\t" \
  "v_smfmac_f32_16x16x64_bf16 v[92:95], v[60:63], v[8:15], v106\n\t" \
  "ds_read_b128 v[28:31], v103 offset:52224\n\t" \
  "v_smfmac_f32_16x16x64_bf16 v[32:35], v[96:99], v[200:207], v106\n\t" \
  "buffer_store_dwordx4 v[68:71], v108, %[ru], s76 offen sc1\n\t" \
  "v_cvt_pk_bf16_f32 v72, v88, v89\n\t" \
  "v_smfmac_f32_16x16x64_bf16 v[36:39], v[96:99], v[232:239], v106\n\t" \
  "v_cvt_pk_bf16_f32 v73, v90, v91\n\t" \
  "v_smfmac_f32_16x16x64_bf16 v[32:35], v[52:55], v[208:215], v106\n\t" \
  "v_cvt_pk_bf16_f32 v74, v92, v93\n\t" \
  "v_smfmac_f32_16x16x64_bf16 v[36:39], v[52:55], v[240:247], v106\n\t" \
  "v_cvt_pk_bf16_f32 v75, v94, v95\n\t" \
  "v_smfmac_f32_16x16x64_bf16 v[32:35], v[56:59], v[216:223], v106\n\t" \
  "v_permlane16_swap_b32 v72, v74\n\t" \
  "v_smfmac_f32_16x16x64_bf16 v[32:35], v[60:63], v[224:231], v106\n\t" \
  "v_permlane16_swap_b32 v73, v75\n\t" \
  "s_waitcnt lgkmcnt(2)\n\t" \
  "v_smfmac_f32_16x16x64_bf16 v[36:39], v[56:59], v[16:23], v106\n\t" \
  "s_waitcnt lgkmcnt(0)\n\t" \
  "v_smfmac_f32_16x16x64_bf16 v[36:39], v[60:63], v[24:31], v106\n\t" \
  "buffer_store_dwordx4 v[72:75], v108, %[ru], s77 offen sc1\n\t" \
  "v_cvt_pk_bf16_f32 v76, v32, v33\n\t" \
  "v_cvt_pk_bf16_f32 v77, v34, v35\n\t" \
  "s_nop 5\n\t" \
  "v_cvt_pk_bf16_f32 v78, v36, v37\n\t" \
  "v_cvt_pk_bf16_f32 v79, v38, v39\n\t" \
  "s_nop 0\n\t" \
  "v_permlane16_swap_b32 v76, v78\n\t" \
  "v_permlane16_swap_b32 v77, v79\n\t" \
  "buffer_store_dwordx4 v[76:79], v108, %[ru], s78 offen sc1\n\t" \
  "s_memtime s[76:77]\n\t" \
  "s_mov_b32 s42, 0\n\t" \
  "s_waitcnt lgkmcnt(0)\n\t" \
  "s_mov_b32 s57, s76\n\t" \
  "s_add_i32 s41, s56, 1950\n\t" \
  "s_sub_i32 s76, s92, s51\n\t" \
  "s_sub_i32 s77, s93, s53\n\t" \
  "s_cmp_eq_u32 s76, 0\n\t" \
  "s_cbranch_scc1 21f\n\t" \
  "v_cvt_f32_u32 v117, s76\n\t" \
  "v_cvt_f32_u32 v116, s77\n\t" \
  "v_rcp_f32 v117, v117\n\t" \
  "s_mov_b32 s51, s92\n\t" \
  "v_mul_f32 v117, v117, v116\n\t" \
  "v_max_f32 v117, 0x3f028f5c, v117\n\t" \
  "v_min_f32 v117, 0x3ffeb852, v117\n\t" \
  "v_rcp_f32 v116, v117\n\t" \
  "v_mul_f32 v117, 0x4f000000, v117\n\t" \
  "v_mul_f32 v116, 0x47800000, v116\n\t" \
  "v_cvt_u32_f32 v117, v117\n\t" \
  "v_cvt_u32_f32 v116, v116\n\t" \
  "s_mov_b32 s53, s93\n\t" \
  "v_readfirstlane_b32 s46, v117\n\t" \
  "v_readfirstlane_b32 s52, v116\n\t" \
  "21:\n\t" \
  "s_add_i32 s44, s44, 1\n\t" \
  "s_and_b32 s58, s58, 4\n\t" \
  "s_and_b32 s76, s44, 64\n\t" \
  "s_or_b32 s42, s42, s76\n\t" \
  "s_cbranch_scc0 20b\n\t" \
  "s_branch 99f\n\t" \
  "14:\n\t" \
  "s_sleep 3\n\t" \
  "s_add_u32 s59, s59, 0x10000\n\t" \
  "s_cmp_lt_u32 s59, 0x100000\n\t" \
  "s_cbranch_scc1 13b\n\t" \
  "s_mov_b64 exec, -1\n\t" \
  "s_mov_b32 s42, 16\n\t" \
  "s_branch 99f\n\t" \
  "8:\n\t" \
  "s_bitcmp1_b32 s58, 1\n\t" \
  "s_cbranch_scc1 4b\n\t" \
  "s_bitcmp1_b32 s58, 2\n\t" \
  "s_cbranch_scc1 5b\n\t" \
  "v_max3_u32 v116, v32, v33, v34\n\t" \
  "v_max3_u32 v117, v35, v36, v37\n\t" \
  "v_max3_u32 v116, v116, v38, v39\n\t" \
  "v_max_u32 v116, v116, v117\n\t" \
  "v_cmp_eq_u32 vcc, -1, v116\n\t" \
  "s_cbranch_vccz 5b\n\t" \
  "s_branch 0f\n\t" \
  "9:\n\t" \
  "s_bitcmp1_b32 s58, 2\n\t" \
  "s_cbranch_scc1 6b\n\t" \
  "v_max3_u32 v116, v40, v41, v42\n\t" \
  "v_max3_u32 v117, v43, v44, v45\n\t" \
  "v_max3_u32 v116, v116, v46, v47\n\t" \
  "v_max_u32 v116, v116, v117\n\t" \
  "v_cmp_eq_u32 vcc, -1, v116\n\t" \
  "s_cbranch_vccz 6b\n\t" \
  "s_or_b32 s58, s58, 2\n\t" \
  "0:\n\t" \
  "s_or_b32 s58, s58, 1\n\t" \
  "11:\n\t" \
  "buffer_load_dwordx4 v[32:35], v100, %[rh], s40 offen sc1\n\t" \
  "buffer_load_dwordx4 v[36:39], v100, %[rh], s76 offen sc1\n\t" \
  "buffer_load_dwordx4 v[40:43], v100, %[rh], s77 offen sc1\n\t" \
  "buffer_load_dwordx4 v[44:47], v100, %[rh], s78 offen sc1\n\t" \
  "s_waitcnt lgkmcnt(0)\n\t" \
  "s_branch 10b\n\t" \
  "17:\n\t" \
  "s_add_u32 s59, s59, 1\n\t" \
  "s_bitcmp1_b32 s58, 2\n\t" \
  "s_cbranch_scc1 3b\n\t" \
  "s_waitcnt vmcnt(0)\n\t" \
  "v_max_u32 v116, v32, v33\n\t" \
  "v_max3_u32 v116, v116, v34, v35\n\t" \
  "v_max3_u32 v116, v116, v36, v37\n\t" \
  "v_max3_u32 v116, v116, v38, v39\n\t" \
  "v_max3_u32 v116, v116, v40, v41\n\t" \
  "v_max3_u32 v116, v116, v42, v43\n\t" \
  "v_max3_u32 v116, v116, v44, v45\n\t" \
  "v_max3_u32 v116, v116, v46, v47\n\t" \
  "v_cmp_eq_u32 vcc, -1, v116\n\t" \
  "s_cbranch_vccz 3b\n\t" \
  "s_bitcmp1_b32 s59, 6\n\t" \
  "s_cbranch_scc1 12f\n\t" \
  "buffer_load_dwordx4 v[32:35], v100, %[rh], s40 offen sc1\n\t" \
  "buffer_load_dwordx4 v[36:39], v100, %[rh], s76 offen sc1\n\t" \
  "buffer_load_dwordx4 v[40:43], v100, %[rh], s77 offen sc1\n\t" \
  "buffer_load_dwordx4 v[44:47], v100, %[rh], s78 offen sc1\n\t" \
  "s_branch 17b\n\t" \
  "12:\n\t" \
  "s_mov_b32 s42, s58\n\t" \
  "s_branch 99f\n\t" \
  "99:\n\t" \
  "s_waitcnt vmcnt(0) lgkmcnt(0)\n\t" \
  ".Lchain_last:\n\t" \
  ""
#define CB_BODY \
  "s_cmp_gt_u32 %[wv], 1\n\t" \
  "s_cbranch_scc1 91f\n\t" \
  "s_mov_b32 s58, s42\n\t" \
  "s_mov_b32 s59, 0\n\t" \
  "s_bitcmp1_b32 s58, 4\n\t" \
  "s_cbranch_scc1 26f\n\t" \
  "s_bitcmp1_b32 s58, 3\n\t" \
  "s_cbranch_scc1 34f\n\t" \
  "s_bitcmp1_b32 s58, 1\n\t" \
  "s_cbranch_scc1 18f\n\t" \
  "17:\n\t" \
  "19:\n\t" \
  "s_add_i32 s85, s40, -1\n\t" \
  "s_and_b32 s82, s85, 2\n\t" \
  "s_lshl_b32 s82, s82, 13\n\t" \
  "s_and_b32 s80, s85, 1\n\t" \
  "s_lshl_b32 s80, s80, 12\n\t" \
  "s_add_i32 s82, s82, s80\n\t" \
  "s_add_i32 s82, s82, 0x84000\n\t" \
  "s_mov_b32 exec_hi, 0\n\t" \
  "buffer_load_dword v184, v200, %[rh], s82 offen sc1\n\t" \
  "buffer_load_dword v186, v203, %[ru], 0 offen sc1\n\t" \
  "s_mov_b64 exec, -1\n\t" \
  "20:\n\t" \
  "s_and_b32 s80, s40, 3\n\t" \
  "s_mul_i32 s77, s80, 0xc000\n\t" \
  "s_add_i32 s77, s77, 0x40000\n\t" \
  "s_lshl_b32 s78, s80, 14\n\t" \
  "s_add_i32 s78, s78, 0x70000\n\t" \
  "s_mov_b32 s57, 0\n\t" \
  "s_add_i32 s96, s77, 0x1000\n\t" \
  "s_lshl_b32 s88, s80, 20\n\t" \
  "s_add_i32 s88, s88, s48\n\t" \
  "s_lshr_b32 s80, s88, 21\n\t" \
  "s_bitcmp1_b64 %[sel], s80\n\t" \
  "s_cselect_b32 s80, 0x2000, 0\n\t" \
  "s_add_i32 s88, s88, s80\n\t" \
  "s_xor_b32 s89, s88, 0x800\n\t" \
  "s_xor_b32 s90, s88, 0x1000\n\t" \
  "s_xor_b32 s91, s88, 0x1800\n\t" \
  "s_add_i32 s92, s77, s50\n\t" \
  "s_xor_b32 s93, s92, 0x100\n\t" \
  "s_xor_b32 s94, s92, 0x200\n\t" \
  "s_xor_b32 s95, s92, 0x300\n\t" \
  "s_add_i32 s97, s40, 1\n\t" \
  "s_and_b32 s97, s97, 7\n\t" \
  "s_lshl_b32 s97, s97, 15\n\t" \
  "s_add_i32 s97, s97, s49\n\t" \
  "s_memtime vcc\n\t" \
  "s_memrealtime s[86:87]\n\t" \
  "s_nop 4\n\t" \
  "s_memrealtime s[60:61]\n\t" \
  "s_nop 4\n\t" \
  "s_memrealtime s[62:63]\n\t" \
  "s_nop 4\n\t" \
  "s_memrealtime s[64:65]\n\t" \
  "s_nop 4\n\t" \
  "s_memrealtime s[66:67]\n\t" \
  "s_nop 4\n\t" \
  "s_memrealtime s[68:69]\n\t" \
  "s_mov_b32 s79, 0\n\t" \
  "s_waitcnt lgkmcnt(0)\n\t" \
  "s_cmp_eq_u32 s86, s60\n\t" \
  "s_addc_u32 s79, s79, 0\n\t" \
  "s_cmp_eq_u32 s86, s62\n\t" \
  "s_addc_u32 s79, s79, 0\n\t" \
  "s_cmp_eq_u32 s86, s64\n\t" \
  "s_addc_u32 s79, s79, 0\n\t" \
  "s_cmp_eq_u32 s86, s66\n\t" \
  "s_addc_u32 s79, s79, 0\n\t" \
  "s_cmp_eq_u32 s86, s68\n\t" \
  "s_addc_u32 s79, s79, 0\n\t" \
  "s_mul_i32 s79, s79, 24\n\t" \
  "s_add_i32 s79, s79, vcc_lo\n\t" \
  "s_lshl4_add_u32 s80, s86, 64\n\t" \
  "s_sub_i32 s86, s79, s41\n\t" \
  "s_sub_i32 s87, s80, s43\n\t" \
  "s_cmp_lt_u32 s86, 4000\n\t" \
  "s_cbranch_scc1 21f\n\t" \
  "v_cvt_f32_u32 v165, s86\n\t" \
  "v_cvt_f32_u32 v164, s87\n\t" \
  "v_rcp_f32 v165, v165\n\t" \
  "s_nop 0\n\t" \
  "v_mul_f32 v165, v165, v164\n\t" \
  "v_max_f32 v165, 0x3f028f5c, v165\n\t" \
  "v_min_f32 v165, 0x3ffeb852, v165\n\t" \
  "v_rcp_f32 v164, v165\n\t" \
  "v_mul_f32 v165, 0x4f000000, v165\n\t" \
  "v_mul_f32 v164, 0x47800000, v164\n\t" \
  "v_cvt_u32_f32 v165, v165\n\t" \
  "v_cvt_u32_f32 v164, v164\n\t" \
  "s_nop 0\n\t" \
  "v_readfirstlane_b32 s45, v165\n\t" \
  "v_readfirstlane_b32 s44, v164\n\t" \
  "21:\n\t" \
  "s_mov_b32 s41, s79\n\t" \
  "s_mov_b32 s43, s80\n\t" \
  "s_lshl_b32 s83, s43, 8\n\t" \
  "s_mov_b32 s84, 650\n\t" \
  "s_mov_b32 exec_hi, 0\n\t" \
  "s_waitcnt vmcnt(0)\n\t" \
  "v_cmp_ne_u32_sdwa vcc, v184, s85 src0_sel:BYTE_0 src1_sel:DWORD\n\t" \
  "v_subrev_u32 v184, s83, v184\n\t" \
  "v_ashrrev_i32 v184, 8, v184\n\t" \
  "s_cbranch_vccnz 22f\n\t" \
  "s_nop 0\n\t" \
  "v_max_i32_dpp v184, v184, v184 quad_perm:[1,0,3,2] row_mask:0xf bank_mask:0xf\n\t" \
  "s_nop 1\n\t" \
  "v_max_i32_dpp v184, v184, v184 quad_perm:[2,3,0,1] row_mask:0xf bank_mask:0xf\n\t" \
  "s_nop 1\n\t" \
  "v_max_i32_dpp v184, v184, v184 row_half_mirror row_mask:0xf bank_mask:0xf\n\t" \
  "s_nop 1\n\t" \
  "v_max_i32_dpp v184, v184, v184 row_mirror row_mask:0xf bank_mask:0xf\n\t" \
  "s_nop 0\n\t" \
  "v_readlane_b32 s81, v184, 0\n\t" \
  "v_readlane_b32 s82, v184, 16\n\t" \
  "s_max_i32 s81, s81, s82\n\t" \
  "s_add_i32 s81, s81, 1056\n\t" \
  "s_max_i32 s81, s81, 0\n\t" \
  "s_min_i32 s81, s81, 20000\n\t" \
  "s_mul_i32 s81, s81, s44\n\t" \
  "s_lshr_b32 s81, s81, 16\n\t" \
  "24:\n\t" \
  "s_add_i32 s81, s81, s41\n\t" \
  "1:\n\t" \
  "s_memtime vcc\n\t" \
  "s_waitcnt lgkmcnt(0)\n\t" \
  "s_sub_i32 s79, s81, vcc_lo\n\t" \
  "s_cmp_gt_i32 s79, 128\n\t" \
  "s_cbranch_scc1 1b\n\t" \
  ".p2align 4\n\t" \
  "2:\n\t" \
  "s_sub_i32 s79, s79, 33\n\t" \
  "s_cmp_gt_i32 s79, 56\n\t" \
  "s_cbranch_scc1 2b\n\t" \
  "buffer_load_dword v187, v200, %[rh], s78 offen sc1\n\t" \
  "s_memtime vcc\n\t" \
  "s_memrealtime s[86:87]\n\t" \
  "s_nop 4\n\t" \
  "s_memrealtime s[60:61]\n\t" \
  "s_nop 4\n\t" \
  "s_memrealtime s[62:63]\n\t" \
  "s_nop 4\n\t" \
  "s_memrealtime s[64:65]\n\t" \
  "s_nop 4\n\t" \
  "s_memrealtime s[66:67]\n\t" \
  "s_nop 4\n\t" \
  "s_memrealtime s[68:69]\n\t" \
  "s_mov_b32 s79, 0\n\t" \
  "s_waitcnt lgkmcnt(0)\n\t" \
  "s_cmp_eq_u32 s86, s60\n\t" \
  "s_addc_u32 s79, s79, 0\n\t" \
  "s_cmp_eq_u32 s86, s62\n\t" \
  "s_addc_u32 s79, s79, 0\n\t" \
  "s_cmp_eq_u32 s86, s64\n\t" \
  "s_addc_u32 s79, s79, 0\n\t" \
  "s_cmp_eq_u32 s86, s66\n\t" \
  "s_addc_u32 s79, s79, 0\n\t" \
  "s_cmp_eq_u32 s86, s68\n\t" \
  "s_addc_u32 s79, s79, 0\n\t" \
  "s_mul_i32 s79, s79, 24\n\t" \
  "s_add_i32 s79, s79, vcc_lo\n\t" \
  "s_lshl4_add_u32 s80, s86, 64\n\t" \
  "s_mov_b32 s41, s79\n\t" \
  "s_mov_b32 s43, s80\n\t" \
  "s_lshl_b32 s83, s43, 8\n\t" \
  "13:\n\t" \
  "s_waitcnt vmcnt(0)\n\t" \
  "s_memtime s[86:87]\n\t" \
  "v_cmp_ne_u32_sdwa vcc, v187, s40 src0_sel:BYTE_0 src1_sel:DWORD\n\t" \
  "v_subrev_u32 v184, s83, v187\n\t" \
  "v_ashrrev_i32 v184, 8, v184\n\t" \
  "s_nop 1\n\t" \
  "v_max_i32_dpp v184, v184, v184 quad_perm:[1,0,3,2] row_mask:0xf bank_mask:0xf\n\t" \
  "s_nop 1\n\t" \
  "v_max_i32_dpp v184, v184, v184 quad_perm:[2,3,0,1] row_mask:0xf bank_mask:0xf\n\t" \
  "s_cbranch_vccnz 14f\n\t" \
  "15:\n\t" \
  "s_nop 0\n\t" \
  "v_max_i32_dpp v184, v184, v184 row_half_mirror row_mask:0xf bank_mask:0xf\n\t" \
  "s_nop 1\n\t" \
  "v_max_i32_dpp v184, v184, v184 row_mirror row_mask:0xf bank_mask:0xf\n\t" \
  "s_mov_b64 exec, -1\n\t" \
  "v_readlane_b32 s57, v184, 0\n\t" \
  "v_readlane_b32 s82, v184, 16\n\t" \
  "s_waitcnt lgkmcnt(0)\n\t" \
  "s_sub_i32 s79, s41, s86\n\t" \
  "s_max_i32 s57, s57, s82\n\t" \
  "s_add_i32 s81, s57, 255\n\t" \
  "s_max_i32 s81, s81, 0\n\t" \
  "s_min_i32 s81, s81, 20000\n\t" \
  "s_mul_i32 s81, s81, s44\n\t" \
  "s_lshr_b32 s81, s81, 16\n\t" \
  "s_add_i32 s79, s79, s81\n\t" \
  "s_add_i32 s79, s79, -370\n\t" \
  ".p2align 4\n\t" \
  "3:\n\t" \
  "s_sub_i32 s79, s79, 33\n\t" \
  "s_cmp_gt_i32 s79, 56\n\t" \
  "s_cbranch_scc1 3b\n\t" \
  "buffer_load_dwordx4 v[72:75], v192, %[ru], s88 offen offset:0 sc1\n\t" \
  "s_nop 4\n\t" \
  "buffer_load_dwordx4 v[76:79], v192, %[ru], s88 offen offset:1024 sc1\n\t" \
  "s_nop 4\n\t" \
  "s_add_i32 s79, s79, 22\n\t" \
  ".p2align 4\n\t" \
  "4:\n\t" \
  "s_sub_i32 s79, s79, 33\n\t" \
  "s_cmp_gt_i32 s79, 56\n\t" \
  "s_cbranch_scc1 4b\n\t" \
  "buffer_load_dwordx4 v[80:83], v192, %[ru], s89 offen offset:0 sc1\n\t" \
  "s_nop 4\n\t" \
  "buffer_load_dwordx4 v[84:87], v192, %[ru], s89 offen offset:1024 sc1\n\t" \
  "s_nop 4\n\t" \
  "s_add_i32 s79, s79, 42\n\t" \
  ".p2align 4\n\t" \
  "5:\n\t" \
  "s_sub_i32 s79, s79, 33\n\t" \
  "s_cmp_gt_i32 s79, 56\n\t" \
  "s_cbranch_scc1 5b\n\t" \
  "buffer_load_dwordx4 v[88:91], v192, %[ru], s90 offen offset:0 sc1\n\t" \
  "buffer_load_dwordx4 v[92:95], v192, %[ru], s90 offen offset:1024 sc1\n\t" \
  "s_add_i32 s79, s79, 122\n\t" \
  ".p2align 4\n\t" \
  "9:\n\t" \
  "s_sub_i32 s79, s79, 33\n\t" \
  "s_cmp_gt_i32 s79, 56\n\t" \
  "s_cbranch_scc1 9b\n\t" \
  "buffer_load_dwordx4 v[104:107], v194, %[rh], s92 offen sc1\n\t" \
  "s_nop 4\n\t" \
  "buffer_load_dwordx4 v[108:111], v194, %[rh], s93 offen sc1\n\t" \
  "s_nop 4\n\t" \
  "buffer_load_dwordx4 v[112:115], v194, %[rh], s94 offen sc1\n\t" \
  "s_nop 4\n\t" \
  "buffer_load_dwordx4 v[116:119], v194, %[rh], s95 offen sc1\n\t" \
  "s_nop 4\n\t" \
  "buffer_load_dwordx4 v[96:99], v192, %[ru], s91 offen offset:0 sc1\n\t" \
  "s_nop 4\n\t" \
  "buffer_load_dwordx4 v[100:103], v192, %[ru], s91 offen offset:1024 sc1\n\t" \
  "s_nop 4\n\t" \
  "10:\n\t" \
  "s_waitcnt vmcnt(5)\n\t" \
  "v_mfma_f32_16x16x32_bf16 v[136:139], v[104:107], v[72:75], 0\n\t" \
  "v_mfma_f32_16x16x32_bf16 v[140:143], v[104:107], v[76:79], 0\n\t" \
  "s_waitcnt vmcnt(4)\n\t" \
  "v_mfma_f32_16x16x32_bf16 v[136:139], v[108:111], v[80:83], v[136:139]\n\t" \
  "v_mfma_f32_16x16x32_bf16 v[140:143], v[108:111], v[84:87], v[140:143]\n\t" \
  "s_waitcnt vmcnt(3)\n\t" \
  "v_mfma_f32_16x16x32_bf16 v[136:139], v[112:115], v[88:91], v[136:139]\n\t" \
  "v_mfma_f32_16x16x32_bf16 v[140:143], v[112:115], v[92:95], v[140:143]\n\t" \
  "s_waitcnt vmcnt(1)\n\t" \
  "v_mfma_f32_16x16x32_bf16 v[136:139], v[116:119], v[96:99], v[136:139]\n\t" \
  "s_waitcnt vmcnt(0)\n\t" \
  "v_mfma_f32_16x16x32_bf16 v[140:143], v[116:119], v[100:103], v[140:143]\n\t" \
  "s_barrier\n\t" \
  "ds_read2st64_b64 v[144:147], v195 offset1:1\n\t" \
  "s_nop 1\n\t" \
  "v_permlane32_swap_b32 v136, v138\n\t" \
  "v_permlane32_swap_b32 v137, v139\n\t" \
  "s_nop 1\n\t" \
  "v_permlane32_swap_b32 v140, v142\n\t" \
  "v_permlane32_swap_b32 v141, v143\n\t" \
  "v_add_f32 v136, v136, v142\n\t" \
  "v_add_f32 v137, v137, v143\n\t" \
  "s_waitcnt lgkmcnt(0)\n\t" \
  "v_add_f32 v136, v136, v144\n\t" \
  "v_add_f32 v137, v137, v145\n\t" \
  "v_add_f32 v136, v136, v146\n\t" \
  "v_add_f32 v137, v137, v147\n\t" \
  "v_cvt_pk_bf16_f32 v148, v136, v137\n\t" \
  "s_nop 1\n\t" \
  "v_mov_b32_dpp v149, v148 row_half_mirror row_mask:0xf bank_mask:0xf\n\t" \
  "v_perm_b32 v150, v148, v149, v205\n\t" \
  "v_cmpx_o_f16_sdwa vcc, v150, v150 src0_sel:WORD_0 src1_sel:WORD_1\n\t" \
  "6:\n\t" \
  "buffer_store_dword v150, v201, %[rh], s97 offen sc1\n\t" \
  "s_memtime s[82:83]\n\t" \
  "s_mov_b64 exec, -1\n\t" \
  "s_cmp_eq_u64 vcc, -1\n\t" \
  "s_cbranch_scc0 8f\n\t" \
  "v_mov_b32 v151, 0\n\t" \
  "5:\n\t" \
  "ds_write_b32 v206, v151\n\t" \
  "s_waitcnt lgkmcnt(0)\n\t" \
  "s_mov_b32 s56, s82\n\t" \
  "s_barrier\n\t" \
  "ds_read2_b32 v[148:149], v207 offset1:8\n\t" \
  "s_waitcnt lgkmcnt(0)\n\t" \
  "v_or_b32 v148, v148, v149\n\t" \
  "v_cmp_ne_u32 vcc, 0, v148\n\t" \
  "s_cbranch_vccnz 12f\n\t" \
  "s_and_b32 s58, s58, 4\n\t" \
  "25:\n\t" \
  "s_add_i32 s82, s40, 2\n\t" \
  "s_min_i32 s83, s82, 63\n\t" \
  "s_mov_b32 exec_hi, 0\n\t" \
  "v_cmp_gt_i32 vcc, s83, v186\n\t" \
  "s_mov_b64 exec, -1\n\t" \
  "s_and_b32 s81, s82, 15\n\t" \
  "s_lshl_b32 s81, s81, 22\n\t" \
  "s_add_i32 s81, s81, s51\n\t" \
  "s_lshr_b32 s80, s81, 21\n\t" \
  "s_bitcmp0_b64 %[sel], s80\n\t" \
  "s_cselect_b32 s80, 0x2000, 0\n\t" \
  "s_add_i32 s81, s81, s80\n\t" \
  "s_cbranch_vccnz 27f\n\t" \
  "28:\n\t" \
  "s_cmp_ge_i32 s40, s53\n\t" \
  "s_cbranch_scc1 37f\n\t" \
  "buffer_load_dwordx4 v[0:3], v210, %[ru], s81 offen offset:0 sc1\n\t" \
  "buffer_load_dwordx4 v[4:7], v210, %[ru], s81 offen offset:1024 sc1\n\t" \
  "s_add_i32 s81, s81, 0x20000\n\t" \
  "buffer_load_dwordx4 v[8:11], v210, %[ru], s81 offen offset:0 sc1\n\t" \
  "buffer_load_dwordx4 v[12:15], v210, %[ru], s81 offen offset:1024 sc1\n\t" \
  "s_add_i32 s81, s81, 0x20000\n\t" \
  "buffer_load_dwordx4 v[16:19], v210, %[ru], s81 offen offset:0 sc1\n\t" \
  "buffer_load_dwordx4 v[20:23], v210, %[ru], s81 offen offset:1024 sc1\n\t" \
  "s_add_i32 s81, s81, 0x20000\n\t" \
  "buffer_load_dwordx4 v[24:27], v210, %[ru], s81 offen offset:0 sc1\n\t" \
  "buffer_load_dwordx4 v[28:31], v210, %[ru], s81 offen offset:1024 sc1\n\t" \
  "s_add_i32 s81, s81, 0x20000\n\t" \
  "s_bitcmp1_b32 s58, 4\n\t" \
  "s_cbranch_scc1 29f\n\t" \
  "s_sub_i32 s80, s56, s41\n\t" \
  "s_mul_hi_u32 s80, s80, s45\n\t" \
  "s_lshl_b32 s80, s80, 1\n\t" \
  "s_sub_i32 s80, s80, s57\n\t" \
  "s_min_u32 s47, s47, s46\n\t" \
  "s_min_u32 s47, s47, s80\n\t" \
  "v_mov_b32 v188, s47\n\t" \
  "s_mov_b32 s47, s46\n\t" \
  "s_mov_b32 s46, s80\n\t" \
  "buffer_store_dword v188, v202, %[rh], s52 offen sc1\n\t" \
  "v_mov_b64 v[188:189], -1\n\t" \
  "v_mov_b64 v[190:191], -1\n\t" \
  "v_mov_b32 v187, s40\n\t" \
  "s_add_i32 s80, s40, -1\n\t" \
  "s_and_b32 s80, s80, 7\n\t" \
  "s_lshl_b32 s80, s80, 15\n\t" \
  "s_add_i32 s80, s80, s49\n\t" \
  "buffer_store_dwordx4 v[188:191], v208, %[rh], s80 offen sc1\n\t" \
  "buffer_store_dword v187, v209, %[ru], 0 offen sc1\n\t" \
  "ds_write_b128 v197, v[72:75] offset:16384\n\t" \
  "ds_write_b128 v197, v[76:79] offset:17408\n\t" \
  "ds_write_b128 v197, v[80:83] offset:18432\n\t" \
  "ds_write_b128 v197, v[84:87] offset:19456\n\t" \
  "ds_write_b128 v197, v[88:91] offset:20480\n\t" \
  "ds_write_b128 v197, v[92:95] offset:21504\n\t" \
  "ds_write_b128 v197, v[96:99] offset:22528\n\t" \
  "ds_write_b128 v197, v[100:103] offset:23552\n\t" \
  "29:\n\t" \
  "s_and_b32 s58, s58, 36\n\t" \
  "buffer_load_dwordx4 v[32:35], v210, %[ru], s81 offen offset:0 sc1\n\t" \
  "buffer_load_dwordx4 v[36:39], v210, %[ru], s81 offen offset:1024 sc1\n\t" \
  "s_add_i32 s81, s81, 0x20000\n\t" \
  "buffer_load_dwordx4 v[40:43], v210, %[ru], s81 offen offset:0 sc1\n\t" \
  "buffer_load_dwordx4 v[44:47], v210, %[ru], s81 offen offset:1024 sc1\n\t" \
  "s_add_i32 s81, s81, 0x20000\n\t" \
  "buffer_load_dwordx4 v[48:51], v210, %[ru], s81 offen offset:0 sc1\n\t" \
  "buffer_load_dwordx4 v[52:55], v210, %[ru], s81 offen offset:1024 sc1\n\t" \
  "s_add_i32 s81, s81, 0x20000\n\t" \
  "buffer_load_dwordx4 v[56:59], v210, %[ru], s81 offen offset:0 sc1\n\t" \
  "buffer_load_dwordx4 v[60:63], v210, %[ru], s81 offen offset:1024 sc1\n\t" \
  "s_add_i32 s81, s81, 0x20000\n\t" \
  "buffer_load_dwordx4 v[64:67], v210, %[ru], s81 offen offset:0 sc1\n\t" \
  "buffer_load_dwordx4 v[68:71], v210, %[ru], s81 offen offset:1024 sc1\n\t" \
  "s_add_i32 s81, s81, 0x20000\n\t" \
  "buffer_load_dwordx4 v[104:107], v210, %[ru], s81 offen offset:0 sc1\n\t" \
  "buffer_load_dwordx4 v[108:111], v210, %[ru], s81 offen offset:1024 sc1\n\t" \
  "s_add_i32 s81, s81, 0x20000\n\t" \
  "ds_read_b128 v[160:163], v193 offset:0\n\t" \
  "ds_read_b128 v[164:167], v193 offset:256\n\t" \
  "ds_read_b128 v[168:171], v193 offset:2048\n\t" \
  "ds_read_b128 v[172:175], v193 offset:2304\n\t" \
  "ds_read_b128 v[176:179], v193 offset:4096\n\t" \
  "ds_read_b128 v[180:183], v193 offset:4352\n\t" \
  "s_waitcnt lgkmcnt(2)\n\t" \
  "v_pk_add_f32 v[160:161], v[160:161], v[168:169]\n\t" \
  "v_pk_add_f32 v[162:163], v[162:163], v[170:171]\n\t" \
  "v_pk_add_f32 v[164:165], v[164:165], v[172:173]\n\t" \
  "v_pk_add_f32 v[166:167], v[166:167], v[174:175]\n\t" \
  "s_waitcnt lgkmcnt(0)\n\t" \
  "v_pk_add_f32 v[160:161], v[160:161], v[176:177]\n\t" \
  "v_pk_add_f32 v[162:163], v[162:163], v[178:179]\n\t" \
  "v_pk_add_f32 v[164:165], v[164:165], v[180:181]\n\t" \
  "v_pk_add_f32 v[166:167], v[166:167], v[182:183]\n\t" \
  "v_cvt_pk_bf16_f32 v160, v160, v161\n\t" \
  "v_cvt_pk_bf16_f32 v161, v162, v163\n\t" \
  "v_cvt_pk_bf16_f32 v162, v164, v165\n\t" \
  "v_cvt_pk_bf16_f32 v163, v166, v167\n\t" \
  "buffer_load_dwordx4 v[112:115], v210, %[ru], s81 offen offset:0 sc1\n\t" \
  "buffer_load_dwordx4 v[116:119], v210, %[ru], s81 offen offset:1024 sc1\n\t" \
  "s_add_i32 s81, s81, 0x20000\n\t" \
  "buffer_load_dwordx4 v[120:123], v210, %[ru], s81 offen offset:0 sc1\n\t" \
  "buffer_load_dwordx4 v[124:127], v210, %[ru], s81 offen offset:1024 sc1\n\t" \
  "s_add_i32 s81, s81, 0x20000\n\t" \
  "s_waitcnt vmcnt(20)\n\t" \
  "v_swap_b32 v1, v8\n\t" \
  "v_swap_b32 v3, v10\n\t" \
  "v_swap_b32 v5, v12\n\t" \
  "v_swap_b32 v7, v14\n\t" \
  "ds_write_b128 v204, v[0:3] offset:0\n\t" \
  "ds_write_b128 v204, v[8:11] offset:1024\n\t" \
  "ds_write_b128 v204, v[4:7] offset:2048\n\t" \
  "ds_write_b128 v204, v[12:15] offset:3072\n\t" \
  "buffer_load_dwordx4 v[128:131], v210, %[ru], s81 offen offset:0 sc1\n\t" \
  "buffer_load_dwordx4 v[132:135], v210, %[ru], s81 offen offset:1024 sc1\n\t" \
  "s_add_i32 s81, s81, 0x20000\n\t" \
  "buffer_load_dwordx4 v[136:139], v210, %[ru], s81 offen offset:0 sc1\n\t" \
  "buffer_load_dwordx4 v[140:143], v210, %[ru], s81 offen offset:1024 sc1\n\t" \
  "s_add_i32 s81, s81, 0x20000\n\t" \
  "s_waitcnt vmcnt(20)\n\t" \
  "v_swap_b32 v17, v24\n\t" \
  "v_swap_b32 v19, v26\n\t" \
  "v_swap_b32 v21, v28\n\t" \
  "v_swap_b32 v23, v30\n\t" \
  "ds_write_b128 v204, v[16:19] offset:4096\n\t" \
  "ds_write_b128 v204, v[24:27] offset:5120\n\t" \
  "ds_write_b128 v204, v[20:23] offset:6144\n\t" \
  "ds_write_b128 v204, v[28:31] offset:7168\n\t" \
  "buffer_load_dwordx4 v[144:147], v210, %[ru], s81 offen offset:0 sc1\n\t" \
  "buffer_load_dwordx4 v[148:151], v210, %[ru], s81 offen offset:1024 sc1\n\t" \
  "s_add_i32 s81, s81, 0x20000\n\t" \
  "buffer_load_dwordx4 v[152:155], v210, %[ru], s81 offen offset:0 sc1\n\t" \
  "buffer_load_dwordx4 v[156:159], v210, %[ru], s81 offen offset:1024 sc1\n\t" \
  "s_add_i32 s81, s81, 0x20000\n\t" \
  "s_barrier\n\t" \
  "s_waitcnt vmcnt(20)\n\t" \
  "v_swap_b32 v33, v40\n\t" \
  "v_swap_b32 v35, v42\n\t" \
  "v_swap_b32 v37, v44\n\t" \
  "v_swap_b32 v39, v46\n\t" \
  "ds_write_b128 v204, v[32:35] offset:8192\n\t" \
  "ds_write_b128 v204, v[40:43] offset:9216\n\t" \
  "ds_write_b128 v204, v[36:39] offset:10240\n\t" \
  "ds_write_b128 v204, v[44:47] offset:11264\n\t" \
  "s_waitcnt vmcnt(16)\n\t" \
  "v_swap_b32 v49, v56\n\t" \
  "v_swap_b32 v51, v58\n\t" \
  "v_swap_b32 v53, v60\n\t" \
  "v_swap_b32 v55, v62\n\t" \
  "ds_write_b128 v204, v[48:51] offset:12288\n\t" \
  "ds_write_b128 v204, v[56:59] offset:13312\n\t" \
  "ds_write_b128 v204, v[52:55] offset:14336\n\t" \
  "ds_write_b128 v204, v[60:63] offset:15360\n\t" \
  "s_waitcnt vmcnt(12)\n\t" \
  "v_swap_b32 v65, v104\n\t" \
  "v_swap_b32 v67, v106\n\t" \
  "v_swap_b32 v69, v108\n\t" \
  "v_swap_b32 v71, v110\n\t" \
  "ds_write_b128 v204, v[64:67] offset:16384\n\t" \
  "ds_write_b128 v204, v[104:107] offset:17408\n\t" \
  "ds_write_b128 v204, v[68:71] offset:18432\n\t" \
  "ds_write_b128 v204, v[108:111] offset:19456\n\t" \
  "s_waitcnt vmcnt(8)\n\t" \
  "v_swap_b32 v113, v120\n\t" \
  "v_swap_b32 v115, v122\n\t" \
  "v_swap_b32 v117, v124\n\t" \
  "v_swap_b32 v119, v126\n\t" \
  "ds_write_b128 v204, v[112:115] offset:20480\n\t" \
  "ds_write_b128 v204, v[120:123] offset:21504\n\t" \
  "ds_write_b128 v204, v[116:119] offset:22528\n\t" \
  "ds_write_b128 v204, v[124:127] offset:23552\n\t" \
  "s_waitcnt vmcnt(4)\n\t" \
  "v_swap_b32 v129, v136\n\t" \
  "v_swap_b32 v131, v138\n\t" \
  "v_swap_b32 v133, v140\n\t" \
  "v_swap_b32 v135, v142\n\t" \
  "ds_write_b128 v204, v[128:131] offset:24576\n\t" \
  "ds_write_b128 v204, v[136:139] offset:25600\n\t" \
  "ds_write_b128 v204, v[132:135] offset:26624\n\t" \
  "ds_write_b128 v204, v[140:143] offset:27648\n\t" \
  "s_waitcnt vmcnt(0)\n\t" \
  "v_swap_b32 v145, v152\n\t" \
  "v_swap_b32 v147, v154\n\t" \
  "v_swap_b32 v149, v156\n\t" \
  "v_swap_b32 v151, v158\n\t" \
  "ds_write_b128 v204, v[144:147] offset:28672\n\t" \
  "ds_write_b128 v204, v[152:155] offset:29696\n\t" \
  "ds_write_b128 v204, v[148:151] offset:30720\n\t" \
  "ds_write_b128 v204, v[156:159] offset:31744\n\t" \
  "38:\n\t" \
  "s_waitcnt lgkmcnt(0)\n\t" \
  "s_barrier\n\t" \
  "s_add_i32 s85, s40, 1\n\t" \
  "s_and_b32 s76, s40, 3\n\t" \
  "s_lshl_b32 s76, s76, 18\n\t" \
  "buffer_load_dwordx4 a[0:3], v211, %[rh], s76 offen offset:0 sc1\n\t" \
  "buffer_load_dwordx4 a[4:7], v211, %[rh], s76 offen offset:1024 sc1\n\t" \
  "buffer_load_dwordx4 a[8:11], v212, %[rh], s76 offen offset:0 sc1\n\t" \
  "buffer_load_dwordx4 a[12:15], v212, %[rh], s76 offen offset:1024 sc1\n\t" \
  "buffer_load_dwordx4 a[16:19], v213, %[rh], s76 offen offset:0 sc1\n\t" \
  "buffer_load_dwordx4 a[20:23], v213, %[rh], s76 offen offset:1024 sc1\n\t" \
  "buffer_load_dwordx4 a[24:27], v214, %[rh], s76 offen offset:0 sc1\n\t" \
  "buffer_load_dwordx4 a[28:31], v214, %[rh], s76 offen offset:1024 sc1\n\t" \
  "s_and_b32 s82, s85, 2\n\t" \
  "s_lshl_b32 s82, s82, 13\n\t" \
  "s_and_b32 s80, s85, 1\n\t" \
  "s_lshl_b32 s80, s80, 12\n\t" \
  "s_add_i32 s82, s82, s80\n\t" \
  "s_add_i32 s82, s82, 0x84000\n\t" \
  "s_mov_b32 exec_hi, 0\n\t" \
  "buffer_load_dword v184, v200, %[rh], s82 offen sc1\n\t" \
  "buffer_load_dword v186, v203, %[ru], 0 offen sc1\n\t" \
  "s_mov_b64 exec, -1\n\t" \
  "s_add_i32 s80, s40, -2\n\t" \
  "s_lshl_b32 s80, s80, 17\n\t" \
  "s_min_u32 s80, s80, 0x70000000\n\t" \
  "buffer_store_dwordx4 v[160:163], v196, %[ro], s80 offen nt\n\t" \
  "ds_read_b128 a[32:35], v198 offset:0\n\t" \
  "ds_read_b128 a[36:39], v198 offset:1024\n\t" \
  "ds_read_b128 a[40:43], v198 offset:2048\n\t" \
  "ds_read_b128 a[44:47], v198 offset:3072\n\t" \
  "ds_read_b128 a[48:51], v198 offset:4096\n\t" \
  "ds_read_b128 a[52:55], v198 offset:5120\n\t" \
  "ds_read_b128 a[56:59], v198 offset:6144\n\t" \
  "ds_read_b128 a[60:63], v198 offset:7168\n\t" \
  "ds_read_b128 a[64:67], v198 offset:8192\n\t" \
  "ds_read_b128 a[68:71], v198 offset:9216\n\t" \
  "ds_read_b128 a[72:75], v198 offset:10240\n\t" \
  "ds_read_b128 a[76:79], v198 offset:11264\n\t" \
  "ds_read_b128 a[80:83], v198 offset:12288\n\t" \
  "ds_read_b128 a[84:87], v198 offset:13312\n\t" \
  "ds_read_b128 a[88:91], v198 offset:14336\n\t" \
  "ds_read_b128 a[92:95], v198 offset:15360\n\t" \
  "s_waitcnt vmcnt(3) lgkmcnt(0)\n\t" \
  "31:\n\t" \
  "v_mfma_f32_16x16x32_bf16 v[168:171], a[32:35], a[0:3], 0\n\t" \
  "v_mfma_f32_16x16x32_bf16 v[172:175], a[64:67], a[0:3], 0\n\t" \
  "v_mfma_f32_16x16x32_bf16 v[168:171], a[36:39], a[4:7], v[168:171]\n\t" \
  "v_mfma_f32_16x16x32_bf16 v[172:175], a[68:71], a[4:7], v[172:175]\n\t" \
  "v_mfma_f32_16x16x32_bf16 v[168:171], a[40:43], a[8:11], v[168:171]\n\t" \
  "v_mfma_f32_16x16x32_bf16 v[172:175], a[72:75], a[8:11], v[172:175]\n\t" \
  "v_mfma_f32_16x16x32_bf16 v[168:171], a[44:47], a[12:15], v[168:171]\n\t" \
  "v_mfma_f32_16x16x32_bf16 v[172:175], a[76:79], a[12:15], v[172:175]\n\t" \
  "v_mfma_f32_16x16x32_bf16 v[168:171], a[48:51], a[16:19], v[168:171]\n\t" \
  "v_mfma_f32_16x16x32_bf16 v[172:175], a[80:83], a[16:19], v[172:175]\n\t" \
  "v_mfma_f32_16x16x32_bf16 v[168:171], a[52:55], a[20:23], v[168:171]\n\t" \
  "v_mfma_f32_16x16x32_bf16 v[172:175], a[84:87], a[20:23], v[172:175]\n\t" \
  "v_mfma_f32_16x16x32_bf16 v[168:171], a[56:59], a[24:27], v[168:171]\n\t" \
  "v_mfma_f32_16x16x32_bf16 v[172:175], a[88:91], a[24:27], v[172:175]\n\t" \
  "v_mfma_f32_16x16x32_bf16 v[168:171], a[60:63], a[28:31], v[168:171]\n\t" \
  "v_mfma_f32_16x16x32_bf16 v[172:175], a[92:95], a[28:31], v[172:175]\n\t" \
  "s_nop 7\n\t" \
  "s_add_i32 s40, s40, 2\n\t" \
  "v_cmp_u_f32 vcc, v168, v168\n\t" \
  "s_cbranch_vccnz 32f\n\t" \
  "33:\n\t" \
  "ds_write_b128 v199, v[168:171] offset:0\n\t" \
  "ds_write_b128 v199, v[172:175] offset:1024\n\t" \
  "s_mov_b32 s59, 0\n\t" \
  "s_and_b32 s58, s58, 4\n\t" \
  "s_cmp_lt_i32 s40, 64\n\t" \
  "s_cbranch_scc1 20b\n\t" \
  "s_waitcnt lgkmcnt(0)\n\t" \
  "s_barrier\n\t" \
  "ds_read_b128 v[160:163], v193 offset:0\n\t" \
  "ds_read_b128 v[164:167], v193 offset:256\n\t" \
  "ds_read_b128 v[168:171], v193 offset:2048\n\t" \
  "ds_read_b128 v[172:175], v193 offset:2304\n\t" \
  "ds_read_b128 v[176:179], v193 offset:4096\n\t" \
  "ds_read_b128 v[180:183], v193 offset:4352\n\t" \
  "s_waitcnt lgkmcnt(2)\n\t" \
  "v_pk_add_f32 v[160:161], v[160:161], v[168:169]\n\t" \
  "v_pk_add_f32 v[162:163], v[162:163], v[170:171]\n\t" \
  "v_pk_add_f32 v[164:165], v[164:165], v[172:173]\n\t" \
  "v_pk_add_f32 v[166:167], v[166:167], v[174:175]\n\t" \
  "s_waitcnt lgkmcnt(0)\n\t" \
  "v_pk_add_f32 v[160:161], v[160:161], v[176:177]\n\t" \
  "v_pk_add_f32 v[162:163], v[162:163], v[178:179]\n\t" \
  "v_pk_add_f32 v[164:165], v[164:165], v[180:181]\n\t" \
  "v_pk_add_f32 v[166:167], v[166:167], v[182:183]\n\t" \
  "v_cvt_pk_bf16_f32 v160, v160, v161\n\t" \
  "v_cvt_pk_bf16_f32 v161, v162, v163\n\t" \
  "v_cvt_pk_bf16_f32 v162, v164, v165\n\t" \
  "v_cvt_pk_bf16_f32 v163, v166, v167\n\t" \
  "s_add_i32 s80, s40, -2\n\t" \
  "s_lshl_b32 s80, s80, 17\n\t" \
  "s_min_u32 s80, s80, 0x70000000\n\t" \
  "buffer_store_dwordx4 v[160:163], v196, %[ro], s80 offen nt\n\t" \
  "s_mov_b32 s58, 64\n\t" \
  "s_branch 7f\n\t" \
  "22:\n\t" \
  "s_mov_b32 s81, 0\n\t" \
  "s_cmp_eq_u32 s40, 0\n\t" \
  "s_cbranch_scc1 24b\n\t" \
  "s_bitcmp1_b32 s58, 2\n\t" \
  "s_cbranch_scc1 24b\n\t" \
  "s_mov_b64 exec, -1\n\t" \
  "s_sleep 10\n\t" \
  "s_add_u32 s59, s59, 0x10000\n\t" \
  "s_cmp_lt_u32 s59, 0x100000\n\t" \
  "s_cbranch_scc1 19b\n\t" \
  "s_or_b32 s58, s58, 1\n\t" \
  "s_branch 7f\n\t" \
  "14:\n\t" \
  "v_mov_b32 v184, 0\n\t" \
  "s_bitcmp1_b32 s58, 2\n\t" \
  "s_nop 0\n\t" \
  "s_cbranch_scc1 15b\n\t" \
  "s_mov_b32 s84, 950\n\t" \
  "s_add_u32 s59, s59, 0x10000\n\t" \
  "s_cmp_lt_u32 s59, 0x100000\n\t" \
  "s_mov_b64 exec, vcc\n\t" \
  "buffer_load_dword v187, v200, %[rh], s78 offen sc1\n\t" \
  "s_mov_b32 exec_lo, -1\n\t" \
  "s_cbranch_scc1 13b\n\t" \
  "s_mov_b64 exec, -1\n\t" \
  "s_or_b32 s58, s58, 1\n\t" \
  "s_branch 7f\n\t" \
  "27:\n\t" \
  "s_bitcmp1_b32 s58, 2\n\t" \
  "s_cbranch_scc1 28b\n\t" \
  "s_bitcmp1_b32 s58, 4\n\t" \
  "s_cbranch_scc1 23f\n\t" \
  "s_sub_i32 s80, s56, s41\n\t" \
  "s_mul_hi_u32 s80, s80, s45\n\t" \
  "s_lshl_b32 s80, s80, 1\n\t" \
  "s_sub_i32 s80, s80, s57\n\t" \
  "s_min_u32 s47, s47, s46\n\t" \
  "s_min_u32 s47, s47, s80\n\t" \
  "v_mov_b32 v188, s47\n\t" \
  "s_mov_b32 s47, s46\n\t" \
  "s_mov_b32 s46, s80\n\t" \
  "buffer_store_dword v188, v202, %[rh], s52 offen sc1\n\t" \
  "v_mov_b64 v[188:189], -1\n\t" \
  "v_mov_b64 v[190:191], -1\n\t" \
  "v_mov_b32 v187, s40\n\t" \
  "s_add_i32 s80, s40, -1\n\t" \
  "s_and_b32 s80, s80, 7\n\t" \
  "s_lshl_b32 s80, s80, 15\n\t" \
  "s_add_i32 s80, s80, s49\n\t" \
  "buffer_store_dwordx4 v[188:191], v208, %[rh], s80 offen sc1\n\t" \
  "buffer_store_dword v187, v209, %[ru], 0 offen sc1\n\t" \
  "ds_write_b128 v197, v[72:75] offset:16384\n\t" \
  "ds_write_b128 v197, v[76:79] offset:17408\n\t" \
  "ds_write_b128 v197, v[80:83] offset:18432\n\t" \
  "ds_write_b128 v197, v[84:87] offset:19456\n\t" \
  "ds_write_b128 v197, v[88:91] offset:20480\n\t" \
  "ds_write_b128 v197, v[92:95] offset:21504\n\t" \
  "ds_write_b128 v197, v[96:99] offset:22528\n\t" \
  "ds_write_b128 v197, v[100:103] offset:23552\n\t" \
  "s_or_b32 s58, s58, 16\n\t" \
  "23:\n\t" \
  "s_add_u32 s59, s59, 0x1000000\n\t" \
  "s_cmp_lt_u32 s59, 0x10000000\n\t" \
  "s_cbranch_scc1 26f\n\t" \
  "s_or_b32 s58, s58, 1\n\t" \
  "s_branch 7f\n\t" \
  "26:\n\t" \
  "buffer_load_dword v186, v203, %[ru], 0 offen sc1\n\t" \
  "s_waitcnt vmcnt(0)\n\t" \
  "s_branch 25b\n\t" \
  "37:\n\t" \
  "s_barrier\n\t" \
  "s_bitcmp1_b32 s58, 4\n\t" \
  "s_cbranch_scc1 29f\n\t" \
  "s_sub_i32 s80, s56, s41\n\t" \
  "s_mul_hi_u32 s80, s80, s45\n\t" \
  "s_lshl_b32 s80, s80, 1\n\t" \
  "s_sub_i32 s80, s80, s57\n\t" \
  "s_min_u32 s47, s47, s46\n\t" \
  "s_min_u32 s47, s47, s80\n\t" \
  "v_mov_b32 v188, s47\n\t" \
  "s_mov_b32 s47, s46\n\t" \
  "s_mov_b32 s46, s80\n\t" \
  "buffer_store_dword v188, v202, %[rh], s52 offen sc1\n\t" \
  "v_mov_b64 v[188:189], -1\n\t" \
  "v_mov_b64 v[190:191], -1\n\t" \
  "v_mov_b32 v187, s40\n\t" \
  "s_add_i32 s80, s40, -1\n\t" \
  "s_and_b32 s80, s80, 7\n\t" \
  "s_lshl_b32 s80, s80, 15\n\t" \
  "s_add_i32 s80, s80, s49\n\t" \
  "buffer_store_dwordx4 v[188:191], v208, %[rh], s80 offen sc1\n\t" \
  "buffer_store_dword v187, v209, %[ru], 0 offen sc1\n\t" \
  "ds_write_b128 v197, v[72:75] offset:16384\n\t" \
  "ds_write_b128 v197, v[76:79] offset:17408\n\t" \
  "ds_write_b128 v197, v[80:83] offset:18432\n\t" \
  "ds_write_b128 v197, v[84:87] offset:19456\n\t" \
  "ds_write_b128 v197, v[88:91] offset:20480\n\t" \
  "ds_write_b128 v197, v[92:95] offset:21504\n\t" \
  "ds_write_b128 v197, v[96:99] offset:22528\n\t" \
  "ds_write_b128 v197, v[100:103] offset:23552\n\t" \
  "29:\n\t" \
  "s_and_b32 s58, s58, 36\n\t" \
  "ds_read_b128 v[160:163], v193 offset:0\n\t" \
  "ds_read_b128 v[164:167], v193 offset:256\n\t" \
  "ds_read_b128 v[168:171], v193 offset:2048\n\t" \
  "ds_read_b128 v[172:175], v193 offset:2304\n\t" \
  "ds_read_b128 v[176:179], v193 offset:4096\n\t" \
  "ds_read_b128 v[180:183], v193 offset:4352\n\t" \
  "s_waitcnt lgkmcnt(2)\n\t" \
  "v_pk_add_f32 v[160:161], v[160:161], v[168:169]\n\t" \
  "v_pk_add_f32 v[162:163], v[162:163], v[170:171]\n\t" \
  "v_pk_add_f32 v[164:165], v[164:165], v[172:173]\n\t" \
  "v_pk_add_f32 v[166:167], v[166:167], v[174:175]\n\t" \
  "s_waitcnt lgkmcnt(0)\n\t" \
  "v_pk_add_f32 v[160:161], v[160:161], v[176:177]\n\t" \
  "v_pk_add_f32 v[162:163], v[162:163], v[178:179]\n\t" \
  "v_pk_add_f32 v[164:165], v[164:165], v[180:181]\n\t" \
  "v_pk_add_f32 v[166:167], v[166:167], v[182:183]\n\t" \
  "v_cvt_pk_bf16_f32 v160, v160, v161\n\t" \
  "v_cvt_pk_bf16_f32 v161, v162, v163\n\t" \
  "v_cvt_pk_bf16_f32 v162, v164, v165\n\t" \
  "v_cvt_pk_bf16_f32 v163, v166, v167\n\t" \
  "s_branch 38b\n\t" \
  "8:\n\t" \
  "s_mov_b64 vcc, -1\n\t" \
  "s_and_b32 s80, s58, 6\n\t" \
  "s_cbranch_scc1 6b\n\t" \
  "v_mov_b32 v151, 1\n\t" \
  "s_branch 5b\n\t" \
  "32:\n\t" \
  "s_and_b32 s80, s58, 36\n\t" \
  "s_cbranch_scc1 33b\n\t" \
  "s_add_i32 s40, s40, -2\n\t" \
  "34:\n\t" \
  "s_or_b32 s59, s59, 128\n\t" \
  "s_and_b32 s76, s40, 3\n\t" \
  "s_lshl_b32 s76, s76, 18\n\t" \
  "35:\n\t" \
  "buffer_load_dwordx4 a[0:3], v211, %[rh], s76 offen offset:0 sc1\n\t" \
  "buffer_load_dwordx4 a[4:7], v211, %[rh], s76 offen offset:1024 sc1\n\t" \
  "buffer_load_dwordx4 a[8:11], v212, %[rh], s76 offen offset:0 sc1\n\t" \
  "buffer_load_dwordx4 a[12:15], v212, %[rh], s76 offen offset:1024 sc1\n\t" \
  "buffer_load_dwordx4 a[16:19], v213, %[rh], s76 offen offset:0 sc1\n\t" \
  "buffer_load_dwordx4 a[20:23], v213, %[rh], s76 offen offset:1024 sc1\n\t" \
  "buffer_load_dwordx4 a[24:27], v214, %[rh], s76 offen offset:0 sc1\n\t" \
  "buffer_load_dwordx4 a[28:31], v214, %[rh], s76 offen offset:1024 sc1\n\t" \
  "s_add_i32 s85, s40, 1\n\t" \
  "s_and_b32 s82, s85, 2\n\t" \
  "s_lshl_b32 s82, s82, 13\n\t" \
  "s_and_b32 s80, s85, 1\n\t" \
  "s_lshl_b32 s80, s80, 12\n\t" \
  "s_add_i32 s82, s82, s80\n\t" \
  "s_add_i32 s82, s82, 0x84000\n\t" \
  "s_mov_b32 exec_hi, 0\n\t" \
  "buffer_load_dword v184, v200, %[rh], s82 offen sc1\n\t" \
  "buffer_load_dword v186, v203, %[ru], 0 offen sc1\n\t" \
  "s_mov_b64 exec, -1\n\t" \
  "s_waitcnt vmcnt(0)\n\t" \
  "s_bitcmp1_b32 s58, 2\n\t" \
  "s_cbranch_scc1 36f\n\t" \
  "v_accvgpr_read_b32 v0, a0\n\t" \
  "v_accvgpr_read_b32 v1, a1\n\t" \
  "v_accvgpr_read_b32 v2, a2\n\t" \
  "v_accvgpr_read_b32 v3, a3\n\t" \
  "v_accvgpr_read_b32 v4, a4\n\t" \
  "v_accvgpr_read_b32 v5, a5\n\t" \
  "v_accvgpr_read_b32 v6, a6\n\t" \
  "v_accvgpr_read_b32 v7, a7\n\t" \
  "v_accvgpr_read_b32 v8, a8\n\t" \
  "v_accvgpr_read_b32 v9, a9\n\t" \
  "v_accvgpr_read_b32 v10, a10\n\t" \
  "v_accvgpr_read_b32 v11, a11\n\t" \
  "v_accvgpr_read_b32 v12, a12\n\t" \
  "v_accvgpr_read_b32 v13, a13\n\t" \
  "v_accvgpr_read_b32 v14, a14\n\t" \
  "v_accvgpr_read_b32 v15, a15\n\t" \
  "v_accvgpr_read_b32 v16, a16\n\t" \
  "v_accvgpr_read_b32 v17, a17\n\t" \
  "v_accvgpr_read_b32 v18, a18\n\t" \
  "v_accvgpr_read_b32 v19, a19\n\t" \
  "v_accvgpr_read_b32 v20, a20\n\t" \
  "v_accvgpr_read_b32 v21, a21\n\t" \
  "v_accvgpr_read_b32 v22, a22\n\t" \
  "v_accvgpr_read_b32 v23, a23\n\t" \
  "v_accvgpr_read_b32 v24, a24\n\t" \
  "v_accvgpr_read_b32 v25, a25\n\t" \
  "v_accvgpr_read_b32 v26, a26\n\t" \
  "v_accvgpr_read_b32 v27, a27\n\t" \
  "v_accvgpr_read_b32 v28, a28\n\t" \
  "v_accvgpr_read_b32 v29, a29\n\t" \
  "v_accvgpr_read_b32 v30, a30\n\t" \
  "v_accvgpr_read_b32 v31, a31\n\t" \
  "v_max_u32 v148, v0, v1\n\t" \
  "v_max3_u32 v148, v148, v2, v3\n\t" \
  "v_max3_u32 v148, v148, v4, v5\n\t" \
  "v_max3_u32 v148, v148, v6, v7\n\t" \
  "v_max3_u32 v148, v148, v8, v9\n\t" \
  "v_max3_u32 v148, v148, v10, v11\n\t" \
  "v_max3_u32 v148, v148, v12, v13\n\t" \
  "v_max3_u32 v148, v148, v14, v15\n\t" \
  "v_max3_u32 v148, v148, v16, v17\n\t" \
  "v_max3_u32 v148, v148, v18, v19\n\t" \
  "v_max3_u32 v148, v148, v20, v21\n\t" \
  "v_max3_u32 v148, v148, v22, v23\n\t" \
  "v_max3_u32 v148, v148, v24, v25\n\t" \
  "v_max3_u32 v148, v148, v26, v27\n\t" \
  "v_max3_u32 v148, v148, v28, v29\n\t" \
  "v_max3_u32 v148, v148, v30, v31\n\t" \
  "v_cmp_eq_u32 vcc, -1, v148\n\t" \
  "s_cbranch_vccz 36f\n\t" \
  "s_add_u32 s59, s59, 0x1000000\n\t" \
  "s_cmp_lt_u32 s59, 0x10000000\n\t" \
  "s_cbranch_scc1 35b\n\t" \
  "s_or_b32 s58, s58, 9\n\t" \
  "s_branch 7f\n\t" \
  "36:\n\t" \
  "s_and_b32 s58, s58, 4\n\t" \
  "s_or_b32 s58, s58, 32\n\t" \
  "s_branch 31b\n\t" \
  "18:\n\t" \
  "v_mov_b32 v186, -1\n\t" \
  "s_and_b32 s80, s40, 3\n\t" \
  "s_mul_i32 s77, s80, 0xc000\n\t" \
  "s_add_i32 s77, s77, 0x40000\n\t" \
  "s_lshl_b32 s78, s80, 14\n\t" \
  "s_add_i32 s78, s78, 0x70000\n\t" \
  "s_mov_b32 s57, 0\n\t" \
  "s_add_i32 s96, s77, 0x1000\n\t" \
  "s_lshl_b32 s88, s80, 20\n\t" \
  "s_add_i32 s88, s88, s48\n\t" \
  "s_lshr_b32 s80, s88, 21\n\t" \
  "s_bitcmp1_b64 %[sel], s80\n\t" \
  "s_cselect_b32 s80, 0x2000, 0\n\t" \
  "s_add_i32 s88, s88, s80\n\t" \
  "s_xor_b32 s89, s88, 0x800\n\t" \
  "s_xor_b32 s90, s88, 0x1000\n\t" \
  "s_xor_b32 s91, s88, 0x1800\n\t" \
  "s_add_i32 s92, s77, s50\n\t" \
  "s_xor_b32 s93, s92, 0x100\n\t" \
  "s_xor_b32 s94, s92, 0x200\n\t" \
  "s_xor_b32 s95, s92, 0x300\n\t" \
  "s_add_i32 s97, s40, 1\n\t" \
  "s_and_b32 s97, s97, 7\n\t" \
  "s_lshl_b32 s97, s97, 15\n\t" \
  "s_add_i32 s97, s97, s49\n\t" \
  "12:\n\t" \
  "s_or_b32 s58, s58, 3\n\t" \
  "11:\n\t" \
  "buffer_load_dwordx4 v[72:75], v192, %[ru], s88 offen offset:0 sc1\n\t" \
  "buffer_load_dwordx4 v[76:79], v192, %[ru], s88 offen offset:1024 sc1\n\t" \
  "buffer_load_dwordx4 v[80:83], v192, %[ru], s89 offen offset:0 sc1\n\t" \
  "buffer_load_dwordx4 v[84:87], v192, %[ru], s89 offen offset:1024 sc1\n\t" \
  "buffer_load_dwordx4 v[88:91], v192, %[ru], s90 offen offset:0 sc1\n\t" \
  "buffer_load_dwordx4 v[92:95], v192, %[ru], s90 offen offset:1024 sc1\n\t" \
  "buffer_load_dwordx4 v[96:99], v192, %[ru], s91 offen offset:0 sc1\n\t" \
  "buffer_load_dwordx4 v[100:103], v192, %[ru], s91 offen offset:1024 sc1\n\t" \
  "buffer_load_dwordx4 v[104:107], v194, %[rh], s92 offen sc1\n\t" \
  "buffer_load_dwordx4 v[108:111], v194, %[rh], s93 offen sc1\n\t" \
  "buffer_load_dwordx4 v[112:115], v194, %[rh], s94 offen sc1\n\t" \
  "buffer_load_dwordx4 v[116:119], v194, %[rh], s95 offen sc1\n\t" \
  "s_waitcnt vmcnt(0)\n\t" \
  "s_add_u32 s59, s59, 1\n\t" \
  "s_bitcmp1_b32 s58, 2\n\t" \
  "s_cbranch_scc1 10b\n\t" \
  "v_max_u32 v148, v72, v73\n\t" \
  "v_max3_u32 v148, v148, v74, v75\n\t" \
  "v_max3_u32 v148, v148, v76, v77\n\t" \
  "v_max3_u32 v148, v148, v78, v79\n\t" \
  "v_max3_u32 v148, v148, v80, v81\n\t" \
  "v_max3_u32 v148, v148, v82, v83\n\t" \
  "v_max3_u32 v148, v148, v84, v85\n\t" \
  "v_max3_u32 v148, v148, v86, v87\n\t" \
  "v_max3_u32 v148, v148, v88, v89\n\t" \
  "v_max3_u32 v148, v148, v90, v91\n\t" \
  "v_max3_u32 v148, v148, v92, v93\n\t" \
  "v_max3_u32 v148, v148, v94, v95\n\t" \
  "v_max3_u32 v148, v148, v96, v97\n\t" \
  "v_max3_u32 v148, v148, v98, v99\n\t" \
  "v_max3_u32 v148, v148, v100, v101\n\t" \
  "v_max3_u32 v148, v148, v102, v103\n\t" \
  "v_max3_u32 v148, v148, v104, v105\n\t" \
  "v_max3_u32 v148, v148, v106, v107\n\t" \
  "v_max3_u32 v148, v148, v108, v109\n\t" \
  "v_max3_u32 v148, v148, v110, v111\n\t" \
  "v_max3_u32 v148, v148, v112, v113\n\t" \
  "v_max3_u32 v148, v148, v114, v115\n\t" \
  "v_max3_u32 v148, v148, v116, v117\n\t" \
  "v_max3_u32 v148, v148, v118, v119\n\t" \
  "v_cmp_eq_u32 vcc, -1, v148\n\t" \
  "s_cbranch_vccz 10b\n\t" \
  "s_bitcmp1_b32 s59, 6\n\t" \
  "s_cbranch_scc0 11b\n\t" \
  "7:\n\t" \
  "s_branch 92f\n\t" \
  "91:\n\t" \
  "s_mov_b32 s58, s42\n\t" \
  "s_mov_b32 s59, 0\n\t" \
  "s_bitcmp1_b32 s58, 4\n\t" \
  "s_cbranch_scc1 37f\n\t" \
  "s_bitcmp1_b32 s58, 3\n\t" \
  "s_cbranch_scc1 34f\n\t" \
  "s_bitcmp1_b32 s58, 1\n\t" \
  "s_cbranch_scc1 18f\n\t" \
  "17:\n\t" \
  "v_mov_b64 v[40:41], 0\n\t" \
  "v_mov_b64 v[42:43], 0\n\t" \
  "v_mov_b64 v[44:45], 0\n\t" \
  "v_mov_b64 v[46:47], 0\n\t" \
  "v_mov_b64 v[48:49], 0\n\t" \
  "v_mov_b64 v[50:51], 0\n\t" \
  "v_mov_b64 v[52:53], 0\n\t" \
  "v_mov_b64 v[54:55], 0\n\t" \
  "v_mov_b64 v[56:57], 0\n\t" \
  "v_mov_b64 v[58:59], 0\n\t" \
  "v_mov_b64 v[60:61], 0\n\t" \
  "v_mov_b64 v[62:63], 0\n\t" \
  "v_mov_b64 v[64:65], 0\n\t" \
  "v_mov_b64 v[66:67], 0\n\t" \
  "v_mov_b64 v[68:69], 0\n\t" \
  "v_mov_b64 v[70:71], 0\n\t" \
  "v_mov_b64 v[72:73], 0\n\t" \
  "v_mov_b64 v[74:75], 0\n\t" \
  "v_mov_b64 v[76:77], 0\n\t" \
  "v_mov_b64 v[78:79], 0\n\t" \
  "v_mov_b64 v[80:81], 0\n\t" \
  "v_mov_b64 v[82:83], 0\n\t" \
  "v_mov_b64 v[84:85], 0\n\t" \
  "v_mov_b64 v[86:87], 0\n\t" \
  "v_mov_b64 v[88:89], 0\n\t" \
  "v_mov_b64 v[90:91], 0\n\t" \
  "v_mov_b64 v[92:93], 0\n\t" \
  "v_mov_b64 v[94:95], 0\n\t" \
  "v_mov_b64 v[96:97], 0\n\t" \
  "v_mov_b64 v[98:99], 0\n\t" \
  "v_mov_b64 v[100:101], 0\n\t" \
  "v_mov_b64 v[102:103], 0\n\t" \
  "v_mov_b64 v[136:137], 0\n\t" \
  "v_mov_b64 v[138:139], 0\n\t" \
  "v_mov_b64 v[140:141], 0\n\t" \
  "v_mov_b64 v[142:143], 0\n\t" \
  "19:\n\t" \
  "s_add_i32 s85, s40, -1\n\t" \
  "s_and_b32 s82, s85, 2\n\t" \
  "s_lshl_b32 s82, s82, 13\n\t" \
  "s_and_b32 s80, s85, 1\n\t" \
  "s_lshl_b32 s80, s80, 12\n\t" \
  "s_add_i32 s82, s82, s80\n\t" \
  "s_add_i32 s82, s82, 0x84000\n\t" \
  "s_mov_b32 exec_hi, 0\n\t" \
  "buffer_load_dword v184, v200, %[rh], s82 offen sc1\n\t" \
  "buffer_load_dword v185, v200, %[rh], s52 offen sc1\n\t" \
  "s_mov_b64 exec, -1\n\t" \
  "20:\n\t" \
  "s_and_b32 s80, s40, 3\n\t" \
  "s_mul_i32 s77, s80, 0xc000\n\t" \
  "s_add_i32 s77, s77, 0x40000\n\t" \
  "s_lshl_b32 s78, s80, 14\n\t" \
  "s_add_i32 s78, s78, 0x70000\n\t" \
  "s_mov_b32 s57, 0\n\t" \
  "s_add_i32 s96, s77, 0x1000\n\t" \
  "s_and_b32 s98, s40, 2\n\t" \
  "s_lshl_b32 s98, s98, 13\n\t" \
  "s_and_b32 s80, s40, 1\n\t" \
  "s_lshl_b32 s80, s80, 12\n\t" \
  "s_add_i32 s98, s98, s80\n\t" \
  "s_add_i32 s98, s98, 0x84000\n\t" \
  "s_memtime vcc\n\t" \
  "s_memrealtime s[86:87]\n\t" \
  "s_nop 4\n\t" \
  "s_memrealtime s[60:61]\n\t" \
  "s_nop 4\n\t" \
  "s_memrealtime s[62:63]\n\t" \
  "s_nop 4\n\t" \
  "s_memrealtime s[64:65]\n\t" \
  "s_nop 4\n\t" \
  "s_memrealtime s[66:67]\n\t" \
  "s_nop 4\n\t" \
  "s_memrealtime s[68:69]\n\t" \
  "s_mov_b32 s79, 0\n\t" \
  "s_waitcnt lgkmcnt(0)\n\t" \
  "s_cmp_eq_u32 s86, s60\n\t" \
  "s_addc_u32 s79, s79, 0\n\t" \
  "s_cmp_eq_u32 s86, s62\n\t" \
  "s_addc_u32 s79, s79, 0\n\t" \
  "s_cmp_eq_u32 s86, s64\n\t" \
  "s_addc_u32 s79, s79, 0\n\t" \
  "s_cmp_eq_u32 s86, s66\n\t" \
  "s_addc_u32 s79, s79, 0\n\t" \
  "s_cmp_eq_u32 s86, s68\n\t" \
  "s_addc_u32 s79, s79, 0\n\t" \
  "s_mul_i32 s79, s79, 24\n\t" \
  "s_add_i32 s79, s79, vcc_lo\n\t" \
  "s_lshl4_add_u32 s80, s86, 64\n\t" \
  "s_sub_i32 s86, s79, s41\n\t" \
  "s_sub_i32 s87, s80, s43\n\t" \
  "s_cmp_lt_u32 s86, 4000\n\t" \
  "s_cbranch_scc1 21f\n\t" \
  "v_cvt_f32_u32 v165, s86\n\t" \
  "v_cvt_f32_u32 v164, s87\n\t" \
  "v_rcp_f32 v165, v165\n\t" \
  "s_nop 0\n\t" \
  "v_mul_f32 v165, v165, v164\n\t" \
  "v_max_f32 v165, 0x3f028f5c, v165\n\t" \
  "v_min_f32 v165, 0x3ffeb852, v165\n\t" \
  "v_rcp_f32 v164, v165\n\t" \
  "v_mul_f32 v165, 0x4f000000, v165\n\t" \
  "v_mul_f32 v164, 0x47800000, v164\n\t" \
  "v_cvt_u32_f32 v165, v165\n\t" \
  "v_cvt_u32_f32 v164, v164\n\t" \
  "s_nop 0\n\t" \
  "v_readfirstlane_b32 s45, v165\n\t" \
  "v_readfirstlane_b32 s44, v164\n\t" \
  "21:\n\t" \
  "s_mov_b32 s41, s79\n\t" \
  "s_mov_b32 s43, s80\n\t" \
  "s_lshl_b32 s83, s43, 8\n\t" \
  "s_mov_b32 s84, 650\n\t" \
  "s_mov_b32 exec_hi, 0\n\t" \
  "s_waitcnt vmcnt(0)\n\t" \
  "v_cmp_ne_u32_sdwa vcc, v184, s85 src0_sel:BYTE_0 src1_sel:DWORD\n\t" \
  "v_subrev_u32 v184, s83, v184\n\t" \
  "v_ashrrev_i32 v184, 8, v184\n\t" \
  "s_cbranch_vccnz 22f\n\t" \
  "s_nop 0\n\t" \
  "v_max_i32_dpp v184, v184, v184 quad_perm:[1,0,3,2] row_mask:0xf bank_mask:0xf\n\t" \
  "s_nop 1\n\t" \
  "v_max_i32_dpp v184, v184, v184 quad_perm:[2,3,0,1] row_mask:0xf bank_mask:0xf\n\t" \
  "s_nop 1\n\t" \
  "v_max_i32_dpp v184, v184, v184 row_half_mirror row_mask:0xf bank_mask:0xf\n\t" \
  "s_nop 1\n\t" \
  "v_max_i32_dpp v184, v184, v184 row_mirror row_mask:0xf bank_mask:0xf\n\t" \
  "s_nop 0\n\t" \
  "v_readlane_b32 s81, v184, 0\n\t" \
  "v_readlane_b32 s82, v184, 16\n\t" \
  "s_max_i32 s81, s81, s82\n\t" \
  "s_add_i32 s81, s81, 1056\n\t" \
  "s_max_i32 s81, s81, 0\n\t" \
  "s_min_i32 s81, s81, 20000\n\t" \
  "s_mul_i32 s81, s81, s44\n\t" \
  "s_lshr_b32 s81, s81, 16\n\t" \
  "24:\n\t" \
  "s_add_i32 s81, s81, s41\n\t" \
  "v_min_u32 v185, 1760, v185\n\t" \
  "s_nop 1\n\t" \
  "v_max_i32_dpp v185, v185, v185 quad_perm:[1,0,3,2] row_mask:0xf bank_mask:0xf\n\t" \
  "s_nop 1\n\t" \
  "v_max_i32_dpp v185, v185, v185 quad_perm:[2,3,0,1] row_mask:0xf bank_mask:0xf\n\t" \
  "s_nop 1\n\t" \
  "v_max_i32_dpp v185, v185, v185 row_half_mirror row_mask:0xf bank_mask:0xf\n\t" \
  "s_nop 1\n\t" \
  "v_max_i32_dpp v185, v185, v185 row_mirror row_mask:0xf bank_mask:0xf\n\t" \
  "s_nop 0\n\t" \
  "v_readlane_b32 s54, v185, 0\n\t" \
  "v_readlane_b32 s82, v185, 16\n\t" \
  "s_max_i32 s54, s54, s82\n\t" \
  "s_add_i32 s54, s54, 330\n\t" \
  "1:\n\t" \
  "s_memtime vcc\n\t" \
  "s_waitcnt lgkmcnt(0)\n\t" \
  "s_sub_i32 s79, s81, vcc_lo\n\t" \
  "s_cmp_gt_i32 s79, 128\n\t" \
  "s_cbranch_scc1 1b\n\t" \
  ".p2align 4\n\t" \
  "2:\n\t" \
  "s_sub_i32 s79, s79, 33\n\t" \
  "s_cmp_gt_i32 s79, 56\n\t" \
  "s_cbranch_scc1 2b\n\t" \
  "buffer_load_dword v187, v200, %[rh], s78 offen sc1\n\t" \
  "s_memtime vcc\n\t" \
  "s_memrealtime s[86:87]\n\t" \
  "s_nop 4\n\t" \
  "s_memrealtime s[60:61]\n\t" \
  "s_nop 4\n\t" \
  "s_memrealtime s[62:63]\n\t" \
  "s_nop 4\n\t" \
  "s_memrealtime s[64:65]\n\t" \
  "s_nop 4\n\t" \
  "s_memrealtime s[66:67]\n\t" \
  "s_nop 4\n\t" \
  "s_memrealtime s[68:69]\n\t" \
  "s_mov_b32 s79, 0\n\t" \
  "s_waitcnt lgkmcnt(0)\n\t" \
  "s_cmp_eq_u32 s86, s60\n\t" \
  "s_addc_u32 s79, s79, 0\n\t" \
  "s_cmp_eq_u32 s86, s62\n\t" \
  "s_addc_u32 s79, s79, 0\n\t" \
  "s_cmp_eq_u32 s86, s64\n\t" \
  "s_addc_u32 s79, s79, 0\n\t" \
  "s_cmp_eq_u32 s86, s66\n\t" \
  "s_addc_u32 s79, s79, 0\n\t" \
  "s_cmp_eq_u32 s86, s68\n\t" \
  "s_addc_u32 s79, s79, 0\n\t" \
  "s_mul_i32 s79, s79, 24\n\t" \
  "s_add_i32 s79, s79, vcc_lo\n\t" \
  "s_lshl4_add_u32 s80, s86, 64\n\t" \
  "s_mov_b32 s41, s79\n\t" \
  "s_mov_b32 s43, s80\n\t" \
  "s_lshl_b32 s83, s43, 8\n\t" \
  "s_add_i32 s54, s54, s43\n\t" \
  "13:\n\t" \
  "s_waitcnt vmcnt(0)\n\t" \
  "s_memtime s[86:87]\n\t" \
  "v_cmp_ne_u32_sdwa vcc, v187, s40 src0_sel:BYTE_0 src1_sel:DWORD\n\t" \
  "v_subrev_u32 v184, s83, v187\n\t" \
  "v_ashrrev_i32 v184, 8, v184\n\t" \
  "s_nop 1\n\t" \
  "v_max_i32_dpp v184, v184, v184 quad_perm:[1,0,3,2] row_mask:0xf bank_mask:0xf\n\t" \
  "s_nop 1\n\t" \
  "v_max_i32_dpp v184, v184, v184 quad_perm:[2,3,0,1] row_mask:0xf bank_mask:0xf\n\t" \
  "s_cbranch_vccnz 14f\n\t" \
  "15:\n\t" \
  "s_nop 0\n\t" \
  "v_max_i32_dpp v184, v184, v184 row_half_mirror row_mask:0xf bank_mask:0xf\n\t" \
  "s_nop 1\n\t" \
  "v_max_i32_dpp v184, v184, v184 row_mirror row_mask:0xf bank_mask:0xf\n\t" \
  "s_mov_b64 exec, -1\n\t" \
  "v_readlane_b32 s57, v184, 0\n\t" \
  "v_readlane_b32 s82, v184, 16\n\t" \
  "s_waitcnt lgkmcnt(0)\n\t" \
  "s_sub_i32 s79, s41, s86\n\t" \
  "s_max_i32 s57, s57, s82\n\t" \
  "s_add_i32 s81, s57, 255\n\t" \
  "s_max_i32 s81, s81, 0\n\t" \
  "s_min_i32 s81, s81, 20000\n\t" \
  "s_mul_i32 s81, s81, s44\n\t" \
  "s_lshr_b32 s81, s81, 16\n\t" \
  "s_add_i32 s79, s79, s81\n\t" \
  "s_sub_i32 s85, s84, s79\n\t" \
  "s_max_i32 s85, s85, 0\n\t" \
  "s_mul_hi_u32 s85, s85, s45\n\t" \
  "s_lshl1_add_u32 s85, s85, s57\n\t" \
  "s_add_i32 s85, s85, s54\n\t" \
  "s_lshl_b32 s85, s85, 8\n\t" \
  "s_or_b32 s85, s85, s40\n\t" \
  "v_mov_b32 v184, s85\n\t" \
  "buffer_store_dword v184, v202, %[rh], s98 offen sc1\n\t" \
  "s_add_i32 s79, s79, -640\n\t" \
  ".p2align 4\n\t" \
  "3:\n\t" \
  "s_sub_i32 s79, s79, 33\n\t" \
  "s_cmp_gt_i32 s79, 56\n\t" \
  "s_cbranch_scc1 3b\n\t" \
  "buffer_load_dwordx4 v[0:3], v193, %[rh], s77 offen offset:0 sc1\n\t" \
  "s_nop 4\n\t" \
  "buffer_load_dwordx4 v[4:7], v193, %[rh], s77 offen offset:1024 sc1\n\t" \
  "s_nop 4\n\t" \
  "buffer_load_dwordx4 v[8:11], v193, %[rh], s77 offen offset:2048 sc1\n\t" \
  "s_nop 4\n\t" \
  "buffer_load_dwordx4 v[12:15], v193, %[rh], s77 offen offset:3072 sc1\n\t" \
  "s_nop 4\n\t" \
  "buffer_load_dwordx4 v[16:19], v193, %[rh], s96 offen offset:0 sc1\n\t" \
  "s_nop 4\n\t" \
  "buffer_load_dwordx4 v[20:23], v193, %[rh], s96 offen offset:1024 sc1\n\t" \
  "s_nop 4\n\t" \
  "buffer_load_dwordx4 v[24:27], v193, %[rh], s96 offen offset:2048 sc1\n\t" \
  "s_nop 4\n\t" \
  "buffer_load_dwordx4 v[28:31], v193, %[rh], s96 offen offset:3072 sc1\n\t" \
  "s_add_i32 s79, s79, 272\n\t" \
  ".p2align 4\n\t" \
  "4:\n\t" \
  "s_sub_i32 s79, s79, 33\n\t" \
  "s_cmp_gt_i32 s79, 56\n\t" \
  "s_cbranch_scc1 4b\n\t" \
  "buffer_load_dwordx4 v[32:35], v195, %[rh], s77 offen offset:0 sc1\n\t" \
  "buffer_load_dwordx4 v[36:39], v195, %[rh], s77 offen offset:2048 sc1\n\t" \
  "10:\n\t" \
  "s_waitcnt vmcnt(9)\n\t" \
  "v_smfmac_f32_16x16x64_bf16 v[40:43], v[0:3], a[0:7], v203\n\t" \
  "v_smfmac_f32_16x16x64_bf16 v[56:59], v[0:3], a[8:15], v203\n\t" \
  "s_waitcnt vmcnt(8)\n\t" \
  "v_smfmac_f32_16x16x64_bf16 v[44:47], v[4:7], a[16:23], v203\n\t" \
  "v_cvt_pk_bf16_f32 v40, v40, v41\n\t" \
  "v_cvt_pk_bf16_f32 v41, v42, v43\n\t" \
  "v_smfmac_f32_16x16x64_bf16 v[60:63], v[4:7], a[24:31], v203\n\t" \
  "v_cvt_pk_bf16_f32 v56, v56, v57\n\t" \
  "v_cvt_pk_bf16_f32 v57, v58, v59\n\t" \
  "s_waitcnt vmcnt(7)\n\t" \
  "v_smfmac_f32_16x16x64_bf16 v[48:51], v[8:11], a[32:39], v203\n\t" \
  "v_cvt_pk_bf16_f32 v42, v44, v45\n\t" \
  "v_cvt_pk_bf16_f32 v43, v46, v47\n\t" \
  "v_smfmac_f32_16x16x64_bf16 v[64:67], v[8:11], a[40:47], v203\n\t" \
  "v_cvt_pk_bf16_f32 v58, v60, v61\n\t" \
  "v_cvt_pk_bf16_f32 v59, v62, v63\n\t" \
  "s_waitcnt vmcnt(6)\n\t" \
  "v_smfmac_f32_16x16x64_bf16 v[52:55], v[12:15], a[48:55], v203\n\t" \
  "v_cvt_pk_bf16_f32 v44, v48, v49\n\t" \
  "v_cvt_pk_bf16_f32 v45, v50, v51\n\t" \
  "v_smfmac_f32_16x16x64_bf16 v[68:71], v[12:15], a[56:63], v203\n\t" \
  "v_cvt_pk_bf16_f32 v60, v64, v65\n\t" \
  "v_cvt_pk_bf16_f32 v61, v66, v67\n\t" \
  "s_waitcnt vmcnt(5)\n\t" \
  "v_smfmac_f32_16x16x64_bf16 v[72:75], v[16:19], a[64:71], v203\n\t" \
  "v_cvt_pk_bf16_f32 v46, v52, v53\n\t" \
  "v_cvt_pk_bf16_f32 v47, v54, v55\n\t" \
  "v_smfmac_f32_16x16x64_bf16 v[88:91], v[16:19], a[72:79], v203\n\t" \
  "v_cvt_pk_bf16_f32 v62, v68, v69\n\t" \
  "v_cvt_pk_bf16_f32 v63, v70, v71\n\t" \
  "s_waitcnt vmcnt(4)\n\t" \
  "v_smfmac_f32_16x16x64_bf16 v[76:79], v[20:23], a[80:87], v203\n\t" \
  "v_cvt_pk_bf16_f32 v72, v72, v73\n\t" \
  "v_cvt_pk_bf16_f32 v73, v74, v75\n\t" \
  "v_smfmac_f32_16x16x64_bf16 v[92:95], v[20:23], a[88:95], v203\n\t" \
  "v_cvt_pk_bf16_f32 v88, v88, v89\n\t" \
  "v_cvt_pk_bf16_f32 v89, v90, v91\n\t" \
  "s_waitcnt vmcnt(3)\n\t" \
  "v_smfmac_f32_16x16x64_bf16 v[80:83], v[24:27], a[96:103], v203\n\t" \
  "v_cvt_pk_bf16_f32 v74, v76, v77\n\t" \
  "v_cvt_pk_bf16_f32 v75, v78, v79\n\t" \
  "v_smfmac_f32_16x16x64_bf16 v[96:99], v[24:27], a[104:111], v203\n\t" \
  "v_cvt_pk_bf16_f32 v90, v92, v93\n\t" \
  "v_cvt_pk_bf16_f32 v91, v94, v95\n\t" \
  "s_waitcnt vmcnt(2)\n\t" \
  "v_smfmac_f32_16x16x64_bf16 v[84:87], v[28:31], a[112:119], v203\n\t" \
  "v_cvt_pk_bf16_f32 v76, v80, v81\n\t" \
  "v_cvt_pk_bf16_f32 v77, v82, v83\n\t" \
  "v_smfmac_f32_16x16x64_bf16 v[100:103], v[28:31], a[120:127], v203\n\t" \
  "v_cvt_pk_bf16_f32 v92, v96, v97\n\t" \
  "v_cvt_pk_bf16_f32 v93, v98, v99\n\t" \
  "s_waitcnt vmcnt(1)\n\t" \
  "v_smfmac_f32_16x16x64_bf16 v[136:139], v[32:35], v[40:47], v204\n\t" \
  "v_cvt_pk_bf16_f32 v78, v84, v85\n\t" \
  "v_cvt_pk_bf16_f32 v79, v86, v87\n\t" \
  "v_smfmac_f32_16x16x64_bf16 v[140:143], v[32:35], v[56:63], v204\n\t" \
  "v_cvt_pk_bf16_f32 v94, v100, v101\n\t" \
  "v_cvt_pk_bf16_f32 v95, v102, v103\n\t" \
  "s_waitcnt vmcnt(0)\n\t" \
  "v_smfmac_f32_16x16x64_bf16 v[136:139], v[36:39], v[72:79], v204\n\t" \
  "v_smfmac_f32_16x16x64_bf16 v[140:143], v[36:39], v[88:95], v204\n\t" \
  "s_nop 3\n\t" \
  "v_permlane32_swap_b32 v136, v138\n\t" \
  "v_permlane32_swap_b32 v137, v139\n\t" \
  "v_add_f32 v136, v136, v138\n\t" \
  "v_add_f32 v137, v137, v139\n\t" \
  "v_permlane32_swap_b32 v140, v142\n\t" \
  "v_permlane32_swap_b32 v141, v143\n\t" \
  "v_add_f32 v138, v140, v142\n\t" \
  "v_add_f32 v139, v141, v143\n\t" \
  "ds_write2st64_b64 v197, v[136:137], v[138:139] offset1:2\n\t" \
  "s_waitcnt lgkmcnt(0)\n\t" \
  "s_barrier\n\t" \
  "s_barrier\n\t" \
  "ds_read2_b32 v[148:149], v207 offset1:8\n\t" \
  "s_waitcnt lgkmcnt(0)\n\t" \
  "v_or_b32 v148, v148, v149\n\t" \
  "v_cmp_ne_u32 vcc, 0, v148\n\t" \
  "s_cbranch_vccnz 12f\n\t" \
  "s_and_b32 s58, s58, 4\n\t" \
  "s_cmp_ge_i32 s40, s53\n\t" \
  "s_cbranch_scc1 26f\n\t" \
  "s_and_b32 s82, s40, 3\n\t" \
  "s_lshl_b32 s82, s82, 20\n\t" \
  "s_add_i32 s82, s82, s48\n\t" \
  "s_andn2_b32 s82, s82, 0x1800\n\t" \
  "s_lshr_b32 s80, s82, 21\n\t" \
  "s_bitcmp1_b64 %[sel], s80\n\t" \
  "s_cselect_b32 s80, 0x2000, 0\n\t" \
  "s_add_i32 s82, s82, s80\n\t" \
  "s_add_i32 s80, s82, 0x1000\n\t" \
  "v_mov_b64 v[188:189], -1\n\t" \
  "v_mov_b64 v[190:191], -1\n\t" \
  "s_sleep 18\n\t" \
  "buffer_store_dwordx4 v[188:191], v192, %[ru], s82 offen offset:0 sc1\n\t" \
  "buffer_store_dwordx4 v[188:191], v192, %[ru], s82 offen offset:1024 sc1\n\t" \
  "buffer_store_dwordx4 v[188:191], v192, %[ru], s82 offen offset:2048 sc1\n\t" \
  "buffer_store_dwordx4 v[188:191], v192, %[ru], s82 offen offset:3072 sc1\n\t" \
  "buffer_store_dwordx4 v[188:191], v192, %[ru], s80 offen offset:0 sc1\n\t" \
  "buffer_store_dwordx4 v[188:191], v192, %[ru], s80 offen offset:1024 sc1\n\t" \
  "buffer_store_dwordx4 v[188:191], v192, %[ru], s80 offen offset:2048 sc1\n\t" \
  "buffer_store_dwordx4 v[188:191], v192, %[ru], s80 offen offset:3072 sc1\n\t" \
  "26:\n\t" \
  "s_and_b32 s58, s58, 36\n\t" \
  "s_and_b32 s76, s40, 3\n\t" \
  "s_lshl_b32 s76, s76, 18\n\t" \
  "s_add_i32 s77, s76, 0x10000\n\t" \
  "s_add_i32 s78, s76, 0x1000\n\t" \
  "s_add_i32 s79, s76, 0x11000\n\t" \
  "s_add_i32 s85, s40, 1\n\t" \
  "s_barrier\n\t" \
  "buffer_load_dwordx4 v[0:3], v210, %[rh], s76 offen offset:0 sc1\n\t" \
  "buffer_load_dwordx4 v[4:7], v210, %[rh], s76 offen offset:1024 sc1\n\t" \
  "buffer_load_dwordx4 v[8:11], v210, %[rh], s77 offen offset:0 sc1\n\t" \
  "buffer_load_dwordx4 v[12:15], v210, %[rh], s77 offen offset:1024 sc1\n\t" \
  "buffer_load_dwordx4 v[16:19], v210, %[rh], s76 offen offset:2048 sc1\n\t" \
  "buffer_load_dwordx4 v[20:23], v210, %[rh], s76 offen offset:3072 sc1\n\t" \
  "buffer_load_dwordx4 v[24:27], v210, %[rh], s77 offen offset:2048 sc1\n\t" \
  "buffer_load_dwordx4 v[28:31], v210, %[rh], s77 offen offset:3072 sc1\n\t" \
  "buffer_load_dwordx4 v[32:35], v210, %[rh], s78 offen offset:0 sc1\n\t" \
  "buffer_load_dwordx4 v[36:39], v210, %[rh], s78 offen offset:1024 sc1\n\t" \
  "buffer_load_dwordx4 v[40:43], v210, %[rh], s79 offen offset:0 sc1\n\t" \
  "buffer_load_dwordx4 v[44:47], v210, %[rh], s79 offen offset:1024 sc1\n\t" \
  "buffer_load_dwordx4 v[48:51], v210, %[rh], s78 offen offset:2048 sc1\n\t" \
  "buffer_load_dwordx4 v[52:55], v210, %[rh], s78 offen offset:3072 sc1\n\t" \
  "buffer_load_dwordx4 v[56:59], v210, %[rh], s79 offen offset:2048 sc1\n\t" \
  "buffer_load_dwordx4 v[60:63], v210, %[rh], s79 offen offset:3072 sc1\n\t" \
  "buffer_load_dwordx4 v[64:67], v211, %[rh], s76 offen offset:0 sc1\n\t" \
  "buffer_load_dwordx4 v[68:71], v211, %[rh], s76 offen offset:1024 sc1\n\t" \
  "buffer_load_dwordx4 v[72:75], v211, %[rh], s77 offen offset:0 sc1\n\t" \
  "buffer_load_dwordx4 v[76:79], v211, %[rh], s77 offen offset:1024 sc1\n\t" \
  "buffer_load_dwordx4 v[80:83], v211, %[rh], s76 offen offset:2048 sc1\n\t" \
  "buffer_load_dwordx4 v[84:87], v211, %[rh], s76 offen offset:3072 sc1\n\t" \
  "buffer_load_dwordx4 v[88:91], v211, %[rh], s77 offen offset:2048 sc1\n\t" \
  "buffer_load_dwordx4 v[92:95], v211, %[rh], s77 offen offset:3072 sc1\n\t" \
  "buffer_load_dwordx4 v[96:99], v211, %[rh], s78 offen offset:0 sc1\n\t" \
  "buffer_load_dwordx4 v[100:103], v211, %[rh], s78 offen offset:1024 sc1\n\t" \
  "buffer_load_dwordx4 v[104:107], v211, %[rh], s79 offen offset:0 sc1\n\t" \
  "buffer_load_dwordx4 v[108:111], v211, %[rh], s79 offen offset:1024 sc1\n\t" \
  "buffer_load_dwordx4 v[112:115], v211, %[rh], s78 offen offset:2048 sc1\n\t" \
  "buffer_load_dwordx4 v[116:119], v211, %[rh], s78 offen offset:3072 sc1\n\t" \
  "buffer_load_dwordx4 v[120:123], v211, %[rh], s79 offen offset:2048 sc1\n\t" \
  "buffer_load_dwordx4 v[124:127], v211, %[rh], s79 offen offset:3072 sc1\n\t" \
  "s_and_b32 s82, s85, 2\n\t" \
  "s_lshl_b32 s82, s82, 13\n\t" \
  "s_and_b32 s80, s85, 1\n\t" \
  "s_lshl_b32 s80, s80, 12\n\t" \
  "s_add_i32 s82, s82, s80\n\t" \
  "s_add_i32 s82, s82, 0x84000\n\t" \
  "s_mov_b32 exec_hi, 0\n\t" \
  "buffer_load_dword v184, v200, %[rh], s82 offen sc1\n\t" \
  "buffer_load_dword v185, v200, %[rh], s52 offen sc1\n\t" \
  "s_mov_b64 exec, -1\n\t" \
  "s_barrier\n\t" \
  "31:\n\t" \
  "v_mov_b64 v[136:137], 0\n\t" \
  "v_mov_b64 v[138:139], 0\n\t" \
  "v_mov_b64 v[140:141], 0\n\t" \
  "v_mov_b64 v[142:143], 0\n\t" \
  "s_waitcnt vmcnt(32)\n\t" \
  "v_swap_b32 v1, v4\n\t" \
  "v_swap_b32 v3, v6\n\t" \
  "s_nop 1\n\t" \
  "v_mfma_f32_16x16x32_bf16 v[168:171], a[0:3], v[0:3], 0\n\t" \
  "v_mfma_f32_16x16x32_bf16 v[172:175], a[8:11], v[0:3], 0\n\t" \
  "s_waitcnt vmcnt(30)\n\t" \
  "v_swap_b32 v9, v12\n\t" \
  "v_mfma_f32_16x16x32_bf16 v[168:171], a[4:7], v[4:7], v[168:171]\n\t" \
  "v_swap_b32 v11, v14\n\t" \
  "v_mfma_f32_16x16x32_bf16 v[172:175], a[12:15], v[4:7], v[172:175]\n\t" \
  "s_nop 0\n\t" \
  "v_mfma_f32_16x16x32_bf16 v[176:179], a[0:3], v[8:11], 0\n\t" \
  "ds_read_b128 a[0:3], v194 offset:0\n\t" \
  "v_mfma_f32_16x16x32_bf16 v[180:183], a[8:11], v[8:11], 0\n\t" \
  "ds_read_b128 a[8:11], v194 offset:2048\n\t" \
  "s_waitcnt vmcnt(28)\n\t" \
  "v_swap_b32 v17, v20\n\t" \
  "v_mfma_f32_16x16x32_bf16 v[176:179], a[4:7], v[12:15], v[176:179]\n\t" \
  "ds_read_b128 a[4:7], v194 offset:1024\n\t" \
  "v_swap_b32 v19, v22\n\t" \
  "v_mfma_f32_16x16x32_bf16 v[180:183], a[12:15], v[12:15], v[180:183]\n\t" \
  "ds_read_b128 a[12:15], v194 offset:3072\n\t" \
  "v_mfma_f32_16x16x32_bf16 v[168:171], a[16:19], v[16:19], v[168:171]\n\t" \
  "v_mfma_f32_16x16x32_bf16 v[172:175], a[24:27], v[16:19], v[172:175]\n\t" \
  "s_waitcnt vmcnt(26)\n\t" \
  "v_swap_b32 v25, v28\n\t" \
  "v_mfma_f32_16x16x32_bf16 v[168:171], a[20:23], v[20:23], v[168:171]\n\t" \
  "v_swap_b32 v27, v30\n\t" \
  "v_mfma_f32_16x16x32_bf16 v[172:175], a[28:31], v[20:23], v[172:175]\n\t" \
  "s_nop 0\n\t" \
  "v_mfma_f32_16x16x32_bf16 v[176:179], a[16:19], v[24:27], v[176:179]\n\t" \
  "ds_read_b128 a[16:19], v194 offset:4096\n\t" \
  "v_mfma_f32_16x16x32_bf16 v[180:183], a[24:27], v[24:27], v[180:183]\n\t" \
  "ds_read_b128 a[24:27], v194 offset:6144\n\t" \
  "s_waitcnt vmcnt(24)\n\t" \
  "v_swap_b32 v33, v36\n\t" \
  "v_mfma_f32_16x16x32_bf16 v[176:179], a[20:23], v[28:31], v[176:179]\n\t" \
  "ds_read_b128 a[20:23], v194 offset:5120\n\t" \
  "v_swap_b32 v35, v38\n\t" \
  "v_mfma_f32_16x16x32_bf16 v[180:183], a[28:31], v[28:31], v[180:183]\n\t" \
  "ds_read_b128 a[28:31], v194 offset:7168\n\t" \
  "v_mfma_f32_16x16x32_bf16 v[168:171], a[32:35], v[32:35], v[168:171]\n\t" \
  "v_mfma_f32_16x16x32_bf16 v[172:175], a[40:43], v[32:35], v[172:175]\n\t" \
  "s_waitcnt vmcnt(22)\n\t" \
  "v_swap_b32 v41, v44\n\t" \
  "v_mfma_f32_16x16x32_bf16 v[168:171], a[36:39], v[36:39], v[168:171]\n\t" \
  "v_swap_b32 v43, v46\n\t" \
  "v_mfma_f32_16x16x32_bf16 v[172:175], a[44:47], v[36:39], v[172:175]\n\t" \
  "s_nop 0\n\t" \
  "v_mfma_f32_16x16x32_bf16 v[176:179], a[32:35], v[40:43], v[176:179]\n\t" \
  "ds_read_b128 a[32:35], v194 offset:8192\n\t" \
  "v_mfma_f32_16x16x32_bf16 v[180:183], a[40:43], v[40:43], v[180:183]\n\t" \
  "ds_read_b128 a[40:43], v194 offset:10240\n\t" \
  "s_waitcnt vmcnt(20)\n\t" \
  "v_swap_b32 v49, v52\n\t" \
  "v_mfma_f32_16x16x32_bf16 v[176:179], a[36:39], v[44:47], v[176:179]\n\t" \
  "ds_read_b128 a[36:39], v194 offset:9216\n\t" \
  "v_swap_b32 v51, v54\n\t" \
  "v_mfma_f32_16x16x32_bf16 v[180:183], a[44:47], v[44:47], v[180:183]\n\t" \
  "ds_read_b128 a[44:47], v194 offset:11264\n\t" \
  "v_mov_b64 v[40:41], 0\n\t" \
  "v_mov_b64 v[42:43], 0\n\t" \
  "v_mfma_f32_16x16x32_bf16 v[168:171], a[48:51], v[48:51], v[168:171]\n\t" \
  "v_mov_b64 v[44:45], 0\n\t" \
  "v_mov_b64 v[46:47], 0\n\t" \
  "v_mfma_f32_16x16x32_bf16 v[172:175], a[56:59], v[48:51], v[172:175]\n\t" \
  "s_waitcnt vmcnt(18)\n\t" \
  "v_swap_b32 v57, v60\n\t" \
  "v_mfma_f32_16x16x32_bf16 v[168:171], a[52:55], v[52:55], v[168:171]\n\t" \
  "v_swap_b32 v59, v62\n\t" \
  "v_mfma_f32_16x16x32_bf16 v[172:175], a[60:63], v[52:55], v[172:175]\n\t" \
  "v_mov_b64 v[48:49], 0\n\t" \
  "v_mov_b64 v[50:51], 0\n\t" \
  "v_mfma_f32_16x16x32_bf16 v[176:179], a[48:51], v[56:59], v[176:179]\n\t" \
  "ds_read_b128 a[48:51], v194 offset:12288\n\t" \
  "v_mov_b64 v[52:53], 0\n\t" \
  "v_mov_b64 v[54:55], 0\n\t" \
  "v_mfma_f32_16x16x32_bf16 v[180:183], a[56:59], v[56:59], v[180:183]\n\t" \
  "ds_read_b128 a[56:59], v194 offset:14336\n\t" \
  "s_waitcnt vmcnt(16)\n\t" \
  "v_swap_b32 v65, v68\n\t" \
  "v_mfma_f32_16x16x32_bf16 v[176:179], a[52:55], v[60:63], v[176:179]\n\t" \
  "ds_read_b128 a[52:55], v194 offset:13312\n\t" \
  "v_swap_b32 v67, v70\n\t" \
  "v_mfma_f32_16x16x32_bf16 v[180:183], a[60:63], v[60:63], v[180:183]\n\t" \
  "ds_read_b128 a[60:63], v194 offset:15360\n\t" \
  "v_mov_b64 v[56:57], 0\n\t" \
  "v_mov_b64 v[58:59], 0\n\t" \
  "v_mfma_f32_16x16x32_bf16 v[168:171], a[64:67], v[64:67], v[168:171]\n\t" \
  "v_mov_b64 v[60:61], 0\n\t" \
  "v_mov_b64 v[62:63], 0\n\t" \
  "v_mfma_f32_16x16x32_bf16 v[172:175], a[72:75], v[64:67], v[172:175]\n\t" \
  "s_waitcnt vmcnt(14)\n\t" \
  "v_swap_b32 v73, v76\n\t" \
  "v_mfma_f32_16x16x32_bf16 v[168:171], a[68:71], v[68:71], v[168:171]\n\t" \
  "v_swap_b32 v75, v78\n\t" \
  "v_mfma_f32_16x16x32_bf16 v[172:175], a[76:79], v[68:71], v[172:175]\n\t" \
  "v_mov_b64 v[64:65], 0\n\t" \
  "v_mov_b64 v[66:67], 0\n\t" \
  "v_mfma_f32_16x16x32_bf16 v[176:179], a[64:67], v[72:75], v[176:179]\n\t" \
  "ds_read_b128 a[64:67], v194 offset:16384\n\t" \
  "v_mov_b64 v[68:69], 0\n\t" \
  "v_mov_b64 v[70:71], 0\n\t" \
  "v_mfma_f32_16x16x32_bf16 v[180:183], a[72:75], v[72:75], v[180:183]\n\t" \
  "ds_read_b128 a[72:75], v194 offset:18432\n\t" \
  "s_waitcnt vmcnt(12)\n\t" \
  "v_swap_b32 v81, v84\n\t" \
  "v_mfma_f32_16x16x32_bf16 v[176:179], a[68:71], v[76:79], v[176:179]\n\t" \
  "ds_read_b128 a[68:71], v194 offset:17408\n\t" \
  "v_swap_b32 v83, v86\n\t" \
  "v_mfma_f32_16x16x32_bf16 v[180:183], a[76:79], v[76:79], v[180:183]\n\t" \
  "ds_read_b128 a[76:79], v194 offset:19456\n\t" \
  "v_mov_b64 v[72:73], 0\n\t" \
  "v_mov_b64 v[74:75], 0\n\t" \
  "v_mfma_f32_16x16x32_bf16 v[168:171], a[80:83], v[80:83], v[168:171]\n\t" \
  "v_mov_b64 v[76:77], 0\n\t" \
  "v_mov_b64 v[78:79], 0\n\t" \
  "v_mfma_f32_16x16x32_bf16 v[172:175], a[88:91], v[80:83], v[172:175]\n\t" \
  "s_waitcnt vmcnt(10)\n\t" \
  "v_swap_b32 v89, v92\n\t" \
  "v_mfma_f32_16x16x32_bf16 v[168:171], a[84:87], v[84:87], v[168:171]\n\t" \
  "v_swap_b32 v91, v94\n\t" \
  "v_mfma_f32_16x16x32_bf16 v[172:175], a[92:95], v[84:87], v[172:175]\n\t" \
  "v_mov_b64 v[80:81], 0\n\t" \
  "v_mov_b64 v[82:83], 0\n\t" \
  "v_mfma_f32_16x16x32_bf16 v[176:179], a[80:83], v[88:91], v[176:179]\n\t" \
  "ds_read_b128 a[80:83], v194 offset:20480\n\t" \
  "v_mov_b64 v[84:85], 0\n\t" \
  "v_mov_b64 v[86:87], 0\n\t" \
  "v_mfma_f32_16x16x32_bf16 v[180:183], a[88:91], v[88:91], v[180:183]\n\t" \
  "ds_read_b128 a[88:91], v194 offset:22528\n\t" \
  "s_waitcnt vmcnt(8)\n\t" \
  "v_swap_b32 v97, v100\n\t" \
  "v_mfma_f32_16x16x32_bf16 v[176:179], a[84:87], v[92:95], v[176:179]\n\t" \
  "ds_read_b128 a[84:87], v194 offset:21504\n\t" \
  "v_swap_b32 v99, v102\n\t" \
  "v_mfma_f32_16x16x32_bf16 v[180:183], a[92:95], v[92:95], v[180:183]\n\t" \
  "ds_read_b128 a[92:95], v194 offset:23552\n\t" \
  "v_mov_b64 v[88:89], 0\n\t" \
  "v_mov_b64 v[90:91], 0\n\t" \
  "v_mfma_f32_16x16x32_bf16 v[168:171], a[96:99], v[96:99], v[168:171]\n\t" \
  "v_mov_b64 v[92:93], 0\n\t" \
  "v_mov_b64 v[94:95], 0\n\t" \
  "v_mfma_f32_16x16x32_bf16 v[172:175], a[104:107], v[96:99], v[172:175]\n\t" \
  "s_waitcnt vmcnt(6)\n\t" \
  "v_swap_b32 v105, v108\n\t" \
  "v_mfma_f32_16x16x32_bf16 v[168:171], a[100:103], v[100:103], v[168:171]\n\t" \
  "v_swap_b32 v107, v110\n\t" \
  "v_mfma_f32_16x16x32_bf16 v[172:175], a[108:111], v[100:103], v[172:175]\n\t" \
  "v_mov_b64 v[96:97], 0\n\t" \
  "v_mov_b64 v[98:99], 0\n\t" \
  "v_mfma_f32_16x16x32_bf16 v[176:179], a[96:99], v[104:107], v[176:179]\n\t" \
  "ds_read_b128 a[96:99], v194 offset:24576\n\t" \
  "v_mov_b64 v[100:101], 0\n\t" \
  "v_mov_b64 v[102:103], 0\n\t" \
  "v_mfma_f32_16x16x32_bf16 v[180:183], a[104:107], v[104:107], v[180:183]\n\t" \
  "ds_read_b128 a[104:107], v194 offset:26624\n\t" \
  "s_waitcnt vmcnt(4)\n\t" \
  "v_swap_b32 v113, v116\n\t" \
  "v_mfma_f32_16x16x32_bf16 v[176:179], a[100:103], v[108:111], v[176:179]\n\t" \
  "ds_read_b128 a[100:103], v194 offset:25600\n\t" \
  "v_swap_b32 v115, v118\n\t" \
  "v_mfma_f32_16x16x32_bf16 v[180:183], a[108:111], v[108:111], v[180:183]\n\t" \
  "ds_read_b128 a[108:111], v194 offset:27648\n\t" \
  "v_mfma_f32_16x16x32_bf16 v[168:171], a[112:115], v[112:115], v[168:171]\n\t" \
  "v_mfma_f32_16x16x32_bf16 v[172:175], a[120:123], v[112:115], v[172:175]\n\t" \
  "s_waitcnt vmcnt(2)\n\t" \
  "v_swap_b32 v121, v124\n\t" \
  "v_mfma_f32_16x16x32_bf16 v[168:171], a[116:119], v[116:119], v[168:171]\n\t" \
  "v_swap_b32 v123, v126\n\t" \
  "v_mfma_f32_16x16x32_bf16 v[172:175], a[124:127], v[116:119], v[172:175]\n\t" \
  "s_nop 0\n\t" \
  "v_mfma_f32_16x16x32_bf16 v[176:179], a[112:115], v[120:123], v[176:179]\n\t" \
  "ds_read_b128 a[112:115], v194 offset:28672\n\t" \
  "v_mfma_f32_16x16x32_bf16 v[180:183], a[120:123], v[120:123], v[180:183]\n\t" \
  "ds_read_b128 a[120:123], v194 offset:30720\n\t" \
  "v_mfma_f32_16x16x32_bf16 v[176:179], a[116:119], v[124:127], v[176:179]\n\t" \
  "ds_read_b128 a[116:119], v194 offset:29696\n\t" \
  "v_mfma_f32_16x16x32_bf16 v[180:183], a[124:127], v[124:127], v[180:183]\n\t" \
  "ds_read_b128 a[124:127], v194 offset:31744\n\t" \
  "s_nop 6\n\t" \
  "s_add_i32 s40, s40, 2\n\t" \
  "v_cmp_u_f32 vcc, v168, v176\n\t" \
  "s_cbranch_vccnz 32f\n\t" \
  "33:\n\t" \
  "ds_write_b128 v199, v[168:171] offset:0\n\t" \
  "ds_write_b128 v199, v[172:175] offset:1024\n\t" \
  "ds_write_b128 v199, v[176:179] offset:8192\n\t" \
  "ds_write_b128 v199, v[180:183] offset:9216\n\t" \
  "s_mov_b32 s59, 0\n\t" \
  "s_and_b32 s58, s58, 4\n\t" \
  "s_cmp_lt_i32 s40, 64\n\t" \
  "s_cbranch_scc1 20b\n\t" \
  "s_waitcnt lgkmcnt(0)\n\t" \
  "s_barrier\n\t" \
  "s_mov_b32 s58, 64\n\t" \
  "s_branch 7f\n\t" \
  "22:\n\t" \
  "s_mov_b32 s81, 0\n\t" \
  "s_cmp_eq_u32 s40, 0\n\t" \
  "s_cbranch_scc1 24b\n\t" \
  "s_bitcmp1_b32 s58, 2\n\t" \
  "s_cbranch_scc1 24b\n\t" \
  "s_mov_b64 exec, -1\n\t" \
  "s_sleep 10\n\t" \
  "s_add_u32 s59, s59, 0x10000\n\t" \
  "s_cmp_lt_u32 s59, 0x100000\n\t" \
  "s_cbranch_scc1 19b\n\t" \
  "s_or_b32 s58, s58, 1\n\t" \
  "s_branch 7f\n\t" \
  "14:\n\t" \
  "v_mov_b32 v184, 0\n\t" \
  "s_bitcmp1_b32 s58, 2\n\t" \
  "s_nop 0\n\t" \
  "s_cbranch_scc1 15b\n\t" \
  "s_mov_b32 s84, 950\n\t" \
  "s_add_u32 s59, s59, 0x10000\n\t" \
  "s_cmp_lt_u32 s59, 0x100000\n\t" \
  "s_mov_b64 exec, vcc\n\t" \
  "buffer_load_dword v187, v200, %[rh], s78 offen sc1\n\t" \
  "s_mov_b32 exec_lo, -1\n\t" \
  "s_cbranch_scc1 13b\n\t" \
  "s_mov_b64 exec, -1\n\t" \
  "s_or_b32 s58, s58, 1\n\t" \
  "s_branch 7f\n\t" \
  "37:\n\t" \
  "s_barrier\n\t" \
  "s_barrier\n\t" \
  "ds_read_b128 a[0:3], v194 offset:0\n\t" \
  "ds_read_b128 a[4:7], v194 offset:1024\n\t" \
  "ds_read_b128 a[8:11], v194 offset:2048\n\t" \
  "ds_read_b128 a[12:15], v194 offset:3072\n\t" \
  "ds_read_b128 a[16:19], v194 offset:4096\n\t" \
  "ds_read_b128 a[20:23], v194 offset:5120\n\t" \
  "ds_read_b128 a[24:27], v194 offset:6144\n\t" \
  "ds_read_b128 a[28:31], v194 offset:7168\n\t" \
  "ds_read_b128 a[32:35], v194 offset:8192\n\t" \
  "ds_read_b128 a[36:39], v194 offset:9216\n\t" \
  "ds_read_b128 a[40:43], v194 offset:10240\n\t" \
  "ds_read_b128 a[44:47], v194 offset:11264\n\t" \
  "ds_read_b128 a[48:51], v194 offset:12288\n\t" \
  "ds_read_b128 a[52:55], v194 offset:13312\n\t" \
  "ds_read_b128 a[56:59], v194 offset:14336\n\t" \
  "ds_read_b128 a[60:63], v194 offset:15360\n\t" \
  "ds_read_b128 a[64:67], v194 offset:16384\n\t" \
  "ds_read_b128 a[68:71], v194 offset:17408\n\t" \
  "ds_read_b128 a[72:75], v194 offset:18432\n\t" \
  "ds_read_b128 a[76:79], v194 offset:19456\n\t" \
  "ds_read_b128 a[80:83], v194 offset:20480\n\t" \
  "ds_read_b128 a[84:87], v194 offset:21504\n\t" \
  "ds_read_b128 a[88:91], v194 offset:22528\n\t" \
  "ds_read_b128 a[92:95], v194 offset:23552\n\t" \
  "ds_read_b128 a[96:99], v194 offset:24576\n\t" \
  "ds_read_b128 a[100:103], v194 offset:25600\n\t" \
  "ds_read_b128 a[104:107], v194 offset:26624\n\t" \
  "ds_read_b128 a[108:111], v194 offset:27648\n\t" \
  "ds_read_b128 a[112:115], v194 offset:28672\n\t" \
  "ds_read_b128 a[116:119], v194 offset:29696\n\t" \
  "ds_read_b128 a[120:123], v194 offset:30720\n\t" \
  "ds_read_b128 a[124:127], v194 offset:31744\n\t" \
  "s_add_i32 s40, s40, 2\n\t" \
  "s_and_b32 s58, s58, 4\n\t" \
  "s_branch 17b\n\t" \
  "32:\n\t" \
  "s_and_b32 s80, s58, 36\n\t" \
  "s_cbranch_scc1 33b\n\t" \
  "s_add_i32 s40, s40, -2\n\t" \
  "34:\n\t" \
  "s_or_b32 s59, s59, 128\n\t" \
  "s_and_b32 s76, s40, 3\n\t" \
  "s_lshl_b32 s76, s76, 18\n\t" \
  "s_add_i32 s77, s76, 0x10000\n\t" \
  "s_add_i32 s78, s76, 0x1000\n\t" \
  "s_add_i32 s79, s76, 0x11000\n\t" \
  "35:\n\t" \
  "buffer_load_dwordx4 v[0:3], v210, %[rh], s76 offen offset:0 sc1\n\t" \
  "buffer_load_dwordx4 v[4:7], v210, %[rh], s76 offen offset:1024 sc1\n\t" \
  "buffer_load_dwordx4 v[8:11], v210, %[rh], s77 offen offset:0 sc1\n\t" \
  "buffer_load_dwordx4 v[12:15], v210, %[rh], s77 offen offset:1024 sc1\n\t" \
  "buffer_load_dwordx4 v[16:19], v210, %[rh], s76 offen offset:2048 sc1\n\t" \
  "buffer_load_dwordx4 v[20:23], v210, %[rh], s76 offen offset:3072 sc1\n\t" \
  "buffer_load_dwordx4 v[24:27], v210, %[rh], s77 offen offset:2048 sc1\n\t" \
  "buffer_load_dwordx4 v[28:31], v210, %[rh], s77 offen offset:3072 sc1\n\t" \
  "buffer_load_dwordx4 v[32:35], v210, %[rh], s78 offen offset:0 sc1\n\t" \
  "buffer_load_dwordx4 v[36:39], v210, %[rh], s78 offen offset:1024 sc1\n\t" \
  "buffer_load_dwordx4 v[40:43], v210, %[rh], s79 offen offset:0 sc1\n\t" \
  "buffer_load_dwordx4 v[44:47], v210, %[rh], s79 offen offset:1024 sc1\n\t" \
  "buffer_load_dwordx4 v[48:51], v210, %[rh], s78 offen offset:2048 sc1\n\t" \
  "buffer_load_dwordx4 v[52:55], v210, %[rh], s78 offen offset:3072 sc1\n\t" \
  "buffer_load_dwordx4 v[56:59], v210, %[rh], s79 offen offset:2048 sc1\n\t" \
  "buffer_load_dwordx4 v[60:63], v210, %[rh], s79 offen offset:3072 sc1\n\t" \
  "buffer_load_dwordx4 v[64:67], v211, %[rh], s76 offen offset:0 sc1\n\t" \
  "buffer_load_dwordx4 v[68:71], v211, %[rh], s76 offen offset:1024 sc1\n\t" \
  "buffer_load_dwordx4 v[72:75], v211, %[rh], s77 offen offset:0 sc1\n\t" \
  "buffer_load_dwordx4 v[76:79], v211, %[rh], s77 offen offset:1024 sc1\n\t" \
  "buffer_load_dwordx4 v[80:83], v211, %[rh], s76 offen offset:2048 sc1\n\t" \
  "buffer_load_dwordx4 v[84:87], v211, %[rh], s76 offen offset:3072 sc1\n\t" \
  "buffer_load_dwordx4 v[88:91], v211, %[rh], s77 offen offset:2048 sc1\n\t" \
  "buffer_load_dwordx4 v[92:95], v211, %[rh], s77 offen offset:3072 sc1\n\t" \
  "buffer_load_dwordx4 v[96:99], v211, %[rh], s78 offen offset:0 sc1\n\t" \
  "buffer_load_dwordx4 v[100:103], v211, %[rh], s78 offen offset:1024 sc1\n\t" \
  "buffer_load_dwordx4 v[104:107], v211, %[rh], s79 offen offset:0 sc1\n\t" \
  "buffer_load_dwordx4 v[108:111], v211, %[rh], s79 offen offset:1024 sc1\n\t" \
  "buffer_load_dwordx4 v[112:115], v211, %[rh], s78 offen offset:2048 sc1\n\t" \
  "buffer_load_dwordx4 v[116:119], v211, %[rh], s78 offen offset:3072 sc1\n\t" \
  "buffer_load_dwordx4 v[120:123], v211, %[rh], s79 offen offset:2048 sc1\n\t" \
  "buffer_load_dwordx4 v[124:127], v211, %[rh], s79 offen offset:3072 sc1\n\t" \
  "s_add_i32 s85, s40, 1\n\t" \
  "s_and_b32 s82, s85, 2\n\t" \
  "s_lshl_b32 s82, s82, 13\n\t" \
  "s_and_b32 s80, s85, 1\n\t" \
  "s_lshl_b32 s80, s80, 12\n\t" \
  "s_add_i32 s82, s82, s80\n\t" \
  "s_add_i32 s82, s82, 0x84000\n\t" \
  "s_mov_b32 exec_hi, 0\n\t" \
  "buffer_load_dword v184, v200, %[rh], s82 offen sc1\n\t" \
  "buffer_load_dword v185, v200, %[rh], s52 offen sc1\n\t" \
  "s_mov_b64 exec, -1\n\t" \
  "s_waitcnt vmcnt(0)\n\t" \
  "s_bitcmp1_b32 s58, 2\n\t" \
  "s_cbranch_scc1 36f\n\t" \
  "v_max_u32 v148, v0, v1\n\t" \
  "v_max3_u32 v148, v148, v2, v3\n\t" \
  "v_max3_u32 v148, v148, v4, v5\n\t" \
  "v_max3_u32 v148, v148, v6, v7\n\t" \
  "v_max3_u32 v148, v148, v8, v9\n\t" \
  "v_max3_u32 v148, v148, v10, v11\n\t" \
  "v_max3_u32 v148, v148, v12, v13\n\t" \
  "v_max3_u32 v148, v148, v14, v15\n\t" \
  "v_max3_u32 v148, v148, v16, v17\n\t" \
  "v_max3_u32 v148, v148, v18, v19\n\t" \
  "v_max3_u32 v148, v148, v20, v21\n\t" \
  "v_max3_u32 v148, v148, v22, v23\n\t" \
  "v_max3_u32 v148, v148, v24, v25\n\t" \
  "v_max3_u32 v148, v148, v26, v27\n\t" \
  "v_max3_u32 v148, v148, v28, v29\n\t" \
  "v_max3_u32 v148, v148, v30, v31\n\t" \
  "v_max3_u32 v148, v148, v32, v33\n\t" \
  "v_max3_u32 v148, v148, v34, v35\n\t" \
  "v_max3_u32 v148, v148, v36, v37\n\t" \
  "v_max3_u32 v148, v148, v38, v39\n\t" \
  "v_max3_u32 v148, v148, v40, v41\n\t" \
  "v_max3_u32 v148, v148, v42, v43\n\t" \
  "v_max3_u32 v148, v148, v44, v45\n\t" \
  "v_max3_u32 v148, v148, v46, v47\n\t" \
  "v_max3_u32 v148, v148, v48, v49\n\t" \
  "v_max3_u32 v148, v148, v50, v51\n\t" \
  "v_max3_u32 v148, v148, v52, v53\n\t" \
  "v_max3_u32 v148, v148, v54, v55\n\t" \
  "v_max3_u32 v148, v148, v56, v57\n\t" \
  "v_max3_u32 v148, v148, v58, v59\n\t" \
  "v_max3_u32 v148, v148, v60, v61\n\t" \
  "v_max3_u32 v148, v148, v62, v63\n\t" \
  "v_max3_u32 v148, v148, v64, v65\n\t" \
  "v_max3_u32 v148, v148, v66, v67\n\t" \
  "v_max3_u32 v148, v148, v68, v69\n\t" \
  "v_max3_u32 v148, v148, v70, v71\n\t" \
  "v_max3_u32 v148, v148, v72, v73\n\t" \
  "v_max3_u32 v148, v148, v74, v75\n\t" \
  "v_max3_u32 v148, v148, v76, v77\n\t" \
  "v_max3_u32 v148, v148, v78, v79\n\t" \
  "v_max3_u32 v148, v148, v80, v81\n\t" \
  "v_max3_u32 v148, v148, v82, v83\n\t" \
  "v_max3_u32 v148, v148, v84, v85\n\t" \
  "v_max3_u32 v148, v148, v86, v87\n\t" \
  "v_max3_u32 v148, v148, v88, v89\n\t" \
  "v_max3_u32 v148, v148, v90, v91\n\t" \
  "v_max3_u32 v148, v148, v92, v93\n\t" \
  "v_max3_u32 v148, v148, v94, v95\n\t" \
  "v_max3_u32 v148, v148, v96, v97\n\t" \
  "v_max3_u32 v148, v148, v98, v99\n\t" \
  "v_max3_u32 v148, v148, v100, v101\n\t" \
  "v_max3_u32 v148, v148, v102, v103\n\t" \
  "v_max3_u32 v148, v148, v104, v105\n\t" \
  "v_max3_u32 v148, v148, v106, v107\n\t" \
  "v_max3_u32 v148, v148, v108, v109\n\t" \
  "v_max3_u32 v148, v148, v110, v111\n\t" \
  "v_max3_u32 v148, v148, v112, v113\n\t" \
  "v_max3_u32 v148, v148, v114, v115\n\t" \
  "v_max3_u32 v148, v148, v116, v117\n\t" \
  "v_max3_u32 v148, v148, v118, v119\n\t" \
  "v_max3_u32 v148, v148, v120, v121\n\t" \
  "v_max3_u32 v148, v148, v122, v123\n\t" \
  "v_max3_u32 v148, v148, v124, v125\n\t" \
  "v_max3_u32 v148, v148, v126, v127\n\t" \
  "v_cmp_eq_u32 vcc, -1, v148\n\t" \
  "s_cbranch_vccz 36f\n\t" \
  "s_add_u32 s59, s59, 0x1000000\n\t" \
  "s_cmp_lt_u32 s59, 0x10000000\n\t" \
  "s_cbranch_scc1 35b\n\t" \
  "s_or_b32 s58, s58, 9\n\t" \
  "s_branch 7f\n\t" \
  "36:\n\t" \
  "s_and_b32 s81, s40, 15\n\t" \
  "s_lshl_b32 s81, s81, 22\n\t" \
  "s_add_i32 s81, s81, s51\n\t" \
  "s_lshr_b32 s80, s81, 21\n\t" \
  "s_bitcmp0_b64 %[sel], s80\n\t" \
  "s_cselect_b32 s80, 0x2000, 0\n\t" \
  "s_add_i32 s81, s81, s80\n\t" \
  "buffer_load_dwordx4 a[128:131], v208, %[ru], s81 offen offset:0 sc1\n\t" \
  "buffer_load_dwordx4 a[132:135], v208, %[ru], s81 offen offset:1024 sc1\n\t" \
  "s_add_i32 s81, s81, 0x20000\n\t" \
  "buffer_load_dwordx4 a[136:139], v208, %[ru], s81 offen offset:0 sc1\n\t" \
  "buffer_load_dwordx4 a[140:143], v208, %[ru], s81 offen offset:1024 sc1\n\t" \
  "s_add_i32 s81, s81, 0x20000\n\t" \
  "buffer_load_dwordx4 a[144:147], v208, %[ru], s81 offen offset:0 sc1\n\t" \
  "buffer_load_dwordx4 a[148:151], v208, %[ru], s81 offen offset:1024 sc1\n\t" \
  "s_add_i32 s81, s81, 0x20000\n\t" \
  "buffer_load_dwordx4 a[152:155], v208, %[ru], s81 offen offset:0 sc1\n\t" \
  "buffer_load_dwordx4 a[156:159], v208, %[ru], s81 offen offset:1024 sc1\n\t" \
  "s_add_i32 s81, s81, 0x20000\n\t" \
  "buffer_load_dwordx4 a[160:163], v208, %[ru], s81 offen offset:0 sc1\n\t" \
  "buffer_load_dwordx4 a[164:167], v208, %[ru], s81 offen offset:1024 sc1\n\t" \
  "s_add_i32 s81, s81, 0x20000\n\t" \
  "buffer_load_dwordx4 a[168:171], v208, %[ru], s81 offen offset:0 sc1\n\t" \
  "buffer_load_dwordx4 a[172:175], v208, %[ru], s81 offen offset:1024 sc1\n\t" \
  "s_add_i32 s81, s81, 0x20000\n\t" \
  "buffer_load_dwordx4 a[176:179], v208, %[ru], s81 offen offset:0 sc1\n\t" \
  "buffer_load_dwordx4 a[180:183], v208, %[ru], s81 offen offset:1024 sc1\n\t" \
  "s_add_i32 s81, s81, 0x20000\n\t" \
  "buffer_load_dwordx4 a[184:187], v208, %[ru], s81 offen offset:0 sc1\n\t" \
  "buffer_load_dwordx4 a[188:191], v208, %[ru], s81 offen offset:1024 sc1\n\t" \
  "s_add_i32 s81, s81, 0x20000\n\t" \
  "buffer_load_dwordx4 a[192:195], v208, %[ru], s81 offen offset:0 sc1\n\t" \
  "buffer_load_dwordx4 a[196:199], v208, %[ru], s81 offen offset:1024 sc1\n\t" \
  "s_add_i32 s81, s81, 0x20000\n\t" \
  "buffer_load_dwordx4 a[200:203], v208, %[ru], s81 offen offset:0 sc1\n\t" \
  "buffer_load_dwordx4 a[204:207], v208, %[ru], s81 offen offset:1024 sc1\n\t" \
  "s_add_i32 s81, s81, 0x20000\n\t" \
  "buffer_load_dwordx4 a[208:211], v208, %[ru], s81 offen offset:0 sc1\n\t" \
  "buffer_load_dwordx4 a[212:215], v208, %[ru], s81 offen offset:1024 sc1\n\t" \
  "s_add_i32 s81, s81, 0x20000\n\t" \
  "buffer_load_dwordx4 a[216:219], v208, %[ru], s81 offen offset:0 sc1\n\t" \
  "buffer_load_dwordx4 a[220:223], v208, %[ru], s81 offen offset:1024 sc1\n\t" \
  "s_add_i32 s81, s81, 0x20000\n\t" \
  "buffer_load_dwordx4 a[224:227], v208, %[ru], s81 offen offset:0 sc1\n\t" \
  "buffer_load_dwordx4 a[228:231], v208, %[ru], s81 offen offset:1024 sc1\n\t" \
  "s_add_i32 s81, s81, 0x20000\n\t" \
  "buffer_load_dwordx4 a[232:235], v208, %[ru], s81 offen offset:0 sc1\n\t" \
  "buffer_load_dwordx4 a[236:239], v208, %[ru], s81 offen offset:1024 sc1\n\t" \
  "s_add_i32 s81, s81, 0x20000\n\t" \
  "buffer_load_dwordx4 a[240:243], v208, %[ru], s81 offen offset:0 sc1\n\t" \
  "buffer_load_dwordx4 a[244:247], v208, %[ru], s81 offen offset:1024 sc1\n\t" \
  "s_add_i32 s81, s81, 0x20000\n\t" \
  "buffer_load_dwordx4 a[248:251], v208, %[ru], s81 offen offset:0 sc1\n\t" \
  "buffer_load_dwordx4 a[252:255], v208, %[ru], s81 offen offset:1024 sc1\n\t" \
  "s_add_i32 s81, s81, 0x20000\n\t" \
  "s_waitcnt vmcnt(0) lgkmcnt(0)\n\t" \
  "v_accvgpr_mov_b32 a0, a128\n\t" \
  "v_accvgpr_mov_b32 a1, a136\n\t" \
  "v_accvgpr_mov_b32 a2, a130\n\t" \
  "v_accvgpr_mov_b32 a3, a138\n\t" \
  "v_accvgpr_mov_b32 a4, a129\n\t" \
  "v_accvgpr_mov_b32 a5, a137\n\t" \
  "v_accvgpr_mov_b32 a6, a131\n\t" \
  "v_accvgpr_mov_b32 a7, a139\n\t" \
  "v_accvgpr_mov_b32 a8, a132\n\t" \
  "v_accvgpr_mov_b32 a9, a140\n\t" \
  "v_accvgpr_mov_b32 a10, a134\n\t" \
  "v_accvgpr_mov_b32 a11, a142\n\t" \
  "v_accvgpr_mov_b32 a12, a133\n\t" \
  "v_accvgpr_mov_b32 a13, a141\n\t" \
  "v_accvgpr_mov_b32 a14, a135\n\t" \
  "v_accvgpr_mov_b32 a15, a143\n\t" \
  "v_accvgpr_mov_b32 a16, a144\n\t" \
  "v_accvgpr_mov_b32 a17, a152\n\t" \
  "v_accvgpr_mov_b32 a18, a146\n\t" \
  "v_accvgpr_mov_b32 a19, a154\n\t" \
  "v_accvgpr_mov_b32 a20, a145\n\t" \
  "v_accvgpr_mov_b32 a21, a153\n\t" \
  "v_accvgpr_mov_b32 a22, a147\n\t" \
  "v_accvgpr_mov_b32 a23, a155\n\t" \
  "v_accvgpr_mov_b32 a24, a148\n\t" \
  "v_accvgpr_mov_b32 a25, a156\n\t" \
  "v_accvgpr_mov_b32 a26, a150\n\t" \
  "v_accvgpr_mov_b32 a27, a158\n\t" \
  "v_accvgpr_mov_b32 a28, a149\n\t" \
  "v_accvgpr_mov_b32 a29, a157\n\t" \
  "v_accvgpr_mov_b32 a30, a151\n\t" \
  "v_accvgpr_mov_b32 a31, a159\n\t" \
  "v_accvgpr_mov_b32 a32, a160\n\t" \
  "v_accvgpr_mov_b32 a33, a168\n\t" \
  "v_accvgpr_mov_b32 a34, a162\n\t" \
  "v_accvgpr_mov_b32 a35, a170\n\t" \
  "v_accvgpr_mov_b32 a36, a161\n\t" \
  "v_accvgpr_mov_b32 a37, a169\n\t" \
  "v_accvgpr_mov_b32 a38, a163\n\t" \
  "v_accvgpr_mov_b32 a39, a171\n\t" \
  "v_accvgpr_mov_b32 a40, a164\n\t" \
  "v_accvgpr_mov_b32 a41, a172\n\t" \
  "v_accvgpr_mov_b32 a42, a166\n\t" \
  "v_accvgpr_mov_b32 a43, a174\n\t" \
  "v_accvgpr_mov_b32 a44, a165\n\t" \
  "v_accvgpr_mov_b32 a45, a173\n\t" \
  "v_accvgpr_mov_b32 a46, a167\n\t" \
  "v_accvgpr_mov_b32 a47, a175\n\t" \
  "v_accvgpr_mov_b32 a48, a176\n\t" \
  "v_accvgpr_mov_b32 a49, a184\n\t" \
  "v_accvgpr_mov_b32 a50, a178\n\t" \
  "v_accvgpr_mov_b32 a51, a186\n\t" \
  "v_accvgpr_mov_b32 a52, a177\n\t" \
  "v_accvgpr_mov_b32 a53, a185\n\t" \
  "v_accvgpr_mov_b32 a54, a179\n\t" \
  "v_accvgpr_mov_b32 a55, a187\n\t" \
  "v_accvgpr_mov_b32 a56, a180\n\t" \
  "v_accvgpr_mov_b32 a57, a188\n\t" \
  "v_accvgpr_mov_b32 a58, a182\n\t" \
  "v_accvgpr_mov_b32 a59, a190\n\t" \
  "v_accvgpr_mov_b32 a60, a181\n\t" \
  "v_accvgpr_mov_b32 a61, a189\n\t" \
  "v_accvgpr_mov_b32 a62, a183\n\t" \
  "v_accvgpr_mov_b32 a63, a191\n\t" \
  "v_accvgpr_mov_b32 a64, a192\n\t" \
  "v_accvgpr_mov_b32 a65, a200\n\t" \
  "v_accvgpr_mov_b32 a66, a194\n\t" \
  "v_accvgpr_mov_b32 a67, a202\n\t" \
  "v_accvgpr_mov_b32 a68, a193\n\t" \
  "v_accvgpr_mov_b32 a69, a201\n\t" \
  "v_accvgpr_mov_b32 a70, a195\n\t" \
  "v_accvgpr_mov_b32 a71, a203\n\t" \
  "v_accvgpr_mov_b32 a72, a196\n\t" \
  "v_accvgpr_mov_b32 a73, a204\n\t" \
  "v_accvgpr_mov_b32 a74, a198\n\t" \
  "v_accvgpr_mov_b32 a75, a206\n\t" \
  "v_accvgpr_mov_b32 a76, a197\n\t" \
  "v_accvgpr_mov_b32 a77, a205\n\t" \
  "v_accvgpr_mov_b32 a78, a199\n\t" \
  "v_accvgpr_mov_b32 a79, a207\n\t" \
  "v_accvgpr_mov_b32 a80, a208\n\t" \
  "v_accvgpr_mov_b32 a81, a216\n\t" \
  "v_accvgpr_mov_b32 a82, a210\n\t" \
  "v_accvgpr_mov_b32 a83, a218\n\t" \
  "v_accvgpr_mov_b32 a84, a209\n\t" \
  "v_accvgpr_mov_b32 a85, a217\n\t" \
  "v_accvgpr_mov_b32 a86, a211\n\t" \
  "v_accvgpr_mov_b32 a87, a219\n\t" \
  "v_accvgpr_mov_b32 a88, a212\n\t" \
  "v_accvgpr_mov_b32 a89, a220\n\t" \
  "v_accvgpr_mov_b32 a90, a214\n\t" \
  "v_accvgpr_mov_b32 a91, a222\n\t" \
  "v_accvgpr_mov_b32 a92, a213\n\t" \
  "v_accvgpr_mov_b32 a93, a221\n\t" \
  "v_accvgpr_mov_b32 a94, a215\n\t" \
  "v_accvgpr_mov_b32 a95, a223\n\t" \
  "v_accvgpr_mov_b32 a96, a224\n\t" \
  "v_accvgpr_mov_b32 a97, a232\n\t" \
  "v_accvgpr_mov_b32 a98, a226\n\t" \
  "v_accvgpr_mov_b32 a99, a234\n\t" \
  "v_accvgpr_mov_b32 a100, a225\n\t" \
  "v_accvgpr_mov_b32 a101, a233\n\t" \
  "v_accvgpr_mov_b32 a102, a227\n\t" \
  "v_accvgpr_mov_b32 a103, a235\n\t" \
  "v_accvgpr_mov_b32 a104, a228\n\t" \
  "v_accvgpr_mov_b32 a105, a236\n\t" \
  "v_accvgpr_mov_b32 a106, a230\n\t" \
  "v_accvgpr_mov_b32 a107, a238\n\t" \
  "v_accvgpr_mov_b32 a108, a229\n\t" \
  "v_accvgpr_mov_b32 a109, a237\n\t" \
  "v_accvgpr_mov_b32 a110, a231\n\t" \
  "v_accvgpr_mov_b32 a111, a239\n\t" \
  "v_accvgpr_mov_b32 a112, a240\n\t" \
  "v_accvgpr_mov_b32 a113, a248\n\t" \
  "v_accvgpr_mov_b32 a114, a242\n\t" \
  "v_accvgpr_mov_b32 a115, a250\n\t" \
  "v_accvgpr_mov_b32 a116, a241\n\t" \
  "v_accvgpr_mov_b32 a117, a249\n\t" \
  "v_accvgpr_mov_b32 a118, a243\n\t" \
  "v_accvgpr_mov_b32 a119, a251\n\t" \
  "v_accvgpr_mov_b32 a120, a244\n\t" \
  "v_accvgpr_mov_b32 a121, a252\n\t" \
  "v_accvgpr_mov_b32 a122, a246\n\t" \
  "v_accvgpr_mov_b32 a123, a254\n\t" \
  "v_accvgpr_mov_b32 a124, a245\n\t" \
  "v_accvgpr_mov_b32 a125, a253\n\t" \
  "v_accvgpr_mov_b32 a126, a247\n\t" \
  "v_accvgpr_mov_b32 a127, a255\n\t" \
  "s_and_b32 s58, s58, 4\n\t" \
  "s_or_b32 s58, s58, 32\n\t" \
  "s_branch 31b\n\t" \
  "18:\n\t" \
  "v_mov_b32 v186, -1\n\t" \
  "s_and_b32 s80, s40, 3\n\t" \
  "s_mul_i32 s77, s80, 0xc000\n\t" \
  "s_add_i32 s77, s77, 0x40000\n\t" \
  "s_lshl_b32 s78, s80, 14\n\t" \
  "s_add_i32 s78, s78, 0x70000\n\t" \
  "s_mov_b32 s57, 0\n\t" \
  "s_add_i32 s96, s77, 0x1000\n\t" \
  "s_and_b32 s98, s40, 2\n\t" \
  "s_lshl_b32 s98, s98, 13\n\t" \
  "s_and_b32 s80, s40, 1\n\t" \
  "s_lshl_b32 s80, s80, 12\n\t" \
  "s_add_i32 s98, s98, s80\n\t" \
  "s_add_i32 s98, s98, 0x84000\n\t" \
  "12:\n\t" \
  "s_or_b32 s58, s58, 3\n\t" \
  "11:\n\t" \
  "buffer_load_dwordx4 v[0:3], v193, %[rh], s77 offen offset:0 sc1\n\t" \
  "buffer_load_dwordx4 v[4:7], v193, %[rh], s77 offen offset:1024 sc1\n\t" \
  "buffer_load_dwordx4 v[8:11], v193, %[rh], s77 offen offset:2048 sc1\n\t" \
  "buffer_load_dwordx4 v[12:15], v193, %[rh], s77 offen offset:3072 sc1\n\t" \
  "buffer_load_dwordx4 v[16:19], v193, %[rh], s96 offen offset:0 sc1\n\t" \
  "buffer_load_dwordx4 v[20:23], v193, %[rh], s96 offen offset:1024 sc1\n\t" \
  "buffer_load_dwordx4 v[24:27], v193, %[rh], s96 offen offset:2048 sc1\n\t" \
  "buffer_load_dwordx4 v[28:31], v193, %[rh], s96 offen offset:3072 sc1\n\t" \
  "buffer_load_dwordx4 v[32:35], v195, %[rh], s77 offen offset:0 sc1\n\t" \
  "buffer_load_dwordx4 v[36:39], v195, %[rh], s77 offen offset:2048 sc1\n\t" \
  "v_mov_b64 v[40:41], 0\n\t" \
  "v_mov_b64 v[42:43], 0\n\t" \
  "v_mov_b64 v[44:45], 0\n\t" \
  "v_mov_b64 v[46:47], 0\n\t" \
  "v_mov_b64 v[48:49], 0\n\t" \
  "v_mov_b64 v[50:51], 0\n\t" \
  "v_mov_b64 v[52:53], 0\n\t" \
  "v_mov_b64 v[54:55], 0\n\t" \
  "v_mov_b64 v[56:57], 0\n\t" \
  "v_mov_b64 v[58:59], 0\n\t" \
  "v_mov_b64 v[60:61], 0\n\t" \
  "v_mov_b64 v[62:63], 0\n\t" \
  "v_mov_b64 v[64:65], 0\n\t" \
  "v_mov_b64 v[66:67], 0\n\t" \
  "v_mov_b64 v[68:69], 0\n\t" \
  "v_mov_b64 v[70:71], 0\n\t" \
  "v_mov_b64 v[72:73], 0\n\t" \
  "v_mov_b64 v[74:75], 0\n\t" \
  "v_mov_b64 v[76:77], 0\n\t" \
  "v_mov_b64 v[78:79], 0\n\t" \
  "v_mov_b64 v[80:81], 0\n\t" \
  "v_mov_b64 v[82:83], 0\n\t" \
  "v_mov_b64 v[84:85], 0\n\t" \
  "v_mov_b64 v[86:87], 0\n\t" \
  "v_mov_b64 v[88:89], 0\n\t" \
  "v_mov_b64 v[90:91], 0\n\t" \
  "v_mov_b64 v[92:93], 0\n\t" \
  "v_mov_b64 v[94:95], 0\n\t" \
  "v_mov_b64 v[96:97], 0\n\t" \
  "v_mov_b64 v[98:99], 0\n\t" \
  "v_mov_b64 v[100:101], 0\n\t" \
  "v_mov_b64 v[102:103], 0\n\t" \
  "v_mov_b64 v[136:137], 0\n\t" \
  "v_mov_b64 v[138:139], 0\n\t" \
  "v_mov_b64 v[140:141], 0\n\t" \
  "v_mov_b64 v[142:143], 0\n\t" \
  "s_waitcnt vmcnt(0)\n\t" \
  "s_add_u32 s59, s59, 1\n\t" \
  "s_bitcmp1_b32 s58, 2\n\t" \
  "s_cbranch_scc1 10b\n\t" \
  "v_max_u32 v148, v0, v1\n\t" \
  "v_max3_u32 v148, v148, v2, v3\n\t" \
  "v_max3_u32 v148, v148, v4, v5\n\t" \
  "v_max3_u32 v148, v148, v6, v7\n\t" \
  "v_max3_u32 v148, v148, v8, v9\n\t" \
  "v_max3_u32 v148, v148, v10, v11\n\t" \
  "v_max3_u32 v148, v148, v12, v13\n\t" \
  "v_max3_u32 v148, v148, v14, v15\n\t" \
  "v_max3_u32 v148, v148, v16, v17\n\t" \
  "v_max3_u32 v148, v148, v18, v19\n\t" \
  "v_max3_u32 v148, v148, v20, v21\n\t" \
  "v_max3_u32 v148, v148, v22, v23\n\t" \
  "v_max3_u32 v148, v148, v24, v25\n\t" \
  "v_max3_u32 v148, v148, v26, v27\n\t" \
  "v_max3_u32 v148, v148, v28, v29\n\t" \
  "v_max3_u32 v148, v148, v30, v31\n\t" \
  "v_max3_u32 v148, v148, v32, v33\n\t" \
  "v_max3_u32 v148, v148, v34, v35\n\t" \
  "v_max3_u32 v148, v148, v36, v37\n\t" \
  "v_max3_u32 v148, v148, v38, v39\n\t" \
  "v_cmp_eq_u32 vcc, -1, v148\n\t" \
  "s_cbranch_vccz 10b\n\t" \
  "s_bitcmp1_b32 s59, 6\n\t" \
  "s_cbranch_scc0 11b\n\t" \
  "7:\n\t" \
  "92:\n\t" \
  "s_mov_b32 s42, s58\n\t" \
  "s_waitcnt vmcnt(0) lgkmcnt(0)\n\t" \
  ""

#define V53 "v0", "v1", "v2", "v3", "v4", "v5", "v6", "v7", "v8", "v9", "v10", "v11", "v12", "v13", "v14", "v15", "v16", "v17", \
  "v18", "v19", "v20", "v21", "v22", "v23", "v24", "v25", "v26", "v27", "v28", "v29", "v30", "v31", "v32", "v33", "v34", "v35", \
  "v36", "v37", "v38", "v39", "v40", "v41", "v42", "v43", "v44", "v45", "v46", "v47", "v48", "v49", "v50", "v51", "v52"
#define V100 V53, "v53", "v54", "v55", "v56", "v57", "v58", "v59", "v60", "v61", "v62", "v63", "v64", "v65", "v66", "v67", "v68", "v69", "v70", \
  "v71", "v72", "v73", "v74", "v75", "v76", "v77", "v78", "v79", "v80", "v81", "v82", "v83", "v84", "v85", "v86", "v87", "v88", \
  "v89", "v90", "v91", "v116", "v117", "v118", "v119"
#define STEP_PV
#define CB_CL "s60", "s61", "s62", "s63", "s64", "s65", "s66", "s67", "s68", "s69",
#define STEPS(BODY, ...) asm volatile(BODY : "=&{v[92:99]}"(X), "+{s[40:55]}"(P) STEP_PV \
  : "{v[120:151]}"(W.v[0]), "{v[152:183]}"(W.v[1]), "{v[184:215]}"(W.v[2]), "{v[216:247]}"(W.v[3]), "{v[100:115]}"(C), [rh] "s"(rh), [wv] "s"(w), [ru] "s"(rum), [sel] "s"(sel) __VA_ARGS__ \
  : "memory", "vcc", "scc", "s56", "s57", "s58", "s59", "s76", "s77", "s78", "s79", "s80", "s81", "s82", "s83", "s84", "s85", "s86", "s87", "s88", "s89", \
    "s90", "s91", "s92", "s93", "s94", "s95", "s96", "s97", V100)
__device__ __forceinline__ void keep_a(const Wts& W) {
#pragma unroll
  for (int i = 0; i < 32; ++i) asm volatile("" :: "a"(W.a[i]));
}
__device__ __forceinline__ char* lds_base(char* smem, int off) { asm volatile("" : "+v"(off)); return smem + off; }
__device__ __forceinline__ u32x2 tr4(unsigned p) { return __builtin_bit_cast(u32x2, __builtin_amdgcn_ds_read_tr16_b64_v4i16((LAS bf16x4*)(uintptr_t)p)); }
__device__ __forceinline__ float rb1(float x) { return __uint_as_float(pk2(0.f, x)); }

__device__ __forceinline__ void steps_init(u32x16& P, int h, int w, unsigned place) {
#pragma unroll
  for (int i = 0; i < 16; ++i) P[i] = 0;
  P[2] = 1; P[3] = CDUR; P[14] = H_MEM + (w >> 1) * CHK + (w & 1) * 4096 + (h & RHO) * 1024; P[15] = place;
}
__device__ __forceinline__ void steps_enter(u32x16& P, Clk& ck, bool dead) {
  ck.sync();
  P[2] = (P[2] & 3u) | (dead ? 4u : 0u); P[6] = ck.c2t << 15; P[11] = ck.ce; P[12] = ck.t2c; P[13] = ck.te;
#pragma unroll
  for (int i = 0; i < 16; ++i) P[i] = __builtin_amdgcn_readfirstlane(P[i]);
}

__device__ __forceinline__ void role_qk(const ChainArgs& a, int h, char* smem, const int w, Gate& G) {
  int lane = opaque_tid(0);
  const int b = h; (void)b;
  u64* prof = a.prof; (void)prof;
  PDECL(L_PRF, 4);
  const float CS = 0.08838834764831845f * 1.4426950408889634f;
  GAS unsigned* abortw = (GAS unsigned*)(a.sync + SY_ABORT);
  bool dead = false;
  Wts W;
  Clk ck; ck.init();
  load_weights<0>(a, h, w, lane, smem, W);
  if (!G.pass(a.sync)) return;
  PZERO;
  const Sel sel = G.sel;
  start_wait(a.sync, abortw, lane, dead, CT_K + h, CT_Q + h);
  const __amdgpu_buffer_rsrc_t rh = rsrc_hot(a.sync, sel), rum = rsrc_u(a.sync);
#pragma unroll
  for (int i = 0; i < 2; ++i) *(u32x4*)(smem + L_KX + (2 * w + i) * 1024 + lane * 16) = ld16(rum, x_slot(1, 0, h, sel) + (2 * w + i) * 1024 + lane * 16);
  auto pm_o = [&](int l) { return 2 * CHK + h * 256 + (l & 1) * 128 + (l >> 3) * 16 + (l & 6) * 2; };
  auto fac = [&](int l, int hf) { return ((l & 7) >> 1) == 2 * (l >> 5) + ((h >> 1) & 1) && ((l >> 3) & 1) == hf ? 1.f : 0.f; };
  u32x16 C, P;
  {
    const int r = lane & 15, g = lane >> 4;
    const unsigned l0 = (unsigned)(uintptr_t)(LAS char*)smem;
    C[0] = lane * 16; C[1] = l0 + L_MEM + lane * 16; C[2] = l0 + L_WTS + w * 4096 + lane * 16; C[3] = C[2] + 65536;
    C[4] = (lane & 2) ? 0xEEEE : 0x4444;
    C[5] = l0 + L_XCH + (16 * w + 2 * (r >> 2) + (r & 1)) * 32 + g * 8 + ((r >> 1) & 1) * 4;
    C[6] = l0 + L_XK + (32 * w + r) * 16 + g * 4;
    C[7] = w == 3 ? H_PX + (h >> 3) * CHK + (h & 7) * 1024 + lane * 16 : w ? (2 * h + 1) * CHK + (w - 1) * 4096 + lane * 16 : l0 + L_XCH + (8 * g + (r >> 2)) * 32 + (r & 3) * 8;
    C[8] = l0 + L_XK + (8 * g + (r >> 2)) * 16 + (r & 1) * 8;
    C[9] = w ? (lane & 1) * 4096 : (lane == 0 ? H_ANN + h * 128 : 0x7fffffffu);
    C[10] = w == 0 ? (h >> 4) * CHK + (h & 15) * 512 + ((lane >> 1) & 1) * 256 + (lane >> 5) * 128 + (lane & 1) * 64 + ((lane >> 3) & 3) * 16 + ((lane >> 2) & 1) * 8 :
            w < 3 ? H_PX + ((w - 1) * 4 + (h >> 3)) * CHK + (h & 7) * 1024 + lane * 16 :
            lane < 32 ? (h >> 4) * CHK + (h & 15) * 512 + lane * 16 : (lane < 48 ? 2 * CHK + h * 256 + (lane - 32) * 16 : 0x7fffffffu);
    C[11] = w == 0 ? pm_o(lane) : (lane < (w == 3 ? 32 : 16) ? H_PX + (w == 3 ? 8 + g : 7 + w) * CHK + hpos(h) * 256 + r * 16 : 0x7fffffffu);
    C[12] = H_TR + (lane & 31) * 128; C[13] = l0 + L_PRF + w * 1024 + (lane >> 6);
    unsigned mk = 0;
#pragma unroll
    for (int i = 0; i < 8; ++i) mk |= (16 * ((i >> 1) & 1) + 8 * (g & 1) + 4 * (g >> 1) + 2 * (i & 1) + (i >> 2) > 16 * (w - 1) + r ? 1u : 0u) << i;
    C[14] = w ? mk : __float_as_uint(fac(lane, 0)); C[15] = __float_as_uint(fac(lane, 1));
    asm volatile("" : "+v"(C));
  }
  SSTAMP(96 + h);
  steps_init(P, h, w, 2 * h * CHK + (3 * w - 3) * 1024);
  GAS unsigned* kcp = ctr(a, CT_Q + h);
  unsigned spins = 0;
  while (true) {
    u32x8 X; u32x16 pv; (void)pv;
    steps_enter(P, ck, dead);
    STEPS(QK_BODY, , [cs] "s"(CS), [kc] "s"(kcp));
    keep_a(W);
    const unsigned res = __builtin_amdgcn_readfirstlane(P[2]), s = __builtin_amdgcn_readfirstlane(P[4]);
    lane = opaque_tid(0);
    if (res & 8u) {
      auto sw = [](float v, int k) { return __int_as_float(k == 0 ? __builtin_amdgcn_mov_dpp(__float_as_int(v), 0xB1, 0xf, 0xf, false) :
                                                           k == 1 ? __builtin_amdgcn_mov_dpp(__float_as_int(v), 0x4E, 0xf, 0xf, false) :
                                                                    __builtin_amdgcn_mov_dpp(__float_as_int(v), 0x141, 0xf, 0xf, false)); };
      float x[5], d = fmaxf(__uint_as_float(X[4]), 0.f);
      d = fmaxf(d, sw(d, 0)); d = fmaxf(d, sw(d, 1)); d = fmaxf(d, sw(d, 2));
#pragma unroll
      for (int i = 0; i < 5; ++i) x[i] = __builtin_amdgcn_exp2f(__uint_as_float(X[i]) - d);
      float z = ((x[0] + x[1]) + (x[2] + x[3])) + x[4];
      z += sw(z, 0); z += sw(z, 1); z += sw(z, 2);
      z = __builtin_amdgcn_rcpf(z);
      const float y = sw(x[4], 0), f = __builtin_amdgcn_exp2f(-d) * z;
      const unsigned pm = (lane & 1) ? pk2(fac(lane, 0) * f, fac(lane, 1) * f) : pk2(x[4] * z, y * z);
      if (!(__uint_as_float(X[5]) < 1.0e18f))
        __builtin_amdgcn_raw_buffer_store_b32(nosent(pm), rh, pm_o(lane), H_P + ((s - 1) & 3) * 3 * CHK, ST_AUX);
    }
    if (res & 64u) break;
    if (res & 32u) { ctr_wait(kcp, min(s + 2, (unsigned)NSEG), abortw, lane, dead); ctr_wait(kcp + 1024, min(s + 2, (unsigned)NSEG), abortw, lane, dead); }
    if ((res & 32u) && s == 1) {
      for (int i = 3 * w - 3; i < min(3 * w, 8); ++i) *(u32x4*)(smem + L_KX + i * 1024 + lane * 16) = ld16(rum, x_slot(1, 1, h, sel) + i * 1024 + lane * 16);
    }
    if (res & 17u) {
      spins |= 63u;
      if (dead || poll_fail(spins, abortw, lane)) dead = true;
    }
  }
  PDUMP(0);
}

__device__ __forceinline__ void role_vo(const ChainArgs& a, int h, char* smem, const int w, Gate& G) {
  int lane = opaque_tid(0);
  const int b = 32 + h; (void)b;
  u64* prof = a.prof; (void)prof;
  PDECL(L_PRF, 4);
  GAS unsigned* abortw = (GAS unsigned*)(a.sync + SY_ABORT);
  bool dead = false;
  Wts W;
  Clk ck; ck.init();
  load_weights<1>(a, h, w, lane, smem, W);
  if (!G.pass(a.sync)) return;
  PZERO;
  const Sel sel = G.sel;
  start_wait(a.sync, abortw, lane, dead, CT_INIT, CT_INIT);
  const __amdgpu_buffer_rsrc_t rh = rsrc_hot(a.sync, sel), rum = rsrc_u(a.sync);
  u32x16 C, P;
  {
    const int r = lane & 15, g = lane >> 4;
    const unsigned l0 = (unsigned)(uintptr_t)(LAS char*)smem;
#pragma unroll
    for (int i = 0; i < 16; ++i) C[i] = H_TR + (lane & 31) * 128;
    C[0] = lane * 16; C[1] = l0 + L_MEM + lane * 16; C[2] = l0 + L_WTS + w * 4096 + lane * 16; C[3] = C[2] + 65536;
    C[4] = (lane & 2) ? 0xEEEE : 0x4444;
    C[5] = l0 + L_XCH + (32 * w + r) * 16 + g * 4;
    C[6] = (lane & 8) ? 0xEEEE : 0x4444;
    C[7] = l0 + L_XCH + (32 * (g >> 1) + 8 * (g & 1) + (r >> 2)) * 16 + (r & 1) * 8;
    C[8] = ((g & 1) * 8 + (g >> 1)) * CHK + r * 16;
    C[13] = l0 + L_PRF + w * 1024 + (lane >> 6);
    asm volatile("" : "+v"(C));
  }
  SSTAMP(128 + h);
  steps_init(P, h, w, SY_UM + (16 * (h & 3) + 2 * w) * CHK + hpos(h) * 256);
  unsigned spins = 0;
  while (true) {
    u32x8 X; u32x16 pv; (void)pv;
    steps_enter(P, ck, dead);
    STEPS(VO_BODY);
    keep_a(W);
    lane = opaque_tid(0);
    if (__builtin_amdgcn_readfirstlane(P[2]) & 64u) break;
    spins |= 63u;
    if (dead || poll_fail(spins, abortw, lane)) dead = true;
  }
  PDUMP(0);
}

__device__ __forceinline__ void calibrate(const ChainArgs& a, int cal, int slot, u64 entry) {
  const u32x4 rs = own_rsrc(a.sync, SY_BYTES);
  unsigned best[2] = {1u << 30, 1u << 30};
  for (int i = 0;; ++i) {
    if (i >= CALN && ((min(best[0], best[1]) < CALT && max(best[0], best[1]) - min(best[0], best[1]) > 120u) || __builtin_amdgcn_s_memrealtime() - entry > CALM)) break;
#pragma unroll
    for (int hf = 0; hf < 2; ++hf) {
      u64 c0, c1;
      unsigned z = 0;
      asm volatile("s_mov_b64 exec, 1\n\ts_memtime %0\n\ts_waitcnt lgkmcnt(0)\n\tbuffer_atomic_or %2, off, %3, %4 sc0 sc1\n\ts_waitcnt vmcnt(0)\n\ts_memtime %1\n\ts_mov_b64 exec, -1\n\ts_waitcnt lgkmcnt(0)"
                   : "=&s"(c0), "=&s"(c1), "+v"(z) : "s"(rs), "s"(cal * PAGE + (slot >> 1) * CHK + hf * 8192 + (slot & 1) * 4096) : "memory");
      best[hf] = min(best[hf], (unsigned)(c1 - c0));
    }
  }
  const u64 hs = (__builtin_amdgcn_s_memrealtime() & ~1ull) | ((best[1] < best[0] ? 1u : 0u) ^ die_half());
  if (opaque_tid(0) == 0) __hip_atomic_store((GAS u64*)ctr(a, CT_CAL + (slot >> 1)) + (slot & 1), hs, __ATOMIC_RELAXED, __HIP_MEMORY_SCOPE_AGENT);
}
__device__ __forceinline__ u32x4 pk8(f32x4 a, f32x4 b) { return (u32x4){pk2(a[0], a[1]), pk2(a[2], a[3]), pk2(b[0], b[1]), pk2(b[2], b[3])}; }
__device__ __forceinline__ unsigned pk2v(float a, float b) { unsigned r; asm volatile("v_cvt_pk_bf16_f32 %0, %1, %2" : "=v"(r) : "v"(a), "v"(b)); return r; }
__device__ __forceinline__ u32x4 pk8v(f32x4 a, f32x4 b) { asm volatile("s_nop 7"); return (u32x4){pk2v(a[0], a[1]), pk2v(a[2], a[3]), pk2v(b[0], b[1]), pk2v(b[2], b[3])}; }
__device__ __forceinline__ void st16g(u16* p, u32x4 v) { asm volatile("global_store_dwordx4 %0, %1, off " OUTP "\n\ts_nop 1" :: "v"(p), "v"(v) : "memory"); }
__device__ __forceinline__ void role_cb(const ChainArgs& a, int cbi, char* smem, const int w, Gate& G) {
  int lane = opaque_tid(0);
  const int b = 64 + cbi; (void)b;
  const int par = cbi >> 5, ci = cbi & 31, J = ci >> 3;
  u64* prof = a.prof; (void)prof;
  PDECL(65536, 8); PZERO;
  GAS unsigned* abortw = (GAS unsigned*)(a.sync + SY_ABORT);
  bool dead = false;
  auto pf = [&](int n0, int n1) {
    for (int n = n0; n < n1; ++n) {
      const int blk = m_block(par, n & 1, ((par & n ? W_HELP2 : W_HELP) >> (4 * (n >> 1))) & 15);
      const bool wo = par && blk >= 16;
      const u16* base = wo ? a.Wo + (128 * ci + 64 * (blk & 1)) * DM + 128 * (((blk - 16) >> 1) ^ (2 * (ci & 3)))
                           : (par ? a.Wv : (blk & 1 ? a.Wk : a.Wq)) + 64 * ((par ? blk : blk >> 1) ^ (ci & RHO)) * HD + 128 * ci;
#pragma unroll
      for (int j = 0; j < 4; ++j)
        __builtin_amdgcn_global_load_lds((const GAS void*)(base + ((w * 4 + j) * 4 + (lane >> 4)) * (wo ? DM : HD) + (lane & 15) * 8), (LAS void*)(smem + 32768 + (w * 4 + j) * 1024), 16, 0, (W_NT & 4) / 2);
      wait_vm<24>();
    }
    wait_vm<0>();
  };
  auto sweep = [&](auto n0, auto n1) {
    const __amdgpu_buffer_rsrc_t rx = mk_rsrc((void*)a.X, 4u << 20);
    u32x4 v[16];
#pragma unroll
    for (int i = n0; i < n1; ++i) v[i] = __builtin_amdgcn_raw_buffer_load_b128(rx, (unsigned)(cbi * 4096 + opaque_tid(w) * 16), (unsigned)(i * 262144), 0);
#pragma unroll
    for (int i = n0; i < n1; ++i) asm volatile("" :: "v"(v[i]));
  };
  pf(0, PF1);
  SSTAMP(224 + ci);
  if (!G.pass(a.sync)) return;
  const Sel sel = G.sel;
  SSTAMP(64 + ci);
  const __amdgpu_buffer_rsrc_t rh = rsrc_hot(a.sync, sel), rum = rsrc_u(a.sync);
  const int tw = 2 * ci + (w & 1);
  const unsigned mo = (tw >> 5) * CHK + (tw & 31) * 256;
  {
    const unsigned s1v = ~(unsigned)(lane >> 6);
    const u32x4 sv = {s1v, s1v, s1v, s1v};
#pragma unroll
    for (int i = 0; i < 2; ++i) {
      const unsigned o = w * 2048 + i * 1024 + lane * 16;
#pragma unroll
      for (int k = 0; k < 4; ++k) st16(rum, near_half(SY_UM + (64 * k + cbi) * CHK, sel) + o, sv);
      if (cbi >= 2 && cbi < 35) st16(rh, cbi * CHK + o, sv);
      if ((cbi & 15) < 10) st16(rh, H_PX + cbi * CHK + o, sv);
    }
    if (par == 0 && w < 2) {
      const u16* m = a.mem0 + (2 * (lane >> 4) + ((lane >> 2) & 1)) * DM + 16 * tw + (lane & 8);
      st4(rh, H_MEM + mo + lane * 4, nosent(m[lane & 3] | (unsigned)m[7 - (lane & 3)] << 16));
    }
    asm volatile("s_waitcnt vmcnt(0)" ::: "memory");
    lds_barrier();
    if (w == 0 && lane == 0) ctr_add(a, CT_INIT, 1u);
    SSTAMP(256 + ci);
  }
  sweep(std::integral_constant<int, 0>{}, std::integral_constant<int, 2>{});
  pf(PF1, PF1 + PF2);
  sweep(std::integral_constant<int, 2>{}, std::integral_constant<int, 16>{});
  Wts W;
#pragma unroll
  for (int i = 0; i < 32; ++i) asm volatile("" : "=a"(W.a[i]));
  u32x16 C, P; u32x8 C2;
  {
    const unsigned l0 = (unsigned)(uintptr_t)(LAS char*)smem, oor = 0x7fffffffu;
    const int r = lane & 7, t = (lane >> 3) & 1, g = lane >> 4, e = w & 1;
    const bool L = w < 2;
    C[0] = lane * 16;
    C[1] = L ? l0 + 32768 + e * 8192 + (lane & 2) * 512 + ((lane & 1) * 32 + (lane >> 2)) * 16 : e * CHK + t * 512 + (g & 1) * 256 + (r >> 2) * 128 + (g >> 1) * 64 + (r & 3) * 16;
    C[2] = L ? (4 * t + g) * 1024 + r * 16 : l0 + 81920 + e * 32768 + lane * 16;
    C[3] = L ? l0 + e * 1024 + lane * 8 : 2 * CHK + e * 4096 + (4 * (g >> 1) + 2 * t + (g & 1)) * 256 + 128 + r * 16;
    C[4] = ((16 * e + (lane >> 2)) * OUTW + DM + 32 * ci + 8 * (lane & 3)) * 2;
    C[5] = l0 + (L ? e * 8192 + lane * 16 : e * 512 + lane * 8); C[6] = l0 + 16384 + lane * 16; C[7] = l0 + 32768 + (L ? e * 8192 + 4096 : e * 2048) + lane * 16;
    C[8] = (lane & 31) * 128;
    C[9] = (8 * (g & 1) + 4 * (g >> 1) + 2 * t + (r >> 2)) * 16 + 4 * (r < 4 ? r : 7 - r);
    C[10] = (lane | e) ? oor : ci * 128;
    C[11] = L ? SY_CT + (CT_U + 16 * e + (lane & 15)) * 128 : (t ? 0xEEEE : 0x4444); C[12] = L ? l0 + 81920 + e * 32768 + lane * 16 : ((lane & 2) ? 0xEEEE : 0x4444); C[13] = r < 4 ? 0x01000504u : 0x07060302u;
    C[14] = l0 + 49152 + 32 * e + (lane >> 6); C[15] = l0 + 49152 + (lane >> 6);
    C2[0] = L ? (lane < 16 ? lane * 16 : oor) : ux_tile(2 * ci) + lane * 16;
    C2[1] = lane == 0 && e && ci < 8 ? SY_CT + (CT_DONE + 8 * par + ci) * 128 : oor;
    C2[2] = (L ? ux_tile(2 * ci) : H_PX + 2 * e * CHK) + lane * 16;
#pragma unroll
    for (int n = 0; n < 4; ++n) C2[3 + n] = L ? H_PX + (8 + e) * CHK + (J ^ n) * 2048 + lane * 16 : C2[2] + CHK;
    C2[7] = l0 + 65536 + w * 2048 - 64 + (lane >> 6);
    asm volatile("" : "+v"(C), "+v"(C2));
#pragma unroll
    for (int i = 0; i < 16; ++i) P[i] = par - 2;
    P[2] = 48; P[6] = P[7] = K_DEF * TU; P[8] = SY_UM + tw * CHK + J * 2048; P[9] = H_MEM + mo; P[10] = 2 * CHK + J * 256; P[11] = e * 16 * UXH; P[12] = H_DLT + par * 4096; P[13] = NSEG - 2;
  }
  Clk ck; ck.init();
  SSTAMP(0 + ci);
  unsigned spins = 0;
  const u32x16 P0 = P;
  bool dry = DRY;
  if (dry) { P[0] = NSEG - 1; P[2] = 0; P[13] = NSEG; }
  else ctr_wait(ctr(a, CT_INIT), 64u, abortw, lane, dead);
  while (true) {
    u32x16 pv; (void)pv;
    unsigned res = __builtin_amdgcn_readfirstlane(P[2]);
    if (res & 64u) {
      if (!dry) break;
      dry = false; P = P0; res = 48;
      ctr_wait(ctr(a, CT_INIT), 64u, abortw, opaque_tid(0), dead);
    }
    const u32x4 rh2 = own_rsrc(a.sync + near_half(SY_HOT, sel), dry ? 0 : H_BYTES - 8192), ra = own_rsrc(a.sync, dry ? 0 : SY_BYTES), ro = own_rsrc(a.out, dry ? 0 : S_LEN * OUTW * 2);
    if (res & 1u) {
      spins |= 63u;
      if (dead || poll_fail(spins, abortw, opaque_tid(0))) dead = true;
    }
    ck.sync();
    P[2] = (res & 58u) | (dead || dry ? 4u : 0u); P[1] = ck.ce; P[3] = ck.te; P[4] = ck.t2c; P[5] = ck.c2t << 15;
#pragma unroll
    for (int i = 0; i < 16; ++i) P[i] = __builtin_amdgcn_readfirstlane(P[i]);
    asm volatile(CB_BODY : "+{s[40:55]}"(P) STEP_PV
                 : "{v[192:207]}"(C), "{v[208:215]}"(C2), [rh] "s"(rh2), [ru] "s"(ra), [ro] "s"(ro), [wv] "s"(w), [sel] "s"(sel)
                 : "memory", "vcc", "scc", CB_CL "s56", "s57", "s58", "s59", "s76", "s77", "s78", "s79", "s80", "s81", "s82", "s83", "s84", "s85", "s86", "s87", "s88", "s89", "s90", "s91", "s92", "s93", "s94", "s95", "s96", "s97", "s98", "s99", "s100", "s101", V53, "v53", "v54", "v55", "v56", "v57", "v58", "v59", "v60", "v61", "v62", "v63", "v64", "v65", "v66", "v67", "v68", "v69", "v70", "v71", "v72", "v73", "v74", "v75", "v76", "v77", "v78", "v79", "v80", "v81", "v82", "v83", "v84", "v85", "v86", "v87", "v88", "v89", "v90", "v91", "v92", "v93", "v94", "v95", "v96", "v97", "v98", "v99", "v100", "v101", "v102", "v103", "v104", "v105", "v106", "v107", "v108", "v109", "v110", "v111", "v112", "v113", "v114", "v115", "v116", "v117", "v118", "v119", "v120", "v121", "v122", "v123", "v124", "v125", "v126", "v127", "v128", "v129", "v130", "v131", "v132", "v133", "v134", "v135", "v136", "v137", "v138", "v139", "v140", "v141", "v142", "v143", "v144", "v145", "v146", "v147", "v148", "v149", "v150", "v151", "v152", "v153", "v154", "v155", "v156", "v157", "v158", "v159", "v160", "v161", "v162", "v163", "v164", "v165", "v166", "v167", "v168", "v169", "v170", "v171", "v172", "v173", "v174", "v175", "v176", "v177", "v178", "v179", "v180", "v181", "v182", "v183", "v184", "v185", "v186", "v187", "v188", "v189", "v190", "v191");
    keep_a(W);
  }
  PDUMP(2);
}

__device__ __forceinline__ void bg_drain() {
  asm volatile("s_waitcnt vmcnt(0)" ::: "memory");
  __builtin_amdgcn_s_barrier();
  asm volatile("" ::: "memory");
}
#define BG_SLP 16
#define A3_SLP 16
#define WAH 4
#define DSP 4
#define SIGQ 2
__device__ __forceinline__ void task_w(const ChainArgs& a, int kind, int h, char* smem, int wv, Gate& G) {
  const int lane = opaque_tid(0), w = wv, r = lane & 15, g = lane >> 4;
  const __amdgpu_buffer_rsrc_t rw = mk_rsrc(kind == 0 ? a.Wq : (kind == 1 ? a.Wk : a.Wv), 8u << 20), rx = mk_rsrc(a.X, 4u << 20);
  const unsigned lds0 = (unsigned)(uintptr_t)(LAS char*)smem + w * 4096;
  unsigned vx[2], vw[4];
#pragma unroll
  for (int j = 0; j < 2; ++j) {
    const int rr = 8 * j + (lane >> 3);
    const int tb = kind == 0 ? rr : (kind == 1 ? 16 * ((rr >> 1) & 1) + 8 * ((rr >> 2) & 1) + 4 * (rr >> 3) + 2 * (rr & 1) : 8 * (rr >> 2) + (rr & 3));
    vx[j] = tb * DM * 2 + ((lane & 7) ^ (rr >> 1)) * 16;
  }
#pragma unroll
  for (int j = 0; j < 4; ++j) { const int row = (w * 4 + j) * 4 + g; vw[j] = row * HD * 2 + (r ^ swz_b(row)) * 16; }
  const u64 wo = kind ? bg_order(1) : bg_order(0);
  auto xo = [&](int q) { return ((int)((wo >> (16 * q + 4 * w)) & 15) ^ (h & RHO)) * 128; };
  unsigned dT = (kind == 0 ? 16 : (kind == 1 ? 1 : 4)) * DM * 2;
  asm volatile("s_nop 4" : "+s"(dT));
  const unsigned vx0 = ((w & 1) ? vx[1] : vx[0]) + (w >> 1) * dT;
  auto st_w = [&](int n) {
#pragma unroll
    for (int j = 0; j < 4; ++j) dma16(rw, vw[j], (64 * ((int)((wo >> (4 * n)) & 15) ^ (h & RHO)) * HD + 128 * h) * 2, lds0 + (n & 7) * 16384 + j * 1024, W_NT & 16);
    dma16(rx, vx0, ((int)((wo >> (4 * n)) & 15) ^ (h & RHO)) * 128, lds0 - 3072 * w + 131072 + (n & 7) * 4096);
  };
  auto st_x = [&](int s, int b, int q, int j, bool pad) { dma16(rx, vx[j & 1], s * 65536 + (j >> 1) * dT + xo(q), lds0 + (b * 4 + q) * 16384 + j * 1024, false, pad); };
  if (KQ_D && kind < 2) while (__builtin_amdgcn_s_memrealtime() - G.entry < KQ_D) __builtin_amdgcn_s_sleep(8);
  __builtin_amdgcn_s_waitcnt(0x0F70);
  u32x4 W[32][2], gv;
  char* xb[3][2];
#pragma unroll
  for (int i = 0; i < 6; ++i) xb[i >> 1][i & 1] = lds_base(smem, (i >> 1) * 65536 + (r >> 3) * 1024 + (r & 7) * 128 + ((4 * (i & 1) + g) ^ (r >> 1)) * 16);
  f32x4 acc[2][4];
#pragma unroll
  for (int i = 0; i < 4; ++i) acc[1][i] = vzero();
#pragma unroll
  for (int n = 0; n < WAH; ++n) st_w(n);
#pragma unroll
  for (int n = 0; n < 16; ++n) {
    asm volatile("s_waitcnt vmcnt(%0)" :: "n"(4 * WAH - 4 + (n < 16 - WAH ? WAH - 1 : 15 - n)) : "memory");
    lds_barrier();
    u32x4 f[4], y[4];
#pragma unroll
    for (int i = 0; i < 4; ++i) {
#pragma unroll
      for (int q = 0; q < 2; ++q) {
        const int row = (i & 1) * 32 + 8 * g + 4 * q + (r >> 2);
        const u32x2 u = __builtin_bit_cast(u32x2, __builtin_amdgcn_ds_read_tr16_b64_v4i16((LAS bf16x4*)(smem + (n & 7) * 16384 + row * 256 + (((4 * w + (r & 3)) ^ swz_b(row)) * 16) + 8 * (i >> 1))));
        f[i][2 * q] = u[0]; f[i][2 * q + 1] = u[1];
      }
      y[i] = *(const u32x4*)(xb[2][i & 1] + (n & 7) * 4096 + (i >> 1) * 2048);
    }
    SB0;
    if (n == 16 - WAH) G.ask(a.sync, gv);
    if (n == 15) asm volatile("" : "+v"(gv));
    if (n + WAH < 16) st_w(n + WAH);
    else {
#pragma unroll
      for (int j = 0; j < 4; ++j) st_x(1 + ((n + WAH - 16) >> 2), (n + WAH - 16) >> 2, (n + WAH) & 3, j, true);
    }
#pragma unroll
    for (int i = 0; i < 8; ++i)
      asm volatile("s_nop 1\n\tv_mfma_f32_16x16x32_bf16 %0, %1, %2, %0" : "+v"(acc[1][i & 3]) : "v"(f[2 * (i & 1) + (i >> 2)]), "v"(y[(i & 2) + (i >> 2)]));
#pragma unroll
    for (int i = 0; i < 4; ++i) { asm volatile("" : "+a"(f[i])); W[2 * n + (i & 1)][i >> 1] = f[i]; }
  }
  if (!G.pass(a.sync, gv)) { bg_drain(); return; }
  SSTAMP(160 + h);
  const Sel sel = G.sel;
  const __amdgpu_buffer_rsrc_t ru = mk_rsrc(a.sync, SY_BYTES);
  const unsigned sgo = (w | lane) ? 0x7fffffffu : SY_CT + (CT_Q + 32 * kind + h) * 128;
  const u32x4 rx2 = own_rsrc(a.X, 4u << 20);
  unsigned vq[4][4];
#pragma unroll
  for (int i = 0; i < 16; ++i) { vq[i >> 2][i & 3] = vx[i & 1] + ((i >> 1) & 1) * dT + xo(i >> 2); asm volatile("" : "+v"(vq[i >> 2][i & 3])); }
  u32x4 xf[4][2], o;
  auto rd = [&](int b, int k, int T) { xf[k & 3][T] = *(const u32x4*)(xb[b][k & 1] + (k >> 1) * 4096 + T * 2048); };
  auto signal = [&](int s) {
    st4(ru, sgo, s);
    if (w == 0 && lane == 0 && s >= 0 && (s < 5 || (s & 7) == 0 || (s & 15) == 2 || s > 61)) {
    }
  };
#pragma unroll
  for (int T = 0; T < 2; ++T) st16s(pk8v(acc[1][2 * T], acc[1][2 * T + 1]), ru, lane * 16 + T * 4096, x_slot(kind, 0, h, sel) + w * 1024);
#pragma unroll
  for (int i = 4 * WAH; i < 28; ++i) st_x(1 + (i >> 4), i >> 4, (i >> 2) & 3, i & 3, true);
  wait_vm<26>();
  lds_barrier();
#pragma unroll
  for (int i = 0; i < 6; ++i) rd(0, i >> 1, i & 1);
  WST(kind == 2, 18);
  auto seg = [&](int s, int b) {
    const unsigned so = x_slot(kind, s - 1, h, sel) + w * 1024;
    WST(kind == 2 && s > 0 && s < 4, 18 + s);
#pragma unroll
    for (int k = 0; k < 32; ++k)
#pragma unroll
      for (int i = 0; i < 4; ++i) {
        if (k == 0) asm volatile("v_mfma_f32_16x16x32_bf16 %0, %1, %2, 0" : "=&v"(acc[b][i]) : "a"(W[k][i & 1]), "v"(xf[k & 3][i >> 1]));
        else asm volatile("v_mfma_f32_16x16x32_bf16 %0, %1, %2, %0" : "+v"(acc[b][i]) : "a"(W[k][i & 1]), "v"(xf[k & 3][i >> 1]));
        SB0;
        const int q = k >> 3, n = 4 * k + i;
        if ((k & 7) == 5 && i == 0) {
          if (q == SIGQ) wait_vm<4 * SIGQ>(); else wait_vm<20>();
          if (q == 1 && __builtin_expect(s - 2u < W_NOW, 0)) { wait_vm<4>(); __builtin_amdgcn_s_barrier(); signal(s - 1); }
          else if (q == 0 && b == 0 && __builtin_expect(s == 1, 0)) { wait_vm<32 - 4 * WAH>(); __builtin_amdgcn_s_barrier(); signal(0); }
          else __builtin_amdgcn_s_barrier();
          asm volatile("" ::: "memory");
        }
        if (i == 1 || i == 2) rd(k + 3 < 32 ? b : b ^ 1, (k + 3) & 31, i - 1);
        {
          const int m = (n + 128 - 23) & 127, j = (m & 31) / DSP, q0 = m >> 5;
          if (j < 4 && (m & 31) % DSP == 0) asm volatile("s_add_i32 m0, %0, %1" :: "s"(lds0), "n"(((b ^ (q0 == 0) ^ (n < 23)) * 4 + ((q0 + 3) & 3)) * 16384 + j * 1024) : "m0", "scc");
          if (j < 4 && (m & 31) % DSP == 1)
            asm volatile("buffer_load_dwordx4 %0, %1, %2 offen lds" :: "v"(vq[(q0 + 3) & 3][j & 3]), "s"(rx2), "s"((s + 2 - (q0 == 0) - (n < 23)) * 65536) : "memory");
        }
        if (k == 8 * SIGQ + 5 && i == 2) signal(s - 1);
        if (k >= 1 && k < 5 && (i == 0 || i == 3)) { const f32x4 x = acc[b ^ 1][k - 1]; o[2 * (~k & 1) + (i & 1)] = pk2v(x[2 * (i & 1)], x[2 * (i & 1) + 1]); }
        if ((k == 2 || k == 4) && i == 3) { __builtin_amdgcn_raw_buffer_store_b128(o, ru, (unsigned)(lane * 16 + (k >> 2) * 4096), so, ST_AUX); asm volatile("s_nop 0" :: "v"(o)); }
        SB0;
      }
  };
  for (int s = 1;; s += 2) {
    seg(s, 0);
    if (s == NSEG - 1) break;
    seg(s + 1, 1);
  }
#pragma unroll
  for (int T = 0; T < 2; ++T) st16s(pk8v(acc[0][2 * T], acc[0][2 * T + 1]), ru, lane * 16 + T * 4096, x_slot(kind, NSEG - 1, h, sel) + w * 1024);
  bg_drain();
  signal(NSEG - 1);
}
__device__ __forceinline__ float blo(unsigned d) { return __uint_as_float(d << 16); }
__device__ __forceinline__ float bhi(unsigned d) { return __uint_as_float(d & 0xffff0000u); }
constexpr int A2_CSTR = 4160;
constexpr int A2_UOFF = 4 * A2_CSTR + 16 * 80;
constexpr int A2_USTR = 80 * 80;
constexpr int A2_WLDS = 4 * A2_CSTR + 2 * A2_USTR;
__device__ __forceinline__ void a2_wave(const ChainArgs& a, int d0, int d1, char* smem, int w) {
  const int lane = opaque_tid(0);
  char* Wb = smem + 1024 + w * A2_WLDS;
  char* U = Wb + A2_UOFF;
  const __amdgpu_buffer_rsrc_t rf = mk_rsrc((void*)a.Assm, 4u << 20), rd_ = mk_rsrc((void*)a.AD, 2048);
  const __amdgpu_buffer_rsrc_t rkv = mk_rsrc((void*)a.kvT, 4u << 20);
  u32x4 hi[4], uv[2][4];
  u32x2 lo[4];
  unsigned dn;
  auto ld_h = [&](int d) {
#pragma unroll
    for (int i = 0; i < 4; ++i) {
      hi[i] = __builtin_amdgcn_raw_buffer_load_b128(rf, (unsigned)((255 - 64 * i - lane) * 16), (unsigned)(d * 4096), 2);
      lo[i] = __builtin_amdgcn_raw_buffer_load_b64(rf, i == 3 && lane == 63 ? 0x7fffffffu : (255 - 64 * i - lane) * 16 - 8, (unsigned)(d * 4096), 2);
    }
    dn = __builtin_amdgcn_raw_buffer_load_b16(rd_, 0, 2 * d, 0);
  };
  auto ld_u = [&](int d) {
#pragma unroll
    for (int i = 0; i < 8; ++i) uv[i & 1][i >> 1] = __builtin_amdgcn_raw_buffer_load_b128(rkv, (unsigned)max(((i >> 1) * 64 + lane) * 16 - 8 * (i & 1), 0), (unsigned)(d * 4096), LD_AUX);
  };
  ld_h(d0);
  {
    bool dead = false;
    ctr_wait<BG_SLP>(ctr(a, CT_A1 + (d0 >> 8)), 64u, (GAS unsigned*)(a.sync + SY_ABORT), lane, dead);
  }
  ld_u(d0);
  if (lane < 12) *(f32x4*)(Wb + (lane / 3) * A2_CSTR + (256 + lane % 3) * 16) = vzero();
#pragma unroll
  for (int i = 0; i < 4; ++i) if (64 * (i & 1) + lane < 80) *(f32x4*)(U + (i >> 1) * A2_USTR - 16 * 80 + (64 * (i & 1) + lane) * 16) = vzero();
  const int r = lane & 15, g = lane >> 4;
#pragma unroll 1
  for (int d = d0; d < d1; d += 96) {
    u64 at[6]; (void)at;
    AST(0);
#pragma unroll
    for (int i = 0; i < 4; ++i) {
      const unsigned src[6] = {lo[i][0], lo[i][1], hi[i][0], hi[i][1], hi[i][2], hi[i][3]};
      unsigned f[10];
#pragma unroll
      for (int x = 0; x < 10; ++x) {
        if ((x & 1) == 0) f[x] = __builtin_amdgcn_alignbit(src[5 - x / 2], src[5 - x / 2], 16);
        else f[x] = __builtin_amdgcn_perm(src[5 - (x + 1) / 2], src[5 - (x - 1) / 2], 0x07060100u);
      }
#pragma unroll
      for (int m = 0; m < 4; ++m) *(u32x4*)(Wb + m * A2_CSTR + (64 * i + lane) * 16) = (u32x4){f[3 - m], f[5 - m], f[7 - m], f[9 - m]};
    }
    if (lane == 0) uv[1][0] = (u32x4){0u, 0u, uv[1][0][0], uv[1][0][1]};
#pragma unroll
    for (int i = 0; i < 8; ++i) {
      const int ch = (i >> 1) * 64 + lane;
      *(u32x4*)(U + (i & 1) * A2_USTR + (ch >> 2) * 80 + (ch & 3) * 16) = uv[i & 1][i >> 1];
    }
    const float Dd = blo(dn);
    asm volatile("" :: "v"(Dd) : "memory");
    AST(1);
    ld_h(d + 96 < d1 ? d + 96 : D_A); ld_u(d + 96 < d1 ? d + 96 : D_A);
    f32x4 acc[4][2];
#pragma unroll
    for (int i = 0; i < 8; ++i) acc[i >> 1][i & 1] = vzero();
    const char* pa = Wb + (r & 3) * A2_CSTR + 16 * (255 - (r >> 2) + g) - 3 * 1024 - 64;
    const char* pb = U + (r - 1) * 80 + 16 * g;
    bf16x8 fa[2][4], fb[2][8];
    auto rd = [&](int b, int n, int k) {
      if (n == 0 || n > 8) fa[b][n ? n - 8 : 0] = *(const bf16x8*)(pa + 1024 * (3 - (n ? n - 8 : 0)) + 64 * (1 - k));
      else fb[b][n - 1] = *(const bf16x8*)(pb + ((n - 1) & 1) * A2_USTR + 80 * (16 * ((n - 1) >> 1) + 1 - k));
    };
#pragma unroll
    for (int n = 0; n < 12; ++n) rd(0, n, 0);
    AST(2);
#pragma unroll 1
    for (int k = 0; k < 16; k += 2) {
#pragma unroll
      for (int b = 0; b < 2; ++b) {
        int q = 0;
#pragma unroll
        for (int j = 0; j < 4; ++j)
#pragma unroll
          for (int i = j; i < 4; ++i)
#pragma unroll
            for (int mt = 0; mt < 2; ++mt) {
              asm volatile("v_mfma_f32_16x16x32_bf16 %0, %1, %2, %0" : "+v"(acc[i][mt]) : "v"(fa[b][j]), "v"(fb[b][2 * (i - j) + mt]));
              SB0;
              if (q < 12) rd(b ^ 1, q, 1);
              if (q == 12) { pa -= 64; pb -= 80; }
              ++q;
              SB0;
            }
      }
    }
    AST(3);
    u32x4 kv[4];
#pragma unroll
    for (int i = 0; i < 4; ++i) kv[i] = *(const u32x4*)(U + (16 * i + r) * 80 + 16 * g);
    asm volatile("s_nop 7\n\ts_waitcnt vmcnt(0)" ::: "memory");
#pragma unroll
    for (int i = 0; i < 8; ++i) asm volatile("" : "+v"(acc[i >> 1][i & 1]));
    if (lane == 0 && d > d0) ctr_add(a, CT_A2 + ((d - 96) >> 6), 1u);
#pragma unroll
    for (int i = 0; i < 4; ++i) {
      const f32x4 x = acc[i][1], y = acc[i][0];
      st16s((u32x4){pk2(fmaf(blo(kv[i][0]), Dd, x[0]), fmaf(bhi(kv[i][0]), Dd, x[1])), pk2(fmaf(blo(kv[i][1]), Dd, x[2]), fmaf(bhi(kv[i][1]), Dd, x[3])),
                    pk2(fmaf(blo(kv[i][2]), Dd, y[0]), fmaf(bhi(kv[i][2]), Dd, y[1])), pk2(fmaf(blo(kv[i][3]), Dd, y[2]), fmaf(bhi(kv[i][3]), Dd, y[3]))},
            rkv, (unsigned)(i * 1024 + r * 64 + g * 16), (unsigned)(d * 4096));
    }
    AST(4);
  }
  wait_vm<0>();
  if (lane == 0) ctr_add(a, CT_A2 + ((d0 + (d1 - 1 - d0) / 96 * 96) >> 6), 1u);
}
__device__ __forceinline__ void task_u(const ChainArgs& a, int h, char* smem, int wv, Gate& G) {
  const int lane = opaque_tid(0);
  const int w = wv;
  GAS unsigned* abortw = (GAS unsigned*)(a.sync + SY_ABORT);
  bool dead = false;
  char* sb = smem + w * 32768;
  const __amdgpu_buffer_rsrc_t rwo = mk_rsrc(a.Wo, 8u << 20);
  const unsigned lds0 = (unsigned)(uintptr_t)(LAS char*)sb;
  unsigned wo4[4];
#pragma unroll
  for (int i = 0; i < 4; ++i) { const int row = i * 4 + (lane >> 4); wo4[i] = row * DM * 2 + (((lane & 15) ^ swz_b(row)) * 16); }
  asm volatile("s_nop 4");
  auto stage = [&](int n) {
    const int p = (U_ORD >> (4 * n)) & 3;
#pragma unroll
    for (int i = 0; i < 16; ++i)
      dma16(rwo, wo4[i & 3], (unsigned)(((128 * h + 64 * (p & 1) + 16 * (i >> 2)) * DM + 256 * w + 128 * (p >> 1)) * 2), lds0 + (n & 1) * 16384 + i * 1024, W_NT & 2);
  };
  __builtin_amdgcn_s_waitcnt(0x0F70);
  u32x4 W[16][4];
  stage(0); stage(1);
#pragma unroll
  for (int n = 0; n < 4; ++n) {
    const int p = (U_ORD >> (4 * n)) & 3;
    if (n < 3) wait_vm<16>(); else wait_vm<0>();
#pragma unroll
    for (int t = 0; t < 8; ++t) {
      const u32x8 f = tr_frag(sb + (n & 1) * 16384, t, lane);
      u32x4 f0 = {f[0], f[1], f[2], f[3]}, f1 = {f[4], f[5], f[6], f[7]};
      asm volatile("" : "+a"(f0), "+a"(f1));
      W[8 * (p >> 1) + t][2 * (p & 1)] = f0; W[8 * (p >> 1) + t][2 * (p & 1) + 1] = f1;
    }
    asm volatile("s_waitcnt lgkmcnt(0)" ::: "memory");
    if (n + 2 < 4) stage(n + 2);
  }
  lds_barrier();
  if (!G.pass(a.sync)) return;
  SSTAMP(192 + h);
  const Sel sel = G.sel;
  const u32x4 ru = own_rsrc(a.sync, SY_BYTES);
  u32x4 V[2][8], pc[2], o[2];
  f32x4 c[2][2];
  unsigned cv, cw, vo[4], lk[2], sgo = (w | lane) ? 0x7fffffffu : SY_CT + (CT_U + h) * 128, nd = (lane & 1) ? 5 - URING : 2;
#pragma unroll
  for (int i = 0; i < 4; ++i) { vo[i] = lane * 16 + ux_tile(16 * w + 4 * i); asm volatile("" : "+v"(vo[i])); }
#pragma unroll
  for (int i = 0; i < 2; ++i) lk[i] = SY_CT + ((lane & 1) ? CT_DONE + 8 * i + (h & 7) : CT_V + h) * 128;
  char* vl = lds_base(smem, lane * 16);
  const unsigned vl2 = lane * 16 + w * 2048;
  auto ld_v = [&](int s) {
    asm volatile("buffer_load_dwordx4 %0, %2, %3, %4 offen sc1\n\tbuffer_load_dwordx4 %1, %2, %3, %4 offen offset:1024 sc1" : "=&v"(pc[0]), "=&v"(pc[1]) : "v"(vl2), "s"(ru), "s"(x_slot(2, s, h, sel)) : "memory");
  };
  auto signal = [&](int s) {
    asm volatile("buffer_store_dword %0, %1, %2, 0 offen sc1" :: "v"(s), "v"(sgo), "s"(ru) : "memory");
  };
  auto st_u = [&](int n, unsigned ub) { asm volatile("buffer_store_dwordx4 %0, %1, %2, %3 offen offset:%4 sc1" :: "v"(o[n & 1]), "v"(vo[n >> 2]), "s"(ru), "s"(ub), "n"((n & 3) * 1024) : "memory"); };
  auto fetch = [&](int s) {
    ctr_wait<2>(ctr(a, CT_V + h), s + 1, abortw, lane, dead);
    WST(s == 0, 24);
    ld_v(s);
    asm volatile("s_waitcnt vmcnt(0)" : "+v"(pc[0]), "+v"(pc[1]) :: "memory");
#pragma unroll
    for (int i = 0; i < 2; ++i) *(u32x4*)(vl + 16384 + w * 2048 + i * 1024) = pc[i];
    lds_barrier();
#pragma unroll
    for (int i = 0; i < 8; ++i) V[s][i] = *(const u32x4*)(vl + 16384 + i * 1024);
  };
  fetch(0);
  u64 bad = 0;
  unsigned up = 0x7f000000u;
  c[1][0] = c[1][1] = vzero();
  auto seg = [&](int s, int b) {
    const unsigned ub = ux_base(s, h, sel);
    WST(s < 4, 25 + s);
#pragma unroll
    for (int n = 0; n < 16; ++n)
#pragma unroll
      for (int q = 0; q < 8; ++q) {
        const int kp = q >> 1, m = q & 1;
        if (kp == 0) asm volatile("v_mfma_f32_16x16x32_bf16 %0, %1, %2, 0" : "=&v"(c[n & 1][m]) : "v"(V[b][4 * m + kp]), "a"(W[n][kp]));
        else asm volatile("v_mfma_f32_16x16x32_bf16 %0, %1, %2, %0" : "+v"(c[n & 1][m]) : "v"(V[b][4 * m + kp]), "a"(W[n][kp]));
        SB0;
        if (q >= 2 && q < 6) { const f32x4 x = c[~n & 1][(q - 2) >> 1]; o[~n & 1][q - 2] = pk2v(x[2 * (q & 1)], x[2 * (q & 1) + 1]); }
        if (q == 6) st_u((n - 1) & 15, n ? ub : up);
        if (q == 5) asm volatile("" :: "v"(o[n & 1]));
        if (n == U_LD && q == 0) {
          if (__builtin_expect(bad != 0, 0)) {
            if (s <= U_NOW) {
              asm volatile("s_waitcnt vmcnt(%1)" : "+v"(cw) : "n"(U_LD - 2) : "memory");
              __builtin_amdgcn_s_barrier();
              signal(s - 1);
              bad = (int)__builtin_amdgcn_readfirstlane(cw) <= s;
            }
            if (bad & 1) ctr_wait<2>(ctr(a, CT_V + h), min(s + 2, NSEG), abortw, lane, dead);
            if (bad & 2) {
              ctr_wait<U_SLP>(ctr(a, CT_DONE + 8 * b + (h & 7)), max(s + 5 - URING, 0), abortw, lane, dead);
              for (int i = 0; i < U_STG * (h & 7) + U_ST0; ++i) __builtin_amdgcn_s_sleep(1);
            }
          }
          ld_v(min(s + 1, NSEG - 1));
        }
        if (n == 1 && q == 7) asm volatile("buffer_load_dword %0, %1, %2, 0 offen sc1" : "=&v"(cw) : "v"(s <= U_NOW ? lk[0] : 0x7fffffffu), "s"(ru) : "memory");
        if (n == U_LD && q == 7) asm volatile("buffer_load_dword %0, %1, %2, 0 offen sc1" : "=&v"(cv) : "v"(lk[b ^ 1]), "s"(ru) : "memory");
        if (n == U_SIG2 && q == 0) asm volatile("s_waitcnt vmcnt(%4)" : "+v"(pc[0]), "+v"(pc[1]), "+v"(cv), "+v"(cw) : "n"(U_SIG2 - U_LD - 1) : "memory");
        if (n == U_SIG2 && (q == 1 || q == 7)) *(u32x4*)(vl + (b ^ 1) * 8192 + w * 2048 + (q >> 2) * 1024) = pc[q >> 2];
        if (n == U_SIG2 + 1 && q == 0) { bad = __builtin_amdgcn_ballot_w64((int)cv < (s < U_NOW && lane > 1 ? NSEG : min(s + (int)nd, NSEG - 1))); asm volatile("" : "+s"(bad)); }
        if (n == U_SIG2 + 1 && q == 7) lds_barrier();
        if (n == U_SIG2 + 2 && q == 0) signal(s - 1);
        if ((n == U_SIG2 + 2 || n == U_SIG2 + 3) && (q & 1)) V[b ^ 1][4 * (n - U_SIG2 - 2) + (q >> 1)] = *(const u32x4*)(vl + (b ^ 1) * 8192 + (4 * (n - U_SIG2 - 2) + (q >> 1)) * 1024);
        SB0;
      }
    up = ub;
  };
  for (int s = 0; s < NSEG; s += 2) {
    seg(s, 0);
    if (__builtin_expect(s == 0, 0)) {
      o[1] = pk8v(c[1][0], c[1][1]);
      st_u(15, up);
      bg_drain();
      signal(0);
      fetch(1);
    }
    seg(s + 1, 1);
  }
  o[1] = pk8v(c[1][0], c[1][1]);
  st_u(15, up);
  bg_drain();
  signal(NSEG - 1);
}

__device__ __forceinline__ void task_a1(const ChainArgs& a, int dg, int t0, char* T) {
  const int lane = opaque_tid(0);
  const int d = dg * 64 + 8 * (lane & 7);
  const int tb = t0 + 8 * (lane >> 3);
  u32x4 kk[2][11], vv[2][8], sh[4];
#pragma unroll
  for (int i = 0; i < 4; ++i) sh[i] = *(const u32x4*)(a.Ashift + d * 4 + 8 * i);
  SB0;
#pragma unroll
  for (int gq = 0; gq < 2; ++gq) {
#pragma unroll
    for (int i = 0; i < 11; ++i) {
      const int t = tb + 64 * gq - 3 + i;
      kk[gq][i] = LDNT((const u32x4*)(a.Ak + (long)(t < 0 ? 0 : t) * D_A + d));
    }
#pragma unroll
    for (int i = 0; i < 8; ++i) vv[gq][i] = LDNT((const u32x4*)(a.Av + (long)(tb + 64 * gq + i) * D_A + d));
    SB0;
  }
#pragma unroll
  for (int i = 0; i < 3; ++i) if (tb == 0) kk[0][i] = (u32x4){0u, 0u, 0u, 0u};
#pragma unroll
  for (int gq = 0; gq < 2; ++gq) {
#pragma unroll
    for (int c = 0; c < 8; ++c) {
      const unsigned t01 = sh[c >> 1][2 * (c & 1)], t23 = sh[c >> 1][2 * (c & 1) + 1];
      const float s0 = blo(t01), s1 = bhi(t01), s2 = blo(t23), s3 = bhi(t23);
      float kf[11];
#pragma unroll
      for (int i = 0; i < 11; ++i) kf[i] = (c & 1) ? bhi(kk[gq][i][c >> 1]) : blo(kk[gq][i][c >> 1]);
      u32x4 o;
#pragma unroll
      for (int i = 0; i < 8; i += 2) {
        float r[2];
#pragma unroll
        for (int j = 0; j < 2; ++j)
          r[j] = fmaf(kf[i + j + 3], s0, fmaf(kf[i + j + 2], s1, fmaf(kf[i + j + 1], s2, kf[i + j] * s3))) * ((c & 1) ? bhi(vv[gq][i + j][c >> 1]) : blo(vv[gq][i + j][c >> 1]));
        o[i >> 1] = pk2(r[0], r[1]);
      }
      *(u32x4*)(T + (8 * (lane & 7) + c) * 256 + (((gq * 8 + (lane >> 3)) ^ (lane & 7)) * 16)) = o;
    }
    SB0;
  }
  const __amdgpu_buffer_rsrc_t rkv = mk_rsrc((void*)a.kvT, 4u << 20);
#pragma unroll
  for (int i = 0; i < 16; ++i) {
    const int row = 4 * i + (lane >> 4);
    const u32x4 o = *(const u32x4*)(T + row * 256 + (((lane & 15) ^ (row >> 3)) * 16));
    st16(rkv, (unsigned)(((dg * 64 + row) * S_LEN + t0 + (lane & 15) * 8) * 2), o);
  }
  wait_vm<0>();
  if (lane == 0) ctr_add(a, CT_A1 + (dg >> 2), 1u);
}

__device__ __forceinline__ void task_a3(const ChainArgs& a, int dg, int t0, char* T) {
  const int lane = opaque_tid(0);
  const int d = dg * 64 + 8 * (lane & 7);
  const int tb = t0 + 8 * (lane >> 3);
  const __amdgpu_buffer_rsrc_t rz = mk_rsrc((void*)a.kvT, 4u << 20);
  u32x4 qq[16], zz[8][2];
#pragma unroll
  for (int i = 0; i < 16; ++i) qq[i] = LDNT((const u32x4*)(a.Aq + (long)(tb + 64 * (i >> 3) + (i & 7)) * D_A + d));
  {
    u32x4 zr[16];
    bool dead = false;
    ctr_wait<A3_SLP>(ctr(a, CT_A2 + dg), 64u, (GAS unsigned*)(a.sync + SY_ABORT), lane, dead);
  #pragma unroll
    for (int i = 0; i < 16; ++i) zr[i] = ld16(rz, (unsigned)(((dg * 64 + 4 * i + (lane >> 4)) * S_LEN + t0 + (lane & 15) * 8) * 2));
#pragma unroll
    for (int i = 0; i < 16; ++i) {
      const int row = 4 * i + (lane >> 4);
      *(u32x4*)(T + row * 256 + (((lane & 15) ^ (row >> 3)) * 16)) = zr[i];
    }
  }
#pragma unroll
  for (int c = 0; c < 8; ++c)
#pragma unroll
    for (int gq = 0; gq < 2; ++gq) zz[c][gq] = *(const u32x4*)(T + (8 * (lane & 7) + c) * 256 + (((gq * 8 + (lane >> 3)) ^ (lane & 7)) * 16));
#pragma unroll
  for (int i = 0; i < 16; ++i) {
    u32x4 o;
#pragma unroll
    for (int c = 0; c < 8; c += 2) {
      const unsigned z0 = zz[c][i >> 3][(i >> 1) & 3], z1 = zz[c + 1][i >> 3][(i >> 1) & 3];
      o[c >> 1] = pk2(blo(qq[i][c >> 1]) * ((i & 1) ? bhi(z0) : blo(z0)), bhi(qq[i][c >> 1]) * ((i & 1) ? bhi(z1) : blo(z1)));
    }
    st16g(a.out + (long)(tb + 64 * (i >> 3) + (i & 7)) * OUTW + d, o);
  }
}

__device__ __forceinline__ void part_a(const ChainArgs& a, char* smem, int x, int wi, int w) {
  char* T = smem + 1024 + w * A2_WLDS;
  if (wi < 64) task_a1(a, 4 * x + (wi & 3), (wi >> 2) * 128, T);
  a2_wave(a, 256 * x + wi, 256 * x + 256, smem, w);
  while (true) {
    unsigned t = 0;
    if (opaque_tid(0) == 0) t = __hip_atomic_fetch_add(ctr(a, CT_TK + x), 1u, __ATOMIC_RELAXED, __HIP_MEMORY_SCOPE_AGENT) + 1u;
    t = __builtin_amdgcn_readfirstlane(t);
    if (t >= 64) break;
    task_a3(a, 4 * x + (t >> 4), (t & 15) * 128, T);
  }
}

__global__ __launch_bounds__(256, 1) void chain_kernel(ChainArgs a) {
  extern __shared__ __attribute__((aligned(16))) char smem[];
  const int b = blockIdx.x;
  {
    u32x4 rc;
    asm volatile("s_getpc_b64 s[60:61]\n\t.Lchain_here:\n\ts_sub_u32 s60, s60, .Lchain_here-_Z12chain_kernel9ChainArgs\n\ts_subb_u32 s61, s61, 0\n\ts_and_b32 s61, s61, 0xffff\n\t"
                 "s_mov_b32 s62, .Lchain_end-_Z12chain_kernel9ChainArgs\n\ts_mov_b32 s63, 0x20000\n\t"
                 "buffer_load_dwordx4 a[252:255], %1, s[60:63], 0 offen\n\tbuffer_load_dwordx4 a[252:255], %1, s[60:63], s63 offen"
                 : "={s[60:63]}"(rc) : "v"((b >> 3) * 4096 + threadIdx.x * 16) : "scc", "a252", "a253", "a254", "a255");
  }
  int w = __builtin_amdgcn_readfirstlane(threadIdx.x >> 6);
  Gate G;
  G.entry = __builtin_amdgcn_s_memrealtime(); G.open = false; G.arr = w == 0 ? b & 7 : -1;
  G.rot = ((unsigned)__builtin_amdgcn_s_getreg(20 | (3 << 11)) - b + WRONG) & 7u;
  {
    volatile LAS unsigned* t = (volatile LAS unsigned*)smem;
    if (w == 0) { t[0] = (unsigned)G.entry; t[1] = (unsigned)(G.entry >> 32); }
    __syncthreads();
    G.entry = (u64)(unsigned)__builtin_amdgcn_readfirstlane(t[0]) | ((u64)(unsigned)__builtin_amdgcn_readfirstlane(t[1]) << 32);
    __syncthreads();
  }
  if (((b + G.rot) & 4) == 0 && b < 128) {
    const int c = (b >> 6) * 32 + 8 * ((b + G.rot) & 3) + ((b >> 3) & 7);
    if (w < 2) calibrate(a, w ? SY_HOT / PAGE : c, 2 * c + w, G.entry);
    __builtin_amdgcn_s_barrier();
  }
  if (b == 0) {
    const int lane = opaque_tid(0);
    const __amdgpu_buffer_rsrc_t rs = mk_rsrc(a.sync + SY_ABORT, SY_CT - SY_ABORT + CT_CAL * 128);
    st16(rs, w * 8192 + lane * 128, (u32x4){SENT, SENT, SENT, SENT});
    static_assert(SY_CT - SY_ABORT + CT_CAL * 128 <= 32768, "one store per wave");
    if (w == 3) {
      u32x4 v;
      bool ok;
      do {
        v = ld16(mk_rsrc(a.sync + SY_CT + CT_CAL * 128, 8192), lane * 128);
        ok = ((u64)v[1] << 32 | v[0]) - G.entry + 2000u < (1ull << 31) && ((u64)v[3] << 32 | v[2]) - G.entry + 2000u < (1ull << 31);
      } while (!__all(ok) && __builtin_amdgcn_s_memrealtime() - G.entry < 2 * CALM);
      if (lane == SY_HOT / PAGE) v[0] = 0;
      const u64 m = __builtin_amdgcn_ballot_w64(ok && (v[0] & 1u)) | (u64)(__builtin_popcountll(__builtin_amdgcn_ballot_w64(ok && (v[2] & 1u))) > 32) << (SY_HOT / PAGE);
      if (lane == 0) __hip_atomic_store((GAS u64*)(a.sync + SY_SEL), m, __ATOMIC_RELAXED, __HIP_MEMORY_SCOPE_AGENT);
    }
    bg_drain();
    if (w == 0 && lane == 0) __hip_atomic_store((GAS u64*)(a.sync + SY_EPOCH), (__builtin_amdgcn_s_memrealtime() & ~7ull) | G.rot, __ATOMIC_RELAXED, __HIP_MEMORY_SCOPE_AGENT);
  }
  while (true) {
    const unsigned rot = G.rot;
    asm volatile("" : "+s"(w));
    const int x = (b + rot) & 7, j = b >> 3, h = 8 * (x & 3) + (j & 7), role = (x & 4) + (j >> 3);
    if ((role == 0 || role == 7) && w >= 2) {
      const int n = opaque_tid(w - 2);
      const unsigned x0 = __builtin_amdgcn_raw_buffer_load_b32(mk_rsrc(a.sync, 134u << 20), n * PAGE + 4096 + (j & 31) * 128, 0, 2);
      const unsigned x1 = __builtin_amdgcn_raw_buffer_load_b32(mk_rsrc(a.out, 8u << 20), n * PAGE + (j & 31) * 128, 0, 2);
      asm volatile("" :: "v"(x0), "v"(x1));
    }
    if (role < 2) role_cb(a, 32 * role + h, smem, w, G);
    else if (role == 2) role_qk(a, h, smem, w, G);
    else if (role == 3) role_vo(a, h, smem, w, G);
    else if (role == 4) task_u(a, h, smem, w, G);
    else task_w(a, 7 - role, h, smem, w, G);
    if (G.rot == rot && role > 4) part_a(a, smem, x & 3, 4 * (j - 8) + w, w);
    if (G.rot == rot) break;
    __syncthreads();
  }
  asm volatile(".Lchain_end:");
}

extern "C" void kernel_launch(void* const* d_in, const int* in_sizes, int n_in,
                              void* d_out, int out_size, void* d_ws, size_t ws_size,
                              hipStream_t stream) {
  const u16* A_q = (const u16*)d_in[0];
  const u16* A_k = (const u16*)d_in[1];
  const u16* A_v = (const u16*)d_in[2];
  const u16* A_shift = (const u16*)d_in[3];
  const u16* A_ssmk = (const u16*)d_in[4];
  const u16* A_D = (const u16*)d_in[5];
  const u16* B_x = (const u16*)d_in[6];
  const u16* B_mem0 = (const u16*)d_in[7];
  const u16* B_Wq = (const u16*)d_in[8];
  const u16* B_Wk = (const u16*)d_in[9];
  const u16* B_Wv = (const u16*)d_in[10];
  const u16* B_Wo = (const u16*)d_in[11];
  u16* out = (u16*)d_out;
  char* ws = (char*)(((uintptr_t)d_ws + (2u << 20) - 1) & ~(uintptr_t)((2u << 20) - 1)) + WSOFF * MB;
  u16* kvT = (u16*)(ws + OFF_KVT);
  hipFuncSetAttribute((const void*)chain_kernel, hipFuncAttributeMaxDynamicSharedMemorySize, CHAIN_LDS);
  ChainArgs ca;
  ca.prof = (u64*)((char*)d_ws + PROF_AT);
  ca.mem0 = B_mem0; ca.Wq = B_Wq; ca.Wk = B_Wk; ca.Wv = B_Wv; ca.Wo = B_Wo; ca.X = B_x;
  ca.out = out; ca.sync = ws;
  ca.Aq = A_q; ca.Ak = A_k; ca.Av = A_v; ca.Ashift = A_shift; ca.Assm = A_ssmk; ca.AD = A_D; ca.kvT = kvT;
  chain_kernel<<<256, 256, CHAIN_LDS, stream>>>(ca);
}
```

```cpp
#include <hip/hip_runtime.h>
#include <hip/hip_bf16.h>
#include <stdint.h>
#include <type_traits>

constexpr int S_LEN = 2048;
constexpr int D_A   = 1024;
constexpr int DM    = 1024;
constexpr int NH    = 32;
constexpr int DH    = 128;
constexpr int HD    = NH * DH;
constexpr int MEMR  = 8;
constexpr int SEG   = 32;
constexpr int NSEG  = S_LEN / SEG;
constexpr int OUTW  = 2048;

typedef unsigned short u16;
typedef unsigned long long u64;
typedef short bf16x8 __attribute__((ext_vector_type(8)));
typedef short bf16x4 __attribute__((ext_vector_type(4)));
typedef float f32x4 __attribute__((ext_vector_type(4)));
typedef float f32x16 __attribute__((ext_vector_type(16)));
typedef unsigned u32x4 __attribute__((ext_vector_type(4)));
typedef unsigned u32x2 __attribute__((ext_vector_type(2)));
#define LAS __attribute__((address_space(3)))
#define GAS __attribute__((address_space(1)))

constexpr size_t MB = 1u << 20;
constexpr size_t OFF_KVT = 130 * MB;
constexpr size_t PROF_AT = 0;

__device__ __forceinline__ float bf2f(u16 b) { return __uint_as_float(((unsigned)b) << 16); }
__device__ __forceinline__ u16 f2bf(float f) { __bf16 b = (__bf16)f; return __builtin_bit_cast(u16, b); }
__device__ __forceinline__ float rbf(float f) { return bf2f(f2bf(f)); }
__device__ __forceinline__ unsigned pk2(float a, float b) {
  typedef float f2 __attribute__((ext_vector_type(2)));
  typedef __bf16 b2 __attribute__((ext_vector_type(2)));
  f2 v = {a, b}; b2 r = __builtin_convertvector(v, b2); return __builtin_bit_cast(unsigned, r);
}

__device__ __forceinline__ int opaque_tid(int wv) {
  int l;
  asm volatile("v_mbcnt_lo_u32_b32 %0, -1, 0\n\tv_mbcnt_hi_u32_b32 %0, -1, %0" : "=v"(l));
  return wv * 64 + l;
}
__device__ __forceinline__ int swz_b(int row) { return ((row & 3) << 2) | ((row >> 2) & 3); }

#define KNOB(name, v) constexpr int name = v
KNOB(K_DEF, 72);
KNOB(W_STEP, 33); KNOB(W_OFF, 56);
#define RHOV 3
KNOB(RHO, RHOV);
#define CDUR 1600
#define U_SLP 8
#define U_STG 0
#define U_ST0 0
#define WSOFF 118
#define CALN 3
#define CALM 600u
#define CALT 700u
#define KQ_D 500
#define W_NOW 4
#define U_SIG2 11
#define U_LD 5
#define U_NOW 6
#define WRONG 0
#define OUTP "nt"
#define LDNT(p) __builtin_nontemporal_load(p)
constexpr int CHK      = 16384;
constexpr int PAGE     = 2 << 20;
constexpr int SY_UM    = 34 * PAGE;
constexpr int SY_HOT   = 32 * PAGE;
constexpr int H_MEM    = 0;
constexpr int H_P      = 16 * CHK;
constexpr int H_ANN    = 28 * CHK;
constexpr int H_DLT    = 32 * CHK;
constexpr int H_TR     = 33 * CHK;
constexpr int H_PX     = 64 * CHK;
constexpr int H_BYTES  = 128 * CHK;
constexpr int URING    = 16;
constexpr int SY_SET   = 64 * PAGE;
constexpr int SY_EPOCH = SY_SET + 128;
constexpr int SY_ABORT = SY_SET + 256;
constexpr int SY_SEL   = SY_EPOCH + 8;
constexpr int SY_CT    = SY_ABORT + 128;
constexpr int CT_ARR = 0, CT_INIT = 8, CT_TK = 9, CT_A1 = 13, CT_DONE = 17, CT_A2 = 33;
constexpr int CT_Q = 49, CT_K = CT_Q + 32, CT_V = CT_K + 32, CT_U = CT_V + 32, CT_CAL = CT_U + 32;
constexpr int SY_BYTES = SY_CT + (CT_CAL + 64) * 128;
constexpr unsigned SENT = 0xFFFFFFFFu;
constexpr unsigned SPIN_CAP = 1u << 19;
constexpr int NLF = 16;
constexpr int L_WTS = 0;
constexpr int L_MEM = L_WTS + 4 * NLF * 2048;
constexpr int L_XCH = L_MEM + 16384;
constexpr int L_XK  = L_XCH + 2048;
constexpr int L_KX  = L_XCH + 4096;
constexpr int L_PRF = L_KX + 8192;
constexpr int XRS = 40;
constexpr int CHAIN_LDS = 160 * 1024;
static_assert(L_XCH + 128 * XRS <= L_PRF && L_PRF + 4096 <= CHAIN_LDS, "lds");

typedef __bf16 bfv8 __attribute__((ext_vector_type(8)));
typedef __bf16 bfv16 __attribute__((ext_vector_type(16)));
typedef unsigned u32x8 __attribute__((ext_vector_type(8)));
typedef unsigned u32x12 __attribute__((ext_vector_type(12)));
typedef unsigned u32x16 __attribute__((ext_vector_type(16)));
typedef unsigned u32x32 __attribute__((ext_vector_type(32)));
struct Wts { u32x8 a[32]; u32x32 v[4]; };

#define STAMPL(i) do {} while (0)
#define PVAL(i, v) do {} while (0)
#define PDECL(base, str) do {} while (0)
#define PZERO do {} while (0)
#define PDUMP(mask) do {} while (0)
#define TA(x) do {} while (0)
#define TB(x) TS(x)
#define TS(x) do {} while (0)
#define SSTAMP(i) do {} while (0)
#define WST(c, i) do {} while (0)
#define AST(i) do {} while (0)
struct ChainArgs {
  u64* prof;
  const u16* mem0; const u16* Wq; const u16* Wk; const u16* Wv; const u16* Wo; const u16* X;
  u16* out; char* sync;
  const u16* Aq; const u16* Ak; const u16* Av; const u16* Ashift; const u16* Assm; const u16* AD; u16* kvT;
};
__device__ __forceinline__ GAS unsigned* ctr(const ChainArgs& a, int i) { return (GAS unsigned*)(a.sync + SY_CT + i * 128); }
__device__ __forceinline__ void ctr_add(const ChainArgs& a, int i, unsigned v) {
  __hip_atomic_fetch_add(ctr(a, i), v, __ATOMIC_RELAXED, __HIP_MEMORY_SCOPE_AGENT);
}

__device__ __forceinline__ __amdgpu_buffer_rsrc_t mk_rsrc(const void* p, unsigned bytes) {
  const u64 v = (u64)p;
  const unsigned lo = (unsigned)__builtin_amdgcn_readfirstlane((unsigned)v), hi = (unsigned)__builtin_amdgcn_readfirstlane((unsigned)(v >> 32));
  const u64 u = (u64)lo | ((u64)hi << 32);
  return __builtin_amdgcn_make_buffer_rsrc((void*)u, 0, bytes, 0x00020000);
}
__device__ __forceinline__ u32x4 own_rsrc(const void* p, unsigned bytes) {
  u32x4 r = {(unsigned)__builtin_amdgcn_readfirstlane((unsigned)(u64)p), (unsigned)__builtin_amdgcn_readfirstlane((unsigned)((u64)p >> 32)) & 0xffffu, bytes, 0x00020000u};
  asm volatile("s_nop 4" : "+s"(r));
  return r;
}
constexpr int LD_AUX = 16, ST_AUX = 16;
__device__ __forceinline__ u32x4 ld16(__amdgpu_buffer_rsrc_t rs, unsigned off) {
  return __builtin_amdgcn_raw_buffer_load_b128(rs, off, 0, LD_AUX);
}
__device__ __forceinline__ void st16(__amdgpu_buffer_rsrc_t rs, unsigned off, u32x4 v) {
  __builtin_amdgcn_raw_buffer_store_b128(v, rs, off, 0, ST_AUX);
}
__device__ __forceinline__ void st16s(u32x4 v, __amdgpu_buffer_rsrc_t rs, unsigned voff, unsigned soff) {
  __builtin_amdgcn_raw_buffer_store_b128(v, rs, voff, soff, ST_AUX);
  asm volatile("s_nop 1" :: "v"(v) : "memory");
}
__device__ __forceinline__ void st8(__amdgpu_buffer_rsrc_t rs, unsigned off, u32x2 v) {
  __builtin_amdgcn_raw_buffer_store_b64(v, rs, off, 0, ST_AUX);
}
__device__ __forceinline__ void st4(__amdgpu_buffer_rsrc_t rs, unsigned off, unsigned v) {
  __builtin_amdgcn_raw_buffer_store_b32(v, rs, off, 0, ST_AUX);
}
__device__ __forceinline__ unsigned ld4(__amdgpu_buffer_rsrc_t rs, unsigned off) {
  return __builtin_amdgcn_raw_buffer_load_b32(rs, off, 0, LD_AUX);
}
constexpr int TU = 16;
__device__ __forceinline__ unsigned cyc32() { return (unsigned)__builtin_amdgcn_s_memtime(); }
__device__ __forceinline__ unsigned to_sgpr(unsigned x) {
  unsigned r;
  asm volatile("s_nop 0\n\tv_readfirstlane_b32 %0, %1\n\ts_nop 4" : "=s"(r) : "v"(x));
  return r;
}
struct Clk {
  unsigned ce, te;
  unsigned c2t, t2c;
  __device__ __forceinline__ void edge(unsigned& c, unsigned& t) {
    u64 c0, r0, r1, r2, r3, r4, r5;
    asm volatile("s_memtime %0\n\ts_memrealtime %1\n\ts_nop 4\n\ts_memrealtime %2\n\ts_nop 4\n\ts_memrealtime %3\n\ts_nop 4\n\t"
                 "s_memrealtime %4\n\ts_nop 4\n\ts_memrealtime %5\n\ts_nop 4\n\ts_memrealtime %6\n\ts_waitcnt lgkmcnt(0)"
                 : "=&s"(c0), "=&s"(r0), "=&s"(r1), "=&s"(r2), "=&s"(r3), "=&s"(r4), "=&s"(r5) :: "memory");
    const unsigned x = (unsigned)r0;
    const unsigned n = ((unsigned)r1 == x) + ((unsigned)r2 == x) + ((unsigned)r3 == x) + ((unsigned)r4 == x) + ((unsigned)r5 == x);
    c = to_sgpr((unsigned)c0 + 24u * n); t = (x + 4u) * TU;
  }
  __device__ __forceinline__ void init() { edge(ce, te); c2t = 44000u; t2c = 97600u; }
  __device__ __forceinline__ void sync() {
    unsigned c, t;
    edge(c, t);
    set(c, t);
  }
  __device__ __forceinline__ void set(unsigned c, unsigned t) {
    if (c == ce) return;
    const float q = (float)(t - te) * __builtin_amdgcn_rcpf((float)(c - ce));
    const float qc = fminf(fmaxf(q, 0.51f), 1.99f);
    c2t = to_sgpr((unsigned)(qc * 65536.f));
    t2c = to_sgpr((unsigned)(__builtin_amdgcn_rcpf(qc) * 65536.f));
    ce = c; te = t;
  }
  __device__ __forceinline__ unsigned time(unsigned c) const { return te + ((min(c - ce, 30000u) * c2t) >> 16); }
  __device__ __forceinline__ unsigned now() const { return time(cyc32()); }
  __device__ __forceinline__ unsigned target(unsigned t) const {
    return to_sgpr(ce + (((unsigned)min(max((int)(t - te), 0), 20000) * t2c) >> 16));
  }
  __device__ __forceinline__ void wait(unsigned t) const { wait_c(target(t)); }
  __device__ __forceinline__ void wait_c(unsigned c) const {
    unsigned y;
    asm volatile("1:\n\ts_memtime vcc\n\ts_waitcnt lgkmcnt(0)\n\ts_sub_i32 %0, %1, vcc_lo\n\ts_cmp_gt_i32 %0, 128\n\ts_cbranch_scc1 1b\n\t"
                 "2:\n\ts_sub_i32 %0, %0, %2\n\ts_cmp_gt_i32 %0, %3\n\ts_cbranch_scc1 2b"
                 : "=&s"(y) : "s"(c), "n"(W_STEP), "n"(W_OFF) : "memory", "scc", "vcc");
  }
};
__device__ __forceinline__ unsigned nosent(unsigned d) { return min(d, SENT - 1u); }
__device__ __forceinline__ int arow(int row, int half) { return 4 * (row >> 1) + 2 * half + (row & 1); }
__device__ __forceinline__ unsigned umax4(u32x4 v) { return max(max(v[0], v[1]), max(v[2], v[3])); }
__device__ __forceinline__ void stamp_again(GAS u64* p) {
  __hip_atomic_store(p, (__builtin_amdgcn_s_memrealtime() & ~7ull) | (__hip_atomic_load(p, __ATOMIC_RELAXED, __HIP_MEMORY_SCOPE_AGENT) & 7ull),
                     __ATOMIC_RELAXED, __HIP_MEMORY_SCOPE_AGENT);
}
__device__ __forceinline__ bool poll_fail(unsigned& spins, GAS unsigned* abortw, int lane) {
  ++spins;
  if (spins > SPIN_CAP) {
    if (lane == 0) __hip_atomic_store(abortw, 1u, __ATOMIC_RELAXED, __HIP_MEMORY_SCOPE_AGENT);
    return true;
  }
  if ((spins & 511u) == 0u && lane == 0) {
    unsigned n = 0;
    for (int i = 0; i < 8; ++i) n += __hip_atomic_load(abortw + (SY_CT - SY_ABORT + (CT_ARR + i) * 128) / 4, __ATOMIC_RELAXED, __HIP_MEMORY_SCOPE_AGENT) + 1u;
    if (n < 256u) stamp_again((GAS u64*)(abortw - 32));
  }
  if ((spins & 63u) == 0u)
    return __builtin_amdgcn_readfirstlane(__hip_atomic_load(abortw, __ATOMIC_RELAXED, __HIP_MEMORY_SCOPE_AGENT)) != SENT;
  return false;
}
struct Gate {
  u64 entry, sel; int arr; unsigned rot; bool open;
  __device__ __forceinline__ bool look(char* sync, u32x4 v) {
    if (open) return true;
    for (unsigned spins = 1;; ++spins) {
#pragma unroll
      for (int i = 0; i < 4; ++i) v[i] = __builtin_amdgcn_readfirstlane(v[i]);
      if (((u64)v[1] << 32 | v[0]) - entry + 2000u < (1ull << 31) || spins > 4 * SPIN_CAP) break;
      if (spins > 1) __builtin_amdgcn_s_sleep(8);
      v = ld16(mk_rsrc(sync + SY_EPOCH, 16), 0);
    }
    asm volatile("" ::: "memory");
    sel = (u64)v[3] << 32 | v[2];
    const bool same = rot == (v[0] & 7u);
    rot = v[0] & 7u; open = true;
    return same;
  }
  __device__ __forceinline__ void ask(char* sync, u32x4& v) { asm volatile("buffer_load_dwordx4 %0, off, %1, 0 sc1" : "=v"(v) : "s"(own_rsrc(sync + SY_EPOCH, 16)) : "memory"); }
  __device__ __forceinline__ void arrive(char* sync) {
    if (arr >= 0 && opaque_tid(0) == 0) __hip_atomic_fetch_add((GAS unsigned*)(sync + SY_CT + (CT_ARR + arr) * 128), 1u, __ATOMIC_RELAXED, __HIP_MEMORY_SCOPE_AGENT);
    arr = -1;
  }
  __device__ __forceinline__ bool pass(char* sync, u32x4 v = {0u, 0u, 0u, 0u}) { const bool r = look(sync, v); arrive(sync); return r; }
};
__device__ __forceinline__ unsigned ann_val(unsigned t, int s) { return (t << 8) | (unsigned)s; }
__device__ __forceinline__ unsigned tr_place(int s) { return (s & 2) * (CHK / 2) + (s & 1) * 4096; }
__device__ __forceinline__ unsigned die_half() { return ((unsigned)__builtin_amdgcn_s_getreg(20 | (3 << 11)) >> 2) & 1u; }
typedef u64 Sel;
__device__ __forceinline__ void start_wait(char* sync, GAS unsigned* abortw, int lane, bool& dead, int ci, int cj) {
  unsigned spins = 0;
  GAS unsigned* p = (GAS unsigned*)(sync + SY_CT + (lane == 0 ? CT_INIT : (lane == 1 ? ci : cj)) * 128);
  while (!__all(__hip_atomic_load(p, __ATOMIC_RELAXED, __HIP_MEMORY_SCOPE_AGENT) + 1u >= (lane == 0 ? 64u : 1u))) {
    __builtin_amdgcn_s_sleep(4);
    if (dead || poll_fail(spins, abortw, lane)) { dead = true; break; }
  }
}
__device__ __forceinline__ __amdgpu_buffer_rsrc_t rsrc_u(char* sync) { return mk_rsrc((void*)sync, SY_SET); }
__device__ __forceinline__ unsigned near_half(unsigned off, u64 sel) { return off + ((unsigned)(sel >> ((off >> 21) & 63)) & 1u) * 8192; }
__device__ __forceinline__ unsigned x_slot(int kind, int s, int h, Sel sel) {
  return kind == 2 ? near_half(32 * PAGE + (s * 32 + h) * CHK, ~sel) : near_half((s * 64 + 2 * h + 1 - kind) * CHK, sel);
}
__device__ __forceinline__ int hpos(int h) { return (h & 3) * 8 + (h >> 2); }
constexpr unsigned UXH = 8 * CHK;
__device__ __forceinline__ unsigned ux_base(int s, int h, Sel sel) { return near_half(((s & (URING - 1)) * 32 + h) * UXH, ~sel); }
__device__ __forceinline__ constexpr unsigned ux_tile(int t) { return (t >> 3) * CHK + (t & 7) * 1024; }
__device__ __forceinline__ __amdgpu_buffer_rsrc_t rsrc_hot(char* sync, Sel sel) {
  return mk_rsrc((void*)(sync + near_half(SY_HOT, sel)), H_BYTES - 8192);
}
template <int SLP = 4>
__device__ __forceinline__ void ctr_wait(GAS unsigned* c, unsigned need, GAS unsigned* abortw, int lane, bool& dead) {
  unsigned spins = 0;
  while (true) {
    unsigned v = __builtin_amdgcn_readfirstlane(__hip_atomic_load(c, __ATOMIC_RELAXED, __HIP_MEMORY_SCOPE_AGENT)) + 1u;
    if (v >= need) break;
    __builtin_amdgcn_s_sleep(SLP);
    if (dead || poll_fail(spins, abortw, lane)) { dead = true; break; }
  }
}
template <int N> __device__ __forceinline__ void wait_vm() { asm volatile("s_waitcnt vmcnt(%0)" :: "n"(N) : "memory"); }
__device__ __forceinline__ void lds_barrier() {
  asm volatile("s_waitcnt lgkmcnt(0)" ::: "memory");
  __builtin_amdgcn_s_barrier();
  asm volatile("" ::: "memory");
}
__device__ __forceinline__ f32x4 vzero() { float z; asm volatile("v_mov_b32 %0, 0" : "=v"(z)); f32x4 r = {z, z, z, z}; asm volatile("" : "+v"(r)); return r; }
__device__ __forceinline__ f32x4 smf(u32x4 a, u32x8 b, f32x4 c, int idx) {
  return __builtin_amdgcn_smfmac_f32_16x16x64_bf16(__builtin_bit_cast(bfv8, a), __builtin_bit_cast(bfv16, b), c, idx, 0, 0);
}
__device__ __forceinline__ float red16max(float x) { auto r = __builtin_amdgcn_permlane16_swap(__float_as_uint(x), __float_as_uint(x), false, false); return fmaxf(__uint_as_float(r[0]), __uint_as_float(r[1])); }
__device__ __forceinline__ float red32max(float x) { auto r = __builtin_amdgcn_permlane32_swap(__float_as_uint(x), __float_as_uint(x), false, false); return fmaxf(__uint_as_float(r[0]), __uint_as_float(r[1])); }
__device__ __forceinline__ float red16add(float x) { auto r = __builtin_amdgcn_permlane16_swap(__float_as_uint(x), __float_as_uint(x), false, false); return __uint_as_float(r[0]) + __uint_as_float(r[1]); }
__device__ __forceinline__ float red32add(float x) { auto r = __builtin_amdgcn_permlane32_swap(__float_as_uint(x), __float_as_uint(x), false, false); return __uint_as_float(r[0]) + __uint_as_float(r[1]); }
constexpr bool blk_in_lds(int R, int b) {
  return R == 0 ? ((b & 7) == 1 || (b & 7) == 4) : (b < 16 ? (b & 3) == 0 : (b & 3) == 3);
}
constexpr int lds_index(int R, int b) { int n = 0; for (int i = 0; i < b; ++i) if (blk_in_lds(R, i)) ++n; return n; }
constexpr bool frag_in_lds(int R, int f) { return blk_in_lds(R, f >> 1); }
constexpr int frag_reg_slot(int R, int f) { int n = 0; for (int i = 0; i < f; ++i) if (!frag_in_lds(R, i)) ++n; return n; }

constexpr int m_block(int R, int m, int i) {
  return R == 0 ? 2 * (i < 12 ? (m ? i + i / 3 + 1 : i + (i + 1) / 3) : 4 * (i - 12) + 2 - 2 * m) + m : m ? 16 + 4 * (i & 3) + (i >> 2) : (i < 12 ? i + i / 3 + 1 : 4 * (i - 12));
}
#define VO_ORD 1
constexpr int w_block(int R, int i) { return R && VO_ORD ? m_block(R, i < 12, i % 12) : m_block(R, i & 1, i >> 1); }
#define W_BG 0xfedcba9876543210ull
#define W_NT 0
#define U_ORD 0x3210
constexpr u64 bg_order(int m) { u64 r = 0; for (int n = 0; n < 16; ++n) r |= (u64)(m_block(0, m, (W_BG >> (4 * n)) & 15) >> 1) << (4 * n); return r; }
struct WSched { int ord[32]; int slot[32]; int issue[32]; int out[32]; int reg[32]; };
constexpr WSched make_wsched(int R) {
  WSched S{};
  int n = 0;
  for (int i = 0; i < 24; ++i) S.ord[n++] = w_block(R, i);
  for (int i = 0; i < n; ++i) S.reg[i] = frag_reg_slot(R, 2 * S.ord[i]);
  const int npure = n;
  for (int b = 0; b < 32; ++b) if (blk_in_lds(R, b)) S.ord[n++] = b;
  int seq[32] = {};
  int nissued = 0, next = 0;
  for (int i = 0; i < 10; ++i) { S.slot[i] = (i + 10 - npure % 10) % 10; S.issue[i] = -1; seq[i] = nissued++; }
  next = 10;
  S.out[0] = nissued - 1;
  for (int c = 0; c <= npure; ++c) {
    if (c >= 1) {
      const int k = S.slot[c - 1];
      if (next < npure) { S.slot[next] = k; S.issue[next] = c; seq[next] = nissued++; ++next; }
      else if (k < NLF / 2) { S.slot[npure + k] = k; S.issue[npure + k] = c; seq[npure + k] = nissued++; }
    }
    if (c + 1 < npure) S.out[c + 1] = nissued - 1 - seq[c + 1];
  }
  return S;
}
__device__ __forceinline__ int kcol(int k) { return 8 * ((k >> 1) & 1) + ((k & 1) ? 7 - (k >> 2) : k >> 2); }
template <bool PERM = false>
__device__ __forceinline__ u32x8 tr_frag(const char* sb, int tile, int lane) {
  u32x8 f;
#pragma unroll
  for (int q = 0; q < 4; ++q) {
    int row = (q >> 1) * 32 + 8 * (lane >> 4) + 4 * (q & 1) + ((lane & 15) >> 2);
    if (PERM) row = (row & ~15) + kcol(row & 15);
    int p = lane & 3;
    int lc = 2 * tile + (p >> 1);
    int addr = row * 256 + ((lc ^ swz_b(row)) * 16) + 8 * (p & 1);
    bf16x4 v = __builtin_amdgcn_ds_read_tr16_b64_v4i16((LAS bf16x4*)(sb + addr));
    u32x2 u = __builtin_bit_cast(u32x2, v);
    f[2 * q] = u[0]; f[2 * q + 1] = u[1];
  }
  return f;
}
__device__ __forceinline__ void dma16(__amdgpu_buffer_rsrc_t rs, unsigned voff, unsigned soff, unsigned lds, bool nt = false, bool pad = true) {
  if (nt) asm volatile("s_mov_b32 m0, %0\n\ts_nop 3\n\tbuffer_load_dwordx4 %1, %2, %3 offen nt lds" :: "s"(lds), "v"(voff), "s"(rs), "s"(soff) : "m0", "memory");
  else if (pad) asm volatile("s_mov_b32 m0, %0\n\ts_nop 3\n\tbuffer_load_dwordx4 %1, %2, %3 offen lds" :: "s"(lds), "v"(voff), "s"(rs), "s"(soff) : "m0", "memory");
  else asm volatile("s_mov_b32 m0, %0\n\ts_nop 0\n\tbuffer_load_dwordx4 %1, %2, %3 offen lds" :: "s"(lds), "v"(voff), "s"(rs), "s"(soff) : "m0", "memory");
}
__device__ __forceinline__ u32x8 tr_frag2(const char* sb, int tile, int f, int lane) {
  u32x8 r;
#pragma unroll
  for (int c = 0; c < 2; ++c)
#pragma unroll
    for (int t = 0; t < 2; ++t) {
      const int row = 32 * c + 16 * f + 4 * (lane >> 4) + ((lane & 15) >> 2);
      const int p = lane & 3, lc = 2 * (tile + t) + (p >> 1);
      const u32x2 u = __builtin_bit_cast(u32x2, __builtin_amdgcn_ds_read_tr16_b64_v4i16((LAS bf16x4*)(sb + row * 256 + ((lc ^ swz_b(row)) * 16) + 8 * (p & 1))));
      r[4 * c + t] = u[0]; r[4 * c + 2 + t] = u[1];
    }
  return r;
}
template <int ROLE>
__device__ __forceinline__ void load_weights(const ChainArgs& a, int h, int w, int lane, char* smem, Wts& W) {
#pragma unroll
  for (int i = 0; i < 4; ++i) asm volatile("" : "=v"(W.v[i]));
#pragma unroll
  for (int i = 0; i < 32; ++i) asm volatile("" : "=a"(W.a[i]));
  constexpr WSched S = make_wsched(ROLE);
  constexpr int NP = 32 - NLF / 2;
  const __amdgpu_buffer_rsrc_t r0 = mk_rsrc(ROLE == 0 ? a.Wq : a.Wv, 8u << 20), r1 = mk_rsrc(ROLE == 0 ? a.Wk : a.Wo, 8u << 20);
  const unsigned lds0 = (unsigned)(uintptr_t)(LAS char*)smem;
  unsigned vo[2][4];
#pragma unroll
  for (int j = 0; j < 4; ++j) {
    const int row = (w * 4 + j) * 4 + (lane >> 4), lc = (lane & 15) ^ swz_b(row);
    vo[0][j] = row * HD * 2 + lc * 16; vo[1][j] = row * DM * 2 + lc * 16;
  }
  asm volatile("s_nop 4");
  auto stage = [&](int i) {
    const int blk = S.ord[i];
    const bool second = ROLE == 0 ? (blk & 1) : blk >= 16, wo = ROLE == 1 && second;
    const unsigned so = wo ? (unsigned)(((128 * h + 64 * (blk & 1)) * DM + 128 * (((blk - 16) >> 1) ^ (2 * (h & 3)))) * 2)
                           : (unsigned)((64 * ((ROLE == 0 ? blk >> 1 : blk) ^ (h & RHO)) * HD + 128 * h) * 2);
#pragma unroll
    for (int j = 0; j < 4; ++j) dma16(second ? r1 : r0, vo[wo][j], so, lds0 + 16384 * S.slot[i] + (w * 4 + j) * 1024, W_NT & (ROLE ? 8 : 1));
  };
#pragma unroll
  for (int i = 0; i < 32; ++i) if (S.issue[i] == -1) stage(i);
#pragma unroll
  for (int c = 0; c < NP; ++c) {
    asm volatile("s_waitcnt vmcnt(%0)" :: "n"(4 * S.out[c]) : "memory");
    lds_barrier();
#pragma unroll
    for (int i = 0; i < 32; ++i) if (S.issue[i] == c) stage(i);
    const char* sb = smem + 16384 * S.slot[c];
#pragma unroll
    for (int t = 0; t < 2; ++t) {
      u32x8 f = (ROLE == 1 && S.ord[c] >= 16) ? tr_frag2(sb, 2 * w, t, lane) : tr_frag<true>(sb, 2 * w + t, lane);
      if (S.reg[c] < 32) {
#pragma unroll
        for (int i = 0; i < 8; ++i) asm volatile("v_accvgpr_write_b32 a[%1], %0" :: "v"(f[i]), "n"(8 * ((S.reg[c] + t) & 31) + i));
      }
      else {
#pragma unroll
        for (int i = 0; i < 8; ++i) W.v[(S.reg[c] >> 2) & 3][8 * ((S.reg[c] + t) & 3) + i] = f[i];
      }
    }
    if (S.reg[c] >= 32) asm volatile("" : "+v"(W.v[(S.reg[c] >> 2) & 3]));
  }
  static_assert(S.issue[31] < NP && S.issue[NP] < S.issue[31], "resident blocks are issued in slot order inside the loop");
#pragma unroll
  for (int j = 0; j < NLF / 2; ++j) {
    asm volatile("s_waitcnt vmcnt(%0)" :: "n"(4 * (NLF / 2 - 1 - j)) : "memory");
    lds_barrier();
    const bool two = ROLE == 1 && S.ord[NP + j] >= 16;
    u32x8 f0 = two ? tr_frag2(smem + 16384 * j, 2 * w, 0, lane) : tr_frag<true>(smem + 16384 * j, 2 * w, lane);
    u32x8 f1 = two ? tr_frag2(smem + 16384 * j, 2 * w, 1, lane) : tr_frag<true>(smem + 16384 * j, 2 * w + 1, lane);
    lds_barrier();
    char* d = smem + 16384 * j + w * 4096 + lane * 16;
    *(u32x4*)d = (u32x4){f0[0], f0[1], f0[2], f0[3]};
    *(u32x4*)(d + 1024) = (u32x4){f0[4], f0[5], f0[6], f0[7]};
    *(u32x4*)(d + 2048) = (u32x4){f1[0], f1[1], f1[2], f1[3]};
    *(u32x4*)(d + 3072) = (u32x4){f1[4], f1[5], f1[6], f1[7]};
  }
  lds_barrier();
}
template <int... Is> struct iseq {};
template <int N, int... Is> struct mkseq : mkseq<N - 1, N - 1, Is...> {};
template <int... Is> struct mkseq<0, Is...> { typedef iseq<Is...> type; };

#define SB0 __builtin_amdgcn_sched_barrier(0)
#define CAT8(x, y) ((u32x8){x[0], x[1], x[2], x[3], y[0], y[1], y[2], y[3]})
template <class F, int... I>
__device__ __forceinline__ void for_slots(iseq<I...>, F f) { (f(std::integral_constant<int, I>{}), ...); }
#define QK_BODY \
  "s_cmp_eq_u32 %[wv], 1\n\t" \
  "s_cbranch_scc1 91f\n\t" \
  "s_cmp_eq_u32 %[wv], 2\n\t" \
  "s_cbranch_scc1 92f\n\t" \
  "s_cmp_eq_u32 %[wv], 3\n\t" \
  "s_cbranch_scc1 93f\n\t" \
  "90:\n\t" \
  "s_mov_b32 s58, s42\n\t" \
  "20:\n\t" \
  "s_and_b32 s76, s44, 7\n\t" \
  "s_lshl_b32 s76, s76, 15\n\t" \
  "s_add_i32 s40, s54, s76\n\t" \
  "s_and_b32 s76, s44, 3\n\t" \
  "s_lshl_b32 s45, s76, 14\n\t" \
  "s_mul_i32 s47, s76, 0xc000\n\t" \
  "s_add_i32 s47, s47, 0x40000\n\t" \
  "s_add_i32 s49, s44, -1\n\t" \
  "s_and_b32 s48, s49, 2\n\t" \
  "s_lshl_b32 s48, s48, 13\n\t" \
  "s_and_b32 s76, s49, 1\n\t" \
  "s_lshl_b32 s76, s76, 12\n\t" \
  "s_add_i32 s48, s48, s76\n\t" \
  "s_lshl_b32 s50, s53, 8\n\t" \
  "s_mov_b32 s59, 0\n\t" \
  "s_mov_b32 s92, s51\n\t" \
  "s_mov_b32 s93, s53\n\t" \
  "s_xor_b32 s76, s40, 0x400\n\t" \
  "s_xor_b32 s77, s40, 0x800\n\t" \
  "s_xor_b32 s78, s40, 0xc00\n\t" \
  "s_cmp_lg_u32 s58, 0\n\t" \
  "s_cbranch_scc1 11f\n\t" \
  "10:\n\t" \
  "ds_read_b128 v[48:51], v102 offset:0\n\t" \
  "ds_read_b128 v[52:55], v102 offset:1024\n\t" \
  "ds_read_b128 v[56:59], v102 offset:2048\n\t" \
  "ds_read_b128 v[60:63], v102 offset:3072\n\t" \
  "ds_read_b128 v[16:19], v102 offset:16384\n\t" \
  "ds_read_b128 v[20:23], v102 offset:17408\n\t" \
  "ds_read_b128 v[24:27], v102 offset:18432\n\t" \
  "ds_read_b128 v[28:31], v102 offset:19456\n\t" \
  "ds_read_b128 v[84:87], v102 offset:49152\n\t" \
  "ds_read_b128 v[88:91], v102 offset:50176\n\t" \
  "ds_read_b128 v[92:95], v102 offset:51200\n\t" \
  "ds_read_b128 v[96:99], v102 offset:52224\n\t" \
  "s_waitcnt lgkmcnt(4)\n\t" \
  "ds_read_b128 v[0:3], v103 offset:16384\n\t" \
  "ds_read_b128 v[4:7], v103 offset:17408\n\t" \
  "ds_read_b128 v[8:11], v103 offset:18432\n\t" \
  "ds_read_b128 v[12:15], v103 offset:19456\n\t" \
  "s_bitcmp1_b32 s58, 1\n\t" \
  "s_cselect_b64 s[80:81], -1, 0\n\t" \
  "s_cmp_lg_u32 s58, 0\n\t" \
  "s_cbranch_scc1 17f\n\t" \
  "s_mov_b32 exec_hi, 0\n\t" \
  "16:\n\t" \
  "s_memtime vcc\n\t" \
  "s_waitcnt lgkmcnt(0)\n\t" \
  "s_sub_i32 s79, s41, vcc_lo\n\t" \
  "s_cmp_gt_i32 s79, 64\n\t" \
  "s_cbranch_scc1 16b\n\t" \
  "13:\n\t" \
  "buffer_load_dword v116, v112, %[rh], s48 offen sc1\n\t" \
  "s_waitcnt vmcnt(0)\n\t" \
  "15:\n\t" \
  "v_mov_b32 v117, 20000\n\t" \
  "v_cmp_ne_u32_sdwa vcc, v116, s49 src0_sel:BYTE_0 src1_sel:DWORD\n\t" \
  "v_subrev_u32 v116, s50, v116\n\t" \
  "v_ashrrev_i32 v116, 8, v116\n\t" \
  "s_nop 1\n\t" \
  "v_max_i32_dpp v116, v116, v116 quad_perm:[1,0,3,2] row_mask:0xf bank_mask:0xf\n\t" \
  "s_cbranch_vccnz 14f\n\t" \
  "s_nop 0\n\t" \
  "v_max_i32_dpp v116, v116, v116 quad_perm:[2,3,0,1] row_mask:0xf bank_mask:0xf\n\t" \
  "s_nop 1\n\t" \
  "v_max_i32_dpp v116, v116, v116 row_half_mirror row_mask:0xf bank_mask:0xf\n\t" \
  "s_nop 1\n\t" \
  "v_max_i32_dpp v116, v116, v116 row_mirror row_mask:0xf bank_mask:0xf\n\t" \
  "v_med3_i32 v116, v116, 0, v117\n\t" \
  "v_mul_u32_u24 v116, s52, v116\n\t" \
  "v_lshrrev_b32 v116, 16, v116\n\t" \
  "s_mov_b64 exec, -1\n\t" \
  "v_readfirstlane_b32 s84, v116\n\t" \
  "v_readlane_b32 s85, v116, 16\n\t" \
  "s_max_u32 s84, s84, s85\n\t" \
  "s_add_i32 s84, s84, s51\n\t" \
  "1:\n\t" \
  "s_memtime vcc\n\t" \
  "s_waitcnt lgkmcnt(0)\n\t" \
  "s_sub_i32 s79, s84, vcc_lo\n\t" \
  "s_cmp_gt_i32 s79, 128\n\t" \
  "s_cbranch_scc1 1b\n\t" \
  ".p2align 4\n\t" \
  "2:\n\t" \
  "s_sub_i32 s79, s79, 33\n\t" \
  "s_cmp_gt_i32 s79, 56\n\t" \
  "s_cbranch_scc1 2b\n\t" \
  "buffer_load_dwordx4 v[32:35], v100, %[rh], s40 offen sc1\n\t" \
  "s_nop 12\n\t" \
  "buffer_load_dwordx4 v[36:39], v100, %[rh], s76 offen sc1\n\t" \
  "s_nop 12\n\t" \
  "buffer_load_dwordx4 v[40:43], v100, %[rh], s77 offen sc1\n\t" \
  "s_nop 12\n\t" \
  "buffer_load_dwordx4 v[44:47], v100, %[rh], s78 offen sc1\n\t" \
  "s_memtime vcc\n\t" \
  "s_memrealtime s[82:83]\n\t" \
  "s_nop 4\n\t" \
  "s_memrealtime s[84:85]\n\t" \
  "s_nop 4\n\t" \
  "s_memrealtime s[56:57]\n\t" \
  "s_nop 4\n\t" \
  "s_memrealtime s[86:87]\n\t" \
  "s_nop 4\n\t" \
  "s_memrealtime s[88:89]\n\t" \
  "s_nop 4\n\t" \
  "s_memrealtime s[90:91]\n\t" \
  "s_mov_b32 s92, 0\n\t" \
  "s_waitcnt lgkmcnt(0)\n\t" \
  "s_cmp_eq_u32 s82, s84\n\t" \
  "s_addc_u32 s92, s92, 0\n\t" \
  "s_cmp_eq_u32 s82, s56\n\t" \
  "s_addc_u32 s92, s92, 0\n\t" \
  "s_cmp_eq_u32 s82, s86\n\t" \
  "s_addc_u32 s92, s92, 0\n\t" \
  "s_cmp_eq_u32 s82, s88\n\t" \
  "s_addc_u32 s92, s92, 0\n\t" \
  "s_cmp_eq_u32 s82, s90\n\t" \
  "s_addc_u32 s92, s92, 0\n\t" \
  "s_mul_i32 s92, s92, 24\n\t" \
  "s_add_i32 s92, s92, vcc_lo\n\t" \
  "s_lshl4_add_u32 s93, s82, 64\n\t" \
  "3:\n\t" \
  "s_sub_i32 s84, s92, s43\n\t" \
  "s_lshl_b32 s85, s93, 8\n\t" \
  "s_or_b32 s85, s85, s44\n\t" \
  "v_mov_b32 v118, s85\n\t" \
  "v_mov_b64 v[68:69], 0\n\t" \
  "v_mov_b64 v[70:71], 0\n\t" \
  "v_mov_b64 v[72:73], 0\n\t" \
  "v_mov_b64 v[74:75], 0\n\t" \
  "v_mov_b64 v[76:77], 0\n\t" \
  "v_mov_b64 v[78:79], 0\n\t" \
  "v_mov_b64 v[80:81], 0\n\t" \
  "v_mov_b64 v[82:83], 0\n\t" \
  "s_waitcnt lgkmcnt(0)\n\t" \
  "s_waitcnt vmcnt(3)\n\t" \
  "v_smfmac_f32_16x16x64_bf16 v[68:71], v[32:35], a[0:7], v104\n\t" \
  "ds_write_b128 v101, v[32:35] offset:0\n\t" \
  "v_smfmac_f32_16x16x64_bf16 v[72:75], v[32:35], a[8:15], v104\n\t" \
  "v_smfmac_f32_16x16x64_bf16 v[76:79], v[32:35], v[48:55], v104\n\t" \
  "v_smfmac_f32_16x16x64_bf16 v[80:83], v[32:35], v[56:63], v104\n\t" \
  "s_waitcnt vmcnt(2)\n\t" \
  "v_smfmac_f32_16x16x64_bf16 v[68:71], v[36:39], a[16:23], v104\n\t" \
  "ds_write_b128 v101, v[36:39] offset:1024\n\t" \
  "v_smfmac_f32_16x16x64_bf16 v[72:75], v[36:39], a[24:31], v104\n\t" \
  "v_smfmac_f32_16x16x64_bf16 v[76:79], v[36:39], a[32:39], v104\n\t" \
  "v_cmp_u_f32 vcc, v68, v69\n\t" \
  "v_smfmac_f32_16x16x64_bf16 v[80:83], v[36:39], a[40:47], v104\n\t" \
  "v_cmp_u_f32 s[82:83], v70, v71\n\t" \
  "s_waitcnt vmcnt(1)\n\t" \
  "v_smfmac_f32_16x16x64_bf16 v[68:71], v[40:43], v[16:23], v104\n\t" \
  "ds_write_b128 v101, v[40:43] offset:2048\n\t" \
  "v_smfmac_f32_16x16x64_bf16 v[72:75], v[40:43], v[24:31], v104\n\t" \
  "s_or_b64 vcc, vcc, s[82:83]\n\t" \
  "s_or_b64 vcc, vcc, s[80:81]\n\t" \
  "s_waitcnt lgkmcnt(1)\n\t" \
  "v_smfmac_f32_16x16x64_bf16 v[76:79], v[40:43], a[48:55], v104\n\t" \
  "s_cbranch_scc1 8f\n\t" \
  "5:\n\t" \
  "s_barrier\n\t" \
  "4:\n\t" \
  "s_memtime s[56:57]\n\t" \
  "ds_read_b128 v[48:51], v101 offset:4096\n\t" \
  "v_smfmac_f32_16x16x64_bf16 v[80:83], v[40:43], a[56:63], v104\n\t" \
  "ds_read_b128 v[52:55], v101 offset:5120\n\t" \
  "ds_read_b128 v[56:59], v101 offset:8192\n\t" \
  "s_waitcnt vmcnt(0)\n\t" \
  "v_smfmac_f32_16x16x64_bf16 v[68:71], v[44:47], a[64:71], v104\n\t" \
  "ds_write_b128 v101, v[44:47] offset:3072\n\t" \
  "v_smfmac_f32_16x16x64_bf16 v[72:75], v[44:47], a[72:79], v104\n\t" \
  "v_smfmac_f32_16x16x64_bf16 v[76:79], v[44:47], a[80:87], v104\n\t" \
  "v_cmp_u_f32 vcc, v68, v69\n\t" \
  "v_smfmac_f32_16x16x64_bf16 v[80:83], v[44:47], a[88:95], v104\n\t" \
  "v_cmp_u_f32 s[82:83], v70, v71\n\t" \
  "s_waitcnt lgkmcnt(3)\n\t" \
  "v_smfmac_f32_16x16x64_bf16 v[68:71], v[48:51], a[96:103], v104\n\t" \
  "ds_read_b128 v[60:63], v101 offset:9216\n\t" \
  "ds_read_b128 v[64:67], v101 offset:12288\n\t" \
  "v_smfmac_f32_16x16x64_bf16 v[72:75], v[48:51], a[104:111], v104\n\t" \
  "s_or_b64 vcc, vcc, s[82:83]\n\t" \
  "s_waitcnt lgkmcnt(2)\n\t" \
  "v_smfmac_f32_16x16x64_bf16 v[68:71], v[52:55], a[112:119], v104\n\t" \
  "s_cbranch_scc1 9f\n\t" \
  "6:\n\t" \
  "s_barrier\n\t" \
  "v_smfmac_f32_16x16x64_bf16 v[72:75], v[52:55], a[120:127], v104\n\t" \
  "s_waitcnt lgkmcnt(0)\n\t" \
  "ds_read_b128 v[48:51], v101 offset:13312\n\t" \
  "v_smfmac_f32_16x16x64_bf16 v[68:71], v[56:59], a[192:199], v104\n\t" \
  "ds_read_b128 v[52:55], v101 offset:6144\n\t" \
  "s_sub_i32 s79, s56, s84\n\t" \
  "s_mul_hi_u32 s79, s79, s46\n\t" \
  "v_smfmac_f32_16x16x64_bf16 v[72:75], v[56:59], a[200:207], v104\n\t" \
  "v_lshl_add_u32 v116, s79, 9, v118\n\t" \
  "buffer_store_dword v116, v109, %[rh], s45 offen sc1\n\t" \
  "v_smfmac_f32_16x16x64_bf16 v[68:71], v[60:63], a[208:215], v104\n\t" \
  "ds_read_b128 v[56:59], v101 offset:7168\n\t" \
  "v_smfmac_f32_16x16x64_bf16 v[72:75], v[60:63], a[216:223], v104\n\t" \
  "v_smfmac_f32_16x16x64_bf16 v[68:71], v[64:67], v[152:159], v104\n\t" \
  "ds_read_b128 v[60:63], v101 offset:10240\n\t" \
  "v_smfmac_f32_16x16x64_bf16 v[72:75], v[64:67], v[160:167], v104\n\t" \
  "s_waitcnt lgkmcnt(3)\n\t" \
  "v_smfmac_f32_16x16x64_bf16 v[68:71], v[48:51], v[168:175], v104\n\t" \
  "ds_read_b128 v[64:67], v101 offset:11264\n\t" \
  "ds_read_b128 v[16:19], v103 offset:49152\n\t" \
  "v_smfmac_f32_16x16x64_bf16 v[72:75], v[48:51], v[176:183], v104\n\t" \
  "s_waitcnt lgkmcnt(4)\n\t" \
  "v_smfmac_f32_16x16x64_bf16 v[68:71], v[52:55], v[84:91], v104\n\t" \
  "ds_read_b128 v[48:51], v101 offset:15360\n\t" \
  "ds_read_b128 v[20:23], v103 offset:50176\n\t" \
  "v_smfmac_f32_16x16x64_bf16 v[72:75], v[52:55], v[92:99], v104\n\t" \
  "s_waitcnt lgkmcnt(5)\n\t" \
  "v_smfmac_f32_16x16x64_bf16 v[68:71], v[56:59], a[160:167], v104\n\t" \
  "ds_read_b128 v[52:55], v101 offset:14336\n\t" \
  "ds_read_b128 v[24:27], v103 offset:51200\n\t" \
  "v_smfmac_f32_16x16x64_bf16 v[72:75], v[56:59], a[168:175], v104\n\t" \
  "ds_read_b128 v[28:31], v103 offset:52224\n\t" \
  "s_waitcnt lgkmcnt(7)\n\t" \
  "v_smfmac_f32_16x16x64_bf16 v[68:71], v[60:63], v[0:7], v104\n\t" \
  "ds_read_b128 v[32:35], v101 offset:20480\n\t" \
  "v_smfmac_f32_16x16x64_bf16 v[72:75], v[60:63], v[8:15], v104\n\t" \
  "ds_read_b128 v[36:39], v101 offset:21504\n\t" \
  "v_mov_b64 v[92:93], 0\n\t" \
  "s_waitcnt lgkmcnt(8)\n\t" \
  "v_smfmac_f32_16x16x64_bf16 v[68:71], v[64:67], v[120:127], v104\n\t" \
  "ds_read_b128 v[0:3], v102 offset:32768\n\t" \
  "ds_read_b128 v[40:43], v101 offset:22528\n\t" \
  "v_smfmac_f32_16x16x64_bf16 v[72:75], v[64:67], v[128:135], v104\n\t" \
  "ds_read_b128 v[4:7], v102 offset:33792\n\t" \
  "s_waitcnt lgkmcnt(9)\n\t" \
  "v_smfmac_f32_16x16x64_bf16 v[68:71], v[48:51], v[216:223], v104\n\t" \
  "ds_read_b128 v[44:47], v101 offset:23552\n\t" \
  "v_mov_b64 v[94:95], 0\n\t" \
  "s_waitcnt lgkmcnt(8)\n\t" \
  "v_smfmac_f32_16x16x64_bf16 v[68:71], v[52:55], v[16:23], v104\n\t" \
  "ds_read_b128 v[8:11], v102 offset:34816\n\t" \
  "v_smfmac_f32_16x16x64_bf16 v[72:75], v[48:51], v[224:231], v104\n\t" \
  "ds_read_b128 v[12:15], v102 offset:35840\n\t" \
  "s_waitcnt lgkmcnt(8)\n\t" \
  "v_smfmac_f32_16x16x64_bf16 v[72:75], v[52:55], v[24:31], v104\n\t" \
  "v_add_f32 v96, v68, v70\n\t" \
  "v_smfmac_f32_16x16x64_bf16 v[76:79], v[56:59], a[176:183], v104\n\t" \
  "v_add_f32 v97, v69, v71\n\t" \
  "v_cvt_pk_bf16_f32 v96, v96, v97\n\t" \
  "v_smfmac_f32_16x16x64_bf16 v[80:83], v[56:59], a[184:191], v104\n\t" \
  "v_add_f32 v98, v72, v74\n\t" \
  "v_add_f32 v99, v73, v75\n\t" \
  "v_smfmac_f32_16x16x64_bf16 v[76:79], v[60:63], a[240:247], v104\n\t" \
  "v_cvt_pk_bf16_f32 v98, v98, v99\n\t" \
  "ds_write2st64_b32 v105, v96, v98 offset1:1\n\t" \
  "v_smfmac_f32_16x16x64_bf16 v[80:83], v[60:63], a[248:255], v104\n\t" \
  "ds_read_b128 v[56:59], v101 offset:4096\n\t" \
  "ds_read_b128 v[16:19], v101 offset:24576\n\t" \
  "v_mov_b64 v[68:69], 0\n\t" \
  "v_smfmac_f32_16x16x64_bf16 v[76:79], v[64:67], v[136:143], v104\n\t" \
  "ds_read_b128 v[20:23], v101 offset:25600\n\t" \
  "v_mov_b64 v[70:71], 0\n\t" \
  "v_smfmac_f32_16x16x64_bf16 v[80:83], v[64:67], v[144:151], v104\n\t" \
  "ds_read_b128 v[24:27], v101 offset:26624\n\t" \
  "v_smfmac_f32_16x16x64_bf16 v[76:79], v[48:51], v[232:239], v104\n\t" \
  "s_waitcnt lgkmcnt(4)\n\t" \
  "s_barrier\n\t" \
  "ds_read_b64_tr_b16 v[84:85], v107 offset:0\n\t" \
  "v_smfmac_f32_16x16x64_bf16 v[80:83], v[48:51], v[240:247], v104\n\t" \
  "ds_read_b64_tr_b16 v[86:87], v107 offset:128\n\t" \
  "ds_read_b64_tr_b16 v[88:89], v107 offset:1024\n\t" \
  "v_smfmac_f32_16x16x64_bf16 v[76:79], v[52:55], v[200:207], v104\n\t" \
  "ds_read_b128 v[60:63], v101 offset:13312\n\t" \
  "ds_read_b64_tr_b16 v[90:91], v107 offset:1152\n\t" \
  "v_smfmac_f32_16x16x64_bf16 v[80:83], v[52:55], v[208:215], v104\n\t" \
  "ds_read_b128 v[64:67], v101 offset:6144\n\t" \
  "ds_read_b128 v[28:31], v101 offset:27648\n\t" \
  "s_waitcnt lgkmcnt(10)\n\t" \
  "v_smfmac_f32_16x16x64_bf16 v[76:79], v[56:59], v[0:7], v104\n\t" \
  "ds_read_b128 v[48:51], v101 offset:8192\n\t" \
  "s_waitcnt lgkmcnt(6)\n\t" \
  "v_smfmac_f32_16x16x64_bf16 v[92:95], v[84:87], v[32:39], v104\n\t" \
  "ds_read_b128 v[0:3], v103 offset:0\n\t" \
  "s_waitcnt lgkmcnt(4)\n\t" \
  "v_smfmac_f32_16x16x64_bf16 v[92:95], v[88:91], v[40:47], v104\n\t" \
  "ds_read_b128 v[4:7], v103 offset:1024\n\t" \
  "v_smfmac_f32_16x16x64_bf16 v[68:71], v[84:87], v[16:23], v104\n\t" \
  "s_waitcnt lgkmcnt(3)\n\t" \
  "v_smfmac_f32_16x16x64_bf16 v[68:71], v[88:91], v[24:31], v104\n\t" \
  "v_add_f32 v92, v92, v94\n\t" \
  "v_add_f32 v94, v93, v95\n\t" \
  "v_smfmac_f32_16x16x64_bf16 v[80:83], v[56:59], v[8:15], v104\n\t" \
  "ds_read_b128 v[8:11], v103 offset:2048\n\t" \
  "v_mov_b32 v97, v94\n\t" \
  "v_mov_b32_dpp v94, v92 row_ror:8 row_mask:0xf bank_mask:0x3\n\t" \
  "s_nop 0\n\t" \
  "v_mov_b32_dpp v92, v97 row_ror:8 row_mask:0xf bank_mask:0xc\n\t" \
  "v_smfmac_f32_16x16x64_bf16 v[76:79], v[60:63], v[184:191], v104\n\t" \
  "ds_read_b128 v[52:55], v101 offset:5120\n\t" \
  "ds_read_b128 v[12:15], v103 offset:3072\n\t" \
  "v_add_f32 v93, v68, v70\n\t" \
  "v_add_f32 v95, v69, v71\n\t" \
  "v_max_f32 v98, v92, v94\n\t" \
  "v_mov_b32 v97, v95\n\t" \
  "v_mov_b32_dpp v95, v93 row_ror:8 row_mask:0xf bank_mask:0x3\n\t" \
  "v_smfmac_f32_16x16x64_bf16 v[80:83], v[60:63], v[192:199], v104\n\t" \
  "v_mov_b32_dpp v93, v97 row_ror:8 row_mask:0xf bank_mask:0xc\n\t" \
  "v_max3_f32 v98, v98, v93, v95\n\t" \
  "v_smfmac_f32_16x16x64_bf16 v[76:79], v[64:67], a[144:151], v104\n\t" \
  "ds_read_b128 v[56:59], v101 offset:9216\n\t" \
  "ds_read_b128 v[32:35], v103 offset:32768\n\t" \
  "v_max_f32_dpp v98, v98, v98 quad_perm:[1,0,3,2] row_mask:0xf bank_mask:0xf\n\t" \
  "v_smfmac_f32_16x16x64_bf16 v[80:83], v[64:67], a[152:159], v104\n\t" \
  "ds_read_b128 v[60:63], v101 offset:12288\n\t" \
  "ds_read_b128 v[36:39], v103 offset:33792\n\t" \
  "v_max_f32_dpp v98, v98, v98 quad_perm:[2,3,0,1] row_mask:0xf bank_mask:0xf\n\t" \
  "s_waitcnt lgkmcnt(7)\n\t" \
  "v_smfmac_f32_16x16x64_bf16 v[76:79], v[48:51], v[0:7], v104\n\t" \
  "ds_read_b128 v[40:43], v103 offset:34816\n\t" \
  "v_max_f32_dpp v98, v98, v98 row_half_mirror row_mask:0xf bank_mask:0xf\n\t" \
  "v_mul_f32 v98, %[cs], v98\n\t" \
  "v_fma_f32 v92, v92, %[cs], -v98\n\t" \
  "v_fma_f32 v93, v93, %[cs], -v98\n\t" \
  "v_fma_f32 v94, v94, %[cs], -v98\n\t" \
  "s_waitcnt lgkmcnt(5)\n\t" \
  "v_smfmac_f32_16x16x64_bf16 v[80:83], v[48:51], v[8:15], v104\n\t" \
  "ds_read_b128 v[44:47], v103 offset:35840\n\t" \
  "v_fma_f32 v95, v95, %[cs], -v98\n\t" \
  "v_exp_f32 v72, v92\n\t" \
  "v_exp_f32 v73, v93\n\t" \
  "v_exp_f32 v74, v94\n\t" \
  "v_exp_f32 v75, v95\n\t" \
  "v_cvt_pk_bf16_f32 v68, v72, v73\n\t" \
  "v_cvt_pk_bf16_f32 v69, v74, v75\n\t" \
  "buffer_store_dwordx2 v[68:69], v110, %[rh], s47 offen sc1\n\t" \
  "v_smfmac_f32_16x16x64_bf16 v[76:79], v[52:55], a[128:135], v104\n\t" \
  "v_smfmac_f32_16x16x64_bf16 v[80:83], v[52:55], a[136:143], v104\n\t" \
  "s_waitcnt lgkmcnt(5)\n\t" \
  "v_smfmac_f32_16x16x64_bf16 v[76:79], v[56:59], a[224:231], v104\n\t" \
  "s_waitcnt lgkmcnt(2)\n\t" \
  "v_smfmac_f32_16x16x64_bf16 v[76:79], v[60:63], v[32:39], v104\n\t" \
  "v_smfmac_f32_16x16x64_bf16 v[80:83], v[56:59], a[232:239], v104\n\t" \
  "s_waitcnt lgkmcnt(0)\n\t" \
  "v_smfmac_f32_16x16x64_bf16 v[80:83], v[60:63], v[40:47], v104\n\t" \
  "v_add_f32 v20, v76, v78\n\t" \
  "v_add_f32 v21, v77, v79\n\t" \
  "v_cvt_pk_bf16_f32 v20, v20, v21\n\t" \
  "ds_write_b32 v106, v20\n\t" \
  "s_nop 0\n\t" \
  "s_nop 0\n\t" \
  "s_nop 0\n\t" \
  "s_nop 0\n\t" \
  "v_add_f32 v23, v80, v82\n\t" \
  "v_add_f32 v21, v81, v83\n\t" \
  "v_cvt_pk_bf16_f32 v21, v23, v21\n\t" \
  "ds_write_b32 v106, v21 offset:256\n\t" \
  "s_waitcnt lgkmcnt(0)\n\t" \
  "s_barrier\n\t" \
  "ds_read_b64_tr_b16 v[4:5], v108\n\t" \
  "ds_read_b64_tr_b16 v[6:7], v108 offset:64\n\t" \
  "ds_read_b64_tr_b16 v[8:9], v108 offset:512\n\t" \
  "ds_read_b64_tr_b16 v[10:11], v108 offset:576\n\t" \
  "ds_read_b64_tr_b16 v[12:13], v108 offset:1024\n\t" \
  "ds_read_b64_tr_b16 v[14:15], v108 offset:1088\n\t" \
  "ds_read_b64_tr_b16 v[16:17], v108 offset:1536\n\t" \
  "ds_read_b64_tr_b16 v[18:19], v108 offset:1600\n\t" \
  "v_mov_b64 v[0:1], 0\n\t" \
  "v_mov_b64 v[2:3], 0\n\t" \
  "v_add_f32 v29, v72, v73\n\t" \
  "v_add_f32 v29, v29, v74\n\t" \
  "s_waitcnt lgkmcnt(4)\n\t" \
  "v_smfmac_f32_16x16x64_bf16 v[0:3], v[84:87], v[4:11], v104\n\t" \
  "s_waitcnt lgkmcnt(0)\n\t" \
  "v_smfmac_f32_16x16x64_bf16 v[0:3], v[88:91], v[12:19], v104\n\t" \
  "v_add_f32 v29, v29, v75\n\t" \
  "s_mov_b32 s76, 0xaaaaaaaa\n\t" \
  "s_mov_b32 s77, s76\n\t" \
  "s_nop 4\n\t" \
  "v_add_f32 v96, v0, v2\n\t" \
  "v_add_f32_dpp v96, v1, v3 quad_perm:[0,1,2,3] row_mask:0xf bank_mask:0xc\n\t" \
  "v_fma_f32 v96, v96, %[cs], -v98\n\t" \
  "v_exp_f32 v24, v96\n\t" \
  "s_nop 0\n\t" \
  "v_add_f32 v29, v29, v24\n\t" \
  "s_nop 0\n\t" \
  "v_mov_b32_dpp v25, v24 quad_perm:[1,0,3,2] row_mask:0xf bank_mask:0xf\n\t" \
  "v_add_f32_dpp v29, v29, v29 quad_perm:[1,0,3,2] row_mask:0xf bank_mask:0xf\n\t" \
  "v_cndmask_b32_e64 v24, v24, v114, s[76:77]\n\t" \
  "v_cndmask_b32_e64 v25, v25, v115, s[76:77]\n\t" \
  "v_add_f32_dpp v29, v29, v29 quad_perm:[2,3,0,1] row_mask:0xf bank_mask:0xf\n\t" \
  "s_nop 1\n\t" \
  "v_add_f32_dpp v29, v29, v29 row_half_mirror row_mask:0xf bank_mask:0xf\n\t" \
  "v_rcp_f32 v26, v29\n\t" \
  "v_cmpx_gt_f32 vcc, 0x5d5e0b6b, v29\n\t" \
  "v_mul_f32 v24, v24, v26\n\t" \
  "v_mul_f32 v25, v25, v26\n\t" \
  "v_cvt_pk_bf16_f32 v24, v24, v25\n\t" \
  "buffer_store_dword v24, v111, %[rh], s47 offen sc1\n\t" \
  "s_memtime s[76:77]\n\t" \
  "s_mov_b64 exec, -1\n\t" \
  "v_mov_b32 v97, v29\n\t" \
  "s_cmp_eq_u64 vcc, -1\n\t" \
  "s_cselect_b32 s42, 0, 8\n\t" \
  "s_waitcnt lgkmcnt(0)\n\t" \
  "s_mov_b32 s57, s76\n\t" \
  "s_barrier\n\t" \
  "s_add_i32 s41, s56, 1950\n\t" \
  "s_sub_i32 s76, s57, s56\n\t" \
  "s_add_i32 s77, s43, 8\n\t" \
  "s_min_u32 s76, s76, s77\n\t" \
  "s_sub_i32 s43, s43, 4\n\t" \
  "s_max_u32 s43, s43, s76\n\t" \
  "s_add_i32 s76, s45, 0x4000\n\t" \
  "s_and_b32 s76, s76, 0xc000\n\t" \
  "v_mov_b32 v116, -1\n\t" \
  "buffer_store_dword v116, v109, %[rh], s76 offen sc1\n\t" \
  "s_sub_i32 s76, s92, s51\n\t" \
  "s_sub_i32 s77, s93, s53\n\t" \
  "s_cmp_eq_u32 s76, 0\n\t" \
  "s_cbranch_scc1 21f\n\t" \
  "v_cvt_f32_u32 v117, s76\n\t" \
  "v_cvt_f32_u32 v116, s77\n\t" \
  "v_rcp_f32 v117, v117\n\t" \
  "s_mov_b32 s51, s92\n\t" \
  "v_mul_f32 v117, v117, v116\n\t" \
  "v_max_f32 v117, 0x3f028f5c, v117\n\t" \
  "v_min_f32 v117, 0x3ffeb852, v117\n\t" \
  "v_rcp_f32 v116, v117\n\t" \
  "v_mul_f32 v117, 0x4f000000, v117\n\t" \
  "v_mul_f32 v116, 0x47800000, v116\n\t" \
  "v_cvt_u32_f32 v117, v117\n\t" \
  "v_cvt_u32_f32 v116, v116\n\t" \
  "s_mov_b32 s53, s93\n\t" \
  "v_readfirstlane_b32 s46, v117\n\t" \
  "v_readfirstlane_b32 s52, v116\n\t" \
  "21:\n\t" \
  "s_add_i32 s44, s44, 1\n\t" \
  "s_and_b32 s58, s58, 4\n\t" \
  "s_and_b32 s76, s44, 64\n\t" \
  "s_or_b32 s42, s42, s76\n\t" \
  "s_cbranch_scc0 20b\n\t" \
  "s_branch 99f\n\t" \
  "14:\n\t" \
  "s_sleep 3\n\t" \
  "s_add_u32 s59, s59, 0x10000\n\t" \
  "s_cmp_lt_u32 s59, 0x100000\n\t" \
  "s_cbranch_scc1 13b\n\t" \
  "s_mov_b64 exec, -1\n\t" \
  "s_mov_b32 s42, 16\n\t" \
  "s_branch 99f\n\t" \
  "8:\n\t" \
  "s_bitcmp1_b32 s58, 1\n\t" \
  "s_cbranch_scc1 4b\n\t" \
  "s_bitcmp1_b32 s58, 2\n\t" \
  "s_cbranch_scc1 5b\n\t" \
  "v_max3_u32 v116, v32, v33, v34\n\t" \
  "v_max3_u32 v117, v35, v36, v37\n\t" \
  "v_max3_u32 v116, v116, v38, v39\n\t" \
  "v_max_u32 v116, v116, v117\n\t" \
  "v_cmp_eq_u32 vcc, -1, v116\n\t" \
  "s_cbranch_vccz 5b\n\t" \
  "s_branch 0f\n\t" \
  "9:\n\t" \
  "s_bitcmp1_b32 s58, 2\n\t" \
  "s_cbranch_scc1 6b\n\t" \
  "v_max3_u32 v116, v40, v41, v42\n\t" \
  "v_max3_u32 v117, v43, v44, v45\n\t" \
  "v_max3_u32 v116, v116, v46, v47\n\t" \
  "v_max_u32 v116, v116, v117\n\t" \
  "v_cmp_eq_u32 vcc, -1, v116\n\t" \
  "s_cbranch_vccz 6b\n\t" \
  "s_or_b32 s58, s58, 2\n\t" \
  "0:\n\t" \
  "s_or_b32 s58, s58, 1\n\t" \
  "11:\n\t" \
  "buffer_load_dwordx4 v[32:35], v100, %[rh], s40 offen sc1\n\t" \
  "buffer_load_dwordx4 v[36:39], v100, %[rh], s76 offen sc1\n\t" \
  "buffer_load_dwordx4 v[40:43], v100, %[rh], s77 offen sc1\n\t" \
  "buffer_load_dwordx4 v[44:47], v100, %[rh], s78 offen sc1\n\t" \
  "s_waitcnt lgkmcnt(0)\n\t" \
  "s_branch 10b\n\t" \
  "17:\n\t" \
  "s_add_u32 s59, s59, 1\n\t" \
  "s_bitcmp1_b32 s58, 2\n\t" \
  "s_cbranch_scc1 3b\n\t" \
  "s_waitcnt vmcnt(0)\n\t" \
  "v_max_u32 v116, v32, v33\n\t" \
  "v_max3_u32 v116, v116, v34, v35\n\t" \
  "v_max3_u32 v116, v116, v36, v37\n\t" \
  "v_max3_u32 v116, v116, v38, v39\n\t" \
  "v_max3_u32 v116, v116, v40, v41\n\t" \
  "v_max3_u32 v116, v116, v42, v43\n\t" \
  "v_max3_u32 v116, v116, v44, v45\n\t" \
  "v_max3_u32 v116, v116, v46, v47\n\t" \
  "v_cmp_eq_u32 vcc, -1, v116\n\t" \
  "s_cbranch_vccz 3b\n\t" \
  "s_bitcmp1_b32 s59, 6\n\t" \
  "s_cbranch_scc1 12f\n\t" \
  "buffer_load_dwordx4 v[32:35], v100, %[rh], s40 offen sc1\n\t" \
  "buffer_load_dwordx4 v[36:39], v100, %[rh], s76 offen sc1\n\t" \
  "buffer_load_dwordx4 v[40:43], v100, %[rh], s77 offen sc1\n\t" \
  "buffer_load_dwordx4 v[44:47], v100, %[rh], s78 offen sc1\n\t" \
  "s_branch 17b\n\t" \
  "12:\n\t" \
  "s_mov_b32 s42, s58\n\t" \
  "s_branch 99f\n\t" \
  "91:\n\t" \
  "s_mov_b32 s58, s42\n\t" \
  "s_mov_b32 s95, 0\n\t" \
  "20:\n\t" \
  "s_and_b32 s76, s44, 7\n\t" \
  "s_lshl_b32 s76, s76, 15\n\t" \
  "s_add_i32 s40, s54, s76\n\t" \
  "s_and_b32 s76, s44, 3\n\t" \
  "s_lshl_b32 s45, s76, 14\n\t" \
  "s_add_i32 s47, s44, 1\n\t" \
  "s_min_u32 s47, s47, 63\n\t" \
  "s_lshr_b32 s76, s47, 1\n\t" \
  "s_lshl_b32 s47, s47, 20\n\t" \
  "s_add_i32 s47, s47, s55\n\t" \
  "s_bitcmp1_b64 %[sel], s76\n\t" \
  "s_cselect_b32 s76, 0x2000, 0\n\t" \
  "s_add_i32 s47, s47, s76\n\t" \
  "s_lshr_b32 s76, s44, 1\n\t" \
  "s_lshl_b32 s94, s44, 20\n\t" \
  "s_bitcmp1_b64 %[sel], s76\n\t" \
  "s_cselect_b32 s76, 0x2000, 0\n\t" \
  "s_add_i32 s94, s94, s76\n\t" \
  "s_add_i32 s49, s44, -1\n\t" \
  "s_and_b32 s48, s49, 2\n\t" \
  "s_lshl_b32 s48, s48, 13\n\t" \
  "s_and_b32 s76, s49, 1\n\t" \
  "s_lshl_b32 s76, s76, 12\n\t" \
  "s_add_i32 s48, s48, s76\n\t" \
  "s_lshl_b32 s50, s53, 8\n\t" \
  "s_mov_b32 s59, 0\n\t" \
  "s_mov_b32 s92, s51\n\t" \
  "s_mov_b32 s93, s53\n\t" \
  "s_xor_b32 s76, s40, 0x400\n\t" \
  "s_xor_b32 s77, s40, 0x800\n\t" \
  "s_xor_b32 s78, s40, 0xc00\n\t" \
  "s_cmp_lg_u32 s58, 0\n\t" \
  "s_cbranch_scc1 11f\n\t" \
  "10:\n\t" \
  "ds_read_b128 v[48:51], v102 offset:32768\n\t" \
  "ds_read_b128 v[52:55], v102 offset:33792\n\t" \
  "ds_read_b128 v[56:59], v102 offset:34816\n\t" \
  "ds_read_b128 v[60:63], v102 offset:35840\n\t" \
  "ds_read_b128 v[16:19], v102 offset:49152\n\t" \
  "ds_read_b128 v[20:23], v102 offset:50176\n\t" \
  "ds_read_b128 v[24:27], v102 offset:51200\n\t" \
  "ds_read_b128 v[28:31], v102 offset:52224\n\t" \
  "ds_read_b128 v[84:87], v103 offset:16384\n\t" \
  "ds_read_b128 v[88:91], v103 offset:17408\n\t" \
  "ds_read_b128 v[92:95], v103 offset:18432\n\t" \
  "ds_read_b128 v[96:99], v103 offset:19456\n\t" \
  "s_waitcnt lgkmcnt(4)\n\t" \
  "ds_read_b128 v[0:3], v103 offset:49152\n\t" \
  "ds_read_b128 v[4:7], v103 offset:50176\n\t" \
  "ds_read_b128 v[8:11], v103 offset:51200\n\t" \
  "ds_read_b128 v[12:15], v103 offset:52224\n\t" \
  "s_bitcmp1_b32 s58, 1\n\t" \
  "s_cselect_b64 s[80:81], -1, 0\n\t" \
  "s_cmp_lg_u32 s58, 0\n\t" \
  "s_cbranch_scc1 17f\n\t" \
  "s_mov_b32 exec_hi, 0\n\t" \
  "s_cmp_eq_u32 s95, 0\n\t" \
  "s_cbranch_scc1 16f\n\t" \
  "s_waitcnt vmcnt(2)\n\t" \
  "v_mov_b32 v116, v118\n\t" \
  "s_branch 15f\n\t" \
  "16:\n\t" \
  "s_memtime vcc\n\t" \
  "s_waitcnt lgkmcnt(0)\n\t" \
  "s_sub_i32 s79, s41, vcc_lo\n\t" \
  "s_cmp_gt_i32 s79, 64\n\t" \
  "s_cbranch_scc1 16b\n\t" \
  "13:\n\t" \
  "buffer_load_dword v116, v112, %[rh], s48 offen sc1\n\t" \
  "s_waitcnt vmcnt(0)\n\t" \
  "15:\n\t" \
  "v_mov_b32 v117, 20000\n\t" \
  "v_cmp_ne_u32_sdwa vcc, v116, s49 src0_sel:BYTE_0 src1_sel:DWORD\n\t" \
  "v_subrev_u32 v116, s50, v116\n\t" \
  "v_ashrrev_i32 v116, 8, v116\n\t" \
  "s_nop 1\n\t" \
  "v_max_i32_dpp v116, v116, v116 quad_perm:[1,0,3,2] row_mask:0xf bank_mask:0xf\n\t" \
  "s_cbranch_vccnz 14f\n\t" \
  "s_nop 0\n\t" \
  "v_max_i32_dpp v116, v116, v116 quad_perm:[2,3,0,1] row_mask:0xf bank_mask:0xf\n\t" \
  "s_nop 1\n\t" \
  "v_max_i32_dpp v116, v116, v116 row_half_mirror row_mask:0xf bank_mask:0xf\n\t" \
  "s_nop 1\n\t" \
  "v_max_i32_dpp v116, v116, v116 row_mirror row_mask:0xf bank_mask:0xf\n\t" \
  "v_med3_i32 v116, v116, 0, v117\n\t" \
  "v_mul_u32_u24 v116, s52, v116\n\t" \
  "v_lshrrev_b32 v116, 16, v116\n\t" \
  "s_mov_b64 exec, -1\n\t" \
  "v_readfirstlane_b32 s84, v116\n\t" \
  "v_readlane_b32 s85, v116, 16\n\t" \
  "s_max_u32 s84, s84, s85\n\t" \
  "s_add_i32 s84, s84, s51\n\t" \
  "1:\n\t" \
  "s_memtime vcc\n\t" \
  "s_waitcnt lgkmcnt(0)\n\t" \
  "s_sub_i32 s79, s84, vcc_lo\n\t" \
  "s_cmp_gt_i32 s79, 128\n\t" \
  "s_cbranch_scc1 1b\n\t" \
  ".p2align 4\n\t" \
  "2:\n\t" \
  "s_sub_i32 s79, s79, 33\n\t" \
  "s_cmp_gt_i32 s79, 56\n\t" \
  "s_cbranch_scc1 2b\n\t" \
  "buffer_load_dwordx4 v[32:35], v100, %[rh], s40 offen sc1\n\t" \
  "s_nop 12\n\t" \
  "buffer_load_dwordx4 v[36:39], v100, %[rh], s76 offen sc1\n\t" \
  "s_nop 12\n\t" \
  "buffer_load_dwordx4 v[40:43], v100, %[rh], s77 offen sc1\n\t" \
  "s_nop 12\n\t" \
  "buffer_load_dwordx4 v[44:47], v100, %[rh], s78 offen sc1\n\t" \
  "s_memtime vcc\n\t" \
  "s_memrealtime s[82:83]\n\t" \
  "s_nop 4\n\t" \
  "s_memrealtime s[84:85]\n\t" \
  "s_nop 4\n\t" \
  "s_memrealtime s[56:57]\n\t" \
  "s_nop 4\n\t" \
  "s_memrealtime s[86:87]\n\t" \
  "s_nop 4\n\t" \
  "s_memrealtime s[88:89]\n\t" \
  "s_nop 4\n\t" \
  "s_memrealtime s[90:91]\n\t" \
  "s_mov_b32 s92, 0\n\t" \
  "s_waitcnt lgkmcnt(0)\n\t" \
  "s_cmp_eq_u32 s82, s84\n\t" \
  "s_addc_u32 s92, s92, 0\n\t" \
  "s_cmp_eq_u32 s82, s56\n\t" \
  "s_addc_u32 s92, s92, 0\n\t" \
  "s_cmp_eq_u32 s82, s86\n\t" \
  "s_addc_u32 s92, s92, 0\n\t" \
  "s_cmp_eq_u32 s82, s88\n\t" \
  "s_addc_u32 s92, s92, 0\n\t" \
  "s_cmp_eq_u32 s82, s90\n\t" \
  "s_addc_u32 s92, s92, 0\n\t" \
  "s_mul_i32 s92, s92, 24\n\t" \
  "s_add_i32 s92, s92, vcc_lo\n\t" \
  "s_lshl4_add_u32 s93, s82, 64\n\t" \
  "3:\n\t" \
  "v_mov_b64 v[68:69], 0\n\t" \
  "v_mov_b64 v[70:71], 0\n\t" \
  "v_mov_b64 v[72:73], 0\n\t" \
  "v_mov_b64 v[74:75], 0\n\t" \
  "v_mov_b64 v[76:77], 0\n\t" \
  "v_mov_b64 v[78:79], 0\n\t" \
  "v_mov_b64 v[80:81], 0\n\t" \
  "v_mov_b64 v[82:83], 0\n\t" \
  "s_waitcnt lgkmcnt(0)\n\t" \
  "s_waitcnt vmcnt(3)\n\t" \
  "v_smfmac_f32_16x16x64_bf16 v[68:71], v[32:35], a[96:103], v104\n\t" \
  "ds_write_b128 v101, v[32:35] offset:4096\n\t" \
  "v_smfmac_f32_16x16x64_bf16 v[72:75], v[32:35], a[104:111], v104\n\t" \
  "v_smfmac_f32_16x16x64_bf16 v[76:79], v[32:35], v[48:55], v104\n\t" \
  "v_smfmac_f32_16x16x64_bf16 v[80:83], v[32:35], v[56:63], v104\n\t" \
  "s_waitcnt vmcnt(2)\n\t" \
  "v_smfmac_f32_16x16x64_bf16 v[68:71], v[36:39], a[112:119], v104\n\t" \
  "ds_write_b128 v101, v[36:39] offset:5120\n\t" \
  "v_smfmac_f32_16x16x64_bf16 v[72:75], v[36:39], a[120:127], v104\n\t" \
  "v_smfmac_f32_16x16x64_bf16 v[76:79], v[36:39], a[128:135], v104\n\t" \
  "v_cmp_u_f32 vcc, v68, v69\n\t" \
  "v_smfmac_f32_16x16x64_bf16 v[80:83], v[36:39], a[136:143], v104\n\t" \
  "v_cmp_u_f32 s[82:83], v70, v71\n\t" \
  "s_waitcnt vmcnt(1)\n\t" \
  "v_smfmac_f32_16x16x64_bf16 v[68:71], v[40:43], v[16:23], v104\n\t" \
  "ds_write_b128 v101, v[40:43] offset:6144\n\t" \
  "v_smfmac_f32_16x16x64_bf16 v[72:75], v[40:43], v[24:31], v104\n\t" \
  "s_or_b64 vcc, vcc, s[82:83]\n\t" \
  "s_or_b64 vcc, vcc, s[80:81]\n\t" \
  "s_waitcnt lgkmcnt(1)\n\t" \
  "v_smfmac_f32_16x16x64_bf16 v[76:79], v[40:43], a[144:151], v104\n\t" \
  "s_cbranch_scc1 8f\n\t" \
  "5:\n\t" \
  "s_barrier\n\t" \
  "4:\n\t" \
  "s_memtime s[56:57]\n\t" \
  "ds_read_b128 v[48:51], v101 offset:8192\n\t" \
  "v_smfmac_f32_16x16x64_bf16 v[80:83], v[40:43], a[152:159], v104\n\t" \
  "ds_read_b128 v[52:55], v101 offset:9216\n\t" \
  "ds_read_b128 v[56:59], v101 offset:12288\n\t" \
  "s_waitcnt vmcnt(0)\n\t" \
  "v_smfmac_f32_16x16x64_bf16 v[68:71], v[44:47], a[160:167], v104\n\t" \
  "ds_write_b128 v101, v[44:47] offset:7168\n\t" \
  "v_smfmac_f32_16x16x64_bf16 v[72:75], v[44:47], a[168:175], v104\n\t" \
  "v_smfmac_f32_16x16x64_bf16 v[76:79], v[44:47], a[176:183], v104\n\t" \
  "v_cmp_u_f32 vcc, v68, v69\n\t" \
  "v_smfmac_f32_16x16x64_bf16 v[80:83], v[44:47], a[184:191], v104\n\t" \
  "v_cmp_u_f32 s[82:83], v70, v71\n\t" \
  "s_waitcnt lgkmcnt(3)\n\t" \
  "v_smfmac_f32_16x16x64_bf16 v[68:71], v[48:51], a[192:199], v104\n\t" \
  "ds_read_b128 v[60:63], v101 offset:13312\n\t" \
  "ds_read_b128 v[64:67], v101 offset:0\n\t" \
  "v_smfmac_f32_16x16x64_bf16 v[72:75], v[48:51], a[200:207], v104\n\t" \
  "s_or_b64 vcc, vcc, s[82:83]\n\t" \
  "s_waitcnt lgkmcnt(2)\n\t" \
  "v_smfmac_f32_16x16x64_bf16 v[68:71], v[52:55], a[208:215], v104\n\t" \
  "s_cbranch_scc1 9f\n\t" \
  "6:\n\t" \
  "s_barrier\n\t" \
  "v_smfmac_f32_16x16x64_bf16 v[72:75], v[52:55], a[216:223], v104\n\t" \
  "ds_read_b128 v[48:51], v101 offset:1024\n\t" \
  "v_smfmac_f32_16x16x64_bf16 v[68:71], v[56:59], v[152:159], v104\n\t" \
  "ds_read_b128 v[52:55], v101 offset:10240\n\t" \
  "v_smfmac_f32_16x16x64_bf16 v[72:75], v[56:59], v[160:167], v104\n\t" \
  "s_waitcnt lgkmcnt(3)\n\t" \
  "v_smfmac_f32_16x16x64_bf16 v[68:71], v[60:63], v[168:175], v104\n\t" \
  "ds_read_b128 v[56:59], v101 offset:11264\n\t" \
  "global_load_dword v119, v109, %[kc] sc1\n\t" \
  "v_smfmac_f32_16x16x64_bf16 v[72:75], v[60:63], v[176:183], v104\n\t" \
  "s_waitcnt lgkmcnt(3)\n\t" \
  "v_smfmac_f32_16x16x64_bf16 v[68:71], v[64:67], a[0:7], v104\n\t" \
  "ds_read_b128 v[60:63], v101 offset:14336\n\t" \
  "v_smfmac_f32_16x16x64_bf16 v[72:75], v[64:67], a[8:15], v104\n\t" \
  "s_waitcnt lgkmcnt(3)\n\t" \
  "v_smfmac_f32_16x16x64_bf16 v[68:71], v[48:51], a[16:23], v104\n\t" \
  "ds_read_b128 v[64:67], v101 offset:15360\n\t" \
  "ds_read_b128 v[16:19], v102 offset:16384\n\t" \
  "v_smfmac_f32_16x16x64_bf16 v[72:75], v[48:51], a[24:31], v104\n\t" \
  "s_waitcnt lgkmcnt(4)\n\t" \
  "v_smfmac_f32_16x16x64_bf16 v[68:71], v[52:55], v[84:91], v104\n\t" \
  "ds_read_b128 v[48:51], v101 offset:3072\n\t" \
  "ds_read_b128 v[20:23], v102 offset:17408\n\t" \
  "v_smfmac_f32_16x16x64_bf16 v[72:75], v[52:55], v[92:99], v104\n\t" \
  "s_waitcnt lgkmcnt(5)\n\t" \
  "v_smfmac_f32_16x16x64_bf16 v[68:71], v[56:59], v[120:127], v104\n\t" \
  "ds_read_b128 v[52:55], v101 offset:2048\n\t" \
  "ds_read_b128 v[24:27], v102 offset:18432\n\t" \
  "v_smfmac_f32_16x16x64_bf16 v[72:75], v[56:59], v[128:135], v104\n\t" \
  "ds_read_b128 v[28:31], v102 offset:19456\n\t" \
  "buffer_load_dwordx4 v[84:87], v107, %[ru], s94 offen offset:0 sc1\n\t" \
  "s_waitcnt lgkmcnt(7)\n\t" \
  "v_smfmac_f32_16x16x64_bf16 v[68:71], v[60:63], v[0:7], v104\n\t" \
  "v_smfmac_f32_16x16x64_bf16 v[72:75], v[60:63], v[8:15], v104\n\t" \
  "buffer_load_dwordx4 v[88:91], v107, %[ru], s94 offen offset:1024 sc1\n\t" \
  "s_waitcnt lgkmcnt(6)\n\t" \
  "v_smfmac_f32_16x16x64_bf16 v[68:71], v[64:67], v[216:223], v104\n\t" \
  "ds_read_b128 v[0:3], v103 offset:0\n\t" \
  "v_smfmac_f32_16x16x64_bf16 v[72:75], v[64:67], v[224:231], v104\n\t" \
  "ds_read_b128 v[4:7], v103 offset:1024\n\t" \
  "buffer_load_dwordx4 v[92:95], v107, %[ru], s94 offen offset:2048 sc1\n\t" \
  "s_waitcnt lgkmcnt(6)\n\t" \
  "v_smfmac_f32_16x16x64_bf16 v[68:71], v[48:51], a[64:71], v104\n\t" \
  "s_waitcnt lgkmcnt(4)\n\t" \
  "v_smfmac_f32_16x16x64_bf16 v[68:71], v[52:55], v[16:23], v104\n\t" \
  "ds_read_b128 v[8:11], v103 offset:2048\n\t" \
  "buffer_load_dwordx4 v[96:99], v107, %[ru], s94 offen offset:3072 sc1\n\t" \
  "v_smfmac_f32_16x16x64_bf16 v[72:75], v[48:51], a[72:79], v104\n\t" \
  "ds_read_b128 v[12:15], v103 offset:3072\n\t" \
  "s_waitcnt lgkmcnt(4)\n\t" \
  "v_smfmac_f32_16x16x64_bf16 v[72:75], v[52:55], v[24:31], v104\n\t" \
  "v_add_f32 v44, v68, v70\n\t" \
  "v_smfmac_f32_16x16x64_bf16 v[76:79], v[56:59], v[136:143], v104\n\t" \
  "v_add_f32 v45, v69, v71\n\t" \
  "v_cvt_pk_bf16_f32 v44, v44, v45\n\t" \
  "v_smfmac_f32_16x16x64_bf16 v[80:83], v[56:59], v[144:151], v104\n\t" \
  "v_add_f32 v46, v72, v74\n\t" \
  "v_add_f32 v47, v73, v75\n\t" \
  "v_smfmac_f32_16x16x64_bf16 v[76:79], v[60:63], v[200:207], v104\n\t" \
  "v_cvt_pk_bf16_f32 v46, v46, v47\n\t" \
  "ds_write2st64_b32 v105, v44, v46 offset1:1\n\t" \
  "v_smfmac_f32_16x16x64_bf16 v[80:83], v[60:63], v[208:215], v104\n\t" \
  "ds_read_b128 v[56:59], v101 offset:8192\n\t" \
  "v_smfmac_f32_16x16x64_bf16 v[76:79], v[64:67], v[232:239], v104\n\t" \
  "ds_read_b128 v[16:19], v103 offset:32768\n\t" \
  "v_smfmac_f32_16x16x64_bf16 v[80:83], v[64:67], v[240:247], v104\n\t" \
  "ds_read_b128 v[20:23], v103 offset:33792\n\t" \
  "v_smfmac_f32_16x16x64_bf16 v[76:79], v[48:51], a[80:87], v104\n\t" \
  "s_waitcnt lgkmcnt(3)\n\t" \
  "s_barrier\n\t" \
  "v_smfmac_f32_16x16x64_bf16 v[80:83], v[48:51], a[88:95], v104\n\t" \
  "v_smfmac_f32_16x16x64_bf16 v[76:79], v[52:55], a[48:55], v104\n\t" \
  "ds_read_b128 v[60:63], v101 offset:1024\n\t" \
  "ds_read_b128 v[24:27], v103 offset:34816\n\t" \
  "v_smfmac_f32_16x16x64_bf16 v[80:83], v[52:55], a[56:63], v104\n\t" \
  "ds_read_b128 v[64:67], v101 offset:10240\n\t" \
  "s_waitcnt lgkmcnt(5)\n\t" \
  "v_smfmac_f32_16x16x64_bf16 v[76:79], v[56:59], v[0:7], v104\n\t" \
  "ds_read_b128 v[48:51], v101 offset:12288\n\t" \
  "ds_read_b128 v[28:31], v103 offset:35840\n\t" \
  "v_smfmac_f32_16x16x64_bf16 v[80:83], v[56:59], v[8:15], v104\n\t" \
  "buffer_load_dwordx4 v[32:35], v100, %[ru], s47 offen offset:0 sc1\n\t" \
  "s_waitcnt lgkmcnt(4)\n\t" \
  "v_smfmac_f32_16x16x64_bf16 v[76:79], v[60:63], a[32:39], v104\n\t" \
  "ds_read_b128 v[52:55], v101 offset:9216\n\t" \
  "v_smfmac_f32_16x16x64_bf16 v[80:83], v[60:63], a[40:47], v104\n\t" \
  "ds_read_b128 v[0:3], v102 offset:0\n\t" \
  "s_waitcnt lgkmcnt(4)\n\t" \
  "v_smfmac_f32_16x16x64_bf16 v[76:79], v[64:67], a[240:247], v104\n\t" \
  "ds_read_b128 v[56:59], v101 offset:13312\n\t" \
  "buffer_load_dwordx4 v[36:39], v100, %[ru], s47 offen offset:1024 sc1\n\t" \
  "v_smfmac_f32_16x16x64_bf16 v[80:83], v[64:67], a[248:255], v104\n\t" \
  "ds_read_b128 v[60:63], v101 offset:0\n\t" \
  "ds_read_b128 v[4:7], v102 offset:1024\n\t" \
  "s_waitcnt lgkmcnt(6)\n\t" \
  "v_smfmac_f32_16x16x64_bf16 v[76:79], v[48:51], v[16:23], v104\n\t" \
  "ds_read_b128 v[8:11], v102 offset:2048\n\t" \
  "s_waitcnt lgkmcnt(6)\n\t" \
  "v_smfmac_f32_16x16x64_bf16 v[80:83], v[48:51], v[24:31], v104\n\t" \
  "ds_read_b128 v[12:15], v102 offset:3072\n\t" \
  "buffer_load_dwordx4 v[40:43], v100, %[ru], s47 offen offset:2048 sc1\n\t" \
  "s_waitcnt lgkmcnt(6)\n\t" \
  "v_smfmac_f32_16x16x64_bf16 v[76:79], v[52:55], a[224:231], v104\n\t" \
  "v_smfmac_f32_16x16x64_bf16 v[80:83], v[52:55], a[232:239], v104\n\t" \
  "s_waitcnt lgkmcnt(4)\n\t" \
  "v_smfmac_f32_16x16x64_bf16 v[76:79], v[56:59], v[184:191], v104\n\t" \
  "s_waitcnt lgkmcnt(2)\n\t" \
  "v_smfmac_f32_16x16x64_bf16 v[76:79], v[60:63], v[0:7], v104\n\t" \
  "v_smfmac_f32_16x16x64_bf16 v[80:83], v[56:59], v[192:199], v104\n\t" \
  "s_waitcnt lgkmcnt(0)\n\t" \
  "v_smfmac_f32_16x16x64_bf16 v[80:83], v[60:63], v[8:15], v104\n\t" \
  "v_add_f32 v20, v76, v78\n\t" \
  "v_add_f32 v21, v77, v79\n\t" \
  "v_cvt_pk_bf16_f32 v20, v20, v21\n\t" \
  "ds_write_b32 v106, v20\n\t" \
  "s_nop 3\n\t" \
  "v_add_f32 v23, v80, v82\n\t" \
  "v_add_f32 v21, v81, v83\n\t" \
  "v_cvt_pk_bf16_f32 v21, v23, v21\n\t" \
  "ds_write_b32 v106, v21 offset:256\n\t" \
  "s_waitcnt lgkmcnt(0)\n\t" \
  "s_barrier\n\t" \
  "v_bfe_i32 v64, v114, 0, 1\n\t" \
  "v_and_b32 v64, 0xff800000, v64\n\t" \
  "v_bfe_i32 v65, v114, 1, 1\n\t" \
  "v_and_b32 v65, 0xff800000, v65\n\t" \
  "v_bfe_i32 v66, v114, 2, 1\n\t" \
  "v_and_b32 v66, 0xff800000, v66\n\t" \
  "v_bfe_i32 v67, v114, 3, 1\n\t" \
  "v_and_b32 v67, 0xff800000, v67\n\t" \
  "v_bfe_i32 v68, v114, 4, 1\n\t" \
  "v_and_b32 v68, 0xff800000, v68\n\t" \
  "v_bfe_i32 v69, v114, 5, 1\n\t" \
  "v_and_b32 v69, 0xff800000, v69\n\t" \
  "v_bfe_i32 v70, v114, 6, 1\n\t" \
  "v_and_b32 v70, 0xff800000, v70\n\t" \
  "v_bfe_i32 v71, v114, 7, 1\n\t" \
  "v_and_b32 v71, 0xff800000, v71\n\t" \
  "s_lshl_b32 s94, s45, 4\n\t" \
  "s_and_b32 s96, s44, 2\n\t" \
  "s_lshl_b32 s96, s96, 13\n\t" \
  "s_and_b32 s76, s44, 1\n\t" \
  "s_lshl_b32 s76, s76, 12\n\t" \
  "s_add_i32 s96, s96, s76\n\t" \
  "s_mov_b32 s95, 1\n\t" \
  "s_sleep 1\n\t" \
  "ds_read_b128 v[0:3], v101 offset:20480\n\t" \
  "ds_read_b128 v[4:7], v101 offset:21504\n\t" \
  "ds_read_b128 v[8:11], v101 offset:22528\n\t" \
  "ds_read_b128 v[12:15], v101 offset:23552\n\t" \
  "ds_read_b128 v[16:19], v101 offset:24576\n\t" \
  "ds_read_b128 v[20:23], v101 offset:25600\n\t" \
  "ds_read_b128 v[24:27], v101 offset:26624\n\t" \
  "ds_read_b128 v[28:31], v101 offset:27648\n\t" \
  "ds_read_b64_tr_b16 v[48:49], v108 offset:0\n\t" \
  "ds_read_b64_tr_b16 v[50:51], v108 offset:64\n\t" \
  "ds_read_b64_tr_b16 v[52:53], v108 offset:512\n\t" \
  "ds_read_b64_tr_b16 v[54:55], v108 offset:576\n\t" \
  "ds_read_b64_tr_b16 v[56:57], v108 offset:1024\n\t" \
  "ds_read_b64_tr_b16 v[58:59], v108 offset:1088\n\t" \
  "ds_read_b64_tr_b16 v[60:61], v108 offset:1536\n\t" \
  "ds_read_b64_tr_b16 v[62:63], v108 offset:1600\n\t" \
  "s_waitcnt vmcnt(3) lgkmcnt(8)\n\t" \
  "v_mfma_f32_16x16x32_bf16 v[64:67], v[0:3], v[84:87], v[64:67]\n\t" \
  "v_mfma_f32_16x16x32_bf16 v[68:71], v[16:19], v[84:87], v[68:71]\n\t" \
  "v_mfma_f32_16x16x32_bf16 v[64:67], v[4:7], v[88:91], v[64:67]\n\t" \
  "v_mfma_f32_16x16x32_bf16 v[68:71], v[20:23], v[88:91], v[68:71]\n\t" \
  "v_mfma_f32_16x16x32_bf16 v[64:67], v[8:11], v[92:95], v[64:67]\n\t" \
  "v_mfma_f32_16x16x32_bf16 v[68:71], v[24:27], v[92:95], v[68:71]\n\t" \
  "v_mfma_f32_16x16x32_bf16 v[64:67], v[12:15], v[96:99], v[64:67]\n\t" \
  "v_mfma_f32_16x16x32_bf16 v[68:71], v[28:31], v[96:99], v[68:71]\n\t" \
  "s_waitcnt lgkmcnt(0)\n\t" \
  "v_mfma_f32_16x16x32_bf16 v[72:75], v[48:51], v[84:87], 0\n\t" \
  "v_mfma_f32_16x16x32_bf16 v[72:75], v[52:55], v[88:91], v[72:75]\n\t" \
  "v_mfma_f32_16x16x32_bf16 v[72:75], v[56:59], v[92:95], v[72:75]\n\t" \
  "v_mfma_f32_16x16x32_bf16 v[72:75], v[60:63], v[96:99], v[72:75]\n\t" \
  "s_barrier\n\t" \
  "v_max3_f32 v76, v64, v65, v66\n\t" \
  "v_max3_f32 v77, v67, v68, v69\n\t" \
  "v_max3_f32 v76, v76, v70, v71\n\t" \
  "s_nop 4\n\t" \
  "v_max3_f32 v77, v77, v72, v73\n\t" \
  "v_max3_f32 v76, v76, v74, v75\n\t" \
  "v_max_f32 v76, v76, v77\n\t" \
  "v_mov_b32 v77, v76\n\t" \
  "s_nop 1\n\t" \
  "v_permlane16_swap_b32 v76, v77\n\t" \
  "v_max_f32 v76, v76, v77\n\t" \
  "v_mov_b32 v77, v76\n\t" \
  "s_nop 1\n\t" \
  "v_permlane32_swap_b32 v76, v77\n\t" \
  "v_max_f32 v76, v76, v77\n\t" \
  "v_mul_f32 v76, %[cs], v76\n\t" \
  "v_fma_f32 v64, v64, %[cs], -v76\n\t" \
  "v_fma_f32 v65, v65, %[cs], -v76\n\t" \
  "v_fma_f32 v66, v66, %[cs], -v76\n\t" \
  "v_fma_f32 v67, v67, %[cs], -v76\n\t" \
  "v_fma_f32 v68, v68, %[cs], -v76\n\t" \
  "v_fma_f32 v69, v69, %[cs], -v76\n\t" \
  "v_fma_f32 v70, v70, %[cs], -v76\n\t" \
  "v_fma_f32 v71, v71, %[cs], -v76\n\t" \
  "v_fma_f32 v72, v72, %[cs], -v76\n\t" \
  "v_fma_f32 v73, v73, %[cs], -v76\n\t" \
  "v_fma_f32 v74, v74, %[cs], -v76\n\t" \
  "v_fma_f32 v75, v75, %[cs], -v76\n\t" \
  "v_exp_f32 v64, v64\n\t" \
  "v_exp_f32 v65, v65\n\t" \
  "v_exp_f32 v66, v66\n\t" \
  "v_exp_f32 v67, v67\n\t" \
  "v_exp_f32 v68, v68\n\t" \
  "v_exp_f32 v69, v69\n\t" \
  "v_exp_f32 v70, v70\n\t" \
  "v_exp_f32 v71, v71\n\t" \
  "v_exp_f32 v72, v72\n\t" \
  "v_exp_f32 v73, v73\n\t" \
  "v_exp_f32 v74, v74\n\t" \
  "v_exp_f32 v75, v75\n\t" \
  "v_add_f32 v76, v72, v73\n\t" \
  "v_add_f32 v77, v74, v75\n\t" \
  "v_add_f32 v78, v64, v65\n\t" \
  "v_add_f32 v79, v66, v67\n\t" \
  "v_add_f32 v76, v76, v77\n\t" \
  "v_add_f32 v78, v78, v68\n\t" \
  "v_add_f32 v79, v79, v69\n\t" \
  "v_add_f32 v78, v78, v70\n\t" \
  "v_add_f32 v79, v79, v71\n\t" \
  "v_fma_f32 v76, v76, 0.5, v78\n\t" \
  "v_add_f32 v76, v76, v79\n\t" \
  "v_mov_b32 v77, v76\n\t" \
  "s_nop 1\n\t" \
  "v_permlane16_swap_b32 v76, v77\n\t" \
  "v_add_f32 v76, v76, v77\n\t" \
  "v_mov_b32 v77, v76\n\t" \
  "s_nop 1\n\t" \
  "v_permlane32_swap_b32 v76, v77\n\t" \
  "v_add_f32 v76, v76, v77\n\t" \
  "v_rcp_f32 v76, v76\n\t" \
  "s_nop 0\n\t" \
  "v_mul_f32 v64, v64, v76\n\t" \
  "v_mul_f32 v65, v65, v76\n\t" \
  "v_mul_f32 v66, v66, v76\n\t" \
  "v_mul_f32 v67, v67, v76\n\t" \
  "v_mul_f32 v68, v68, v76\n\t" \
  "v_mul_f32 v69, v69, v76\n\t" \
  "v_mul_f32 v70, v70, v76\n\t" \
  "v_mul_f32 v71, v71, v76\n\t" \
  "v_mul_f32 v72, v72, v76\n\t" \
  "v_mul_f32 v73, v73, v76\n\t" \
  "v_mul_f32 v74, v74, v76\n\t" \
  "v_mul_f32 v75, v75, v76\n\t" \
  "v_cvt_pk_bf16_f32 v0, v64, v68\n\t" \
  "v_cvt_pk_bf16_f32 v1, v65, v69\n\t" \
  "v_cvt_pk_bf16_f32 v2, v66, v70\n\t" \
  "v_cvt_pk_bf16_f32 v3, v67, v71\n\t" \
  "v_cvt_pk_bf16_f32 v4, v72, v73\n\t" \
  "v_cvt_pk_bf16_f32 v5, v74, v75\n\t" \
  "v_permlane32_swap_b32 v0, v2\n\t" \
  "v_permlane32_swap_b32 v1, v3\n\t" \
  "v_mov_b64 v[6:7], v[4:5]\n\t" \
  "s_mov_b32 exec_hi, 0\n\t" \
  "buffer_load_dword v118, v112, %[rh], s96 offen sc1\n\t" \
  "s_mov_b64 exec, -1\n\t" \
  "buffer_store_dwordx4 v[0:3], v110, %[rh], s94 offen sc1\n\t" \
  "v_permlane16_swap_b32 v4, v6\n\t" \
  "v_permlane16_swap_b32 v5, v7\n\t" \
  "buffer_store_dwordx4 v[4:7], v111, %[rh], s94 offen sc1\n\t" \
  "s_waitcnt vmcnt(3)\n\t" \
  "ds_write_b128 v101, v[32:35] offset:20480\n\t" \
  "ds_write_b128 v101, v[36:39] offset:21504\n\t" \
  "ds_write_b128 v101, v[40:43] offset:22528\n\t" \
  "s_add_i32 s77, s44, 2\n\t" \
  "s_min_u32 s77, s77, 63\n\t" \
  "v_cmp_gt_i32 vcc, s77, v119\n\t" \
  "s_cmp_lg_u64 vcc, 0\n\t" \
  "s_cselect_b32 s42, 32, 0\n\t" \
  "s_cmp_eq_u32 s44, 0\n\t" \
  "s_cselect_b32 s42, 32, s42\n\t" \
  "s_add_i32 s41, s56, 1950\n\t" \
  "s_sub_i32 s76, s92, s51\n\t" \
  "s_sub_i32 s77, s93, s53\n\t" \
  "s_cmp_eq_u32 s76, 0\n\t" \
  "s_cbranch_scc1 21f\n\t" \
  "v_cvt_f32_u32 v117, s76\n\t" \
  "v_cvt_f32_u32 v116, s77\n\t" \
  "v_rcp_f32 v117, v117\n\t" \
  "s_mov_b32 s51, s92\n\t" \
  "v_mul_f32 v117, v117, v116\n\t" \
  "v_max_f32 v117, 0x3f028f5c, v117\n\t" \
  "v_min_f32 v117, 0x3ffeb852, v117\n\t" \
  "v_rcp_f32 v116, v117\n\t" \
  "v_mul_f32 v117, 0x4f000000, v117\n\t" \
  "v_mul_f32 v116, 0x47800000, v116\n\t" \
  "v_cvt_u32_f32 v117, v117\n\t" \
  "v_cvt_u32_f32 v116, v116\n\t" \
  "s_mov_b32 s53, s93\n\t" \
  "v_readfirstlane_b32 s46, v117\n\t" \
  "v_readfirstlane_b32 s52, v116\n\t" \
  "21:\n\t" \
  "s_add_i32 s44, s44, 1\n\t" \
  "s_and_b32 s58, s58, 4\n\t" \
  "s_and_b32 s76, s44, 64\n\t" \
  "s_or_b32 s42, s42, s76\n\t" \
  "s_cbranch_scc0 20b\n\t" \
  "s_branch 99f\n\t" \
  "14:\n\t" \
  "s_sleep 3\n\t" \
  "s_add_u32 s59, s59, 0x10000\n\t" \
  "s_cmp_lt_u32 s59, 0x100000\n\t" \
  "s_cbranch_scc1 13b\n\t" \
  "s_mov_b64 exec, -1\n\t" \
  "s_mov_b32 s42, 16\n\t" \
  "s_branch 99f\n\t" \
  "8:\n\t" \
  "s_bitcmp1_b32 s58, 1\n\t" \
  "s_cbranch_scc1 4b\n\t" \
  "s_bitcmp1_b32 s58, 2\n\t" \
  "s_cbranch_scc1 5b\n\t" \
  "v_max3_u32 v116, v32, v33, v34\n\t" \
  "v_max3_u32 v117, v35, v36, v37\n\t" \
  "v_max3_u32 v116, v116, v38, v39\n\t" \
  "v_max_u32 v116, v116, v117\n\t" \
  "v_cmp_eq_u32 vcc, -1, v116\n\t" \
  "s_cbranch_vccz 5b\n\t" \
  "s_branch 0f\n\t" \
  "9:\n\t" \
  "s_bitcmp1_b32 s58, 2\n\t" \
  "s_cbranch_scc1 6b\n\t" \
  "v_max3_u32 v116, v40, v41, v42\n\t" \
  "v_max3_u32 v117, v43, v44, v45\n\t" \
  "v_max3_u32 v116, v116, v46, v47\n\t" \
  "v_max_u32 v116, v116, v117\n\t" \
  "v_cmp_eq_u32 vcc, -1, v116\n\t" \
  "s_cbranch_vccz 6b\n\t" \
  "s_or_b32 s58, s58, 2\n\t" \
  "0:\n\t" \
  "s_or_b32 s58, s58, 1\n\t" \
  "11:\n\t" \
  "buffer_load_dwordx4 v[32:35], v100, %[rh], s40 offen sc1\n\t" \
  "buffer_load_dwordx4 v[36:39], v100, %[rh], s76 offen sc1\n\t" \
  "buffer_load_dwordx4 v[40:43], v100, %[rh], s77 offen sc1\n\t" \
  "buffer_load_dwordx4 v[44:47], v100, %[rh], s78 offen sc1\n\t" \
  "s_waitcnt lgkmcnt(0)\n\t" \
  "s_branch 10b\n\t" \
  "17:\n\t" \
  "s_add_u32 s59, s59, 1\n\t" \
  "s_bitcmp1_b32 s58, 2\n\t" \
  "s_cbranch_scc1 3b\n\t" \
  "s_waitcnt vmcnt(0)\n\t" \
  "v_max_u32 v116, v32, v33\n\t" \
  "v_max3_u32 v116, v116, v34, v35\n\t" \
  "v_max3_u32 v116, v116, v36, v37\n\t" \
  "v_max3_u32 v116, v116, v38, v39\n\t" \
  "v_max3_u32 v116, v116, v40, v41\n\t" \
  "v_max3_u32 v116, v116, v42, v43\n\t" \
  "v_max3_u32 v116, v116, v44, v45\n\t" \
  "v_max3_u32 v116, v116, v46, v47\n\t" \
  "v_cmp_eq_u32 vcc, -1, v116\n\t" \
  "s_cbranch_vccz 3b\n\t" \
  "s_bitcmp1_b32 s59, 6\n\t" \
  "s_cbranch_scc1 12f\n\t" \
  "buffer_load_dwordx4 v[32:35], v100, %[rh], s40 offen sc1\n\t" \
  "buffer_load_dwordx4 v[36:39], v100, %[rh], s76 offen sc1\n\t" \
  "buffer_load_dwordx4 v[40:43], v100, %[rh], s77 offen sc1\n\t" \
  "buffer_load_dwordx4 v[44:47], v100, %[rh], s78 offen sc1\n\t" \
  "s_branch 17b\n\t" \
  "12:\n\t" \
  "s_mov_b32 s42, s58\n\t" \
  "s_branch 99f\n\t" \
  "92:\n\t" \
  "s_mov_b32 s58, s42\n\t" \
  "s_mov_b32 s95, 0\n\t" \
  "20:\n\t" \
  "s_and_b32 s76, s44, 7\n\t" \
  "s_lshl_b32 s76, s76, 15\n\t" \
  "s_add_i32 s40, s54, s76\n\t" \
  "s_and_b32 s76, s44, 3\n\t" \
  "s_lshl_b32 s45, s76, 14\n\t" \
  "s_add_i32 s47, s44, 1\n\t" \
  "s_min_u32 s47, s47, 63\n\t" \
  "s_lshr_b32 s76, s47, 1\n\t" \
  "s_lshl_b32 s47, s47, 20\n\t" \
  "s_add_i32 s47, s47, s55\n\t" \
  "s_bitcmp1_b64 %[sel], s76\n\t" \
  "s_cselect_b32 s76, 0x2000, 0\n\t" \
  "s_add_i32 s47, s47, s76\n\t" \
  "s_lshr_b32 s76, s44, 1\n\t" \
  "s_lshl_b32 s94, s44, 20\n\t" \
  "s_bitcmp1_b64 %[sel], s76\n\t" \
  "s_cselect_b32 s76, 0x2000, 0\n\t" \
  "s_add_i32 s94, s94, s76\n\t" \
  "s_add_i32 s49, s44, -1\n\t" \
  "s_and_b32 s48, s49, 2\n\t" \
  "s_lshl_b32 s48, s48, 13\n\t" \
  "s_and_b32 s76, s49, 1\n\t" \
  "s_lshl_b32 s76, s76, 12\n\t" \
  "s_add_i32 s48, s48, s76\n\t" \
  "s_lshl_b32 s50, s53, 8\n\t" \
  "s_mov_b32 s59, 0\n\t" \
  "s_mov_b32 s92, s51\n\t" \
  "s_mov_b32 s93, s53\n\t" \
  "s_xor_b32 s76, s40, 0x400\n\t" \
  "s_xor_b32 s77, s40, 0x800\n\t" \
  "s_xor_b32 s78, s40, 0xc00\n\t" \
  "s_cmp_lg_u32 s58, 0\n\t" \
  "s_cbranch_scc1 11f\n\t" \
  "10:\n\t" \
  "ds_read_b128 v[48:51], v103 offset:0\n\t" \
  "ds_read_b128 v[52:55], v103 offset:1024\n\t" \
  "ds_read_b128 v[56:59], v103 offset:2048\n\t" \
  "ds_read_b128 v[60:63], v103 offset:3072\n\t" \
  "ds_read_b128 v[16:19], v103 offset:16384\n\t" \
  "ds_read_b128 v[20:23], v103 offset:17408\n\t" \
  "ds_read_b128 v[24:27], v103 offset:18432\n\t" \
  "ds_read_b128 v[28:31], v103 offset:19456\n\t" \
  "ds_read_b128 v[84:87], v103 offset:49152\n\t" \
  "ds_read_b128 v[88:91], v103 offset:50176\n\t" \
  "ds_read_b128 v[92:95], v103 offset:51200\n\t" \
  "ds_read_b128 v[96:99], v103 offset:52224\n\t" \
  "s_waitcnt lgkmcnt(4)\n\t" \
  "ds_read_b128 v[0:3], v102 offset:16384\n\t" \
  "ds_read_b128 v[4:7], v102 offset:17408\n\t" \
  "ds_read_b128 v[8:11], v102 offset:18432\n\t" \
  "ds_read_b128 v[12:15], v102 offset:19456\n\t" \
  "s_bitcmp1_b32 s58, 1\n\t" \
  "s_cselect_b64 s[80:81], -1, 0\n\t" \
  "s_cmp_lg_u32 s58, 0\n\t" \
  "s_cbranch_scc1 17f\n\t" \
  "s_mov_b32 exec_hi, 0\n\t" \
  "s_cmp_eq_u32 s95, 0\n\t" \
  "s_cbranch_scc1 16f\n\t" \
  "s_waitcnt vmcnt(2)\n\t" \
  "v_mov_b32 v116, v118\n\t" \
  "s_branch 15f\n\t" \
  "16:\n\t" \
  "s_memtime vcc\n\t" \
  "s_waitcnt lgkmcnt(0)\n\t" \
  "s_sub_i32 s79, s41, vcc_lo\n\t" \
  "s_cmp_gt_i32 s79, 64\n\t" \
  "s_cbranch_scc1 16b\n\t" \
  "13:\n\t" \
  "buffer_load_dword v116, v112, %[rh], s48 offen sc1\n\t" \
  "s_waitcnt vmcnt(0)\n\t" \
  "15:\n\t" \
  "v_mov_b32 v117, 20000\n\t" \
  "v_cmp_ne_u32_sdwa vcc, v116, s49 src0_sel:BYTE_0 src1_sel:DWORD\n\t" \
  "v_subrev_u32 v116, s50, v116\n\t" \
  "v_ashrrev_i32 v116, 8, v116\n\t" \
  "s_nop 1\n\t" \
  "v_max_i32_dpp v116, v116, v116 quad_perm:[1,0,3,2] row_mask:0xf bank_mask:0xf\n\t" \
  "s_cbranch_vccnz 14f\n\t" \
  "s_nop 0\n\t" \
  "v_max_i32_dpp v116, v116, v116 quad_perm:[2,3,0,1] row_mask:0xf bank_mask:0xf\n\t" \
  "s_nop 1\n\t" \
  "v_max_i32_dpp v116, v116, v116 row_half_mirror row_mask:0xf bank_mask:0xf\n\t" \
  "s_nop 1\n\t" \
  "v_max_i32_dpp v116, v116, v116 row_mirror row_mask:0xf bank_mask:0xf\n\t" \
  "v_med3_i32 v116, v116, 0, v117\n\t" \
  "v_mul_u32_u24 v116, s52, v116\n\t" \
  "v_lshrrev_b32 v116, 16, v116\n\t" \
  "s_mov_b64 exec, -1\n\t" \
  "v_readfirstlane_b32 s84, v116\n\t" \
  "v_readlane_b32 s85, v116, 16\n\t" \
  "s_max_u32 s84, s84, s85\n\t" \
  "s_add_i32 s84, s84, s51\n\t" \
  "1:\n\t" \
  "s_memtime vcc\n\t" \
  "s_waitcnt lgkmcnt(0)\n\t" \
  "s_sub_i32 s79, s84, vcc_lo\n\t" \
  "s_cmp_gt_i32 s79, 128\n\t" \
  "s_cbranch_scc1 1b\n\t" \
  ".p2align 4\n\t" \
  "2:\n\t" \
  "s_sub_i32 s79, s79, 33\n\t" \
  "s_cmp_gt_i32 s79, 56\n\t" \
  "s_cbranch_scc1 2b\n\t" \
  "buffer_load_dwordx4 v[32:35], v100, %[rh], s40 offen sc1\n\t" \
  "s_nop 12\n\t" \
  "buffer_load_dwordx4 v[36:39], v100, %[rh], s76 offen sc1\n\t" \
  "s_nop 12\n\t" \
  "buffer_load_dwordx4 v[40:43], v100, %[rh], s77 offen sc1\n\t" \
  "s_nop 12\n\t" \
  "buffer_load_dwordx4 v[44:47], v100, %[rh], s78 offen sc1\n\t" \
  "s_memtime vcc\n\t" \
  "s_memrealtime s[82:83]\n\t" \
  "s_nop 4\n\t" \
  "s_memrealtime s[84:85]\n\t" \
  "s_nop 4\n\t" \
  "s_memrealtime s[56:57]\n\t" \
  "s_nop 4\n\t" \
  "s_memrealtime s[86:87]\n\t" \
  "s_nop 4\n\t" \
  "s_memrealtime s[88:89]\n\t" \
  "s_nop 4\n\t" \
  "s_memrealtime s[90:91]\n\t" \
  "s_mov_b32 s92, 0\n\t" \
  "s_waitcnt lgkmcnt(0)\n\t" \
  "s_cmp_eq_u32 s82, s84\n\t" \
  "s_addc_u32 s92, s92, 0\n\t" \
  "s_cmp_eq_u32 s82, s56\n\t" \
  "s_addc_u32 s92, s92, 0\n\t" \
  "s_cmp_eq_u32 s82, s86\n\t" \
  "s_addc_u32 s92, s92, 0\n\t" \
  "s_cmp_eq_u32 s82, s88\n\t" \
  "s_addc_u32 s92, s92, 0\n\t" \
  "s_cmp_eq_u32 s82, s90\n\t" \
  "s_addc_u32 s92, s92, 0\n\t" \
  "s_mul_i32 s92, s92, 24\n\t" \
  "s_add_i32 s92, s92, vcc_lo\n\t" \
  "s_lshl4_add_u32 s93, s82, 64\n\t" \
  "3:\n\t" \
  "v_mov_b64 v[68:69], 0\n\t" \
  "v_mov_b64 v[70:71], 0\n\t" \
  "v_mov_b64 v[72:73], 0\n\t" \
  "v_mov_b64 v[74:75], 0\n\t" \
  "v_mov_b64 v[76:77], 0\n\t" \
  "v_mov_b64 v[78:79], 0\n\t" \
  "v_mov_b64 v[80:81], 0\n\t" \
  "v_mov_b64 v[82:83], 0\n\t" \
  "s_waitcnt lgkmcnt(0)\n\t" \
  "s_waitcnt vmcnt(3)\n\t" \
  "v_smfmac_f32_16x16x64_bf16 v[68:71], v[32:35], a[192:199], v104\n\t" \
  "ds_write_b128 v101, v[32:35] offset:8192\n\t" \
  "v_smfmac_f32_16x16x64_bf16 v[72:75], v[32:35], a[200:207], v104\n\t" \
  "v_smfmac_f32_16x16x64_bf16 v[76:79], v[32:35], v[48:55], v104\n\t" \
  "v_smfmac_f32_16x16x64_bf16 v[80:83], v[32:35], v[56:63], v104\n\t" \
  "s_waitcnt vmcnt(2)\n\t" \
  "v_smfmac_f32_16x16x64_bf16 v[68:71], v[36:39], a[208:215], v104\n\t" \
  "ds_write_b128 v101, v[36:39] offset:9216\n\t" \
  "v_smfmac_f32_16x16x64_bf16 v[72:75], v[36:39], a[216:223], v104\n\t" \
  "v_smfmac_f32_16x16x64_bf16 v[76:79], v[36:39], a[224:231], v104\n\t" \
  "v_cmp_u_f32 vcc, v68, v69\n\t" \
  "v_smfmac_f32_16x16x64_bf16 v[80:83], v[36:39], a[232:239], v104\n\t" \
  "v_cmp_u_f32 s[82:83], v70, v71\n\t" \
  "s_waitcnt vmcnt(1)\n\t" \
  "v_smfmac_f32_16x16x64_bf16 v[68:71], v[40:43], v[16:23], v104\n\t" \
  "ds_write_b128 v101, v[40:43] offset:10240\n\t" \
  "v_smfmac_f32_16x16x64_bf16 v[72:75], v[40:43], v[24:31], v104\n\t" \
  "s_or_b64 vcc, vcc, s[82:83]\n\t" \
  "s_or_b64 vcc, vcc, s[80:81]\n\t" \
  "s_waitcnt lgkmcnt(1)\n\t" \
  "v_smfmac_f32_16x16x64_bf16 v[76:79], v[40:43], a[240:247], v104\n\t" \
  "s_cbranch_scc1 8f\n\t" \
  "5:\n\t" \
  "s_barrier\n\t" \
  "4:\n\t" \
  "s_memtime s[56:57]\n\t" \
  "ds_read_b128 v[48:51], v101 offset:12288\n\t" \
  "v_smfmac_f32_16x16x64_bf16 v[80:83], v[40:43], a[248:255], v104\n\t" \
  "ds_read_b128 v[52:55], v101 offset:13312\n\t" \
  "ds_read_b128 v[56:59], v101 offset:0\n\t" \
  "s_waitcnt vmcnt(0)\n\t" \
  "v_smfmac_f32_16x16x64_bf16 v[68:71], v[44:47], v[120:127], v104\n\t" \
  "v_smfmac_f32_16x16x64_bf16 v[72:75], v[44:47], v[128:135], v104\n\t" \
  "ds_write_b128 v101, v[44:47] offset:11264\n\t" \
  "v_smfmac_f32_16x16x64_bf16 v[76:79], v[44:47], v[136:143], v104\n\t" \
  "v_cmp_u_f32 vcc, v68, v69\n\t" \
  "v_smfmac_f32_16x16x64_bf16 v[80:83], v[44:47], v[144:151], v104\n\t" \
  "v_cmp_u_f32 s[82:83], v70, v71\n\t" \
  "s_waitcnt lgkmcnt(3)\n\t" \
  "v_smfmac_f32_16x16x64_bf16 v[68:71], v[48:51], v[152:159], v104\n\t" \
  "ds_read_b128 v[60:63], v101 offset:1024\n\t" \
  "ds_read_b128 v[64:67], v101 offset:4096\n\t" \
  "v_smfmac_f32_16x16x64_bf16 v[72:75], v[48:51], v[160:167], v104\n\t" \
  "s_or_b64 vcc, vcc, s[82:83]\n\t" \
  "s_waitcnt lgkmcnt(2)\n\t" \
  "v_smfmac_f32_16x16x64_bf16 v[68:71], v[52:55], v[168:175], v104\n\t" \
  "s_cbranch_scc1 9f\n\t" \
  "6:\n\t" \
  "s_barrier\n\t" \
  "v_smfmac_f32_16x16x64_bf16 v[72:75], v[52:55], v[176:183], v104\n\t" \
  "ds_read_b128 v[48:51], v101 offset:5120\n\t" \
  "v_smfmac_f32_16x16x64_bf16 v[68:71], v[56:59], a[0:7], v104\n\t" \
  "ds_read_b128 v[52:55], v101 offset:14336\n\t" \
  "v_smfmac_f32_16x16x64_bf16 v[72:75], v[56:59], a[8:15], v104\n\t" \
  "s_waitcnt lgkmcnt(3)\n\t" \
  "v_smfmac_f32_16x16x64_bf16 v[68:71], v[60:63], a[16:23], v104\n\t" \
  "ds_read_b128 v[56:59], v101 offset:15360\n\t" \
  "v_smfmac_f32_16x16x64_bf16 v[72:75], v[60:63], a[24:31], v104\n\t" \
  "s_waitcnt lgkmcnt(3)\n\t" \
  "v_smfmac_f32_16x16x64_bf16 v[68:71], v[64:67], a[96:103], v104\n\t" \
  "ds_read_b128 v[60:63], v101 offset:2048\n\t" \
  "global_load_dword v119, v109, %[kc] sc1\n\t" \
  "v_smfmac_f32_16x16x64_bf16 v[72:75], v[64:67], a[104:111], v104\n\t" \
  "s_waitcnt lgkmcnt(3)\n\t" \
  "v_smfmac_f32_16x16x64_bf16 v[68:71], v[48:51], a[112:119], v104\n\t" \
  "ds_read_b128 v[64:67], v101 offset:3072\n\t" \
  "ds_read_b128 v[16:19], v102 offset:49152\n\t" \
  "v_smfmac_f32_16x16x64_bf16 v[72:75], v[48:51], a[120:127], v104\n\t" \
  "s_waitcnt lgkmcnt(4)\n\t" \
  "v_smfmac_f32_16x16x64_bf16 v[68:71], v[52:55], v[84:91], v104\n\t" \
  "ds_read_b128 v[48:51], v101 offset:7168\n\t" \
  "ds_read_b128 v[20:23], v102 offset:50176\n\t" \
  "v_smfmac_f32_16x16x64_bf16 v[72:75], v[52:55], v[92:99], v104\n\t" \
  "s_waitcnt lgkmcnt(5)\n\t" \
  "v_smfmac_f32_16x16x64_bf16 v[68:71], v[56:59], v[216:223], v104\n\t" \
  "ds_read_b128 v[52:55], v101 offset:6144\n\t" \
  "ds_read_b128 v[24:27], v102 offset:51200\n\t" \
  "v_smfmac_f32_16x16x64_bf16 v[72:75], v[56:59], v[224:231], v104\n\t" \
  "ds_read_b128 v[28:31], v102 offset:52224\n\t" \
  "s_waitcnt lgkmcnt(7)\n\t" \
  "v_smfmac_f32_16x16x64_bf16 v[68:71], v[60:63], v[0:7], v104\n\t" \
  "buffer_load_dwordx4 v[84:87], v107, %[ru], s94 offen offset:0 sc1\n\t" \
  "v_smfmac_f32_16x16x64_bf16 v[72:75], v[60:63], v[8:15], v104\n\t" \
  "s_waitcnt lgkmcnt(6)\n\t" \
  "v_smfmac_f32_16x16x64_bf16 v[68:71], v[64:67], a[64:71], v104\n\t" \
  "ds_read_b128 v[0:3], v103 offset:32768\n\t" \
  "buffer_load_dwordx4 v[88:91], v107, %[ru], s94 offen offset:1024 sc1\n\t" \
  "v_smfmac_f32_16x16x64_bf16 v[72:75], v[64:67], a[72:79], v104\n\t" \
  "ds_read_b128 v[4:7], v103 offset:33792\n\t" \
  "s_waitcnt lgkmcnt(6)\n\t" \
  "v_smfmac_f32_16x16x64_bf16 v[68:71], v[48:51], a[160:167], v104\n\t" \
  "buffer_load_dwordx4 v[92:95], v107, %[ru], s94 offen offset:2048 sc1\n\t" \
  "s_waitcnt lgkmcnt(4)\n\t" \
  "v_smfmac_f32_16x16x64_bf16 v[68:71], v[52:55], v[16:23], v104\n\t" \
  "ds_read_b128 v[8:11], v103 offset:34816\n\t" \
  "v_smfmac_f32_16x16x64_bf16 v[72:75], v[48:51], a[168:175], v104\n\t" \
  "ds_read_b128 v[12:15], v103 offset:35840\n\t" \
  "buffer_load_dwordx4 v[96:99], v107, %[ru], s94 offen offset:3072 sc1\n\t" \
  "s_waitcnt lgkmcnt(4)\n\t" \
  "v_smfmac_f32_16x16x64_bf16 v[72:75], v[52:55], v[24:31], v104\n\t" \
  "v_add_f32 v44, v68, v70\n\t" \
  "v_smfmac_f32_16x16x64_bf16 v[76:79], v[56:59], v[232:239], v104\n\t" \
  "v_add_f32 v45, v69, v71\n\t" \
  "v_cvt_pk_bf16_f32 v44, v44, v45\n\t" \
  "v_smfmac_f32_16x16x64_bf16 v[80:83], v[56:59], v[240:247], v104\n\t" \
  "v_add_f32 v46, v72, v74\n\t" \
  "v_add_f32 v47, v73, v75\n\t" \
  "v_smfmac_f32_16x16x64_bf16 v[76:79], v[60:63], a[48:55], v104\n\t" \
  "v_cvt_pk_bf16_f32 v46, v46, v47\n\t" \
  "ds_write2st64_b32 v105, v44, v46 offset1:1\n\t" \
  "v_smfmac_f32_16x16x64_bf16 v[80:83], v[60:63], a[56:63], v104\n\t" \
  "ds_read_b128 v[56:59], v101 offset:12288\n\t" \
  "v_smfmac_f32_16x16x64_bf16 v[76:79], v[64:67], a[80:87], v104\n\t" \
  "ds_read_b128 v[16:19], v102 offset:0\n\t" \
  "v_smfmac_f32_16x16x64_bf16 v[80:83], v[64:67], a[88:95], v104\n\t" \
  "ds_read_b128 v[20:23], v102 offset:1024\n\t" \
  "v_smfmac_f32_16x16x64_bf16 v[76:79], v[48:51], a[176:183], v104\n\t" \
  "s_waitcnt lgkmcnt(3)\n\t" \
  "s_barrier\n\t" \
  "v_smfmac_f32_16x16x64_bf16 v[80:83], v[48:51], a[184:191], v104\n\t" \
  "v_smfmac_f32_16x16x64_bf16 v[76:79], v[52:55], a[144:151], v104\n\t" \
  "ds_read_b128 v[60:63], v101 offset:5120\n\t" \
  "ds_read_b128 v[24:27], v102 offset:2048\n\t" \
  "v_smfmac_f32_16x16x64_bf16 v[80:83], v[52:55], a[152:159], v104\n\t" \
  "ds_read_b128 v[64:67], v101 offset:14336\n\t" \
  "s_waitcnt lgkmcnt(5)\n\t" \
  "v_smfmac_f32_16x16x64_bf16 v[76:79], v[56:59], v[0:7], v104\n\t" \
  "ds_read_b128 v[48:51], v101 offset:0\n\t" \
  "ds_read_b128 v[28:31], v102 offset:3072\n\t" \
  "v_smfmac_f32_16x16x64_bf16 v[80:83], v[56:59], v[8:15], v104\n\t" \
  "s_waitcnt lgkmcnt(4)\n\t" \
  "v_smfmac_f32_16x16x64_bf16 v[76:79], v[60:63], a[128:135], v104\n\t" \
  "ds_read_b128 v[52:55], v101 offset:13312\n\t" \
  "buffer_load_dwordx4 v[32:35], v100, %[ru], s47 offen offset:0 sc1\n\t" \
  "v_smfmac_f32_16x16x64_bf16 v[80:83], v[60:63], a[136:143], v104\n\t" \
  "ds_read_b128 v[0:3], v102 offset:32768\n\t" \
  "s_waitcnt lgkmcnt(4)\n\t" \
  "v_smfmac_f32_16x16x64_bf16 v[76:79], v[64:67], v[200:207], v104\n\t" \
  "ds_read_b128 v[56:59], v101 offset:1024\n\t" \
  "v_smfmac_f32_16x16x64_bf16 v[80:83], v[64:67], v[208:215], v104\n\t" \
  "ds_read_b128 v[60:63], v101 offset:4096\n\t" \
  "ds_read_b128 v[4:7], v102 offset:33792\n\t" \
  "buffer_load_dwordx4 v[36:39], v100, %[ru], s47 offen offset:1024 sc1\n\t" \
  "s_waitcnt lgkmcnt(6)\n\t" \
  "v_smfmac_f32_16x16x64_bf16 v[76:79], v[48:51], v[16:23], v104\n\t" \
  "ds_read_b128 v[8:11], v102 offset:34816\n\t" \
  "s_waitcnt lgkmcnt(6)\n\t" \
  "v_smfmac_f32_16x16x64_bf16 v[80:83], v[48:51], v[24:31], v104\n\t" \
  "ds_read_b128 v[12:15], v102 offset:35840\n\t" \
  "s_waitcnt lgkmcnt(6)\n\t" \
  "v_smfmac_f32_16x16x64_bf16 v[76:79], v[52:55], v[184:191], v104\n\t" \
  "buffer_load_dwordx4 v[40:43], v100, %[ru], s47 offen offset:2048 sc1\n\t" \
  "v_smfmac_f32_16x16x64_bf16 v[80:83], v[52:55], v[192:199], v104\n\t" \
  "s_waitcnt lgkmcnt(4)\n\t" \
  "v_smfmac_f32_16x16x64_bf16 v[76:79], v[56:59], a[32:39], v104\n\t" \
  "s_waitcnt lgkmcnt(2)\n\t" \
  "v_smfmac_f32_16x16x64_bf16 v[76:79], v[60:63], v[0:7], v104\n\t" \
  "v_smfmac_f32_16x16x64_bf16 v[80:83], v[56:59], a[40:47], v104\n\t" \
  "s_waitcnt lgkmcnt(0)\n\t" \
  "v_smfmac_f32_16x16x64_bf16 v[80:83], v[60:63], v[8:15], v104\n\t" \
  "v_add_f32 v20, v76, v78\n\t" \
  "v_add_f32 v21, v77, v79\n\t" \
  "v_cvt_pk_bf16_f32 v20, v20, v21\n\t" \
  "ds_write_b32 v106, v20\n\t" \
  "s_nop 3\n\t" \
  "v_add_f32 v23, v80, v82\n\t" \
  "v_add_f32 v21, v81, v83\n\t" \
  "v_cvt_pk_bf16_f32 v21, v23, v21\n\t" \
  "ds_write_b32 v106, v21 offset:256\n\t" \
  "s_waitcnt lgkmcnt(0)\n\t" \
  "s_barrier\n\t" \
  "v_bfe_i32 v64, v114, 0, 1\n\t" \
  "v_and_b32 v64, 0xff800000, v64\n\t" \
  "v_bfe_i32 v65, v114, 1, 1\n\t" \
  "v_and_b32 v65, 0xff800000, v65\n\t" \
  "v_bfe_i32 v66, v114, 2, 1\n\t" \
  "v_and_b32 v66, 0xff800000, v66\n\t" \
  "v_bfe_i32 v67, v114, 3, 1\n\t" \
  "v_and_b32 v67, 0xff800000, v67\n\t" \
  "v_bfe_i32 v68, v114, 4, 1\n\t" \
  "v_and_b32 v68, 0xff800000, v68\n\t" \
  "v_bfe_i32 v69, v114, 5, 1\n\t" \
  "v_and_b32 v69, 0xff800000, v69\n\t" \
  "v_bfe_i32 v70, v114, 6, 1\n\t" \
  "v_and_b32 v70, 0xff800000, v70\n\t" \
  "v_bfe_i32 v71, v114, 7, 1\n\t" \
  "v_and_b32 v71, 0xff800000, v71\n\t" \
  "s_lshl_b32 s94, s45, 4\n\t" \
  "s_and_b32 s96, s44, 2\n\t" \
  "s_lshl_b32 s96, s96, 13\n\t" \
  "s_and_b32 s76, s44, 1\n\t" \
  "s_lshl_b32 s76, s76, 12\n\t" \
  "s_add_i32 s96, s96, s76\n\t" \
  "s_mov_b32 s95, 1\n\t" \
  "s_sleep 1\n\t" \
  "ds_read_b128 v[0:3], v101 offset:20480\n\t" \
  "ds_read_b128 v[4:7], v101 offset:21504\n\t" \
  "ds_read_b128 v[8:11], v101 offset:22528\n\t" \
  "ds_read_b128 v[12:15], v101 offset:23552\n\t" \
  "ds_read_b128 v[16:19], v101 offset:24576\n\t" \
  "ds_read_b128 v[20:23], v101 offset:25600\n\t" \
  "ds_read_b128 v[24:27], v101 offset:26624\n\t" \
  "ds_read_b128 v[28:31], v101 offset:27648\n\t" \
  "ds_read_b64_tr_b16 v[48:49], v108 offset:0\n\t" \
  "ds_read_b64_tr_b16 v[50:51], v108 offset:64\n\t" \
  "ds_read_b64_tr_b16 v[52:53], v108 offset:512\n\t" \
  "ds_read_b64_tr_b16 v[54:55], v108 offset:576\n\t" \
  "ds_read_b64_tr_b16 v[56:57], v108 offset:1024\n\t" \
  "ds_read_b64_tr_b16 v[58:59], v108 offset:1088\n\t" \
  "ds_read_b64_tr_b16 v[60:61], v108 offset:1536\n\t" \
  "ds_read_b64_tr_b16 v[62:63], v108 offset:1600\n\t" \
  "s_waitcnt vmcnt(3) lgkmcnt(8)\n\t" \
  "v_mfma_f32_16x16x32_bf16 v[64:67], v[0:3], v[84:87], v[64:67]\n\t" \
  "v_mfma_f32_16x16x32_bf16 v[68:71], v[16:19], v[84:87], v[68:71]\n\t" \
  "v_mfma_f32_16x16x32_bf16 v[64:67], v[4:7], v[88:91], v[64:67]\n\t" \
  "v_mfma_f32_16x16x32_bf16 v[68:71], v[20:23], v[88:91], v[68:71]\n\t" \
  "v_mfma_f32_16x16x32_bf16 v[64:67], v[8:11], v[92:95], v[64:67]\n\t" \
  "v_mfma_f32_16x16x32_bf16 v[68:71], v[24:27], v[92:95], v[68:71]\n\t" \
  "v_mfma_f32_16x16x32_bf16 v[64:67], v[12:15], v[96:99], v[64:67]\n\t" \
  "v_mfma_f32_16x16x32_bf16 v[68:71], v[28:31], v[96:99], v[68:71]\n\t" \
  "s_waitcnt lgkmcnt(0)\n\t" \
  "v_mfma_f32_16x16x32_bf16 v[72:75], v[48:51], v[84:87], 0\n\t" \
  "v_mfma_f32_16x16x32_bf16 v[72:75], v[52:55], v[88:91], v[72:75]\n\t" \
  "v_mfma_f32_16x16x32_bf16 v[72:75], v[56:59], v[92:95], v[72:75]\n\t" \
  "v_mfma_f32_16x16x32_bf16 v[72:75], v[60:63], v[96:99], v[72:75]\n\t" \
  "s_barrier\n\t" \
  "v_max3_f32 v76, v64, v65, v66\n\t" \
  "v_max3_f32 v77, v67, v68, v69\n\t" \
  "v_max3_f32 v76, v76, v70, v71\n\t" \
  "s_nop 4\n\t" \
  "v_max3_f32 v77, v77, v72, v73\n\t" \
  "v_max3_f32 v76, v76, v74, v75\n\t" \
  "v_max_f32 v76, v76, v77\n\t" \
  "v_mov_b32 v77, v76\n\t" \
  "s_nop 1\n\t" \
  "v_permlane16_swap_b32 v76, v77\n\t" \
  "v_max_f32 v76, v76, v77\n\t" \
  "v_mov_b32 v77, v76\n\t" \
  "s_nop 1\n\t" \
  "v_permlane32_swap_b32 v76, v77\n\t" \
  "v_max_f32 v76, v76, v77\n\t" \
  "v_mul_f32 v76, %[cs], v76\n\t" \
  "v_fma_f32 v64, v64, %[cs], -v76\n\t" \
  "v_fma_f32 v65, v65, %[cs], -v76\n\t" \
  "v_fma_f32 v66, v66, %[cs], -v76\n\t" \
  "v_fma_f32 v67, v67, %[cs], -v76\n\t" \
  "v_fma_f32 v68, v68, %[cs], -v76\n\t" \
  "v_fma_f32 v69, v69, %[cs], -v76\n\t" \
  "v_fma_f32 v70, v70, %[cs], -v76\n\t" \
  "v_fma_f32 v71, v71, %[cs], -v76\n\t" \
  "v_fma_f32 v72, v72, %[cs], -v76\n\t" \
  "v_fma_f32 v73, v73, %[cs], -v76\n\t" \
  "v_fma_f32 v74, v74, %[cs], -v76\n\t" \
  "v_fma_f32 v75, v75, %[cs], -v76\n\t" \
  "v_exp_f32 v64, v64\n\t" \
  "v_exp_f32 v65, v65\n\t" \
  "v_exp_f32 v66, v66\n\t" \
  "v_exp_f32 v67, v67\n\t" \
  "v_exp_f32 v68, v68\n\t" \
  "v_exp_f32 v69, v69\n\t" \
  "v_exp_f32 v70, v70\n\t" \
  "v_exp_f32 v71, v71\n\t" \
  "v_exp_f32 v72, v72\n\t" \
  "v_exp_f32 v73, v73\n\t" \
  "v_exp_f32 v74, v74\n\t" \
  "v_exp_f32 v75, v75\n\t" \
  "v_add_f32 v76, v72, v73\n\t" \
  "v_add_f32 v77, v74, v75\n\t" \
  "v_add_f32 v78, v64, v65\n\t" \
  "v_add_f32 v79, v66, v67\n\t" \
  "v_add_f32 v76, v76, v77\n\t" \
  "v_add_f32 v78, v78, v68\n\t" \
  "v_add_f32 v79, v79, v69\n\t" \
  "v_add_f32 v78, v78, v70\n\t" \
  "v_add_f32 v79, v79, v71\n\t" \
  "v_fma_f32 v76, v76, 0.5, v78\n\t" \
  "v_add_f32 v76, v76, v79\n\t" \
  "v_mov_b32 v77, v76\n\t" \
  "s_nop 1\n\t" \
  "v_permlane16_swap_b32 v76, v77\n\t" \
  "v_add_f32 v76, v76, v77\n\t" \
  "v_mov_b32 v77, v76\n\t" \
  "s_nop 1\n\t" \
  "v_permlane32_swap_b32 v76, v77\n\t" \
  "v_add_f32 v76, v76, v77\n\t" \
  "v_rcp_f32 v76, v76\n\t" \
  "s_nop 0\n\t" \
  "v_mul_f32 v64, v64, v76\n\t" \
  "v_mul_f32 v65, v65, v76\n\t" \
  "v_mul_f32 v66, v66, v76\n\t" \
  "v_mul_f32 v67, v67, v76\n\t" \
  "v_mul_f32 v68, v68, v76\n\t" \
  "v_mul_f32 v69, v69, v76\n\t" \
  "v_mul_f32 v70, v70, v76\n\t" \
  "v_mul_f32 v71, v71, v76\n\t" \
  "v_mul_f32 v72, v72, v76\n\t" \
  "v_mul_f32 v73, v73, v76\n\t" \
  "v_mul_f32 v74, v74, v76\n\t" \
  "v_mul_f32 v75, v75, v76\n\t" \
  "v_cvt_pk_bf16_f32 v0, v64, v68\n\t" \
  "v_cvt_pk_bf16_f32 v1, v65, v69\n\t" \
  "v_cvt_pk_bf16_f32 v2, v66, v70\n\t" \
  "v_cvt_pk_bf16_f32 v3, v67, v71\n\t" \
  "v_cvt_pk_bf16_f32 v4, v72, v73\n\t" \
  "v_cvt_pk_bf16_f32 v5, v74, v75\n\t" \
  "v_permlane32_swap_b32 v0, v2\n\t" \
  "v_permlane32_swap_b32 v1, v3\n\t" \
  "v_mov_b64 v[6:7], v[4:5]\n\t" \
  "s_mov_b32 exec_hi, 0\n\t" \
  "buffer_load_dword v118, v112, %[rh], s96 offen sc1\n\t" \
  "s_mov_b64 exec, -1\n\t" \
  "buffer_store_dwordx4 v[0:3], v110, %[rh], s94 offen sc1\n\t" \
  "v_permlane16_swap_b32 v4, v6\n\t" \
  "v_permlane16_swap_b32 v5, v7\n\t" \
  "buffer_store_dwordx4 v[4:7], v111, %[rh], s94 offen sc1\n\t" \
  "s_waitcnt vmcnt(3)\n\t" \
  "ds_write_b128 v101, v[32:35] offset:23552\n\t" \
  "ds_write_b128 v101, v[36:39] offset:24576\n\t" \
  "ds_write_b128 v101, v[40:43] offset:25600\n\t" \
  "s_add_i32 s77, s44, 2\n\t" \
  "s_min_u32 s77, s77, 63\n\t" \
  "v_cmp_gt_i32 vcc, s77, v119\n\t" \
  "s_cmp_lg_u64 vcc, 0\n\t" \
  "s_cselect_b32 s42, 32, 0\n\t" \
  "s_cmp_eq_u32 s44, 0\n\t" \
  "s_cselect_b32 s42, 32, s42\n\t" \
  "s_add_i32 s41, s56, 1950\n\t" \
  "s_sub_i32 s76, s92, s51\n\t" \
  "s_sub_i32 s77, s93, s53\n\t" \
  "s_cmp_eq_u32 s76, 0\n\t" \
  "s_cbranch_scc1 21f\n\t" \
  "v_cvt_f32_u32 v117, s76\n\t" \
  "v_cvt_f32_u32 v116, s77\n\t" \
  "v_rcp_f32 v117, v117\n\t" \
  "s_mov_b32 s51, s92\n\t" \
  "v_mul_f32 v117, v117, v116\n\t" \
  "v_max_f32 v117, 0x3f028f5c, v117\n\t" \
  "v_min_f32 v117, 0x3ffeb852, v117\n\t" \
  "v_rcp_f32 v116, v117\n\t" \
  "v_mul_f32 v117, 0x4f000000, v117\n\t" \
  "v_mul_f32 v116, 0x47800000, v116\n\t" \
  "v_cvt_u32_f32 v117, v117\n\t" \
  "v_cvt_u32_f32 v116, v116\n\t" \
  "s_mov_b32 s53, s93\n\t" \
  "v_readfirstlane_b32 s46, v117\n\t" \
  "v_readfirstlane_b32 s52, v116\n\t" \
  "21:\n\t" \
  "s_add_i32 s44, s44, 1\n\t" \
  "s_and_b32 s58, s58, 4\n\t" \
  "s_and_b32 s76, s44, 64\n\t" \
  "s_or_b32 s42, s42, s76\n\t" \
  "s_cbranch_scc0 20b\n\t" \
  "s_branch 99f\n\t" \
  "14:\n\t" \
  "s_sleep 3\n\t" \
  "s_add_u32 s59, s59, 0x10000\n\t" \
  "s_cmp_lt_u32 s59, 0x100000\n\t" \
  "s_cbranch_scc1 13b\n\t" \
  "s_mov_b64 exec, -1\n\t" \
  "s_mov_b32 s42, 16\n\t" \
  "s_branch 99f\n\t" \
  "8:\n\t" \
  "s_bitcmp1_b32 s58, 1\n\t" \
  "s_cbranch_scc1 4b\n\t" \
  "s_bitcmp1_b32 s58, 2\n\t" \
  "s_cbranch_scc1 5b\n\t" \
  "v_max3_u32 v116, v32, v33, v34\n\t" \
  "v_max3_u32 v117, v35, v36, v37\n\t" \
  "v_max3_u32 v116, v116, v38, v39\n\t" \
  "v_max_u32 v116, v116, v117\n\t" \
  "v_cmp_eq_u32 vcc, -1, v116\n\t" \
  "s_cbranch_vccz 5b\n\t" \
  "s_branch 0f\n\t" \
  "9:\n\t" \
  "s_bitcmp1_b32 s58, 2\n\t" \
  "s_cbranch_scc1 6b\n\t" \
  "v_max3_u32 v116, v40, v41, v42\n\t" \
  "v_max3_u32 v117, v43, v44, v45\n\t" \
  "v_max3_u32 v116, v116, v46, v47\n\t" \
  "v_max_u32 v116, v116, v117\n\t" \
  "v_cmp_eq_u32 vcc, -1, v116\n\t" \
  "s_cbranch_vccz 6b\n\t" \
  "s_or_b32 s58, s58, 2\n\t" \
  "0:\n\t" \
  "s_or_b32 s58, s58, 1\n\t" \
  "11:\n\t" \
  "buffer_load_dwordx4 v[32:35], v100, %[rh], s40 offen sc1\n\t" \
  "buffer_load_dwordx4 v[36:39], v100, %[rh], s76 offen sc1\n\t" \
  "buffer_load_dwordx4 v[40:43], v100, %[rh], s77 offen sc1\n\t" \
  "buffer_load_dwordx4 v[44:47], v100, %[rh], s78 offen sc1\n\t" \
  "s_waitcnt lgkmcnt(0)\n\t" \
  "s_branch 10b\n\t" \
  "17:\n\t" \
  "s_add_u32 s59, s59, 1\n\t" \
  "s_bitcmp1_b32 s58, 2\n\t" \
  "s_cbranch_scc1 3b\n\t" \
  "s_waitcnt vmcnt(0)\n\t" \
  "v_max_u32 v116, v32, v33\n\t" \
  "v_max3_u32 v116, v116, v34, v35\n\t" \
  "v_max3_u32 v116, v116, v36, v37\n\t" \
  "v_max3_u32 v116, v116, v38, v39\n\t" \
  "v_max3_u32 v116, v116, v40, v41\n\t" \
  "v_max3_u32 v116, v116, v42, v43\n\t" \
  "v_max3_u32 v116, v116, v44, v45\n\t" \
  "v_max3_u32 v116, v116, v46, v47\n\t" \
  "v_cmp_eq_u32 vcc, -1, v116\n\t" \
  "s_cbranch_vccz 3b\n\t" \
  "s_bitcmp1_b32 s59, 6\n\t" \
  "s_cbranch_scc1 12f\n\t" \
  "buffer_load_dwordx4 v[32:35], v100, %[rh], s40 offen sc1\n\t" \
  "buffer_load_dwordx4 v[36:39], v100, %[rh], s76 offen sc1\n\t" \
  "buffer_load_dwordx4 v[40:43], v100, %[rh], s77 offen sc1\n\t" \
  "buffer_load_dwordx4 v[44:47], v100, %[rh], s78 offen sc1\n\t" \
  "s_branch 17b\n\t" \
  "12:\n\t" \
  "s_mov_b32 s42, s58\n\t" \
  "s_branch 99f\n\t" \
  "93:\n\t" \
  "s_mov_b32 s58, s42\n\t" \
  "20:\n\t" \
  "s_and_b32 s76, s44, 7\n\t" \
  "s_lshl_b32 s76, s76, 15\n\t" \
  "s_add_i32 s40, s54, s76\n\t" \
  "s_and_b32 s76, s44, 3\n\t" \
  "s_lshl_b32 s45, s76, 14\n\t" \
  "s_add_i32 s47, s44, 1\n\t" \
  "s_min_u32 s47, s47, 63\n\t" \
  "s_lshr_b32 s76, s47, 1\n\t" \
  "s_lshl_b32 s47, s47, 20\n\t" \
  "s_add_i32 s47, s47, s55\n\t" \
  "s_bitcmp1_b64 %[sel], s76\n\t" \
  "s_cselect_b32 s76, 0x2000, 0\n\t" \
  "s_add_i32 s47, s47, s76\n\t" \
  "s_add_i32 s49, s44, -1\n\t" \
  "s_and_b32 s48, s49, 2\n\t" \
  "s_lshl_b32 s48, s48, 13\n\t" \
  "s_and_b32 s76, s49, 1\n\t" \
  "s_lshl_b32 s76, s76, 12\n\t" \
  "s_add_i32 s48, s48, s76\n\t" \
  "s_lshl_b32 s50, s53, 8\n\t" \
  "s_mov_b32 s59, 0\n\t" \
  "s_mov_b32 s92, s51\n\t" \
  "s_mov_b32 s93, s53\n\t" \
  "s_xor_b32 s76, s40, 0x400\n\t" \
  "s_xor_b32 s77, s40, 0x800\n\t" \
  "s_xor_b32 s78, s40, 0xc00\n\t" \
  "s_cmp_lg_u32 s58, 0\n\t" \
  "s_cbranch_scc1 11f\n\t" \
  "10:\n\t" \
  "ds_read_b128 v[48:51], v103 offset:32768\n\t" \
  "ds_read_b128 v[52:55], v103 offset:33792\n\t" \
  "ds_read_b128 v[56:59], v103 offset:34816\n\t" \
  "ds_read_b128 v[60:63], v103 offset:35840\n\t" \
  "ds_read_b128 v[16:19], v103 offset:49152\n\t" \
  "ds_read_b128 v[20:23], v103 offset:50176\n\t" \
  "ds_read_b128 v[24:27], v103 offset:51200\n\t" \
  "ds_read_b128 v[28:31], v103 offset:52224\n\t" \
  "ds_read_b128 v[84:87], v102 offset:16384\n\t" \
  "ds_read_b128 v[88:91], v102 offset:17408\n\t" \
  "ds_read_b128 v[92:95], v102 offset:18432\n\t" \
  "ds_read_b128 v[96:99], v102 offset:19456\n\t" \
  "s_waitcnt lgkmcnt(4)\n\t" \
  "ds_read_b128 v[0:3], v102 offset:49152\n\t" \
  "ds_read_b128 v[4:7], v102 offset:50176\n\t" \
  "ds_read_b128 v[8:11], v102 offset:51200\n\t" \
  "ds_read_b128 v[12:15], v102 offset:52224\n\t" \
  "s_bitcmp1_b32 s58, 1\n\t" \
  "s_cselect_b64 s[80:81], -1, 0\n\t" \
  "s_cmp_lg_u32 s58, 0\n\t" \
  "s_cbranch_scc1 17f\n\t" \
  "s_mov_b32 exec_hi, 0\n\t" \
  "16:\n\t" \
  "s_memtime vcc\n\t" \
  "s_waitcnt lgkmcnt(0)\n\t" \
  "s_sub_i32 s79, s41, vcc_lo\n\t" \
  "s_cmp_gt_i32 s79, 64\n\t" \
  "s_cbranch_scc1 16b\n\t" \
  "13:\n\t" \
  "buffer_load_dword v116, v112, %[rh], s48 offen sc1\n\t" \
  "s_waitcnt vmcnt(0)\n\t" \
  "15:\n\t" \
  "v_mov_b32 v117, 20000\n\t" \
  "v_cmp_ne_u32_sdwa vcc, v116, s49 src0_sel:BYTE_0 src1_sel:DWORD\n\t" \
  "v_subrev_u32 v116, s50, v116\n\t" \
  "v_ashrrev_i32 v116, 8, v116\n\t" \
  "s_nop 1\n\t" \
  "v_max_i32_dpp v116, v116, v116 quad_perm:[1,0,3,2] row_mask:0xf bank_mask:0xf\n\t" \
  "s_cbranch_vccnz 14f\n\t" \
  "s_nop 0\n\t" \
  "v_max_i32_dpp v116, v116, v116 quad_perm:[2,3,0,1] row_mask:0xf bank_mask:0xf\n\t" \
  "s_nop 1\n\t" \
  "v_max_i32_dpp v116, v116, v116 row_half_mirror row_mask:0xf bank_mask:0xf\n\t" \
  "s_nop 1\n\t" \
  "v_max_i32_dpp v116, v116, v116 row_mirror row_mask:0xf bank_mask:0xf\n\t" \
  "v_med3_i32 v116, v116, 0, v117\n\t" \
  "v_mul_u32_u24 v116, s52, v116\n\t" \
  "v_lshrrev_b32 v116, 16, v116\n\t" \
  "s_mov_b64 exec, -1\n\t" \
  "v_readfirstlane_b32 s84, v116\n\t" \
  "v_readlane_b32 s85, v116, 16\n\t" \
  "s_max_u32 s84, s84, s85\n\t" \
  "s_add_i32 s84, s84, s51\n\t" \
  "1:\n\t" \
  "s_memtime vcc\n\t" \
  "s_waitcnt lgkmcnt(0)\n\t" \
  "s_sub_i32 s79, s84, vcc_lo\n\t" \
  "s_cmp_gt_i32 s79, 128\n\t" \
  "s_cbranch_scc1 1b\n\t" \
  ".p2align 4\n\t" \
  "2:\n\t" \
  "s_sub_i32 s79, s79, 33\n\t" \
  "s_cmp_gt_i32 s79, 56\n\t" \
  "s_cbranch_scc1 2b\n\t" \
  "buffer_load_dwordx4 v[32:35], v100, %[rh], s40 offen sc1\n\t" \
  "s_nop 12\n\t" \
  "buffer_load_dwordx4 v[36:39], v100, %[rh], s76 offen sc1\n\t" \
  "s_nop 12\n\t" \
  "buffer_load_dwordx4 v[40:43], v100, %[rh], s77 offen sc1\n\t" \
  "s_nop 12\n\t" \
  "buffer_load_dwordx4 v[44:47], v100, %[rh], s78 offen sc1\n\t" \
  "s_memtime vcc\n\t" \
  "s_memrealtime s[82:83]\n\t" \
  "s_nop 4\n\t" \
  "s_memrealtime s[84:85]\n\t" \
  "s_nop 4\n\t" \
  "s_memrealtime s[56:57]\n\t" \
  "s_nop 4\n\t" \
  "s_memrealtime s[86:87]\n\t" \
  "s_nop 4\n\t" \
  "s_memrealtime s[88:89]\n\t" \
  "s_nop 4\n\t" \
  "s_memrealtime s[90:91]\n\t" \
  "s_mov_b32 s92, 0\n\t" \
  "s_waitcnt lgkmcnt(0)\n\t" \
  "s_cmp_eq_u32 s82, s84\n\t" \
  "s_addc_u32 s92, s92, 0\n\t" \
  "s_cmp_eq_u32 s82, s56\n\t" \
  "s_addc_u32 s92, s92, 0\n\t" \
  "s_cmp_eq_u32 s82, s86\n\t" \
  "s_addc_u32 s92, s92, 0\n\t" \
  "s_cmp_eq_u32 s82, s88\n\t" \
  "s_addc_u32 s92, s92, 0\n\t" \
  "s_cmp_eq_u32 s82, s90\n\t" \
  "s_addc_u32 s92, s92, 0\n\t" \
  "s_mul_i32 s92, s92, 24\n\t" \
  "s_add_i32 s92, s92, vcc_lo\n\t" \
  "s_lshl4_add_u32 s93, s82, 64\n\t" \
  "3:\n\t" \
  "v_mov_b64 v[68:69], 0\n\t" \
  "v_mov_b64 v[70:71], 0\n\t" \
  "v_mov_b64 v[72:73], 0\n\t" \
  "v_mov_b64 v[74:75], 0\n\t" \
  "v_mov_b64 v[76:77], 0\n\t" \
  "v_mov_b64 v[78:79], 0\n\t" \
  "v_mov_b64 v[80:81], 0\n\t" \
  "v_mov_b64 v[82:83], 0\n\t" \
  "s_waitcnt lgkmcnt(0)\n\t" \
  "s_waitcnt vmcnt(3)\n\t" \
  "v_smfmac_f32_16x16x64_bf16 v[68:71], v[32:35], v[152:159], v104\n\t" \
  "ds_write_b128 v101, v[32:35] offset:12288\n\t" \
  "v_smfmac_f32_16x16x64_bf16 v[72:75], v[32:35], v[160:167], v104\n\t" \
  "v_smfmac_f32_16x16x64_bf16 v[76:79], v[32:35], v[48:55], v104\n\t" \
  "v_smfmac_f32_16x16x64_bf16 v[80:83], v[32:35], v[56:63], v104\n\t" \
  "s_waitcnt vmcnt(2)\n\t" \
  "v_smfmac_f32_16x16x64_bf16 v[68:71], v[36:39], v[168:175], v104\n\t" \
  "ds_write_b128 v101, v[36:39] offset:13312\n\t" \
  "v_smfmac_f32_16x16x64_bf16 v[72:75], v[36:39], v[176:183], v104\n\t" \
  "v_smfmac_f32_16x16x64_bf16 v[76:79], v[36:39], v[184:191], v104\n\t" \
  "v_cmp_u_f32 vcc, v68, v69\n\t" \
  "v_smfmac_f32_16x16x64_bf16 v[80:83], v[36:39], v[192:199], v104\n\t" \
  "v_cmp_u_f32 s[82:83], v70, v71\n\t" \
  "s_waitcnt vmcnt(1)\n\t" \
  "v_smfmac_f32_16x16x64_bf16 v[68:71], v[40:43], v[16:23], v104\n\t" \
  "ds_write_b128 v101, v[40:43] offset:14336\n\t" \
  "v_smfmac_f32_16x16x64_bf16 v[72:75], v[40:43], v[24:31], v104\n\t" \
  "s_or_b64 vcc, vcc, s[82:83]\n\t" \
  "s_or_b64 vcc, vcc, s[80:81]\n\t" \
  "s_waitcnt lgkmcnt(1)\n\t" \
  "v_smfmac_f32_16x16x64_bf16 v[76:79], v[40:43], v[200:207], v104\n\t" \
  "s_cbranch_scc1 8f\n\t" \
  "5:\n\t" \
  "s_barrier\n\t" \
  "4:\n\t" \
  "s_memtime s[56:57]\n\t" \
  "ds_read_b128 v[48:51], v101 offset:0\n\t" \
  "v_smfmac_f32_16x16x64_bf16 v[80:83], v[40:43], v[208:215], v104\n\t" \
  "ds_read_b128 v[52:55], v101 offset:1024\n\t" \
  "ds_read_b128 v[56:59], v101 offset:4096\n\t" \
  "s_waitcnt vmcnt(0)\n\t" \
  "v_smfmac_f32_16x16x64_bf16 v[68:71], v[44:47], v[216:223], v104\n\t" \
  "v_smfmac_f32_16x16x64_bf16 v[72:75], v[44:47], v[224:231], v104\n\t" \
  "ds_write_b128 v101, v[44:47] offset:15360\n\t" \
  "v_smfmac_f32_16x16x64_bf16 v[76:79], v[44:47], v[232:239], v104\n\t" \
  "v_cmp_u_f32 vcc, v68, v69\n\t" \
  "v_smfmac_f32_16x16x64_bf16 v[80:83], v[44:47], v[240:247], v104\n\t" \
  "v_cmp_u_f32 s[82:83], v70, v71\n\t" \
  "s_waitcnt lgkmcnt(3)\n\t" \
  "v_smfmac_f32_16x16x64_bf16 v[68:71], v[48:51], a[0:7], v104\n\t" \
  "ds_read_b128 v[60:63], v101 offset:5120\n\t" \
  "ds_read_b128 v[64:67], v101 offset:8192\n\t" \
  "v_smfmac_f32_16x16x64_bf16 v[72:75], v[48:51], a[8:15], v104\n\t" \
  "s_or_b64 vcc, vcc, s[82:83]\n\t" \
  "s_waitcnt lgkmcnt(2)\n\t" \
  "v_smfmac_f32_16x16x64_bf16 v[68:71], v[52:55], a[16:23], v104\n\t" \
  "s_cbranch_scc1 9f\n\t" \
  "6:\n\t" \
  "s_barrier\n\t" \
  "v_smfmac_f32_16x16x64_bf16 v[72:75], v[52:55], a[24:31], v104\n\t" \
  "ds_read_b128 v[48:51], v101 offset:9216\n\t" \
  "v_smfmac_f32_16x16x64_bf16 v[68:71], v[56:59], a[96:103], v104\n\t" \
  "ds_read_b128 v[52:55], v101 offset:2048\n\t" \
  "v_smfmac_f32_16x16x64_bf16 v[72:75], v[56:59], a[104:111], v104\n\t" \
  "s_waitcnt lgkmcnt(3)\n\t" \
  "v_smfmac_f32_16x16x64_bf16 v[68:71], v[60:63], a[112:119], v104\n\t" \
  "ds_read_b128 v[56:59], v101 offset:3072\n\t" \
  "v_smfmac_f32_16x16x64_bf16 v[72:75], v[60:63], a[120:127], v104\n\t" \
  "s_waitcnt lgkmcnt(3)\n\t" \
  "v_smfmac_f32_16x16x64_bf16 v[68:71], v[64:67], a[192:199], v104\n\t" \
  "ds_read_b128 v[60:63], v101 offset:6144\n\t" \
  "v_smfmac_f32_16x16x64_bf16 v[72:75], v[64:67], a[200:207], v104\n\t" \
  "s_waitcnt lgkmcnt(3)\n\t" \
  "v_smfmac_f32_16x16x64_bf16 v[68:71], v[48:51], a[208:215], v104\n\t" \
  "ds_read_b128 v[64:67], v101 offset:7168\n\t" \
  "ds_read_b128 v[16:19], v103 offset:16384\n\t" \
  "global_load_dword v119, v109, %[kc] sc1\n\t" \
  "v_smfmac_f32_16x16x64_bf16 v[72:75], v[48:51], a[216:223], v104\n\t" \
  "s_waitcnt lgkmcnt(4)\n\t" \
  "v_smfmac_f32_16x16x64_bf16 v[68:71], v[52:55], v[84:91], v104\n\t" \
  "ds_read_b128 v[48:51], v101 offset:11264\n\t" \
  "ds_read_b128 v[20:23], v103 offset:17408\n\t" \
  "v_smfmac_f32_16x16x64_bf16 v[72:75], v[52:55], v[92:99], v104\n\t" \
  "s_waitcnt lgkmcnt(5)\n\t" \
  "v_smfmac_f32_16x16x64_bf16 v[68:71], v[56:59], a[64:71], v104\n\t" \
  "ds_read_b128 v[52:55], v101 offset:10240\n\t" \
  "ds_read_b128 v[24:27], v103 offset:18432\n\t" \
  "v_smfmac_f32_16x16x64_bf16 v[72:75], v[56:59], a[72:79], v104\n\t" \
  "ds_read_b128 v[28:31], v103 offset:19456\n\t" \
  "s_waitcnt lgkmcnt(7)\n\t" \
  "v_smfmac_f32_16x16x64_bf16 v[68:71], v[60:63], v[0:7], v104\n\t" \
  "v_smfmac_f32_16x16x64_bf16 v[72:75], v[60:63], v[8:15], v104\n\t" \
  "s_waitcnt lgkmcnt(6)\n\t" \
  "v_smfmac_f32_16x16x64_bf16 v[68:71], v[64:67], a[160:167], v104\n\t" \
  "ds_read_b128 v[0:3], v102 offset:0\n\t" \
  "v_smfmac_f32_16x16x64_bf16 v[72:75], v[64:67], a[168:175], v104\n\t" \
  "ds_read_b128 v[4:7], v102 offset:1024\n\t" \
  "s_waitcnt lgkmcnt(6)\n\t" \
  "v_smfmac_f32_16x16x64_bf16 v[68:71], v[48:51], v[120:127], v104\n\t" \
  "s_waitcnt lgkmcnt(4)\n\t" \
  "v_smfmac_f32_16x16x64_bf16 v[68:71], v[52:55], v[16:23], v104\n\t" \
  "ds_read_b128 v[8:11], v102 offset:2048\n\t" \
  "v_smfmac_f32_16x16x64_bf16 v[72:75], v[48:51], v[128:135], v104\n\t" \
  "ds_read_b128 v[12:15], v102 offset:3072\n\t" \
  "s_waitcnt lgkmcnt(4)\n\t" \
  "v_smfmac_f32_16x16x64_bf16 v[72:75], v[52:55], v[24:31], v104\n\t" \
  "v_add_f32 v44, v68, v70\n\t" \
  "v_smfmac_f32_16x16x64_bf16 v[76:79], v[56:59], a[80:87], v104\n\t" \
  "v_add_f32 v45, v69, v71\n\t" \
  "v_cvt_pk_bf16_f32 v44, v44, v45\n\t" \
  "v_smfmac_f32_16x16x64_bf16 v[80:83], v[56:59], a[88:95], v104\n\t" \
  "v_add_f32 v46, v72, v74\n\t" \
  "v_add_f32 v47, v73, v75\n\t" \
  "v_smfmac_f32_16x16x64_bf16 v[76:79], v[60:63], a[144:151], v104\n\t" \
  "v_cvt_pk_bf16_f32 v46, v46, v47\n\t" \
  "ds_write2st64_b32 v105, v44, v46 offset1:1\n\t" \
  "v_smfmac_f32_16x16x64_bf16 v[80:83], v[60:63], a[152:159], v104\n\t" \
  "ds_read_b128 v[56:59], v101 offset:0\n\t" \
  "v_smfmac_f32_16x16x64_bf16 v[76:79], v[64:67], a[176:183], v104\n\t" \
  "ds_read_b128 v[16:19], v102 offset:32768\n\t" \
  "v_smfmac_f32_16x16x64_bf16 v[80:83], v[64:67], a[184:191], v104\n\t" \
  "ds_read_b128 v[20:23], v102 offset:33792\n\t" \
  "v_smfmac_f32_16x16x64_bf16 v[76:79], v[48:51], v[136:143], v104\n\t" \
  "s_waitcnt lgkmcnt(3)\n\t" \
  "s_barrier\n\t" \
  "v_smfmac_f32_16x16x64_bf16 v[80:83], v[48:51], v[144:151], v104\n\t" \
  "v_smfmac_f32_16x16x64_bf16 v[76:79], v[52:55], a[240:247], v104\n\t" \
  "ds_read_b128 v[60:63], v101 offset:9216\n\t" \
  "ds_read_b128 v[24:27], v102 offset:34816\n\t" \
  "v_smfmac_f32_16x16x64_bf16 v[80:83], v[52:55], a[248:255], v104\n\t" \
  "ds_read_b128 v[64:67], v101 offset:2048\n\t" \
  "s_waitcnt lgkmcnt(5)\n\t" \
  "v_smfmac_f32_16x16x64_bf16 v[76:79], v[56:59], v[0:7], v104\n\t" \
  "ds_read_b128 v[48:51], v101 offset:4096\n\t" \
  "ds_read_b128 v[28:31], v102 offset:35840\n\t" \
  "v_smfmac_f32_16x16x64_bf16 v[80:83], v[56:59], v[8:15], v104\n\t" \
  "s_waitcnt lgkmcnt(4)\n\t" \
  "v_smfmac_f32_16x16x64_bf16 v[76:79], v[60:63], a[224:231], v104\n\t" \
  "ds_read_b128 v[52:55], v101 offset:1024\n\t" \
  "v_smfmac_f32_16x16x64_bf16 v[80:83], v[60:63], a[232:239], v104\n\t" \
  "ds_read_b128 v[0:3], v103 offset:0\n\t" \
  "buffer_load_dwordx4 v[32:35], v100, %[ru], s47 offen offset:0 sc1\n\t" \
  "s_waitcnt lgkmcnt(4)\n\t" \
  "v_smfmac_f32_16x16x64_bf16 v[76:79], v[64:67], a[48:55], v104\n\t" \
  "ds_read_b128 v[56:59], v101 offset:5120\n\t" \
  "v_smfmac_f32_16x16x64_bf16 v[80:83], v[64:67], a[56:63], v104\n\t" \
  "ds_read_b128 v[60:63], v101 offset:8192\n\t" \
  "ds_read_b128 v[4:7], v103 offset:1024\n\t" \
  "s_waitcnt lgkmcnt(6)\n\t" \
  "v_smfmac_f32_16x16x64_bf16 v[76:79], v[48:51], v[16:23], v104\n\t" \
  "ds_read_b128 v[8:11], v103 offset:2048\n\t" \
  "buffer_load_dwordx4 v[36:39], v100, %[ru], s47 offen offset:1024 sc1\n\t" \
  "s_waitcnt lgkmcnt(6)\n\t" \
  "v_smfmac_f32_16x16x64_bf16 v[80:83], v[48:51], v[24:31], v104\n\t" \
  "ds_read_b128 v[12:15], v103 offset:3072\n\t" \
  "s_waitcnt lgkmcnt(6)\n\t" \
  "v_smfmac_f32_16x16x64_bf16 v[76:79], v[52:55], a[32:39], v104\n\t" \
  "v_smfmac_f32_16x16x64_bf16 v[80:83], v[52:55], a[40:47], v104\n\t" \
  "s_waitcnt lgkmcnt(4)\n\t" \
  "v_smfmac_f32_16x16x64_bf16 v[76:79], v[56:59], a[128:135], v104\n\t" \
  "s_waitcnt lgkmcnt(2)\n\t" \
  "v_smfmac_f32_16x16x64_bf16 v[76:79], v[60:63], v[0:7], v104\n\t" \
  "v_smfmac_f32_16x16x64_bf16 v[80:83], v[56:59], a[136:143], v104\n\t" \
  "s_waitcnt lgkmcnt(0)\n\t" \
  "v_smfmac_f32_16x16x64_bf16 v[80:83], v[60:63], v[8:15], v104\n\t" \
  "v_add_f32 v20, v76, v78\n\t" \
  "v_add_f32 v21, v77, v79\n\t" \
  "v_cvt_pk_bf16_f32 v20, v20, v21\n\t" \
  "ds_write_b32 v106, v20\n\t" \
  "s_nop 3\n\t" \
  "v_add_f32 v23, v80, v82\n\t" \
  "v_add_f32 v21, v81, v83\n\t" \
  "v_cvt_pk_bf16_f32 v21, v23, v21\n\t" \
  "ds_write_b32 v106, v21 offset:256\n\t" \
  "s_waitcnt lgkmcnt(0)\n\t" \
  "s_barrier\n\t" \
  "s_waitcnt vmcnt(0)\n\t" \
  "s_barrier\n\t" \
  "ds_write_b128 v101, v[32:35] offset:26624\n\t" \
  "ds_write_b128 v101, v[36:39] offset:27648\n\t" \
  "s_add_i32 s77, s44, 2\n\t" \
  "s_min_u32 s77, s77, 63\n\t" \
  "v_cmp_gt_i32 vcc, s77, v119\n\t" \
  "s_cmp_lg_u64 vcc, 0\n\t" \
  "s_cselect_b32 s42, 32, 0\n\t" \
  "s_cmp_eq_u32 s44, 0\n\t" \
  "s_cselect_b32 s42, 32, s42\n\t" \
  "s_add_i32 s41, s56, 1950\n\t" \
  "s_sub_i32 s76, s92, s51\n\t" \
  "s_sub_i32 s77, s93, s53\n\t" \
  "s_cmp_eq_u32 s76, 0\n\t" \
  "s_cbranch_scc1 21f\n\t" \
  "v_cvt_f32_u32 v117, s76\n\t" \
  "v_cvt_f32_u32 v116, s77\n\t" \
  "v_rcp_f32 v117, v117\n\t" \
  "s_mov_b32 s51, s92\n\t" \
  "v_mul_f32 v117, v117, v116\n\t" \
  "v_max_f32 v117, 0x3f028f5c, v117\n\t" \
  "v_min_f32 v117, 0x3ffeb852, v117\n\t" \
  "v_rcp_f32 v116, v117\n\t" \
  "v_mul_f32 v117, 0x4f000000, v117\n\t" \
  "v_mul_f32 v116, 0x47800000, v116\n\t" \
  "v_cvt_u32_f32 v117, v117\n\t" \
  "v_cvt_u32_f32 v116, v116\n\t" \
  "s_mov_b32 s53, s93\n\t" \
  "v_readfirstlane_b32 s46, v117\n\t" \
  "v_readfirstlane_b32 s52, v116\n\t" \
  "21:\n\t" \
  "v_mov_b32 v0, -1\n\t" \
  "v_mov_b32 v1, -1\n\t" \
  "v_mov_b32 v2, -1\n\t" \
  "v_mov_b32 v3, -1\n\t" \
  "s_and_b32 s76, s49, 3\n\t" \
  "s_mul_i32 s76, s76, 0xc000\n\t" \
  "s_add_i32 s76, s76, 0x40000\n\t" \
  "buffer_store_dwordx4 v[0:3], v110, %[rh], s76 offen sc1\n\t" \
  "s_add_i32 s76, s44, 1\n\t" \
  "s_and_b32 s76, s76, 3\n\t" \
  "s_lshl_b32 s76, s76, 18\n\t" \
  "s_add_i32 s77, s76, 0x10000\n\t" \
  "buffer_store_dwordx4 v[0:3], v107, %[rh], s76 offen sc1\n\t" \
  "buffer_store_dwordx4 v[0:3], v107, %[rh], s77 offen sc1\n\t" \
  "buffer_store_dwordx4 v[0:3], v111, %[rh], s76 offen sc1\n\t" \
  "s_add_i32 s44, s44, 1\n\t" \
  "s_and_b32 s58, s58, 4\n\t" \
  "s_and_b32 s76, s44, 64\n\t" \
  "s_or_b32 s42, s42, s76\n\t" \
  "s_cbranch_scc0 20b\n\t" \
  "s_branch 99f\n\t" \
  "14:\n\t" \
  "s_sleep 3\n\t" \
  "s_add_u32 s59, s59, 0x10000\n\t" \
  "s_cmp_lt_u32 s59, 0x100000\n\t" \
  "s_cbranch_scc1 13b\n\t" \
  "s_mov_b64 exec, -1\n\t" \
  "s_mov_b32 s42, 16\n\t" \
  "s_branch 99f\n\t" \
  "8:\n\t" \
  "s_bitcmp1_b32 s58, 1\n\t" \
  "s_cbranch_scc1 4b\n\t" \
  "s_bitcmp1_b32 s58, 2\n\t" \
  "s_cbranch_scc1 5b\n\t" \
  "v_max3_u32 v116, v32, v33, v34\n\t" \
  "v_max3_u32 v117, v35, v36, v37\n\t" \
  "v_max3_u32 v116, v116, v38, v39\n\t" \
  "v_max_u32 v116, v116, v117\n\t" \
  "v_cmp_eq_u32 vcc, -1, v116\n\t" \
  "s_cbranch_vccz 5b\n\t" \
  "s_branch 0f\n\t" \
  "9:\n\t" \
  "s_bitcmp1_b32 s58, 2\n\t" \
  "s_cbranch_scc1 6b\n\t" \
  "v_max3_u32 v116, v40, v41, v42\n\t" \
  "v_max3_u32 v117, v43, v44, v45\n\t" \
  "v_max3_u32 v116, v116, v46, v47\n\t" \
  "v_max_u32 v116, v116, v117\n\t" \
  "v_cmp_eq_u32 vcc, -1, v116\n\t" \
  "s_cbranch_vccz 6b\n\t" \
  "s_or_b32 s58, s58, 2\n\t" \
  "0:\n\t" \
  "s_or_b32 s58, s58, 1\n\t" \
  "11:\n\t" \
  "buffer_load_dwordx4 v[32:35], v100, %[rh], s40 offen sc1\n\t" \
  "buffer_load_dwordx4 v[36:39], v100, %[rh], s76 offen sc1\n\t" \
  "buffer_load_dwordx4 v[40:43], v100, %[rh], s77 offen sc1\n\t" \
  "buffer_load_dwordx4 v[44:47], v100, %[rh], s78 offen sc1\n\t" \
  "s_waitcnt lgkmcnt(0)\n\t" \
  "s_branch 10b\n\t" \
  "17:\n\t" \
  "s_add_u32 s59, s59, 1\n\t" \
  "s_bitcmp1_b32 s58, 2\n\t" \
  "s_cbranch_scc1 3b\n\t" \
  "s_waitcnt vmcnt(0)\n\t" \
  "v_max_u32 v116, v32, v33\n\t" \
  "v_max3_u32 v116, v116, v34, v35\n\t" \
  "v_max3_u32 v116, v116, v36, v37\n\t" \
  "v_max3_u32 v116, v116, v38, v39\n\t" \
  "v_max3_u32 v116, v116, v40, v41\n\t" \
  "v_max3_u32 v116, v116, v42, v43\n\t" \
  "v_max3_u32 v116, v116, v44, v45\n\t" \
  "v_max3_u32 v116, v116, v46, v47\n\t" \
  "v_cmp_eq_u32 vcc, -1, v116\n\t" \
  "s_cbranch_vccz 3b\n\t" \
  "s_bitcmp1_b32 s59, 6\n\t" \
  "s_cbranch_scc1 12f\n\t" \
  "buffer_load_dwordx4 v[32:35], v100, %[rh], s40 offen sc1\n\t" \
  "buffer_load_dwordx4 v[36:39], v100, %[rh], s76 offen sc1\n\t" \
  "buffer_load_dwordx4 v[40:43], v100, %[rh], s77 offen sc1\n\t" \
  "buffer_load_dwordx4 v[44:47], v100, %[rh], s78 offen sc1\n\t" \
  "s_branch 17b\n\t" \
  "12:\n\t" \
  "s_mov_b32 s42, s58\n\t" \
  "s_branch 99f\n\t" \
  "99:\n\t" \
  "s_waitcnt vmcnt(0) lgkmcnt(0)\n\t" \
  ""
#define VO_BODY \
  "s_cmp_eq_u32 %[wv], 1\n\t" \
  "s_cbranch_scc1 91f\n\t" \
  "s_cmp_eq_u32 %[wv], 2\n\t" \
  "s_cbranch_scc1 92f\n\t" \
  "s_cmp_eq_u32 %[wv], 3\n\t" \
  "s_cbranch_scc1 93f\n\t" \
  "90:\n\t" \
  "s_mov_b32 s58, s42\n\t" \
  "20:\n\t" \
  "s_and_b32 s76, s44, 7\n\t" \
  "s_lshl_b32 s76, s76, 15\n\t" \
  "s_add_i32 s40, s54, s76\n\t" \
  "s_and_b32 s76, s44, 3\n\t" \
  "s_lshl_b32 s45, s76, 14\n\t" \
  "s_lshl_b32 s47, s76, 20\n\t" \
  "s_add_i32 s47, s47, s55\n\t" \
  "s_lshr_b32 s76, s47, 21\n\t" \
  "s_bitcmp1_b64 %[sel], s76\n\t" \
  "s_cselect_b32 s76, 0x2000, 0\n\t" \
  "s_add_i32 s47, s47, s76\n\t" \
  "s_add_i32 s49, s44, -1\n\t" \
  "s_and_b32 s48, s49, 2\n\t" \
  "s_lshl_b32 s48, s48, 13\n\t" \
  "s_and_b32 s76, s49, 1\n\t" \
  "s_lshl_b32 s76, s76, 12\n\t" \
  "s_add_i32 s48, s48, s76\n\t" \
  "s_lshl_b32 s50, s53, 8\n\t" \
  "s_mov_b32 s59, 0\n\t" \
  "s_mov_b32 s92, s51\n\t" \
  "s_mov_b32 s93, s53\n\t" \
  "s_xor_b32 s76, s40, 0x400\n\t" \
  "s_xor_b32 s77, s40, 0x800\n\t" \
  "s_xor_b32 s78, s40, 0xc00\n\t" \
  "s_cmp_lg_u32 s58, 0\n\t" \
  "s_cbranch_scc1 11f\n\t" \
  "10:\n\t" \
  "ds_read_b128 v[48:51], v102 offset:0\n\t" \
  "ds_read_b128 v[52:55], v102 offset:1024\n\t" \
  "ds_read_b128 v[56:59], v102 offset:2048\n\t" \
  "ds_read_b128 v[60:63], v102 offset:3072\n\t" \
  "ds_read_b128 v[0:3], v102 offset:16384\n\t" \
  "ds_read_b128 v[4:7], v102 offset:17408\n\t" \
  "ds_read_b128 v[8:11], v102 offset:18432\n\t" \
  "ds_read_b128 v[12:15], v102 offset:19456\n\t" \
  "ds_read_b128 v[16:19], v102 offset:32768\n\t" \
  "ds_read_b128 v[20:23], v102 offset:33792\n\t" \
  "ds_read_b128 v[24:27], v102 offset:34816\n\t" \
  "ds_read_b128 v[28:31], v102 offset:35840\n\t" \
  "s_waitcnt lgkmcnt(4)\n\t" \
  "ds_read_b128 v[80:83], v102 offset:49152\n\t" \
  "ds_read_b128 v[84:87], v102 offset:50176\n\t" \
  "ds_read_b128 v[88:91], v102 offset:51200\n\t" \
  "ds_read_b128 v[92:95], v102 offset:52224\n\t" \
  "s_bitcmp1_b32 s58, 1\n\t" \
  "s_cselect_b64 s[80:81], -1, 0\n\t" \
  "s_cmp_lg_u32 s58, 0\n\t" \
  "s_cbranch_scc1 17f\n\t" \
  "s_mov_b32 exec_hi, 0\n\t" \
  "16:\n\t" \
  "s_memtime vcc\n\t" \
  "s_waitcnt lgkmcnt(0)\n\t" \
  "s_sub_i32 s79, s41, vcc_lo\n\t" \
  "s_cmp_gt_i32 s79, 64\n\t" \
  "s_cbranch_scc1 16b\n\t" \
  "13:\n\t" \
  "buffer_load_dword v116, v112, %[rh], s48 offen sc1\n\t" \
  "s_waitcnt vmcnt(0)\n\t" \
  "15:\n\t" \
  "v_mov_b32 v117, 20000\n\t" \
  "v_cmp_ne_u32_sdwa vcc, v116, s49 src0_sel:BYTE_0 src1_sel:DWORD\n\t" \
  "v_subrev_u32 v116, s50, v116\n\t" \
  "v_ashrrev_i32 v116, 8, v116\n\t" \
  "s_nop 1\n\t" \
  "v_max_i32_dpp v116, v116, v116 quad_perm:[1,0,3,2] row_mask:0xf bank_mask:0xf\n\t" \
  "s_cbranch_vccnz 14f\n\t" \
  "s_nop 0\n\t" \
  "v_max_i32_dpp v116, v116, v116 quad_perm:[2,3,0,1] row_mask:0xf bank_mask:0xf\n\t" \
  "s_nop 1\n\t" \
  "v_max_i32_dpp v116, v116, v116 row_half_mirror row_mask:0xf bank_mask:0xf\n\t" \
  "s_nop 1\n\t" \
  "v_max_i32_dpp v116, v116, v116 row_mirror row_mask:0xf bank_mask:0xf\n\t" \
  "v_med3_i32 v116, v116, 0, v117\n\t" \
  "v_mul_u32_u24 v116, s52, v116\n\t" \
  "v_lshrrev_b32 v116, 16, v116\n\t" \
  "s_mov_b64 exec, -1\n\t" \
  "v_readfirstlane_b32 s84, v116\n\t" \
  "v_readlane_b32 s85, v116, 16\n\t" \
  "s_max_u32 s84, s84, s85\n\t" \
  "s_add_i32 s84, s84, s51\n\t" \
  "1:\n\t" \
  "s_memtime vcc\n\t" \
  "s_waitcnt lgkmcnt(0)\n\t" \
  "s_sub_i32 s79, s84, vcc_lo\n\t" \
  "s_cmp_gt_i32 s79, 128\n\t" \
  "s_cbranch_scc1 1b\n\t" \
  ".p2align 4\n\t" \
  "2:\n\t" \
  "s_sub_i32 s79, s79, 33\n\t" \
  "s_cmp_gt_i32 s79, 56\n\t" \
  "s_cbranch_scc1 2b\n\t" \
  "buffer_load_dwordx4 v[32:35], v100, %[rh], s40 offen sc1\n\t" \
  "s_nop 12\n\t" \
  "buffer_load_dwordx4 v[36:39], v100, %[rh], s76 offen sc1\n\t" \
  "s_nop 12\n\t" \
  "buffer_load_dwordx4 v[40:43], v100, %[rh], s77 offen sc1\n\t" \
  "s_nop 12\n\t" \
  "buffer_load_dwordx4 v[44:47], v100, %[rh], s78 offen sc1\n\t" \
  "s_memtime vcc\n\t" \
  "s_memrealtime s[82:83]\n\t" \
  "s_nop 4\n\t" \
  "s_memrealtime s[84:85]\n\t" \
  "s_nop 4\n\t" \
  "s_memrealtime s[56:57]\n\t" \
  "s_nop 4\n\t" \
  "s_memrealtime s[86:87]\n\t" \
  "s_nop 4\n\t" \
  "s_memrealtime s[88:89]\n\t" \
  "s_nop 4\n\t" \
  "s_memrealtime s[90:91]\n\t" \
  "s_mov_b32 s92, 0\n\t" \
  "s_waitcnt lgkmcnt(0)\n\t" \
  "s_cmp_eq_u32 s82, s84\n\t" \
  "s_addc_u32 s92, s92, 0\n\t" \
  "s_cmp_eq_u32 s82, s56\n\t" \
  "s_addc_u32 s92, s92, 0\n\t" \
  "s_cmp_eq_u32 s82, s86\n\t" \
  "s_addc_u32 s92, s92, 0\n\t" \
  "s_cmp_eq_u32 s82, s88\n\t" \
  "s_addc_u32 s92, s92, 0\n\t" \
  "s_cmp_eq_u32 s82, s90\n\t" \
  "s_addc_u32 s92, s92, 0\n\t" \
  "s_mul_i32 s92, s92, 24\n\t" \
  "s_add_i32 s92, s92, vcc_lo\n\t" \
  "s_lshl4_add_u32 s93, s82, 64\n\t" \
  "3:\n\t" \
  "v_mov_b64 v[72:73], 0\n\t" \
  "v_mov_b64 v[74:75], 0\n\t" \
  "v_mov_b64 v[76:77], 0\n\t" \
  "v_mov_b64 v[78:79], 0\n\t" \
  "s_waitcnt lgkmcnt(0)\n\t" \
  "s_waitcnt vmcnt(3)\n\t" \
  "v_smfmac_f32_16x16x64_bf16 v[72:75], v[32:35], v[48:55], v104\n\t" \
  "ds_write_b128 v101, v[32:35] offset:0\n\t" \
  "v_smfmac_f32_16x16x64_bf16 v[76:79], v[32:35], v[56:63], v104\n\t" \
  "s_waitcnt vmcnt(2)\n\t" \
  "ds_write_b128 v101, v[36:39] offset:1024\n\t" \
  "v_smfmac_f32_16x16x64_bf16 v[72:75], v[36:39], a[0:7], v104\n\t" \
  "v_smfmac_f32_16x16x64_bf16 v[76:79], v[36:39], a[8:15], v104\n\t" \
  "s_nop 3\n\t" \
  "v_cmp_u_f32 vcc, v72, v73\n\t" \
  "v_cmp_u_f32 s[82:83], v74, v75\n\t" \
  "s_waitcnt vmcnt(1)\n\t" \
  "v_smfmac_f32_16x16x64_bf16 v[72:75], v[40:43], a[16:23], v104\n\t" \
  "s_or_b64 vcc, vcc, s[82:83]\n\t" \
  "s_or_b64 vcc, vcc, s[80:81]\n\t" \
  "v_smfmac_f32_16x16x64_bf16 v[76:79], v[40:43], a[24:31], v104\n\t" \
  "s_waitcnt lgkmcnt(0)\n\t" \
  "s_cbranch_scc1 8f\n\t" \
  "5:\n\t" \
  "s_barrier\n\t" \
  "4:\n\t" \
  "s_memtime s[56:57]\n\t" \
  "ds_read_b128 v[48:51], v101 offset:4096\n\t" \
  "ds_read_b128 v[52:55], v101 offset:5120\n\t" \
  "ds_read_b128 v[56:59], v101 offset:9216\n\t" \
  "s_waitcnt vmcnt(0)\n\t" \
  "v_smfmac_f32_16x16x64_bf16 v[72:75], v[44:47], a[32:39], v104\n\t" \
  "ds_read_b128 v[60:63], v101 offset:8192\n\t" \
  "ds_read_b128 v[64:67], v101 offset:13312\n\t" \
  "v_smfmac_f32_16x16x64_bf16 v[76:79], v[44:47], a[40:47], v104\n\t" \
  "ds_read_b128 v[68:71], v101 offset:12288\n\t" \
  "ds_write_b128 v101, v[40:43] offset:2048\n\t" \
  "s_nop 1\n\t" \
  "v_cmp_u_f32 vcc, v72, v73\n\t" \
  "v_cmp_u_f32 s[82:83], v74, v75\n\t" \
  "s_waitcnt lgkmcnt(6)\n\t" \
  "v_smfmac_f32_16x16x64_bf16 v[72:75], v[48:51], v[0:7], v104\n\t" \
  "s_or_b64 vcc, vcc, s[82:83]\n\t" \
  "v_smfmac_f32_16x16x64_bf16 v[76:79], v[48:51], v[8:15], v104\n\t" \
  "s_cbranch_scc1 9f\n\t" \
  "6:\n\t" \
  "s_waitcnt lgkmcnt(5)\n\t" \
  "v_smfmac_f32_16x16x64_bf16 v[72:75], v[52:55], a[48:55], v104\n\t" \
  "ds_write_b128 v101, v[44:47] offset:3072\n\t" \
  "v_smfmac_f32_16x16x64_bf16 v[76:79], v[52:55], a[56:63], v104\n\t" \
  "v_mov_b64 v[40:41], 0\n\t" \
  "s_waitcnt lgkmcnt(5)\n\t" \
  "v_smfmac_f32_16x16x64_bf16 v[72:75], v[56:59], a[96:103], v104\n\t" \
  "v_mov_b64 v[42:43], 0\n\t" \
  "v_smfmac_f32_16x16x64_bf16 v[76:79], v[56:59], a[104:111], v104\n\t" \
  "s_waitcnt lgkmcnt(4)\n\t" \
  "v_smfmac_f32_16x16x64_bf16 v[72:75], v[60:63], v[16:23], v104\n\t" \
  "s_waitcnt lgkmcnt(0)\n\t" \
  "s_barrier\n\t" \
  "v_smfmac_f32_16x16x64_bf16 v[76:79], v[60:63], v[24:31], v104\n\t" \
  "ds_read_b128 v[48:51], v101 offset:6144\n\t" \
  "ds_read_b128 v[52:55], v101 offset:10240\n\t" \
  "v_smfmac_f32_16x16x64_bf16 v[72:75], v[64:67], a[144:151], v104\n\t" \
  "ds_read_b128 v[56:59], v101 offset:14336\n\t" \
  "ds_read_b128 v[60:63], v101 offset:7168\n\t" \
  "v_smfmac_f32_16x16x64_bf16 v[76:79], v[64:67], a[152:159], v104\n\t" \
  "ds_read_b128 v[32:35], v101 offset:11264\n\t" \
  "ds_read_b128 v[36:39], v101 offset:15360\n\t" \
  "v_smfmac_f32_16x16x64_bf16 v[72:75], v[68:71], v[80:87], v104\n\t" \
  "ds_read_b128 v[0:3], v103 offset:0\n\t" \
  "v_mov_b64 v[44:45], 0\n\t" \
  "v_smfmac_f32_16x16x64_bf16 v[76:79], v[68:71], v[88:95], v104\n\t" \
  "v_mov_b64 v[46:47], 0\n\t" \
  "s_waitcnt lgkmcnt(5)\n\t" \
  "v_smfmac_f32_16x16x64_bf16 v[72:75], v[48:51], a[64:71], v104\n\t" \
  "ds_read_b128 v[4:7], v103 offset:1024\n\t" \
  "v_mov_b64 v[80:81], 0\n\t" \
  "v_smfmac_f32_16x16x64_bf16 v[72:75], v[52:55], a[112:119], v104\n\t" \
  "v_mov_b64 v[82:83], 0\n\t" \
  "s_waitcnt lgkmcnt(2)\n\t" \
  "v_smfmac_f32_16x16x64_bf16 v[72:75], v[56:59], a[160:167], v104\n\t" \
  "ds_read_b128 v[8:11], v103 offset:2048\n\t" \
  "v_mov_b64 v[84:85], 0\n\t" \
  "v_smfmac_f32_16x16x64_bf16 v[72:75], v[60:63], a[80:87], v104\n\t" \
  "ds_read_b128 v[12:15], v103 offset:3072\n\t" \
  "v_mov_b64 v[86:87], 0\n\t" \
  "v_smfmac_f32_16x16x64_bf16 v[72:75], v[32:35], a[128:135], v104\n\t" \
  "ds_read_b128 v[16:19], v103 offset:16384\n\t" \
  "v_mov_b64 v[88:89], 0\n\t" \
  "v_smfmac_f32_16x16x64_bf16 v[72:75], v[36:39], a[176:183], v104\n\t" \
  "ds_read_b128 v[20:23], v103 offset:17408\n\t" \
  "v_mov_b64 v[90:91], 0\n\t" \
  "v_smfmac_f32_16x16x64_bf16 v[76:79], v[48:51], a[72:79], v104\n\t" \
  "ds_read_b128 v[24:27], v103 offset:18432\n\t" \
  "v_mov_b64 v[92:93], 0\n\t" \
  "v_smfmac_f32_16x16x64_bf16 v[76:79], v[52:55], a[120:127], v104\n\t" \
  "v_add_f32 v96, v72, v74\n\t" \
  "v_add_f32 v97, v73, v75\n\t" \
  "v_cvt_pk_bf16_f32 v96, v96, v97\n\t" \
  "ds_write_b32 v105, v96\n\t" \
  "v_smfmac_f32_16x16x64_bf16 v[76:79], v[56:59], a[168:175], v104\n\t" \
  "ds_read_b128 v[28:31], v103 offset:19456\n\t" \
  "v_mov_b64 v[94:95], 0\n\t" \
  "v_smfmac_f32_16x16x64_bf16 v[76:79], v[60:63], a[88:95], v104\n\t" \
  "v_smfmac_f32_16x16x64_bf16 v[76:79], v[32:35], a[136:143], v104\n\t" \
  "v_smfmac_f32_16x16x64_bf16 v[76:79], v[36:39], a[184:191], v104\n\t" \
  "s_waitcnt lgkmcnt(1)\n\t" \
  "s_barrier\n\t" \
  "ds_read_b64_tr_b16 v[96:97], v107 offset:0\n\t" \
  "ds_read_b64_tr_b16 v[98:99], v107 offset:64\n\t" \
  "ds_read_b64_tr_b16 v[56:57], v107 offset:1024\n\t" \
  "ds_read_b64_tr_b16 v[58:59], v107 offset:1088\n\t" \
  "s_nop 1\n\t" \
  "v_add_f32 v116, v76, v78\n\t" \
  "v_add_f32 v117, v77, v79\n\t" \
  "v_cvt_pk_bf16_f32 v116, v116, v117\n\t" \
  "ds_write_b32 v105, v116 offset:256\n\t" \
  "s_waitcnt lgkmcnt(3)\n\t" \
  "v_smfmac_f32_16x16x64_bf16 v[40:43], v[96:99], a[192:199], v106\n\t" \
  "v_smfmac_f32_16x16x64_bf16 v[44:47], v[96:99], a[224:231], v106\n\t" \
  "s_waitcnt lgkmcnt(0)\n\t" \
  "s_barrier\n\t" \
  "ds_read_b64_tr_b16 v[52:53], v107 offset:256\n\t" \
  "ds_read_b64_tr_b16 v[54:55], v107 offset:320\n\t" \
  "v_smfmac_f32_16x16x64_bf16 v[40:43], v[56:59], a[208:215], v106\n\t" \
  "ds_read_b64_tr_b16 v[60:61], v107 offset:1280\n\t" \
  "ds_read_b64_tr_b16 v[62:63], v107 offset:1344\n\t" \
  "v_smfmac_f32_16x16x64_bf16 v[44:47], v[56:59], v[0:7], v106\n\t" \
  "v_smfmac_f32_16x16x64_bf16 v[80:83], v[96:99], a[240:247], v106\n\t" \
  "v_mov_b64 v[32:33], 0\n\t" \
  "v_smfmac_f32_16x16x64_bf16 v[84:87], v[96:99], v[136:143], v106\n\t" \
  "v_mov_b64 v[34:35], 0\n\t" \
  "v_smfmac_f32_16x16x64_bf16 v[80:83], v[56:59], v[120:127], v106\n\t" \
  "v_mov_b64 v[36:37], 0\n\t" \
  "v_smfmac_f32_16x16x64_bf16 v[84:87], v[56:59], v[16:23], v106\n\t" \
  "s_waitcnt lgkmcnt(2)\n\t" \
  "v_smfmac_f32_16x16x64_bf16 v[40:43], v[52:55], a[200:207], v106\n\t" \
  "v_smfmac_f32_16x16x64_bf16 v[44:47], v[52:55], a[232:239], v106\n\t" \
  "s_xor_b32 s76, s47, 0x40000\n\t" \
  "s_waitcnt lgkmcnt(0)\n\t" \
  "v_smfmac_f32_16x16x64_bf16 v[40:43], v[60:63], a[216:223], v106\n\t" \
  "v_mov_b64 v[38:39], 0\n\t" \
  "s_xor_b32 s77, s47, 0x80000\n\t" \
  "v_smfmac_f32_16x16x64_bf16 v[44:47], v[60:63], v[8:15], v106\n\t" \
  "s_xor_b32 s78, s47, 0xc0000\n\t" \
  "v_smfmac_f32_16x16x64_bf16 v[80:83], v[52:55], a[248:255], v106\n\t" \
  "ds_read_b128 v[0:3], v103 offset:32768\n\t" \
  "v_cvt_pk_bf16_f32 v64, v40, v41\n\t" \
  "v_smfmac_f32_16x16x64_bf16 v[84:87], v[52:55], v[144:151], v106\n\t" \
  "ds_read_b128 v[4:7], v103 offset:33792\n\t" \
  "v_cvt_pk_bf16_f32 v65, v42, v43\n\t" \
  "v_smfmac_f32_16x16x64_bf16 v[80:83], v[60:63], v[128:135], v106\n\t" \
  "ds_read_b128 v[8:11], v103 offset:34816\n\t" \
  "v_cvt_pk_bf16_f32 v66, v44, v45\n\t" \
  "v_smfmac_f32_16x16x64_bf16 v[84:87], v[60:63], v[24:31], v106\n\t" \
  "ds_read_b128 v[12:15], v103 offset:35840\n\t" \
  "v_cvt_pk_bf16_f32 v67, v46, v47\n\t" \
  "v_smfmac_f32_16x16x64_bf16 v[88:91], v[96:99], v[152:159], v106\n\t" \
  "v_permlane16_swap_b32 v64, v66\n\t" \
  "v_cvt_pk_bf16_f32 v68, v80, v81\n\t" \
  "v_smfmac_f32_16x16x64_bf16 v[92:95], v[96:99], v[184:191], v106\n\t" \
  "v_permlane16_swap_b32 v65, v67\n\t" \
  "buffer_store_dwordx4 v[64:67], v108, %[ru], s47 offen sc1\n\t" \
  "v_cvt_pk_bf16_f32 v69, v82, v83\n\t" \
  "v_smfmac_f32_16x16x64_bf16 v[88:91], v[52:55], v[160:167], v106\n\t" \
  "v_cvt_pk_bf16_f32 v70, v84, v85\n\t" \
  "v_smfmac_f32_16x16x64_bf16 v[92:95], v[52:55], v[192:199], v106\n\t" \
  "v_cvt_pk_bf16_f32 v71, v86, v87\n\t" \
  "v_smfmac_f32_16x16x64_bf16 v[88:91], v[56:59], v[168:175], v106\n\t" \
  "ds_read_b128 v[16:19], v103 offset:49152\n\t" \
  "v_permlane16_swap_b32 v68, v70\n\t" \
  "s_waitcnt lgkmcnt(3)\n\t" \
  "v_smfmac_f32_16x16x64_bf16 v[92:95], v[56:59], v[0:7], v106\n\t" \
  "ds_read_b128 v[20:23], v103 offset:50176\n\t" \
  "v_permlane16_swap_b32 v69, v71\n\t" \
  "buffer_store_dwordx4 v[68:71], v108, %[ru], s76 offen sc1\n\t" \
  "v_smfmac_f32_16x16x64_bf16 v[88:91], v[60:63], v[176:183], v106\n\t" \
  "ds_read_b128 v[24:27], v103 offset:51200\n\t" \
  "s_waitcnt lgkmcnt(3)\n\t" \
  "v_smfmac_f32_16x16x64_bf16 v[92:95], v[60:63], v[8:15], v106\n\t" \
  "ds_read_b128 v[28:31], v103 offset:52224\n\t" \
  "v_smfmac_f32_16x16x64_bf16 v[32:35], v[96:99], v[200:207], v106\n\t" \
  "v_cvt_pk_bf16_f32 v72, v88, v89\n\t" \
  "v_smfmac_f32_16x16x64_bf16 v[36:39], v[96:99], v[232:239], v106\n\t" \
  "v_cvt_pk_bf16_f32 v73, v90, v91\n\t" \
  "v_smfmac_f32_16x16x64_bf16 v[32:35], v[52:55], v[208:215], v106\n\t" \
  "v_cvt_pk_bf16_f32 v74, v92, v93\n\t" \
  "v_smfmac_f32_16x16x64_bf16 v[36:39], v[52:55], v[240:247], v106\n\t" \
  "v_cvt_pk_bf16_f32 v75, v94, v95\n\t" \
  "v_smfmac_f32_16x16x64_bf16 v[32:35], v[56:59], v[216:223], v106\n\t" \
  "v_permlane16_swap_b32 v72, v74\n\t" \
  "v_smfmac_f32_16x16x64_bf16 v[32:35], v[60:63], v[224:231], v106\n\t" \
  "v_permlane16_swap_b32 v73, v75\n\t" \
  "buffer_store_dwordx4 v[72:75], v108, %[ru], s77 offen sc1\n\t" \
  "s_waitcnt lgkmcnt(2)\n\t" \
  "v_smfmac_f32_16x16x64_bf16 v[36:39], v[56:59], v[16:23], v106\n\t" \
  "s_waitcnt lgkmcnt(0)\n\t" \
  "v_smfmac_f32_16x16x64_bf16 v[36:39], v[60:63], v[24:31], v106\n\t" \
  "v_cvt_pk_bf16_f32 v76, v32, v33\n\t" \
  "v_cvt_pk_bf16_f32 v77, v34, v35\n\t" \
  "s_nop 5\n\t" \
  "v_cvt_pk_bf16_f32 v78, v36, v37\n\t" \
  "v_cvt_pk_bf16_f32 v79, v38, v39\n\t" \
  "s_nop 0\n\t" \
  "v_permlane16_swap_b32 v76, v78\n\t" \
  "v_permlane16_swap_b32 v77, v79\n\t" \
  "buffer_store_dwordx4 v[76:79], v108, %[ru], s78 offen sc1\n\t" \
  "s_memtime s[76:77]\n\t" \
  "s_mov_b32 s42, 0\n\t" \
  "s_waitcnt lgkmcnt(0)\n\t" \
  "s_mov_b32 s57, s76\n\t" \
  "s_add_i32 s41, s56, 1950\n\t" \
  "s_sub_i32 s76, s92, s51\n\t" \
  "s_sub_i32 s77, s93, s53\n\t" \
  "s_cmp_eq_u32 s76, 0\n\t" \
  "s_cbranch_scc1 21f\n\t" \
  "v_cvt_f32_u32 v117, s76\n\t" \
  "v_cvt_f32_u32 v116, s77\n\t" \
  "v_rcp_f32 v117, v117\n\t" \
  "s_mov_b32 s51, s92\n\t" \
  "v_mul_f32 v117, v117, v116\n\t" \
  "v_max_f32 v117, 0x3f028f5c, v117\n\t" \
  "v_min_f32 v117, 0x3ffeb852, v117\n\t" \
  "v_rcp_f32 v116, v117\n\t" \
  "v_mul_f32 v117, 0x4f000000, v117\n\t" \
  "v_mul_f32 v116, 0x47800000, v116\n\t" \
  "v_cvt_u32_f32 v117, v117\n\t" \
  "v_cvt_u32_f32 v116, v116\n\t" \
  "s_mov_b32 s53, s93\n\t" \
  "v_readfirstlane_b32 s46, v117\n\t" \
  "v_readfirstlane_b32 s52, v116\n\t" \
  "21:\n\t" \
  "s_add_i32 s44, s44, 1\n\t" \
  "s_and_b32 s58, s58, 4\n\t" \
  "s_and_b32 s76, s44, 64\n\t" \
  "s_or_b32 s42, s42, s76\n\t" \
  "s_cbranch_scc0 20b\n\t" \
  "s_branch 99f\n\t" \
  "14:\n\t" \
  "s_sleep 3\n\t" \
  "s_add_u32 s59, s59, 0x10000\n\t" \
  "s_cmp_lt_u32 s59, 0x100000\n\t" \
  "s_cbranch_scc1 13b\n\t" \
  "s_mov_b64 exec, -1\n\t" \
  "s_mov_b32 s42, 16\n\t" \
  "s_branch 99f\n\t" \
  "8:\n\t" \
  "s_bitcmp1_b32 s58, 1\n\t" \
  "s_cbranch_scc1 4b\n\t" \
  "s_bitcmp1_b32 s58, 2\n\t" \
  "s_cbranch_scc1 5b\n\t" \
  "v_max3_u32 v116, v32, v33, v34\n\t" \
  "v_max3_u32 v117, v35, v36, v37\n\t" \
  "v_max3_u32 v116, v116, v38, v39\n\t" \
  "v_max_u32 v116, v116, v117\n\t" \
  "v_cmp_eq_u32 vcc, -1, v116\n\t" \
  "s_cbranch_vccz 5b\n\t" \
  "s_branch 0f\n\t" \
  "9:\n\t" \
  "s_bitcmp1_b32 s58, 2\n\t" \
  "s_cbranch_scc1 6b\n\t" \
  "v_max3_u32 v116, v40, v41, v42\n\t" \
  "v_max3_u32 v117, v43, v44, v45\n\t" \
  "v_max3_u32 v116, v116, v46, v47\n\t" \
  "v_max_u32 v116, v116, v117\n\t" \
  "v_cmp_eq_u32 vcc, -1, v116\n\t" \
  "s_cbranch_vccz 6b\n\t" \
  "s_or_b32 s58, s58, 2\n\t" \
  "0:\n\t" \
  "s_or_b32 s58, s58, 1\n\t" \
  "11:\n\t" \
  "buffer_load_dwordx4 v[32:35], v100, %[rh], s40 offen sc1\n\t" \
  "buffer_load_dwordx4 v[36:39], v100, %[rh], s76 offen sc1\n\t" \
  "buffer_load_dwordx4 v[40:43], v100, %[rh], s77 offen sc1\n\t" \
  "buffer_load_dwordx4 v[44:47], v100, %[rh], s78 offen sc1\n\t" \
  "s_waitcnt lgkmcnt(0)\n\t" \
  "s_branch 10b\n\t" \
  "17:\n\t" \
  "s_add_u32 s59, s59, 1\n\t" \
  "s_bitcmp1_b32 s58, 2\n\t" \
  "s_cbranch_scc1 3b\n\t" \
  "s_waitcnt vmcnt(0)\n\t" \
  "v_max_u32 v116, v32, v33\n\t" \
  "v_max3_u32 v116, v116, v34, v35\n\t" \
  "v_max3_u32 v116, v116, v36, v37\n\t" \
  "v_max3_u32 v116, v116, v38, v39\n\t" \
  "v_max3_u32 v116, v116, v40, v41\n\t" \
  "v_max3_u32 v116, v116, v42, v43\n\t" \
  "v_max3_u32 v116, v116, v44, v45\n\t" \
  "v_max3_u32 v116, v116, v46, v47\n\t" \
  "v_cmp_eq_u32 vcc, -1, v116\n\t" \
  "s_cbranch_vccz 3b\n\t" \
  "s_bitcmp1_b32 s59, 6\n\t" \
  "s_cbranch_scc1 12f\n\t" \
  "buffer_load_dwordx4 v[32:35], v100, %[rh], s40 offen sc1\n\t" \
  "buffer_load_dwordx4 v[36:39], v100, %[rh], s76 offen sc1\n\t" \
  "buffer_load_dwordx4 v[40:43], v100, %[rh], s77 offen sc1\n\t" \
  "buffer_load_dwordx4 v[44:47], v100, %[rh], s78 offen sc1\n\t" \
  "s_branch 17b\n\t" \
  "12:\n\t" \
  "s_mov_b32 s42, s58\n\t" \
  "s_branch 99f\n\t" \
  "91:\n\t" \
  "s_mov_b32 s58, s42\n\t" \
  "20:\n\t" \
  "s_and_b32 s76, s44, 7\n\t" \
  "s_lshl_b32 s76, s76, 15\n\t" \
  "s_add_i32 s40, s54, s76\n\t" \
  "s_and_b32 s76, s44, 3\n\t" \
  "s_lshl_b32 s45, s76, 14\n\t" \
  "s_lshl_b32 s47, s76, 20\n\t" \
  "s_add_i32 s47, s47, s55\n\t" \
  "s_lshr_b32 s76, s47, 21\n\t" \
  "s_bitcmp1_b64 %[sel], s76\n\t" \
  "s_cselect_b32 s76, 0x2000, 0\n\t" \
  "s_add_i32 s47, s47, s76\n\t" \
  "s_add_i32 s49, s44, -1\n\t" \
  "s_and_b32 s48, s49, 2\n\t" \
  "s_lshl_b32 s48, s48, 13\n\t" \
  "s_and_b32 s76, s49, 1\n\t" \
  "s_lshl_b32 s76, s76, 12\n\t" \
  "s_add_i32 s48, s48, s76\n\t" \
  "s_lshl_b32 s50, s53, 8\n\t" \
  "s_mov_b32 s59, 0\n\t" \
  "s_mov_b32 s92, s51\n\t" \
  "s_mov_b32 s93, s53\n\t" \
  "s_xor_b32 s76, s40, 0x400\n\t" \
  "s_xor_b32 s77, s40, 0x800\n\t" \
  "s_xor_b32 s78, s40, 0xc00\n\t" \
  "s_cmp_lg_u32 s58, 0\n\t" \
  "s_cbranch_scc1 11f\n\t" \
  "10:\n\t" \
  "ds_read_b128 v[48:51], v102 offset:16384\n\t" \
  "ds_read_b128 v[52:55], v102 offset:17408\n\t" \
  "ds_read_b128 v[56:59], v102 offset:18432\n\t" \
  "ds_read_b128 v[60:63], v102 offset:19456\n\t" \
  "ds_read_b128 v[0:3], v102 offset:32768\n\t" \
  "ds_read_b128 v[4:7], v102 offset:33792\n\t" \
  "ds_read_b128 v[8:11], v102 offset:34816\n\t" \
  "ds_read_b128 v[12:15], v102 offset:35840\n\t" \
  "ds_read_b128 v[16:19], v102 offset:49152\n\t" \
  "ds_read_b128 v[20:23], v102 offset:50176\n\t" \
  "ds_read_b128 v[24:27], v102 offset:51200\n\t" \
  "ds_read_b128 v[28:31], v102 offset:52224\n\t" \
  "s_waitcnt lgkmcnt(4)\n\t" \
  "ds_read_b128 v[80:83], v102 offset:0\n\t" \
  "ds_read_b128 v[84:87], v102 offset:1024\n\t" \
  "ds_read_b128 v[88:91], v102 offset:2048\n\t" \
  "ds_read_b128 v[92:95], v102 offset:3072\n\t" \
  "s_bitcmp1_b32 s58, 1\n\t" \
  "s_cselect_b64 s[80:81], -1, 0\n\t" \
  "s_cmp_lg_u32 s58, 0\n\t" \
  "s_cbranch_scc1 17f\n\t" \
  "s_mov_b32 exec_hi, 0\n\t" \
  "16:\n\t" \
  "s_memtime vcc\n\t" \
  "s_waitcnt lgkmcnt(0)\n\t" \
  "s_sub_i32 s79, s41, vcc_lo\n\t" \
  "s_cmp_gt_i32 s79, 64\n\t" \
  "s_cbranch_scc1 16b\n\t" \
  "13:\n\t" \
  "buffer_load_dword v116, v112, %[rh], s48 offen sc1\n\t" \
  "s_waitcnt vmcnt(0)\n\t" \
  "15:\n\t" \
  "v_mov_b32 v117, 20000\n\t" \
  "v_cmp_ne_u32_sdwa vcc, v116, s49 src0_sel:BYTE_0 src1_sel:DWORD\n\t" \
  "v_subrev_u32 v116, s50, v116\n\t" \
  "v_ashrrev_i32 v116, 8, v116\n\t" \
  "s_nop 1\n\t" \
  "v_max_i32_dpp v116, v116, v116 quad_perm:[1,0,3,2] row_mask:0xf bank_mask:0xf\n\t" \
  "s_cbranch_vccnz 14f\n\t" \
  "s_nop 0\n\t" \
  "v_max_i32_dpp v116, v116, v116 quad_perm:[2,3,0,1] row_mask:0xf bank_mask:0xf\n\t" \
  "s_nop 1\n\t" \
  "v_max_i32_dpp v116, v116, v116 row_half_mirror row_mask:0xf bank_mask:0xf\n\t" \
  "s_nop 1\n\t" \
  "v_max_i32_dpp v116, v116, v116 row_mirror row_mask:0xf bank_mask:0xf\n\t" \
  "v_med3_i32 v116, v116, 0, v117\n\t" \
  "v_mul_u32_u24 v116, s52, v116\n\t" \
  "v_lshrrev_b32 v116, 16, v116\n\t" \
  "s_mov_b64 exec, -1\n\t" \
  "v_readfirstlane_b32 s84, v116\n\t" \
  "v_readlane_b32 s85, v116, 16\n\t" \
  "s_max_u32 s84, s84, s85\n\t" \
  "s_add_i32 s84, s84, s51\n\t" \
  "1:\n\t" \
  "s_memtime vcc\n\t" \
  "s_waitcnt lgkmcnt(0)\n\t" \
  "s_sub_i32 s79, s84, vcc_lo\n\t" \
  "s_cmp_gt_i32 s79, 128\n\t" \
  "s_cbranch_scc1 1b\n\t" \
  ".p2align 4\n\t" \
  "2:\n\t" \
  "s_sub_i32 s79, s79, 33\n\t" \
  "s_cmp_gt_i32 s79, 56\n\t" \
  "s_cbranch_scc1 2b\n\t" \
  "buffer_load_dwordx4 v[32:35], v100, %[rh], s40 offen sc1\n\t" \
  "s_nop 12\n\t" \
  "buffer_load_dwordx4 v[36:39], v100, %[rh], s76 offen sc1\n\t" \
  "s_nop 12\n\t" \
  "buffer_load_dwordx4 v[40:43], v100, %[rh], s77 offen sc1\n\t" \
  "s_nop 12\n\t" \
  "buffer_load_dwordx4 v[44:47], v100, %[rh], s78 offen sc1\n\t" \
  "s_memtime vcc\n\t" \
  "s_memrealtime s[82:83]\n\t" \
  "s_nop 4\n\t" \
  "s_memrealtime s[84:85]\n\t" \
  "s_nop 4\n\t" \
  "s_memrealtime s[56:57]\n\t" \
  "s_nop 4\n\t" \
  "s_memrealtime s[86:87]\n\t" \
  "s_nop 4\n\t" \
  "s_memrealtime s[88:89]\n\t" \
  "s_nop 4\n\t" \
  "s_memrealtime s[90:91]\n\t" \
  "s_mov_b32 s92, 0\n\t" \
  "s_waitcnt lgkmcnt(0)\n\t" \
  "s_cmp_eq_u32 s82, s84\n\t" \
  "s_addc_u32 s92, s92, 0\n\t" \
  "s_cmp_eq_u32 s82, s56\n\t" \
  "s_addc_u32 s92, s92, 0\n\t" \
  "s_cmp_eq_u32 s82, s86\n\t" \
  "s_addc_u32 s92, s92, 0\n\t" \
  "s_cmp_eq_u32 s82, s88\n\t" \
  "s_addc_u32 s92, s92, 0\n\t" \
  "s_cmp_eq_u32 s82, s90\n\t" \
  "s_addc_u32 s92, s92, 0\n\t" \
  "s_mul_i32 s92, s92, 24\n\t" \
  "s_add_i32 s92, s92, vcc_lo\n\t" \
  "s_lshl4_add_u32 s93, s82, 64\n\t" \
  "3:\n\t" \
  "v_mov_b64 v[72:73], 0\n\t" \
  "v_mov_b64 v[74:75], 0\n\t" \
  "v_mov_b64 v[76:77], 0\n\t" \
  "v_mov_b64 v[78:79], 0\n\t" \
  "s_waitcnt lgkmcnt(0)\n\t" \
  "s_waitcnt vmcnt(3)\n\t" \
  "v_smfmac_f32_16x16x64_bf16 v[72:75], v[32:35], v[48:55], v104\n\t" \
  "ds_write_b128 v101, v[32:35] offset:4096\n\t" \
  "v_smfmac_f32_16x16x64_bf16 v[76:79], v[32:35], v[56:63], v104\n\t" \
  "s_waitcnt vmcnt(2)\n\t" \
  "ds_write_b128 v101, v[36:39] offset:5120\n\t" \
  "v_smfmac_f32_16x16x64_bf16 v[72:75], v[36:39], a[48:55], v104\n\t" \
  "v_smfmac_f32_16x16x64_bf16 v[76:79], v[36:39], a[56:63], v104\n\t" \
  "s_nop 3\n\t" \
  "v_cmp_u_f32 vcc, v72, v73\n\t" \
  "v_cmp_u_f32 s[82:83], v74, v75\n\t" \
  "s_waitcnt vmcnt(1)\n\t" \
  "v_smfmac_f32_16x16x64_bf16 v[72:75], v[40:43], a[64:71], v104\n\t" \
  "s_or_b64 vcc, vcc, s[82:83]\n\t" \
  "s_or_b64 vcc, vcc, s[80:81]\n\t" \
  "v_smfmac_f32_16x16x64_bf16 v[76:79], v[40:43], a[72:79], v104\n\t" \
  "s_waitcnt lgkmcnt(0)\n\t" \
  "s_cbranch_scc1 8f\n\t" \
  "5:\n\t" \
  "s_barrier\n\t" \
  "4:\n\t" \
  "s_memtime s[56:57]\n\t" \
  "ds_read_b128 v[48:51], v101 offset:8192\n\t" \
  "ds_read_b128 v[52:55], v101 offset:9216\n\t" \
  "ds_read_b128 v[56:59], v101 offset:13312\n\t" \
  "s_waitcnt vmcnt(0)\n\t" \
  "v_smfmac_f32_16x16x64_bf16 v[72:75], v[44:47], a[80:87], v104\n\t" \
  "ds_read_b128 v[60:63], v101 offset:12288\n\t" \
  "ds_read_b128 v[64:67], v101 offset:1024\n\t" \
  "v_smfmac_f32_16x16x64_bf16 v[76:79], v[44:47], a[88:95], v104\n\t" \
  "ds_read_b128 v[68:71], v101 offset:0\n\t" \
  "ds_write_b128 v101, v[40:43] offset:6144\n\t" \
  "s_nop 1\n\t" \
  "v_cmp_u_f32 vcc, v72, v73\n\t" \
  "v_cmp_u_f32 s[82:83], v74, v75\n\t" \
  "s_waitcnt lgkmcnt(6)\n\t" \
  "v_smfmac_f32_16x16x64_bf16 v[72:75], v[48:51], v[0:7], v104\n\t" \
  "s_or_b64 vcc, vcc, s[82:83]\n\t" \
  "v_smfmac_f32_16x16x64_bf16 v[76:79], v[48:51], v[8:15], v104\n\t" \
  "s_cbranch_scc1 9f\n\t" \
  "6:\n\t" \
  "s_waitcnt lgkmcnt(5)\n\t" \
  "v_smfmac_f32_16x16x64_bf16 v[72:75], v[52:55], a[96:103], v104\n\t" \
  "ds_write_b128 v101, v[44:47] offset:7168\n\t" \
  "v_smfmac_f32_16x16x64_bf16 v[76:79], v[52:55], a[104:111], v104\n\t" \
  "v_mov_b64 v[40:41], 0\n\t" \
  "s_waitcnt lgkmcnt(5)\n\t" \
  "v_smfmac_f32_16x16x64_bf16 v[72:75], v[56:59], a[144:151], v104\n\t" \
  "v_mov_b64 v[42:43], 0\n\t" \
  "v_smfmac_f32_16x16x64_bf16 v[76:79], v[56:59], a[152:159], v104\n\t" \
  "s_waitcnt lgkmcnt(4)\n\t" \
  "v_smfmac_f32_16x16x64_bf16 v[72:75], v[60:63], v[16:23], v104\n\t" \
  "s_waitcnt lgkmcnt(0)\n\t" \
  "s_barrier\n\t" \
  "v_smfmac_f32_16x16x64_bf16 v[76:79], v[60:63], v[24:31], v104\n\t" \
  "ds_read_b128 v[48:51], v101 offset:10240\n\t" \
  "ds_read_b128 v[52:55], v101 offset:14336\n\t" \
  "v_smfmac_f32_16x16x64_bf16 v[72:75], v[64:67], a[0:7], v104\n\t" \
  "ds_read_b128 v[56:59], v101 offset:2048\n\t" \
  "ds_read_b128 v[60:63], v101 offset:11264\n\t" \
  "v_smfmac_f32_16x16x64_bf16 v[76:79], v[64:67], a[8:15], v104\n\t" \
  "ds_read_b128 v[32:35], v101 offset:15360\n\t" \
  "ds_read_b128 v[36:39], v101 offset:3072\n\t" \
  "v_smfmac_f32_16x16x64_bf16 v[72:75], v[68:71], v[80:87], v104\n\t" \
  "ds_read_b128 v[0:3], v103 offset:0\n\t" \
  "v_mov_b64 v[44:45], 0\n\t" \
  "v_smfmac_f32_16x16x64_bf16 v[76:79], v[68:71], v[88:95], v104\n\t" \
  "v_mov_b64 v[46:47], 0\n\t" \
  "s_waitcnt lgkmcnt(5)\n\t" \
  "v_smfmac_f32_16x16x64_bf16 v[72:75], v[48:51], a[112:119], v104\n\t" \
  "ds_read_b128 v[4:7], v103 offset:1024\n\t" \
  "v_mov_b64 v[80:81], 0\n\t" \
  "v_smfmac_f32_16x16x64_bf16 v[72:75], v[52:55], a[160:167], v104\n\t" \
  "v_mov_b64 v[82:83], 0\n\t" \
  "s_waitcnt lgkmcnt(2)\n\t" \
  "v_smfmac_f32_16x16x64_bf16 v[72:75], v[56:59], a[16:23], v104\n\t" \
  "ds_read_b128 v[8:11], v103 offset:2048\n\t" \
  "v_mov_b64 v[84:85], 0\n\t" \
  "v_smfmac_f32_16x16x64_bf16 v[72:75], v[60:63], a[128:135], v104\n\t" \
  "ds_read_b128 v[12:15], v103 offset:3072\n\t" \
  "v_mov_b64 v[86:87], 0\n\t" \
  "v_smfmac_f32_16x16x64_bf16 v[72:75], v[32:35], a[176:183], v104\n\t" \
  "ds_read_b128 v[16:19], v103 offset:16384\n\t" \
  "v_mov_b64 v[88:89], 0\n\t" \
  "v_smfmac_f32_16x16x64_bf16 v[72:75], v[36:39], a[32:39], v104\n\t" \
  "ds_read_b128 v[20:23], v103 offset:17408\n\t" \
  "v_mov_b64 v[90:91], 0\n\t" \
  "v_smfmac_f32_16x16x64_bf16 v[76:79], v[48:51], a[120:127], v104\n\t" \
  "ds_read_b128 v[24:27], v103 offset:18432\n\t" \
  "v_mov_b64 v[92:93], 0\n\t" \
  "v_smfmac_f32_16x16x64_bf16 v[76:79], v[52:55], a[168:175], v104\n\t" \
  "v_add_f32 v96, v72, v74\n\t" \
  "v_add_f32 v97, v73, v75\n\t" \
  "v_cvt_pk_bf16_f32 v96, v96, v97\n\t" \
  "ds_write_b32 v105, v96\n\t" \
  "v_smfmac_f32_16x16x64_bf16 v[76:79], v[56:59], a[24:31], v104\n\t" \
  "ds_read_b128 v[28:31], v103 offset:19456\n\t" \
  "v_mov_b64 v[94:95], 0\n\t" \
  "v_smfmac_f32_16x16x64_bf16 v[76:79], v[60:63], a[136:143], v104\n\t" \
  "v_smfmac_f32_16x16x64_bf16 v[76:79], v[32:35], a[184:191], v104\n\t" \
  "v_smfmac_f32_16x16x64_bf16 v[76:79], v[36:39], a[40:47], v104\n\t" \
  "s_waitcnt lgkmcnt(1)\n\t" \
  "s_barrier\n\t" \
  "ds_read_b64_tr_b16 v[96:97], v107 offset:0\n\t" \
  "ds_read_b64_tr_b16 v[98:99], v107 offset:64\n\t" \
  "ds_read_b64_tr_b16 v[56:57], v107 offset:1024\n\t" \
  "ds_read_b64_tr_b16 v[58:59], v107 offset:1088\n\t" \
  "s_nop 1\n\t" \
  "v_add_f32 v116, v76, v78\n\t" \
  "v_add_f32 v117, v77, v79\n\t" \
  "v_cvt_pk_bf16_f32 v116, v116, v117\n\t" \
  "ds_write_b32 v105, v116 offset:256\n\t" \
  "s_waitcnt lgkmcnt(3)\n\t" \
  "v_smfmac_f32_16x16x64_bf16 v[40:43], v[96:99], a[192:199], v106\n\t" \
  "v_smfmac_f32_16x16x64_bf16 v[44:47], v[96:99], a[224:231], v106\n\t" \
  "s_waitcnt lgkmcnt(0)\n\t" \
  "s_barrier\n\t" \
  "ds_read_b64_tr_b16 v[52:53], v107 offset:256\n\t" \
  "ds_read_b64_tr_b16 v[54:55], v107 offset:320\n\t" \
  "v_smfmac_f32_16x16x64_bf16 v[40:43], v[56:59], a[208:215], v106\n\t" \
  "ds_read_b64_tr_b16 v[60:61], v107 offset:1280\n\t" \
  "ds_read_b64_tr_b16 v[62:63], v107 offset:1344\n\t" \
  "v_smfmac_f32_16x16x64_bf16 v[44:47], v[56:59], v[0:7], v106\n\t" \
  "v_smfmac_f32_16x16x64_bf16 v[80:83], v[96:99], a[240:247], v106\n\t" \
  "v_mov_b64 v[32:33], 0\n\t" \
  "v_smfmac_f32_16x16x64_bf16 v[84:87], v[96:99], v[136:143], v106\n\t" \
  "v_mov_b64 v[34:35], 0\n\t" \
  "v_smfmac_f32_16x16x64_bf16 v[80:83], v[56:59], v[120:127], v106\n\t" \
  "v_mov_b64 v[36:37], 0\n\t" \
  "v_smfmac_f32_16x16x64_bf16 v[84:87], v[56:59], v[16:23], v106\n\t" \
  "s_waitcnt lgkmcnt(2)\n\t" \
  "v_smfmac_f32_16x16x64_bf16 v[40:43], v[52:55], a[200:207], v106\n\t" \
  "v_smfmac_f32_16x16x64_bf16 v[44:47], v[52:55], a[232:239], v106\n\t" \
  "s_xor_b32 s76, s47, 0x40000\n\t" \
  "s_waitcnt lgkmcnt(0)\n\t" \
  "v_smfmac_f32_16x16x64_bf16 v[40:43], v[60:63], a[216:223], v106\n\t" \
  "v_mov_b64 v[38:39], 0\n\t" \
  "s_xor_b32 s77, s47, 0x80000\n\t" \
  "v_smfmac_f32_16x16x64_bf16 v[44:47], v[60:63], v[8:15], v106\n\t" \
  "s_xor_b32 s78, s47, 0xc0000\n\t" \
  "v_smfmac_f32_16x16x64_bf16 v[80:83], v[52:55], a[248:255], v106\n\t" \
  "ds_read_b128 v[0:3], v103 offset:32768\n\t" \
  "v_cvt_pk_bf16_f32 v64, v40, v41\n\t" \
  "v_smfmac_f32_16x16x64_bf16 v[84:87], v[52:55], v[144:151], v106\n\t" \
  "ds_read_b128 v[4:7], v103 offset:33792\n\t" \
  "v_cvt_pk_bf16_f32 v65, v42, v43\n\t" \
  "v_smfmac_f32_16x16x64_bf16 v[80:83], v[60:63], v[128:135], v106\n\t" \
  "ds_read_b128 v[8:11], v103 offset:34816\n\t" \
  "v_cvt_pk_bf16_f32 v66, v44, v45\n\t" \
  "v_smfmac_f32_16x16x64_bf16 v[84:87], v[60:63], v[24:31], v106\n\t" \
  "ds_read_b128 v[12:15], v103 offset:35840\n\t" \
  "v_cvt_pk_bf16_f32 v67, v46, v47\n\t" \
  "v_smfmac_f32_16x16x64_bf16 v[88:91], v[96:99], v[152:159], v106\n\t" \
  "v_permlane16_swap_b32 v64, v66\n\t" \
  "v_cvt_pk_bf16_f32 v68, v80, v81\n\t" \
  "v_smfmac_f32_16x16x64_bf16 v[92:95], v[96:99], v[184:191], v106\n\t" \
  "v_permlane16_swap_b32 v65, v67\n\t" \
  "v_cvt_pk_bf16_f32 v69, v82, v83\n\t" \
  "v_smfmac_f32_16x16x64_bf16 v[88:91], v[52:55], v[160:167], v106\n\t" \
  "buffer_store_dwordx4 v[64:67], v108, %[ru], s47 offen sc1\n\t" \
  "v_cvt_pk_bf16_f32 v70, v84, v85\n\t" \
  "v_smfmac_f32_16x16x64_bf16 v[92:95], v[52:55], v[192:199], v106\n\t" \
  "v_cvt_pk_bf16_f32 v71, v86, v87\n\t" \
  "v_smfmac_f32_16x16x64_bf16 v[88:91], v[56:59], v[168:175], v106\n\t" \
  "ds_read_b128 v[16:19], v103 offset:49152\n\t" \
  "v_permlane16_swap_b32 v68, v70\n\t" \
  "s_waitcnt lgkmcnt(3)\n\t" \
  "v_smfmac_f32_16x16x64_bf16 v[92:95], v[56:59], v[0:7], v106\n\t" \
  "ds_read_b128 v[20:23], v103 offset:50176\n\t" \
  "v_permlane16_swap_b32 v69, v71\n\t" \
  "v_smfmac_f32_16x16x64_bf16 v[88:91], v[60:63], v[176:183], v106\n\t" \
  "ds_read_b128 v[24:27], v103 offset:51200\n\t" \
  "buffer_store_dwordx4 v[68:71], v108, %[ru], s76 offen sc1\n\t" \
  "s_waitcnt lgkmcnt(3)\n\t" \
  "v_smfmac_f32_16x16x64_bf16 v[92:95], v[60:63], v[8:15], v106\n\t" \
  "ds_read_b128 v[28:31], v103 offset:52224\n\t" \
  "v_smfmac_f32_16x16x64_bf16 v[32:35], v[96:99], v[200:207], v106\n\t" \
  "v_cvt_pk_bf16_f32 v72, v88, v89\n\t" \
  "v_smfmac_f32_16x16x64_bf16 v[36:39], v[96:99], v[232:239], v106\n\t" \
  "v_cvt_pk_bf16_f32 v73, v90, v91\n\t" \
  "v_smfmac_f32_16x16x64_bf16 v[32:35], v[52:55], v[208:215], v106\n\t" \
  "v_cvt_pk_bf16_f32 v74, v92, v93\n\t" \
  "v_smfmac_f32_16x16x64_bf16 v[36:39], v[52:55], v[240:247], v106\n\t" \
  "v_cvt_pk_bf16_f32 v75, v94, v95\n\t" \
  "v_smfmac_f32_16x16x64_bf16 v[32:35], v[56:59], v[216:223], v106\n\t" \
  "v_permlane16_swap_b32 v72, v74\n\t" \
  "v_smfmac_f32_16x16x64_bf16 v[32:35], v[60:63], v[224:231], v106\n\t" \
  "v_permlane16_swap_b32 v73, v75\n\t" \
  "s_waitcnt lgkmcnt(2)\n\t" \
  "v_smfmac_f32_16x16x64_bf16 v[36:39], v[56:59], v[16:23], v106\n\t" \
  "buffer_store_dwordx4 v[72:75], v108, %[ru], s77 offen sc1\n\t" \
  "s_waitcnt lgkmcnt(0)\n\t" \
  "v_smfmac_f32_16x16x64_bf16 v[36:39], v[60:63], v[24:31], v106\n\t" \
  "v_cvt_pk_bf16_f32 v76, v32, v33\n\t" \
  "v_cvt_pk_bf16_f32 v77, v34, v35\n\t" \
  "s_nop 5\n\t" \
  "v_cvt_pk_bf16_f32 v78, v36, v37\n\t" \
  "v_cvt_pk_bf16_f32 v79, v38, v39\n\t" \
  "s_nop 0\n\t" \
  "v_permlane16_swap_b32 v76, v78\n\t" \
  "v_permlane16_swap_b32 v77, v79\n\t" \
  "buffer_store_dwordx4 v[76:79], v108, %[ru], s78 offen sc1\n\t" \
  "s_memtime s[76:77]\n\t" \
  "s_mov_b32 s42, 0\n\t" \
  "s_waitcnt lgkmcnt(0)\n\t" \
  "s_mov_b32 s57, s76\n\t" \
  "s_add_i32 s41, s56, 1950\n\t" \
  "s_sub_i32 s76, s92, s51\n\t" \
  "s_sub_i32 s77, s93, s53\n\t" \
  "s_cmp_eq_u32 s76, 0\n\t" \
  "s_cbranch_scc1 21f\n\t" \
  "v_cvt_f32_u32 v117, s76\n\t" \
  "v_cvt_f32_u32 v116, s77\n\t" \
  "v_rcp_f32 v117, v117\n\t" \
  "s_mov_b32 s51, s92\n\t" \
  "v_mul_f32 v117, v117, v116\n\t" \
  "v_max_f32 v117, 0x3f028f5c, v117\n\t" \
  "v_min_f32 v117, 0x3ffeb852, v117\n\t" \
  "v_rcp_f32 v116, v117\n\t" \
  "v_mul_f32 v117, 0x4f000000, v117\n\t" \
  "v_mul_f32 v116, 0x47800000, v116\n\t" \
  "v_cvt_u32_f32 v117, v117\n\t" \
  "v_cvt_u32_f32 v116, v116\n\t" \
  "s_mov_b32 s53, s93\n\t" \
  "v_readfirstlane_b32 s46, v117\n\t" \
  "v_readfirstlane_b32 s52, v116\n\t" \
  "21:\n\t" \
  "s_add_i32 s44, s44, 1\n\t" \
  "s_and_b32 s58, s58, 4\n\t" \
  "s_and_b32 s76, s44, 64\n\t" \
  "s_or_b32 s42, s42, s76\n\t" \
  "s_cbranch_scc0 20b\n\t" \
  "s_branch 99f\n\t" \
  "14:\n\t" \
  "s_sleep 3\n\t" \
  "s_add_u32 s59, s59, 0x10000\n\t" \
  "s_cmp_lt_u32 s59, 0x100000\n\t" \
  "s_cbranch_scc1 13b\n\t" \
  "s_mov_b64 exec, -1\n\t" \
  "s_mov_b32 s42, 16\n\t" \
  "s_branch 99f\n\t" \
  "8:\n\t" \
  "s_bitcmp1_b32 s58, 1\n\t" \
  "s_cbranch_scc1 4b\n\t" \
  "s_bitcmp1_b32 s58, 2\n\t" \
  "s_cbranch_scc1 5b\n\t" \
  "v_max3_u32 v116, v32, v33, v34\n\t" \
  "v_max3_u32 v117, v35, v36, v37\n\t" \
  "v_max3_u32 v116, v116, v38, v39\n\t" \
  "v_max_u32 v116, v116, v117\n\t" \
  "v_cmp_eq_u32 vcc, -1, v116\n\t" \
  "s_cbranch_vccz 5b\n\t" \
  "s_branch 0f\n\t" \
  "9:\n\t" \
  "s_bitcmp1_b32 s58, 2\n\t" \
  "s_cbranch_scc1 6b\n\t" \
  "v_max3_u32 v116, v40, v41, v42\n\t" \
  "v_max3_u32 v117, v43, v44, v45\n\t" \
  "v_max3_u32 v116, v116, v46, v47\n\t" \
  "v_max_u32 v116, v116, v117\n\t" \
  "v_cmp_eq_u32 vcc, -1, v116\n\t" \
  "s_cbranch_vccz 6b\n\t" \
  "s_or_b32 s58, s58, 2\n\t" \
  "0:\n\t" \
  "s_or_b32 s58, s58, 1\n\t" \
  "11:\n\t" \
  "buffer_load_dwordx4 v[32:35], v100, %[rh], s40 offen sc1\n\t" \
  "buffer_load_dwordx4 v[36:39], v100, %[rh], s76 offen sc1\n\t" \
  "buffer_load_dwordx4 v[40:43], v100, %[rh], s77 offen sc1\n\t" \
  "buffer_load_dwordx4 v[44:47], v100, %[rh], s78 offen sc1\n\t" \
  "s_waitcnt lgkmcnt(0)\n\t" \
  "s_branch 10b\n\t" \
  "17:\n\t" \
  "s_add_u32 s59, s59, 1\n\t" \
  "s_bitcmp1_b32 s58, 2\n\t" \
  "s_cbranch_scc1 3b\n\t" \
  "s_waitcnt vmcnt(0)\n\t" \
  "v_max_u32 v116, v32, v33\n\t" \
  "v_max3_u32 v116, v116, v34, v35\n\t" \
  "v_max3_u32 v116, v116, v36, v37\n\t" \
  "v_max3_u32 v116, v116, v38, v39\n\t" \
  "v_max3_u32 v116, v116, v40, v41\n\t" \
  "v_max3_u32 v116, v116, v42, v43\n\t" \
  "v_max3_u32 v116, v116, v44, v45\n\t" \
  "v_max3_u32 v116, v116, v46, v47\n\t" \
  "v_cmp_eq_u32 vcc, -1, v116\n\t" \
  "s_cbranch_vccz 3b\n\t" \
  "s_bitcmp1_b32 s59, 6\n\t" \
  "s_cbranch_scc1 12f\n\t" \
  "buffer_load_dwordx4 v[32:35], v100, %[rh], s40 offen sc1\n\t" \
  "buffer_load_dwordx4 v[36:39], v100, %[rh], s76 offen sc1\n\t" \
  "buffer_load_dwordx4 v[40:43], v100, %[rh], s77 offen sc1\n\t" \
  "buffer_load_dwordx4 v[44:47], v100, %[rh], s78 offen sc1\n\t" \
  "s_branch 17b\n\t" \
  "12:\n\t" \
  "s_mov_b32 s42, s58\n\t" \
  "s_branch 99f\n\t" \
  "92:\n\t" \
  "s_mov_b32 s58, s42\n\t" \
  "20:\n\t" \
  "s_and_b32 s76, s44, 7\n\t" \
  "s_lshl_b32 s76, s76, 15\n\t" \
  "s_add_i32 s40, s54, s76\n\t" \
  "s_and_b32 s76, s44, 3\n\t" \
  "s_lshl_b32 s45, s76, 14\n\t" \
  "s_lshl_b32 s47, s76, 20\n\t" \
  "s_add_i32 s47, s47, s55\n\t" \
  "s_lshr_b32 s76, s47, 21\n\t" \
  "s_bitcmp1_b64 %[sel], s76\n\t" \
  "s_cselect_b32 s76, 0x2000, 0\n\t" \
  "s_add_i32 s47, s47, s76\n\t" \
  "s_add_i32 s49, s44, -1\n\t" \
  "s_and_b32 s48, s49, 2\n\t" \
  "s_lshl_b32 s48, s48, 13\n\t" \
  "s_and_b32 s76, s49, 1\n\t" \
  "s_lshl_b32 s76, s76, 12\n\t" \
  "s_add_i32 s48, s48, s76\n\t" \
  "s_lshl_b32 s50, s53, 8\n\t" \
  "s_mov_b32 s59, 0\n\t" \
  "s_mov_b32 s92, s51\n\t" \
  "s_mov_b32 s93, s53\n\t" \
  "s_xor_b32 s76, s40, 0x400\n\t" \
  "s_xor_b32 s77, s40, 0x800\n\t" \
  "s_xor_b32 s78, s40, 0xc00\n\t" \
  "s_cmp_lg_u32 s58, 0\n\t" \
  "s_cbranch_scc1 11f\n\t" \
  "10:\n\t" \
  "ds_read_b128 v[48:51], v102 offset:32768\n\t" \
  "ds_read_b128 v[52:55], v102 offset:33792\n\t" \
  "ds_read_b128 v[56:59], v102 offset:34816\n\t" \
  "ds_read_b128 v[60:63], v102 offset:35840\n\t" \
  "ds_read_b128 v[0:3], v102 offset:49152\n\t" \
  "ds_read_b128 v[4:7], v102 offset:50176\n\t" \
  "ds_read_b128 v[8:11], v102 offset:51200\n\t" \
  "ds_read_b128 v[12:15], v102 offset:52224\n\t" \
  "ds_read_b128 v[16:19], v102 offset:0\n\t" \
  "ds_read_b128 v[20:23], v102 offset:1024\n\t" \
  "ds_read_b128 v[24:27], v102 offset:2048\n\t" \
  "ds_read_b128 v[28:31], v102 offset:3072\n\t" \
  "s_waitcnt lgkmcnt(4)\n\t" \
  "ds_read_b128 v[80:83], v102 offset:16384\n\t" \
  "ds_read_b128 v[84:87], v102 offset:17408\n\t" \
  "ds_read_b128 v[88:91], v102 offset:18432\n\t" \
  "ds_read_b128 v[92:95], v102 offset:19456\n\t" \
  "s_bitcmp1_b32 s58, 1\n\t" \
  "s_cselect_b64 s[80:81], -1, 0\n\t" \
  "s_cmp_lg_u32 s58, 0\n\t" \
  "s_cbranch_scc1 17f\n\t" \
  "s_mov_b32 exec_hi, 0\n\t" \
  "16:\n\t" \
  "s_memtime vcc\n\t" \
  "s_waitcnt lgkmcnt(0)\n\t" \
  "s_sub_i32 s79, s41, vcc_lo\n\t" \
  "s_cmp_gt_i32 s79, 64\n\t" \
  "s_cbranch_scc1 16b\n\t" \
  "13:\n\t" \
  "buffer_load_dword v116, v112, %[rh], s48 offen sc1\n\t" \
  "s_waitcnt vmcnt(0)\n\t" \
  "15:\n\t" \
  "v_mov_b32 v117, 20000\n\t" \
  "v_cmp_ne_u32_sdwa vcc, v116, s49 src0_sel:BYTE_0 src1_sel:DWORD\n\t" \
  "v_subrev_u32 v116, s50, v116\n\t" \
  "v_ashrrev_i32 v116, 8, v116\n\t" \
  "s_nop 1\n\t" \
  "v_max_i32_dpp v116, v116, v116 quad_perm:[1,0,3,2] row_mask:0xf bank_mask:0xf\n\t" \
  "s_cbranch_vccnz 14f\n\t" \
  "s_nop 0\n\t" \
  "v_max_i32_dpp v116, v116, v116 quad_perm:[2,3,0,1] row_mask:0xf bank_mask:0xf\n\t" \
  "s_nop 1\n\t" \
  "v_max_i32_dpp v116, v116, v116 row_half_mirror row_mask:0xf bank_mask:0xf\n\t" \
  "s_nop 1\n\t" \
  "v_max_i32_dpp v116, v116, v116 row_mirror row_mask:0xf bank_mask:0xf\n\t" \
  "v_med3_i32 v116, v116, 0, v117\n\t" \
  "v_mul_u32_u24 v116, s52, v116\n\t" \
  "v_lshrrev_b32 v116, 16, v116\n\t" \
  "s_mov_b64 exec, -1\n\t" \
  "v_readfirstlane_b32 s84, v116\n\t" \
  "v_readlane_b32 s85, v116, 16\n\t" \
  "s_max_u32 s84, s84, s85\n\t" \
  "s_add_i32 s84, s84, s51\n\t" \
  "1:\n\t" \
  "s_memtime vcc\n\t" \
  "s_waitcnt lgkmcnt(0)\n\t" \
  "s_sub_i32 s79, s84, vcc_lo\n\t" \
  "s_cmp_gt_i32 s79, 128\n\t" \
  "s_cbranch_scc1 1b\n\t" \
  ".p2align 4\n\t" \
  "2:\n\t" \
  "s_sub_i32 s79, s79, 33\n\t" \
  "s_cmp_gt_i32 s79, 56\n\t" \
  "s_cbranch_scc1 2b\n\t" \
  "buffer_load_dwordx4 v[32:35], v100, %[rh], s40 offen sc1\n\t" \
  "s_nop 12\n\t" \
  "buffer_load_dwordx4 v[36:39], v100, %[rh], s76 offen sc1\n\t" \
  "s_nop 12\n\t" \
  "buffer_load_dwordx4 v[40:43], v100, %[rh], s77 offen sc1\n\t" \
  "s_nop 12\n\t" \
  "buffer_load_dwordx4 v[44:47], v100, %[rh], s78 offen sc1\n\t" \
  "s_memtime vcc\n\t" \
  "s_memrealtime s[82:83]\n\t" \
  "s_nop 4\n\t" \
  "s_memrealtime s[84:85]\n\t" \
  "s_nop 4\n\t" \
  "s_memrealtime s[56:57]\n\t" \
  "s_nop 4\n\t" \
  "s_memrealtime s[86:87]\n\t" \
  "s_nop 4\n\t" \
  "s_memrealtime s[88:89]\n\t" \
  "s_nop 4\n\t" \
  "s_memrealtime s[90:91]\n\t" \
  "s_mov_b32 s92, 0\n\t" \
  "s_waitcnt lgkmcnt(0)\n\t" \
  "s_cmp_eq_u32 s82, s84\n\t" \
  "s_addc_u32 s92, s92, 0\n\t" \
  "s_cmp_eq_u32 s82, s56\n\t" \
  "s_addc_u32 s92, s92, 0\n\t" \
  "s_cmp_eq_u32 s82, s86\n\t" \
  "s_addc_u32 s92, s92, 0\n\t" \
  "s_cmp_eq_u32 s82, s88\n\t" \
  "s_addc_u32 s92, s92, 0\n\t" \
  "s_cmp_eq_u32 s82, s90\n\t" \
  "s_addc_u32 s92, s92, 0\n\t" \
  "s_mul_i32 s92, s92, 24\n\t" \
  "s_add_i32 s92, s92, vcc_lo\n\t" \
  "s_lshl4_add_u32 s93, s82, 64\n\t" \
  "3:\n\t" \
  "v_mov_b64 v[72:73], 0\n\t" \
  "v_mov_b64 v[74:75], 0\n\t" \
  "v_mov_b64 v[76:77], 0\n\t" \
  "v_mov_b64 v[78:79], 0\n\t" \
  "s_waitcnt lgkmcnt(0)\n\t" \
  "s_waitcnt vmcnt(3)\n\t" \
  "v_smfmac_f32_16x16x64_bf16 v[72:75], v[32:35], v[48:55], v104\n\t" \
  "ds_write_b128 v101, v[32:35] offset:8192\n\t" \
  "v_smfmac_f32_16x16x64_bf16 v[76:79], v[32:35], v[56:63], v104\n\t" \
  "s_waitcnt vmcnt(2)\n\t" \
  "ds_write_b128 v101, v[36:39] offset:9216\n\t" \
  "v_smfmac_f32_16x16x64_bf16 v[72:75], v[36:39], a[96:103], v104\n\t" \
  "v_smfmac_f32_16x16x64_bf16 v[76:79], v[36:39], a[104:111], v104\n\t" \
  "s_nop 3\n\t" \
  "v_cmp_u_f32 vcc, v72, v73\n\t" \
  "v_cmp_u_f32 s[82:83], v74, v75\n\t" \
  "s_waitcnt vmcnt(1)\n\t" \
  "v_smfmac_f32_16x16x64_bf16 v[72:75], v[40:43], a[112:119], v104\n\t" \
  "s_or_b64 vcc, vcc, s[82:83]\n\t" \
  "s_or_b64 vcc, vcc, s[80:81]\n\t" \
  "v_smfmac_f32_16x16x64_bf16 v[76:79], v[40:43], a[120:127], v104\n\t" \
  "s_waitcnt lgkmcnt(0)\n\t" \
  "s_cbranch_scc1 8f\n\t" \
  "5:\n\t" \
  "s_barrier\n\t" \
  "4:\n\t" \
  "s_memtime s[56:57]\n\t" \
  "ds_read_b128 v[48:51], v101 offset:12288\n\t" \
  "ds_read_b128 v[52:55], v101 offset:13312\n\t" \
  "ds_read_b128 v[56:59], v101 offset:1024\n\t" \
  "s_waitcnt vmcnt(0)\n\t" \
  "v_smfmac_f32_16x16x64_bf16 v[72:75], v[44:47], a[128:135], v104\n\t" \
  "ds_read_b128 v[60:63], v101 offset:0\n\t" \
  "ds_read_b128 v[64:67], v101 offset:5120\n\t" \
  "v_smfmac_f32_16x16x64_bf16 v[76:79], v[44:47], a[136:143], v104\n\t" \
  "ds_read_b128 v[68:71], v101 offset:4096\n\t" \
  "ds_write_b128 v101, v[40:43] offset:10240\n\t" \
  "s_nop 1\n\t" \
  "v_cmp_u_f32 vcc, v72, v73\n\t" \
  "v_cmp_u_f32 s[82:83], v74, v75\n\t" \
  "s_waitcnt lgkmcnt(6)\n\t" \
  "v_smfmac_f32_16x16x64_bf16 v[72:75], v[48:51], v[0:7], v104\n\t" \
  "s_or_b64 vcc, vcc, s[82:83]\n\t" \
  "v_smfmac_f32_16x16x64_bf16 v[76:79], v[48:51], v[8:15], v104\n\t" \
  "s_cbranch_scc1 9f\n\t" \
  "6:\n\t" \
  "s_waitcnt lgkmcnt(5)\n\t" \
  "v_smfmac_f32_16x16x64_bf16 v[72:75], v[52:55], a[144:151], v104\n\t" \
  "ds_write_b128 v101, v[44:47] offset:11264\n\t" \
  "v_smfmac_f32_16x16x64_bf16 v[76:79], v[52:55], a[152:159], v104\n\t" \
  "v_mov_b64 v[40:41], 0\n\t" \
  "s_waitcnt lgkmcnt(5)\n\t" \
  "v_smfmac_f32_16x16x64_bf16 v[72:75], v[56:59], a[0:7], v104\n\t" \
  "v_mov_b64 v[42:43], 0\n\t" \
  "v_smfmac_f32_16x16x64_bf16 v[76:79], v[56:59], a[8:15], v104\n\t" \
  "s_waitcnt lgkmcnt(4)\n\t" \
  "v_smfmac_f32_16x16x64_bf16 v[72:75], v[60:63], v[16:23], v104\n\t" \
  "s_waitcnt lgkmcnt(0)\n\t" \
  "s_barrier\n\t" \
  "v_smfmac_f32_16x16x64_bf16 v[76:79], v[60:63], v[24:31], v104\n\t" \
  "ds_read_b128 v[48:51], v101 offset:14336\n\t" \
  "ds_read_b128 v[52:55], v101 offset:2048\n\t" \
  "v_smfmac_f32_16x16x64_bf16 v[72:75], v[64:67], a[48:55], v104\n\t" \
  "ds_read_b128 v[56:59], v101 offset:6144\n\t" \
  "ds_read_b128 v[60:63], v101 offset:15360\n\t" \
  "v_smfmac_f32_16x16x64_bf16 v[76:79], v[64:67], a[56:63], v104\n\t" \
  "ds_read_b128 v[32:35], v101 offset:3072\n\t" \
  "ds_read_b128 v[36:39], v101 offset:7168\n\t" \
  "v_smfmac_f32_16x16x64_bf16 v[72:75], v[68:71], v[80:87], v104\n\t" \
  "ds_read_b128 v[0:3], v103 offset:0\n\t" \
  "v_mov_b64 v[44:45], 0\n\t" \
  "v_smfmac_f32_16x16x64_bf16 v[76:79], v[68:71], v[88:95], v104\n\t" \
  "v_mov_b64 v[46:47], 0\n\t" \
  "s_waitcnt lgkmcnt(5)\n\t" \
  "v_smfmac_f32_16x16x64_bf16 v[72:75], v[48:51], a[160:167], v104\n\t" \
  "ds_read_b128 v[4:7], v103 offset:1024\n\t" \
  "v_mov_b64 v[80:81], 0\n\t" \
  "v_smfmac_f32_16x16x64_bf16 v[72:75], v[52:55], a[16:23], v104\n\t" \
  "v_mov_b64 v[82:83], 0\n\t" \
  "s_waitcnt lgkmcnt(2)\n\t" \
  "v_smfmac_f32_16x16x64_bf16 v[72:75], v[56:59], a[64:71], v104\n\t" \
  "ds_read_b128 v[8:11], v103 offset:2048\n\t" \
  "v_mov_b64 v[84:85], 0\n\t" \
  "v_smfmac_f32_16x16x64_bf16 v[72:75], v[60:63], a[176:183], v104\n\t" \
  "ds_read_b128 v[12:15], v103 offset:3072\n\t" \
  "v_mov_b64 v[86:87], 0\n\t" \
  "v_smfmac_f32_16x16x64_bf16 v[72:75], v[32:35], a[32:39], v104\n\t" \
  "ds_read_b128 v[16:19], v103 offset:16384\n\t" \
  "v_mov_b64 v[88:89], 0\n\t" \
  "v_smfmac_f32_16x16x64_bf16 v[72:75], v[36:39], a[80:87], v104\n\t" \
  "ds_read_b128 v[20:23], v103 offset:17408\n\t" \
  "v_mov_b64 v[90:91], 0\n\t" \
  "v_smfmac_f32_16x16x64_bf16 v[76:79], v[48:51], a[168:175], v104\n\t" \
  "ds_read_b128 v[24:27], v103 offset:18432\n\t" \
  "v_mov_b64 v[92:93], 0\n\t" \
  "v_smfmac_f32_16x16x64_bf16 v[76:79], v[52:55], a[24:31], v104\n\t" \
  "v_add_f32 v96, v72, v74\n\t" \
  "v_add_f32 v97, v73, v75\n\t" \
  "v_cvt_pk_bf16_f32 v96, v96, v97\n\t" \
  "ds_write_b32 v105, v96\n\t" \
  "v_smfmac_f32_16x16x64_bf16 v[76:79], v[56:59], a[72:79], v104\n\t" \
  "ds_read_b128 v[28:31], v103 offset:19456\n\t" \
  "v_mov_b64 v[94:95], 0\n\t" \
  "v_smfmac_f32_16x16x64_bf16 v[76:79], v[60:63], a[184:191], v104\n\t" \
  "v_smfmac_f32_16x16x64_bf16 v[76:79], v[32:35], a[40:47], v104\n\t" \
  "v_smfmac_f32_16x16x64_bf16 v[76:79], v[36:39], a[88:95], v104\n\t" \
  "s_waitcnt lgkmcnt(1)\n\t" \
  "s_barrier\n\t" \
  "ds_read_b64_tr_b16 v[96:97], v107 offset:0\n\t" \
  "ds_read_b64_tr_b16 v[98:99], v107 offset:64\n\t" \
  "ds_read_b64_tr_b16 v[56:57], v107 offset:1024\n\t" \
  "ds_read_b64_tr_b16 v[58:59], v107 offset:1088\n\t" \
  "s_nop 1\n\t" \
  "v_add_f32 v116, v76, v78\n\t" \
  "v_add_f32 v117, v77, v79\n\t" \
  "v_cvt_pk_bf16_f32 v116, v116, v117\n\t" \
  "ds_write_b32 v105, v116 offset:256\n\t" \
  "s_waitcnt lgkmcnt(3)\n\t" \
  "v_smfmac_f32_16x16x64_bf16 v[40:43], v[96:99], a[192:199], v106\n\t" \
  "v_smfmac_f32_16x16x64_bf16 v[44:47], v[96:99], a[224:231], v106\n\t" \
  "s_waitcnt lgkmcnt(0)\n\t" \
  "s_barrier\n\t" \
  "ds_read_b64_tr_b16 v[52:53], v107 offset:256\n\t" \
  "ds_read_b64_tr_b16 v[54:55], v107 offset:320\n\t" \
  "v_smfmac_f32_16x16x64_bf16 v[40:43], v[56:59], a[208:215], v106\n\t" \
  "ds_read_b64_tr_b16 v[60:61], v107 offset:1280\n\t" \
  "ds_read_b64_tr_b16 v[62:63], v107 offset:1344\n\t" \
  "v_smfmac_f32_16x16x64_bf16 v[44:47], v[56:59], v[0:7], v106\n\t" \
  "v_smfmac_f32_16x16x64_bf16 v[80:83], v[96:99], a[240:247], v106\n\t" \
  "v_mov_b64 v[32:33], 0\n\t" \
  "v_smfmac_f32_16x16x64_bf16 v[84:87], v[96:99], v[136:143], v106\n\t" \
  "v_mov_b64 v[34:35], 0\n\t" \
  "v_smfmac_f32_16x16x64_bf16 v[80:83], v[56:59], v[120:127], v106\n\t" \
  "v_mov_b64 v[36:37], 0\n\t" \
  "v_smfmac_f32_16x16x64_bf16 v[84:87], v[56:59], v[16:23], v106\n\t" \
  "s_waitcnt lgkmcnt(2)\n\t" \
  "v_smfmac_f32_16x16x64_bf16 v[40:43], v[52:55], a[200:207], v106\n\t" \
  "v_smfmac_f32_16x16x64_bf16 v[44:47], v[52:55], a[232:239], v106\n\t" \
  "s_xor_b32 s76, s47, 0x40000\n\t" \
  "s_waitcnt lgkmcnt(0)\n\t" \
  "v_smfmac_f32_16x16x64_bf16 v[40:43], v[60:63], a[216:223], v106\n\t" \
  "v_mov_b64 v[38:39], 0\n\t" \
  "s_xor_b32 s77, s47, 0x80000\n\t" \
  "v_smfmac_f32_16x16x64_bf16 v[44:47], v[60:63], v[8:15], v106\n\t" \
  "s_xor_b32 s78, s47, 0xc0000\n\t" \
  "v_smfmac_f32_16x16x64_bf16 v[80:83], v[52:55], a[248:255], v106\n\t" \
  "ds_read_b128 v[0:3], v103 offset:32768\n\t" \
  "v_cvt_pk_bf16_f32 v64, v40, v41\n\t" \
  "v_smfmac_f32_16x16x64_bf16 v[84:87], v[52:55], v[144:151], v106\n\t" \
  "ds_read_b128 v[4:7], v103 offset:33792\n\t" \
  "v_cvt_pk_bf16_f32 v65, v42, v43\n\t" \
  "v_smfmac_f32_16x16x64_bf16 v[80:83], v[60:63], v[128:135], v106\n\t" \
  "ds_read_b128 v[8:11], v103 offset:34816\n\t" \
  "v_cvt_pk_bf16_f32 v66, v44, v45\n\t" \
  "v_smfmac_f32_16x16x64_bf16 v[84:87], v[60:63], v[24:31], v106\n\t" \
  "ds_read_b128 v[12:15], v103 offset:35840\n\t" \
  "v_cvt_pk_bf16_f32 v67, v46, v47\n\t" \
  "v_smfmac_f32_16x16x64_bf16 v[88:91], v[96:99], v[152:159], v106\n\t" \
  "v_permlane16_swap_b32 v64, v66\n\t" \
  "v_cvt_pk_bf16_f32 v68, v80, v81\n\t" \
  "v_smfmac_f32_16x16x64_bf16 v[92:95], v[96:99], v[184:191], v106\n\t" \
  "v_permlane16_swap_b32 v65, v67\n\t" \
  "v_cvt_pk_bf16_f32 v69, v82, v83\n\t" \
  "v_smfmac_f32_16x16x64_bf16 v[88:91], v[52:55], v[160:167], v106\n\t" \
  "v_cvt_pk_bf16_f32 v70, v84, v85\n\t" \
  "v_smfmac_f32_16x16x64_bf16 v[92:95], v[52:55], v[192:199], v106\n\t" \
  "buffer_store_dwordx4 v[64:67], v108, %[ru], s47 offen sc1\n\t" \
  "v_cvt_pk_bf16_f32 v71, v86, v87\n\t" \
  "v_smfmac_f32_16x16x64_bf16 v[88:91], v[56:59], v[168:175], v106\n\t" \
  "ds_read_b128 v[16:19], v103 offset:49152\n\t" \
  "v_permlane16_swap_b32 v68, v70\n\t" \
  "s_waitcnt lgkmcnt(3)\n\t" \
  "v_smfmac_f32_16x16x64_bf16 v[92:95], v[56:59], v[0:7], v106\n\t" \
  "ds_read_b128 v[20:23], v103 offset:50176\n\t" \
  "v_permlane16_swap_b32 v69, v71\n\t" \
  "v_smfmac_f32_16x16x64_bf16 v[88:91], v[60:63], v[176:183], v106\n\t" \
  "ds_read_b128 v[24:27], v103 offset:51200\n\t" \
  "s_waitcnt lgkmcnt(3)\n\t" \
  "v_smfmac_f32_16x16x64_bf16 v[92:95], v[60:63], v[8:15], v106\n\t" \
  "ds_read_b128 v[28:31], v103 offset:52224\n\t" \
  "buffer_store_dwordx4 v[68:71], v108, %[ru], s76 offen sc1\n\t" \
  "v_smfmac_f32_16x16x64_bf16 v[32:35], v[96:99], v[200:207], v106\n\t" \
  "v_cvt_pk_bf16_f32 v72, v88, v89\n\t" \
  "v_smfmac_f32_16x16x64_bf16 v[36:39], v[96:99], v[232:239], v106\n\t" \
  "v_cvt_pk_bf16_f32 v73, v90, v91\n\t" \
  "v_smfmac_f32_16x16x64_bf16 v[32:35], v[52:55], v[208:215], v106\n\t" \
  "v_cvt_pk_bf16_f32 v74, v92, v93\n\t" \
  "v_smfmac_f32_16x16x64_bf16 v[36:39], v[52:55], v[240:247], v106\n\t" \
  "v_cvt_pk_bf16_f32 v75, v94, v95\n\t" \
  "v_smfmac_f32_16x16x64_bf16 v[32:35], v[56:59], v[216:223], v106\n\t" \
  "v_permlane16_swap_b32 v72, v74\n\t" \
  "v_smfmac_f32_16x16x64_bf16 v[32:35], v[60:63], v[224:231], v106\n\t" \
  "v_permlane16_swap_b32 v73, v75\n\t" \
  "s_waitcnt lgkmcnt(2)\n\t" \
  "v_smfmac_f32_16x16x64_bf16 v[36:39], v[56:59], v[16:23], v106\n\t" \
  "s_waitcnt lgkmcnt(0)\n\t" \
  "v_smfmac_f32_16x16x64_bf16 v[36:39], v[60:63], v[24:31], v106\n\t" \
  "buffer_store_dwordx4 v[72:75], v108, %[ru], s77 offen sc1\n\t" \
  "v_cvt_pk_bf16_f32 v76, v32, v33\n\t" \
  "v_cvt_pk_bf16_f32 v77, v34, v35\n\t" \
  "s_nop 5\n\t" \
  "v_cvt_pk_bf16_f32 v78, v36, v37\n\t" \
  "v_cvt_pk_bf16_f32 v79, v38, v39\n\t" \
  "s_nop 0\n\t" \
  "v_permlane16_swap_b32 v76, v78\n\t" \
  "v_permlane16_swap_b32 v77, v79\n\t" \
  "buffer_store_dwordx4 v[76:79], v108, %[ru], s78 offen sc1\n\t" \
  "s_memtime s[76:77]\n\t" \
  "s_mov_b32 s42, 0\n\t" \
  "s_waitcnt lgkmcnt(0)\n\t" \
  "s_mov_b32 s57, s76\n\t" \
  "s_add_i32 s41, s56, 1950\n\t" \
  "s_sub_i32 s76, s92, s51\n\t" \
  "s_sub_i32 s77, s93, s53\n\t" \
  "s_cmp_eq_u32 s76, 0\n\t" \
  "s_cbranch_scc1 21f\n\t" \
  "v_cvt_f32_u32 v117, s76\n\t" \
  "v_cvt_f32_u32 v116, s77\n\t" \
  "v_rcp_f32 v117, v117\n\t" \
  "s_mov_b32 s51, s92\n\t" \
  "v_mul_f32 v117, v117, v116\n\t" \
  "v_max_f32 v117, 0x3f028f5c, v117\n\t" \
  "v_min_f32 v117, 0x3ffeb852, v117\n\t" \
  "v_rcp_f32 v116, v117\n\t" \
  "v_mul_f32 v117, 0x4f000000, v117\n\t" \
  "v_mul_f32 v116, 0x47800000, v116\n\t" \
  "v_cvt_u32_f32 v117, v117\n\t" \
  "v_cvt_u32_f32 v116, v116\n\t" \
  "s_mov_b32 s53, s93\n\t" \
  "v_readfirstlane_b32 s46, v117\n\t" \
  "v_readfirstlane_b32 s52, v116\n\t" \
  "21:\n\t" \
  "s_add_i32 s44, s44, 1\n\t" \
  "s_and_b32 s58, s58, 4\n\t" \
  "s_and_b32 s76, s44, 64\n\t" \
  "s_or_b32 s42, s42, s76\n\t" \
  "s_cbranch_scc0 20b\n\t" \
  "s_branch 99f\n\t" \
  "14:\n\t" \
  "s_sleep 3\n\t" \
  "s_add_u32 s59, s59, 0x10000\n\t" \
  "s_cmp_lt_u32 s59, 0x100000\n\t" \
  "s_cbranch_scc1 13b\n\t" \
  "s_mov_b64 exec, -1\n\t" \
  "s_mov_b32 s42, 16\n\t" \
  "s_branch 99f\n\t" \
  "8:\n\t" \
  "s_bitcmp1_b32 s58, 1\n\t" \
  "s_cbranch_scc1 4b\n\t" \
  "s_bitcmp1_b32 s58, 2\n\t" \
  "s_cbranch_scc1 5b\n\t" \
  "v_max3_u32 v116, v32, v33, v34\n\t" \
  "v_max3_u32 v117, v35, v36, v37\n\t" \
  "v_max3_u32 v116, v116, v38, v39\n\t" \
  "v_max_u32 v116, v116, v117\n\t" \
  "v_cmp_eq_u32 vcc, -1, v116\n\t" \
  "s_cbranch_vccz 5b\n\t" \
  "s_branch 0f\n\t" \
  "9:\n\t" \
  "s_bitcmp1_b32 s58, 2\n\t" \
  "s_cbranch_scc1 6b\n\t" \
  "v_max3_u32 v116, v40, v41, v42\n\t" \
  "v_max3_u32 v117, v43, v44, v45\n\t" \
  "v_max3_u32 v116, v116, v46, v47\n\t" \
  "v_max_u32 v116, v116, v117\n\t" \
  "v_cmp_eq_u32 vcc, -1, v116\n\t" \
  "s_cbranch_vccz 6b\n\t" \
  "s_or_b32 s58, s58, 2\n\t" \
  "0:\n\t" \
  "s_or_b32 s58, s58, 1\n\t" \
  "11:\n\t" \
  "buffer_load_dwordx4 v[32:35], v100, %[rh], s40 offen sc1\n\t" \
  "buffer_load_dwordx4 v[36:39], v100, %[rh], s76 offen sc1\n\t" \
  "buffer_load_dwordx4 v[40:43], v100, %[rh], s77 offen sc1\n\t" \
  "buffer_load_dwordx4 v[44:47], v100, %[rh], s78 offen sc1\n\t" \
  "s_waitcnt lgkmcnt(0)\n\t" \
  "s_branch 10b\n\t" \
  "17:\n\t" \
  "s_add_u32 s59, s59, 1\n\t" \
  "s_bitcmp1_b32 s58, 2\n\t" \
  "s_cbranch_scc1 3b\n\t" \
  "s_waitcnt vmcnt(0)\n\t" \
  "v_max_u32 v116, v32, v33\n\t" \
  "v_max3_u32 v116, v116, v34, v35\n\t" \
  "v_max3_u32 v116, v116, v36, v37\n\t" \
  "v_max3_u32 v116, v116, v38, v39\n\t" \
  "v_max3_u32 v116, v116, v40, v41\n\t" \
  "v_max3_u32 v116, v116, v42, v43\n\t" \
  "v_max3_u32 v116, v116, v44, v45\n\t" \
  "v_max3_u32 v116, v116, v46, v47\n\t" \
  "v_cmp_eq_u32 vcc, -1, v116\n\t" \
  "s_cbranch_vccz 3b\n\t" \
  "s_bitcmp1_b32 s59, 6\n\t" \
  "s_cbranch_scc1 12f\n\t" \
  "buffer_load_dwordx4 v[32:35], v100, %[rh], s40 offen sc1\n\t" \
  "buffer_load_dwordx4 v[36:39], v100, %[rh], s76 offen sc1\n\t" \
  "buffer_load_dwordx4 v[40:43], v100, %[rh], s77 offen sc1\n\t" \
  "buffer_load_dwordx4 v[44:47], v100, %[rh], s78 offen sc1\n\t" \
  "s_branch 17b\n\t" \
  "12:\n\t" \
  "s_mov_b32 s42, s58\n\t" \
  "s_branch 99f\n\t" \
  "93:\n\t" \
  "s_mov_b32 s58, s42\n\t" \
  "20:\n\t" \
  "s_and_b32 s76, s44, 7\n\t" \
  "s_lshl_b32 s76, s76, 15\n\t" \
  "s_add_i32 s40, s54, s76\n\t" \
  "s_and_b32 s76, s44, 3\n\t" \
  "s_lshl_b32 s45, s76, 14\n\t" \
  "s_lshl_b32 s47, s76, 20\n\t" \
  "s_add_i32 s47, s47, s55\n\t" \
  "s_lshr_b32 s76, s47, 21\n\t" \
  "s_bitcmp1_b64 %[sel], s76\n\t" \
  "s_cselect_b32 s76, 0x2000, 0\n\t" \
  "s_add_i32 s47, s47, s76\n\t" \
  "s_add_i32 s49, s44, -1\n\t" \
  "s_and_b32 s48, s49, 2\n\t" \
  "s_lshl_b32 s48, s48, 13\n\t" \
  "s_and_b32 s76, s49, 1\n\t" \
  "s_lshl_b32 s76, s76, 12\n\t" \
  "s_add_i32 s48, s48, s76\n\t" \
  "s_lshl_b32 s50, s53, 8\n\t" \
  "s_mov_b32 s59, 0\n\t" \
  "s_mov_b32 s92, s51\n\t" \
  "s_mov_b32 s93, s53\n\t" \
  "s_xor_b32 s76, s40, 0x400\n\t" \
  "s_xor_b32 s77, s40, 0x800\n\t" \
  "s_xor_b32 s78, s40, 0xc00\n\t" \
  "s_cmp_lg_u32 s58, 0\n\t" \
  "s_cbranch_scc1 11f\n\t" \
  "10:\n\t" \
  "ds_read_b128 v[48:51], v102 offset:49152\n\t" \
  "ds_read_b128 v[52:55], v102 offset:50176\n\t" \
  "ds_read_b128 v[56:59], v102 offset:51200\n\t" \
  "ds_read_b128 v[60:63], v102 offset:52224\n\t" \
  "ds_read_b128 v[0:3], v102 offset:0\n\t" \
  "ds_read_b128 v[4:7], v102 offset:1024\n\t" \
  "ds_read_b128 v[8:11], v102 offset:2048\n\t" \
  "ds_read_b128 v[12:15], v102 offset:3072\n\t" \
  "ds_read_b128 v[16:19], v102 offset:16384\n\t" \
  "ds_read_b128 v[20:23], v102 offset:17408\n\t" \
  "ds_read_b128 v[24:27], v102 offset:18432\n\t" \
  "ds_read_b128 v[28:31], v102 offset:19456\n\t" \
  "s_waitcnt lgkmcnt(4)\n\t" \
  "ds_read_b128 v[80:83], v102 offset:32768\n\t" \
  "ds_read_b128 v[84:87], v102 offset:33792\n\t" \
  "ds_read_b128 v[88:91], v102 offset:34816\n\t" \
  "ds_read_b128 v[92:95], v102 offset:35840\n\t" \
  "s_bitcmp1_b32 s58, 1\n\t" \
  "s_cselect_b64 s[80:81], -1, 0\n\t" \
  "s_cmp_lg_u32 s58, 0\n\t" \
  "s_cbranch_scc1 17f\n\t" \
  "s_mov_b32 exec_hi, 0\n\t" \
  "16:\n\t" \
  "s_memtime vcc\n\t" \
  "s_waitcnt lgkmcnt(0)\n\t" \
  "s_sub_i32 s79, s41, vcc_lo\n\t" \
  "s_cmp_gt_i32 s79, 64\n\t" \
  "s_cbranch_scc1 16b\n\t" \
  "13:\n\t" \
  "buffer_load_dword v116, v112, %[rh], s48 offen sc1\n\t" \
  "s_waitcnt vmcnt(0)\n\t" \
  "15:\n\t" \
  "v_mov_b32 v117, 20000\n\t" \
  "v_cmp_ne_u32_sdwa vcc, v116, s49 src0_sel:BYTE_0 src1_sel:DWORD\n\t" \
  "v_subrev_u32 v116, s50, v116\n\t" \
  "v_ashrrev_i32 v116, 8, v116\n\t" \
  "s_nop 1\n\t" \
  "v_max_i32_dpp v116, v116, v116 quad_perm:[1,0,3,2] row_mask:0xf bank_mask:0xf\n\t" \
  "s_cbranch_vccnz 14f\n\t" \
  "s_nop 0\n\t" \
  "v_max_i32_dpp v116, v116, v116 quad_perm:[2,3,0,1] row_mask:0xf bank_mask:0xf\n\t" \
  "s_nop 1\n\t" \
  "v_max_i32_dpp v116, v116, v116 row_half_mirror row_mask:0xf bank_mask:0xf\n\t" \
  "s_nop 1\n\t" \
  "v_max_i32_dpp v116, v116, v116 row_mirror row_mask:0xf bank_mask:0xf\n\t" \
  "v_med3_i32 v116, v116, 0, v117\n\t" \
  "v_mul_u32_u24 v116, s52, v116\n\t" \
  "v_lshrrev_b32 v116, 16, v116\n\t" \
  "s_mov_b64 exec, -1\n\t" \
  "v_readfirstlane_b32 s84, v116\n\t" \
  "v_readlane_b32 s85, v116, 16\n\t" \
  "s_max_u32 s84, s84, s85\n\t" \
  "s_add_i32 s84, s84, s51\n\t" \
  "1:\n\t" \
  "s_memtime vcc\n\t" \
  "s_waitcnt lgkmcnt(0)\n\t" \
  "s_sub_i32 s79, s84, vcc_lo\n\t" \
  "s_cmp_gt_i32 s79, 128\n\t" \
  "s_cbranch_scc1 1b\n\t" \
  ".p2align 4\n\t" \
  "2:\n\t" \
  "s_sub_i32 s79, s79, 33\n\t" \
  "s_cmp_gt_i32 s79, 56\n\t" \
  "s_cbranch_scc1 2b\n\t" \
  "buffer_load_dwordx4 v[32:35], v100, %[rh], s40 offen sc1\n\t" \
  "s_nop 12\n\t" \
  "buffer_load_dwordx4 v[36:39], v100, %[rh], s76 offen sc1\n\t" \
  "s_nop 12\n\t" \
  "buffer_load_dwordx4 v[40:43], v100, %[rh], s77 offen sc1\n\t" \
  "s_nop 12\n\t" \
  "buffer_load_dwordx4 v[44:47], v100, %[rh], s78 offen sc1\n\t" \
  "s_memtime vcc\n\t" \
  "s_memrealtime s[82:83]\n\t" \
  "s_nop 4\n\t" \
  "s_memrealtime s[84:85]\n\t" \
  "s_nop 4\n\t" \
  "s_memrealtime s[56:57]\n\t" \
  "s_nop 4\n\t" \
  "s_memrealtime s[86:87]\n\t" \
  "s_nop 4\n\t" \
  "s_memrealtime s[88:89]\n\t" \
  "s_nop 4\n\t" \
  "s_memrealtime s[90:91]\n\t" \
  "s_mov_b32 s92, 0\n\t" \
  "s_waitcnt lgkmcnt(0)\n\t" \
  "s_cmp_eq_u32 s82, s84\n\t" \
  "s_addc_u32 s92, s92, 0\n\t" \
  "s_cmp_eq_u32 s82, s56\n\t" \
  "s_addc_u32 s92, s92, 0\n\t" \
  "s_cmp_eq_u32 s82, s86\n\t" \
  "s_addc_u32 s92, s92, 0\n\t" \
  "s_cmp_eq_u32 s82, s88\n\t" \
  "s_addc_u32 s92, s92, 0\n\t" \
  "s_cmp_eq_u32 s82, s90\n\t" \
  "s_addc_u32 s92, s92, 0\n\t" \
  "s_mul_i32 s92, s92, 24\n\t" \
  "s_add_i32 s92, s92, vcc_lo\n\t" \
  "s_lshl4_add_u32 s93, s82, 64\n\t" \
  "3:\n\t" \
  "v_mov_b64 v[72:73], 0\n\t" \
  "v_mov_b64 v[74:75], 0\n\t" \
  "v_mov_b64 v[76:77], 0\n\t" \
  "v_mov_b64 v[78:79], 0\n\t" \
  "s_waitcnt lgkmcnt(0)\n\t" \
  "s_waitcnt vmcnt(3)\n\t" \
  "v_smfmac_f32_16x16x64_bf16 v[72:75], v[32:35], v[48:55], v104\n\t" \
  "ds_write_b128 v101, v[32:35] offset:12288\n\t" \
  "v_smfmac_f32_16x16x64_bf16 v[76:79], v[32:35], v[56:63], v104\n\t" \
  "s_waitcnt vmcnt(2)\n\t" \
  "ds_write_b128 v101, v[36:39] offset:13312\n\t" \
  "v_smfmac_f32_16x16x64_bf16 v[72:75], v[36:39], a[144:151], v104\n\t" \
  "v_smfmac_f32_16x16x64_bf16 v[76:79], v[36:39], a[152:159], v104\n\t" \
  "s_nop 3\n\t" \
  "v_cmp_u_f32 vcc, v72, v73\n\t" \
  "v_cmp_u_f32 s[82:83], v74, v75\n\t" \
  "s_waitcnt vmcnt(1)\n\t" \
  "v_smfmac_f32_16x16x64_bf16 v[72:75], v[40:43], a[160:167], v104\n\t" \
  "s_or_b64 vcc, vcc, s[82:83]\n\t" \
  "s_or_b64 vcc, vcc, s[80:81]\n\t" \
  "v_smfmac_f32_16x16x64_bf16 v[76:79], v[40:43], a[168:175], v104\n\t" \
  "s_waitcnt lgkmcnt(0)\n\t" \
  "s_cbranch_scc1 8f\n\t" \
  "5:\n\t" \
  "s_barrier\n\t" \
  "4:\n\t" \
  "s_memtime s[56:57]\n\t" \
  "ds_read_b128 v[48:51], v101 offset:0\n\t" \
  "ds_read_b128 v[52:55], v101 offset:1024\n\t" \
  "ds_read_b128 v[56:59], v101 offset:5120\n\t" \
  "s_waitcnt vmcnt(0)\n\t" \
  "v_smfmac_f32_16x16x64_bf16 v[72:75], v[44:47], a[176:183], v104\n\t" \
  "ds_read_b128 v[60:63], v101 offset:4096\n\t" \
  "ds_read_b128 v[64:67], v101 offset:9216\n\t" \
  "v_smfmac_f32_16x16x64_bf16 v[76:79], v[44:47], a[184:191], v104\n\t" \
  "ds_read_b128 v[68:71], v101 offset:8192\n\t" \
  "ds_write_b128 v101, v[40:43] offset:14336\n\t" \
  "s_nop 1\n\t" \
  "v_cmp_u_f32 vcc, v72, v73\n\t" \
  "v_cmp_u_f32 s[82:83], v74, v75\n\t" \
  "s_waitcnt lgkmcnt(6)\n\t" \
  "v_smfmac_f32_16x16x64_bf16 v[72:75], v[48:51], v[0:7], v104\n\t" \
  "s_or_b64 vcc, vcc, s[82:83]\n\t" \
  "v_smfmac_f32_16x16x64_bf16 v[76:79], v[48:51], v[8:15], v104\n\t" \
  "s_cbranch_scc1 9f\n\t" \
  "6:\n\t" \
  "s_waitcnt lgkmcnt(5)\n\t" \
  "v_smfmac_f32_16x16x64_bf16 v[72:75], v[52:55], a[0:7], v104\n\t" \
  "ds_write_b128 v101, v[44:47] offset:15360\n\t" \
  "v_smfmac_f32_16x16x64_bf16 v[76:79], v[52:55], a[8:15], v104\n\t" \
  "v_mov_b64 v[40:41], 0\n\t" \
  "s_waitcnt lgkmcnt(5)\n\t" \
  "v_smfmac_f32_16x16x64_bf16 v[72:75], v[56:59], a[48:55], v104\n\t" \
  "v_mov_b64 v[42:43], 0\n\t" \
  "v_smfmac_f32_16x16x64_bf16 v[76:79], v[56:59], a[56:63], v104\n\t" \
  "s_waitcnt lgkmcnt(4)\n\t" \
  "v_smfmac_f32_16x16x64_bf16 v[72:75], v[60:63], v[16:23], v104\n\t" \
  "s_waitcnt lgkmcnt(0)\n\t" \
  "s_barrier\n\t" \
  "v_smfmac_f32_16x16x64_bf16 v[76:79], v[60:63], v[24:31], v104\n\t" \
  "ds_read_b128 v[48:51], v101 offset:2048\n\t" \
  "ds_read_b128 v[52:55], v101 offset:6144\n\t" \
  "v_smfmac_f32_16x16x64_bf16 v[72:75], v[64:67], a[96:103], v104\n\t" \
  "ds_read_b128 v[56:59], v101 offset:10240\n\t" \
  "ds_read_b128 v[60:63], v101 offset:3072\n\t" \
  "v_smfmac_f32_16x16x64_bf16 v[76:79], v[64:67], a[104:111], v104\n\t" \
  "ds_read_b128 v[32:35], v101 offset:7168\n\t" \
  "ds_read_b128 v[36:39], v101 offset:11264\n\t" \
  "v_smfmac_f32_16x16x64_bf16 v[72:75], v[68:71], v[80:87], v104\n\t" \
  "ds_read_b128 v[0:3], v103 offset:0\n\t" \
  "v_mov_b64 v[44:45], 0\n\t" \
  "v_smfmac_f32_16x16x64_bf16 v[76:79], v[68:71], v[88:95], v104\n\t" \
  "v_mov_b64 v[46:47], 0\n\t" \
  "s_waitcnt lgkmcnt(5)\n\t" \
  "v_smfmac_f32_16x16x64_bf16 v[72:75], v[48:51], a[16:23], v104\n\t" \
  "ds_read_b128 v[4:7], v103 offset:1024\n\t" \
  "v_mov_b64 v[80:81], 0\n\t" \
  "v_smfmac_f32_16x16x64_bf16 v[72:75], v[52:55], a[64:71], v104\n\t" \
  "v_mov_b64 v[82:83], 0\n\t" \
  "s_waitcnt lgkmcnt(2)\n\t" \
  "v_smfmac_f32_16x16x64_bf16 v[72:75], v[56:59], a[112:119], v104\n\t" \
  "ds_read_b128 v[8:11], v103 offset:2048\n\t" \
  "v_mov_b64 v[84:85], 0\n\t" \
  "v_smfmac_f32_16x16x64_bf16 v[72:75], v[60:63], a[32:39], v104\n\t" \
  "ds_read_b128 v[12:15], v103 offset:3072\n\t" \
  "v_mov_b64 v[86:87], 0\n\t" \
  "v_smfmac_f32_16x16x64_bf16 v[72:75], v[32:35], a[80:87], v104\n\t" \
  "ds_read_b128 v[16:19], v103 offset:16384\n\t" \
  "v_mov_b64 v[88:89], 0\n\t" \
  "v_smfmac_f32_16x16x64_bf16 v[72:75], v[36:39], a[128:135], v104\n\t" \
  "ds_read_b128 v[20:23], v103 offset:17408\n\t" \
  "v_mov_b64 v[90:91], 0\n\t" \
  "v_smfmac_f32_16x16x64_bf16 v[76:79], v[48:51], a[24:31], v104\n\t" \
  "ds_read_b128 v[24:27], v103 offset:18432\n\t" \
  "v_mov_b64 v[92:93], 0\n\t" \
  "v_smfmac_f32_16x16x64_bf16 v[76:79], v[52:55], a[72:79], v104\n\t" \
  "v_add_f32 v96, v72, v74\n\t" \
  "v_add_f32 v97, v73, v75\n\t" \
  "v_cvt_pk_bf16_f32 v96, v96, v97\n\t" \
  "ds_write_b32 v105, v96\n\t" \
  "v_smfmac_f32_16x16x64_bf16 v[76:79], v[56:59], a[120:127], v104\n\t" \
  "ds_read_b128 v[28:31], v103 offset:19456\n\t" \
  "v_mov_b64 v[94:95], 0\n\t" \
  "v_smfmac_f32_16x16x64_bf16 v[76:79], v[60:63], a[40:47], v104\n\t" \
  "v_smfmac_f32_16x16x64_bf16 v[76:79], v[32:35], a[88:95], v104\n\t" \
  "v_smfmac_f32_16x16x64_bf16 v[76:79], v[36:39], a[136:143], v104\n\t" \
  "s_waitcnt lgkmcnt(1)\n\t" \
  "s_barrier\n\t" \
  "ds_read_b64_tr_b16 v[96:97], v107 offset:0\n\t" \
  "ds_read_b64_tr_b16 v[98:99], v107 offset:64\n\t" \
  "ds_read_b64_tr_b16 v[56:57], v107 offset:1024\n\t" \
  "ds_read_b64_tr_b16 v[58:59], v107 offset:1088\n\t" \
  "s_nop 1\n\t" \
  "v_add_f32 v116, v76, v78\n\t" \
  "v_add_f32 v117, v77, v79\n\t" \
  "v_cvt_pk_bf16_f32 v116, v116, v117\n\t" \
  "ds_write_b32 v105, v116 offset:256\n\t" \
  "s_waitcnt lgkmcnt(3)\n\t" \
  "v_smfmac_f32_16x16x64_bf16 v[40:43], v[96:99], a[192:199], v106\n\t" \
  "v_smfmac_f32_16x16x64_bf16 v[44:47], v[96:99], a[224:231], v106\n\t" \
  "s_waitcnt lgkmcnt(0)\n\t" \
  "s_barrier\n\t" \
  "ds_read_b64_tr_b16 v[52:53], v107 offset:256\n\t" \
  "ds_read_b64_tr_b16 v[54:55], v107 offset:320\n\t" \
  "v_smfmac_f32_16x16x64_bf16 v[40:43], v[56:59], a[208:215], v106\n\t" \
  "ds_read_b64_tr_b16 v[60:61], v107 offset:1280\n\t" \
  "ds_read_b64_tr_b16 v[62:63], v107 offset:1344\n\t" \
  "v_smfmac_f32_16x16x64_bf16 v[44:47], v[56:59], v[0:7], v106\n\t" \
  "v_smfmac_f32_16x16x64_bf16 v[80:83], v[96:99], a[240:247], v106\n\t" \
  "v_mov_b64 v[32:33], 0\n\t" \
  "v_smfmac_f32_16x16x64_bf16 v[84:87], v[96:99], v[136:143], v106\n\t" \
  "v_mov_b64 v[34:35], 0\n\t" \
  "v_smfmac_f32_16x16x64_bf16 v[80:83], v[56:59], v[120:127], v106\n\t" \
  "v_mov_b64 v[36:37], 0\n\t" \
  "v_smfmac_f32_16x16x64_bf16 v[84:87], v[56:59], v[16:23], v106\n\t" \
  "s_waitcnt lgkmcnt(2)\n\t" \
  "v_smfmac_f32_16x16x64_bf16 v[40:43], v[52:55], a[200:207], v106\n\t" \
  "v_smfmac_f32_16x16x64_bf16 v[44:47], v[52:55], a[232:239], v106\n\t" \
  "s_xor_b32 s76, s47, 0x40000\n\t" \
  "s_waitcnt lgkmcnt(0)\n\t" \
  "v_smfmac_f32_16x16x64_bf16 v[40:43], v[60:63], a[216:223], v106\n\t" \
  "v_mov_b64 v[38:39], 0\n\t" \
  "s_xor_b32 s77, s47, 0x80000\n\t" \
  "v_smfmac_f32_16x16x64_bf16 v[44:47], v[60:63], v[8:15], v106\n\t" \
  "s_xor_b32 s78, s47, 0xc0000\n\t" \
  "v_smfmac_f32_16x16x64_bf16 v[80:83], v[52:55], a[248:255], v106\n\t" \
  "ds_read_b128 v[0:3], v103 offset:32768\n\t" \
  "v_cvt_pk_bf16_f32 v64, v40, v41\n\t" \
  "v_smfmac_f32_16x16x64_bf16 v[84:87], v[52:55], v[144:151], v106\n\t" \
  "ds_read_b128 v[4:7], v103 offset:33792\n\t" \
  "v_cvt_pk_bf16_f32 v65, v42, v43\n\t" \
  "v_smfmac_f32_16x16x64_bf16 v[80:83], v[60:63], v[128:135], v106\n\t" \
  "ds_read_b128 v[8:11], v103 offset:34816\n\t" \
  "v_cvt_pk_bf16_f32 v66, v44, v45\n\t" \
  "v_smfmac_f32_16x16x64_bf16 v[84:87], v[60:63], v[24:31], v106\n\t" \
  "ds_read_b128 v[12:15], v103 offset:35840\n\t" \
  "v_cvt_pk_bf16_f32 v67, v46, v47\n\t" \
  "v_smfmac_f32_16x16x64_bf16 v[88:91], v[96:99], v[152:159], v106\n\t" \
  "v_permlane16_swap_b32 v64, v66\n\t" \
  "v_cvt_pk_bf16_f32 v68, v80, v81\n\t" \
  "v_smfmac_f32_16x16x64_bf16 v[92:95], v[96:99], v[184:191], v106\n\t" \
  "v_permlane16_swap_b32 v65, v67\n\t" \
  "v_cvt_pk_bf16_f32 v69, v82, v83\n\t" \
  "v_smfmac_f32_16x16x64_bf16 v[88:91], v[52:55], v[160:167], v106\n\t" \
  "v_cvt_pk_bf16_f32 v70, v84, v85\n\t" \
  "v_smfmac_f32_16x16x64_bf16 v[92:95], v[52:55], v[192:199], v106\n\t" \
  "v_cvt_pk_bf16_f32 v71, v86, v87\n\t" \
  "v_smfmac_f32_16x16x64_bf16 v[88:91], v[56:59], v[168:175], v106\n\t" \
  "ds_read_b128 v[16:19], v103 offset:49152\n\t" \
  "buffer_store_dwordx4 v[64:67], v108, %[ru], s47 offen sc1\n\t" \
  "v_permlane16_swap_b32 v68, v70\n\t" \
  "s_waitcnt lgkmcnt(3)\n\t" \
  "v_smfmac_f32_16x16x64_bf16 v[92:95], v[56:59], v[0:7], v106\n\t" \
  "ds_read_b128 v[20:23], v103 offset:50176\n\t" \
  "v_permlane16_swap_b32 v69, v71\n\t" \
  "v_smfmac_f32_16x16x64_bf16 v[88:91], v[60:63], v[176:183], v106\n\t" \
  "ds_read_b128 v[24:27], v103 offset:51200\n\t" \
  "s_waitcnt lgkmcnt(3)\n\t" \
  "v_smfmac_f32_16x16x64_bf16 v[92:95], v[60:63], v[8:15], v106\n\t" \
  "ds_read_b128 v[28:31], v103 offset:52224\n\t" \
  "v_smfmac_f32_16x16x64_bf16 v[32:35], v[96:99], v[200:207], v106\n\t" \
  "buffer_store_dwordx4 v[68:71], v108, %[ru], s76 offen sc1\n\t" \
  "v_cvt_pk_bf16_f32 v72, v88, v89\n\t" \
  "v_smfmac_f32_16x16x64_bf16 v[36:39], v[96:99], v[232:239], v106\n\t" \
  "v_cvt_pk_bf16_f32 v73, v90, v91\n\t" \
  "v_smfmac_f32_16x16x64_bf16 v[32:35], v[52:55], v[208:215], v106\n\t" \
  "v_cvt_pk_bf16_f32 v74, v92, v93\n\t" \
  "v_smfmac_f32_16x16x64_bf16 v[36:39], v[52:55], v[240:247], v106\n\t" \
  "v_cvt_pk_bf16_f32 v75, v94, v95\n\t" \
  "v_smfmac_f32_16x16x64_bf16 v[32:35], v[56:59], v[216:223], v106\n\t" \
  "v_permlane16_swap_b32 v72, v74\n\t" \
  "v_smfmac_f32_16x16x64_bf16 v[32:35], v[60:63], v[224:231], v106\n\t" \
  "v_permlane16_swap_b32 v73, v75\n\t" \
  "s_waitcnt lgkmcnt(2)\n\t" \
  "v_smfmac_f32_16x16x64_bf16 v[36:39], v[56:59], v[16:23], v106\n\t" \
  "s_waitcnt lgkmcnt(0)\n\t" \
  "v_smfmac_f32_16x16x64_bf16 v[36:39], v[60:63], v[24:31], v106\n\t" \
  "buffer_store_dwordx4 v[72:75], v108, %[ru], s77 offen sc1\n\t" \
  "v_cvt_pk_bf16_f32 v76, v32, v33\n\t" \
  "v_cvt_pk_bf16_f32 v77, v34, v35\n\t" \
  "s_nop 5\n\t" \
  "v_cvt_pk_bf16_f32 v78, v36, v37\n\t" \
  "v_cvt_pk_bf16_f32 v79, v38, v39\n\t" \
  "s_nop 0\n\t" \
  "v_permlane16_swap_b32 v76, v78\n\t" \
  "v_permlane16_swap_b32 v77, v79\n\t" \
  "buffer_store_dwordx4 v[76:79], v108, %[ru], s78 offen sc1\n\t" \
  "s_memtime s[76:77]\n\t" \
  "s_mov_b32 s42, 0\n\t" \
  "s_waitcnt lgkmcnt(0)\n\t" \
  "s_mov_b32 s57, s76\n\t" \
  "s_add_i32 s41, s56, 1950\n\t" \
  "s_sub_i32 s76, s92, s51\n\t" \
  "s_sub_i32 s77, s93, s53\n\t" \
  "s_cmp_eq_u32 s76, 0\n\t" \
  "s_cbranch_scc1 21f\n\t" \
  "v_cvt_f32_u32 v117, s76\n\t" \
  "v_cvt_f32_u32 v116, s77\n\t" \
  "v_rcp_f32 v117, v117\n\t" \
  "s_mov_b32 s51, s92\n\t" \
  "v_mul_f32 v117, v117, v116\n\t" \
  "v_max_f32 v117, 0x3f028f5c, v117\n\t" \
  "v_min_f32 v117, 0x3ffeb852, v117\n\t" \
  "v_rcp_f32 v116, v117\n\t" \
  "v_mul_f32 v117, 0x4f000000, v117\n\t" \
  "v_mul_f32 v116, 0x47800000, v116\n\t" \
  "v_cvt_u32_f32 v117, v117\n\t" \
  "v_cvt_u32_f32 v116, v116\n\t" \
  "s_mov_b32 s53, s93\n\t" \
  "v_readfirstlane_b32 s46, v117\n\t" \
  "v_readfirstlane_b32 s52, v116\n\t" \
  "21:\n\t" \
  "s_add_i32 s44, s44, 1\n\t" \
  "s_and_b32 s58, s58, 4\n\t" \
  "s_and_b32 s76, s44, 64\n\t" \
  "s_or_b32 s42, s42, s76\n\t" \
  "s_cbranch_scc0 20b\n\t" \
  "s_branch 99f\n\t" \
  "14:\n\t" \
  "s_sleep 3\n\t" \
  "s_add_u32 s59, s59, 0x10000\n\t" \
  "s_cmp_lt_u32 s59, 0x100000\n\t" \
  "s_cbranch_scc1 13b\n\t" \
  "s_mov_b64 exec, -1\n\t" \
  "s_mov_b32 s42, 16\n\t" \
  "s_branch 99f\n\t" \
  "8:\n\t" \
  "s_bitcmp1_b32 s58, 1\n\t" \
  "s_cbranch_scc1 4b\n\t" \
  "s_bitcmp1_b32 s58, 2\n\t" \
  "s_cbranch_scc1 5b\n\t" \
  "v_max3_u32 v116, v32, v33, v34\n\t" \
  "v_max3_u32 v117, v35, v36, v37\n\t" \
  "v_max3_u32 v116, v116, v38, v39\n\t" \
  "v_max_u32 v116, v116, v117\n\t" \
  "v_cmp_eq_u32 vcc, -1, v116\n\t" \
  "s_cbranch_vccz 5b\n\t" \
  "s_branch 0f\n\t" \
  "9:\n\t" \
  "s_bitcmp1_b32 s58, 2\n\t" \
  "s_cbranch_scc1 6b\n\t" \
  "v_max3_u32 v116, v40, v41, v42\n\t" \
  "v_max3_u32 v117, v43, v44, v45\n\t" \
  "v_max3_u32 v116, v116, v46, v47\n\t" \
  "v_max_u32 v116, v116, v117\n\t" \
  "v_cmp_eq_u32 vcc, -1, v116\n\t" \
  "s_cbranch_vccz 6b\n\t" \
  "s_or_b32 s58, s58, 2\n\t" \
  "0:\n\t" \
  "s_or_b32 s58, s58, 1\n\t" \
  "11:\n\t" \
  "buffer_load_dwordx4 v[32:35], v100, %[rh], s40 offen sc1\n\t" \
  "buffer_load_dwordx4 v[36:39], v100, %[rh], s76 offen sc1\n\t" \
  "buffer_load_dwordx4 v[40:43], v100, %[rh], s77 offen sc1\n\t" \
  "buffer_load_dwordx4 v[44:47], v100, %[rh], s78 offen sc1\n\t" \
  "s_waitcnt lgkmcnt(0)\n\t" \
  "s_branch 10b\n\t" \
  "17:\n\t" \
  "s_add_u32 s59, s59, 1\n\t" \
  "s_bitcmp1_b32 s58, 2\n\t" \
  "s_cbranch_scc1 3b\n\t" \
  "s_waitcnt vmcnt(0)\n\t" \
  "v_max_u32 v116, v32, v33\n\t" \
  "v_max3_u32 v116, v116, v34, v35\n\t" \
  "v_max3_u32 v116, v116, v36, v37\n\t" \
  "v_max3_u32 v116, v116, v38, v39\n\t" \
  "v_max3_u32 v116, v116, v40, v41\n\t" \
  "v_max3_u32 v116, v116, v42, v43\n\t" \
  "v_max3_u32 v116, v116, v44, v45\n\t" \
  "v_max3_u32 v116, v116, v46, v47\n\t" \
  "v_cmp_eq_u32 vcc, -1, v116\n\t" \
  "s_cbranch_vccz 3b\n\t" \
  "s_bitcmp1_b32 s59, 6\n\t" \
  "s_cbranch_scc1 12f\n\t" \
  "buffer_load_dwordx4 v[32:35], v100, %[rh], s40 offen sc1\n\t" \
  "buffer_load_dwordx4 v[36:39], v100, %[rh], s76 offen sc1\n\t" \
  "buffer_load_dwordx4 v[40:43], v100, %[rh], s77 offen sc1\n\t" \
  "buffer_load_dwordx4 v[44:47], v100, %[rh], s78 offen sc1\n\t" \
  "s_branch 17b\n\t" \
  "12:\n\t" \
  "s_mov_b32 s42, s58\n\t" \
  "s_branch 99f\n\t" \
  "99:\n\t" \
  "s_waitcnt vmcnt(0) lgkmcnt(0)\n\t" \
  ".Lchain_last:\n\t" \
  ""
#define CB_BODY \
  "s_cmp_gt_u32 %[wv], 1\n\t" \
  "s_cbranch_scc1 91f\n\t" \
  "s_mov_b32 s58, s42\n\t" \
  "s_mov_b32 s59, 0\n\t" \
  "s_bitcmp1_b32 s58, 4\n\t" \
  "s_cbranch_scc1 26f\n\t" \
  "s_bitcmp1_b32 s58, 3\n\t" \
  "s_cbranch_scc1 34f\n\t" \
  "s_bitcmp1_b32 s58, 1\n\t" \
  "s_cbranch_scc1 18f\n\t" \
  "17:\n\t" \
  "19:\n\t" \
  "s_add_i32 s85, s40, -1\n\t" \
  "s_and_b32 s82, s85, 2\n\t" \
  "s_lshl_b32 s82, s82, 13\n\t" \
  "s_and_b32 s80, s85, 1\n\t" \
  "s_lshl_b32 s80, s80, 12\n\t" \
  "s_add_i32 s82, s82, s80\n\t" \
  "s_add_i32 s82, s82, 0x84000\n\t" \
  "s_mov_b32 exec_hi, 0\n\t" \
  "buffer_load_dword v184, v200, %[rh], s82 offen sc1\n\t" \
  "buffer_load_dword v186, v203, %[ru], 0 offen sc1\n\t" \
  "s_mov_b64 exec, -1\n\t" \
  "20:\n\t" \
  "s_and_b32 s80, s40, 3\n\t" \
  "s_mul_i32 s77, s80, 0xc000\n\t" \
  "s_add_i32 s77, s77, 0x40000\n\t" \
  "s_lshl_b32 s78, s80, 14\n\t" \
  "s_add_i32 s78, s78, 0x70000\n\t" \
  "s_mov_b32 s57, 0\n\t" \
  "s_add_i32 s96, s77, 0x1000\n\t" \
  "s_lshl_b32 s88, s80, 20\n\t" \
  "s_add_i32 s88, s88, s48\n\t" \
  "s_lshr_b32 s80, s88, 21\n\t" \
  "s_bitcmp1_b64 %[sel], s80\n\t" \
  "s_cselect_b32 s80, 0x2000, 0\n\t" \
  "s_add_i32 s88, s88, s80\n\t" \
  "s_xor_b32 s89, s88, 0x800\n\t" \
  "s_xor_b32 s90, s88, 0x1000\n\t" \
  "s_xor_b32 s91, s88, 0x1800\n\t" \
  "s_add_i32 s92, s77, s50\n\t" \
  "s_xor_b32 s93, s92, 0x100\n\t" \
  "s_xor_b32 s94, s92, 0x200\n\t" \
  "s_xor_b32 s95, s92, 0x300\n\t" \
  "s_add_i32 s97, s40, 1\n\t" \
  "s_and_b32 s97, s97, 7\n\t" \
  "s_lshl_b32 s97, s97, 15\n\t" \
  "s_add_i32 s97, s97, s49\n\t" \
  "s_memtime vcc\n\t" \
  "s_memrealtime s[86:87]\n\t" \
  "s_nop 4\n\t" \
  "s_memrealtime s[60:61]\n\t" \
  "s_nop 4\n\t" \
  "s_memrealtime s[62:63]\n\t" \
  "s_nop 4\n\t" \
  "s_memrealtime s[64:65]\n\t" \
  "s_nop 4\n\t" \
  "s_memrealtime s[66:67]\n\t" \
  "s_nop 4\n\t" \
  "s_memrealtime s[68:69]\n\t" \
  "s_mov_b32 s79, 0\n\t" \
  "s_waitcnt lgkmcnt(0)\n\t" \
  "s_cmp_eq_u32 s86, s60\n\t" \
  "s_addc_u32 s79, s79, 0\n\t" \
  "s_cmp_eq_u32 s86, s62\n\t" \
  "s_addc_u32 s79, s79, 0\n\t" \
  "s_cmp_eq_u32 s86, s64\n\t" \
  "s_addc_u32 s79, s79, 0\n\t" \
  "s_cmp_eq_u32 s86, s66\n\t" \
  "s_addc_u32 s79, s79, 0\n\t" \
  "s_cmp_eq_u32 s86, s68\n\t" \
  "s_addc_u32 s79, s79, 0\n\t" \
  "s_mul_i32 s79, s79, 24\n\t" \
  "s_add_i32 s79, s79, vcc_lo\n\t" \
  "s_lshl4_add_u32 s80, s86, 64\n\t" \
  "s_sub_i32 s86, s79, s41\n\t" \
  "s_sub_i32 s87, s80, s43\n\t" \
  "s_cmp_lt_u32 s86, 4000\n\t" \
  "s_cbranch_scc1 21f\n\t" \
  "v_cvt_f32_u32 v165, s86\n\t" \
  "v_cvt_f32_u32 v164, s87\n\t" \
  "v_rcp_f32 v165, v165\n\t" \
  "s_nop 0\n\t" \
  "v_mul_f32 v165, v165, v164\n\t" \
  "v_max_f32 v165, 0x3f028f5c, v165\n\t" \
  "v_min_f32 v165, 0x3ffeb852, v165\n\t" \
  "v_rcp_f32 v164, v165\n\t" \
  "v_mul_f32 v165, 0x4f000000, v165\n\t" \
  "v_mul_f32 v164, 0x47800000, v164\n\t" \
  "v_cvt_u32_f32 v165, v165\n\t" \
  "v_cvt_u32_f32 v164, v164\n\t" \
  "s_nop 0\n\t" \
  "v_readfirstlane_b32 s45, v165\n\t" \
  "v_readfirstlane_b32 s44, v164\n\t" \
  "21:\n\t" \
  "s_mov_b32 s41, s79\n\t" \
  "s_mov_b32 s43, s80\n\t" \
  "s_lshl_b32 s83, s43, 8\n\t" \
  "s_mov_b32 s84, 650\n\t" \
  "s_mov_b32 exec_hi, 0\n\t" \
  "s_waitcnt vmcnt(0)\n\t" \
  "v_cmp_ne_u32_sdwa vcc, v184, s85 src0_sel:BYTE_0 src1_sel:DWORD\n\t" \
  "v_subrev_u32 v184, s83, v184\n\t" \
  "v_ashrrev_i32 v184, 8, v184\n\t" \
  "s_cbranch_vccnz 22f\n\t" \
  "s_nop 0\n\t" \
  "v_max_i32_dpp v184, v184, v184 quad_perm:[1,0,3,2] row_mask:0xf bank_mask:0xf\n\t" \
  "s_nop 1\n\t" \
  "v_max_i32_dpp v184, v184, v184 quad_perm:[2,3,0,1] row_mask:0xf bank_mask:0xf\n\t" \
  "s_nop 1\n\t" \
  "v_max_i32_dpp v184, v184, v184 row_half_mirror row_mask:0xf bank_mask:0xf\n\t" \
  "s_nop 1\n\t" \
  "v_max_i32_dpp v184, v184, v184 row_mirror row_mask:0xf bank_mask:0xf\n\t" \
  "s_nop 0\n\t" \
  "v_readlane_b32 s81, v184, 0\n\t" \
  "v_readlane_b32 s82, v184, 16\n\t" \
  "s_max_i32 s81, s81, s82\n\t" \
  "s_add_i32 s81, s81, 1056\n\t" \
  "s_max_i32 s81, s81, 0\n\t" \
  "s_min_i32 s81, s81, 20000\n\t" \
  "s_mul_i32 s81, s81, s44\n\t" \
  "s_lshr_b32 s81, s81, 16\n\t" \
  "24:\n\t" \
  "s_add_i32 s81, s81, s41\n\t" \
  "1:\n\t" \
  "s_memtime vcc\n\t" \
  "s_waitcnt lgkmcnt(0)\n\t" \
  "s_sub_i32 s79, s81, vcc_lo\n\t" \
  "s_cmp_gt_i32 s79, 128\n\t" \
  "s_cbranch_scc1 1b\n\t" \
  ".p2align 4\n\t" \
  "2:\n\t" \
  "s_sub_i32 s79, s79, 33\n\t" \
  "s_cmp_gt_i32 s79, 56\n\t" \
  "s_cbranch_scc1 2b\n\t" \
  "buffer_load_dword v187, v200, %[rh], s78 offen sc1\n\t" \
  "s_memtime vcc\n\t" \
  "s_memrealtime s[86:87]\n\t" \
  "s_nop 4\n\t" \
  "s_memrealtime s[60:61]\n\t" \
  "s_nop 4\n\t" \
  "s_memrealtime s[62:63]\n\t" \
  "s_nop 4\n\t" \
  "s_memrealtime s[64:65]\n\t" \
  "s_nop 4\n\t" \
  "s_memrealtime s[66:67]\n\t" \
  "s_nop 4\n\t" \
  "s_memrealtime s[68:69]\n\t" \
  "s_mov_b32 s79, 0\n\t" \
  "s_waitcnt lgkmcnt(0)\n\t" \
  "s_cmp_eq_u32 s86, s60\n\t" \
  "s_addc_u32 s79, s79, 0\n\t" \
  "s_cmp_eq_u32 s86, s62\n\t" \
  "s_addc_u32 s79, s79, 0\n\t" \
  "s_cmp_eq_u32 s86, s64\n\t" \
  "s_addc_u32 s79, s79, 0\n\t" \
  "s_cmp_eq_u32 s86, s66\n\t" \
  "s_addc_u32 s79, s79, 0\n\t" \
  "s_cmp_eq_u32 s86, s68\n\t" \
  "s_addc_u32 s79, s79, 0\n\t" \
  "s_mul_i32 s79, s79, 24\n\t" \
  "s_add_i32 s79, s79, vcc_lo\n\t" \
  "s_lshl4_add_u32 s80, s86, 64\n\t" \
  "s_mov_b32 s41, s79\n\t" \
  "s_mov_b32 s43, s80\n\t" \
  "s_lshl_b32 s83, s43, 8\n\t" \
  "13:\n\t" \
  "s_waitcnt vmcnt(0)\n\t" \
  "s_memtime s[86:87]\n\t" \
  "v_cmp_ne_u32_sdwa vcc, v187, s40 src0_sel:BYTE_0 src1_sel:DWORD\n\t" \
  "v_subrev_u32 v184, s83, v187\n\t" \
  "v_ashrrev_i32 v184, 8, v184\n\t" \
  "s_nop 1\n\t" \
  "v_max_i32_dpp v184, v184, v184 quad_perm:[1,0,3,2] row_mask:0xf bank_mask:0xf\n\t" \
  "s_nop 1\n\t" \
  "v_max_i32_dpp v184, v184, v184 quad_perm:[2,3,0,1] row_mask:0xf bank_mask:0xf\n\t" \
  "s_cbranch_vccnz 14f\n\t" \
  "15:\n\t" \
  "s_nop 0\n\t" \
  "v_max_i32_dpp v184, v184, v184 row_half_mirror row_mask:0xf bank_mask:0xf\n\t" \
  "s_nop 1\n\t" \
  "v_max_i32_dpp v184, v184, v184 row_mirror row_mask:0xf bank_mask:0xf\n\t" \
  "s_mov_b64 exec, -1\n\t" \
  "v_readlane_b32 s57, v184, 0\n\t" \
  "v_readlane_b32 s82, v184, 16\n\t" \
  "s_waitcnt lgkmcnt(0)\n\t" \
  "s_sub_i32 s79, s41, s86\n\t" \
  "s_max_i32 s57, s57, s82\n\t" \
  "s_add_i32 s81, s57, 215\n\t" \
  "s_max_i32 s81, s81, 0\n\t" \
  "s_min_i32 s81, s81, 20000\n\t" \
  "s_mul_i32 s81, s81, s44\n\t" \
  "s_lshr_b32 s81, s81, 16\n\t" \
  "s_add_i32 s79, s79, s81\n\t" \
  "s_add_i32 s79, s79, -370\n\t" \
  ".p2align 4\n\t" \
  "3:\n\t" \
  "s_sub_i32 s79, s79, 33\n\t" \
  "s_cmp_gt_i32 s79, 56\n\t" \
  "s_cbranch_scc1 3b\n\t" \
  "buffer_load_dwordx4 v[72:75], v192, %[ru], s88 offen offset:0 sc1\n\t" \
  "s_nop 4\n\t" \
  "buffer_load_dwordx4 v[76:79], v192, %[ru], s88 offen offset:1024 sc1\n\t" \
  "s_nop 4\n\t" \
  "s_add_i32 s79, s79, 22\n\t" \
  ".p2align 4\n\t" \
  "4:\n\t" \
  "s_sub_i32 s79, s79, 33\n\t" \
  "s_cmp_gt_i32 s79, 56\n\t" \
  "s_cbranch_scc1 4b\n\t" \
  "buffer_load_dwordx4 v[80:83], v192, %[ru], s89 offen offset:0 sc1\n\t" \
  "s_nop 4\n\t" \
  "buffer_load_dwordx4 v[84:87], v192, %[ru], s89 offen offset:1024 sc1\n\t" \
  "s_nop 4\n\t" \
  "s_add_i32 s79, s79, 42\n\t" \
  ".p2align 4\n\t" \
  "5:\n\t" \
  "s_sub_i32 s79, s79, 33\n\t" \
  "s_cmp_gt_i32 s79, 56\n\t" \
  "s_cbranch_scc1 5b\n\t" \
  "buffer_load_dwordx4 v[88:91], v192, %[ru], s90 offen offset:0 sc1\n\t" \
  "buffer_load_dwordx4 v[92:95], v192, %[ru], s90 offen offset:1024 sc1\n\t" \
  "s_add_i32 s79, s79, 122\n\t" \
  ".p2align 4\n\t" \
  "9:\n\t" \
  "s_sub_i32 s79, s79, 33\n\t" \
  "s_cmp_gt_i32 s79, 56\n\t" \
  "s_cbranch_scc1 9b\n\t" \
  "buffer_load_dwordx4 v[104:107], v194, %[rh], s92 offen sc1\n\t" \
  "s_nop 4\n\t" \
  "buffer_load_dwordx4 v[108:111], v194, %[rh], s93 offen sc1\n\t" \
  "s_nop 4\n\t" \
  "buffer_load_dwordx4 v[112:115], v194, %[rh], s94 offen sc1\n\t" \
  "s_nop 4\n\t" \
  "buffer_load_dwordx4 v[116:119], v194, %[rh], s95 offen sc1\n\t" \
  "s_nop 4\n\t" \
  "buffer_load_dwordx4 v[96:99], v192, %[ru], s91 offen offset:0 sc1\n\t" \
  "s_nop 4\n\t" \
  "buffer_load_dwordx4 v[100:103], v192, %[ru], s91 offen offset:1024 sc1\n\t" \
  "s_nop 4\n\t" \
  "10:\n\t" \
  "s_waitcnt vmcnt(5)\n\t" \
  "v_mfma_f32_16x16x32_bf16 v[136:139], v[104:107], v[72:75], 0\n\t" \
  "v_mfma_f32_16x16x32_bf16 v[140:143], v[104:107], v[76:79], 0\n\t" \
  "s_waitcnt vmcnt(4)\n\t" \
  "v_mfma_f32_16x16x32_bf16 v[136:139], v[108:111], v[80:83], v[136:139]\n\t" \
  "v_mfma_f32_16x16x32_bf16 v[140:143], v[108:111], v[84:87], v[140:143]\n\t" \
  "s_waitcnt vmcnt(3)\n\t" \
  "v_mfma_f32_16x16x32_bf16 v[136:139], v[112:115], v[88:91], v[136:139]\n\t" \
  "v_mfma_f32_16x16x32_bf16 v[140:143], v[112:115], v[92:95], v[140:143]\n\t" \
  "s_waitcnt vmcnt(1)\n\t" \
  "v_mfma_f32_16x16x32_bf16 v[136:139], v[116:119], v[96:99], v[136:139]\n\t" \
  "s_waitcnt vmcnt(0)\n\t" \
  "v_mfma_f32_16x16x32_bf16 v[140:143], v[116:119], v[100:103], v[140:143]\n\t" \
  "s_barrier\n\t" \
  "ds_read2st64_b64 v[144:147], v195 offset1:1\n\t" \
  "s_nop 1\n\t" \
  "v_permlane32_swap_b32 v136, v138\n\t" \
  "v_permlane32_swap_b32 v137, v139\n\t" \
  "s_nop 1\n\t" \
  "v_permlane32_swap_b32 v140, v142\n\t" \
  "v_permlane32_swap_b32 v141, v143\n\t" \
  "v_add_f32 v136, v136, v142\n\t" \
  "v_add_f32 v137, v137, v143\n\t" \
  "s_waitcnt lgkmcnt(0)\n\t" \
  "v_add_f32 v136, v136, v144\n\t" \
  "v_add_f32 v137, v137, v145\n\t" \
  "v_add_f32 v136, v136, v146\n\t" \
  "v_add_f32 v137, v137, v147\n\t" \
  "v_cvt_pk_bf16_f32 v148, v136, v137\n\t" \
  "s_nop 1\n\t" \
  "v_mov_b32_dpp v149, v148 row_half_mirror row_mask:0xf bank_mask:0xf\n\t" \
  "v_perm_b32 v150, v148, v149, v205\n\t" \
  "v_cmpx_o_f16_sdwa vcc, v150, v150 src0_sel:WORD_0 src1_sel:WORD_1\n\t" \
  "6:\n\t" \
  "buffer_store_dword v150, v201, %[rh], s97 offen sc1\n\t" \
  "s_memtime s[82:83]\n\t" \
  "s_mov_b64 exec, -1\n\t" \
  "s_cmp_eq_u64 vcc, -1\n\t" \
  "s_cbranch_scc0 8f\n\t" \
  "v_mov_b32 v151, 0\n\t" \
  "5:\n\t" \
  "ds_write_b32 v206, v151\n\t" \
  "s_waitcnt lgkmcnt(0)\n\t" \
  "s_mov_b32 s56, s82\n\t" \
  "s_barrier\n\t" \
  "ds_read2_b32 v[148:149], v207 offset1:8\n\t" \
  "s_waitcnt lgkmcnt(0)\n\t" \
  "v_or_b32 v148, v148, v149\n\t" \
  "v_cmp_ne_u32 vcc, 0, v148\n\t" \
  "s_cbranch_vccnz 12f\n\t" \
  "s_and_b32 s58, s58, 4\n\t" \
  "25:\n\t" \
  "s_add_i32 s82, s40, 2\n\t" \
  "s_min_i32 s83, s82, 63\n\t" \
  "s_mov_b32 exec_hi, 0\n\t" \
  "v_cmp_gt_i32 vcc, s83, v186\n\t" \
  "s_mov_b64 exec, -1\n\t" \
  "s_and_b32 s81, s82, 15\n\t" \
  "s_lshl_b32 s81, s81, 22\n\t" \
  "s_add_i32 s81, s81, s51\n\t" \
  "s_lshr_b32 s80, s81, 21\n\t" \
  "s_bitcmp0_b64 %[sel], s80\n\t" \
  "s_cselect_b32 s80, 0x2000, 0\n\t" \
  "s_add_i32 s81, s81, s80\n\t" \
  "s_cbranch_vccnz 27f\n\t" \
  "28:\n\t" \
  "s_cmp_ge_i32 s40, 62\n\t" \
  "s_cbranch_scc1 37f\n\t" \
  "s_and_b32 s76, s40, 3\n\t" \
  "s_lshl_b32 s76, s76, 18\n\t" \
  "buffer_load_dwordx4 v[0:3], v210, %[ru], s81 offen offset:0 sc1\n\t" \
  "buffer_load_dwordx4 v[4:7], v210, %[ru], s81 offen offset:1024 sc1\n\t" \
  "s_add_i32 s81, s81, 0x20000\n\t" \
  "buffer_load_dwordx4 v[8:11], v210, %[ru], s81 offen offset:0 sc1\n\t" \
  "buffer_load_dwordx4 v[12:15], v210, %[ru], s81 offen offset:1024 sc1\n\t" \
  "s_add_i32 s81, s81, 0x20000\n\t" \
  "buffer_load_dwordx4 v[16:19], v210, %[ru], s81 offen offset:0 sc1\n\t" \
  "buffer_load_dwordx4 v[20:23], v210, %[ru], s81 offen offset:1024 sc1\n\t" \
  "s_add_i32 s81, s81, 0x20000\n\t" \
  "buffer_load_dwordx4 v[24:27], v210, %[ru], s81 offen offset:0 sc1\n\t" \
  "buffer_load_dwordx4 v[28:31], v210, %[ru], s81 offen offset:1024 sc1\n\t" \
  "s_add_i32 s81, s81, 0x20000\n\t" \
  "s_bitcmp1_b32 s58, 4\n\t" \
  "s_cbranch_scc1 29f\n\t" \
  "s_sub_i32 s80, s56, s41\n\t" \
  "s_mul_hi_u32 s80, s80, s45\n\t" \
  "s_lshl_b32 s80, s80, 1\n\t" \
  "s_sub_i32 s80, s80, s57\n\t" \
  "s_min_u32 s47, s47, s46\n\t" \
  "s_min_u32 s47, s47, s80\n\t" \
  "v_mov_b32 v188, s47\n\t" \
  "s_mov_b32 s47, s46\n\t" \
  "s_mov_b32 s46, s80\n\t" \
  "buffer_store_dword v188, v202, %[rh], s52 offen sc1\n\t" \
  "v_mov_b64 v[188:189], -1\n\t" \
  "v_mov_b64 v[190:191], -1\n\t" \
  "v_mov_b32 v187, s40\n\t" \
  "s_add_i32 s80, s40, -1\n\t" \
  "s_and_b32 s80, s80, 7\n\t" \
  "s_lshl_b32 s80, s80, 15\n\t" \
  "s_add_i32 s80, s80, s49\n\t" \
  "buffer_store_dwordx4 v[188:191], v208, %[rh], s80 offen sc1\n\t" \
  "buffer_store_dword v187, v209, %[ru], 0 offen sc1\n\t" \
  "ds_write_b128 v197, v[72:75] offset:16384\n\t" \
  "ds_write_b128 v197, v[76:79] offset:17408\n\t" \
  "ds_write_b128 v197, v[80:83] offset:18432\n\t" \
  "ds_write_b128 v197, v[84:87] offset:19456\n\t" \
  "ds_write_b128 v197, v[88:91] offset:20480\n\t" \
  "ds_write_b128 v197, v[92:95] offset:21504\n\t" \
  "ds_write_b128 v197, v[96:99] offset:22528\n\t" \
  "ds_write_b128 v197, v[100:103] offset:23552\n\t" \
  "29:\n\t" \
  "s_and_b32 s58, s58, 36\n\t" \
  "buffer_load_dwordx4 v[32:35], v210, %[ru], s81 offen offset:0 sc1\n\t" \
  "buffer_load_dwordx4 v[36:39], v210, %[ru], s81 offen offset:1024 sc1\n\t" \
  "s_add_i32 s81, s81, 0x20000\n\t" \
  "buffer_load_dwordx4 v[40:43], v210, %[ru], s81 offen offset:0 sc1\n\t" \
  "buffer_load_dwordx4 v[44:47], v210, %[ru], s81 offen offset:1024 sc1\n\t" \
  "s_add_i32 s81, s81, 0x20000\n\t" \
  "buffer_load_dwordx4 a[0:3], v211, %[rh], s76 offen offset:0 sc1\n\t" \
  "buffer_load_dwordx4 a[4:7], v211, %[rh], s76 offen offset:1024 sc1\n\t" \
  "s_waitcnt lgkmcnt(0)\n\t" \
  "s_barrier\n\t" \
  "buffer_load_dwordx4 v[48:51], v210, %[ru], s81 offen offset:0 sc1\n\t" \
  "buffer_load_dwordx4 v[52:55], v210, %[ru], s81 offen offset:1024 sc1\n\t" \
  "s_add_i32 s81, s81, 0x20000\n\t" \
  "buffer_load_dwordx4 v[56:59], v210, %[ru], s81 offen offset:0 sc1\n\t" \
  "buffer_load_dwordx4 v[60:63], v210, %[ru], s81 offen offset:1024 sc1\n\t" \
  "s_add_i32 s81, s81, 0x20000\n\t" \
  "buffer_load_dwordx4 a[8:11], v212, %[rh], s76 offen offset:0 sc1\n\t" \
  "buffer_load_dwordx4 a[12:15], v212, %[rh], s76 offen offset:1024 sc1\n\t" \
  "buffer_load_dwordx4 v[64:67], v210, %[ru], s81 offen offset:0 sc1\n\t" \
  "buffer_load_dwordx4 v[68:71], v210, %[ru], s81 offen offset:1024 sc1\n\t" \
  "s_add_i32 s81, s81, 0x20000\n\t" \
  "buffer_load_dwordx4 v[104:107], v210, %[ru], s81 offen offset:0 sc1\n\t" \
  "buffer_load_dwordx4 v[108:111], v210, %[ru], s81 offen offset:1024 sc1\n\t" \
  "s_add_i32 s81, s81, 0x20000\n\t" \
  "buffer_load_dwordx4 a[16:19], v213, %[rh], s76 offen offset:0 sc1\n\t" \
  "buffer_load_dwordx4 a[20:23], v213, %[rh], s76 offen offset:1024 sc1\n\t" \
  "ds_read_b128 v[160:163], v193 offset:0\n\t" \
  "ds_read_b128 v[164:167], v193 offset:256\n\t" \
  "ds_read_b128 v[168:171], v193 offset:2048\n\t" \
  "ds_read_b128 v[172:175], v193 offset:2304\n\t" \
  "ds_read_b128 v[176:179], v193 offset:4096\n\t" \
  "ds_read_b128 v[180:183], v193 offset:4352\n\t" \
  "s_waitcnt lgkmcnt(2)\n\t" \
  "v_pk_add_f32 v[160:161], v[160:161], v[168:169]\n\t" \
  "v_pk_add_f32 v[162:163], v[162:163], v[170:171]\n\t" \
  "v_pk_add_f32 v[164:165], v[164:165], v[172:173]\n\t" \
  "v_pk_add_f32 v[166:167], v[166:167], v[174:175]\n\t" \
  "s_waitcnt lgkmcnt(0)\n\t" \
  "v_pk_add_f32 v[160:161], v[160:161], v[176:177]\n\t" \
  "v_pk_add_f32 v[162:163], v[162:163], v[178:179]\n\t" \
  "v_pk_add_f32 v[164:165], v[164:165], v[180:181]\n\t" \
  "v_pk_add_f32 v[166:167], v[166:167], v[182:183]\n\t" \
  "v_cvt_pk_bf16_f32 v160, v160, v161\n\t" \
  "v_cvt_pk_bf16_f32 v161, v162, v163\n\t" \
  "v_cvt_pk_bf16_f32 v162, v164, v165\n\t" \
  "v_cvt_pk_bf16_f32 v163, v166, v167\n\t" \
  "buffer_load_dwordx4 v[112:115], v210, %[ru], s81 offen offset:0 sc1\n\t" \
  "buffer_load_dwordx4 v[116:119], v210, %[ru], s81 offen offset:1024 sc1\n\t" \
  "s_add_i32 s81, s81, 0x20000\n\t" \
  "buffer_load_dwordx4 v[120:123], v210, %[ru], s81 offen offset:0 sc1\n\t" \
  "buffer_load_dwordx4 v[124:127], v210, %[ru], s81 offen offset:1024 sc1\n\t" \
  "s_add_i32 s81, s81, 0x20000\n\t" \
  "buffer_load_dwordx4 a[24:27], v214, %[rh], s76 offen offset:0 sc1\n\t" \
  "buffer_load_dwordx4 a[28:31], v214, %[rh], s76 offen offset:1024 sc1\n\t" \
  "s_waitcnt vmcnt(28)\n\t" \
  "v_swap_b32 v1, v8\n\t" \
  "v_swap_b32 v3, v10\n\t" \
  "v_swap_b32 v5, v12\n\t" \
  "v_swap_b32 v7, v14\n\t" \
  "ds_write_b128 v204, v[0:3] offset:0\n\t" \
  "ds_write_b128 v204, v[8:11] offset:1024\n\t" \
  "ds_write_b128 v204, v[4:7] offset:2048\n\t" \
  "ds_write_b128 v204, v[12:15] offset:3072\n\t" \
  "buffer_load_dwordx4 v[128:131], v210, %[ru], s81 offen offset:0 sc1\n\t" \
  "buffer_load_dwordx4 v[132:135], v210, %[ru], s81 offen offset:1024 sc1\n\t" \
  "s_add_i32 s81, s81, 0x20000\n\t" \
  "buffer_load_dwordx4 v[136:139], v210, %[ru], s81 offen offset:0 sc1\n\t" \
  "buffer_load_dwordx4 v[140:143], v210, %[ru], s81 offen offset:1024 sc1\n\t" \
  "s_add_i32 s81, s81, 0x20000\n\t" \
  "s_waitcnt vmcnt(28)\n\t" \
  "v_swap_b32 v17, v24\n\t" \
  "v_swap_b32 v19, v26\n\t" \
  "v_swap_b32 v21, v28\n\t" \
  "v_swap_b32 v23, v30\n\t" \
  "ds_write_b128 v204, v[16:19] offset:4096\n\t" \
  "ds_write_b128 v204, v[24:27] offset:5120\n\t" \
  "ds_write_b128 v204, v[20:23] offset:6144\n\t" \
  "ds_write_b128 v204, v[28:31] offset:7168\n\t" \
  "buffer_load_dwordx4 v[144:147], v210, %[ru], s81 offen offset:0 sc1\n\t" \
  "buffer_load_dwordx4 v[148:151], v210, %[ru], s81 offen offset:1024 sc1\n\t" \
  "s_add_i32 s81, s81, 0x20000\n\t" \
  "buffer_load_dwordx4 v[152:155], v210, %[ru], s81 offen offset:0 sc1\n\t" \
  "buffer_load_dwordx4 v[156:159], v210, %[ru], s81 offen offset:1024 sc1\n\t" \
  "s_add_i32 s81, s81, 0x20000\n\t" \
  "s_waitcnt vmcnt(28)\n\t" \
  "v_swap_b32 v33, v40\n\t" \
  "v_swap_b32 v35, v42\n\t" \
  "v_swap_b32 v37, v44\n\t" \
  "v_swap_b32 v39, v46\n\t" \
  "ds_write_b128 v204, v[32:35] offset:8192\n\t" \
  "ds_write_b128 v204, v[40:43] offset:9216\n\t" \
  "ds_write_b128 v204, v[36:39] offset:10240\n\t" \
  "ds_write_b128 v204, v[44:47] offset:11264\n\t" \
  "s_waitcnt vmcnt(22)\n\t" \
  "v_swap_b32 v49, v56\n\t" \
  "v_swap_b32 v51, v58\n\t" \
  "v_swap_b32 v53, v60\n\t" \
  "v_swap_b32 v55, v62\n\t" \
  "ds_write_b128 v204, v[48:51] offset:12288\n\t" \
  "ds_write_b128 v204, v[56:59] offset:13312\n\t" \
  "ds_write_b128 v204, v[52:55] offset:14336\n\t" \
  "ds_write_b128 v204, v[60:63] offset:15360\n\t" \
  "s_waitcnt vmcnt(16)\n\t" \
  "v_swap_b32 v65, v104\n\t" \
  "v_swap_b32 v67, v106\n\t" \
  "v_swap_b32 v69, v108\n\t" \
  "v_swap_b32 v71, v110\n\t" \
  "ds_write_b128 v204, v[64:67] offset:16384\n\t" \
  "ds_write_b128 v204, v[104:107] offset:17408\n\t" \
  "ds_write_b128 v204, v[68:71] offset:18432\n\t" \
  "ds_write_b128 v204, v[108:111] offset:19456\n\t" \
  "s_waitcnt vmcnt(10)\n\t" \
  "v_swap_b32 v113, v120\n\t" \
  "v_swap_b32 v115, v122\n\t" \
  "v_swap_b32 v117, v124\n\t" \
  "v_swap_b32 v119, v126\n\t" \
  "ds_write_b128 v204, v[112:115] offset:20480\n\t" \
  "ds_write_b128 v204, v[120:123] offset:21504\n\t" \
  "ds_write_b128 v204, v[116:119] offset:22528\n\t" \
  "ds_write_b128 v204, v[124:127] offset:23552\n\t" \
  "s_waitcnt vmcnt(4)\n\t" \
  "v_swap_b32 v129, v136\n\t" \
  "v_swap_b32 v131, v138\n\t" \
  "v_swap_b32 v133, v140\n\t" \
  "v_swap_b32 v135, v142\n\t" \
  "ds_write_b128 v204, v[128:131] offset:24576\n\t" \
  "ds_write_b128 v204, v[136:139] offset:25600\n\t" \
  "ds_write_b128 v204, v[132:135] offset:26624\n\t" \
  "ds_write_b128 v204, v[140:143] offset:27648\n\t" \
  "s_waitcnt vmcnt(0)\n\t" \
  "v_swap_b32 v145, v152\n\t" \
  "v_swap_b32 v147, v154\n\t" \
  "v_swap_b32 v149, v156\n\t" \
  "v_swap_b32 v151, v158\n\t" \
  "ds_write_b128 v204, v[144:147] offset:28672\n\t" \
  "ds_write_b128 v204, v[152:155] offset:29696\n\t" \
  "ds_write_b128 v204, v[148:151] offset:30720\n\t" \
  "ds_write_b128 v204, v[156:159] offset:31744\n\t" \
  "38:\n\t" \
  "s_add_i32 s80, s40, -2\n\t" \
  "s_lshl_b32 s80, s80, 17\n\t" \
  "s_min_u32 s80, s80, 0x70000000\n\t" \
  "buffer_store_dwordx4 v[160:163], v196, %[ro], s80 offen nt\n\t" \
  "ds_read_b128 a[32:35], v198 offset:0\n\t" \
  "ds_read_b128 a[36:39], v198 offset:1024\n\t" \
  "ds_read_b128 a[40:43], v198 offset:2048\n\t" \
  "ds_read_b128 a[44:47], v198 offset:3072\n\t" \
  "ds_read_b128 a[48:51], v198 offset:4096\n\t" \
  "ds_read_b128 a[52:55], v198 offset:5120\n\t" \
  "ds_read_b128 a[56:59], v198 offset:6144\n\t" \
  "ds_read_b128 a[60:63], v198 offset:7168\n\t" \
  "ds_read_b128 a[64:67], v198 offset:8192\n\t" \
  "ds_read_b128 a[68:71], v198 offset:9216\n\t" \
  "ds_read_b128 a[72:75], v198 offset:10240\n\t" \
  "ds_read_b128 a[76:79], v198 offset:11264\n\t" \
  "ds_read_b128 a[80:83], v198 offset:12288\n\t" \
  "ds_read_b128 a[84:87], v198 offset:13312\n\t" \
  "ds_read_b128 a[88:91], v198 offset:14336\n\t" \
  "ds_read_b128 a[92:95], v198 offset:15360\n\t" \
  "s_waitcnt vmcnt(1) lgkmcnt(0)\n\t" \
  "31:\n\t" \
  "v_mfma_f32_16x16x32_bf16 v[168:171], a[32:35], a[0:3], 0\n\t" \
  "v_mfma_f32_16x16x32_bf16 v[172:175], a[64:67], a[0:3], 0\n\t" \
  "v_mfma_f32_16x16x32_bf16 v[168:171], a[36:39], a[4:7], v[168:171]\n\t" \
  "v_mfma_f32_16x16x32_bf16 v[172:175], a[68:71], a[4:7], v[172:175]\n\t" \
  "v_mfma_f32_16x16x32_bf16 v[168:171], a[40:43], a[8:11], v[168:171]\n\t" \
  "v_mfma_f32_16x16x32_bf16 v[172:175], a[72:75], a[8:11], v[172:175]\n\t" \
  "v_mfma_f32_16x16x32_bf16 v[168:171], a[44:47], a[12:15], v[168:171]\n\t" \
  "v_mfma_f32_16x16x32_bf16 v[172:175], a[76:79], a[12:15], v[172:175]\n\t" \
  "v_mfma_f32_16x16x32_bf16 v[168:171], a[48:51], a[16:19], v[168:171]\n\t" \
  "v_mfma_f32_16x16x32_bf16 v[172:175], a[80:83], a[16:19], v[172:175]\n\t" \
  "v_mfma_f32_16x16x32_bf16 v[168:171], a[52:55], a[20:23], v[168:171]\n\t" \
  "v_mfma_f32_16x16x32_bf16 v[172:175], a[84:87], a[20:23], v[172:175]\n\t" \
  "v_mfma_f32_16x16x32_bf16 v[168:171], a[56:59], a[24:27], v[168:171]\n\t" \
  "v_mfma_f32_16x16x32_bf16 v[172:175], a[88:91], a[24:27], v[172:175]\n\t" \
  "v_mfma_f32_16x16x32_bf16 v[168:171], a[60:63], a[28:31], v[168:171]\n\t" \
  "v_mfma_f32_16x16x32_bf16 v[172:175], a[92:95], a[28:31], v[172:175]\n\t" \
  "s_nop 7\n\t" \
  "s_add_i32 s40, s40, 2\n\t" \
  "v_cmp_u_f32 vcc, v168, v168\n\t" \
  "s_cbranch_vccnz 32f\n\t" \
  "33:\n\t" \
  "s_barrier\n\t" \
  "s_add_i32 s85, s40, -1\n\t" \
  "s_and_b32 s82, s85, 2\n\t" \
  "s_lshl_b32 s82, s82, 13\n\t" \
  "s_and_b32 s80, s85, 1\n\t" \
  "s_lshl_b32 s80, s80, 12\n\t" \
  "s_add_i32 s82, s82, s80\n\t" \
  "s_add_i32 s82, s82, 0x84000\n\t" \
  "s_mov_b32 exec_hi, 0\n\t" \
  "buffer_load_dword v184, v200, %[rh], s82 offen sc1\n\t" \
  "buffer_load_dword v186, v203, %[ru], 0 offen sc1\n\t" \
  "s_mov_b64 exec, -1\n\t" \
  "ds_write_b128 v199, v[168:171] offset:0\n\t" \
  "ds_write_b128 v199, v[172:175] offset:1024\n\t" \
  "s_mov_b32 s59, 0\n\t" \
  "s_and_b32 s58, s58, 4\n\t" \
  "s_cmp_lt_i32 s40, 64\n\t" \
  "s_cbranch_scc1 20b\n\t" \
  "s_waitcnt lgkmcnt(0)\n\t" \
  "s_barrier\n\t" \
  "ds_read_b128 v[160:163], v193 offset:0\n\t" \
  "ds_read_b128 v[164:167], v193 offset:256\n\t" \
  "ds_read_b128 v[168:171], v193 offset:2048\n\t" \
  "ds_read_b128 v[172:175], v193 offset:2304\n\t" \
  "ds_read_b128 v[176:179], v193 offset:4096\n\t" \
  "ds_read_b128 v[180:183], v193 offset:4352\n\t" \
  "s_waitcnt lgkmcnt(2)\n\t" \
  "v_pk_add_f32 v[160:161], v[160:161], v[168:169]\n\t" \
  "v_pk_add_f32 v[162:163], v[162:163], v[170:171]\n\t" \
  "v_pk_add_f32 v[164:165], v[164:165], v[172:173]\n\t" \
  "v_pk_add_f32 v[166:167], v[166:167], v[174:175]\n\t" \
  "s_waitcnt lgkmcnt(0)\n\t" \
  "v_pk_add_f32 v[160:161], v[160:161], v[176:177]\n\t" \
  "v_pk_add_f32 v[162:163], v[162:163], v[178:179]\n\t" \
  "v_pk_add_f32 v[164:165], v[164:165], v[180:181]\n\t" \
  "v_pk_add_f32 v[166:167], v[166:167], v[182:183]\n\t" \
  "v_cvt_pk_bf16_f32 v160, v160, v161\n\t" \
  "v_cvt_pk_bf16_f32 v161, v162, v163\n\t" \
  "v_cvt_pk_bf16_f32 v162, v164, v165\n\t" \
  "v_cvt_pk_bf16_f32 v163, v166, v167\n\t" \
  "s_add_i32 s80, s40, -2\n\t" \
  "s_lshl_b32 s80, s80, 17\n\t" \
  "s_min_u32 s80, s80, 0x70000000\n\t" \
  "buffer_store_dwordx4 v[160:163], v196, %[ro], s80 offen nt\n\t" \
  "s_mov_b32 s58, 64\n\t" \
  "s_branch 7f\n\t" \
  "22:\n\t" \
  "s_mov_b32 s81, 0\n\t" \
  "s_cmp_eq_u32 s40, 0\n\t" \
  "s_cbranch_scc1 24b\n\t" \
  "s_bitcmp1_b32 s58, 2\n\t" \
  "s_cbranch_scc1 24b\n\t" \
  "s_mov_b64 exec, -1\n\t" \
  "s_sleep 10\n\t" \
  "s_add_u32 s59, s59, 0x10000\n\t" \
  "s_cmp_lt_u32 s59, 0x100000\n\t" \
  "s_cbranch_scc1 19b\n\t" \
  "s_or_b32 s58, s58, 1\n\t" \
  "s_branch 7f\n\t" \
  "14:\n\t" \
  "v_mov_b32 v184, 0\n\t" \
  "s_bitcmp1_b32 s58, 2\n\t" \
  "s_nop 0\n\t" \
  "s_cbranch_scc1 15b\n\t" \
  "s_mov_b32 s84, 950\n\t" \
  "s_add_u32 s59, s59, 0x10000\n\t" \
  "s_cmp_lt_u32 s59, 0x100000\n\t" \
  "s_mov_b64 exec, vcc\n\t" \
  "buffer_load_dword v187, v200, %[rh], s78 offen sc1\n\t" \
  "s_mov_b32 exec_lo, -1\n\t" \
  "s_cbranch_scc1 13b\n\t" \
  "s_mov_b64 exec, -1\n\t" \
  "s_or_b32 s58, s58, 1\n\t" \
  "s_branch 7f\n\t" \
  "27:\n\t" \
  "s_bitcmp1_b32 s58, 2\n\t" \
  "s_cbranch_scc1 28b\n\t" \
  "s_bitcmp1_b32 s58, 4\n\t" \
  "s_cbranch_scc1 23f\n\t" \
  "s_sub_i32 s80, s56, s41\n\t" \
  "s_mul_hi_u32 s80, s80, s45\n\t" \
  "s_lshl_b32 s80, s80, 1\n\t" \
  "s_sub_i32 s80, s80, s57\n\t" \
  "s_min_u32 s47, s47, s46\n\t" \
  "s_min_u32 s47, s47, s80\n\t" \
  "v_mov_b32 v188, s47\n\t" \
  "s_mov_b32 s47, s46\n\t" \
  "s_mov_b32 s46, s80\n\t" \
  "buffer_store_dword v188, v202, %[rh], s52 offen sc1\n\t" \
  "v_mov_b64 v[188:189], -1\n\t" \
  "v_mov_b64 v[190:191], -1\n\t" \
  "v_mov_b32 v187, s40\n\t" \
  "s_add_i32 s80, s40, -1\n\t" \
  "s_and_b32 s80, s80, 7\n\t" \
  "s_lshl_b32 s80, s80, 15\n\t" \
  "s_add_i32 s80, s80, s49\n\t" \
  "buffer_store_dwordx4 v[188:191], v208, %[rh], s80 offen sc1\n\t" \
  "buffer_store_dword v187, v209, %[ru], 0 offen sc1\n\t" \
  "ds_write_b128 v197, v[72:75] offset:16384\n\t" \
  "ds_write_b128 v197, v[76:79] offset:17408\n\t" \
  "ds_write_b128 v197, v[80:83] offset:18432\n\t" \
  "ds_write_b128 v197, v[84:87] offset:19456\n\t" \
  "ds_write_b128 v197, v[88:91] offset:20480\n\t" \
  "ds_write_b128 v197, v[92:95] offset:21504\n\t" \
  "ds_write_b128 v197, v[96:99] offset:22528\n\t" \
  "ds_write_b128 v197, v[100:103] offset:23552\n\t" \
  "s_or_b32 s58, s58, 16\n\t" \
  "23:\n\t" \
  "s_add_u32 s59, s59, 0x1000000\n\t" \
  "s_cmp_lt_u32 s59, 0x10000000\n\t" \
  "s_cbranch_scc1 26f\n\t" \
  "s_or_b32 s58, s58, 1\n\t" \
  "s_branch 7f\n\t" \
  "26:\n\t" \
  "buffer_load_dword v186, v203, %[ru], 0 offen sc1\n\t" \
  "s_waitcnt vmcnt(0)\n\t" \
  "s_branch 25b\n\t" \
  "37:\n\t" \
  "s_and_b32 s76, s40, 3\n\t" \
  "s_lshl_b32 s76, s76, 18\n\t" \
  "buffer_load_dwordx4 a[0:3], v211, %[rh], s76 offen offset:0 sc1\n\t" \
  "buffer_load_dwordx4 a[4:7], v211, %[rh], s76 offen offset:1024 sc1\n\t" \
  "buffer_load_dwordx4 a[8:11], v212, %[rh], s76 offen offset:0 sc1\n\t" \
  "buffer_load_dwordx4 a[12:15], v212, %[rh], s76 offen offset:1024 sc1\n\t" \
  "buffer_load_dwordx4 a[16:19], v213, %[rh], s76 offen offset:0 sc1\n\t" \
  "buffer_load_dwordx4 a[20:23], v213, %[rh], s76 offen offset:1024 sc1\n\t" \
  "buffer_load_dwordx4 a[24:27], v214, %[rh], s76 offen offset:0 sc1\n\t" \
  "buffer_load_dwordx4 a[28:31], v214, %[rh], s76 offen offset:1024 sc1\n\t" \
  "s_bitcmp1_b32 s58, 4\n\t" \
  "s_cbranch_scc1 29f\n\t" \
  "s_sub_i32 s80, s56, s41\n\t" \
  "s_mul_hi_u32 s80, s80, s45\n\t" \
  "s_lshl_b32 s80, s80, 1\n\t" \
  "s_sub_i32 s80, s80, s57\n\t" \
  "s_min_u32 s47, s47, s46\n\t" \
  "s_min_u32 s47, s47, s80\n\t" \
  "v_mov_b32 v188, s47\n\t" \
  "s_mov_b32 s47, s46\n\t" \
  "s_mov_b32 s46, s80\n\t" \
  "buffer_store_dword v188, v202, %[rh], s52 offen sc1\n\t" \
  "v_mov_b64 v[188:189], -1\n\t" \
  "v_mov_b64 v[190:191], -1\n\t" \
  "v_mov_b32 v187, s40\n\t" \
  "s_add_i32 s80, s40, -1\n\t" \
  "s_and_b32 s80, s80, 7\n\t" \
  "s_lshl_b32 s80, s80, 15\n\t" \
  "s_add_i32 s80, s80, s49\n\t" \
  "buffer_store_dwordx4 v[188:191], v208, %[rh], s80 offen sc1\n\t" \
  "buffer_store_dword v187, v209, %[ru], 0 offen sc1\n\t" \
  "ds_write_b128 v197, v[72:75] offset:16384\n\t" \
  "ds_write_b128 v197, v[76:79] offset:17408\n\t" \
  "ds_write_b128 v197, v[80:83] offset:18432\n\t" \
  "ds_write_b128 v197, v[84:87] offset:19456\n\t" \
  "ds_write_b128 v197, v[88:91] offset:20480\n\t" \
  "ds_write_b128 v197, v[92:95] offset:21504\n\t" \
  "ds_write_b128 v197, v[96:99] offset:22528\n\t" \
  "ds_write_b128 v197, v[100:103] offset:23552\n\t" \
  "29:\n\t" \
  "s_and_b32 s58, s58, 36\n\t" \
  "s_waitcnt lgkmcnt(0)\n\t" \
  "s_barrier\n\t" \
  "ds_read_b128 v[160:163], v193 offset:0\n\t" \
  "ds_read_b128 v[164:167], v193 offset:256\n\t" \
  "ds_read_b128 v[168:171], v193 offset:2048\n\t" \
  "ds_read_b128 v[172:175], v193 offset:2304\n\t" \
  "ds_read_b128 v[176:179], v193 offset:4096\n\t" \
  "ds_read_b128 v[180:183], v193 offset:4352\n\t" \
  "s_waitcnt lgkmcnt(2)\n\t" \
  "v_pk_add_f32 v[160:161], v[160:161], v[168:169]\n\t" \
  "v_pk_add_f32 v[162:163], v[162:163], v[170:171]\n\t" \
  "v_pk_add_f32 v[164:165], v[164:165], v[172:173]\n\t" \
  "v_pk_add_f32 v[166:167], v[166:167], v[174:175]\n\t" \
  "s_waitcnt lgkmcnt(0)\n\t" \
  "v_pk_add_f32 v[160:161], v[160:161], v[176:177]\n\t" \
  "v_pk_add_f32 v[162:163], v[162:163], v[178:179]\n\t" \
  "v_pk_add_f32 v[164:165], v[164:165], v[180:181]\n\t" \
  "v_pk_add_f32 v[166:167], v[166:167], v[182:183]\n\t" \
  "v_cvt_pk_bf16_f32 v160, v160, v161\n\t" \
  "v_cvt_pk_bf16_f32 v161, v162, v163\n\t" \
  "v_cvt_pk_bf16_f32 v162, v164, v165\n\t" \
  "v_cvt_pk_bf16_f32 v163, v166, v167\n\t" \
  "s_branch 38b\n\t" \
  "8:\n\t" \
  "s_mov_b64 vcc, -1\n\t" \
  "s_and_b32 s80, s58, 6\n\t" \
  "s_cbranch_scc1 6b\n\t" \
  "v_mov_b32 v151, 1\n\t" \
  "s_branch 5b\n\t" \
  "32:\n\t" \
  "s_and_b32 s80, s58, 36\n\t" \
  "s_cbranch_scc1 33b\n\t" \
  "s_add_i32 s40, s40, -2\n\t" \
  "34:\n\t" \
  "s_or_b32 s59, s59, 128\n\t" \
  "s_and_b32 s76, s40, 3\n\t" \
  "s_lshl_b32 s76, s76, 18\n\t" \
  "35:\n\t" \
  "buffer_load_dwordx4 a[0:3], v211, %[rh], s76 offen offset:0 sc1\n\t" \
  "buffer_load_dwordx4 a[4:7], v211, %[rh], s76 offen offset:1024 sc1\n\t" \
  "buffer_load_dwordx4 a[8:11], v212, %[rh], s76 offen offset:0 sc1\n\t" \
  "buffer_load_dwordx4 a[12:15], v212, %[rh], s76 offen offset:1024 sc1\n\t" \
  "buffer_load_dwordx4 a[16:19], v213, %[rh], s76 offen offset:0 sc1\n\t" \
  "buffer_load_dwordx4 a[20:23], v213, %[rh], s76 offen offset:1024 sc1\n\t" \
  "buffer_load_dwordx4 a[24:27], v214, %[rh], s76 offen offset:0 sc1\n\t" \
  "buffer_load_dwordx4 a[28:31], v214, %[rh], s76 offen offset:1024 sc1\n\t" \
  "s_waitcnt vmcnt(0)\n\t" \
  "s_bitcmp1_b32 s58, 2\n\t" \
  "s_cbranch_scc1 36f\n\t" \
  "v_accvgpr_read_b32 v0, a0\n\t" \
  "v_accvgpr_read_b32 v1, a1\n\t" \
  "v_accvgpr_read_b32 v2, a2\n\t" \
  "v_accvgpr_read_b32 v3, a3\n\t" \
  "v_accvgpr_read_b32 v4, a4\n\t" \
  "v_accvgpr_read_b32 v5, a5\n\t" \
  "v_accvgpr_read_b32 v6, a6\n\t" \
  "v_accvgpr_read_b32 v7, a7\n\t" \
  "v_accvgpr_read_b32 v8, a8\n\t" \
  "v_accvgpr_read_b32 v9, a9\n\t" \
  "v_accvgpr_read_b32 v10, a10\n\t" \
  "v_accvgpr_read_b32 v11, a11\n\t" \
  "v_accvgpr_read_b32 v12, a12\n\t" \
  "v_accvgpr_read_b32 v13, a13\n\t" \
  "v_accvgpr_read_b32 v14, a14\n\t" \
  "v_accvgpr_read_b32 v15, a15\n\t" \
  "v_accvgpr_read_b32 v16, a16\n\t" \
  "v_accvgpr_read_b32 v17, a17\n\t" \
  "v_accvgpr_read_b32 v18, a18\n\t" \
  "v_accvgpr_read_b32 v19, a19\n\t" \
  "v_accvgpr_read_b32 v20, a20\n\t" \
  "v_accvgpr_read_b32 v21, a21\n\t" \
  "v_accvgpr_read_b32 v22, a22\n\t" \
  "v_accvgpr_read_b32 v23, a23\n\t" \
  "v_accvgpr_read_b32 v24, a24\n\t" \
  "v_accvgpr_read_b32 v25, a25\n\t" \
  "v_accvgpr_read_b32 v26, a26\n\t" \
  "v_accvgpr_read_b32 v27, a27\n\t" \
  "v_accvgpr_read_b32 v28, a28\n\t" \
  "v_accvgpr_read_b32 v29, a29\n\t" \
  "v_accvgpr_read_b32 v30, a30\n\t" \
  "v_accvgpr_read_b32 v31, a31\n\t" \
  "v_max_u32 v148, v0, v1\n\t" \
  "v_max3_u32 v148, v148, v2, v3\n\t" \
  "v_max3_u32 v148, v148, v4, v5\n\t" \
  "v_max3_u32 v148, v148, v6, v7\n\t" \
  "v_max3_u32 v148, v148, v8, v9\n\t" \
  "v_max3_u32 v148, v148, v10, v11\n\t" \
  "v_max3_u32 v148, v148, v12, v13\n\t" \
  "v_max3_u32 v148, v148, v14, v15\n\t" \
  "v_max3_u32 v148, v148, v16, v17\n\t" \
  "v_max3_u32 v148, v148, v18, v19\n\t" \
  "v_max3_u32 v148, v148, v20, v21\n\t" \
  "v_max3_u32 v148, v148, v22, v23\n\t" \
  "v_max3_u32 v148, v148, v24, v25\n\t" \
  "v_max3_u32 v148, v148, v26, v27\n\t" \
  "v_max3_u32 v148, v148, v28, v29\n\t" \
  "v_max3_u32 v148, v148, v30, v31\n\t" \
  "v_cmp_eq_u32 vcc, -1, v148\n\t" \
  "s_cbranch_vccz 36f\n\t" \
  "s_add_u32 s59, s59, 0x1000000\n\t" \
  "s_cmp_lt_u32 s59, 0x10000000\n\t" \
  "s_cbranch_scc1 35b\n\t" \
  "s_or_b32 s58, s58, 9\n\t" \
  "s_branch 7f\n\t" \
  "36:\n\t" \
  "s_and_b32 s58, s58, 4\n\t" \
  "s_or_b32 s58, s58, 32\n\t" \
  "s_branch 31b\n\t" \
  "18:\n\t" \
  "v_mov_b32 v186, -1\n\t" \
  "s_and_b32 s80, s40, 3\n\t" \
  "s_mul_i32 s77, s80, 0xc000\n\t" \
  "s_add_i32 s77, s77, 0x40000\n\t" \
  "s_lshl_b32 s78, s80, 14\n\t" \
  "s_add_i32 s78, s78, 0x70000\n\t" \
  "s_mov_b32 s57, 0\n\t" \
  "s_add_i32 s96, s77, 0x1000\n\t" \
  "s_lshl_b32 s88, s80, 20\n\t" \
  "s_add_i32 s88, s88, s48\n\t" \
  "s_lshr_b32 s80, s88, 21\n\t" \
  "s_bitcmp1_b64 %[sel], s80\n\t" \
  "s_cselect_b32 s80, 0x2000, 0\n\t" \
  "s_add_i32 s88, s88, s80\n\t" \
  "s_xor_b32 s89, s88, 0x800\n\t" \
  "s_xor_b32 s90, s88, 0x1000\n\t" \
  "s_xor_b32 s91, s88, 0x1800\n\t" \
  "s_add_i32 s92, s77, s50\n\t" \
  "s_xor_b32 s93, s92, 0x100\n\t" \
  "s_xor_b32 s94, s92, 0x200\n\t" \
  "s_xor_b32 s95, s92, 0x300\n\t" \
  "s_add_i32 s97, s40, 1\n\t" \
  "s_and_b32 s97, s97, 7\n\t" \
  "s_lshl_b32 s97, s97, 15\n\t" \
  "s_add_i32 s97, s97, s49\n\t" \
  "12:\n\t" \
  "s_or_b32 s58, s58, 3\n\t" \
  "11:\n\t" \
  "buffer_load_dwordx4 v[72:75], v192, %[ru], s88 offen offset:0 sc1\n\t" \
  "buffer_load_dwordx4 v[76:79], v192, %[ru], s88 offen offset:1024 sc1\n\t" \
  "buffer_load_dwordx4 v[80:83], v192, %[ru], s89 offen offset:0 sc1\n\t" \
  "buffer_load_dwordx4 v[84:87], v192, %[ru], s89 offen offset:1024 sc1\n\t" \
  "buffer_load_dwordx4 v[88:91], v192, %[ru], s90 offen offset:0 sc1\n\t" \
  "buffer_load_dwordx4 v[92:95], v192, %[ru], s90 offen offset:1024 sc1\n\t" \
  "buffer_load_dwordx4 v[96:99], v192, %[ru], s91 offen offset:0 sc1\n\t" \
  "buffer_load_dwordx4 v[100:103], v192, %[ru], s91 offen offset:1024 sc1\n\t" \
  "buffer_load_dwordx4 v[104:107], v194, %[rh], s92 offen sc1\n\t" \
  "buffer_load_dwordx4 v[108:111], v194, %[rh], s93 offen sc1\n\t" \
  "buffer_load_dwordx4 v[112:115], v194, %[rh], s94 offen sc1\n\t" \
  "buffer_load_dwordx4 v[116:119], v194, %[rh], s95 offen sc1\n\t" \
  "s_waitcnt vmcnt(0)\n\t" \
  "s_add_u32 s59, s59, 1\n\t" \
  "s_bitcmp1_b32 s58, 2\n\t" \
  "s_cbranch_scc1 10b\n\t" \
  "v_max_u32 v148, v72, v73\n\t" \
  "v_max3_u32 v148, v148, v74, v75\n\t" \
  "v_max3_u32 v148, v148, v76, v77\n\t" \
  "v_max3_u32 v148, v148, v78, v79\n\t" \
  "v_max3_u32 v148, v148, v80, v81\n\t" \
  "v_max3_u32 v148, v148, v82, v83\n\t" \
  "v_max3_u32 v148, v148, v84, v85\n\t" \
  "v_max3_u32 v148, v148, v86, v87\n\t" \
  "v_max3_u32 v148, v148, v88, v89\n\t" \
  "v_max3_u32 v148, v148, v90, v91\n\t" \
  "v_max3_u32 v148, v148, v92, v93\n\t" \
  "v_max3_u32 v148, v148, v94, v95\n\t" \
  "v_max3_u32 v148, v148, v96, v97\n\t" \
  "v_max3_u32 v148, v148, v98, v99\n\t" \
  "v_max3_u32 v148, v148, v100, v101\n\t" \
  "v_max3_u32 v148, v148, v102, v103\n\t" \
  "v_max3_u32 v148, v148, v104, v105\n\t" \
  "v_max3_u32 v148, v148, v106, v107\n\t" \
  "v_max3_u32 v148, v148, v108, v109\n\t" \
  "v_max3_u32 v148, v148, v110, v111\n\t" \
  "v_max3_u32 v148, v148, v112, v113\n\t" \
  "v_max3_u32 v148, v148, v114, v115\n\t" \
  "v_max3_u32 v148, v148, v116, v117\n\t" \
  "v_max3_u32 v148, v148, v118, v119\n\t" \
  "v_cmp_eq_u32 vcc, -1, v148\n\t" \
  "s_cbranch_vccz 10b\n\t" \
  "s_bitcmp1_b32 s59, 6\n\t" \
  "s_cbranch_scc0 11b\n\t" \
  "7:\n\t" \
  "s_branch 92f\n\t" \
  "91:\n\t" \
  "s_mov_b32 s58, s42\n\t" \
  "s_mov_b32 s59, 0\n\t" \
  "s_bitcmp1_b32 s58, 4\n\t" \
  "s_cbranch_scc1 37f\n\t" \
  "s_bitcmp1_b32 s58, 3\n\t" \
  "s_cbranch_scc1 34f\n\t" \
  "s_bitcmp1_b32 s58, 1\n\t" \
  "s_cbranch_scc1 18f\n\t" \
  "17:\n\t" \
  "v_mov_b64 v[40:41], 0\n\t" \
  "v_mov_b64 v[42:43], 0\n\t" \
  "v_mov_b64 v[44:45], 0\n\t" \
  "v_mov_b64 v[46:47], 0\n\t" \
  "v_mov_b64 v[48:49], 0\n\t" \
  "v_mov_b64 v[50:51], 0\n\t" \
  "v_mov_b64 v[52:53], 0\n\t" \
  "v_mov_b64 v[54:55], 0\n\t" \
  "v_mov_b64 v[56:57], 0\n\t" \
  "v_mov_b64 v[58:59], 0\n\t" \
  "v_mov_b64 v[60:61], 0\n\t" \
  "v_mov_b64 v[62:63], 0\n\t" \
  "v_mov_b64 v[64:65], 0\n\t" \
  "v_mov_b64 v[66:67], 0\n\t" \
  "v_mov_b64 v[68:69], 0\n\t" \
  "v_mov_b64 v[70:71], 0\n\t" \
  "v_mov_b64 v[72:73], 0\n\t" \
  "v_mov_b64 v[74:75], 0\n\t" \
  "v_mov_b64 v[76:77], 0\n\t" \
  "v_mov_b64 v[78:79], 0\n\t" \
  "v_mov_b64 v[80:81], 0\n\t" \
  "v_mov_b64 v[82:83], 0\n\t" \
  "v_mov_b64 v[84:85], 0\n\t" \
  "v_mov_b64 v[86:87], 0\n\t" \
  "v_mov_b64 v[88:89], 0\n\t" \
  "v_mov_b64 v[90:91], 0\n\t" \
  "v_mov_b64 v[92:93], 0\n\t" \
  "v_mov_b64 v[94:95], 0\n\t" \
  "v_mov_b64 v[96:97], 0\n\t" \
  "v_mov_b64 v[98:99], 0\n\t" \
  "v_mov_b64 v[100:101], 0\n\t" \
  "v_mov_b64 v[102:103], 0\n\t" \
  "v_mov_b64 v[136:137], 0\n\t" \
  "v_mov_b64 v[138:139], 0\n\t" \
  "v_mov_b64 v[140:141], 0\n\t" \
  "v_mov_b64 v[142:143], 0\n\t" \
  "19:\n\t" \
  "s_add_i32 s85, s40, -1\n\t" \
  "s_and_b32 s82, s85, 2\n\t" \
  "s_lshl_b32 s82, s82, 13\n\t" \
  "s_and_b32 s80, s85, 1\n\t" \
  "s_lshl_b32 s80, s80, 12\n\t" \
  "s_add_i32 s82, s82, s80\n\t" \
  "s_add_i32 s82, s82, 0x84000\n\t" \
  "s_mov_b32 exec_hi, 0\n\t" \
  "buffer_load_dword v184, v200, %[rh], s82 offen sc1\n\t" \
  "buffer_load_dword v185, v200, %[rh], s52 offen sc1\n\t" \
  "s_mov_b64 exec, -1\n\t" \
  "20:\n\t" \
  "s_and_b32 s80, s40, 3\n\t" \
  "s_mul_i32 s77, s80, 0xc000\n\t" \
  "s_add_i32 s77, s77, 0x40000\n\t" \
  "s_lshl_b32 s78, s80, 14\n\t" \
  "s_add_i32 s78, s78, 0x70000\n\t" \
  "s_mov_b32 s57, 0\n\t" \
  "s_add_i32 s96, s77, 0x1000\n\t" \
  "s_and_b32 s98, s40, 2\n\t" \
  "s_lshl_b32 s98, s98, 13\n\t" \
  "s_and_b32 s80, s40, 1\n\t" \
  "s_lshl_b32 s80, s80, 12\n\t" \
  "s_add_i32 s98, s98, s80\n\t" \
  "s_add_i32 s98, s98, 0x84000\n\t" \
  "s_memtime vcc\n\t" \
  "s_memrealtime s[86:87]\n\t" \
  "s_nop 4\n\t" \
  "s_memrealtime s[60:61]\n\t" \
  "s_nop 4\n\t" \
  "s_memrealtime s[62:63]\n\t" \
  "s_nop 4\n\t" \
  "s_memrealtime s[64:65]\n\t" \
  "s_nop 4\n\t" \
  "s_memrealtime s[66:67]\n\t" \
  "s_nop 4\n\t" \
  "s_memrealtime s[68:69]\n\t" \
  "s_mov_b32 s79, 0\n\t" \
  "s_waitcnt lgkmcnt(0)\n\t" \
  "s_cmp_eq_u32 s86, s60\n\t" \
  "s_addc_u32 s79, s79, 0\n\t" \
  "s_cmp_eq_u32 s86, s62\n\t" \
  "s_addc_u32 s79, s79, 0\n\t" \
  "s_cmp_eq_u32 s86, s64\n\t" \
  "s_addc_u32 s79, s79, 0\n\t" \
  "s_cmp_eq_u32 s86, s66\n\t" \
  "s_addc_u32 s79, s79, 0\n\t" \
  "s_cmp_eq_u32 s86, s68\n\t" \
  "s_addc_u32 s79, s79, 0\n\t" \
  "s_mul_i32 s79, s79, 24\n\t" \
  "s_add_i32 s79, s79, vcc_lo\n\t" \
  "s_lshl4_add_u32 s80, s86, 64\n\t" \
  "s_sub_i32 s86, s79, s41\n\t" \
  "s_sub_i32 s87, s80, s43\n\t" \
  "s_cmp_lt_u32 s86, 4000\n\t" \
  "s_cbranch_scc1 21f\n\t" \
  "v_cvt_f32_u32 v165, s86\n\t" \
  "v_cvt_f32_u32 v164, s87\n\t" \
  "v_rcp_f32 v165, v165\n\t" \
  "s_nop 0\n\t" \
  "v_mul_f32 v165, v165, v164\n\t" \
  "v_max_f32 v165, 0x3f028f5c, v165\n\t" \
  "v_min_f32 v165, 0x3ffeb852, v165\n\t" \
  "v_rcp_f32 v164, v165\n\t" \
  "v_mul_f32 v165, 0x4f000000, v165\n\t" \
  "v_mul_f32 v164, 0x47800000, v164\n\t" \
  "v_cvt_u32_f32 v165, v165\n\t" \
  "v_cvt_u32_f32 v164, v164\n\t" \
  "s_nop 0\n\t" \
  "v_readfirstlane_b32 s45, v165\n\t" \
  "v_readfirstlane_b32 s44, v164\n\t" \
  "21:\n\t" \
  "s_mov_b32 s41, s79\n\t" \
  "s_mov_b32 s43, s80\n\t" \
  "s_lshl_b32 s83, s43, 8\n\t" \
  "s_mov_b32 s84, 650\n\t" \
  "s_mov_b32 exec_hi, 0\n\t" \
  "s_waitcnt vmcnt(0)\n\t" \
  "v_cmp_ne_u32_sdwa vcc, v184, s85 src0_sel:BYTE_0 src1_sel:DWORD\n\t" \
  "v_subrev_u32 v184, s83, v184\n\t" \
  "v_ashrrev_i32 v184, 8, v184\n\t" \
  "s_cbranch_vccnz 22f\n\t" \
  "s_nop 0\n\t" \
  "v_max_i32_dpp v184, v184, v184 quad_perm:[1,0,3,2] row_mask:0xf bank_mask:0xf\n\t" \
  "s_nop 1\n\t" \
  "v_max_i32_dpp v184, v184, v184 quad_perm:[2,3,0,1] row_mask:0xf bank_mask:0xf\n\t" \
  "s_nop 1\n\t" \
  "v_max_i32_dpp v184, v184, v184 row_half_mirror row_mask:0xf bank_mask:0xf\n\t" \
  "s_nop 1\n\t" \
  "v_max_i32_dpp v184, v184, v184 row_mirror row_mask:0xf bank_mask:0xf\n\t" \
  "s_nop 0\n\t" \
  "v_readlane_b32 s81, v184, 0\n\t" \
  "v_readlane_b32 s82, v184, 16\n\t" \
  "s_max_i32 s81, s81, s82\n\t" \
  "s_add_i32 s81, s81, 1056\n\t" \
  "s_max_i32 s81, s81, 0\n\t" \
  "s_min_i32 s81, s81, 20000\n\t" \
  "s_mul_i32 s81, s81, s44\n\t" \
  "s_lshr_b32 s81, s81, 16\n\t" \
  "24:\n\t" \
  "s_add_i32 s81, s81, s41\n\t" \
  "v_min_u32 v185, 1760, v185\n\t" \
  "s_nop 1\n\t" \
  "v_max_i32_dpp v185, v185, v185 quad_perm:[1,0,3,2] row_mask:0xf bank_mask:0xf\n\t" \
  "s_nop 1\n\t" \
  "v_max_i32_dpp v185, v185, v185 quad_perm:[2,3,0,1] row_mask:0xf bank_mask:0xf\n\t" \
  "s_nop 1\n\t" \
  "v_max_i32_dpp v185, v185, v185 row_half_mirror row_mask:0xf bank_mask:0xf\n\t" \
  "s_nop 1\n\t" \
  "v_max_i32_dpp v185, v185, v185 row_mirror row_mask:0xf bank_mask:0xf\n\t" \
  "s_nop 0\n\t" \
  "v_readlane_b32 s54, v185, 0\n\t" \
  "v_readlane_b32 s82, v185, 16\n\t" \
  "s_max_i32 s54, s54, s82\n\t" \
  "s_add_i32 s54, s54, 265\n\t" \
  "1:\n\t" \
  "s_memtime vcc\n\t" \
  "s_waitcnt lgkmcnt(0)\n\t" \
  "s_sub_i32 s79, s81, vcc_lo\n\t" \
  "s_cmp_gt_i32 s79, 128\n\t" \
  "s_cbranch_scc1 1b\n\t" \
  ".p2align 4\n\t" \
  "2:\n\t" \
  "s_sub_i32 s79, s79, 33\n\t" \
  "s_cmp_gt_i32 s79, 56\n\t" \
  "s_cbranch_scc1 2b\n\t" \
  "buffer_load_dword v187, v200, %[rh], s78 offen sc1\n\t" \
  "s_memtime vcc\n\t" \
  "s_memrealtime s[86:87]\n\t" \
  "s_nop 4\n\t" \
  "s_memrealtime s[60:61]\n\t" \
  "s_nop 4\n\t" \
  "s_memrealtime s[62:63]\n\t" \
  "s_nop 4\n\t" \
  "s_memrealtime s[64:65]\n\t" \
  "s_nop 4\n\t" \
  "s_memrealtime s[66:67]\n\t" \
  "s_nop 4\n\t" \
  "s_memrealtime s[68:69]\n\t" \
  "s_mov_b32 s79, 0\n\t" \
  "s_waitcnt lgkmcnt(0)\n\t" \
  "s_cmp_eq_u32 s86, s60\n\t" \
  "s_addc_u32 s79, s79, 0\n\t" \
  "s_cmp_eq_u32 s86, s62\n\t" \
  "s_addc_u32 s79, s79, 0\n\t" \
  "s_cmp_eq_u32 s86, s64\n\t" \
  "s_addc_u32 s79, s79, 0\n\t" \
  "s_cmp_eq_u32 s86, s66\n\t" \
  "s_addc_u32 s79, s79, 0\n\t" \
  "s_cmp_eq_u32 s86, s68\n\t" \
  "s_addc_u32 s79, s79, 0\n\t" \
  "s_mul_i32 s79, s79, 24\n\t" \
  "s_add_i32 s79, s79, vcc_lo\n\t" \
  "s_lshl4_add_u32 s80, s86, 64\n\t" \
  "s_mov_b32 s41, s79\n\t" \
  "s_mov_b32 s43, s80\n\t" \
  "s_lshl_b32 s83, s43, 8\n\t" \
  "s_add_i32 s54, s54, s43\n\t" \
  "13:\n\t" \
  "s_waitcnt vmcnt(0)\n\t" \
  "s_memtime s[86:87]\n\t" \
  "v_cmp_ne_u32_sdwa vcc, v187, s40 src0_sel:BYTE_0 src1_sel:DWORD\n\t" \
  "v_subrev_u32 v184, s83, v187\n\t" \
  "v_ashrrev_i32 v184, 8, v184\n\t" \
  "s_nop 1\n\t" \
  "v_max_i32_dpp v184, v184, v184 quad_perm:[1,0,3,2] row_mask:0xf bank_mask:0xf\n\t" \
  "s_nop 1\n\t" \
  "v_max_i32_dpp v184, v184, v184 quad_perm:[2,3,0,1] row_mask:0xf bank_mask:0xf\n\t" \
  "s_cbranch_vccnz 14f\n\t" \
  "15:\n\t" \
  "s_nop 0\n\t" \
  "v_max_i32_dpp v184, v184, v184 row_half_mirror row_mask:0xf bank_mask:0xf\n\t" \
  "s_nop 1\n\t" \
  "v_max_i32_dpp v184, v184, v184 row_mirror row_mask:0xf bank_mask:0xf\n\t" \
  "s_mov_b64 exec, -1\n\t" \
  "v_readlane_b32 s57, v184, 0\n\t" \
  "v_readlane_b32 s82, v184, 16\n\t" \
  "s_waitcnt lgkmcnt(0)\n\t" \
  "s_sub_i32 s79, s41, s86\n\t" \
  "s_max_i32 s57, s57, s82\n\t" \
  "s_add_i32 s81, s57, 215\n\t" \
  "s_max_i32 s81, s81, 0\n\t" \
  "s_min_i32 s81, s81, 20000\n\t" \
  "s_mul_i32 s81, s81, s44\n\t" \
  "s_lshr_b32 s81, s81, 16\n\t" \
  "s_add_i32 s79, s79, s81\n\t" \
  "s_sub_i32 s85, s84, s79\n\t" \
  "s_max_i32 s85, s85, 0\n\t" \
  "s_mul_hi_u32 s85, s85, s45\n\t" \
  "s_lshl1_add_u32 s85, s85, s57\n\t" \
  "s_add_i32 s85, s85, s54\n\t" \
  "s_lshl_b32 s85, s85, 8\n\t" \
  "s_or_b32 s85, s85, s40\n\t" \
  "v_mov_b32 v184, s85\n\t" \
  "buffer_store_dword v184, v202, %[rh], s98 offen sc1\n\t" \
  "s_add_i32 s79, s79, -640\n\t" \
  ".p2align 4\n\t" \
  "3:\n\t" \
  "s_sub_i32 s79, s79, 33\n\t" \
  "s_cmp_gt_i32 s79, 56\n\t" \
  "s_cbranch_scc1 3b\n\t" \
  "buffer_load_dwordx4 v[0:3], v193, %[rh], s77 offen offset:0 sc1\n\t" \
  "s_nop 4\n\t" \
  "buffer_load_dwordx4 v[4:7], v193, %[rh], s77 offen offset:1024 sc1\n\t" \
  "s_nop 4\n\t" \
  "buffer_load_dwordx4 v[8:11], v193, %[rh], s77 offen offset:2048 sc1\n\t" \
  "s_nop 4\n\t" \
  "buffer_load_dwordx4 v[12:15], v193, %[rh], s77 offen offset:3072 sc1\n\t" \
  "s_nop 4\n\t" \
  "buffer_load_dwordx4 v[16:19], v193, %[rh], s96 offen offset:0 sc1\n\t" \
  "s_nop 4\n\t" \
  "buffer_load_dwordx4 v[20:23], v193, %[rh], s96 offen offset:1024 sc1\n\t" \
  "s_nop 4\n\t" \
  "buffer_load_dwordx4 v[24:27], v193, %[rh], s96 offen offset:2048 sc1\n\t" \
  "s_nop 4\n\t" \
  "buffer_load_dwordx4 v[28:31], v193, %[rh], s96 offen offset:3072 sc1\n\t" \
  "s_add_i32 s79, s79, 272\n\t" \
  ".p2align 4\n\t" \
  "4:\n\t" \
  "s_sub_i32 s79, s79, 33\n\t" \
  "s_cmp_gt_i32 s79, 56\n\t" \
  "s_cbranch_scc1 4b\n\t" \
  "buffer_load_dwordx4 v[32:35], v195, %[rh], s77 offen offset:0 sc1\n\t" \
  "buffer_load_dwordx4 v[36:39], v195, %[rh], s77 offen offset:2048 sc1\n\t" \
  "10:\n\t" \
  "s_waitcnt vmcnt(9)\n\t" \
  "v_smfmac_f32_16x16x64_bf16 v[40:43], v[0:3], a[0:7], v203\n\t" \
  "v_smfmac_f32_16x16x64_bf16 v[56:59], v[0:3], a[8:15], v203\n\t" \
  "s_waitcnt vmcnt(8)\n\t" \
  "v_smfmac_f32_16x16x64_bf16 v[44:47], v[4:7], a[16:23], v203\n\t" \
  "v_cvt_pk_bf16_f32 v40, v40, v41\n\t" \
  "v_cvt_pk_bf16_f32 v41, v42, v43\n\t" \
  "v_smfmac_f32_16x16x64_bf16 v[60:63], v[4:7], a[24:31], v203\n\t" \
  "v_cvt_pk_bf16_f32 v56, v56, v57\n\t" \
  "v_cvt_pk_bf16_f32 v57, v58, v59\n\t" \
  "s_waitcnt vmcnt(7)\n\t" \
  "v_smfmac_f32_16x16x64_bf16 v[48:51], v[8:11], a[32:39], v203\n\t" \
  "v_cvt_pk_bf16_f32 v42, v44, v45\n\t" \
  "v_cvt_pk_bf16_f32 v43, v46, v47\n\t" \
  "v_smfmac_f32_16x16x64_bf16 v[64:67], v[8:11], a[40:47], v203\n\t" \
  "v_cvt_pk_bf16_f32 v58, v60, v61\n\t" \
  "v_cvt_pk_bf16_f32 v59, v62, v63\n\t" \
  "s_waitcnt vmcnt(6)\n\t" \
  "v_smfmac_f32_16x16x64_bf16 v[52:55], v[12:15], a[48:55], v203\n\t" \
  "v_cvt_pk_bf16_f32 v44, v48, v49\n\t" \
  "v_cvt_pk_bf16_f32 v45, v50, v51\n\t" \
  "v_smfmac_f32_16x16x64_bf16 v[68:71], v[12:15], a[56:63], v203\n\t" \
  "v_cvt_pk_bf16_f32 v60, v64, v65\n\t" \
  "v_cvt_pk_bf16_f32 v61, v66, v67\n\t" \
  "s_waitcnt vmcnt(5)\n\t" \
  "v_smfmac_f32_16x16x64_bf16 v[72:75], v[16:19], a[64:71], v203\n\t" \
  "v_cvt_pk_bf16_f32 v46, v52, v53\n\t" \
  "v_cvt_pk_bf16_f32 v47, v54, v55\n\t" \
  "v_smfmac_f32_16x16x64_bf16 v[88:91], v[16:19], a[72:79], v203\n\t" \
  "v_cvt_pk_bf16_f32 v62, v68, v69\n\t" \
  "v_cvt_pk_bf16_f32 v63, v70, v71\n\t" \
  "s_waitcnt vmcnt(4)\n\t" \
  "v_smfmac_f32_16x16x64_bf16 v[76:79], v[20:23], a[80:87], v203\n\t" \
  "v_cvt_pk_bf16_f32 v72, v72, v73\n\t" \
  "v_cvt_pk_bf16_f32 v73, v74, v75\n\t" \
  "v_smfmac_f32_16x16x64_bf16 v[92:95], v[20:23], a[88:95], v203\n\t" \
  "v_cvt_pk_bf16_f32 v88, v88, v89\n\t" \
  "v_cvt_pk_bf16_f32 v89, v90, v91\n\t" \
  "s_waitcnt vmcnt(3)\n\t" \
  "v_smfmac_f32_16x16x64_bf16 v[80:83], v[24:27], a[96:103], v203\n\t" \
  "v_cvt_pk_bf16_f32 v74, v76, v77\n\t" \
  "v_cvt_pk_bf16_f32 v75, v78, v79\n\t" \
  "v_smfmac_f32_16x16x64_bf16 v[96:99], v[24:27], a[104:111], v203\n\t" \
  "v_cvt_pk_bf16_f32 v90, v92, v93\n\t" \
  "v_cvt_pk_bf16_f32 v91, v94, v95\n\t" \
  "s_waitcnt vmcnt(2)\n\t" \
  "v_smfmac_f32_16x16x64_bf16 v[84:87], v[28:31], a[112:119], v203\n\t" \
  "v_cvt_pk_bf16_f32 v76, v80, v81\n\t" \
  "v_cvt_pk_bf16_f32 v77, v82, v83\n\t" \
  "v_smfmac_f32_16x16x64_bf16 v[100:103], v[28:31], a[120:127], v203\n\t" \
  "v_cvt_pk_bf16_f32 v92, v96, v97\n\t" \
  "v_cvt_pk_bf16_f32 v93, v98, v99\n\t" \
  "s_waitcnt vmcnt(1)\n\t" \
  "v_smfmac_f32_16x16x64_bf16 v[136:139], v[32:35], v[40:47], v204\n\t" \
  "v_cvt_pk_bf16_f32 v78, v84, v85\n\t" \
  "v_cvt_pk_bf16_f32 v79, v86, v87\n\t" \
  "v_smfmac_f32_16x16x64_bf16 v[140:143], v[32:35], v[56:63], v204\n\t" \
  "v_cvt_pk_bf16_f32 v94, v100, v101\n\t" \
  "v_cvt_pk_bf16_f32 v95, v102, v103\n\t" \
  "s_waitcnt vmcnt(0)\n\t" \
  "v_smfmac_f32_16x16x64_bf16 v[136:139], v[36:39], v[72:79], v204\n\t" \
  "v_smfmac_f32_16x16x64_bf16 v[140:143], v[36:39], v[88:95], v204\n\t" \
  "s_nop 3\n\t" \
  "v_permlane32_swap_b32 v136, v138\n\t" \
  "v_permlane32_swap_b32 v137, v139\n\t" \
  "v_add_f32 v136, v136, v138\n\t" \
  "v_add_f32 v137, v137, v139\n\t" \
  "v_permlane32_swap_b32 v140, v142\n\t" \
  "v_permlane32_swap_b32 v141, v143\n\t" \
  "v_add_f32 v138, v140, v142\n\t" \
  "v_add_f32 v139, v141, v143\n\t" \
  "ds_write2st64_b64 v197, v[136:137], v[138:139] offset1:2\n\t" \
  "s_waitcnt lgkmcnt(0)\n\t" \
  "s_barrier\n\t" \
  "s_barrier\n\t" \
  "ds_read2_b32 v[148:149], v207 offset1:8\n\t" \
  "s_waitcnt lgkmcnt(0)\n\t" \
  "v_or_b32 v148, v148, v149\n\t" \
  "v_cmp_ne_u32 vcc, 0, v148\n\t" \
  "s_cbranch_vccnz 12f\n\t" \
  "s_and_b32 s58, s58, 4\n\t" \
  "s_cmp_ge_i32 s40, 62\n\t" \
  "s_cbranch_scc1 26f\n\t" \
  "s_and_b32 s82, s40, 3\n\t" \
  "s_lshl_b32 s82, s82, 20\n\t" \
  "s_add_i32 s82, s82, s48\n\t" \
  "s_andn2_b32 s82, s82, 0x1800\n\t" \
  "s_lshr_b32 s80, s82, 21\n\t" \
  "s_bitcmp1_b64 %[sel], s80\n\t" \
  "s_cselect_b32 s80, 0x2000, 0\n\t" \
  "s_add_i32 s82, s82, s80\n\t" \
  "s_add_i32 s80, s82, 0x1000\n\t" \
  "v_mov_b64 v[188:189], -1\n\t" \
  "v_mov_b64 v[190:191], -1\n\t" \
  "s_sleep 8\n\t" \
  "buffer_store_dwordx4 v[188:191], v192, %[ru], s82 offen offset:0 sc1\n\t" \
  "buffer_store_dwordx4 v[188:191], v192, %[ru], s82 offen offset:1024 sc1\n\t" \
  "buffer_store_dwordx4 v[188:191], v192, %[ru], s82 offen offset:2048 sc1\n\t" \
  "buffer_store_dwordx4 v[188:191], v192, %[ru], s82 offen offset:3072 sc1\n\t" \
  "buffer_store_dwordx4 v[188:191], v192, %[ru], s80 offen offset:0 sc1\n\t" \
  "buffer_store_dwordx4 v[188:191], v192, %[ru], s80 offen offset:1024 sc1\n\t" \
  "buffer_store_dwordx4 v[188:191], v192, %[ru], s80 offen offset:2048 sc1\n\t" \
  "buffer_store_dwordx4 v[188:191], v192, %[ru], s80 offen offset:3072 sc1\n\t" \
  "26:\n\t" \
  "s_barrier\n\t" \
  "s_and_b32 s76, s40, 3\n\t" \
  "s_lshl_b32 s76, s76, 18\n\t" \
  "s_add_i32 s77, s76, 0x10000\n\t" \
  "s_add_i32 s78, s76, 0x1000\n\t" \
  "s_add_i32 s79, s76, 0x11000\n\t" \
  "31:\n\t" \
  "buffer_load_dwordx4 v[0:3], v210, %[rh], s76 offen offset:0 sc1\n\t" \
  "s_nop 10\n\t" \
  "buffer_load_dwordx4 v[4:7], v210, %[rh], s76 offen offset:1024 sc1\n\t" \
  "s_nop 10\n\t" \
  "buffer_load_dwordx4 v[8:11], v210, %[rh], s77 offen offset:0 sc1\n\t" \
  "s_nop 10\n\t" \
  "buffer_load_dwordx4 v[12:15], v210, %[rh], s77 offen offset:1024 sc1\n\t" \
  "s_nop 10\n\t" \
  "buffer_load_dwordx4 v[16:19], v210, %[rh], s76 offen offset:2048 sc1\n\t" \
  "s_nop 10\n\t" \
  "buffer_load_dwordx4 v[20:23], v210, %[rh], s76 offen offset:3072 sc1\n\t" \
  "s_nop 10\n\t" \
  "buffer_load_dwordx4 v[24:27], v210, %[rh], s77 offen offset:2048 sc1\n\t" \
  "s_nop 10\n\t" \
  "buffer_load_dwordx4 v[28:31], v210, %[rh], s77 offen offset:3072 sc1\n\t" \
  "s_nop 10\n\t" \
  "buffer_load_dwordx4 v[32:35], v210, %[rh], s78 offen offset:0 sc1\n\t" \
  "s_nop 10\n\t" \
  "buffer_load_dwordx4 v[36:39], v210, %[rh], s78 offen offset:1024 sc1\n\t" \
  "s_nop 10\n\t" \
  "buffer_load_dwordx4 v[40:43], v210, %[rh], s79 offen offset:0 sc1\n\t" \
  "s_nop 10\n\t" \
  "buffer_load_dwordx4 v[44:47], v210, %[rh], s79 offen offset:1024 sc1\n\t" \
  "s_nop 10\n\t" \
  "buffer_load_dwordx4 v[48:51], v210, %[rh], s78 offen offset:2048 sc1\n\t" \
  "s_nop 10\n\t" \
  "buffer_load_dwordx4 v[52:55], v210, %[rh], s78 offen offset:3072 sc1\n\t" \
  "s_nop 10\n\t" \
  "buffer_load_dwordx4 v[56:59], v210, %[rh], s79 offen offset:2048 sc1\n\t" \
  "s_nop 10\n\t" \
  "buffer_load_dwordx4 v[60:63], v210, %[rh], s79 offen offset:3072 sc1\n\t" \
  "s_nop 10\n\t" \
  "buffer_load_dwordx4 v[64:67], v211, %[rh], s76 offen offset:0 sc1\n\t" \
  "s_nop 10\n\t" \
  "buffer_load_dwordx4 v[68:71], v211, %[rh], s76 offen offset:1024 sc1\n\t" \
  "s_nop 10\n\t" \
  "buffer_load_dwordx4 v[72:75], v211, %[rh], s77 offen offset:0 sc1\n\t" \
  "s_nop 10\n\t" \
  "buffer_load_dwordx4 v[76:79], v211, %[rh], s77 offen offset:1024 sc1\n\t" \
  "s_nop 10\n\t" \
  "buffer_load_dwordx4 v[80:83], v211, %[rh], s76 offen offset:2048 sc1\n\t" \
  "s_nop 10\n\t" \
  "buffer_load_dwordx4 v[84:87], v211, %[rh], s76 offen offset:3072 sc1\n\t" \
  "s_nop 10\n\t" \
  "v_mov_b64 v[136:137], 0\n\t" \
  "v_mov_b64 v[138:139], 0\n\t" \
  "v_mov_b64 v[140:141], 0\n\t" \
  "v_mov_b64 v[142:143], 0\n\t" \
  "s_waitcnt vmcnt(20)\n\t" \
  "v_swap_b32 v1, v4\n\t" \
  "v_swap_b32 v3, v6\n\t" \
  "s_nop 1\n\t" \
  "v_mfma_f32_16x16x32_bf16 v[168:171], a[0:3], v[0:3], 0\n\t" \
  "buffer_load_dwordx4 v[88:91], v211, %[rh], s77 offen offset:2048 sc1\n\t" \
  "v_mfma_f32_16x16x32_bf16 v[172:175], a[8:11], v[0:3], 0\n\t" \
  "s_waitcnt vmcnt(19)\n\t" \
  "v_swap_b32 v9, v12\n\t" \
  "v_mfma_f32_16x16x32_bf16 v[168:171], a[4:7], v[4:7], v[168:171]\n\t" \
  "v_swap_b32 v11, v14\n\t" \
  "v_mfma_f32_16x16x32_bf16 v[172:175], a[12:15], v[4:7], v[172:175]\n\t" \
  "buffer_load_dwordx4 v[92:95], v211, %[rh], s77 offen offset:3072 sc1\n\t" \
  "v_mfma_f32_16x16x32_bf16 v[176:179], a[0:3], v[8:11], 0\n\t" \
  "buffer_load_dwordx4 v[96:99], v211, %[rh], s78 offen offset:0 sc1\n\t" \
  "v_mfma_f32_16x16x32_bf16 v[180:183], a[8:11], v[8:11], 0\n\t" \
  "s_waitcnt vmcnt(19)\n\t" \
  "v_swap_b32 v17, v20\n\t" \
  "v_mfma_f32_16x16x32_bf16 v[176:179], a[4:7], v[12:15], v[176:179]\n\t" \
  "v_swap_b32 v19, v22\n\t" \
  "v_mfma_f32_16x16x32_bf16 v[180:183], a[12:15], v[12:15], v[180:183]\n\t" \
  "buffer_load_dwordx4 v[100:103], v211, %[rh], s78 offen offset:1024 sc1\n\t" \
  "v_mfma_f32_16x16x32_bf16 v[168:171], a[16:19], v[16:19], v[168:171]\n\t" \
  "buffer_load_dwordx4 v[104:107], v211, %[rh], s79 offen offset:0 sc1\n\t" \
  "v_mfma_f32_16x16x32_bf16 v[172:175], a[24:27], v[16:19], v[172:175]\n\t" \
  "s_waitcnt vmcnt(19)\n\t" \
  "v_swap_b32 v25, v28\n\t" \
  "v_mfma_f32_16x16x32_bf16 v[168:171], a[20:23], v[20:23], v[168:171]\n\t" \
  "v_swap_b32 v27, v30\n\t" \
  "v_mfma_f32_16x16x32_bf16 v[172:175], a[28:31], v[20:23], v[172:175]\n\t" \
  "buffer_load_dwordx4 v[108:111], v211, %[rh], s79 offen offset:1024 sc1\n\t" \
  "v_mfma_f32_16x16x32_bf16 v[176:179], a[16:19], v[24:27], v[176:179]\n\t" \
  "buffer_load_dwordx4 v[112:115], v211, %[rh], s78 offen offset:2048 sc1\n\t" \
  "v_mfma_f32_16x16x32_bf16 v[180:183], a[24:27], v[24:27], v[180:183]\n\t" \
  "s_waitcnt vmcnt(19)\n\t" \
  "v_swap_b32 v33, v36\n\t" \
  "v_mfma_f32_16x16x32_bf16 v[176:179], a[20:23], v[28:31], v[176:179]\n\t" \
  "v_swap_b32 v35, v38\n\t" \
  "v_mfma_f32_16x16x32_bf16 v[180:183], a[28:31], v[28:31], v[180:183]\n\t" \
  "buffer_load_dwordx4 v[116:119], v211, %[rh], s78 offen offset:3072 sc1\n\t" \
  "v_mfma_f32_16x16x32_bf16 v[168:171], a[32:35], v[32:35], v[168:171]\n\t" \
  "buffer_load_dwordx4 v[120:123], v211, %[rh], s79 offen offset:2048 sc1\n\t" \
  "v_mfma_f32_16x16x32_bf16 v[172:175], a[40:43], v[32:35], v[172:175]\n\t" \
  "s_waitcnt vmcnt(19)\n\t" \
  "v_swap_b32 v41, v44\n\t" \
  "v_mfma_f32_16x16x32_bf16 v[168:171], a[36:39], v[36:39], v[168:171]\n\t" \
  "v_swap_b32 v43, v46\n\t" \
  "v_mfma_f32_16x16x32_bf16 v[172:175], a[44:47], v[36:39], v[172:175]\n\t" \
  "buffer_load_dwordx4 v[124:127], v211, %[rh], s79 offen offset:3072 sc1\n\t" \
  "v_mfma_f32_16x16x32_bf16 v[176:179], a[32:35], v[40:43], v[176:179]\n\t" \
  "v_mfma_f32_16x16x32_bf16 v[180:183], a[40:43], v[40:43], v[180:183]\n\t" \
  "s_waitcnt vmcnt(18)\n\t" \
  "v_swap_b32 v49, v52\n\t" \
  "v_mfma_f32_16x16x32_bf16 v[176:179], a[36:39], v[44:47], v[176:179]\n\t" \
  "v_swap_b32 v51, v54\n\t" \
  "v_mfma_f32_16x16x32_bf16 v[180:183], a[44:47], v[44:47], v[180:183]\n\t" \
  "v_mov_b64 v[40:41], 0\n\t" \
  "v_mov_b64 v[42:43], 0\n\t" \
  "v_mfma_f32_16x16x32_bf16 v[168:171], a[48:51], v[48:51], v[168:171]\n\t" \
  "v_mov_b64 v[44:45], 0\n\t" \
  "v_mov_b64 v[46:47], 0\n\t" \
  "v_mfma_f32_16x16x32_bf16 v[172:175], a[56:59], v[48:51], v[172:175]\n\t" \
  "s_waitcnt vmcnt(16)\n\t" \
  "v_swap_b32 v57, v60\n\t" \
  "v_mfma_f32_16x16x32_bf16 v[168:171], a[52:55], v[52:55], v[168:171]\n\t" \
  "v_swap_b32 v59, v62\n\t" \
  "v_mfma_f32_16x16x32_bf16 v[172:175], a[60:63], v[52:55], v[172:175]\n\t" \
  "v_mov_b64 v[48:49], 0\n\t" \
  "v_mov_b64 v[50:51], 0\n\t" \
  "v_mfma_f32_16x16x32_bf16 v[176:179], a[48:51], v[56:59], v[176:179]\n\t" \
  "v_mov_b64 v[52:53], 0\n\t" \
  "v_mov_b64 v[54:55], 0\n\t" \
  "v_mfma_f32_16x16x32_bf16 v[180:183], a[56:59], v[56:59], v[180:183]\n\t" \
  "s_waitcnt vmcnt(14)\n\t" \
  "v_swap_b32 v65, v68\n\t" \
  "v_mfma_f32_16x16x32_bf16 v[176:179], a[52:55], v[60:63], v[176:179]\n\t" \
  "v_swap_b32 v67, v70\n\t" \
  "v_mfma_f32_16x16x32_bf16 v[180:183], a[60:63], v[60:63], v[180:183]\n\t" \
  "v_mov_b64 v[56:57], 0\n\t" \
  "v_mov_b64 v[58:59], 0\n\t" \
  "v_mfma_f32_16x16x32_bf16 v[168:171], a[64:67], v[64:67], v[168:171]\n\t" \
  "v_mov_b64 v[60:61], 0\n\t" \
  "v_mov_b64 v[62:63], 0\n\t" \
  "v_mfma_f32_16x16x32_bf16 v[172:175], a[72:75], v[64:67], v[172:175]\n\t" \
  "s_waitcnt vmcnt(12)\n\t" \
  "v_swap_b32 v73, v76\n\t" \
  "v_mfma_f32_16x16x32_bf16 v[168:171], a[68:71], v[68:71], v[168:171]\n\t" \
  "v_swap_b32 v75, v78\n\t" \
  "v_mfma_f32_16x16x32_bf16 v[172:175], a[76:79], v[68:71], v[172:175]\n\t" \
  "v_mov_b64 v[64:65], 0\n\t" \
  "v_mov_b64 v[66:67], 0\n\t" \
  "v_mfma_f32_16x16x32_bf16 v[176:179], a[64:67], v[72:75], v[176:179]\n\t" \
  "v_mov_b64 v[68:69], 0\n\t" \
  "v_mov_b64 v[70:71], 0\n\t" \
  "v_mfma_f32_16x16x32_bf16 v[180:183], a[72:75], v[72:75], v[180:183]\n\t" \
  "s_waitcnt vmcnt(10)\n\t" \
  "v_swap_b32 v81, v84\n\t" \
  "v_mfma_f32_16x16x32_bf16 v[176:179], a[68:71], v[76:79], v[176:179]\n\t" \
  "v_swap_b32 v83, v86\n\t" \
  "v_mfma_f32_16x16x32_bf16 v[180:183], a[76:79], v[76:79], v[180:183]\n\t" \
  "v_mov_b64 v[72:73], 0\n\t" \
  "v_mov_b64 v[74:75], 0\n\t" \
  "v_mfma_f32_16x16x32_bf16 v[168:171], a[80:83], v[80:83], v[168:171]\n\t" \
  "v_mov_b64 v[76:77], 0\n\t" \
  "v_mov_b64 v[78:79], 0\n\t" \
  "v_mfma_f32_16x16x32_bf16 v[172:175], a[88:91], v[80:83], v[172:175]\n\t" \
  "s_waitcnt vmcnt(8)\n\t" \
  "v_swap_b32 v89, v92\n\t" \
  "v_mfma_f32_16x16x32_bf16 v[168:171], a[84:87], v[84:87], v[168:171]\n\t" \
  "v_swap_b32 v91, v94\n\t" \
  "v_mfma_f32_16x16x32_bf16 v[172:175], a[92:95], v[84:87], v[172:175]\n\t" \
  "v_mov_b64 v[80:81], 0\n\t" \
  "v_mov_b64 v[82:83], 0\n\t" \
  "v_mfma_f32_16x16x32_bf16 v[176:179], a[80:83], v[88:91], v[176:179]\n\t" \
  "v_mov_b64 v[84:85], 0\n\t" \
  "v_mov_b64 v[86:87], 0\n\t" \
  "v_mfma_f32_16x16x32_bf16 v[180:183], a[88:91], v[88:91], v[180:183]\n\t" \
  "s_waitcnt vmcnt(6)\n\t" \
  "v_swap_b32 v97, v100\n\t" \
  "v_mfma_f32_16x16x32_bf16 v[176:179], a[84:87], v[92:95], v[176:179]\n\t" \
  "v_swap_b32 v99, v102\n\t" \
  "v_mfma_f32_16x16x32_bf16 v[180:183], a[92:95], v[92:95], v[180:183]\n\t" \
  "v_mov_b64 v[88:89], 0\n\t" \
  "v_mov_b64 v[90:91], 0\n\t" \
  "v_mfma_f32_16x16x32_bf16 v[168:171], a[96:99], v[96:99], v[168:171]\n\t" \
  "v_mov_b64 v[92:93], 0\n\t" \
  "v_mov_b64 v[94:95], 0\n\t" \
  "v_mfma_f32_16x16x32_bf16 v[172:175], a[104:107], v[96:99], v[172:175]\n\t" \
  "s_waitcnt vmcnt(4)\n\t" \
  "v_swap_b32 v105, v108\n\t" \
  "v_mfma_f32_16x16x32_bf16 v[168:171], a[100:103], v[100:103], v[168:171]\n\t" \
  "v_swap_b32 v107, v110\n\t" \
  "v_mfma_f32_16x16x32_bf16 v[172:175], a[108:111], v[100:103], v[172:175]\n\t" \
  "v_mov_b64 v[96:97], 0\n\t" \
  "v_mov_b64 v[98:99], 0\n\t" \
  "v_mfma_f32_16x16x32_bf16 v[176:179], a[96:99], v[104:107], v[176:179]\n\t" \
  "v_mov_b64 v[100:101], 0\n\t" \
  "v_mov_b64 v[102:103], 0\n\t" \
  "v_mfma_f32_16x16x32_bf16 v[180:183], a[104:107], v[104:107], v[180:183]\n\t" \
  "s_waitcnt vmcnt(2)\n\t" \
  "v_swap_b32 v113, v116\n\t" \
  "v_mfma_f32_16x16x32_bf16 v[176:179], a[100:103], v[108:111], v[176:179]\n\t" \
  "v_swap_b32 v115, v118\n\t" \
  "v_mfma_f32_16x16x32_bf16 v[180:183], a[108:111], v[108:111], v[180:183]\n\t" \
  "s_nop 0\n\t" \
  "v_mfma_f32_16x16x32_bf16 v[168:171], a[112:115], v[112:115], v[168:171]\n\t" \
  "v_mfma_f32_16x16x32_bf16 v[172:175], a[120:123], v[112:115], v[172:175]\n\t" \
  "s_waitcnt vmcnt(0)\n\t" \
  "v_swap_b32 v121, v124\n\t" \
  "v_mfma_f32_16x16x32_bf16 v[168:171], a[116:119], v[116:119], v[168:171]\n\t" \
  "v_swap_b32 v123, v126\n\t" \
  "v_mfma_f32_16x16x32_bf16 v[172:175], a[124:127], v[116:119], v[172:175]\n\t" \
  "s_nop 0\n\t" \
  "v_mfma_f32_16x16x32_bf16 v[176:179], a[112:115], v[120:123], v[176:179]\n\t" \
  "v_mfma_f32_16x16x32_bf16 v[180:183], a[120:123], v[120:123], v[180:183]\n\t" \
  "v_mfma_f32_16x16x32_bf16 v[176:179], a[116:119], v[124:127], v[176:179]\n\t" \
  "v_mfma_f32_16x16x32_bf16 v[180:183], a[124:127], v[124:127], v[180:183]\n\t" \
  "s_nop 6\n\t" \
  "s_add_i32 s40, s40, 2\n\t" \
  "v_cmp_u_f32 vcc, v168, v176\n\t" \
  "s_cbranch_vccnz 32f\n\t" \
  "33:\n\t" \
  "s_barrier\n\t" \
  "s_add_i32 s85, s40, -1\n\t" \
  "s_and_b32 s82, s85, 2\n\t" \
  "s_lshl_b32 s82, s82, 13\n\t" \
  "s_and_b32 s80, s85, 1\n\t" \
  "s_lshl_b32 s80, s80, 12\n\t" \
  "s_add_i32 s82, s82, s80\n\t" \
  "s_add_i32 s82, s82, 0x84000\n\t" \
  "s_mov_b32 exec_hi, 0\n\t" \
  "buffer_load_dword v184, v200, %[rh], s82 offen sc1\n\t" \
  "buffer_load_dword v185, v200, %[rh], s52 offen sc1\n\t" \
  "s_mov_b64 exec, -1\n\t" \
  "ds_read_b128 a[0:3], v194 offset:0\n\t" \
  "ds_read_b128 a[4:7], v194 offset:1024\n\t" \
  "ds_read_b128 a[8:11], v194 offset:2048\n\t" \
  "ds_read_b128 a[12:15], v194 offset:3072\n\t" \
  "ds_read_b128 a[16:19], v194 offset:4096\n\t" \
  "ds_read_b128 a[20:23], v194 offset:5120\n\t" \
  "ds_read_b128 a[24:27], v194 offset:6144\n\t" \
  "ds_read_b128 a[28:31], v194 offset:7168\n\t" \
  "ds_read_b128 a[32:35], v194 offset:8192\n\t" \
  "ds_read_b128 a[36:39], v194 offset:9216\n\t" \
  "ds_read_b128 a[40:43], v194 offset:10240\n\t" \
  "ds_read_b128 a[44:47], v194 offset:11264\n\t" \
  "ds_read_b128 a[48:51], v194 offset:12288\n\t" \
  "ds_read_b128 a[52:55], v194 offset:13312\n\t" \
  "ds_read_b128 a[56:59], v194 offset:14336\n\t" \
  "ds_read_b128 a[60:63], v194 offset:15360\n\t" \
  "ds_read_b128 a[64:67], v194 offset:16384\n\t" \
  "ds_read_b128 a[68:71], v194 offset:17408\n\t" \
  "ds_read_b128 a[72:75], v194 offset:18432\n\t" \
  "ds_read_b128 a[76:79], v194 offset:19456\n\t" \
  "ds_read_b128 a[80:83], v194 offset:20480\n\t" \
  "ds_read_b128 a[84:87], v194 offset:21504\n\t" \
  "ds_read_b128 a[88:91], v194 offset:22528\n\t" \
  "ds_read_b128 a[92:95], v194 offset:23552\n\t" \
  "ds_read_b128 a[96:99], v194 offset:24576\n\t" \
  "ds_read_b128 a[100:103], v194 offset:25600\n\t" \
  "ds_read_b128 a[104:107], v194 offset:26624\n\t" \
  "ds_read_b128 a[108:111], v194 offset:27648\n\t" \
  "ds_read_b128 a[112:115], v194 offset:28672\n\t" \
  "ds_read_b128 a[116:119], v194 offset:29696\n\t" \
  "ds_read_b128 a[120:123], v194 offset:30720\n\t" \
  "ds_read_b128 a[124:127], v194 offset:31744\n\t" \
  "ds_write_b128 v199, v[168:171] offset:0\n\t" \
  "ds_write_b128 v199, v[172:175] offset:1024\n\t" \
  "ds_write_b128 v199, v[176:179] offset:8192\n\t" \
  "ds_write_b128 v199, v[180:183] offset:9216\n\t" \
  "s_mov_b32 s59, 0\n\t" \
  "s_and_b32 s58, s58, 4\n\t" \
  "s_cmp_lt_i32 s40, 64\n\t" \
  "s_cbranch_scc1 20b\n\t" \
  "s_waitcnt lgkmcnt(0)\n\t" \
  "s_barrier\n\t" \
  "s_mov_b32 s58, 64\n\t" \
  "s_branch 7f\n\t" \
  "22:\n\t" \
  "s_mov_b32 s81, 0\n\t" \
  "s_cmp_eq_u32 s40, 0\n\t" \
  "s_cbranch_scc1 24b\n\t" \
  "s_bitcmp1_b32 s58, 2\n\t" \
  "s_cbranch_scc1 24b\n\t" \
  "s_mov_b64 exec, -1\n\t" \
  "s_sleep 10\n\t" \
  "s_add_u32 s59, s59, 0x10000\n\t" \
  "s_cmp_lt_u32 s59, 0x100000\n\t" \
  "s_cbranch_scc1 19b\n\t" \
  "s_or_b32 s58, s58, 1\n\t" \
  "s_branch 7f\n\t" \
  "14:\n\t" \
  "v_mov_b32 v184, 0\n\t" \
  "s_bitcmp1_b32 s58, 2\n\t" \
  "s_nop 0\n\t" \
  "s_cbranch_scc1 15b\n\t" \
  "s_mov_b32 s84, 950\n\t" \
  "s_add_u32 s59, s59, 0x10000\n\t" \
  "s_cmp_lt_u32 s59, 0x100000\n\t" \
  "s_mov_b64 exec, vcc\n\t" \
  "buffer_load_dword v187, v200, %[rh], s78 offen sc1\n\t" \
  "s_mov_b32 exec_lo, -1\n\t" \
  "s_cbranch_scc1 13b\n\t" \
  "s_mov_b64 exec, -1\n\t" \
  "s_or_b32 s58, s58, 1\n\t" \
  "s_branch 7f\n\t" \
  "37:\n\t" \
  "s_barrier\n\t" \
  "v_mov_b64 v[40:41], 0\n\t" \
  "v_mov_b64 v[42:43], 0\n\t" \
  "v_mov_b64 v[44:45], 0\n\t" \
  "v_mov_b64 v[46:47], 0\n\t" \
  "v_mov_b64 v[48:49], 0\n\t" \
  "v_mov_b64 v[50:51], 0\n\t" \
  "v_mov_b64 v[52:53], 0\n\t" \
  "v_mov_b64 v[54:55], 0\n\t" \
  "v_mov_b64 v[56:57], 0\n\t" \
  "v_mov_b64 v[58:59], 0\n\t" \
  "v_mov_b64 v[60:61], 0\n\t" \
  "v_mov_b64 v[62:63], 0\n\t" \
  "v_mov_b64 v[64:65], 0\n\t" \
  "v_mov_b64 v[66:67], 0\n\t" \
  "v_mov_b64 v[68:69], 0\n\t" \
  "v_mov_b64 v[70:71], 0\n\t" \
  "v_mov_b64 v[72:73], 0\n\t" \
  "v_mov_b64 v[74:75], 0\n\t" \
  "v_mov_b64 v[76:77], 0\n\t" \
  "v_mov_b64 v[78:79], 0\n\t" \
  "v_mov_b64 v[80:81], 0\n\t" \
  "v_mov_b64 v[82:83], 0\n\t" \
  "v_mov_b64 v[84:85], 0\n\t" \
  "v_mov_b64 v[86:87], 0\n\t" \
  "v_mov_b64 v[88:89], 0\n\t" \
  "v_mov_b64 v[90:91], 0\n\t" \
  "v_mov_b64 v[92:93], 0\n\t" \
  "v_mov_b64 v[94:95], 0\n\t" \
  "v_mov_b64 v[96:97], 0\n\t" \
  "v_mov_b64 v[98:99], 0\n\t" \
  "v_mov_b64 v[100:101], 0\n\t" \
  "v_mov_b64 v[102:103], 0\n\t" \
  "v_mov_b64 v[136:137], 0\n\t" \
  "v_mov_b64 v[138:139], 0\n\t" \
  "v_mov_b64 v[140:141], 0\n\t" \
  "v_mov_b64 v[142:143], 0\n\t" \
  "s_add_i32 s40, s40, 2\n\t" \
  "s_branch 33b\n\t" \
  "32:\n\t" \
  "s_and_b32 s80, s58, 36\n\t" \
  "s_cbranch_scc1 33b\n\t" \
  "s_add_i32 s40, s40, -2\n\t" \
  "34:\n\t" \
  "s_or_b32 s59, s59, 128\n\t" \
  "s_and_b32 s76, s40, 3\n\t" \
  "s_lshl_b32 s76, s76, 18\n\t" \
  "s_add_i32 s77, s76, 0x10000\n\t" \
  "s_add_i32 s78, s76, 0x1000\n\t" \
  "s_add_i32 s79, s76, 0x11000\n\t" \
  "35:\n\t" \
  "buffer_load_dwordx4 v[0:3], v210, %[rh], s76 offen offset:0 sc1\n\t" \
  "buffer_load_dwordx4 v[4:7], v210, %[rh], s76 offen offset:1024 sc1\n\t" \
  "buffer_load_dwordx4 v[8:11], v210, %[rh], s77 offen offset:0 sc1\n\t" \
  "buffer_load_dwordx4 v[12:15], v210, %[rh], s77 offen offset:1024 sc1\n\t" \
  "buffer_load_dwordx4 v[16:19], v210, %[rh], s76 offen offset:2048 sc1\n\t" \
  "buffer_load_dwordx4 v[20:23], v210, %[rh], s76 offen offset:3072 sc1\n\t" \
  "buffer_load_dwordx4 v[24:27], v210, %[rh], s77 offen offset:2048 sc1\n\t" \
  "buffer_load_dwordx4 v[28:31], v210, %[rh], s77 offen offset:3072 sc1\n\t" \
  "buffer_load_dwordx4 v[32:35], v210, %[rh], s78 offen offset:0 sc1\n\t" \
  "buffer_load_dwordx4 v[36:39], v210, %[rh], s78 offen offset:1024 sc1\n\t" \
  "buffer_load_dwordx4 v[40:43], v210, %[rh], s79 offen offset:0 sc1\n\t" \
  "buffer_load_dwordx4 v[44:47], v210, %[rh], s79 offen offset:1024 sc1\n\t" \
  "buffer_load_dwordx4 v[48:51], v210, %[rh], s78 offen offset:2048 sc1\n\t" \
  "buffer_load_dwordx4 v[52:55], v210, %[rh], s78 offen offset:3072 sc1\n\t" \
  "buffer_load_dwordx4 v[56:59], v210, %[rh], s79 offen offset:2048 sc1\n\t" \
  "buffer_load_dwordx4 v[60:63], v210, %[rh], s79 offen offset:3072 sc1\n\t" \
  "buffer_load_dwordx4 v[64:67], v211, %[rh], s76 offen offset:0 sc1\n\t" \
  "buffer_load_dwordx4 v[68:71], v211, %[rh], s76 offen offset:1024 sc1\n\t" \
  "buffer_load_dwordx4 v[72:75], v211, %[rh], s77 offen offset:0 sc1\n\t" \
  "buffer_load_dwordx4 v[76:79], v211, %[rh], s77 offen offset:1024 sc1\n\t" \
  "buffer_load_dwordx4 v[80:83], v211, %[rh], s76 offen offset:2048 sc1\n\t" \
  "buffer_load_dwordx4 v[84:87], v211, %[rh], s76 offen offset:3072 sc1\n\t" \
  "buffer_load_dwordx4 v[88:91], v211, %[rh], s77 offen offset:2048 sc1\n\t" \
  "buffer_load_dwordx4 v[92:95], v211, %[rh], s77 offen offset:3072 sc1\n\t" \
  "buffer_load_dwordx4 v[96:99], v211, %[rh], s78 offen offset:0 sc1\n\t" \
  "buffer_load_dwordx4 v[100:103], v211, %[rh], s78 offen offset:1024 sc1\n\t" \
  "buffer_load_dwordx4 v[104:107], v211, %[rh], s79 offen offset:0 sc1\n\t" \
  "buffer_load_dwordx4 v[108:111], v211, %[rh], s79 offen offset:1024 sc1\n\t" \
  "buffer_load_dwordx4 v[112:115], v211, %[rh], s78 offen offset:2048 sc1\n\t" \
  "buffer_load_dwordx4 v[116:119], v211, %[rh], s78 offen offset:3072 sc1\n\t" \
  "buffer_load_dwordx4 v[120:123], v211, %[rh], s79 offen offset:2048 sc1\n\t" \
  "buffer_load_dwordx4 v[124:127], v211, %[rh], s79 offen offset:3072 sc1\n\t" \
  "s_waitcnt vmcnt(0)\n\t" \
  "s_bitcmp1_b32 s58, 2\n\t" \
  "s_cbranch_scc1 36f\n\t" \
  "v_max_u32 v148, v0, v1\n\t" \
  "v_max3_u32 v148, v148, v2, v3\n\t" \
  "v_max3_u32 v148, v148, v4, v5\n\t" \
  "v_max3_u32 v148, v148, v6, v7\n\t" \
  "v_max3_u32 v148, v148, v8, v9\n\t" \
  "v_max3_u32 v148, v148, v10, v11\n\t" \
  "v_max3_u32 v148, v148, v12, v13\n\t" \
  "v_max3_u32 v148, v148, v14, v15\n\t" \
  "v_max3_u32 v148, v148, v16, v17\n\t" \
  "v_max3_u32 v148, v148, v18, v19\n\t" \
  "v_max3_u32 v148, v148, v20, v21\n\t" \
  "v_max3_u32 v148, v148, v22, v23\n\t" \
  "v_max3_u32 v148, v148, v24, v25\n\t" \
  "v_max3_u32 v148, v148, v26, v27\n\t" \
  "v_max3_u32 v148, v148, v28, v29\n\t" \
  "v_max3_u32 v148, v148, v30, v31\n\t" \
  "v_max3_u32 v148, v148, v32, v33\n\t" \
  "v_max3_u32 v148, v148, v34, v35\n\t" \
  "v_max3_u32 v148, v148, v36, v37\n\t" \
  "v_max3_u32 v148, v148, v38, v39\n\t" \
  "v_max3_u32 v148, v148, v40, v41\n\t" \
  "v_max3_u32 v148, v148, v42, v43\n\t" \
  "v_max3_u32 v148, v148, v44, v45\n\t" \
  "v_max3_u32 v148, v148, v46, v47\n\t" \
  "v_max3_u32 v148, v148, v48, v49\n\t" \
  "v_max3_u32 v148, v148, v50, v51\n\t" \
  "v_max3_u32 v148, v148, v52, v53\n\t" \
  "v_max3_u32 v148, v148, v54, v55\n\t" \
  "v_max3_u32 v148, v148, v56, v57\n\t" \
  "v_max3_u32 v148, v148, v58, v59\n\t" \
  "v_max3_u32 v148, v148, v60, v61\n\t" \
  "v_max3_u32 v148, v148, v62, v63\n\t" \
  "v_max3_u32 v148, v148, v64, v65\n\t" \
  "v_max3_u32 v148, v148, v66, v67\n\t" \
  "v_max3_u32 v148, v148, v68, v69\n\t" \
  "v_max3_u32 v148, v148, v70, v71\n\t" \
  "v_max3_u32 v148, v148, v72, v73\n\t" \
  "v_max3_u32 v148, v148, v74, v75\n\t" \
  "v_max3_u32 v148, v148, v76, v77\n\t" \
  "v_max3_u32 v148, v148, v78, v79\n\t" \
  "v_max3_u32 v148, v148, v80, v81\n\t" \
  "v_max3_u32 v148, v148, v82, v83\n\t" \
  "v_max3_u32 v148, v148, v84, v85\n\t" \
  "v_max3_u32 v148, v148, v86, v87\n\t" \
  "v_max3_u32 v148, v148, v88, v89\n\t" \
  "v_max3_u32 v148, v148, v90, v91\n\t" \
  "v_max3_u32 v148, v148, v92, v93\n\t" \
  "v_max3_u32 v148, v148, v94, v95\n\t" \
  "v_max3_u32 v148, v148, v96, v97\n\t" \
  "v_max3_u32 v148, v148, v98, v99\n\t" \
  "v_max3_u32 v148, v148, v100, v101\n\t" \
  "v_max3_u32 v148, v148, v102, v103\n\t" \
  "v_max3_u32 v148, v148, v104, v105\n\t" \
  "v_max3_u32 v148, v148, v106, v107\n\t" \
  "v_max3_u32 v148, v148, v108, v109\n\t" \
  "v_max3_u32 v148, v148, v110, v111\n\t" \
  "v_max3_u32 v148, v148, v112, v113\n\t" \
  "v_max3_u32 v148, v148, v114, v115\n\t" \
  "v_max3_u32 v148, v148, v116, v117\n\t" \
  "v_max3_u32 v148, v148, v118, v119\n\t" \
  "v_max3_u32 v148, v148, v120, v121\n\t" \
  "v_max3_u32 v148, v148, v122, v123\n\t" \
  "v_max3_u32 v148, v148, v124, v125\n\t" \
  "v_max3_u32 v148, v148, v126, v127\n\t" \
  "v_cmp_eq_u32 vcc, -1, v148\n\t" \
  "s_cbranch_vccz 36f\n\t" \
  "s_add_u32 s59, s59, 0x1000000\n\t" \
  "s_cmp_lt_u32 s59, 0x10000000\n\t" \
  "s_cbranch_scc1 35b\n\t" \
  "s_or_b32 s58, s58, 9\n\t" \
  "s_branch 7f\n\t" \
  "36:\n\t" \
  "s_and_b32 s58, s58, 4\n\t" \
  "s_or_b32 s58, s58, 32\n\t" \
  "s_branch 31b\n\t" \
  "18:\n\t" \
  "v_mov_b32 v186, -1\n\t" \
  "s_and_b32 s80, s40, 3\n\t" \
  "s_mul_i32 s77, s80, 0xc000\n\t" \
  "s_add_i32 s77, s77, 0x40000\n\t" \
  "s_lshl_b32 s78, s80, 14\n\t" \
  "s_add_i32 s78, s78, 0x70000\n\t" \
  "s_mov_b32 s57, 0\n\t" \
  "s_add_i32 s96, s77, 0x1000\n\t" \
  "s_and_b32 s98, s40, 2\n\t" \
  "s_lshl_b32 s98, s98, 13\n\t" \
  "s_and_b32 s80, s40, 1\n\t" \
  "s_lshl_b32 s80, s80, 12\n\t" \
  "s_add_i32 s98, s98, s80\n\t" \
  "s_add_i32 s98, s98, 0x84000\n\t" \
  "12:\n\t" \
  "s_or_b32 s58, s58, 3\n\t" \
  "11:\n\t" \
  "buffer_load_dwordx4 v[0:3], v193, %[rh], s77 offen offset:0 sc1\n\t" \
  "buffer_load_dwordx4 v[4:7], v193, %[rh], s77 offen offset:1024 sc1\n\t" \
  "buffer_load_dwordx4 v[8:11], v193, %[rh], s77 offen offset:2048 sc1\n\t" \
  "buffer_load_dwordx4 v[12:15], v193, %[rh], s77 offen offset:3072 sc1\n\t" \
  "buffer_load_dwordx4 v[16:19], v193, %[rh], s96 offen offset:0 sc1\n\t" \
  "buffer_load_dwordx4 v[20:23], v193, %[rh], s96 offen offset:1024 sc1\n\t" \
  "buffer_load_dwordx4 v[24:27], v193, %[rh], s96 offen offset:2048 sc1\n\t" \
  "buffer_load_dwordx4 v[28:31], v193, %[rh], s96 offen offset:3072 sc1\n\t" \
  "buffer_load_dwordx4 v[32:35], v195, %[rh], s77 offen offset:0 sc1\n\t" \
  "buffer_load_dwordx4 v[36:39], v195, %[rh], s77 offen offset:2048 sc1\n\t" \
  "v_mov_b64 v[40:41], 0\n\t" \
  "v_mov_b64 v[42:43], 0\n\t" \
  "v_mov_b64 v[44:45], 0\n\t" \
  "v_mov_b64 v[46:47], 0\n\t" \
  "v_mov_b64 v[48:49], 0\n\t" \
  "v_mov_b64 v[50:51], 0\n\t" \
  "v_mov_b64 v[52:53], 0\n\t" \
  "v_mov_b64 v[54:55], 0\n\t" \
  "v_mov_b64 v[56:57], 0\n\t" \
  "v_mov_b64 v[58:59], 0\n\t" \
  "v_mov_b64 v[60:61], 0\n\t" \
  "v_mov_b64 v[62:63], 0\n\t" \
  "v_mov_b64 v[64:65], 0\n\t" \
  "v_mov_b64 v[66:67], 0\n\t" \
  "v_mov_b64 v[68:69], 0\n\t" \
  "v_mov_b64 v[70:71], 0\n\t" \
  "v_mov_b64 v[72:73], 0\n\t" \
  "v_mov_b64 v[74:75], 0\n\t" \
  "v_mov_b64 v[76:77], 0\n\t" \
  "v_mov_b64 v[78:79], 0\n\t" \
  "v_mov_b64 v[80:81], 0\n\t" \
  "v_mov_b64 v[82:83], 0\n\t" \
  "v_mov_b64 v[84:85], 0\n\t" \
  "v_mov_b64 v[86:87], 0\n\t" \
  "v_mov_b64 v[88:89], 0\n\t" \
  "v_mov_b64 v[90:91], 0\n\t" \
  "v_mov_b64 v[92:93], 0\n\t" \
  "v_mov_b64 v[94:95], 0\n\t" \
  "v_mov_b64 v[96:97], 0\n\t" \
  "v_mov_b64 v[98:99], 0\n\t" \
  "v_mov_b64 v[100:101], 0\n\t" \
  "v_mov_b64 v[102:103], 0\n\t" \
  "v_mov_b64 v[136:137], 0\n\t" \
  "v_mov_b64 v[138:139], 0\n\t" \
  "v_mov_b64 v[140:141], 0\n\t" \
  "v_mov_b64 v[142:143], 0\n\t" \
  "s_waitcnt vmcnt(0)\n\t" \
  "s_add_u32 s59, s59, 1\n\t" \
  "s_bitcmp1_b32 s58, 2\n\t" \
  "s_cbranch_scc1 10b\n\t" \
  "v_max_u32 v148, v0, v1\n\t" \
  "v_max3_u32 v148, v148, v2, v3\n\t" \
  "v_max3_u32 v148, v148, v4, v5\n\t" \
  "v_max3_u32 v148, v148, v6, v7\n\t" \
  "v_max3_u32 v148, v148, v8, v9\n\t" \
  "v_max3_u32 v148, v148, v10, v11\n\t" \
  "v_max3_u32 v148, v148, v12, v13\n\t" \
  "v_max3_u32 v148, v148, v14, v15\n\t" \
  "v_max3_u32 v148, v148, v16, v17\n\t" \
  "v_max3_u32 v148, v148, v18, v19\n\t" \
  "v_max3_u32 v148, v148, v20, v21\n\t" \
  "v_max3_u32 v148, v148, v22, v23\n\t" \
  "v_max3_u32 v148, v148, v24, v25\n\t" \
  "v_max3_u32 v148, v148, v26, v27\n\t" \
  "v_max3_u32 v148, v148, v28, v29\n\t" \
  "v_max3_u32 v148, v148, v30, v31\n\t" \
  "v_max3_u32 v148, v148, v32, v33\n\t" \
  "v_max3_u32 v148, v148, v34, v35\n\t" \
  "v_max3_u32 v148, v148, v36, v37\n\t" \
  "v_max3_u32 v148, v148, v38, v39\n\t" \
  "v_cmp_eq_u32 vcc, -1, v148\n\t" \
  "s_cbranch_vccz 10b\n\t" \
  "s_bitcmp1_b32 s59, 6\n\t" \
  "s_cbranch_scc0 11b\n\t" \
  "7:\n\t" \
  "92:\n\t" \
  "s_mov_b32 s42, s58\n\t" \
  "s_waitcnt vmcnt(0) lgkmcnt(0)\n\t" \
  ""

#define V53 "v0", "v1", "v2", "v3", "v4", "v5", "v6", "v7", "v8", "v9", "v10", "v11", "v12", "v13", "v14", "v15", "v16", "v17", \
  "v18", "v19", "v20", "v21", "v22", "v23", "v24", "v25", "v26", "v27", "v28", "v29", "v30", "v31", "v32", "v33", "v34", "v35", \
  "v36", "v37", "v38", "v39", "v40", "v41", "v42", "v43", "v44", "v45", "v46", "v47", "v48", "v49", "v50", "v51", "v52"
#define V100 V53, "v53", "v54", "v55", "v56", "v57", "v58", "v59", "v60", "v61", "v62", "v63", "v64", "v65", "v66", "v67", "v68", "v69", "v70", \
  "v71", "v72", "v73", "v74", "v75", "v76", "v77", "v78", "v79", "v80", "v81", "v82", "v83", "v84", "v85", "v86", "v87", "v88", \
  "v89", "v90", "v91", "v116", "v117", "v118", "v119"
#define STEP_PV
#define CB_CL "s60", "s61", "s62", "s63", "s64", "s65", "s66", "s67", "s68", "s69",
#define STEPS(BODY, ...) asm volatile(BODY : "=&{v[92:99]}"(X), "+{s[40:55]}"(P) STEP_PV \
  : "{v[120:151]}"(W.v[0]), "{v[152:183]}"(W.v[1]), "{v[184:215]}"(W.v[2]), "{v[216:247]}"(W.v[3]), "{v[100:115]}"(C), [rh] "s"(rh), [wv] "s"(w), [ru] "s"(rum), [sel] "s"(sel) __VA_ARGS__ \
  : "memory", "vcc", "scc", "s56", "s57", "s58", "s59", "s76", "s77", "s78", "s79", "s80", "s81", "s82", "s83", "s84", "s85", "s86", "s87", "s88", "s89", \
    "s90", "s91", "s92", "s93", "s94", "s95", "s96", "s97", V100)
__device__ __forceinline__ void keep_a(const Wts& W) {
#pragma unroll
  for (int i = 0; i < 32; ++i) asm volatile("" :: "a"(W.a[i]));
}
__device__ __forceinline__ char* lds_base(char* smem, int off) { asm volatile("" : "+v"(off)); return smem + off; }
__device__ __forceinline__ u32x2 tr4(unsigned p) { return __builtin_bit_cast(u32x2, __builtin_amdgcn_ds_read_tr16_b64_v4i16((LAS bf16x4*)(uintptr_t)p)); }
__device__ __forceinline__ float rb1(float x) { return __uint_as_float(pk2(0.f, x)); }

__device__ __forceinline__ void steps_init(u32x16& P, int h, int w, unsigned place) {
#pragma unroll
  for (int i = 0; i < 16; ++i) P[i] = 0;
  P[2] = 1; P[3] = CDUR; P[14] = H_MEM + (w >> 1) * CHK + (w & 1) * 4096 + (h & RHO) * 1024; P[15] = place;
}
__device__ __forceinline__ void steps_enter(u32x16& P, Clk& ck, bool dead) {
  ck.sync();
  P[2] = (P[2] & 3u) | (dead ? 4u : 0u); P[6] = ck.c2t << 15; P[11] = ck.ce; P[12] = ck.t2c; P[13] = ck.te;
#pragma unroll
  for (int i = 0; i < 16; ++i) P[i] = __builtin_amdgcn_readfirstlane(P[i]);
}

__device__ __forceinline__ void role_qk(const ChainArgs& a, int h, char* smem, const int w, Gate& G) {
  int lane = opaque_tid(0);
  const int b = h; (void)b;
  u64* prof = a.prof; (void)prof;
  PDECL(L_PRF, 4);
  const float CS = 0.08838834764831845f * 1.4426950408889634f;
  GAS unsigned* abortw = (GAS unsigned*)(a.sync + SY_ABORT);
  bool dead = false;
  Wts W;
  Clk ck; ck.init();
  load_weights<0>(a, h, w, lane, smem, W);
  if (!G.pass(a.sync)) return;
  PZERO;
  const Sel sel = G.sel;
  start_wait(a.sync, abortw, lane, dead, CT_K + h, CT_Q + h);
  const __amdgpu_buffer_rsrc_t rh = rsrc_hot(a.sync, sel), rum = rsrc_u(a.sync);
#pragma unroll
  for (int i = 0; i < 2; ++i) *(u32x4*)(smem + L_KX + (2 * w + i) * 1024 + lane * 16) = ld16(rum, x_slot(1, 0, h, sel) + (2 * w + i) * 1024 + lane * 16);
  auto pm_o = [&](int l) { return 2 * CHK + h * 256 + (l & 1) * 128 + (l >> 3) * 16 + (l & 6) * 2; };
  auto fac = [&](int l, int hf) { return ((l & 7) >> 1) == 2 * (l >> 5) + ((h >> 1) & 1) && ((l >> 3) & 1) == hf ? 1.f : 0.f; };
  u32x16 C, P;
  {
    const int r = lane & 15, g = lane >> 4;
    const unsigned l0 = (unsigned)(uintptr_t)(LAS char*)smem;
    C[0] = lane * 16; C[1] = l0 + L_MEM + lane * 16; C[2] = l0 + L_WTS + w * 4096 + lane * 16; C[3] = C[2] + 65536;
    C[4] = (lane & 2) ? 0xEEEE : 0x4444;
    C[5] = l0 + L_XCH + (16 * w + 2 * (r >> 2) + (r & 1)) * 32 + g * 8 + ((r >> 1) & 1) * 4;
    C[6] = l0 + L_XK + (32 * w + r) * 16 + g * 4;
    C[7] = w == 3 ? H_PX + (h >> 3) * CHK + (h & 7) * 1024 + lane * 16 : w ? (2 * h + 1) * CHK + (w - 1) * 4096 + lane * 16 : l0 + L_XCH + (8 * g + (r >> 2)) * 32 + (r & 3) * 8;
    C[8] = l0 + L_XK + (8 * g + (r >> 2)) * 16 + (r & 1) * 8;
    C[9] = w ? (lane & 1) * 4096 : (lane == 0 ? H_ANN + h * 128 : 0x7fffffffu);
    C[10] = w == 0 ? (h >> 4) * CHK + (h & 15) * 512 + ((lane >> 1) & 1) * 256 + (lane >> 5) * 128 + (lane & 1) * 64 + ((lane >> 3) & 3) * 16 + ((lane >> 2) & 1) * 8 :
            w < 3 ? H_PX + ((w - 1) * 4 + (h >> 3)) * CHK + (h & 7) * 1024 + lane * 16 :
            lane < 32 ? (h >> 4) * CHK + (h & 15) * 512 + lane * 16 : (lane < 48 ? 2 * CHK + h * 256 + (lane - 32) * 16 : 0x7fffffffu);
    C[11] = w == 0 ? pm_o(lane) : (lane < (w == 3 ? 32 : 16) ? H_PX + (w == 3 ? 8 + g : 7 + w) * CHK + hpos(h) * 256 + r * 16 : 0x7fffffffu);
    C[12] = H_TR + (lane & 31) * 128; C[13] = l0 + L_PRF + w * 1024 + (lane >> 6);
    unsigned mk = 0;
#pragma unroll
    for (int i = 0; i < 8; ++i) mk |= (16 * ((i >> 1) & 1) + 8 * (g & 1) + 4 * (g >> 1) + 2 * (i & 1) + (i >> 2) > 16 * (w - 1) + r ? 1u : 0u) << i;
    C[14] = w ? mk : __float_as_uint(fac(lane, 0)); C[15] = __float_as_uint(fac(lane, 1));
    asm volatile("" : "+v"(C));
  }
  SSTAMP(96 + h);
  steps_init(P, h, w, 2 * h * CHK + (3 * w - 3) * 1024);
  GAS unsigned* kcp = ctr(a, CT_Q + h);
  unsigned spins = 0;
  while (true) {
    u32x8 X; u32x16 pv; (void)pv;
    steps_enter(P, ck, dead);
    STEPS(QK_BODY, , [cs] "s"(CS), [kc] "s"(kcp));
    keep_a(W);
    const unsigned res = __builtin_amdgcn_readfirstlane(P[2]), s = __builtin_amdgcn_readfirstlane(P[4]);
    lane = opaque_tid(0);
    if (res & 8u) {
      auto sw = [](float v, int k) { return __int_as_float(k == 0 ? __builtin_amdgcn_mov_dpp(__float_as_int(v), 0xB1, 0xf, 0xf, false) :
                                                           k == 1 ? __builtin_amdgcn_mov_dpp(__float_as_int(v), 0x4E, 0xf, 0xf, false) :
                                                                    __builtin_amdgcn_mov_dpp(__float_as_int(v), 0x141, 0xf, 0xf, false)); };
      float x[5], d = fmaxf(__uint_as_float(X[4]), 0.f);
      d = fmaxf(d, sw(d, 0)); d = fmaxf(d, sw(d, 1)); d = fmaxf(d, sw(d, 2));
#pragma unroll
      for (int i = 0; i < 5; ++i) x[i] = __builtin_amdgcn_exp2f(__uint_as_float(X[i]) - d);
      float z = ((x[0] + x[1]) + (x[2] + x[3])) + x[4];
      z += sw(z, 0); z += sw(z, 1); z += sw(z, 2);
      z = __builtin_amdgcn_rcpf(z);
      const float y = sw(x[4], 0), f = __builtin_amdgcn_exp2f(-d) * z;
      const unsigned pm = (lane & 1) ? pk2(fac(lane, 0) * f, fac(lane, 1) * f) : pk2(x[4] * z, y * z);
      if (!(__uint_as_float(X[5]) < 1.0e18f))
        __builtin_amdgcn_raw_buffer_store_b32(nosent(pm), rh, pm_o(lane), H_P + ((s - 1) & 3) * 3 * CHK, ST_AUX);
    }
    if (res & 64u) break;
    if (res & 32u) { ctr_wait(kcp, min(s + 2, (unsigned)NSEG), abortw, lane, dead); ctr_wait(kcp + 1024, min(s + 2, (unsigned)NSEG), abortw, lane, dead); }
    if ((res & 32u) && s == 1) {
      for (int i = 3 * w - 3; i < min(3 * w, 8); ++i) *(u32x4*)(smem + L_KX + i * 1024 + lane * 16) = ld16(rum, x_slot(1, 1, h, sel) + i * 1024 + lane * 16);
    }
    if (res & 17u) {
      spins |= 63u;
      if (dead || poll_fail(spins, abortw, lane)) dead = true;
    }
  }
  PDUMP(0);
}

__device__ __forceinline__ void role_vo(const ChainArgs& a, int h, char* smem, const int w, Gate& G) {
  int lane = opaque_tid(0);
  const int b = 32 + h; (void)b;
  u64* prof = a.prof; (void)prof;
  PDECL(L_PRF, 4);
  GAS unsigned* abortw = (GAS unsigned*)(a.sync + SY_ABORT);
  bool dead = false;
  Wts W;
  Clk ck; ck.init();
  load_weights<1>(a, h, w, lane, smem, W);
  if (!G.pass(a.sync)) return;
  PZERO;
  const Sel sel = G.sel;
  start_wait(a.sync, abortw, lane, dead, CT_INIT, CT_INIT);
  const __amdgpu_buffer_rsrc_t rh = rsrc_hot(a.sync, sel), rum = rsrc_u(a.sync);
  u32x16 C, P;
  {
    const int r = lane & 15, g = lane >> 4;
    const unsigned l0 = (unsigned)(uintptr_t)(LAS char*)smem;
#pragma unroll
    for (int i = 0; i < 16; ++i) C[i] = H_TR + (lane & 31) * 128;
    C[0] = lane * 16; C[1] = l0 + L_MEM + lane * 16; C[2] = l0 + L_WTS + w * 4096 + lane * 16; C[3] = C[2] + 65536;
    C[4] = (lane & 2) ? 0xEEEE : 0x4444;
    C[5] = l0 + L_XCH + (32 * w + r) * 16 + g * 4;
    C[6] = (lane & 8) ? 0xEEEE : 0x4444;
    C[7] = l0 + L_XCH + (32 * (g >> 1) + 8 * (g & 1) + (r >> 2)) * 16 + (r & 1) * 8;
    C[8] = ((g & 1) * 8 + (g >> 1)) * CHK + r * 16;
    C[13] = l0 + L_PRF + w * 1024 + (lane >> 6);
    asm volatile("" : "+v"(C));
  }
  SSTAMP(128 + h);
  steps_init(P, h, w, SY_UM + (16 * (h & 3) + 2 * w) * CHK + hpos(h) * 256);
  unsigned spins = 0;
  while (true) {
    u32x8 X; u32x16 pv; (void)pv;
    steps_enter(P, ck, dead);
    STEPS(VO_BODY);
    keep_a(W);
    lane = opaque_tid(0);
    if (__builtin_amdgcn_readfirstlane(P[2]) & 64u) break;
    spins |= 63u;
    if (dead || poll_fail(spins, abortw, lane)) dead = true;
  }
  PDUMP(0);
}

__device__ __forceinline__ void calibrate(const ChainArgs& a, int cal, int slot, u64 entry) {
  const u32x4 rs = own_rsrc(a.sync, SY_BYTES);
  unsigned best[2] = {1u << 30, 1u << 30};
  for (int i = 0;; ++i) {
    if (i >= CALN && ((min(best[0], best[1]) < CALT && max(best[0], best[1]) - min(best[0], best[1]) > 120u) || __builtin_amdgcn_s_memrealtime() - entry > CALM)) break;
#pragma unroll
    for (int hf = 0; hf < 2; ++hf) {
      u64 c0, c1;
      unsigned z = 0;
      asm volatile("s_mov_b64 exec, 1\n\ts_memtime %0\n\ts_waitcnt lgkmcnt(0)\n\tbuffer_atomic_or %2, off, %3, %4 sc0 sc1\n\ts_waitcnt vmcnt(0)\n\ts_memtime %1\n\ts_mov_b64 exec, -1\n\ts_waitcnt lgkmcnt(0)"
                   : "=&s"(c0), "=&s"(c1), "+v"(z) : "s"(rs), "s"(cal * PAGE + (slot >> 1) * CHK + hf * 8192 + (slot & 1) * 4096) : "memory");
      best[hf] = min(best[hf], (unsigned)(c1 - c0));
    }
  }
  const u64 hs = (__builtin_amdgcn_s_memrealtime() & ~1ull) | ((best[1] < best[0] ? 1u : 0u) ^ die_half());
  if (opaque_tid(0) == 0) __hip_atomic_store((GAS u64*)ctr(a, CT_CAL + (slot >> 1)) + (slot & 1), hs, __ATOMIC_RELAXED, __HIP_MEMORY_SCOPE_AGENT);
}
__device__ __forceinline__ u32x4 pk8(f32x4 a, f32x4 b) { return (u32x4){pk2(a[0], a[1]), pk2(a[2], a[3]), pk2(b[0], b[1]), pk2(b[2], b[3])}; }
__device__ __forceinline__ unsigned pk2v(float a, float b) { unsigned r; asm volatile("v_cvt_pk_bf16_f32 %0, %1, %2" : "=v"(r) : "v"(a), "v"(b)); return r; }
__device__ __forceinline__ u32x4 pk8v(f32x4 a, f32x4 b) { asm volatile("s_nop 7"); return (u32x4){pk2v(a[0], a[1]), pk2v(a[2], a[3]), pk2v(b[0], b[1]), pk2v(b[2], b[3])}; }
__device__ __forceinline__ void st16g(u16* p, u32x4 v) { asm volatile("global_store_dwordx4 %0, %1, off " OUTP "\n\ts_nop 1" :: "v"(p), "v"(v) : "memory"); }
__device__ __forceinline__ void role_cb(const ChainArgs& a, int cbi, char* smem, const int w, Gate& G) {
  int lane = opaque_tid(0);
  const int b = 64 + cbi; (void)b;
  const int par = cbi >> 5, ci = cbi & 31, J = ci >> 3;
  u64* prof = a.prof; (void)prof;
  PDECL(65536, 8); PZERO;
  GAS unsigned* abortw = (GAS unsigned*)(a.sync + SY_ABORT);
  bool dead = false;
  const int tw = 2 * ci + (w & 1);
  const unsigned mo = (tw >> 5) * CHK + (tw & 31) * 256;
  Sel sel = 0;
  auto start = [&]() {
    lane = opaque_tid(0);
    SSTAMP(0 + ci);
    u32x4 gv;
    G.ask(a.sync, gv);
    {
      const __amdgpu_buffer_rsrc_t rx = mk_rsrc((void*)a.X, 4u << 20);
      u32x4 v[16];
#pragma unroll
      for (int i = 0; i < 16; ++i) v[i] = __builtin_amdgcn_raw_buffer_load_b128(rx, (unsigned)(cbi * 4096 + w * 1024 + lane * 16), (unsigned)(i * 262144), 0);
#pragma unroll
      for (int i = 0; i < 16; ++i) asm volatile("" :: "v"(v[i]));
      asm volatile("" : "+v"(gv));
    }
    SSTAMP(224 + ci);
    if (!G.pass(a.sync, gv)) return false;
    sel = G.sel;
    SSTAMP(64 + ci);
    const __amdgpu_buffer_rsrc_t rh = rsrc_hot(a.sync, sel), rum = rsrc_u(a.sync);
    const unsigned s1v = ~(unsigned)(lane >> 6);
    const u32x4 sv = {s1v, s1v, s1v, s1v};
    unsigned m0 = 0;
    if (par == 0 && w < 2) {
      const u16* m = a.mem0 + (2 * (lane >> 4) + ((lane >> 2) & 1)) * DM + 16 * tw + (lane & 8);
      m0 = m[lane & 3] | (unsigned)m[7 - (lane & 3)] << 16;
    }
#pragma unroll
    for (int i = 0; i < 2; ++i) {
      const unsigned o = w * 2048 + i * 1024 + lane * 16;
#pragma unroll
      for (int k = 0; k < 4; ++k) st16(rum, near_half(SY_UM + (64 * k + cbi) * CHK, sel) + o, sv);
      if (cbi >= 2 && cbi < 35) st16(rh, cbi * CHK + o, sv);
      if ((cbi & 15) < 10) st16(rh, H_PX + cbi * CHK + o, sv);
    }
    if (par == 0 && w < 2) st4(rh, H_MEM + mo + lane * 4, nosent(m0));
    asm volatile("s_waitcnt vmcnt(0)" ::: "memory");
    lds_barrier();
    if (w == 0 && lane == 0) ctr_add(a, CT_INIT, 1u);
    SSTAMP(256 + ci);
    ctr_wait(ctr(a, CT_INIT), 64u, abortw, lane, dead);
    return true;
  };
  Wts W;
#pragma unroll
  for (int i = 0; i < 32; ++i) asm volatile("" : "=a"(W.a[i]));
  u32x16 C, P; u32x8 C2;
  {
    const unsigned l0 = (unsigned)(uintptr_t)(LAS char*)smem, oor = 0x7fffffffu;
    const int r = lane & 7, t = (lane >> 3) & 1, g = lane >> 4, e = w & 1;
    const bool L = w < 2;
    C[0] = lane * 16;
    C[1] = L ? l0 + 32768 + e * 8192 + (lane & 2) * 512 + ((lane & 1) * 32 + (lane >> 2)) * 16 : e * CHK + t * 512 + (g & 1) * 256 + (r >> 2) * 128 + (g >> 1) * 64 + (r & 3) * 16;
    C[2] = L ? (4 * t + g) * 1024 + r * 16 : l0 + 81920 + e * 32768 + lane * 16;
    C[3] = L ? l0 + e * 1024 + lane * 8 : 2 * CHK + e * 4096 + (4 * (g >> 1) + 2 * t + (g & 1)) * 256 + 128 + r * 16;
    C[4] = ((16 * e + (lane >> 2)) * OUTW + DM + 32 * ci + 8 * (lane & 3)) * 2;
    C[5] = l0 + (L ? e * 8192 + lane * 16 : e * 512 + lane * 8); C[6] = l0 + 16384 + lane * 16; C[7] = l0 + 32768 + (L ? e * 8192 + 4096 : e * 2048) + lane * 16;
    C[8] = (lane & 31) * 128;
    C[9] = (8 * (g & 1) + 4 * (g >> 1) + 2 * t + (r >> 2)) * 16 + 4 * (r < 4 ? r : 7 - r);
    C[10] = (lane | e) ? oor : ci * 128;
    C[11] = L ? SY_CT + (CT_U + 16 * e + (lane & 15)) * 128 : (t ? 0xEEEE : 0x4444); C[12] = L ? l0 + 81920 + e * 32768 + lane * 16 : ((lane & 2) ? 0xEEEE : 0x4444); C[13] = r < 4 ? 0x01000504u : 0x07060302u;
    C[14] = l0 + 49152 + 32 * e + (lane >> 6); C[15] = l0 + 49152 + (lane >> 6);
    C2[0] = L ? (lane < 16 ? lane * 16 : oor) : ux_tile(2 * ci) + lane * 16;
    C2[1] = lane == 0 && e && ci < 8 ? SY_CT + (CT_DONE + 8 * par + ci) * 128 : oor;
    C2[2] = (L ? ux_tile(2 * ci) : H_PX + 2 * e * CHK) + lane * 16;
#pragma unroll
    for (int n = 0; n < 4; ++n) C2[3 + n] = L ? H_PX + (8 + e) * CHK + (J ^ n) * 2048 + lane * 16 : C2[2] + CHK;
    C2[7] = l0 + 65536 + w * 2048 - 64 + (lane >> 6);
    asm volatile("" : "+v"(C), "+v"(C2));
#pragma unroll
    for (int i = 0; i < 16; ++i) P[i] = par - 2;
    P[2] = 48; P[6] = P[7] = K_DEF * TU; P[8] = SY_UM + tw * CHK + J * 2048; P[9] = H_MEM + mo; P[10] = 2 * CHK + J * 256; P[11] = e * 16 * UXH; P[12] = H_DLT + par * 4096;
  }
  Clk ck; ck.init();
  unsigned spins = 0;
  if (!start()) return;
  while (true) {
    u32x16 pv; (void)pv;
    unsigned res = __builtin_amdgcn_readfirstlane(P[2]);
    if (res & 64u) break;
    const u32x4 rh2 = own_rsrc(a.sync + near_half(SY_HOT, sel), H_BYTES - 8192), ra = own_rsrc(a.sync, SY_BYTES), ro = own_rsrc(a.out, S_LEN * OUTW * 2);
    if (res & 1u) {
      spins |= 63u;
      if (dead || poll_fail(spins, abortw, opaque_tid(0))) dead = true;
    }
    ck.sync();
    P[2] = (res & 58u) | (dead ? 4u : 0u); P[1] = ck.ce; P[3] = ck.te; P[4] = ck.t2c; P[5] = ck.c2t << 15;
#pragma unroll
    for (int i = 0; i < 16; ++i) P[i] = __builtin_amdgcn_readfirstlane(P[i]);
    asm volatile(CB_BODY : "+{s[40:55]}"(P) STEP_PV
                 : "{v[192:207]}"(C), "{v[208:215]}"(C2), [rh] "s"(rh2), [ru] "s"(ra), [ro] "s"(ro), [wv] "s"(w), [sel] "s"(sel)
                 : "memory", "vcc", "scc", CB_CL "s56", "s57", "s58", "s59", "s76", "s77", "s78", "s79", "s80", "s81", "s82", "s83", "s84", "s85", "s86", "s87", "s88", "s89", "s90", "s91", "s92", "s93", "s94", "s95", "s96", "s97", "s98", "s99", "s100", "s101", V53, "v53", "v54", "v55", "v56", "v57", "v58", "v59", "v60", "v61", "v62", "v63", "v64", "v65", "v66", "v67", "v68", "v69", "v70", "v71", "v72", "v73", "v74", "v75", "v76", "v77", "v78", "v79", "v80", "v81", "v82", "v83", "v84", "v85", "v86", "v87", "v88", "v89", "v90", "v91", "v92", "v93", "v94", "v95", "v96", "v97", "v98", "v99", "v100", "v101", "v102", "v103", "v104", "v105", "v106", "v107", "v108", "v109", "v110", "v111", "v112", "v113", "v114", "v115", "v116", "v117", "v118", "v119", "v120", "v121", "v122", "v123", "v124", "v125", "v126", "v127", "v128", "v129", "v130", "v131", "v132", "v133", "v134", "v135", "v136", "v137", "v138", "v139", "v140", "v141", "v142", "v143", "v144", "v145", "v146", "v147", "v148", "v149", "v150", "v151", "v152", "v153", "v154", "v155", "v156", "v157", "v158", "v159", "v160", "v161", "v162", "v163", "v164", "v165", "v166", "v167", "v168", "v169", "v170", "v171", "v172", "v173", "v174", "v175", "v176", "v177", "v178", "v179", "v180", "v181", "v182", "v183", "v184", "v185", "v186", "v187", "v188", "v189", "v190", "v191");
    keep_a(W);
  }
  PDUMP(2);
}

__device__ __forceinline__ void bg_drain() {
  asm volatile("s_waitcnt vmcnt(0)" ::: "memory");
  __builtin_amdgcn_s_barrier();
  asm volatile("" ::: "memory");
}
#define BG_SLP 16
#define A3_SLP 16
#define WAH 4
#define DSP 4
#define SIGQ 2
__device__ __forceinline__ void task_w(const ChainArgs& a, int kind, int h, char* smem, int wv, Gate& G) {
  const int lane = opaque_tid(0), w = wv, r = lane & 15, g = lane >> 4;
  const __amdgpu_buffer_rsrc_t rw = mk_rsrc(kind == 0 ? a.Wq : (kind == 1 ? a.Wk : a.Wv), 8u << 20), rx = mk_rsrc(a.X, 4u << 20);
  const unsigned lds0 = (unsigned)(uintptr_t)(LAS char*)smem + w * 4096;
  unsigned vx[2], vw[4];
#pragma unroll
  for (int j = 0; j < 2; ++j) {
    const int rr = 8 * j + (lane >> 3);
    const int tb = kind == 0 ? rr : (kind == 1 ? 16 * ((rr >> 1) & 1) + 8 * ((rr >> 2) & 1) + 4 * (rr >> 3) + 2 * (rr & 1) : 8 * (rr >> 2) + (rr & 3));
    vx[j] = tb * DM * 2 + ((lane & 7) ^ (rr >> 1)) * 16;
  }
#pragma unroll
  for (int j = 0; j < 4; ++j) { const int row = (w * 4 + j) * 4 + g; vw[j] = row * HD * 2 + (r ^ swz_b(row)) * 16; }
  const u64 wo = kind ? bg_order(1) : bg_order(0);
  auto xo = [&](int q) { return ((int)((wo >> (16 * q + 4 * w)) & 15) ^ (h & RHO)) * 128; };
  unsigned dT = (kind == 0 ? 16 : (kind == 1 ? 1 : 4)) * DM * 2;
  asm volatile("s_nop 4" : "+s"(dT));
  const unsigned vx0 = ((w & 1) ? vx[1] : vx[0]) + (w >> 1) * dT;
  auto st_w = [&](int n) {
#pragma unroll
    for (int j = 0; j < 4; ++j) dma16(rw, vw[j], (64 * ((int)((wo >> (4 * n)) & 15) ^ (h & RHO)) * HD + 128 * h) * 2, lds0 + (n & 7) * 16384 + j * 1024, W_NT & 16);
    dma16(rx, vx0, ((int)((wo >> (4 * n)) & 15) ^ (h & RHO)) * 128, lds0 - 3072 * w + 131072 + (n & 7) * 4096);
  };
  auto st_x = [&](int s, int b, int q, int j, bool pad) { dma16(rx, vx[j & 1], s * 65536 + (j >> 1) * dT + xo(q), lds0 + (b * 4 + q) * 16384 + j * 1024, false, pad); };
  if (KQ_D && kind < 2) while (__builtin_amdgcn_s_memrealtime() - G.entry < KQ_D) __builtin_amdgcn_s_sleep(8);
  __builtin_amdgcn_s_waitcnt(0x0F70);
  u32x4 W[32][2], gv;
  char* xb[3][2];
#pragma unroll
  for (int i = 0; i < 6; ++i) xb[i >> 1][i & 1] = lds_base(smem, (i >> 1) * 65536 + (r >> 3) * 1024 + (r & 7) * 128 + ((4 * (i & 1) + g) ^ (r >> 1)) * 16);
  f32x4 acc[2][4];
#pragma unroll
  for (int i = 0; i < 4; ++i) acc[1][i] = vzero();
#pragma unroll
  for (int n = 0; n < WAH; ++n) st_w(n);
#pragma unroll
  for (int n = 0; n < 16; ++n) {
    asm volatile("s_waitcnt vmcnt(%0)" :: "n"(4 * WAH - 4 + (n < 16 - WAH ? WAH - 1 : 15 - n)) : "memory");
    lds_barrier();
    u32x4 f[4], y[4];
#pragma unroll
    for (int i = 0; i < 4; ++i) {
#pragma unroll
      for (int q = 0; q < 2; ++q) {
        const int row = (i & 1) * 32 + 8 * g + 4 * q + (r >> 2);
        const u32x2 u = __builtin_bit_cast(u32x2, __builtin_amdgcn_ds_read_tr16_b64_v4i16((LAS bf16x4*)(smem + (n & 7) * 16384 + row * 256 + (((4 * w + (r & 3)) ^ swz_b(row)) * 16) + 8 * (i >> 1))));
        f[i][2 * q] = u[0]; f[i][2 * q + 1] = u[1];
      }
      y[i] = *(const u32x4*)(xb[2][i & 1] + (n & 7) * 4096 + (i >> 1) * 2048);
    }
    SB0;
    if (n == 16 - WAH) G.ask(a.sync, gv);
    if (n == 15) asm volatile("" : "+v"(gv));
    if (n + WAH < 16) st_w(n + WAH);
    else {
#pragma unroll
      for (int j = 0; j < 4; ++j) st_x(1 + ((n + WAH - 16) >> 2), (n + WAH - 16) >> 2, (n + WAH) & 3, j, true);
    }
#pragma unroll
    for (int i = 0; i < 8; ++i)
      asm volatile("s_nop 1\n\tv_mfma_f32_16x16x32_bf16 %0, %1, %2, %0" : "+v"(acc[1][i & 3]) : "v"(f[2 * (i & 1) + (i >> 2)]), "v"(y[(i & 2) + (i >> 2)]));
#pragma unroll
    for (int i = 0; i < 4; ++i) { asm volatile("" : "+a"(f[i])); W[2 * n + (i & 1)][i >> 1] = f[i]; }
  }
  if (!G.pass(a.sync, gv)) { bg_drain(); return; }
  SSTAMP(160 + h);
  const Sel sel = G.sel;
  const __amdgpu_buffer_rsrc_t ru = mk_rsrc(a.sync, SY_BYTES);
  const unsigned sgo = (w | lane) ? 0x7fffffffu : SY_CT + (CT_Q + 32 * kind + h) * 128;
  const u32x4 rx2 = own_rsrc(a.X, 4u << 20);
  unsigned vq[4][4];
#pragma unroll
  for (int i = 0; i < 16; ++i) { vq[i >> 2][i & 3] = vx[i & 1] + ((i >> 1) & 1) * dT + xo(i >> 2); asm volatile("" : "+v"(vq[i >> 2][i & 3])); }
  u32x4 xf[4][2], o;
  auto rd = [&](int b, int k, int T) { xf[k & 3][T] = *(const u32x4*)(xb[b][k & 1] + (k >> 1) * 4096 + T * 2048); };
  auto signal = [&](int s) {
    st4(ru, sgo, s);
    if (w == 0 && lane == 0 && s >= 0 && (s < 5 || (s & 7) == 0 || (s & 15) == 2 || s > 61)) {
    }
  };
#pragma unroll
  for (int T = 0; T < 2; ++T) st16s(pk8v(acc[1][2 * T], acc[1][2 * T + 1]), ru, lane * 16 + T * 4096, x_slot(kind, 0, h, sel) + w * 1024);
#pragma unroll
  for (int i = 4 * WAH; i < 28; ++i) st_x(1 + (i >> 4), i >> 4, (i >> 2) & 3, i & 3, true);
  wait_vm<26>();
  lds_barrier();
#pragma unroll
  for (int i = 0; i < 6; ++i) rd(0, i >> 1, i & 1);
  WST(kind == 2, 18);
  auto seg = [&](int s, int b) {
    const unsigned so = x_slot(kind, s - 1, h, sel) + w * 1024;
    WST(kind == 2 && s > 0 && s < 4, 18 + s);
#pragma unroll
    for (int k = 0; k < 32; ++k)
#pragma unroll
      for (int i = 0; i < 4; ++i) {
        if (k == 0) asm volatile("v_mfma_f32_16x16x32_bf16 %0, %1, %2, 0" : "=&v"(acc[b][i]) : "a"(W[k][i & 1]), "v"(xf[k & 3][i >> 1]));
        else asm volatile("v_mfma_f32_16x16x32_bf16 %0, %1, %2, %0" : "+v"(acc[b][i]) : "a"(W[k][i & 1]), "v"(xf[k & 3][i >> 1]));
        SB0;
        const int q = k >> 3, n = 4 * k + i;
        if ((k & 7) == 5 && i == 0) {
          if (q == SIGQ) wait_vm<4 * SIGQ>(); else wait_vm<20>();
          if (q == 1 && __builtin_expect(s - 2u < W_NOW, 0)) { wait_vm<4>(); __builtin_amdgcn_s_barrier(); signal(s - 1); }
          else if (q == 0 && b == 0 && __builtin_expect(s == 1, 0)) { wait_vm<32 - 4 * WAH>(); __builtin_amdgcn_s_barrier(); signal(0); }
          else __builtin_amdgcn_s_barrier();
          asm volatile("" ::: "memory");
        }
        if (i == 1 || i == 2) rd(k + 3 < 32 ? b : b ^ 1, (k + 3) & 31, i - 1);
        {
          const int m = (n + 128 - 23) & 127, j = (m & 31) / DSP, q0 = m >> 5;
          if (j < 4 && (m & 31) % DSP == 0) asm volatile("s_add_i32 m0, %0, %1" :: "s"(lds0), "n"(((b ^ (q0 == 0) ^ (n < 23)) * 4 + ((q0 + 3) & 3)) * 16384 + j * 1024) : "m0", "scc");
          if (j < 4 && (m & 31) % DSP == 1)
            asm volatile("buffer_load_dwordx4 %0, %1, %2 offen lds" :: "v"(vq[(q0 + 3) & 3][j & 3]), "s"(rx2), "s"((s + 2 - (q0 == 0) - (n < 23)) * 65536) : "memory");
        }
        if (k == 8 * SIGQ + 5 && i == 2) signal(s - 1);
        if (k >= 1 && k < 5 && (i == 0 || i == 3)) { const f32x4 x = acc[b ^ 1][k - 1]; o[2 * (~k & 1) + (i & 1)] = pk2v(x[2 * (i & 1)], x[2 * (i & 1) + 1]); }
        if ((k == 2 || k == 4) && i == 3) { __builtin_amdgcn_raw_buffer_store_b128(o, ru, (unsigned)(lane * 16 + (k >> 2) * 4096), so, ST_AUX); asm volatile("s_nop 0" :: "v"(o)); }
        SB0;
      }
  };
  for (int s = 1;; s += 2) {
    seg(s, 0);
    if (s == NSEG - 1) break;
    seg(s + 1, 1);
  }
#pragma unroll
  for (int T = 0; T < 2; ++T) st16s(pk8v(acc[0][2 * T], acc[0][2 * T + 1]), ru, lane * 16 + T * 4096, x_slot(kind, NSEG - 1, h, sel) + w * 1024);
  bg_drain();
  signal(NSEG - 1);
}
__device__ __forceinline__ float blo(unsigned d) { return __uint_as_float(d << 16); }
__device__ __forceinline__ float bhi(unsigned d) { return __uint_as_float(d & 0xffff0000u); }
constexpr int A2_CSTR = 4160;
constexpr int A2_UOFF = 4 * A2_CSTR + 16 * 80;
constexpr int A2_USTR = 80 * 80;
constexpr int A2_WLDS = 4 * A2_CSTR + 2 * A2_USTR;
__device__ __forceinline__ void a2_wave(const ChainArgs& a, int d0, int d1, char* smem, int w) {
  const int lane = opaque_tid(0);
  char* Wb = smem + 1024 + w * A2_WLDS;
  char* U = Wb + A2_UOFF;
  const __amdgpu_buffer_rsrc_t rf = mk_rsrc((void*)a.Assm, 4u << 20), rd_ = mk_rsrc((void*)a.AD, 2048);
  const __amdgpu_buffer_rsrc_t rkv = mk_rsrc((void*)a.kvT, 4u << 20);
  u32x4 hi[4], uv[2][4];
  u32x2 lo[4];
  unsigned dn;
  auto ld_h = [&](int d) {
#pragma unroll
    for (int i = 0; i < 4; ++i) {
      hi[i] = __builtin_amdgcn_raw_buffer_load_b128(rf, (unsigned)((255 - 64 * i - lane) * 16), (unsigned)(d * 4096), 2);
      lo[i] = __builtin_amdgcn_raw_buffer_load_b64(rf, i == 3 && lane == 63 ? 0x7fffffffu : (255 - 64 * i - lane) * 16 - 8, (unsigned)(d * 4096), 2);
    }
    dn = __builtin_amdgcn_raw_buffer_load_b16(rd_, 0, 2 * d, 0);
  };
  auto ld_u = [&](int d) {
#pragma unroll
    for (int i = 0; i < 8; ++i) uv[i & 1][i >> 1] = __builtin_amdgcn_raw_buffer_load_b128(rkv, (unsigned)max(((i >> 1) * 64 + lane) * 16 - 8 * (i & 1), 0), (unsigned)(d * 4096), LD_AUX);
  };
  ld_h(d0);
  {
    bool dead = false;
    ctr_wait<BG_SLP>(ctr(a, CT_A1 + (d0 >> 8)), 64u, (GAS unsigned*)(a.sync + SY_ABORT), lane, dead);
  }
  ld_u(d0);
  if (lane < 12) *(f32x4*)(Wb + (lane / 3) * A2_CSTR + (256 + lane % 3) * 16) = vzero();
#pragma unroll
  for (int i = 0; i < 4; ++i) if (64 * (i & 1) + lane < 80) *(f32x4*)(U + (i >> 1) * A2_USTR - 16 * 80 + (64 * (i & 1) + lane) * 16) = vzero();
  const int r = lane & 15, g = lane >> 4;
#pragma unroll 1
  for (int d = d0; d < d1; d += 96) {
    u64 at[6]; (void)at;
    AST(0);
#pragma unroll
    for (int i = 0; i < 4; ++i) {
      const unsigned src[6] = {lo[i][0], lo[i][1], hi[i][0], hi[i][1], hi[i][2], hi[i][3]};
      unsigned f[10];
#pragma unroll
      for (int x = 0; x < 10; ++x) {
        if ((x & 1) == 0) f[x] = __builtin_amdgcn_alignbit(src[5 - x / 2], src[5 - x / 2], 16);
        else f[x] = __builtin_amdgcn_perm(src[5 - (x + 1) / 2], src[5 - (x - 1) / 2], 0x07060100u);
      }
#pragma unroll
      for (int m = 0; m < 4; ++m) *(u32x4*)(Wb + m * A2_CSTR + (64 * i + lane) * 16) = (u32x4){f[3 - m], f[5 - m], f[7 - m], f[9 - m]};
    }
    if (lane == 0) uv[1][0] = (u32x4){0u, 0u, uv[1][0][0], uv[1][0][1]};
#pragma unroll
    for (int i = 0; i < 8; ++i) {
      const int ch = (i >> 1) * 64 + lane;
      *(u32x4*)(U + (i & 1) * A2_USTR + (ch >> 2) * 80 + (ch & 3) * 16) = uv[i & 1][i >> 1];
    }
    const float Dd = blo(dn);
    asm volatile("" :: "v"(Dd) : "memory");
    AST(1);
    ld_h(d + 96 < d1 ? d + 96 : D_A); ld_u(d + 96 < d1 ? d + 96 : D_A);
    f32x4 acc[4][2];
#pragma unroll
    for (int i = 0; i < 8; ++i) acc[i >> 1][i & 1] = vzero();
    const char* pa = Wb + (r & 3) * A2_CSTR + 16 * (255 - (r >> 2) + g) - 3 * 1024 - 64;
    const char* pb = U + (r - 1) * 80 + 16 * g;
    bf16x8 fa[2][4], fb[2][8];
    auto rd = [&](int b, int n, int k) {
      if (n == 0 || n > 8) fa[b][n ? n - 8 : 0] = *(const bf16x8*)(pa + 1024 * (3 - (n ? n - 8 : 0)) + 64 * (1 - k));
      else fb[b][n - 1] = *(const bf16x8*)(pb + ((n - 1) & 1) * A2_USTR + 80 * (16 * ((n - 1) >> 1) + 1 - k));
    };
#pragma unroll
    for (int n = 0; n < 12; ++n) rd(0, n, 0);
    AST(2);
#pragma unroll 1
    for (int k = 0; k < 16; k += 2) {
#pragma unroll
      for (int b = 0; b < 2; ++b) {
        int q = 0;
#pragma unroll
        for (int j = 0; j < 4; ++j)
#pragma unroll
          for (int i = j; i < 4; ++i)
#pragma unroll
            for (int mt = 0; mt < 2; ++mt) {
              asm volatile("v_mfma_f32_16x16x32_bf16 %0, %1, %2, %0" : "+v"(acc[i][mt]) : "v"(fa[b][j]), "v"(fb[b][2 * (i - j) + mt]));
              SB0;
              if (q < 12) rd(b ^ 1, q, 1);
              if (q == 12) { pa -= 64; pb -= 80; }
              ++q;
              SB0;
            }
      }
    }
    AST(3);
    u32x4 kv[4];
#pragma unroll
    for (int i = 0; i < 4; ++i) kv[i] = *(const u32x4*)(U + (16 * i + r) * 80 + 16 * g);
    asm volatile("s_nop 7\n\ts_waitcnt vmcnt(0)" ::: "memory");
#pragma unroll
    for (int i = 0; i < 8; ++i) asm volatile("" : "+v"(acc[i >> 1][i & 1]));
    if (lane == 0 && d > d0) ctr_add(a, CT_A2 + ((d - 96) >> 6), 1u);
#pragma unroll
    for (int i = 0; i < 4; ++i) {
      const f32x4 x = acc[i][1], y = acc[i][0];
      st16s((u32x4){pk2(fmaf(blo(kv[i][0]), Dd, x[0]), fmaf(bhi(kv[i][0]), Dd, x[1])), pk2(fmaf(blo(kv[i][1]), Dd, x[2]), fmaf(bhi(kv[i][1]), Dd, x[3])),
                    pk2(fmaf(blo(kv[i][2]), Dd, y[0]), fmaf(bhi(kv[i][2]), Dd, y[1])), pk2(fmaf(blo(kv[i][3]), Dd, y[2]), fmaf(bhi(kv[i][3]), Dd, y[3]))},
            rkv, (unsigned)(i * 1024 + r * 64 + g * 16), (unsigned)(d * 4096));
    }
    AST(4);
  }
  wait_vm<0>();
  if (lane == 0) ctr_add(a, CT_A2 + ((d0 + (d1 - 1 - d0) / 96 * 96) >> 6), 1u);
}
__device__ __forceinline__ void task_u(const ChainArgs& a, int h, char* smem, int wv, Gate& G) {
  const int lane = opaque_tid(0);
  const int w = wv;
  GAS unsigned* abortw = (GAS unsigned*)(a.sync + SY_ABORT);
  bool dead = false;
  char* sb = smem + w * 32768;
  const __amdgpu_buffer_rsrc_t rwo = mk_rsrc(a.Wo, 8u << 20);
  const unsigned lds0 = (unsigned)(uintptr_t)(LAS char*)sb;
  unsigned wo4[4];
#pragma unroll
  for (int i = 0; i < 4; ++i) { const int row = i * 4 + (lane >> 4); wo4[i] = row * DM * 2 + (((lane & 15) ^ swz_b(row)) * 16); }
  asm volatile("s_nop 4");
  auto stage = [&](int n) {
    const int p = (U_ORD >> (4 * n)) & 3;
#pragma unroll
    for (int i = 0; i < 16; ++i)
      dma16(rwo, wo4[i & 3], (unsigned)(((128 * h + 64 * (p & 1) + 16 * (i >> 2)) * DM + 256 * w + 128 * (p >> 1)) * 2), lds0 + (n & 1) * 16384 + i * 1024, W_NT & 2);
  };
  __builtin_amdgcn_s_waitcnt(0x0F70);
  u32x4 W[16][4];
  stage(0); stage(1);
#pragma unroll
  for (int n = 0; n < 4; ++n) {
    const int p = (U_ORD >> (4 * n)) & 3;
    if (n < 3) wait_vm<16>(); else wait_vm<0>();
#pragma unroll
    for (int t = 0; t < 8; ++t) {
      const u32x8 f = tr_frag(sb + (n & 1) * 16384, t, lane);
      u32x4 f0 = {f[0], f[1], f[2], f[3]}, f1 = {f[4], f[5], f[6], f[7]};
      asm volatile("" : "+a"(f0), "+a"(f1));
      W[8 * (p >> 1) + t][2 * (p & 1)] = f0; W[8 * (p >> 1) + t][2 * (p & 1) + 1] = f1;
    }
    asm volatile("s_waitcnt lgkmcnt(0)" ::: "memory");
    if (n + 2 < 4) stage(n + 2);
  }
  lds_barrier();
  if (!G.pass(a.sync)) return;
  SSTAMP(192 + h);
  const Sel sel = G.sel;
  const u32x4 ru = own_rsrc(a.sync, SY_BYTES);
  u32x4 V[2][8], pc[2], o[2];
  f32x4 c[2][2];
  unsigned cv, cw, vo[4], lk[2], sgo = (w | lane) ? 0x7fffffffu : SY_CT + (CT_U + h) * 128, nd = (lane & 1) ? 3 - URING : 2;
#pragma unroll
  for (int i = 0; i < 4; ++i) { vo[i] = lane * 16 + ux_tile(16 * w + 4 * i); asm volatile("" : "+v"(vo[i])); }
#pragma unroll
  for (int i = 0; i < 2; ++i) lk[i] = SY_CT + ((lane & 1) ? CT_DONE + 8 * i + (h & 7) : CT_V + h) * 128;
  char* vl = lds_base(smem, lane * 16);
  const unsigned vl2 = lane * 16 + w * 2048;
  auto ld_v = [&](int s) {
    asm volatile("buffer_load_dwordx4 %0, %2, %3, %4 offen sc1\n\tbuffer_load_dwordx4 %1, %2, %3, %4 offen offset:1024 sc1" : "=&v"(pc[0]), "=&v"(pc[1]) : "v"(vl2), "s"(ru), "s"(x_slot(2, s, h, sel)) : "memory");
  };
  auto signal = [&](int s) {
    asm volatile("buffer_store_dword %0, %1, %2, 0 offen sc1" :: "v"(s), "v"(sgo), "s"(ru) : "memory");
  };
  auto st_u = [&](int n, unsigned ub) { asm volatile("buffer_store_dwordx4 %0, %1, %2, %3 offen offset:%4 sc1" :: "v"(o[n & 1]), "v"(vo[n >> 2]), "s"(ru), "s"(ub), "n"((n & 3) * 1024) : "memory"); };
  auto fetch = [&](int s) {
    ctr_wait<2>(ctr(a, CT_V + h), s + 1, abortw, lane, dead);
    WST(s == 0, 24);
    ld_v(s);
    asm volatile("s_waitcnt vmcnt(0)" : "+v"(pc[0]), "+v"(pc[1]) :: "memory");
#pragma unroll
    for (int i = 0; i < 2; ++i) *(u32x4*)(vl + 16384 + w * 2048 + i * 1024) = pc[i];
    lds_barrier();
#pragma unroll
    for (int i = 0; i < 8; ++i) V[s][i] = *(const u32x4*)(vl + 16384 + i * 1024);
  };
  fetch(0);
  u64 bad = 0;
  unsigned up = 0x7f000000u;
  c[1][0] = c[1][1] = vzero();
  auto seg = [&](int s, int b) {
    const unsigned ub = ux_base(s, h, sel);
    WST(s < 4, 25 + s);
#pragma unroll
    for (int n = 0; n < 16; ++n)
#pragma unroll
      for (int q = 0; q < 8; ++q) {
        const int kp = q >> 1, m = q & 1;
        if (kp == 0) asm volatile("v_mfma_f32_16x16x32_bf16 %0, %1, %2, 0" : "=&v"(c[n & 1][m]) : "v"(V[b][4 * m + kp]), "a"(W[n][kp]));
        else asm volatile("v_mfma_f32_16x16x32_bf16 %0, %1, %2, %0" : "+v"(c[n & 1][m]) : "v"(V[b][4 * m + kp]), "a"(W[n][kp]));
        SB0;
        if (q >= 2 && q < 6) { const f32x4 x = c[~n & 1][(q - 2) >> 1]; o[~n & 1][q - 2] = pk2v(x[2 * (q & 1)], x[2 * (q & 1) + 1]); }
        if (q == 6) st_u((n - 1) & 15, n ? ub : up);
        if (q == 5) asm volatile("" :: "v"(o[n & 1]));
        if (n == U_LD && q == 0) {
          if (__builtin_expect(bad != 0, 0)) {
            if (s <= U_NOW) {
              asm volatile("s_waitcnt vmcnt(%1)" : "+v"(cw) : "n"(U_LD - 2) : "memory");
              __builtin_amdgcn_s_barrier();
              signal(s - 1);
              bad = (int)__builtin_amdgcn_readfirstlane(cw) <= s;
            }
            if (bad & 1) ctr_wait<2>(ctr(a, CT_V + h), min(s + 2, NSEG), abortw, lane, dead);
            if (bad & 2) {
              ctr_wait<U_SLP>(ctr(a, CT_DONE + 8 * b + (h & 7)), max(s + 3 - URING, 0), abortw, lane, dead);
              for (int i = 0; i < U_STG * (h & 7) + U_ST0; ++i) __builtin_amdgcn_s_sleep(1);
            }
          }
          ld_v(min(s + 1, NSEG - 1));
        }
        if (n == 1 && q == 7) asm volatile("buffer_load_dword %0, %1, %2, 0 offen sc1" : "=&v"(cw) : "v"(s <= U_NOW ? lk[0] : 0x7fffffffu), "s"(ru) : "memory");
        if (n == U_LD && q == 7) asm volatile("buffer_load_dword %0, %1, %2, 0 offen sc1" : "=&v"(cv) : "v"(lk[b ^ 1]), "s"(ru) : "memory");
        if (n == U_SIG2 && q == 0) asm volatile("s_waitcnt vmcnt(%4)" : "+v"(pc[0]), "+v"(pc[1]), "+v"(cv), "+v"(cw) : "n"(U_SIG2 - U_LD - 1) : "memory");
        if (n == U_SIG2 && (q == 1 || q == 7)) *(u32x4*)(vl + (b ^ 1) * 8192 + w * 2048 + (q >> 2) * 1024) = pc[q >> 2];
        if (n == U_SIG2 + 1 && q == 0) { bad = __builtin_amdgcn_ballot_w64((int)cv < (s < U_NOW && lane > 1 ? NSEG : min(s + (int)nd, NSEG - 1))); asm volatile("" : "+s"(bad)); }
        if (n == U_SIG2 + 1 && q == 7) lds_barrier();
        if (n == U_SIG2 + 2 && q == 0) signal(s - 1);
        if ((n == U_SIG2 + 2 || n == U_SIG2 + 3) && (q & 1)) V[b ^ 1][4 * (n - U_SIG2 - 2) + (q >> 1)] = *(const u32x4*)(vl + (b ^ 1) * 8192 + (4 * (n - U_SIG2 - 2) + (q >> 1)) * 1024);
        SB0;
      }
    up = ub;
  };
  for (int s = 0; s < NSEG; s += 2) {
    seg(s, 0);
    if (__builtin_expect(s == 0, 0)) {
      o[1] = pk8v(c[1][0], c[1][1]);
      st_u(15, up);
      bg_drain();
      signal(0);
      fetch(1);
    }
    seg(s + 1, 1);
  }
  o[1] = pk8v(c[1][0], c[1][1]);
  st_u(15, up);
  bg_drain();
  signal(NSEG - 1);
}

__device__ __forceinline__ void task_a1(const ChainArgs& a, int dg, int t0, char* T) {
  const int lane = opaque_tid(0);
  const int d = dg * 64 + 8 * (lane & 7);
  const int tb = t0 + 8 * (lane >> 3);
  u32x4 kk[2][11], vv[2][8], sh[4];
#pragma unroll
  for (int i = 0; i < 4; ++i) sh[i] = *(const u32x4*)(a.Ashift + d * 4 + 8 * i);
  SB0;
#pragma unroll
  for (int gq = 0; gq < 2; ++gq) {
#pragma unroll
    for (int i = 0; i < 11; ++i) {
      const int t = tb + 64 * gq - 3 + i;
      kk[gq][i] = LDNT((const u32x4*)(a.Ak + (long)(t < 0 ? 0 : t) * D_A + d));
    }
#pragma unroll
    for (int i = 0; i < 8; ++i) vv[gq][i] = LDNT((const u32x4*)(a.Av + (long)(tb + 64 * gq + i) * D_A + d));
    SB0;
  }
#pragma unroll
  for (int i = 0; i < 3; ++i) if (tb == 0) kk[0][i] = (u32x4){0u, 0u, 0u, 0u};
#pragma unroll
  for (int gq = 0; gq < 2; ++gq) {
#pragma unroll
    for (int c = 0; c < 8; ++c) {
      const unsigned t01 = sh[c >> 1][2 * (c & 1)], t23 = sh[c >> 1][2 * (c & 1) + 1];
      const float s0 = blo(t01), s1 = bhi(t01), s2 = blo(t23), s3 = bhi(t23);
      float kf[11];
#pragma unroll
      for (int i = 0; i < 11; ++i) kf[i] = (c & 1) ? bhi(kk[gq][i][c >> 1]) : blo(kk[gq][i][c >> 1]);
      u32x4 o;
#pragma unroll
      for (int i = 0; i < 8; i += 2) {
        float r[2];
#pragma unroll
        for (int j = 0; j < 2; ++j)
          r[j] = fmaf(kf[i + j + 3], s0, fmaf(kf[i + j + 2], s1, fmaf(kf[i + j + 1], s2, kf[i + j] * s3))) * ((c & 1) ? bhi(vv[gq][i + j][c >> 1]) : blo(vv[gq][i + j][c >> 1]));
        o[i >> 1] = pk2(r[0], r[1]);
      }
      *(u32x4*)(T + (8 * (lane & 7) + c) * 256 + (((gq * 8 + (lane >> 3)) ^ (lane & 7)) * 16)) = o;
    }
    SB0;
  }
  const __amdgpu_buffer_rsrc_t rkv = mk_rsrc((void*)a.kvT, 4u << 20);
#pragma unroll
  for (int i = 0; i < 16; ++i) {
    const int row = 4 * i + (lane >> 4);
    const u32x4 o = *(const u32x4*)(T + row * 256 + (((lane & 15) ^ (row >> 3)) * 16));
    st16(rkv, (unsigned)(((dg * 64 + row) * S_LEN + t0 + (lane & 15) * 8) * 2), o);
  }
  wait_vm<0>();
  if (lane == 0) ctr_add(a, CT_A1 + (dg >> 2), 1u);
}

__device__ __forceinline__ void task_a3(const ChainArgs& a, int dg, int t0, char* T) {
  const int lane = opaque_tid(0);
  const int d = dg * 64 + 8 * (lane & 7);
  const int tb = t0 + 8 * (lane >> 3);
  const __amdgpu_buffer_rsrc_t rz = mk_rsrc((void*)a.kvT, 4u << 20);
  u32x4 qq[16], zz[8][2];
#pragma unroll
  for (int i = 0; i < 16; ++i) qq[i] = LDNT((const u32x4*)(a.Aq + (long)(tb + 64 * (i >> 3) + (i & 7)) * D_A + d));
  {
    u32x4 zr[16];
    bool dead = false;
    ctr_wait<A3_SLP>(ctr(a, CT_A2 + dg), 64u, (GAS unsigned*)(a.sync + SY_ABORT), lane, dead);
  #pragma unroll
    for (int i = 0; i < 16; ++i) zr[i] = ld16(rz, (unsigned)(((dg * 64 + 4 * i + (lane >> 4)) * S_LEN + t0 + (lane & 15) * 8) * 2));
#pragma unroll
    for (int i = 0; i < 16; ++i) {
      const int row = 4 * i + (lane >> 4);
      *(u32x4*)(T + row * 256 + (((lane & 15) ^ (row >> 3)) * 16)) = zr[i];
    }
  }
#pragma unroll
  for (int c = 0; c < 8; ++c)
#pragma unroll
    for (int gq = 0; gq < 2; ++gq) zz[c][gq] = *(const u32x4*)(T + (8 * (lane & 7) + c) * 256 + (((gq * 8 + (lane >> 3)) ^ (lane & 7)) * 16));
#pragma unroll
  for (int i = 0; i < 16; ++i) {
    u32x4 o;
#pragma unroll
    for (int c = 0; c < 8; c += 2) {
      const unsigned z0 = zz[c][i >> 3][(i >> 1) & 3], z1 = zz[c + 1][i >> 3][(i >> 1) & 3];
      o[c >> 1] = pk2(blo(qq[i][c >> 1]) * ((i & 1) ? bhi(z0) : blo(z0)), bhi(qq[i][c >> 1]) * ((i & 1) ? bhi(z1) : blo(z1)));
    }
    st16g(a.out + (long)(tb + 64 * (i >> 3) + (i & 7)) * OUTW + d, o);
  }
}

__device__ __forceinline__ void part_a(const ChainArgs& a, char* smem, int x, int wi, int w) {
  char* T = smem + 1024 + w * A2_WLDS;
  if (wi < 64) task_a1(a, 4 * x + (wi & 3), (wi >> 2) * 128, T);
  a2_wave(a, 256 * x + wi, 256 * x + 256, smem, w);
  while (true) {
    unsigned t = 0;
    if (opaque_tid(0) == 0) t = __hip_atomic_fetch_add(ctr(a, CT_TK + x), 1u, __ATOMIC_RELAXED, __HIP_MEMORY_SCOPE_AGENT) + 1u;
    t = __builtin_amdgcn_readfirstlane(t);
    if (t >= 64) break;
    task_a3(a, 4 * x + (t >> 4), (t & 15) * 128, T);
  }
}

__global__ __launch_bounds__(256, 1) void chain_kernel(ChainArgs a) {
  extern __shared__ __attribute__((aligned(16))) char smem[];
  const int b = blockIdx.x;
  {
    u32x4 rc;
    asm volatile("s_getpc_b64 s[60:61]\n\t.Lchain_here:\n\ts_sub_u32 s60, s60, .Lchain_here-_Z12chain_kernel9ChainArgs\n\ts_subb_u32 s61, s61, 0\n\ts_and_b32 s61, s61, 0xffff\n\t"
                 "s_mov_b32 s62, .Lchain_end-_Z12chain_kernel9ChainArgs\n\ts_mov_b32 s63, 0x20000\n\t"
                 "buffer_load_dwordx4 a[252:255], %1, s[60:63], 0 offen\n\tbuffer_load_dwordx4 a[252:255], %1, s[60:63], s63 offen"
                 : "={s[60:63]}"(rc) : "v"((b >> 3) * 4096 + threadIdx.x * 16) : "scc", "a252", "a253", "a254", "a255");
  }
  int w = __builtin_amdgcn_readfirstlane(threadIdx.x >> 6);
  Gate G;
  G.entry = __builtin_amdgcn_s_memrealtime(); G.open = false; G.arr = w == 0 ? b & 7 : -1;
  G.rot = ((unsigned)__builtin_amdgcn_s_getreg(20 | (3 << 11)) - b + WRONG) & 7u;
  {
    volatile LAS unsigned* t = (volatile LAS unsigned*)smem;
    if (w == 0) { t[0] = (unsigned)G.entry; t[1] = (unsigned)(G.entry >> 32); }
    __syncthreads();
    G.entry = (u64)(unsigned)__builtin_amdgcn_readfirstlane(t[0]) | ((u64)(unsigned)__builtin_amdgcn_readfirstlane(t[1]) << 32);
    __syncthreads();
  }
  if (((b + G.rot) & 4) == 0 && b < 128) {
    const int c = (b >> 6) * 32 + 8 * ((b + G.rot) & 3) + ((b >> 3) & 7);
    if (w < 2) calibrate(a, w ? SY_HOT / PAGE : c, 2 * c + w, G.entry);
    __builtin_amdgcn_s_barrier();
  }
  if (b == 0) {
    const int lane = opaque_tid(0);
    const __amdgpu_buffer_rsrc_t rs = mk_rsrc(a.sync + SY_ABORT, SY_CT - SY_ABORT + CT_CAL * 128);
    st16(rs, w * 8192 + lane * 128, (u32x4){SENT, SENT, SENT, SENT});
    static_assert(SY_CT - SY_ABORT + CT_CAL * 128 <= 32768, "one store per wave");
    if (w == 3) {
      u32x4 v;
      bool ok;
      do {
        v = ld16(mk_rsrc(a.sync + SY_CT + CT_CAL * 128, 8192), lane * 128);
        ok = ((u64)v[1] << 32 | v[0]) - G.entry + 2000u < (1ull << 31) && ((u64)v[3] << 32 | v[2]) - G.entry + 2000u < (1ull << 31);
      } while (!__all(ok) && __builtin_amdgcn_s_memrealtime() - G.entry < 2 * CALM);
      if (lane == SY_HOT / PAGE) v[0] = 0;
      const u64 m = __builtin_amdgcn_ballot_w64(ok && (v[0] & 1u)) | (u64)(__builtin_popcountll(__builtin_amdgcn_ballot_w64(ok && (v[2] & 1u))) > 32) << (SY_HOT / PAGE);
      if (lane == 0) __hip_atomic_store((GAS u64*)(a.sync + SY_SEL), m, __ATOMIC_RELAXED, __HIP_MEMORY_SCOPE_AGENT);
    }
    bg_drain();
    if (w == 0 && lane == 0) __hip_atomic_store((GAS u64*)(a.sync + SY_EPOCH), (__builtin_amdgcn_s_memrealtime() & ~7ull) | G.rot, __ATOMIC_RELAXED, __HIP_MEMORY_SCOPE_AGENT);
  }
  while (true) {
    const unsigned rot = G.rot;
    asm volatile("" : "+s"(w));
    const int x = (b + rot) & 7, j = b >> 3, h = 8 * (x & 3) + (j & 7), role = (x & 4) + (j >> 3);
    if ((role == 0 || role == 7) && w >= 2) {
      const int n = opaque_tid(w - 2);
      const unsigned x0 = __builtin_amdgcn_raw_buffer_load_b32(mk_rsrc(a.sync, 134u << 20), n * PAGE + 4096 + (j & 31) * 128, 0, 2);
      const unsigned x1 = __builtin_amdgcn_raw_buffer_load_b32(mk_rsrc(a.out, 8u << 20), n * PAGE + (j & 31) * 128, 0, 2);
      asm volatile("" :: "v"(x0), "v"(x1));
    }
    if (role < 2) role_cb(a, 32 * role + h, smem, w, G);
    else if (role == 2) role_qk(a, h, smem, w, G);
    else if (role == 3) role_vo(a, h, smem, w, G);
    else if (role == 4) task_u(a, h, smem, w, G);
    else task_w(a, 7 - role, h, smem, w, G);
    if (G.rot == rot && role > 4) part_a(a, smem, x & 3, 4 * (j - 8) + w, w);
    if (G.rot == rot) break;
    __syncthreads();
  }
  asm volatile(".Lchain_end:");
}

extern "C" void kernel_launch(void* const* d_in, const int* in_sizes, int n_in,
                              void* d_out, int out_size, void* d_ws, size_t ws_size,
                              hipStream_t stream) {
  const u16* A_q = (const u16*)d_in[0];
  const u16* A_k = (const u16*)d_in[1];
  const u16* A_v = (const u16*)d_in[2];
  const u16* A_shift = (const u16*)d_in[3];
  const u16* A_ssmk = (const u16*)d_in[4];
  const u16* A_D = (const u16*)d_in[5];
  const u16* B_x = (const u16*)d_in[6];
  const u16* B_mem0 = (const u16*)d_in[7];
  const u16* B_Wq = (const u16*)d_in[8];
  const u16* B_Wk = (const u16*)d_in[9];
  const u16* B_Wv = (const u16*)d_in[10];
  const u16* B_Wo = (const u16*)d_in[11];
  u16* out = (u16*)d_out;
  char* ws = (char*)(((uintptr_t)d_ws + (2u << 20) - 1) & ~(uintptr_t)((2u << 20) - 1)) + WSOFF * MB;
  u16* kvT = (u16*)(ws + OFF_KVT);
  hipFuncSetAttribute((const void*)chain_kernel, hipFuncAttributeMaxDynamicSharedMemorySize, CHAIN_LDS);
  ChainArgs ca;
  ca.prof = (u64*)((char*)d_ws + PROF_AT);
  ca.mem0 = B_mem0; ca.Wq = B_Wq; ca.Wk = B_Wk; ca.Wv = B_Wv; ca.Wo = B_Wo; ca.X = B_x;
  ca.out = out; ca.sync = ws;
  ca.Aq = A_q; ca.Ak = A_k; ca.Av = A_v; ca.Ashift = A_shift; ca.Assm = A_ssmk; ca.AD = A_D; ca.kvT = kvT;
  chain_kernel<<<256, 256, CHAIN_LDS, stream>>>(ca);
}
```

```cpp
#include <hip/hip_runtime.h>
#include <hip/hip_bf16.h>
#include <stdint.h>
#include <type_traits>

constexpr int S_LEN = 2048;
constexpr int D_A   = 1024;
constexpr int DM    = 1024;
constexpr int NH    = 32;
constexpr int DH    = 128;
constexpr int HD    = NH * DH;
constexpr int MEMR  = 8;
constexpr int SEG   = 32;
constexpr int NSEG  = S_LEN / SEG;
constexpr int OUTW  = 2048;

typedef unsigned short u16;
typedef unsigned long long u64;
typedef short bf16x8 __attribute__((ext_vector_type(8)));
typedef short bf16x4 __attribute__((ext_vector_type(4)));
typedef float f32x4 __attribute__((ext_vector_type(4)));
typedef float f32x16 __attribute__((ext_vector_type(16)));
typedef unsigned u32x4 __attribute__((ext_vector_type(4)));
typedef unsigned u32x2 __attribute__((ext_vector_type(2)));
#define LAS __attribute__((address_space(3)))
#define GAS __attribute__((address_space(1)))

constexpr size_t MB = 1u << 20;
constexpr size_t PROF_AT = 0;

__device__ __forceinline__ float bf2f(u16 b) { return __uint_as_float(((unsigned)b) << 16); }
__device__ __forceinline__ u16 f2bf(float f) { __bf16 b = (__bf16)f; return __builtin_bit_cast(u16, b); }
__device__ __forceinline__ float rbf(float f) { return bf2f(f2bf(f)); }
__device__ __forceinline__ unsigned pk2(float a, float b) {
  typedef float f2 __attribute__((ext_vector_type(2)));
  typedef __bf16 b2 __attribute__((ext_vector_type(2)));
  f2 v = {a, b}; b2 r = __builtin_convertvector(v, b2); return __builtin_bit_cast(unsigned, r);
}

__device__ __forceinline__ int opaque_tid(int wv) {
  int l;
  asm volatile("v_mbcnt_lo_u32_b32 %0, -1, 0\n\tv_mbcnt_hi_u32_b32 %0, -1, %0" : "=v"(l));
  return wv * 64 + l;
}
__device__ __forceinline__ int swz_b(int row) { return ((row & 3) << 2) | ((row >> 2) & 3); }

#define KNOB(name, v) constexpr int name = v
KNOB(K_DEF, 70);
KNOB(W_STEP, 33); KNOB(W_OFF, 56);
#define RHOV 3
KNOB(RHO, RHOV);
#define CDUR 1600
#define U_SLP 8
#define U_STG 0
#define U_ST0 0
#define WSOFF 122
#define START_SLP 8
#define QK_D 0
#define U_DRY 0
#define CALN 3
#define CALM 600u
#define CALT 700u
#define KQ_D 500
#define W_NOW 4
#define U_SIG2 11
#define U_LD 5
#define U_NOW 6
#define WRONG 0
#define OUTP "nt"
#define LDNT(p) __builtin_nontemporal_load(p)
constexpr int CHK      = 16384;
constexpr int PAGE     = 2 << 20;
constexpr int SY_UM    = 34 * PAGE;
constexpr int SY_HOT   = 32 * PAGE;
constexpr int H_MEM    = 0;
constexpr int H_P      = 16 * CHK;
constexpr int H_ANN    = 28 * CHK;
constexpr int H_DLT    = 32 * CHK;
constexpr int H_TR     = 33 * CHK;
constexpr int H_PX     = 64 * CHK;
constexpr int H_BYTES  = 128 * CHK;
constexpr int URING    = 16;
constexpr int SY_SET   = 52 * PAGE;
constexpr int SY_EPOCH = SY_SET + 128;
constexpr int SY_ABORT = SY_SET + 256;
constexpr int SY_SEL   = SY_EPOCH + 8;
constexpr int SY_CT    = SY_SET + CHK;
constexpr int CT_Q = 0, CT_K = 32, CT_U = 128;
constexpr int CT_V = 0, CT_DONE = 32, CT_A2 = 48, CT_A1 = 128, CT_TK = 132;
constexpr int CT_ARR = -124, CT_INIT = -116, CT_CAL = 256;
constexpr int SY_KV    = 48 * PAGE;
constexpr int SY_BYTES = SY_CT + (CT_CAL + 64) * 128;
constexpr unsigned SENT = 0xFFFFFFFFu;
constexpr unsigned SPIN_CAP = 1u << 19;
constexpr int NLF = 16;
constexpr int L_WTS = 0;
constexpr int L_MEM = L_WTS + 4 * NLF * 2048;
constexpr int L_XCH = L_MEM + 16384;
constexpr int L_XK  = L_XCH + 2048;
constexpr int L_KX  = L_XCH + 4096;
constexpr int L_PRF = L_KX + 8192;
constexpr int XRS = 40;
constexpr int CHAIN_LDS = 160 * 1024;
static_assert(L_XCH + 128 * XRS <= L_PRF && L_PRF + 4096 <= CHAIN_LDS, "lds");

typedef __bf16 bfv8 __attribute__((ext_vector_type(8)));
typedef __bf16 bfv16 __attribute__((ext_vector_type(16)));
typedef unsigned u32x8 __attribute__((ext_vector_type(8)));
typedef unsigned u32x12 __attribute__((ext_vector_type(12)));
typedef unsigned u32x16 __attribute__((ext_vector_type(16)));
typedef unsigned u32x32 __attribute__((ext_vector_type(32)));
struct Wts { u32x8 a[32]; u32x32 v[4]; };

#define STAMPL(i) do {} while (0)
#define PVAL(i, v) do {} while (0)
#define PDECL(base, str) do {} while (0)
#define PZERO do {} while (0)
#define PDUMP(mask) do {} while (0)
#define TA(x) do {} while (0)
#define TB(x) TS(x)
#define TS(x) do {} while (0)
#define SSTAMP(i) do {} while (0)
#define WST(c, i) do {} while (0)
#define AST(i) do {} while (0)
struct ChainArgs {
  u64* prof;
  const u16* mem0; const u16* Wq; const u16* Wk; const u16* Wv; const u16* Wo; const u16* X;
  u16* out; char* sync;
  const u16* Aq; const u16* Ak; const u16* Av; const u16* Ashift; const u16* Assm; const u16* AD;
};
__device__ __forceinline__ GAS unsigned* ctr(const ChainArgs& a, int i) { return (GAS unsigned*)(a.sync + SY_CT + i * 128); }
__device__ __forceinline__ void ctr_add(const ChainArgs& a, int i, unsigned v) {
  __hip_atomic_fetch_add(ctr(a, i), v, __ATOMIC_RELAXED, __HIP_MEMORY_SCOPE_AGENT);
}

__device__ __forceinline__ __amdgpu_buffer_rsrc_t mk_rsrc(const void* p, unsigned bytes) {
  const u64 v = (u64)p;
  const unsigned lo = (unsigned)__builtin_amdgcn_readfirstlane((unsigned)v), hi = (unsigned)__builtin_amdgcn_readfirstlane((unsigned)(v >> 32));
  const u64 u = (u64)lo | ((u64)hi << 32);
  return __builtin_amdgcn_make_buffer_rsrc((void*)u, 0, bytes, 0x00020000);
}
__device__ __forceinline__ u32x4 own_rsrc(const void* p, unsigned bytes) {
  u32x4 r = {(unsigned)__builtin_amdgcn_readfirstlane((unsigned)(u64)p), (unsigned)__builtin_amdgcn_readfirstlane((unsigned)((u64)p >> 32)) & 0xffffu, bytes, 0x00020000u};
  asm volatile("s_nop 4" : "+s"(r));
  return r;
}
constexpr int LD_AUX = 16, ST_AUX = 16;
__device__ __forceinline__ u32x4 ld16(__amdgpu_buffer_rsrc_t rs, unsigned off) {
  return __builtin_amdgcn_raw_buffer_load_b128(rs, off, 0, LD_AUX);
}
__device__ __forceinline__ void st16(__amdgpu_buffer_rsrc_t rs, unsigned off, u32x4 v) {
  __builtin_amdgcn_raw_buffer_store_b128(v, rs, off, 0, ST_AUX);
}
__device__ __forceinline__ void st16s(u32x4 v, __amdgpu_buffer_rsrc_t rs, unsigned voff, unsigned soff) {
  __builtin_amdgcn_raw_buffer_store_b128(v, rs, voff, soff, ST_AUX);
  asm volatile("s_nop 1" :: "v"(v) : "memory");
}
__device__ __forceinline__ void st8(__amdgpu_buffer_rsrc_t rs, unsigned off, u32x2 v) {
  __builtin_amdgcn_raw_buffer_store_b64(v, rs, off, 0, ST_AUX);
}
__device__ __forceinline__ void st4(__amdgpu_buffer_rsrc_t rs, unsigned off, unsigned v) {
  __builtin_amdgcn_raw_buffer_store_b32(v, rs, off, 0, ST_AUX);
}
__device__ __forceinline__ unsigned ld4(__amdgpu_buffer_rsrc_t rs, unsigned off) {
  return __builtin_amdgcn_raw_buffer_load_b32(rs, off, 0, LD_AUX);
}
constexpr int TU = 16;
__device__ __forceinline__ unsigned cyc32() { return (unsigned)__builtin_amdgcn_s_memtime(); }
__device__ __forceinline__ unsigned to_sgpr(unsigned x) {
  unsigned r;
  asm volatile("s_nop 0\n\tv_readfirstlane_b32 %0, %1\n\ts_nop 4" : "=s"(r) : "v"(x));
  return r;
}
struct Clk {
  unsigned ce, te;
  unsigned c2t, t2c;
  __device__ __forceinline__ void edge(unsigned& c, unsigned& t) {
    u64 c0, r0, r1, r2, r3, r4, r5;
    asm volatile("s_memtime %0\n\ts_memrealtime %1\n\ts_nop 4\n\ts_memrealtime %2\n\ts_nop 4\n\ts_memrealtime %3\n\ts_nop 4\n\t"
                 "s_memrealtime %4\n\ts_nop 4\n\ts_memrealtime %5\n\ts_nop 4\n\ts_memrealtime %6\n\ts_waitcnt lgkmcnt(0)"
                 : "=&s"(c0), "=&s"(r0), "=&s"(r1), "=&s"(r2), "=&s"(r3), "=&s"(r4), "=&s"(r5) :: "memory");
    const unsigned x = (unsigned)r0;
    const unsigned n = ((unsigned)r1 == x) + ((unsigned)r2 == x) + ((unsigned)r3 == x) + ((unsigned)r4 == x) + ((unsigned)r5 == x);
    c = to_sgpr((unsigned)c0 + 24u * n); t = (x + 4u) * TU;
  }
  __device__ __forceinline__ void init() { edge(ce, te); c2t = 44000u; t2c = 97600u; }
  __device__ __forceinline__ void sync() {
    unsigned c, t;
    edge(c, t);
    set(c, t);
  }
  __device__ __forceinline__ void set(unsigned c, unsigned t) {
    if (c == ce) return;
    const float q = (float)(t - te) * __builtin_amdgcn_rcpf((float)(c - ce));
    const float qc = fminf(fmaxf(q, 0.51f), 1.99f);
    c2t = to_sgpr((unsigned)(qc * 65536.f));
    t2c = to_sgpr((unsigned)(__builtin_amdgcn_rcpf(qc) * 65536.f));
    ce = c; te = t;
  }
  __device__ __forceinline__ unsigned time(unsigned c) const { return te + ((min(c - ce, 30000u) * c2t) >> 16); }
  __device__ __forceinline__ unsigned now() const { return time(cyc32()); }
  __device__ __forceinline__ unsigned target(unsigned t) const {
    return to_sgpr(ce + (((unsigned)min(max((int)(t - te), 0), 20000) * t2c) >> 16));
  }
  __device__ __forceinline__ void wait(unsigned t) const { wait_c(target(t)); }
  __device__ __forceinline__ void wait_c(unsigned c) const {
    unsigned y;
    asm volatile("1:\n\ts_memtime vcc\n\ts_waitcnt lgkmcnt(0)\n\ts_sub_i32 %0, %1, vcc_lo\n\ts_cmp_gt_i32 %0, 128\n\ts_cbranch_scc1 1b\n\t"
                 "2:\n\ts_sub_i32 %0, %0, %2\n\ts_cmp_gt_i32 %0, %3\n\ts_cbranch_scc1 2b"
                 : "=&s"(y) : "s"(c), "n"(W_STEP), "n"(W_OFF) : "memory", "scc", "vcc");
  }
};
__device__ __forceinline__ unsigned nosent(unsigned d) { return min(d, SENT - 1u); }
__device__ __forceinline__ int arow(int row, int half) { return 4 * (row >> 1) + 2 * half + (row & 1); }
__device__ __forceinline__ unsigned umax4(u32x4 v) { return max(max(v[0], v[1]), max(v[2], v[3])); }
__device__ __forceinline__ void stamp_again(GAS u64* p) {
  __hip_atomic_store(p, (__builtin_amdgcn_s_memrealtime() & ~7ull) | (__hip_atomic_load(p, __ATOMIC_RELAXED, __HIP_MEMORY_SCOPE_AGENT) & 7ull),
                     __ATOMIC_RELAXED, __HIP_MEMORY_SCOPE_AGENT);
}
__device__ __forceinline__ bool poll_fail(unsigned& spins, GAS unsigned* abortw, int lane) {
  ++spins;
  if (spins > SPIN_CAP) {
    if (lane == 0) __hip_atomic_store(abortw, 1u, __ATOMIC_RELAXED, __HIP_MEMORY_SCOPE_AGENT);
    return true;
  }
  if ((spins & 511u) == 0u && lane == 0) {
    unsigned n = 0;
    for (int i = 0; i < 8; ++i) n += __hip_atomic_load(abortw + (SY_CT - SY_ABORT + (CT_ARR + i) * 128) / 4, __ATOMIC_RELAXED, __HIP_MEMORY_SCOPE_AGENT) + 1u;
    if (n < 256u) stamp_again((GAS u64*)(abortw - 32));
  }
  if ((spins & 63u) == 0u)
    return __builtin_amdgcn_readfirstlane(__hip_atomic_load(abortw, __ATOMIC_RELAXED, __HIP_MEMORY_SCOPE_AGENT)) != SENT;
  return false;
}
struct Gate {
  u64 entry, sel; int arr; unsigned rot; bool open;
  __device__ __forceinline__ bool look(char* sync, u32x4 v) {
    if (open) return true;
    for (unsigned spins = 1;; ++spins) {
#pragma unroll
      for (int i = 0; i < 4; ++i) v[i] = __builtin_amdgcn_readfirstlane(v[i]);
      if (((u64)v[1] << 32 | v[0]) - entry + 2000u < (1ull << 31) || spins > 4 * SPIN_CAP) break;
      if (spins > 1) __builtin_amdgcn_s_sleep(8);
      v = ld16(mk_rsrc(sync + SY_EPOCH, 16), 0);
    }
    asm volatile("" ::: "memory");
    sel = (u64)v[3] << 32 | v[2];
    const bool same = rot == (v[0] & 7u);
    rot = v[0] & 7u; open = true;
    return same;
  }
  __device__ __forceinline__ void ask(char* sync, u32x4& v) { asm volatile("s_nop 4\n\tbuffer_load_dwordx4 %0, off, %1, 0 sc1" : "=v"(v) : "s"(own_rsrc(sync + SY_EPOCH, 16)) : "memory"); }
  __device__ __forceinline__ void arrive(char* sync) {
    if (arr >= 0 && opaque_tid(0) == 0) __hip_atomic_fetch_add((GAS unsigned*)(sync + SY_CT + (CT_ARR + arr) * 128), 1u, __ATOMIC_RELAXED, __HIP_MEMORY_SCOPE_AGENT);
    arr = -1;
  }
  __device__ __forceinline__ bool pass(char* sync, u32x4 v = {0u, 0u, 0u, 0u}) { const bool r = look(sync, v); arrive(sync); return r; }
};
__device__ __forceinline__ unsigned ann_val(unsigned t, int s) { return (t << 8) | (unsigned)s; }
__device__ __forceinline__ unsigned tr_place(int s) { return (s & 2) * (CHK / 2) + (s & 1) * 4096; }
__device__ __forceinline__ unsigned die_half() { return ((unsigned)__builtin_amdgcn_s_getreg(20 | (3 << 11)) >> 2) & 1u; }
typedef u64 Sel;
template <class F>
__device__ __forceinline__ bool start_wait(char* sync, GAS unsigned* abortw, int lane, bool& dead, int ci, F f) {
  unsigned spins = 0, v;
  const bool m = lane < 4 || ci < 0;
  while (true) {
    v = ld4(mk_rsrc(sync + SY_SET, SY_BYTES - SY_SET), lane < 6 ? CHK + (m ? CT_INIT + (lane & 3) : ci + 32 * (lane & 1)) * 128 : 0x7fffffffu) + 1u;
    asm volatile("" ::: "memory");
    f();
    if (__all(lane > 5 || v >= (m ? 16u : 1u))) break;
    __builtin_amdgcn_s_sleep(START_SLP);
    if (dead || poll_fail(spins, abortw, lane)) { dead = true; break; }
  }
  return __all(lane > 5 || v >= 2u);
}
__device__ __forceinline__ __amdgpu_buffer_rsrc_t rsrc_u(char* sync) { return mk_rsrc((void*)sync, SY_SET); }
__device__ __forceinline__ unsigned near_half(unsigned off, u64 sel) { return off + ((unsigned)(sel >> ((off >> 21) & 63)) & 1u) * 8192; }
__device__ __forceinline__ unsigned x_slot(int kind, int s, int h, Sel sel) {
  return kind == 2 ? near_half(32 * PAGE + (s * 32 + h) * CHK, ~sel) : near_half((s * 64 + 2 * h + 1 - kind) * CHK, sel);
}
__device__ __forceinline__ int ct_near(Sel sel) { return 64 * ((unsigned)(sel >> (SY_SET / PAGE)) & 1u); }
__device__ __forceinline__ unsigned kv_row(int d, Sel sel) { return near_half(SY_KV + (d >> 1) * CHK, ~sel) + (d & 1) * 4096; }
__device__ __forceinline__ int hpos(int h) { return (h & 3) * 8 + (h >> 2); }
constexpr unsigned UXH = 8 * CHK;
__device__ __forceinline__ unsigned ux_base(int s, int h, Sel sel) { return near_half(((s & (URING - 1)) * 32 + h) * UXH, ~sel); }
__device__ __forceinline__ constexpr unsigned ux_tile(int t) { return (t >> 3) * CHK + (t & 7) * 1024; }
__device__ __forceinline__ __amdgpu_buffer_rsrc_t rsrc_hot(char* sync, Sel sel) {
  return mk_rsrc((void*)(sync + near_half(SY_HOT, sel)), H_BYTES - 8192);
}
template <int SLP = 4>
__device__ __forceinline__ void ctr_wait(GAS unsigned* c, unsigned need, GAS unsigned* abortw, int lane, bool& dead) {
  unsigned spins = 0;
  while (true) {
    unsigned v = __builtin_amdgcn_readfirstlane(__hip_atomic_load(c, __ATOMIC_RELAXED, __HIP_MEMORY_SCOPE_AGENT)) + 1u;
    if (v >= need) break;
    __builtin_amdgcn_s_sleep(SLP);
    if (dead || poll_fail(spins, abortw, lane)) { dead = true; break; }
  }
}
template <int N> __device__ __forceinline__ void wait_vm() { asm volatile("s_waitcnt vmcnt(%0)" :: "n"(N) : "memory"); }
__device__ __forceinline__ void lds_barrier() {
  asm volatile("s_waitcnt lgkmcnt(0)" ::: "memory");
  __builtin_amdgcn_s_barrier();
  asm volatile("" ::: "memory");
}
__device__ __forceinline__ f32x4 vzero() { float z; asm volatile("v_mov_b32 %0, 0" : "=v"(z)); f32x4 r = {z, z, z, z}; asm volatile("" : "+v"(r)); return r; }
__device__ __forceinline__ f32x4 smf(u32x4 a, u32x8 b, f32x4 c, int idx) {
  return __builtin_amdgcn_smfmac_f32_16x16x64_bf16(__builtin_bit_cast(bfv8, a), __builtin_bit_cast(bfv16, b), c, idx, 0, 0);
}
__device__ __forceinline__ float red16max(float x) { auto r = __builtin_amdgcn_permlane16_swap(__float_as_uint(x), __float_as_uint(x), false, false); return fmaxf(__uint_as_float(r[0]), __uint_as_float(r[1])); }
__device__ __forceinline__ float red32max(float x) { auto r = __builtin_amdgcn_permlane32_swap(__float_as_uint(x), __float_as_uint(x), false, false); return fmaxf(__uint_as_float(r[0]), __uint_as_float(r[1])); }
__device__ __forceinline__ float red16add(float x) { auto r = __builtin_amdgcn_permlane16_swap(__float_as_uint(x), __float_as_uint(x), false, false); return __uint_as_float(r[0]) + __uint_as_float(r[1]); }
__device__ __forceinline__ float red32add(float x) { auto r = __builtin_amdgcn_permlane32_swap(__float_as_uint(x), __float_as_uint(x), false, false); return __uint_as_float(r[0]) + __uint_as_float(r[1]); }
constexpr bool blk_in_lds(int R, int b) {
  return R == 0 ? ((b & 7) == 1 || (b & 7) == 4) : (b < 16 ? (b & 3) == 0 : (b & 3) == 3);
}
constexpr int lds_index(int R, int b) { int n = 0; for (int i = 0; i < b; ++i) if (blk_in_lds(R, i)) ++n; return n; }
constexpr bool frag_in_lds(int R, int f) { return blk_in_lds(R, f >> 1); }
constexpr int frag_reg_slot(int R, int f) { int n = 0; for (int i = 0; i < f; ++i) if (!frag_in_lds(R, i)) ++n; return n; }

constexpr int m_block(int R, int m, int i) {
  return R == 0 ? 2 * (i < 12 ? (m ? i + i / 3 + 1 : i + (i + 1) / 3) : 4 * (i - 12) + 2 - 2 * m) + m : m ? 16 + 4 * (i & 3) + (i >> 2) : (i < 12 ? i + i / 3 + 1 : 4 * (i - 12));
}
#define VO_ORD 1
constexpr int w_block(int R, int i) { return R && VO_ORD ? m_block(R, i < 12, i % 12) : m_block(R, i & 1, i >> 1); }
#define W_BG 0xfedcba9876543210ull
#define W_NT 0
#define U_ORD 0x3210
constexpr u64 bg_order(int m) { u64 r = 0; for (int n = 0; n < 16; ++n) r |= (u64)(m_block(0, m, (W_BG >> (4 * n)) & 15) >> 1) << (4 * n); return r; }
struct WSched { int ord[32]; int slot[32]; int issue[32]; int out[32]; int reg[32]; };
constexpr WSched make_wsched(int R) {
  WSched S{};
  int n = 0;
  for (int i = 0; i < 24; ++i) S.ord[n++] = w_block(R, i);
  for (int i = 0; i < n; ++i) S.reg[i] = frag_reg_slot(R, 2 * S.ord[i]);
  const int npure = n;
  for (int b = 0; b < 32; ++b) if (blk_in_lds(R, b)) S.ord[n++] = b;
  int seq[32] = {};
  int nissued = 0, next = 0;
  for (int i = 0; i < 10; ++i) { S.slot[i] = (i + 10 - npure % 10) % 10; S.issue[i] = -1; seq[i] = nissued++; }
  next = 10;
  S.out[0] = nissued - 1;
  for (int c = 0; c <= npure; ++c) {
    if (c >= 1) {
      const int k = S.slot[c - 1];
      if (next < npure) { S.slot[next] = k; S.issue[next] = c; seq[next] = nissued++; ++next; }
      else if (k < NLF / 2) { S.slot[npure + k] = k; S.issue[npure + k] = c; seq[npure + k] = nissued++; }
    }
    if (c + 1 < npure) S.out[c + 1] = nissued - 1 - seq[c + 1];
  }
  return S;
}
__device__ __forceinline__ int kcol(int k) { return 8 * ((k >> 1) & 1) + ((k & 1) ? 7 - (k >> 2) : k >> 2); }
template <bool PERM = false>
__device__ __forceinline__ u32x8 tr_frag(const char* sb, int tile, int lane) {
  u32x8 f;
#pragma unroll
  for (int q = 0; q < 4; ++q) {
    int row = (q >> 1) * 32 + 8 * (lane >> 4) + 4 * (q & 1) + ((lane & 15) >> 2);
    if (PERM) row = (row & ~15) + kcol(row & 15);
    int p = lane & 3;
    int lc = 2 * tile + (p >> 1);
    int addr = row * 256 + ((lc ^ swz_b(row)) * 16) + 8 * (p & 1);
    bf16x4 v = __builtin_amdgcn_ds_read_tr16_b64_v4i16((LAS bf16x4*)(sb + addr));
    u32x2 u = __builtin_bit_cast(u32x2, v);
    f[2 * q] = u[0]; f[2 * q + 1] = u[1];
  }
  return f;
}
__device__ __forceinline__ void dma16(__amdgpu_buffer_rsrc_t rs, unsigned voff, unsigned soff, unsigned lds, bool nt = false, bool pad = true) {
  if (nt) asm volatile("s_mov_b32 m0, %0\n\ts_nop 3\n\tbuffer_load_dwordx4 %1, %2, %3 offen nt lds" :: "s"(lds), "v"(voff), "s"(rs), "s"(soff) : "m0", "memory");
  else if (pad) asm volatile("s_mov_b32 m0, %0\n\ts_nop 3\n\tbuffer_load_dwordx4 %1, %2, %3 offen lds" :: "s"(lds), "v"(voff), "s"(rs), "s"(soff) : "m0", "memory");
  else asm volatile("s_mov_b32 m0, %0\n\ts_nop 0\n\tbuffer_load_dwordx4 %1, %2, %3 offen lds" :: "s"(lds), "v"(voff), "s"(rs), "s"(soff) : "m0", "memory");
}
__device__ __forceinline__ u32x8 tr_frag2(const char* sb, int tile, int f, int lane) {
  u32x8 r;
#pragma unroll
  for (int c = 0; c < 2; ++c)
#pragma unroll
    for (int t = 0; t < 2; ++t) {
      const int row = 32 * c + 16 * f + 4 * (lane >> 4) + ((lane & 15) >> 2);
      const int p = lane & 3, lc = 2 * (tile + t) + (p >> 1);
      const u32x2 u = __builtin_bit_cast(u32x2, __builtin_amdgcn_ds_read_tr16_b64_v4i16((LAS bf16x4*)(sb + row * 256 + ((lc ^ swz_b(row)) * 16) + 8 * (p & 1))));
      r[4 * c + t] = u[0]; r[4 * c + 2 + t] = u[1];
    }
  return r;
}
template <int ROLE>
__device__ __forceinline__ void load_weights(const ChainArgs& a, int h, int w, int lane, char* smem, Wts& W, Gate& G, u32x4& gv) {
#pragma unroll
  for (int i = 0; i < 4; ++i) asm volatile("" : "=v"(W.v[i]));
#pragma unroll
  for (int i = 0; i < 32; ++i) asm volatile("" : "=a"(W.a[i]));
  constexpr WSched S = make_wsched(ROLE);
  constexpr int NP = 32 - NLF / 2;
  const __amdgpu_buffer_rsrc_t r0 = mk_rsrc(ROLE == 0 ? a.Wq : a.Wv, 8u << 20), r1 = mk_rsrc(ROLE == 0 ? a.Wk : a.Wo, 8u << 20);
  const unsigned lds0 = (unsigned)(uintptr_t)(LAS char*)smem;
  unsigned vo[2][4];
#pragma unroll
  for (int j = 0; j < 4; ++j) {
    const int row = (w * 4 + j) * 4 + (lane >> 4), lc = (lane & 15) ^ swz_b(row);
    vo[0][j] = row * HD * 2 + lc * 16; vo[1][j] = row * DM * 2 + lc * 16;
  }
  asm volatile("s_nop 4");
  auto stage = [&](int i) {
    const int blk = S.ord[i];
    const bool second = ROLE == 0 ? (blk & 1) : blk >= 16, wo = ROLE == 1 && second;
    const unsigned so = wo ? (unsigned)(((128 * h + 64 * (blk & 1)) * DM + 128 * (((blk - 16) >> 1) ^ (2 * (h & 3)))) * 2)
                           : (unsigned)((64 * ((ROLE == 0 ? blk >> 1 : blk) ^ (h & RHO)) * HD + 128 * h) * 2);
#pragma unroll
    for (int j = 0; j < 4; ++j) dma16(second ? r1 : r0, vo[wo][j], so, lds0 + 16384 * S.slot[i] + (w * 4 + j) * 1024, W_NT & (ROLE ? 8 : 1));
  };
#pragma unroll
  for (int i = 0; i < 32; ++i) if (S.issue[i] == -1) stage(i);
#pragma unroll
  for (int c = 0; c < NP; ++c) {
    asm volatile("s_waitcnt vmcnt(%0)" :: "n"(4 * S.out[c]) : "memory");
    lds_barrier();
#pragma unroll
    for (int i = 0; i < 32; ++i) if (S.issue[i] == c) stage(i);
    const char* sb = smem + 16384 * S.slot[c];
#pragma unroll
    for (int t = 0; t < 2; ++t) {
      u32x8 f = (ROLE == 1 && S.ord[c] >= 16) ? tr_frag2(sb, 2 * w, t, lane) : tr_frag<true>(sb, 2 * w + t, lane);
      if (S.reg[c] < 32) {
#pragma unroll
        for (int i = 0; i < 8; ++i) asm volatile("v_accvgpr_write_b32 a[%1], %0" :: "v"(f[i]), "n"(8 * ((S.reg[c] + t) & 31) + i));
      }
      else {
#pragma unroll
        for (int i = 0; i < 8; ++i) W.v[(S.reg[c] >> 2) & 3][8 * ((S.reg[c] + t) & 3) + i] = f[i];
      }
    }
    if (S.reg[c] >= 32) asm volatile("" : "+v"(W.v[(S.reg[c] >> 2) & 3]));
  }
  static_assert(S.issue[31] < NP && S.issue[NP] < S.issue[31], "resident blocks are issued in slot order inside the loop");
  G.ask(a.sync, gv);
#pragma unroll
  for (int j = 0; j < NLF / 2; ++j) {
    asm volatile("s_waitcnt vmcnt(%0)" :: "n"(4 * (NLF / 2 - 1 - j) + 1) : "memory");
    lds_barrier();
    const bool two = ROLE == 1 && S.ord[NP + j] >= 16;
    u32x8 f0 = two ? tr_frag2(smem + 16384 * j, 2 * w, 0, lane) : tr_frag<true>(smem + 16384 * j, 2 * w, lane);
    u32x8 f1 = two ? tr_frag2(smem + 16384 * j, 2 * w, 1, lane) : tr_frag<true>(smem + 16384 * j, 2 * w + 1, lane);
    lds_barrier();
    char* d = smem + 16384 * j + w * 4096 + lane * 16;
    *(u32x4*)d = (u32x4){f0[0], f0[1], f0[2], f0[3]};
    *(u32x4*)(d + 1024) = (u32x4){f0[4], f0[5], f0[6], f0[7]};
    *(u32x4*)(d + 2048) = (u32x4){f1[0], f1[1], f1[2], f1[3]};
    *(u32x4*)(d + 3072) = (u32x4){f1[4], f1[5], f1[6], f1[7]};
  }
  asm volatile("s_waitcnt vmcnt(0)" : "+v"(gv) :: "memory");
  lds_barrier();
}
template <int... Is> struct iseq {};
template <int N, int... Is> struct mkseq : mkseq<N - 1, N - 1, Is...> {};
template <int... Is> struct mkseq<0, Is...> { typedef iseq<Is...> type; };

#define SB0 __builtin_amdgcn_sched_barrier(0)
#define CAT8(x, y) ((u32x8){x[0], x[1], x[2], x[3], y[0], y[1], y[2], y[3]})
template <class F, int... I>
__device__ __forceinline__ void for_slots(iseq<I...>, F f) { (f(std::integral_constant<int, I>{}), ...); }
#define QK_BODY \
  "s_cmp_eq_u32 %[wv], 1\n\t" \
  "s_cbranch_scc1 91f\n\t" \
  "s_cmp_eq_u32 %[wv], 2\n\t" \
  "s_cbranch_scc1 92f\n\t" \
  "s_cmp_eq_u32 %[wv], 3\n\t" \
  "s_cbranch_scc1 93f\n\t" \
  "90:\n\t" \
  "s_mov_b32 s58, s42\n\t" \
  "20:\n\t" \
  "s_and_b32 s76, s44, 7\n\t" \
  "s_lshl_b32 s76, s76, 15\n\t" \
  "s_add_i32 s40, s54, s76\n\t" \
  "s_and_b32 s76, s44, 3\n\t" \
  "s_lshl_b32 s45, s76, 14\n\t" \
  "s_mul_i32 s47, s76, 0xc000\n\t" \
  "s_add_i32 s47, s47, 0x40000\n\t" \
  "s_add_i32 s49, s44, -1\n\t" \
  "s_and_b32 s48, s49, 2\n\t" \
  "s_lshl_b32 s48, s48, 13\n\t" \
  "s_and_b32 s76, s49, 1\n\t" \
  "s_lshl_b32 s76, s76, 12\n\t" \
  "s_add_i32 s48, s48, s76\n\t" \
  "s_lshl_b32 s50, s53, 8\n\t" \
  "s_mov_b32 s59, 0\n\t" \
  "s_mov_b32 s92, s51\n\t" \
  "s_mov_b32 s93, s53\n\t" \
  "s_xor_b32 s76, s40, 0x400\n\t" \
  "s_xor_b32 s77, s40, 0x800\n\t" \
  "s_xor_b32 s78, s40, 0xc00\n\t" \
  "s_cmp_lg_u32 s58, 0\n\t" \
  "s_cbranch_scc1 11f\n\t" \
  "10:\n\t" \
  "ds_read_b128 v[48:51], v102 offset:0\n\t" \
  "ds_read_b128 v[52:55], v102 offset:1024\n\t" \
  "ds_read_b128 v[56:59], v102 offset:2048\n\t" \
  "ds_read_b128 v[60:63], v102 offset:3072\n\t" \
  "ds_read_b128 v[16:19], v102 offset:16384\n\t" \
  "ds_read_b128 v[20:23], v102 offset:17408\n\t" \
  "ds_read_b128 v[24:27], v102 offset:18432\n\t" \
  "ds_read_b128 v[28:31], v102 offset:19456\n\t" \
  "ds_read_b128 v[84:87], v102 offset:49152\n\t" \
  "ds_read_b128 v[88:91], v102 offset:50176\n\t" \
  "ds_read_b128 v[92:95], v102 offset:51200\n\t" \
  "ds_read_b128 v[96:99], v102 offset:52224\n\t" \
  "s_waitcnt lgkmcnt(4)\n\t" \
  "ds_read_b128 v[0:3], v103 offset:16384\n\t" \
  "ds_read_b128 v[4:7], v103 offset:17408\n\t" \
  "ds_read_b128 v[8:11], v103 offset:18432\n\t" \
  "ds_read_b128 v[12:15], v103 offset:19456\n\t" \
  "s_bitcmp1_b32 s58, 1\n\t" \
  "s_cselect_b64 s[80:81], -1, 0\n\t" \
  "s_cmp_lg_u32 s58, 0\n\t" \
  "s_cbranch_scc1 17f\n\t" \
  "s_mov_b32 exec_hi, 0\n\t" \
  "16:\n\t" \
  "s_memtime vcc\n\t" \
  "s_waitcnt lgkmcnt(0)\n\t" \
  "s_sub_i32 s79, s41, vcc_lo\n\t" \
  "s_cmp_gt_i32 s79, 64\n\t" \
  "s_cbranch_scc1 16b\n\t" \
  "13:\n\t" \
  "buffer_load_dword v116, v112, %[rh], s48 offen sc1\n\t" \
  "s_waitcnt vmcnt(0)\n\t" \
  "15:\n\t" \
  "v_mov_b32 v117, 20000\n\t" \
  "v_cmp_ne_u32_sdwa vcc, v116, s49 src0_sel:BYTE_0 src1_sel:DWORD\n\t" \
  "v_subrev_u32 v116, s50, v116\n\t" \
  "v_ashrrev_i32 v116, 8, v116\n\t" \
  "s_nop 1\n\t" \
  "v_max_i32_dpp v116, v116, v116 quad_perm:[1,0,3,2] row_mask:0xf bank_mask:0xf\n\t" \
  "s_cbranch_vccnz 14f\n\t" \
  "s_nop 0\n\t" \
  "v_max_i32_dpp v116, v116, v116 quad_perm:[2,3,0,1] row_mask:0xf bank_mask:0xf\n\t" \
  "s_nop 1\n\t" \
  "v_max_i32_dpp v116, v116, v116 row_half_mirror row_mask:0xf bank_mask:0xf\n\t" \
  "s_nop 1\n\t" \
  "v_max_i32_dpp v116, v116, v116 row_mirror row_mask:0xf bank_mask:0xf\n\t" \
  "v_med3_i32 v116, v116, 0, v117\n\t" \
  "v_mul_u32_u24 v116, s52, v116\n\t" \
  "v_lshrrev_b32 v116, 16, v116\n\t" \
  "s_mov_b64 exec, -1\n\t" \
  "v_readfirstlane_b32 s84, v116\n\t" \
  "v_readlane_b32 s85, v116, 16\n\t" \
  "s_max_u32 s84, s84, s85\n\t" \
  "s_add_i32 s84, s84, s51\n\t" \
  "1:\n\t" \
  "s_memtime vcc\n\t" \
  "s_waitcnt lgkmcnt(0)\n\t" \
  "s_sub_i32 s79, s84, vcc_lo\n\t" \
  "s_cmp_gt_i32 s79, 128\n\t" \
  "s_cbranch_scc1 1b\n\t" \
  ".p2align 4\n\t" \
  "2:\n\t" \
  "s_sub_i32 s79, s79, 33\n\t" \
  "s_cmp_gt_i32 s79, 56\n\t" \
  "s_cbranch_scc1 2b\n\t" \
  "buffer_load_dwordx4 v[32:35], v100, %[rh], s40 offen sc1\n\t" \
  "s_nop 12\n\t" \
  "buffer_load_dwordx4 v[36:39], v100, %[rh], s76 offen sc1\n\t" \
  "s_nop 12\n\t" \
  "buffer_load_dwordx4 v[40:43], v100, %[rh], s77 offen sc1\n\t" \
  "s_nop 12\n\t" \
  "buffer_load_dwordx4 v[44:47], v100, %[rh], s78 offen sc1\n\t" \
  "s_memtime vcc\n\t" \
  "s_memrealtime s[82:83]\n\t" \
  "s_nop 4\n\t" \
  "s_memrealtime s[84:85]\n\t" \
  "s_nop 4\n\t" \
  "s_memrealtime s[56:57]\n\t" \
  "s_nop 4\n\t" \
  "s_memrealtime s[86:87]\n\t" \
  "s_nop 4\n\t" \
  "s_memrealtime s[88:89]\n\t" \
  "s_nop 4\n\t" \
  "s_memrealtime s[90:91]\n\t" \
  "s_mov_b32 s92, 0\n\t" \
  "s_waitcnt lgkmcnt(0)\n\t" \
  "s_cmp_eq_u32 s82, s84\n\t" \
  "s_addc_u32 s92, s92, 0\n\t" \
  "s_cmp_eq_u32 s82, s56\n\t" \
  "s_addc_u32 s92, s92, 0\n\t" \
  "s_cmp_eq_u32 s82, s86\n\t" \
  "s_addc_u32 s92, s92, 0\n\t" \
  "s_cmp_eq_u32 s82, s88\n\t" \
  "s_addc_u32 s92, s92, 0\n\t" \
  "s_cmp_eq_u32 s82, s90\n\t" \
  "s_addc_u32 s92, s92, 0\n\t" \
  "s_mul_i32 s92, s92, 24\n\t" \
  "s_add_i32 s92, s92, vcc_lo\n\t" \
  "s_lshl4_add_u32 s93, s82, 64\n\t" \
  "3:\n\t" \
  "s_sub_i32 s84, s92, s43\n\t" \
  "s_lshl_b32 s85, s93, 8\n\t" \
  "s_or_b32 s85, s85, s44\n\t" \
  "v_mov_b32 v118, s85\n\t" \
  "v_mov_b64 v[68:69], 0\n\t" \
  "v_mov_b64 v[70:71], 0\n\t" \
  "v_mov_b64 v[72:73], 0\n\t" \
  "v_mov_b64 v[74:75], 0\n\t" \
  "v_mov_b64 v[76:77], 0\n\t" \
  "v_mov_b64 v[78:79], 0\n\t" \
  "v_mov_b64 v[80:81], 0\n\t" \
  "v_mov_b64 v[82:83], 0\n\t" \
  "s_waitcnt lgkmcnt(0)\n\t" \
  "s_waitcnt vmcnt(3)\n\t" \
  "v_smfmac_f32_16x16x64_bf16 v[68:71], v[32:35], a[0:7], v104\n\t" \
  "ds_write_b128 v101, v[32:35] offset:0\n\t" \
  "v_smfmac_f32_16x16x64_bf16 v[72:75], v[32:35], a[8:15], v104\n\t" \
  "v_smfmac_f32_16x16x64_bf16 v[76:79], v[32:35], v[48:55], v104\n\t" \
  "v_smfmac_f32_16x16x64_bf16 v[80:83], v[32:35], v[56:63], v104\n\t" \
  "s_waitcnt vmcnt(2)\n\t" \
  "v_smfmac_f32_16x16x64_bf16 v[68:71], v[36:39], a[16:23], v104\n\t" \
  "ds_write_b128 v101, v[36:39] offset:1024\n\t" \
  "v_smfmac_f32_16x16x64_bf16 v[72:75], v[36:39], a[24:31], v104\n\t" \
  "v_smfmac_f32_16x16x64_bf16 v[76:79], v[36:39], a[32:39], v104\n\t" \
  "v_cmp_u_f32 vcc, v68, v69\n\t" \
  "v_smfmac_f32_16x16x64_bf16 v[80:83], v[36:39], a[40:47], v104\n\t" \
  "v_cmp_u_f32 s[82:83], v70, v71\n\t" \
  "s_waitcnt vmcnt(1)\n\t" \
  "v_smfmac_f32_16x16x64_bf16 v[68:71], v[40:43], v[16:23], v104\n\t" \
  "ds_write_b128 v101, v[40:43] offset:2048\n\t" \
  "v_smfmac_f32_16x16x64_bf16 v[72:75], v[40:43], v[24:31], v104\n\t" \
  "s_or_b64 vcc, vcc, s[82:83]\n\t" \
  "s_or_b64 vcc, vcc, s[80:81]\n\t" \
  "s_waitcnt lgkmcnt(1)\n\t" \
  "v_smfmac_f32_16x16x64_bf16 v[76:79], v[40:43], a[48:55], v104\n\t" \
  "s_cbranch_scc1 8f\n\t" \
  "5:\n\t" \
  "s_barrier\n\t" \
  "4:\n\t" \
  "s_memtime s[56:57]\n\t" \
  "ds_read_b128 v[48:51], v101 offset:4096\n\t" \
  "v_smfmac_f32_16x16x64_bf16 v[80:83], v[40:43], a[56:63], v104\n\t" \
  "ds_read_b128 v[52:55], v101 offset:5120\n\t" \
  "ds_read_b128 v[56:59], v101 offset:8192\n\t" \
  "s_waitcnt vmcnt(0)\n\t" \
  "v_smfmac_f32_16x16x64_bf16 v[68:71], v[44:47], a[64:71], v104\n\t" \
  "ds_write_b128 v101, v[44:47] offset:3072\n\t" \
  "v_smfmac_f32_16x16x64_bf16 v[72:75], v[44:47], a[72:79], v104\n\t" \
  "v_smfmac_f32_16x16x64_bf16 v[76:79], v[44:47], a[80:87], v104\n\t" \
  "v_cmp_u_f32 vcc, v68, v69\n\t" \
  "v_smfmac_f32_16x16x64_bf16 v[80:83], v[44:47], a[88:95], v104\n\t" \
  "v_cmp_u_f32 s[82:83], v70, v71\n\t" \
  "s_waitcnt lgkmcnt(3)\n\t" \
  "v_smfmac_f32_16x16x64_bf16 v[68:71], v[48:51], a[96:103], v104\n\t" \
  "ds_read_b128 v[60:63], v101 offset:9216\n\t" \
  "ds_read_b128 v[64:67], v101 offset:12288\n\t" \
  "v_smfmac_f32_16x16x64_bf16 v[72:75], v[48:51], a[104:111], v104\n\t" \
  "s_or_b64 vcc, vcc, s[82:83]\n\t" \
  "s_waitcnt lgkmcnt(2)\n\t" \
  "v_smfmac_f32_16x16x64_bf16 v[68:71], v[52:55], a[112:119], v104\n\t" \
  "s_cbranch_scc1 9f\n\t" \
  "6:\n\t" \
  "s_barrier\n\t" \
  "v_smfmac_f32_16x16x64_bf16 v[72:75], v[52:55], a[120:127], v104\n\t" \
  "s_waitcnt lgkmcnt(0)\n\t" \
  "ds_read_b128 v[48:51], v101 offset:13312\n\t" \
  "v_smfmac_f32_16x16x64_bf16 v[68:71], v[56:59], a[192:199], v104\n\t" \
  "ds_read_b128 v[52:55], v101 offset:6144\n\t" \
  "s_sub_i32 s79, s56, s84\n\t" \
  "s_mul_hi_u32 s79, s79, s46\n\t" \
  "v_smfmac_f32_16x16x64_bf16 v[72:75], v[56:59], a[200:207], v104\n\t" \
  "v_lshl_add_u32 v116, s79, 9, v118\n\t" \
  "buffer_store_dword v116, v109, %[rh], s45 offen sc1\n\t" \
  "v_smfmac_f32_16x16x64_bf16 v[68:71], v[60:63], a[208:215], v104\n\t" \
  "ds_read_b128 v[56:59], v101 offset:7168\n\t" \
  "v_smfmac_f32_16x16x64_bf16 v[72:75], v[60:63], a[216:223], v104\n\t" \
  "v_smfmac_f32_16x16x64_bf16 v[68:71], v[64:67], v[152:159], v104\n\t" \
  "ds_read_b128 v[60:63], v101 offset:10240\n\t" \
  "v_smfmac_f32_16x16x64_bf16 v[72:75], v[64:67], v[160:167], v104\n\t" \
  "s_waitcnt lgkmcnt(3)\n\t" \
  "v_smfmac_f32_16x16x64_bf16 v[68:71], v[48:51], v[168:175], v104\n\t" \
  "ds_read_b128 v[64:67], v101 offset:11264\n\t" \
  "ds_read_b128 v[16:19], v103 offset:49152\n\t" \
  "v_smfmac_f32_16x16x64_bf16 v[72:75], v[48:51], v[176:183], v104\n\t" \
  "s_waitcnt lgkmcnt(4)\n\t" \
  "v_smfmac_f32_16x16x64_bf16 v[68:71], v[52:55], v[84:91], v104\n\t" \
  "ds_read_b128 v[48:51], v101 offset:15360\n\t" \
  "ds_read_b128 v[20:23], v103 offset:50176\n\t" \
  "v_smfmac_f32_16x16x64_bf16 v[72:75], v[52:55], v[92:99], v104\n\t" \
  "s_waitcnt lgkmcnt(5)\n\t" \
  "v_smfmac_f32_16x16x64_bf16 v[68:71], v[56:59], a[160:167], v104\n\t" \
  "ds_read_b128 v[52:55], v101 offset:14336\n\t" \
  "ds_read_b128 v[24:27], v103 offset:51200\n\t" \
  "v_smfmac_f32_16x16x64_bf16 v[72:75], v[56:59], a[168:175], v104\n\t" \
  "ds_read_b128 v[28:31], v103 offset:52224\n\t" \
  "s_waitcnt lgkmcnt(7)\n\t" \
  "v_smfmac_f32_16x16x64_bf16 v[68:71], v[60:63], v[0:7], v104\n\t" \
  "ds_read_b128 v[32:35], v101 offset:20480\n\t" \
  "v_smfmac_f32_16x16x64_bf16 v[72:75], v[60:63], v[8:15], v104\n\t" \
  "ds_read_b128 v[36:39], v101 offset:21504\n\t" \
  "v_mov_b64 v[92:93], 0\n\t" \
  "s_waitcnt lgkmcnt(8)\n\t" \
  "v_smfmac_f32_16x16x64_bf16 v[68:71], v[64:67], v[120:127], v104\n\t" \
  "ds_read_b128 v[0:3], v102 offset:32768\n\t" \
  "ds_read_b128 v[40:43], v101 offset:22528\n\t" \
  "v_smfmac_f32_16x16x64_bf16 v[72:75], v[64:67], v[128:135], v104\n\t" \
  "ds_read_b128 v[4:7], v102 offset:33792\n\t" \
  "s_waitcnt lgkmcnt(9)\n\t" \
  "v_smfmac_f32_16x16x64_bf16 v[68:71], v[48:51], v[216:223], v104\n\t" \
  "ds_read_b128 v[44:47], v101 offset:23552\n\t" \
  "v_mov_b64 v[94:95], 0\n\t" \
  "s_waitcnt lgkmcnt(8)\n\t" \
  "v_smfmac_f32_16x16x64_bf16 v[68:71], v[52:55], v[16:23], v104\n\t" \
  "ds_read_b128 v[8:11], v102 offset:34816\n\t" \
  "v_smfmac_f32_16x16x64_bf16 v[72:75], v[48:51], v[224:231], v104\n\t" \
  "ds_read_b128 v[12:15], v102 offset:35840\n\t" \
  "s_waitcnt lgkmcnt(8)\n\t" \
  "v_smfmac_f32_16x16x64_bf16 v[72:75], v[52:55], v[24:31], v104\n\t" \
  "v_add_f32 v96, v68, v70\n\t" \
  "v_smfmac_f32_16x16x64_bf16 v[76:79], v[56:59], a[176:183], v104\n\t" \
  "v_add_f32 v97, v69, v71\n\t" \
  "v_cvt_pk_bf16_f32 v96, v96, v97\n\t" \
  "v_smfmac_f32_16x16x64_bf16 v[80:83], v[56:59], a[184:191], v104\n\t" \
  "v_add_f32 v98, v72, v74\n\t" \
  "v_add_f32 v99, v73, v75\n\t" \
  "v_smfmac_f32_16x16x64_bf16 v[76:79], v[60:63], a[240:247], v104\n\t" \
  "v_cvt_pk_bf16_f32 v98, v98, v99\n\t" \
  "ds_write2st64_b32 v105, v96, v98 offset1:1\n\t" \
  "v_smfmac_f32_16x16x64_bf16 v[80:83], v[60:63], a[248:255], v104\n\t" \
  "ds_read_b128 v[56:59], v101 offset:4096\n\t" \
  "ds_read_b128 v[16:19], v101 offset:24576\n\t" \
  "v_mov_b64 v[68:69], 0\n\t" \
  "v_smfmac_f32_16x16x64_bf16 v[76:79], v[64:67], v[136:143], v104\n\t" \
  "ds_read_b128 v[20:23], v101 offset:25600\n\t" \
  "v_mov_b64 v[70:71], 0\n\t" \
  "v_smfmac_f32_16x16x64_bf16 v[80:83], v[64:67], v[144:151], v104\n\t" \
  "ds_read_b128 v[24:27], v101 offset:26624\n\t" \
  "v_smfmac_f32_16x16x64_bf16 v[76:79], v[48:51], v[232:239], v104\n\t" \
  "s_waitcnt lgkmcnt(4)\n\t" \
  "s_barrier\n\t" \
  "ds_read_b64_tr_b16 v[84:85], v107 offset:0\n\t" \
  "v_smfmac_f32_16x16x64_bf16 v[80:83], v[48:51], v[240:247], v104\n\t" \
  "ds_read_b64_tr_b16 v[86:87], v107 offset:128\n\t" \
  "ds_read_b64_tr_b16 v[88:89], v107 offset:1024\n\t" \
  "v_smfmac_f32_16x16x64_bf16 v[76:79], v[52:55], v[200:207], v104\n\t" \
  "ds_read_b128 v[60:63], v101 offset:13312\n\t" \
  "ds_read_b64_tr_b16 v[90:91], v107 offset:1152\n\t" \
  "v_smfmac_f32_16x16x64_bf16 v[80:83], v[52:55], v[208:215], v104\n\t" \
  "ds_read_b128 v[64:67], v101 offset:6144\n\t" \
  "ds_read_b128 v[28:31], v101 offset:27648\n\t" \
  "s_waitcnt lgkmcnt(10)\n\t" \
  "v_smfmac_f32_16x16x64_bf16 v[76:79], v[56:59], v[0:7], v104\n\t" \
  "ds_read_b128 v[48:51], v101 offset:8192\n\t" \
  "s_waitcnt lgkmcnt(6)\n\t" \
  "v_smfmac_f32_16x16x64_bf16 v[92:95], v[84:87], v[32:39], v104\n\t" \
  "ds_read_b128 v[0:3], v103 offset:0\n\t" \
  "s_waitcnt lgkmcnt(4)\n\t" \
  "v_smfmac_f32_16x16x64_bf16 v[92:95], v[88:91], v[40:47], v104\n\t" \
  "ds_read_b128 v[4:7], v103 offset:1024\n\t" \
  "v_smfmac_f32_16x16x64_bf16 v[68:71], v[84:87], v[16:23], v104\n\t" \
  "s_waitcnt lgkmcnt(3)\n\t" \
  "v_smfmac_f32_16x16x64_bf16 v[68:71], v[88:91], v[24:31], v104\n\t" \
  "v_add_f32 v92, v92, v94\n\t" \
  "v_add_f32 v94, v93, v95\n\t" \
  "v_smfmac_f32_16x16x64_bf16 v[80:83], v[56:59], v[8:15], v104\n\t" \
  "ds_read_b128 v[8:11], v103 offset:2048\n\t" \
  "v_mov_b32 v97, v94\n\t" \
  "v_mov_b32_dpp v94, v92 row_ror:8 row_mask:0xf bank_mask:0x3\n\t" \
  "s_nop 0\n\t" \
  "v_mov_b32_dpp v92, v97 row_ror:8 row_mask:0xf bank_mask:0xc\n\t" \
  "v_smfmac_f32_16x16x64_bf16 v[76:79], v[60:63], v[184:191], v104\n\t" \
  "ds_read_b128 v[52:55], v101 offset:5120\n\t" \
  "ds_read_b128 v[12:15], v103 offset:3072\n\t" \
  "v_add_f32 v93, v68, v70\n\t" \
  "v_add_f32 v95, v69, v71\n\t" \
  "v_max_f32 v98, v92, v94\n\t" \
  "v_mov_b32 v97, v95\n\t" \
  "v_mov_b32_dpp v95, v93 row_ror:8 row_mask:0xf bank_mask:0x3\n\t" \
  "v_smfmac_f32_16x16x64_bf16 v[80:83], v[60:63], v[192:199], v104\n\t" \
  "v_mov_b32_dpp v93, v97 row_ror:8 row_mask:0xf bank_mask:0xc\n\t" \
  "v_max3_f32 v98, v98, v93, v95\n\t" \
  "v_smfmac_f32_16x16x64_bf16 v[76:79], v[64:67], a[144:151], v104\n\t" \
  "ds_read_b128 v[56:59], v101 offset:9216\n\t" \
  "ds_read_b128 v[32:35], v103 offset:32768\n\t" \
  "v_max_f32_dpp v98, v98, v98 quad_perm:[1,0,3,2] row_mask:0xf bank_mask:0xf\n\t" \
  "v_smfmac_f32_16x16x64_bf16 v[80:83], v[64:67], a[152:159], v104\n\t" \
  "ds_read_b128 v[60:63], v101 offset:12288\n\t" \
  "ds_read_b128 v[36:39], v103 offset:33792\n\t" \
  "v_max_f32_dpp v98, v98, v98 quad_perm:[2,3,0,1] row_mask:0xf bank_mask:0xf\n\t" \
  "s_waitcnt lgkmcnt(7)\n\t" \
  "v_smfmac_f32_16x16x64_bf16 v[76:79], v[48:51], v[0:7], v104\n\t" \
  "ds_read_b128 v[40:43], v103 offset:34816\n\t" \
  "v_max_f32_dpp v98, v98, v98 row_half_mirror row_mask:0xf bank_mask:0xf\n\t" \
  "v_mul_f32 v98, %[cs], v98\n\t" \
  "v_fma_f32 v92, v92, %[cs], -v98\n\t" \
  "v_fma_f32 v93, v93, %[cs], -v98\n\t" \
  "v_fma_f32 v94, v94, %[cs], -v98\n\t" \
  "s_waitcnt lgkmcnt(5)\n\t" \
  "v_smfmac_f32_16x16x64_bf16 v[80:83], v[48:51], v[8:15], v104\n\t" \
  "ds_read_b128 v[44:47], v103 offset:35840\n\t" \
  "v_fma_f32 v95, v95, %[cs], -v98\n\t" \
  "v_exp_f32 v72, v92\n\t" \
  "v_exp_f32 v73, v93\n\t" \
  "v_exp_f32 v74, v94\n\t" \
  "v_exp_f32 v75, v95\n\t" \
  "v_cvt_pk_bf16_f32 v68, v72, v73\n\t" \
  "v_cvt_pk_bf16_f32 v69, v74, v75\n\t" \
  "buffer_store_dwordx2 v[68:69], v110, %[rh], s47 offen sc1\n\t" \
  "v_smfmac_f32_16x16x64_bf16 v[76:79], v[52:55], a[128:135], v104\n\t" \
  "v_smfmac_f32_16x16x64_bf16 v[80:83], v[52:55], a[136:143], v104\n\t" \
  "s_waitcnt lgkmcnt(5)\n\t" \
  "v_smfmac_f32_16x16x64_bf16 v[76:79], v[56:59], a[224:231], v104\n\t" \
  "s_waitcnt lgkmcnt(2)\n\t" \
  "v_smfmac_f32_16x16x64_bf16 v[76:79], v[60:63], v[32:39], v104\n\t" \
  "v_smfmac_f32_16x16x64_bf16 v[80:83], v[56:59], a[232:239], v104\n\t" \
  "s_waitcnt lgkmcnt(0)\n\t" \
  "v_smfmac_f32_16x16x64_bf16 v[80:83], v[60:63], v[40:47], v104\n\t" \
  "v_add_f32 v20, v76, v78\n\t" \
  "v_add_f32 v21, v77, v79\n\t" \
  "v_cvt_pk_bf16_f32 v20, v20, v21\n\t" \
  "ds_write_b32 v106, v20\n\t" \
  "s_nop 0\n\t" \
  "s_nop 0\n\t" \
  "s_nop 0\n\t" \
  "s_nop 0\n\t" \
  "v_add_f32 v23, v80, v82\n\t" \
  "v_add_f32 v21, v81, v83\n\t" \
  "v_cvt_pk_bf16_f32 v21, v23, v21\n\t" \
  "ds_write_b32 v106, v21 offset:256\n\t" \
  "s_waitcnt lgkmcnt(0)\n\t" \
  "s_barrier\n\t" \
  "ds_read_b64_tr_b16 v[4:5], v108\n\t" \
  "ds_read_b64_tr_b16 v[6:7], v108 offset:64\n\t" \
  "ds_read_b64_tr_b16 v[8:9], v108 offset:512\n\t" \
  "ds_read_b64_tr_b16 v[10:11], v108 offset:576\n\t" \
  "ds_read_b64_tr_b16 v[12:13], v108 offset:1024\n\t" \
  "ds_read_b64_tr_b16 v[14:15], v108 offset:1088\n\t" \
  "ds_read_b64_tr_b16 v[16:17], v108 offset:1536\n\t" \
  "ds_read_b64_tr_b16 v[18:19], v108 offset:1600\n\t" \
  "v_mov_b64 v[0:1], 0\n\t" \
  "v_mov_b64 v[2:3], 0\n\t" \
  "v_add_f32 v29, v72, v73\n\t" \
  "v_add_f32 v29, v29, v74\n\t" \
  "s_waitcnt lgkmcnt(4)\n\t" \
  "v_smfmac_f32_16x16x64_bf16 v[0:3], v[84:87], v[4:11], v104\n\t" \
  "s_waitcnt lgkmcnt(0)\n\t" \
  "v_smfmac_f32_16x16x64_bf16 v[0:3], v[88:91], v[12:19], v104\n\t" \
  "v_add_f32 v29, v29, v75\n\t" \
  "s_mov_b32 s76, 0xaaaaaaaa\n\t" \
  "s_mov_b32 s77, s76\n\t" \
  "s_nop 4\n\t" \
  "v_add_f32 v96, v0, v2\n\t" \
  "v_add_f32_dpp v96, v1, v3 quad_perm:[0,1,2,3] row_mask:0xf bank_mask:0xc\n\t" \
  "v_fma_f32 v96, v96, %[cs], -v98\n\t" \
  "v_exp_f32 v24, v96\n\t" \
  "s_nop 0\n\t" \
  "v_add_f32 v29, v29, v24\n\t" \
  "s_nop 0\n\t" \
  "v_mov_b32_dpp v25, v24 quad_perm:[1,0,3,2] row_mask:0xf bank_mask:0xf\n\t" \
  "v_add_f32_dpp v29, v29, v29 quad_perm:[1,0,3,2] row_mask:0xf bank_mask:0xf\n\t" \
  "v_cndmask_b32_e64 v24, v24, v114, s[76:77]\n\t" \
  "v_cndmask_b32_e64 v25, v25, v115, s[76:77]\n\t" \
  "v_add_f32_dpp v29, v29, v29 quad_perm:[2,3,0,1] row_mask:0xf bank_mask:0xf\n\t" \
  "s_nop 1\n\t" \
  "v_add_f32_dpp v29, v29, v29 row_half_mirror row_mask:0xf bank_mask:0xf\n\t" \
  "v_rcp_f32 v26, v29\n\t" \
  "v_cmpx_gt_f32 vcc, 0x5d5e0b6b, v29\n\t" \
  "v_mul_f32 v24, v24, v26\n\t" \
  "v_mul_f32 v25, v25, v26\n\t" \
  "v_cvt_pk_bf16_f32 v24, v24, v25\n\t" \
  "buffer_store_dword v24, v111, %[rh], s47 offen sc1\n\t" \
  "s_memtime s[76:77]\n\t" \
  "s_mov_b64 exec, -1\n\t" \
  "v_mov_b32 v97, v29\n\t" \
  "s_cmp_eq_u64 vcc, -1\n\t" \
  "s_cselect_b32 s42, 0, 8\n\t" \
  "s_waitcnt lgkmcnt(0)\n\t" \
  "s_mov_b32 s57, s76\n\t" \
  "s_barrier\n\t" \
  "s_add_i32 s41, s56, 1950\n\t" \
  "s_sub_i32 s76, s57, s56\n\t" \
  "s_add_i32 s77, s43, 8\n\t" \
  "s_min_u32 s76, s76, s77\n\t" \
  "s_sub_i32 s43, s43, 4\n\t" \
  "s_max_u32 s43, s43, s76\n\t" \
  "s_add_i32 s76, s45, 0x4000\n\t" \
  "s_and_b32 s76, s76, 0xc000\n\t" \
  "v_mov_b32 v116, -1\n\t" \
  "buffer_store_dword v116, v109, %[rh], s76 offen sc1\n\t" \
  "s_sub_i32 s76, s92, s51\n\t" \
  "s_sub_i32 s77, s93, s53\n\t" \
  "s_cmp_eq_u32 s76, 0\n\t" \
  "s_cbranch_scc1 21f\n\t" \
  "v_cvt_f32_u32 v117, s76\n\t" \
  "v_cvt_f32_u32 v116, s77\n\t" \
  "v_rcp_f32 v117, v117\n\t" \
  "s_mov_b32 s51, s92\n\t" \
  "v_mul_f32 v117, v117, v116\n\t" \
  "v_max_f32 v117, 0x3f028f5c, v117\n\t" \
  "v_min_f32 v117, 0x3ffeb852, v117\n\t" \
  "v_rcp_f32 v116, v117\n\t" \
  "v_mul_f32 v117, 0x4f000000, v117\n\t" \
  "v_mul_f32 v116, 0x47800000, v116\n\t" \
  "v_cvt_u32_f32 v117, v117\n\t" \
  "v_cvt_u32_f32 v116, v116\n\t" \
  "s_mov_b32 s53, s93\n\t" \
  "v_readfirstlane_b32 s46, v117\n\t" \
  "v_readfirstlane_b32 s52, v116\n\t" \
  "21:\n\t" \
  "s_add_i32 s44, s44, 1\n\t" \
  "s_and_b32 s58, s58, 4\n\t" \
  "s_and_b32 s76, s44, 64\n\t" \
  "s_or_b32 s42, s42, s76\n\t" \
  "s_cbranch_scc0 20b\n\t" \
  "s_branch 99f\n\t" \
  "14:\n\t" \
  "s_sleep 3\n\t" \
  "s_add_u32 s59, s59, 0x10000\n\t" \
  "s_cmp_lt_u32 s59, 0x100000\n\t" \
  "s_cbranch_scc1 13b\n\t" \
  "s_mov_b64 exec, -1\n\t" \
  "s_mov_b32 s42, 16\n\t" \
  "s_branch 99f\n\t" \
  "8:\n\t" \
  "s_bitcmp1_b32 s58, 1\n\t" \
  "s_cbranch_scc1 4b\n\t" \
  "s_bitcmp1_b32 s58, 2\n\t" \
  "s_cbranch_scc1 5b\n\t" \
  "v_max3_u32 v116, v32, v33, v34\n\t" \
  "v_max3_u32 v117, v35, v36, v37\n\t" \
  "v_max3_u32 v116, v116, v38, v39\n\t" \
  "v_max_u32 v116, v116, v117\n\t" \
  "v_cmp_eq_u32 vcc, -1, v116\n\t" \
  "s_cbranch_vccz 5b\n\t" \
  "s_branch 0f\n\t" \
  "9:\n\t" \
  "s_bitcmp1_b32 s58, 2\n\t" \
  "s_cbranch_scc1 6b\n\t" \
  "v_max3_u32 v116, v40, v41, v42\n\t" \
  "v_max3_u32 v117, v43, v44, v45\n\t" \
  "v_max3_u32 v116, v116, v46, v47\n\t" \
  "v_max_u32 v116, v116, v117\n\t" \
  "v_cmp_eq_u32 vcc, -1, v116\n\t" \
  "s_cbranch_vccz 6b\n\t" \
  "s_or_b32 s58, s58, 2\n\t" \
  "0:\n\t" \
  "s_or_b32 s58, s58, 1\n\t" \
  "11:\n\t" \
  "buffer_load_dwordx4 v[32:35], v100, %[rh], s40 offen sc1\n\t" \
  "buffer_load_dwordx4 v[36:39], v100, %[rh], s76 offen sc1\n\t" \
  "buffer_load_dwordx4 v[40:43], v100, %[rh], s77 offen sc1\n\t" \
  "buffer_load_dwordx4 v[44:47], v100, %[rh], s78 offen sc1\n\t" \
  "s_waitcnt lgkmcnt(0)\n\t" \
  "s_branch 10b\n\t" \
  "17:\n\t" \
  "s_add_u32 s59, s59, 1\n\t" \
  "s_bitcmp1_b32 s58, 2\n\t" \
  "s_cbranch_scc1 3b\n\t" \
  "s_waitcnt vmcnt(0)\n\t" \
  "v_max_u32 v116, v32, v33\n\t" \
  "v_max3_u32 v116, v116, v34, v35\n\t" \
  "v_max3_u32 v116, v116, v36, v37\n\t" \
  "v_max3_u32 v116, v116, v38, v39\n\t" \
  "v_max3_u32 v116, v116, v40, v41\n\t" \
  "v_max3_u32 v116, v116, v42, v43\n\t" \
  "v_max3_u32 v116, v116, v44, v45\n\t" \
  "v_max3_u32 v116, v116, v46, v47\n\t" \
  "v_cmp_eq_u32 vcc, -1, v116\n\t" \
  "s_cbranch_vccz 3b\n\t" \
  "s_bitcmp1_b32 s59, 6\n\t" \
  "s_cbranch_scc1 12f\n\t" \
  "buffer_load_dwordx4 v[32:35], v100, %[rh], s40 offen sc1\n\t" \
  "buffer_load_dwordx4 v[36:39], v100, %[rh], s76 offen sc1\n\t" \
  "buffer_load_dwordx4 v[40:43], v100, %[rh], s77 offen sc1\n\t" \
  "buffer_load_dwordx4 v[44:47], v100, %[rh], s78 offen sc1\n\t" \
  "s_branch 17b\n\t" \
  "12:\n\t" \
  "s_mov_b32 s42, s58\n\t" \
  "s_branch 99f\n\t" \
  "91:\n\t" \
  "s_mov_b32 s58, s42\n\t" \
  "s_mov_b32 s95, 0\n\t" \
  "20:\n\t" \
  "s_and_b32 s76, s44, 7\n\t" \
  "s_lshl_b32 s76, s76, 15\n\t" \
  "s_add_i32 s40, s54, s76\n\t" \
  "s_and_b32 s76, s44, 3\n\t" \
  "s_lshl_b32 s45, s76, 14\n\t" \
  "s_add_i32 s47, s44, 1\n\t" \
  "s_min_u32 s47, s47, 63\n\t" \
  "s_lshr_b32 s76, s47, 1\n\t" \
  "s_lshl_b32 s47, s47, 20\n\t" \
  "s_add_i32 s47, s47, s55\n\t" \
  "s_bitcmp1_b64 %[sel], s76\n\t" \
  "s_cselect_b32 s76, 0x2000, 0\n\t" \
  "s_add_i32 s47, s47, s76\n\t" \
  "s_lshr_b32 s76, s44, 1\n\t" \
  "s_lshl_b32 s94, s44, 20\n\t" \
  "s_bitcmp1_b64 %[sel], s76\n\t" \
  "s_cselect_b32 s76, 0x2000, 0\n\t" \
  "s_add_i32 s94, s94, s76\n\t" \
  "s_add_i32 s49, s44, -1\n\t" \
  "s_and_b32 s48, s49, 2\n\t" \
  "s_lshl_b32 s48, s48, 13\n\t" \
  "s_and_b32 s76, s49, 1\n\t" \
  "s_lshl_b32 s76, s76, 12\n\t" \
  "s_add_i32 s48, s48, s76\n\t" \
  "s_lshl_b32 s50, s53, 8\n\t" \
  "s_mov_b32 s59, 0\n\t" \
  "s_mov_b32 s92, s51\n\t" \
  "s_mov_b32 s93, s53\n\t" \
  "s_xor_b32 s76, s40, 0x400\n\t" \
  "s_xor_b32 s77, s40, 0x800\n\t" \
  "s_xor_b32 s78, s40, 0xc00\n\t" \
  "s_cmp_lg_u32 s58, 0\n\t" \
  "s_cbranch_scc1 11f\n\t" \
  "10:\n\t" \
  "ds_read_b128 v[48:51], v102 offset:32768\n\t" \
  "ds_read_b128 v[52:55], v102 offset:33792\n\t" \
  "ds_read_b128 v[56:59], v102 offset:34816\n\t" \
  "ds_read_b128 v[60:63], v102 offset:35840\n\t" \
  "ds_read_b128 v[16:19], v102 offset:49152\n\t" \
  "ds_read_b128 v[20:23], v102 offset:50176\n\t" \
  "ds_read_b128 v[24:27], v102 offset:51200\n\t" \
  "ds_read_b128 v[28:31], v102 offset:52224\n\t" \
  "ds_read_b128 v[84:87], v103 offset:16384\n\t" \
  "ds_read_b128 v[88:91], v103 offset:17408\n\t" \
  "ds_read_b128 v[92:95], v103 offset:18432\n\t" \
  "ds_read_b128 v[96:99], v103 offset:19456\n\t" \
  "s_waitcnt lgkmcnt(4)\n\t" \
  "ds_read_b128 v[0:3], v103 offset:49152\n\t" \
  "ds_read_b128 v[4:7], v103 offset:50176\n\t" \
  "ds_read_b128 v[8:11], v103 offset:51200\n\t" \
  "ds_read_b128 v[12:15], v103 offset:52224\n\t" \
  "s_bitcmp1_b32 s58, 1\n\t" \
  "s_cselect_b64 s[80:81], -1, 0\n\t" \
  "s_cmp_lg_u32 s58, 0\n\t" \
  "s_cbranch_scc1 17f\n\t" \
  "s_mov_b32 exec_hi, 0\n\t" \
  "s_cmp_eq_u32 s95, 0\n\t" \
  "s_cbranch_scc1 16f\n\t" \
  "s_waitcnt vmcnt(2)\n\t" \
  "v_mov_b32 v116, v118\n\t" \
  "s_branch 15f\n\t" \
  "16:\n\t" \
  "s_memtime vcc\n\t" \
  "s_waitcnt lgkmcnt(0)\n\t" \
  "s_sub_i32 s79, s41, vcc_lo\n\t" \
  "s_cmp_gt_i32 s79, 64\n\t" \
  "s_cbranch_scc1 16b\n\t" \
  "13:\n\t" \
  "buffer_load_dword v116, v112, %[rh], s48 offen sc1\n\t" \
  "s_waitcnt vmcnt(0)\n\t" \
  "15:\n\t" \
  "v_mov_b32 v117, 20000\n\t" \
  "v_cmp_ne_u32_sdwa vcc, v116, s49 src0_sel:BYTE_0 src1_sel:DWORD\n\t" \
  "v_subrev_u32 v116, s50, v116\n\t" \
  "v_ashrrev_i32 v116, 8, v116\n\t" \
  "s_nop 1\n\t" \
  "v_max_i32_dpp v116, v116, v116 quad_perm:[1,0,3,2] row_mask:0xf bank_mask:0xf\n\t" \
  "s_cbranch_vccnz 14f\n\t" \
  "s_nop 0\n\t" \
  "v_max_i32_dpp v116, v116, v116 quad_perm:[2,3,0,1] row_mask:0xf bank_mask:0xf\n\t" \
  "s_nop 1\n\t" \
  "v_max_i32_dpp v116, v116, v116 row_half_mirror row_mask:0xf bank_mask:0xf\n\t" \
  "s_nop 1\n\t" \
  "v_max_i32_dpp v116, v116, v116 row_mirror row_mask:0xf bank_mask:0xf\n\t" \
  "v_med3_i32 v116, v116, 0, v117\n\t" \
  "v_mul_u32_u24 v116, s52, v116\n\t" \
  "v_lshrrev_b32 v116, 16, v116\n\t" \
  "s_mov_b64 exec, -1\n\t" \
  "v_readfirstlane_b32 s84, v116\n\t" \
  "v_readlane_b32 s85, v116, 16\n\t" \
  "s_max_u32 s84, s84, s85\n\t" \
  "s_add_i32 s84, s84, s51\n\t" \
  "1:\n\t" \
  "s_memtime vcc\n\t" \
  "s_waitcnt lgkmcnt(0)\n\t" \
  "s_sub_i32 s79, s84, vcc_lo\n\t" \
  "s_cmp_gt_i32 s79, 128\n\t" \
  "s_cbranch_scc1 1b\n\t" \
  ".p2align 4\n\t" \
  "2:\n\t" \
  "s_sub_i32 s79, s79, 33\n\t" \
  "s_cmp_gt_i32 s79, 56\n\t" \
  "s_cbranch_scc1 2b\n\t" \
  "buffer_load_dwordx4 v[32:35], v100, %[rh], s40 offen sc1\n\t" \
  "s_nop 12\n\t" \
  "buffer_load_dwordx4 v[36:39], v100, %[rh], s76 offen sc1\n\t" \
  "s_nop 12\n\t" \
  "buffer_load_dwordx4 v[40:43], v100, %[rh], s77 offen sc1\n\t" \
  "s_nop 12\n\t" \
  "buffer_load_dwordx4 v[44:47], v100, %[rh], s78 offen sc1\n\t" \
  "s_memtime vcc\n\t" \
  "s_memrealtime s[82:83]\n\t" \
  "s_nop 4\n\t" \
  "s_memrealtime s[84:85]\n\t" \
  "s_nop 4\n\t" \
  "s_memrealtime s[56:57]\n\t" \
  "s_nop 4\n\t" \
  "s_memrealtime s[86:87]\n\t" \
  "s_nop 4\n\t" \
  "s_memrealtime s[88:89]\n\t" \
  "s_nop 4\n\t" \
  "s_memrealtime s[90:91]\n\t" \
  "s_mov_b32 s92, 0\n\t" \
  "s_waitcnt lgkmcnt(0)\n\t" \
  "s_cmp_eq_u32 s82, s84\n\t" \
  "s_addc_u32 s92, s92, 0\n\t" \
  "s_cmp_eq_u32 s82, s56\n\t" \
  "s_addc_u32 s92, s92, 0\n\t" \
  "s_cmp_eq_u32 s82, s86\n\t" \
  "s_addc_u32 s92, s92, 0\n\t" \
  "s_cmp_eq_u32 s82, s88\n\t" \
  "s_addc_u32 s92, s92, 0\n\t" \
  "s_cmp_eq_u32 s82, s90\n\t" \
  "s_addc_u32 s92, s92, 0\n\t" \
  "s_mul_i32 s92, s92, 24\n\t" \
  "s_add_i32 s92, s92, vcc_lo\n\t" \
  "s_lshl4_add_u32 s93, s82, 64\n\t" \
  "3:\n\t" \
  "v_mov_b64 v[68:69], 0\n\t" \
  "v_mov_b64 v[70:71], 0\n\t" \
  "v_mov_b64 v[72:73], 0\n\t" \
  "v_mov_b64 v[74:75], 0\n\t" \
  "v_mov_b64 v[76:77], 0\n\t" \
  "v_mov_b64 v[78:79], 0\n\t" \
  "v_mov_b64 v[80:81], 0\n\t" \
  "v_mov_b64 v[82:83], 0\n\t" \
  "s_waitcnt lgkmcnt(0)\n\t" \
  "s_waitcnt vmcnt(3)\n\t" \
  "v_smfmac_f32_16x16x64_bf16 v[68:71], v[32:35], a[96:103], v104\n\t" \
  "ds_write_b128 v101, v[32:35] offset:4096\n\t" \
  "v_smfmac_f32_16x16x64_bf16 v[72:75], v[32:35], a[104:111], v104\n\t" \
  "v_smfmac_f32_16x16x64_bf16 v[76:79], v[32:35], v[48:55], v104\n\t" \
  "v_smfmac_f32_16x16x64_bf16 v[80:83], v[32:35], v[56:63], v104\n\t" \
  "s_waitcnt vmcnt(2)\n\t" \
  "v_smfmac_f32_16x16x64_bf16 v[68:71], v[36:39], a[112:119], v104\n\t" \
  "ds_write_b128 v101, v[36:39] offset:5120\n\t" \
  "v_smfmac_f32_16x16x64_bf16 v[72:75], v[36:39], a[120:127], v104\n\t" \
  "v_smfmac_f32_16x16x64_bf16 v[76:79], v[36:39], a[128:135], v104\n\t" \
  "v_cmp_u_f32 vcc, v68, v69\n\t" \
  "v_smfmac_f32_16x16x64_bf16 v[80:83], v[36:39], a[136:143], v104\n\t" \
  "v_cmp_u_f32 s[82:83], v70, v71\n\t" \
  "s_waitcnt vmcnt(1)\n\t" \
  "v_smfmac_f32_16x16x64_bf16 v[68:71], v[40:43], v[16:23], v104\n\t" \
  "ds_write_b128 v101, v[40:43] offset:6144\n\t" \
  "v_smfmac_f32_16x16x64_bf16 v[72:75], v[40:43], v[24:31], v104\n\t" \
  "s_or_b64 vcc, vcc, s[82:83]\n\t" \
  "s_or_b64 vcc, vcc, s[80:81]\n\t" \
  "s_waitcnt lgkmcnt(1)\n\t" \
  "v_smfmac_f32_16x16x64_bf16 v[76:79], v[40:43], a[144:151], v104\n\t" \
  "s_cbranch_scc1 8f\n\t" \
  "5:\n\t" \
  "s_barrier\n\t" \
  "4:\n\t" \
  "s_memtime s[56:57]\n\t" \
  "ds_read_b128 v[48:51], v101 offset:8192\n\t" \
  "v_smfmac_f32_16x16x64_bf16 v[80:83], v[40:43], a[152:159], v104\n\t" \
  "ds_read_b128 v[52:55], v101 offset:9216\n\t" \
  "ds_read_b128 v[56:59], v101 offset:12288\n\t" \
  "s_waitcnt vmcnt(0)\n\t" \
  "v_smfmac_f32_16x16x64_bf16 v[68:71], v[44:47], a[160:167], v104\n\t" \
  "ds_write_b128 v101, v[44:47] offset:7168\n\t" \
  "v_smfmac_f32_16x16x64_bf16 v[72:75], v[44:47], a[168:175], v104\n\t" \
  "v_smfmac_f32_16x16x64_bf16 v[76:79], v[44:47], a[176:183], v104\n\t" \
  "v_cmp_u_f32 vcc, v68, v69\n\t" \
  "v_smfmac_f32_16x16x64_bf16 v[80:83], v[44:47], a[184:191], v104\n\t" \
  "v_cmp_u_f32 s[82:83], v70, v71\n\t" \
  "s_waitcnt lgkmcnt(3)\n\t" \
  "v_smfmac_f32_16x16x64_bf16 v[68:71], v[48:51], a[192:199], v104\n\t" \
  "ds_read_b128 v[60:63], v101 offset:13312\n\t" \
  "ds_read_b128 v[64:67], v101 offset:0\n\t" \
  "v_smfmac_f32_16x16x64_bf16 v[72:75], v[48:51], a[200:207], v104\n\t" \
  "s_or_b64 vcc, vcc, s[82:83]\n\t" \
  "s_waitcnt lgkmcnt(2)\n\t" \
  "v_smfmac_f32_16x16x64_bf16 v[68:71], v[52:55], a[208:215], v104\n\t" \
  "s_cbranch_scc1 9f\n\t" \
  "6:\n\t" \
  "s_barrier\n\t" \
  "v_smfmac_f32_16x16x64_bf16 v[72:75], v[52:55], a[216:223], v104\n\t" \
  "ds_read_b128 v[48:51], v101 offset:1024\n\t" \
  "v_smfmac_f32_16x16x64_bf16 v[68:71], v[56:59], v[152:159], v104\n\t" \
  "ds_read_b128 v[52:55], v101 offset:10240\n\t" \
  "v_smfmac_f32_16x16x64_bf16 v[72:75], v[56:59], v[160:167], v104\n\t" \
  "s_waitcnt lgkmcnt(3)\n\t" \
  "v_smfmac_f32_16x16x64_bf16 v[68:71], v[60:63], v[168:175], v104\n\t" \
  "ds_read_b128 v[56:59], v101 offset:11264\n\t" \
  "global_load_dword v119, v109, %[kc] sc1\n\t" \
  "v_smfmac_f32_16x16x64_bf16 v[72:75], v[60:63], v[176:183], v104\n\t" \
  "s_waitcnt lgkmcnt(3)\n\t" \
  "v_smfmac_f32_16x16x64_bf16 v[68:71], v[64:67], a[0:7], v104\n\t" \
  "ds_read_b128 v[60:63], v101 offset:14336\n\t" \
  "v_smfmac_f32_16x16x64_bf16 v[72:75], v[64:67], a[8:15], v104\n\t" \
  "s_waitcnt lgkmcnt(3)\n\t" \
  "v_smfmac_f32_16x16x64_bf16 v[68:71], v[48:51], a[16:23], v104\n\t" \
  "ds_read_b128 v[64:67], v101 offset:15360\n\t" \
  "ds_read_b128 v[16:19], v102 offset:16384\n\t" \
  "v_smfmac_f32_16x16x64_bf16 v[72:75], v[48:51], a[24:31], v104\n\t" \
  "s_waitcnt lgkmcnt(4)\n\t" \
  "v_smfmac_f32_16x16x64_bf16 v[68:71], v[52:55], v[84:91], v104\n\t" \
  "ds_read_b128 v[48:51], v101 offset:3072\n\t" \
  "ds_read_b128 v[20:23], v102 offset:17408\n\t" \
  "v_smfmac_f32_16x16x64_bf16 v[72:75], v[52:55], v[92:99], v104\n\t" \
  "s_waitcnt lgkmcnt(5)\n\t" \
  "v_smfmac_f32_16x16x64_bf16 v[68:71], v[56:59], v[120:127], v104\n\t" \
  "ds_read_b128 v[52:55], v101 offset:2048\n\t" \
  "ds_read_b128 v[24:27], v102 offset:18432\n\t" \
  "v_smfmac_f32_16x16x64_bf16 v[72:75], v[56:59], v[128:135], v104\n\t" \
  "ds_read_b128 v[28:31], v102 offset:19456\n\t" \
  "buffer_load_dwordx4 v[84:87], v107, %[ru], s94 offen offset:0 sc1\n\t" \
  "s_waitcnt lgkmcnt(7)\n\t" \
  "v_smfmac_f32_16x16x64_bf16 v[68:71], v[60:63], v[0:7], v104\n\t" \
  "v_smfmac_f32_16x16x64_bf16 v[72:75], v[60:63], v[8:15], v104\n\t" \
  "buffer_load_dwordx4 v[88:91], v107, %[ru], s94 offen offset:1024 sc1\n\t" \
  "s_waitcnt lgkmcnt(6)\n\t" \
  "v_smfmac_f32_16x16x64_bf16 v[68:71], v[64:67], v[216:223], v104\n\t" \
  "ds_read_b128 v[0:3], v103 offset:0\n\t" \
  "v_smfmac_f32_16x16x64_bf16 v[72:75], v[64:67], v[224:231], v104\n\t" \
  "ds_read_b128 v[4:7], v103 offset:1024\n\t" \
  "buffer_load_dwordx4 v[92:95], v107, %[ru], s94 offen offset:2048 sc1\n\t" \
  "s_waitcnt lgkmcnt(6)\n\t" \
  "v_smfmac_f32_16x16x64_bf16 v[68:71], v[48:51], a[64:71], v104\n\t" \
  "s_waitcnt lgkmcnt(4)\n\t" \
  "v_smfmac_f32_16x16x64_bf16 v[68:71], v[52:55], v[16:23], v104\n\t" \
  "ds_read_b128 v[8:11], v103 offset:2048\n\t" \
  "buffer_load_dwordx4 v[96:99], v107, %[ru], s94 offen offset:3072 sc1\n\t" \
  "v_smfmac_f32_16x16x64_bf16 v[72:75], v[48:51], a[72:79], v104\n\t" \
  "ds_read_b128 v[12:15], v103 offset:3072\n\t" \
  "s_waitcnt lgkmcnt(4)\n\t" \
  "v_smfmac_f32_16x16x64_bf16 v[72:75], v[52:55], v[24:31], v104\n\t" \
  "v_add_f32 v44, v68, v70\n\t" \
  "v_smfmac_f32_16x16x64_bf16 v[76:79], v[56:59], v[136:143], v104\n\t" \
  "v_add_f32 v45, v69, v71\n\t" \
  "v_cvt_pk_bf16_f32 v44, v44, v45\n\t" \
  "v_smfmac_f32_16x16x64_bf16 v[80:83], v[56:59], v[144:151], v104\n\t" \
  "v_add_f32 v46, v72, v74\n\t" \
  "v_add_f32 v47, v73, v75\n\t" \
  "v_smfmac_f32_16x16x64_bf16 v[76:79], v[60:63], v[200:207], v104\n\t" \
  "v_cvt_pk_bf16_f32 v46, v46, v47\n\t" \
  "ds_write2st64_b32 v105, v44, v46 offset1:1\n\t" \
  "v_smfmac_f32_16x16x64_bf16 v[80:83], v[60:63], v[208:215], v104\n\t" \
  "ds_read_b128 v[56:59], v101 offset:8192\n\t" \
  "v_smfmac_f32_16x16x64_bf16 v[76:79], v[64:67], v[232:239], v104\n\t" \
  "ds_read_b128 v[16:19], v103 offset:32768\n\t" \
  "v_smfmac_f32_16x16x64_bf16 v[80:83], v[64:67], v[240:247], v104\n\t" \
  "ds_read_b128 v[20:23], v103 offset:33792\n\t" \
  "v_smfmac_f32_16x16x64_bf16 v[76:79], v[48:51], a[80:87], v104\n\t" \
  "s_waitcnt lgkmcnt(3)\n\t" \
  "s_barrier\n\t" \
  "v_smfmac_f32_16x16x64_bf16 v[80:83], v[48:51], a[88:95], v104\n\t" \
  "v_smfmac_f32_16x16x64_bf16 v[76:79], v[52:55], a[48:55], v104\n\t" \
  "ds_read_b128 v[60:63], v101 offset:1024\n\t" \
  "ds_read_b128 v[24:27], v103 offset:34816\n\t" \
  "v_smfmac_f32_16x16x64_bf16 v[80:83], v[52:55], a[56:63], v104\n\t" \
  "ds_read_b128 v[64:67], v101 offset:10240\n\t" \
  "s_waitcnt lgkmcnt(5)\n\t" \
  "v_smfmac_f32_16x16x64_bf16 v[76:79], v[56:59], v[0:7], v104\n\t" \
  "ds_read_b128 v[48:51], v101 offset:12288\n\t" \
  "ds_read_b128 v[28:31], v103 offset:35840\n\t" \
  "v_smfmac_f32_16x16x64_bf16 v[80:83], v[56:59], v[8:15], v104\n\t" \
  "buffer_load_dwordx4 v[32:35], v100, %[ru], s47 offen offset:0 sc1\n\t" \
  "s_waitcnt lgkmcnt(4)\n\t" \
  "v_smfmac_f32_16x16x64_bf16 v[76:79], v[60:63], a[32:39], v104\n\t" \
  "ds_read_b128 v[52:55], v101 offset:9216\n\t" \
  "v_smfmac_f32_16x16x64_bf16 v[80:83], v[60:63], a[40:47], v104\n\t" \
  "ds_read_b128 v[0:3], v102 offset:0\n\t" \
  "s_waitcnt lgkmcnt(4)\n\t" \
  "v_smfmac_f32_16x16x64_bf16 v[76:79], v[64:67], a[240:247], v104\n\t" \
  "ds_read_b128 v[56:59], v101 offset:13312\n\t" \
  "buffer_load_dwordx4 v[36:39], v100, %[ru], s47 offen offset:1024 sc1\n\t" \
  "v_smfmac_f32_16x16x64_bf16 v[80:83], v[64:67], a[248:255], v104\n\t" \
  "ds_read_b128 v[60:63], v101 offset:0\n\t" \
  "ds_read_b128 v[4:7], v102 offset:1024\n\t" \
  "s_waitcnt lgkmcnt(6)\n\t" \
  "v_smfmac_f32_16x16x64_bf16 v[76:79], v[48:51], v[16:23], v104\n\t" \
  "ds_read_b128 v[8:11], v102 offset:2048\n\t" \
  "s_waitcnt lgkmcnt(6)\n\t" \
  "v_smfmac_f32_16x16x64_bf16 v[80:83], v[48:51], v[24:31], v104\n\t" \
  "ds_read_b128 v[12:15], v102 offset:3072\n\t" \
  "buffer_load_dwordx4 v[40:43], v100, %[ru], s47 offen offset:2048 sc1\n\t" \
  "s_waitcnt lgkmcnt(6)\n\t" \
  "v_smfmac_f32_16x16x64_bf16 v[76:79], v[52:55], a[224:231], v104\n\t" \
  "v_smfmac_f32_16x16x64_bf16 v[80:83], v[52:55], a[232:239], v104\n\t" \
  "s_waitcnt lgkmcnt(4)\n\t" \
  "v_smfmac_f32_16x16x64_bf16 v[76:79], v[56:59], v[184:191], v104\n\t" \
  "s_waitcnt lgkmcnt(2)\n\t" \
  "v_smfmac_f32_16x16x64_bf16 v[76:79], v[60:63], v[0:7], v104\n\t" \
  "v_smfmac_f32_16x16x64_bf16 v[80:83], v[56:59], v[192:199], v104\n\t" \
  "s_waitcnt lgkmcnt(0)\n\t" \
  "v_smfmac_f32_16x16x64_bf16 v[80:83], v[60:63], v[8:15], v104\n\t" \
  "v_add_f32 v20, v76, v78\n\t" \
  "v_add_f32 v21, v77, v79\n\t" \
  "v_cvt_pk_bf16_f32 v20, v20, v21\n\t" \
  "ds_write_b32 v106, v20\n\t" \
  "s_nop 3\n\t" \
  "v_add_f32 v23, v80, v82\n\t" \
  "v_add_f32 v21, v81, v83\n\t" \
  "v_cvt_pk_bf16_f32 v21, v23, v21\n\t" \
  "ds_write_b32 v106, v21 offset:256\n\t" \
  "s_waitcnt lgkmcnt(0)\n\t" \
  "s_barrier\n\t" \
  "v_bfe_i32 v64, v114, 0, 1\n\t" \
  "v_and_b32 v64, 0xff800000, v64\n\t" \
  "v_bfe_i32 v65, v114, 1, 1\n\t" \
  "v_and_b32 v65, 0xff800000, v65\n\t" \
  "v_bfe_i32 v66, v114, 2, 1\n\t" \
  "v_and_b32 v66, 0xff800000, v66\n\t" \
  "v_bfe_i32 v67, v114, 3, 1\n\t" \
  "v_and_b32 v67, 0xff800000, v67\n\t" \
  "v_bfe_i32 v68, v114, 4, 1\n\t" \
  "v_and_b32 v68, 0xff800000, v68\n\t" \
  "v_bfe_i32 v69, v114, 5, 1\n\t" \
  "v_and_b32 v69, 0xff800000, v69\n\t" \
  "v_bfe_i32 v70, v114, 6, 1\n\t" \
  "v_and_b32 v70, 0xff800000, v70\n\t" \
  "v_bfe_i32 v71, v114, 7, 1\n\t" \
  "v_and_b32 v71, 0xff800000, v71\n\t" \
  "s_lshl_b32 s94, s45, 4\n\t" \
  "s_and_b32 s96, s44, 2\n\t" \
  "s_lshl_b32 s96, s96, 13\n\t" \
  "s_and_b32 s76, s44, 1\n\t" \
  "s_lshl_b32 s76, s76, 12\n\t" \
  "s_add_i32 s96, s96, s76\n\t" \
  "s_mov_b32 s95, 1\n\t" \
  "s_sleep 1\n\t" \
  "ds_read_b128 v[0:3], v101 offset:20480\n\t" \
  "ds_read_b128 v[4:7], v101 offset:21504\n\t" \
  "ds_read_b128 v[8:11], v101 offset:22528\n\t" \
  "ds_read_b128 v[12:15], v101 offset:23552\n\t" \
  "ds_read_b128 v[16:19], v101 offset:24576\n\t" \
  "ds_read_b128 v[20:23], v101 offset:25600\n\t" \
  "ds_read_b128 v[24:27], v101 offset:26624\n\t" \
  "ds_read_b128 v[28:31], v101 offset:27648\n\t" \
  "ds_read_b64_tr_b16 v[48:49], v108 offset:0\n\t" \
  "ds_read_b64_tr_b16 v[50:51], v108 offset:64\n\t" \
  "ds_read_b64_tr_b16 v[52:53], v108 offset:512\n\t" \
  "ds_read_b64_tr_b16 v[54:55], v108 offset:576\n\t" \
  "ds_read_b64_tr_b16 v[56:57], v108 offset:1024\n\t" \
  "ds_read_b64_tr_b16 v[58:59], v108 offset:1088\n\t" \
  "ds_read_b64_tr_b16 v[60:61], v108 offset:1536\n\t" \
  "ds_read_b64_tr_b16 v[62:63], v108 offset:1600\n\t" \
  "s_waitcnt vmcnt(3) lgkmcnt(8)\n\t" \
  "v_mfma_f32_16x16x32_bf16 v[64:67], v[0:3], v[84:87], v[64:67]\n\t" \
  "v_mfma_f32_16x16x32_bf16 v[68:71], v[16:19], v[84:87], v[68:71]\n\t" \
  "v_mfma_f32_16x16x32_bf16 v[64:67], v[4:7], v[88:91], v[64:67]\n\t" \
  "v_mfma_f32_16x16x32_bf16 v[68:71], v[20:23], v[88:91], v[68:71]\n\t" \
  "v_mfma_f32_16x16x32_bf16 v[64:67], v[8:11], v[92:95], v[64:67]\n\t" \
  "v_mfma_f32_16x16x32_bf16 v[68:71], v[24:27], v[92:95], v[68:71]\n\t" \
  "v_mfma_f32_16x16x32_bf16 v[64:67], v[12:15], v[96:99], v[64:67]\n\t" \
  "v_mfma_f32_16x16x32_bf16 v[68:71], v[28:31], v[96:99], v[68:71]\n\t" \
  "s_waitcnt lgkmcnt(0)\n\t" \
  "v_mfma_f32_16x16x32_bf16 v[72:75], v[48:51], v[84:87], 0\n\t" \
  "v_mfma_f32_16x16x32_bf16 v[72:75], v[52:55], v[88:91], v[72:75]\n\t" \
  "v_mfma_f32_16x16x32_bf16 v[72:75], v[56:59], v[92:95], v[72:75]\n\t" \
  "v_mfma_f32_16x16x32_bf16 v[72:75], v[60:63], v[96:99], v[72:75]\n\t" \
  "s_barrier\n\t" \
  "v_max3_f32 v76, v64, v65, v66\n\t" \
  "v_max3_f32 v77, v67, v68, v69\n\t" \
  "v_max3_f32 v76, v76, v70, v71\n\t" \
  "s_nop 4\n\t" \
  "v_max3_f32 v77, v77, v72, v73\n\t" \
  "v_max3_f32 v76, v76, v74, v75\n\t" \
  "v_max_f32 v76, v76, v77\n\t" \
  "v_mov_b32 v77, v76\n\t" \
  "s_nop 1\n\t" \
  "v_permlane16_swap_b32 v76, v77\n\t" \
  "v_max_f32 v76, v76, v77\n\t" \
  "v_mov_b32 v77, v76\n\t" \
  "s_nop 1\n\t" \
  "v_permlane32_swap_b32 v76, v77\n\t" \
  "v_max_f32 v76, v76, v77\n\t" \
  "v_mul_f32 v76, %[cs], v76\n\t" \
  "v_fma_f32 v64, v64, %[cs], -v76\n\t" \
  "v_fma_f32 v65, v65, %[cs], -v76\n\t" \
  "v_fma_f32 v66, v66, %[cs], -v76\n\t" \
  "v_fma_f32 v67, v67, %[cs], -v76\n\t" \
  "v_fma_f32 v68, v68, %[cs], -v76\n\t" \
  "v_fma_f32 v69, v69, %[cs], -v76\n\t" \
  "v_fma_f32 v70, v70, %[cs], -v76\n\t" \
  "v_fma_f32 v71, v71, %[cs], -v76\n\t" \
  "v_fma_f32 v72, v72, %[cs], -v76\n\t" \
  "v_fma_f32 v73, v73, %[cs], -v76\n\t" \
  "v_fma_f32 v74, v74, %[cs], -v76\n\t" \
  "v_fma_f32 v75, v75, %[cs], -v76\n\t" \
  "v_exp_f32 v64, v64\n\t" \
  "v_exp_f32 v65, v65\n\t" \
  "v_exp_f32 v66, v66\n\t" \
  "v_exp_f32 v67, v67\n\t" \
  "v_exp_f32 v68, v68\n\t" \
  "v_exp_f32 v69, v69\n\t" \
  "v_exp_f32 v70, v70\n\t" \
  "v_exp_f32 v71, v71\n\t" \
  "v_exp_f32 v72, v72\n\t" \
  "v_exp_f32 v73, v73\n\t" \
  "v_exp_f32 v74, v74\n\t" \
  "v_exp_f32 v75, v75\n\t" \
  "v_add_f32 v76, v72, v73\n\t" \
  "v_add_f32 v77, v74, v75\n\t" \
  "v_add_f32 v78, v64, v65\n\t" \
  "v_add_f32 v79, v66, v67\n\t" \
  "v_add_f32 v76, v76, v77\n\t" \
  "v_add_f32 v78, v78, v68\n\t" \
  "v_add_f32 v79, v79, v69\n\t" \
  "v_add_f32 v78, v78, v70\n\t" \
  "v_add_f32 v79, v79, v71\n\t" \
  "v_fma_f32 v76, v76, 0.5, v78\n\t" \
  "v_add_f32 v76, v76, v79\n\t" \
  "v_mov_b32 v77, v76\n\t" \
  "s_nop 1\n\t" \
  "v_permlane16_swap_b32 v76, v77\n\t" \
  "v_add_f32 v76, v76, v77\n\t" \
  "v_mov_b32 v77, v76\n\t" \
  "s_nop 1\n\t" \
  "v_permlane32_swap_b32 v76, v77\n\t" \
  "v_add_f32 v76, v76, v77\n\t" \
  "v_rcp_f32 v76, v76\n\t" \
  "s_nop 0\n\t" \
  "v_mul_f32 v64, v64, v76\n\t" \
  "v_mul_f32 v65, v65, v76\n\t" \
  "v_mul_f32 v66, v66, v76\n\t" \
  "v_mul_f32 v67, v67, v76\n\t" \
  "v_mul_f32 v68, v68, v76\n\t" \
  "v_mul_f32 v69, v69, v76\n\t" \
  "v_mul_f32 v70, v70, v76\n\t" \
  "v_mul_f32 v71, v71, v76\n\t" \
  "v_mul_f32 v72, v72, v76\n\t" \
  "v_mul_f32 v73, v73, v76\n\t" \
  "v_mul_f32 v74, v74, v76\n\t" \
  "v_mul_f32 v75, v75, v76\n\t" \
  "v_cvt_pk_bf16_f32 v0, v64, v68\n\t" \
  "v_cvt_pk_bf16_f32 v1, v65, v69\n\t" \
  "v_cvt_pk_bf16_f32 v2, v66, v70\n\t" \
  "v_cvt_pk_bf16_f32 v3, v67, v71\n\t" \
  "v_cvt_pk_bf16_f32 v4, v72, v73\n\t" \
  "v_cvt_pk_bf16_f32 v5, v74, v75\n\t" \
  "v_permlane32_swap_b32 v0, v2\n\t" \
  "v_permlane32_swap_b32 v1, v3\n\t" \
  "v_mov_b64 v[6:7], v[4:5]\n\t" \
  "s_mov_b32 exec_hi, 0\n\t" \
  "buffer_load_dword v118, v112, %[rh], s96 offen sc1\n\t" \
  "s_mov_b64 exec, -1\n\t" \
  "buffer_store_dwordx4 v[0:3], v110, %[rh], s94 offen sc1\n\t" \
  "v_permlane16_swap_b32 v4, v6\n\t" \
  "v_permlane16_swap_b32 v5, v7\n\t" \
  "buffer_store_dwordx4 v[4:7], v111, %[rh], s94 offen sc1\n\t" \
  "s_waitcnt vmcnt(3)\n\t" \
  "ds_write_b128 v101, v[32:35] offset:20480\n\t" \
  "ds_write_b128 v101, v[36:39] offset:21504\n\t" \
  "ds_write_b128 v101, v[40:43] offset:22528\n\t" \
  "s_add_i32 s77, s44, 2\n\t" \
  "s_min_u32 s77, s77, 63\n\t" \
  "v_cmp_gt_i32 vcc, s77, v119\n\t" \
  "s_cmp_lg_u64 vcc, 0\n\t" \
  "s_cselect_b32 s42, 32, 0\n\t" \
  "s_cmp_eq_u32 s44, s43\n\t" \
  "s_cselect_b32 s42, 32, s42\n\t" \
  "s_add_i32 s41, s56, 1950\n\t" \
  "s_sub_i32 s76, s92, s51\n\t" \
  "s_sub_i32 s77, s93, s53\n\t" \
  "s_cmp_eq_u32 s76, 0\n\t" \
  "s_cbranch_scc1 21f\n\t" \
  "v_cvt_f32_u32 v117, s76\n\t" \
  "v_cvt_f32_u32 v116, s77\n\t" \
  "v_rcp_f32 v117, v117\n\t" \
  "s_mov_b32 s51, s92\n\t" \
  "v_mul_f32 v117, v117, v116\n\t" \
  "v_max_f32 v117, 0x3f028f5c, v117\n\t" \
  "v_min_f32 v117, 0x3ffeb852, v117\n\t" \
  "v_rcp_f32 v116, v117\n\t" \
  "v_mul_f32 v117, 0x4f000000, v117\n\t" \
  "v_mul_f32 v116, 0x47800000, v116\n\t" \
  "v_cvt_u32_f32 v117, v117\n\t" \
  "v_cvt_u32_f32 v116, v116\n\t" \
  "s_mov_b32 s53, s93\n\t" \
  "v_readfirstlane_b32 s46, v117\n\t" \
  "v_readfirstlane_b32 s52, v116\n\t" \
  "21:\n\t" \
  "s_add_i32 s44, s44, 1\n\t" \
  "s_and_b32 s58, s58, 4\n\t" \
  "s_and_b32 s76, s44, 64\n\t" \
  "s_or_b32 s42, s42, s76\n\t" \
  "s_cbranch_scc0 20b\n\t" \
  "s_branch 99f\n\t" \
  "14:\n\t" \
  "s_sleep 3\n\t" \
  "s_add_u32 s59, s59, 0x10000\n\t" \
  "s_cmp_lt_u32 s59, 0x100000\n\t" \
  "s_cbranch_scc1 13b\n\t" \
  "s_mov_b64 exec, -1\n\t" \
  "s_mov_b32 s42, 16\n\t" \
  "s_branch 99f\n\t" \
  "8:\n\t" \
  "s_bitcmp1_b32 s58, 1\n\t" \
  "s_cbranch_scc1 4b\n\t" \
  "s_bitcmp1_b32 s58, 2\n\t" \
  "s_cbranch_scc1 5b\n\t" \
  "v_max3_u32 v116, v32, v33, v34\n\t" \
  "v_max3_u32 v117, v35, v36, v37\n\t" \
  "v_max3_u32 v116, v116, v38, v39\n\t" \
  "v_max_u32 v116, v116, v117\n\t" \
  "v_cmp_eq_u32 vcc, -1, v116\n\t" \
  "s_cbranch_vccz 5b\n\t" \
  "s_branch 0f\n\t" \
  "9:\n\t" \
  "s_bitcmp1_b32 s58, 2\n\t" \
  "s_cbranch_scc1 6b\n\t" \
  "v_max3_u32 v116, v40, v41, v42\n\t" \
  "v_max3_u32 v117, v43, v44, v45\n\t" \
  "v_max3_u32 v116, v116, v46, v47\n\t" \
  "v_max_u32 v116, v116, v117\n\t" \
  "v_cmp_eq_u32 vcc, -1, v116\n\t" \
  "s_cbranch_vccz 6b\n\t" \
  "s_or_b32 s58, s58, 2\n\t" \
  "0:\n\t" \
  "s_or_b32 s58, s58, 1\n\t" \
  "11:\n\t" \
  "buffer_load_dwordx4 v[32:35], v100, %[rh], s40 offen sc1\n\t" \
  "buffer_load_dwordx4 v[36:39], v100, %[rh], s76 offen sc1\n\t" \
  "buffer_load_dwordx4 v[40:43], v100, %[rh], s77 offen sc1\n\t" \
  "buffer_load_dwordx4 v[44:47], v100, %[rh], s78 offen sc1\n\t" \
  "s_waitcnt lgkmcnt(0)\n\t" \
  "s_branch 10b\n\t" \
  "17:\n\t" \
  "s_add_u32 s59, s59, 1\n\t" \
  "s_bitcmp1_b32 s58, 2\n\t" \
  "s_cbranch_scc1 3b\n\t" \
  "s_waitcnt vmcnt(0)\n\t" \
  "v_max_u32 v116, v32, v33\n\t" \
  "v_max3_u32 v116, v116, v34, v35\n\t" \
  "v_max3_u32 v116, v116, v36, v37\n\t" \
  "v_max3_u32 v116, v116, v38, v39\n\t" \
  "v_max3_u32 v116, v116, v40, v41\n\t" \
  "v_max3_u32 v116, v116, v42, v43\n\t" \
  "v_max3_u32 v116, v116, v44, v45\n\t" \
  "v_max3_u32 v116, v116, v46, v47\n\t" \
  "v_cmp_eq_u32 vcc, -1, v116\n\t" \
  "s_cbranch_vccz 3b\n\t" \
  "s_bitcmp1_b32 s59, 6\n\t" \
  "s_cbranch_scc1 12f\n\t" \
  "buffer_load_dwordx4 v[32:35], v100, %[rh], s40 offen sc1\n\t" \
  "buffer_load_dwordx4 v[36:39], v100, %[rh], s76 offen sc1\n\t" \
  "buffer_load_dwordx4 v[40:43], v100, %[rh], s77 offen sc1\n\t" \
  "buffer_load_dwordx4 v[44:47], v100, %[rh], s78 offen sc1\n\t" \
  "s_branch 17b\n\t" \
  "12:\n\t" \
  "s_mov_b32 s42, s58\n\t" \
  "s_branch 99f\n\t" \
  "92:\n\t" \
  "s_mov_b32 s58, s42\n\t" \
  "s_mov_b32 s95, 0\n\t" \
  "20:\n\t" \
  "s_and_b32 s76, s44, 7\n\t" \
  "s_lshl_b32 s76, s76, 15\n\t" \
  "s_add_i32 s40, s54, s76\n\t" \
  "s_and_b32 s76, s44, 3\n\t" \
  "s_lshl_b32 s45, s76, 14\n\t" \
  "s_add_i32 s47, s44, 1\n\t" \
  "s_min_u32 s47, s47, 63\n\t" \
  "s_lshr_b32 s76, s47, 1\n\t" \
  "s_lshl_b32 s47, s47, 20\n\t" \
  "s_add_i32 s47, s47, s55\n\t" \
  "s_bitcmp1_b64 %[sel], s76\n\t" \
  "s_cselect_b32 s76, 0x2000, 0\n\t" \
  "s_add_i32 s47, s47, s76\n\t" \
  "s_lshr_b32 s76, s44, 1\n\t" \
  "s_lshl_b32 s94, s44, 20\n\t" \
  "s_bitcmp1_b64 %[sel], s76\n\t" \
  "s_cselect_b32 s76, 0x2000, 0\n\t" \
  "s_add_i32 s94, s94, s76\n\t" \
  "s_add_i32 s49, s44, -1\n\t" \
  "s_and_b32 s48, s49, 2\n\t" \
  "s_lshl_b32 s48, s48, 13\n\t" \
  "s_and_b32 s76, s49, 1\n\t" \
  "s_lshl_b32 s76, s76, 12\n\t" \
  "s_add_i32 s48, s48, s76\n\t" \
  "s_lshl_b32 s50, s53, 8\n\t" \
  "s_mov_b32 s59, 0\n\t" \
  "s_mov_b32 s92, s51\n\t" \
  "s_mov_b32 s93, s53\n\t" \
  "s_xor_b32 s76, s40, 0x400\n\t" \
  "s_xor_b32 s77, s40, 0x800\n\t" \
  "s_xor_b32 s78, s40, 0xc00\n\t" \
  "s_cmp_lg_u32 s58, 0\n\t" \
  "s_cbranch_scc1 11f\n\t" \
  "10:\n\t" \
  "ds_read_b128 v[48:51], v103 offset:0\n\t" \
  "ds_read_b128 v[52:55], v103 offset:1024\n\t" \
  "ds_read_b128 v[56:59], v103 offset:2048\n\t" \
  "ds_read_b128 v[60:63], v103 offset:3072\n\t" \
  "ds_read_b128 v[16:19], v103 offset:16384\n\t" \
  "ds_read_b128 v[20:23], v103 offset:17408\n\t" \
  "ds_read_b128 v[24:27], v103 offset:18432\n\t" \
  "ds_read_b128 v[28:31], v103 offset:19456\n\t" \
  "ds_read_b128 v[84:87], v103 offset:49152\n\t" \
  "ds_read_b128 v[88:91], v103 offset:50176\n\t" \
  "ds_read_b128 v[92:95], v103 offset:51200\n\t" \
  "ds_read_b128 v[96:99], v103 offset:52224\n\t" \
  "s_waitcnt lgkmcnt(4)\n\t" \
  "ds_read_b128 v[0:3], v102 offset:16384\n\t" \
  "ds_read_b128 v[4:7], v102 offset:17408\n\t" \
  "ds_read_b128 v[8:11], v102 offset:18432\n\t" \
  "ds_read_b128 v[12:15], v102 offset:19456\n\t" \
  "s_bitcmp1_b32 s58, 1\n\t" \
  "s_cselect_b64 s[80:81], -1, 0\n\t" \
  "s_cmp_lg_u32 s58, 0\n\t" \
  "s_cbranch_scc1 17f\n\t" \
  "s_mov_b32 exec_hi, 0\n\t" \
  "s_cmp_eq_u32 s95, 0\n\t" \
  "s_cbranch_scc1 16f\n\t" \
  "s_waitcnt vmcnt(2)\n\t" \
  "v_mov_b32 v116, v118\n\t" \
  "s_branch 15f\n\t" \
  "16:\n\t" \
  "s_memtime vcc\n\t" \
  "s_waitcnt lgkmcnt(0)\n\t" \
  "s_sub_i32 s79, s41, vcc_lo\n\t" \
  "s_cmp_gt_i32 s79, 64\n\t" \
  "s_cbranch_scc1 16b\n\t" \
  "13:\n\t" \
  "buffer_load_dword v116, v112, %[rh], s48 offen sc1\n\t" \
  "s_waitcnt vmcnt(0)\n\t" \
  "15:\n\t" \
  "v_mov_b32 v117, 20000\n\t" \
  "v_cmp_ne_u32_sdwa vcc, v116, s49 src0_sel:BYTE_0 src1_sel:DWORD\n\t" \
  "v_subrev_u32 v116, s50, v116\n\t" \
  "v_ashrrev_i32 v116, 8, v116\n\t" \
  "s_nop 1\n\t" \
  "v_max_i32_dpp v116, v116, v116 quad_perm:[1,0,3,2] row_mask:0xf bank_mask:0xf\n\t" \
  "s_cbranch_vccnz 14f\n\t" \
  "s_nop 0\n\t" \
  "v_max_i32_dpp v116, v116, v116 quad_perm:[2,3,0,1] row_mask:0xf bank_mask:0xf\n\t" \
  "s_nop 1\n\t" \
  "v_max_i32_dpp v116, v116, v116 row_half_mirror row_mask:0xf bank_mask:0xf\n\t" \
  "s_nop 1\n\t" \
  "v_max_i32_dpp v116, v116, v116 row_mirror row_mask:0xf bank_mask:0xf\n\t" \
  "v_med3_i32 v116, v116, 0, v117\n\t" \
  "v_mul_u32_u24 v116, s52, v116\n\t" \
  "v_lshrrev_b32 v116, 16, v116\n\t" \
  "s_mov_b64 exec, -1\n\t" \
  "v_readfirstlane_b32 s84, v116\n\t" \
  "v_readlane_b32 s85, v116, 16\n\t" \
  "s_max_u32 s84, s84, s85\n\t" \
  "s_add_i32 s84, s84, s51\n\t" \
  "1:\n\t" \
  "s_memtime vcc\n\t" \
  "s_waitcnt lgkmcnt(0)\n\t" \
  "s_sub_i32 s79, s84, vcc_lo\n\t" \
  "s_cmp_gt_i32 s79, 128\n\t" \
  "s_cbranch_scc1 1b\n\t" \
  ".p2align 4\n\t" \
  "2:\n\t" \
  "s_sub_i32 s79, s79, 33\n\t" \
  "s_cmp_gt_i32 s79, 56\n\t" \
  "s_cbranch_scc1 2b\n\t" \
  "buffer_load_dwordx4 v[32:35], v100, %[rh], s40 offen sc1\n\t" \
  "s_nop 12\n\t" \
  "buffer_load_dwordx4 v[36:39], v100, %[rh], s76 offen sc1\n\t" \
  "s_nop 12\n\t" \
  "buffer_load_dwordx4 v[40:43], v100, %[rh], s77 offen sc1\n\t" \
  "s_nop 12\n\t" \
  "buffer_load_dwordx4 v[44:47], v100, %[rh], s78 offen sc1\n\t" \
  "s_memtime vcc\n\t" \
  "s_memrealtime s[82:83]\n\t" \
  "s_nop 4\n\t" \
  "s_memrealtime s[84:85]\n\t" \
  "s_nop 4\n\t" \
  "s_memrealtime s[56:57]\n\t" \
  "s_nop 4\n\t" \
  "s_memrealtime s[86:87]\n\t" \
  "s_nop 4\n\t" \
  "s_memrealtime s[88:89]\n\t" \
  "s_nop 4\n\t" \
  "s_memrealtime s[90:91]\n\t" \
  "s_mov_b32 s92, 0\n\t" \
  "s_waitcnt lgkmcnt(0)\n\t" \
  "s_cmp_eq_u32 s82, s84\n\t" \
  "s_addc_u32 s92, s92, 0\n\t" \
  "s_cmp_eq_u32 s82, s56\n\t" \
  "s_addc_u32 s92, s92, 0\n\t" \
  "s_cmp_eq_u32 s82, s86\n\t" \
  "s_addc_u32 s92, s92, 0\n\t" \
  "s_cmp_eq_u32 s82, s88\n\t" \
  "s_addc_u32 s92, s92, 0\n\t" \
  "s_cmp_eq_u32 s82, s90\n\t" \
  "s_addc_u32 s92, s92, 0\n\t" \
  "s_mul_i32 s92, s92, 24\n\t" \
  "s_add_i32 s92, s92, vcc_lo\n\t" \
  "s_lshl4_add_u32 s93, s82, 64\n\t" \
  "3:\n\t" \
  "v_mov_b64 v[68:69], 0\n\t" \
  "v_mov_b64 v[70:71], 0\n\t" \
  "v_mov_b64 v[72:73], 0\n\t" \
  "v_mov_b64 v[74:75], 0\n\t" \
  "v_mov_b64 v[76:77], 0\n\t" \
  "v_mov_b64 v[78:79], 0\n\t" \
  "v_mov_b64 v[80:81], 0\n\t" \
  "v_mov_b64 v[82:83], 0\n\t" \
  "s_waitcnt lgkmcnt(0)\n\t" \
  "s_waitcnt vmcnt(3)\n\t" \
  "v_smfmac_f32_16x16x64_bf16 v[68:71], v[32:35], a[192:199], v104\n\t" \
  "ds_write_b128 v101, v[32:35] offset:8192\n\t" \
  "v_smfmac_f32_16x16x64_bf16 v[72:75], v[32:35], a[200:207], v104\n\t" \
  "v_smfmac_f32_16x16x64_bf16 v[76:79], v[32:35], v[48:55], v104\n\t" \
  "v_smfmac_f32_16x16x64_bf16 v[80:83], v[32:35], v[56:63], v104\n\t" \
  "s_waitcnt vmcnt(2)\n\t" \
  "v_smfmac_f32_16x16x64_bf16 v[68:71], v[36:39], a[208:215], v104\n\t" \
  "ds_write_b128 v101, v[36:39] offset:9216\n\t" \
  "v_smfmac_f32_16x16x64_bf16 v[72:75], v[36:39], a[216:223], v104\n\t" \
  "v_smfmac_f32_16x16x64_bf16 v[76:79], v[36:39], a[224:231], v104\n\t" \
  "v_cmp_u_f32 vcc, v68, v69\n\t" \
  "v_smfmac_f32_16x16x64_bf16 v[80:83], v[36:39], a[232:239], v104\n\t" \
  "v_cmp_u_f32 s[82:83], v70, v71\n\t" \
  "s_waitcnt vmcnt(1)\n\t" \
  "v_smfmac_f32_16x16x64_bf16 v[68:71], v[40:43], v[16:23], v104\n\t" \
  "ds_write_b128 v101, v[40:43] offset:10240\n\t" \
  "v_smfmac_f32_16x16x64_bf16 v[72:75], v[40:43], v[24:31], v104\n\t" \
  "s_or_b64 vcc, vcc, s[82:83]\n\t" \
  "s_or_b64 vcc, vcc, s[80:81]\n\t" \
  "s_waitcnt lgkmcnt(1)\n\t" \
  "v_smfmac_f32_16x16x64_bf16 v[76:79], v[40:43], a[240:247], v104\n\t" \
  "s_cbranch_scc1 8f\n\t" \
  "5:\n\t" \
  "s_barrier\n\t" \
  "4:\n\t" \
  "s_memtime s[56:57]\n\t" \
  "ds_read_b128 v[48:51], v101 offset:12288\n\t" \
  "v_smfmac_f32_16x16x64_bf16 v[80:83], v[40:43], a[248:255], v104\n\t" \
  "ds_read_b128 v[52:55], v101 offset:13312\n\t" \
  "ds_read_b128 v[56:59], v101 offset:0\n\t" \
  "s_waitcnt vmcnt(0)\n\t" \
  "v_smfmac_f32_16x16x64_bf16 v[68:71], v[44:47], v[120:127], v104\n\t" \
  "v_smfmac_f32_16x16x64_bf16 v[72:75], v[44:47], v[128:135], v104\n\t" \
  "ds_write_b128 v101, v[44:47] offset:11264\n\t" \
  "v_smfmac_f32_16x16x64_bf16 v[76:79], v[44:47], v[136:143], v104\n\t" \
  "v_cmp_u_f32 vcc, v68, v69\n\t" \
  "v_smfmac_f32_16x16x64_bf16 v[80:83], v[44:47], v[144:151], v104\n\t" \
  "v_cmp_u_f32 s[82:83], v70, v71\n\t" \
  "s_waitcnt lgkmcnt(3)\n\t" \
  "v_smfmac_f32_16x16x64_bf16 v[68:71], v[48:51], v[152:159], v104\n\t" \
  "ds_read_b128 v[60:63], v101 offset:1024\n\t" \
  "ds_read_b128 v[64:67], v101 offset:4096\n\t" \
  "v_smfmac_f32_16x16x64_bf16 v[72:75], v[48:51], v[160:167], v104\n\t" \
  "s_or_b64 vcc, vcc, s[82:83]\n\t" \
  "s_waitcnt lgkmcnt(2)\n\t" \
  "v_smfmac_f32_16x16x64_bf16 v[68:71], v[52:55], v[168:175], v104\n\t" \
  "s_cbranch_scc1 9f\n\t" \
  "6:\n\t" \
  "s_barrier\n\t" \
  "v_smfmac_f32_16x16x64_bf16 v[72:75], v[52:55], v[176:183], v104\n\t" \
  "ds_read_b128 v[48:51], v101 offset:5120\n\t" \
  "v_smfmac_f32_16x16x64_bf16 v[68:71], v[56:59], a[0:7], v104\n\t" \
  "ds_read_b128 v[52:55], v101 offset:14336\n\t" \
  "v_smfmac_f32_16x16x64_bf16 v[72:75], v[56:59], a[8:15], v104\n\t" \
  "s_waitcnt lgkmcnt(3)\n\t" \
  "v_smfmac_f32_16x16x64_bf16 v[68:71], v[60:63], a[16:23], v104\n\t" \
  "ds_read_b128 v[56:59], v101 offset:15360\n\t" \
  "v_smfmac_f32_16x16x64_bf16 v[72:75], v[60:63], a[24:31], v104\n\t" \
  "s_waitcnt lgkmcnt(3)\n\t" \
  "v_smfmac_f32_16x16x64_bf16 v[68:71], v[64:67], a[96:103], v104\n\t" \
  "ds_read_b128 v[60:63], v101 offset:2048\n\t" \
  "global_load_dword v119, v109, %[kc] sc1\n\t" \
  "v_smfmac_f32_16x16x64_bf16 v[72:75], v[64:67], a[104:111], v104\n\t" \
  "s_waitcnt lgkmcnt(3)\n\t" \
  "v_smfmac_f32_16x16x64_bf16 v[68:71], v[48:51], a[112:119], v104\n\t" \
  "ds_read_b128 v[64:67], v101 offset:3072\n\t" \
  "ds_read_b128 v[16:19], v102 offset:49152\n\t" \
  "v_smfmac_f32_16x16x64_bf16 v[72:75], v[48:51], a[120:127], v104\n\t" \
  "s_waitcnt lgkmcnt(4)\n\t" \
  "v_smfmac_f32_16x16x64_bf16 v[68:71], v[52:55], v[84:91], v104\n\t" \
  "ds_read_b128 v[48:51], v101 offset:7168\n\t" \
  "ds_read_b128 v[20:23], v102 offset:50176\n\t" \
  "v_smfmac_f32_16x16x64_bf16 v[72:75], v[52:55], v[92:99], v104\n\t" \
  "s_waitcnt lgkmcnt(5)\n\t" \
  "v_smfmac_f32_16x16x64_bf16 v[68:71], v[56:59], v[216:223], v104\n\t" \
  "ds_read_b128 v[52:55], v101 offset:6144\n\t" \
  "ds_read_b128 v[24:27], v102 offset:51200\n\t" \
  "v_smfmac_f32_16x16x64_bf16 v[72:75], v[56:59], v[224:231], v104\n\t" \
  "ds_read_b128 v[28:31], v102 offset:52224\n\t" \
  "s_waitcnt lgkmcnt(7)\n\t" \
  "v_smfmac_f32_16x16x64_bf16 v[68:71], v[60:63], v[0:7], v104\n\t" \
  "buffer_load_dwordx4 v[84:87], v107, %[ru], s94 offen offset:0 sc1\n\t" \
  "v_smfmac_f32_16x16x64_bf16 v[72:75], v[60:63], v[8:15], v104\n\t" \
  "s_waitcnt lgkmcnt(6)\n\t" \
  "v_smfmac_f32_16x16x64_bf16 v[68:71], v[64:67], a[64:71], v104\n\t" \
  "ds_read_b128 v[0:3], v103 offset:32768\n\t" \
  "buffer_load_dwordx4 v[88:91], v107, %[ru], s94 offen offset:1024 sc1\n\t" \
  "v_smfmac_f32_16x16x64_bf16 v[72:75], v[64:67], a[72:79], v104\n\t" \
  "ds_read_b128 v[4:7], v103 offset:33792\n\t" \
  "s_waitcnt lgkmcnt(6)\n\t" \
  "v_smfmac_f32_16x16x64_bf16 v[68:71], v[48:51], a[160:167], v104\n\t" \
  "buffer_load_dwordx4 v[92:95], v107, %[ru], s94 offen offset:2048 sc1\n\t" \
  "s_waitcnt lgkmcnt(4)\n\t" \
  "v_smfmac_f32_16x16x64_bf16 v[68:71], v[52:55], v[16:23], v104\n\t" \
  "ds_read_b128 v[8:11], v103 offset:34816\n\t" \
  "v_smfmac_f32_16x16x64_bf16 v[72:75], v[48:51], a[168:175], v104\n\t" \
  "ds_read_b128 v[12:15], v103 offset:35840\n\t" \
  "buffer_load_dwordx4 v[96:99], v107, %[ru], s94 offen offset:3072 sc1\n\t" \
  "s_waitcnt lgkmcnt(4)\n\t" \
  "v_smfmac_f32_16x16x64_bf16 v[72:75], v[52:55], v[24:31], v104\n\t" \
  "v_add_f32 v44, v68, v70\n\t" \
  "v_smfmac_f32_16x16x64_bf16 v[76:79], v[56:59], v[232:239], v104\n\t" \
  "v_add_f32 v45, v69, v71\n\t" \
  "v_cvt_pk_bf16_f32 v44, v44, v45\n\t" \
  "v_smfmac_f32_16x16x64_bf16 v[80:83], v[56:59], v[240:247], v104\n\t" \
  "v_add_f32 v46, v72, v74\n\t" \
  "v_add_f32 v47, v73, v75\n\t" \
  "v_smfmac_f32_16x16x64_bf16 v[76:79], v[60:63], a[48:55], v104\n\t" \
  "v_cvt_pk_bf16_f32 v46, v46, v47\n\t" \
  "ds_write2st64_b32 v105, v44, v46 offset1:1\n\t" \
  "v_smfmac_f32_16x16x64_bf16 v[80:83], v[60:63], a[56:63], v104\n\t" \
  "ds_read_b128 v[56:59], v101 offset:12288\n\t" \
  "v_smfmac_f32_16x16x64_bf16 v[76:79], v[64:67], a[80:87], v104\n\t" \
  "ds_read_b128 v[16:19], v102 offset:0\n\t" \
  "v_smfmac_f32_16x16x64_bf16 v[80:83], v[64:67], a[88:95], v104\n\t" \
  "ds_read_b128 v[20:23], v102 offset:1024\n\t" \
  "v_smfmac_f32_16x16x64_bf16 v[76:79], v[48:51], a[176:183], v104\n\t" \
  "s_waitcnt lgkmcnt(3)\n\t" \
  "s_barrier\n\t" \
  "v_smfmac_f32_16x16x64_bf16 v[80:83], v[48:51], a[184:191], v104\n\t" \
  "v_smfmac_f32_16x16x64_bf16 v[76:79], v[52:55], a[144:151], v104\n\t" \
  "ds_read_b128 v[60:63], v101 offset:5120\n\t" \
  "ds_read_b128 v[24:27], v102 offset:2048\n\t" \
  "v_smfmac_f32_16x16x64_bf16 v[80:83], v[52:55], a[152:159], v104\n\t" \
  "ds_read_b128 v[64:67], v101 offset:14336\n\t" \
  "s_waitcnt lgkmcnt(5)\n\t" \
  "v_smfmac_f32_16x16x64_bf16 v[76:79], v[56:59], v[0:7], v104\n\t" \
  "ds_read_b128 v[48:51], v101 offset:0\n\t" \
  "ds_read_b128 v[28:31], v102 offset:3072\n\t" \
  "v_smfmac_f32_16x16x64_bf16 v[80:83], v[56:59], v[8:15], v104\n\t" \
  "s_waitcnt lgkmcnt(4)\n\t" \
  "v_smfmac_f32_16x16x64_bf16 v[76:79], v[60:63], a[128:135], v104\n\t" \
  "ds_read_b128 v[52:55], v101 offset:13312\n\t" \
  "buffer_load_dwordx4 v[32:35], v100, %[ru], s47 offen offset:0 sc1\n\t" \
  "v_smfmac_f32_16x16x64_bf16 v[80:83], v[60:63], a[136:143], v104\n\t" \
  "ds_read_b128 v[0:3], v102 offset:32768\n\t" \
  "s_waitcnt lgkmcnt(4)\n\t" \
  "v_smfmac_f32_16x16x64_bf16 v[76:79], v[64:67], v[200:207], v104\n\t" \
  "ds_read_b128 v[56:59], v101 offset:1024\n\t" \
  "v_smfmac_f32_16x16x64_bf16 v[80:83], v[64:67], v[208:215], v104\n\t" \
  "ds_read_b128 v[60:63], v101 offset:4096\n\t" \
  "ds_read_b128 v[4:7], v102 offset:33792\n\t" \
  "buffer_load_dwordx4 v[36:39], v100, %[ru], s47 offen offset:1024 sc1\n\t" \
  "s_waitcnt lgkmcnt(6)\n\t" \
  "v_smfmac_f32_16x16x64_bf16 v[76:79], v[48:51], v[16:23], v104\n\t" \
  "ds_read_b128 v[8:11], v102 offset:34816\n\t" \
  "s_waitcnt lgkmcnt(6)\n\t" \
  "v_smfmac_f32_16x16x64_bf16 v[80:83], v[48:51], v[24:31], v104\n\t" \
  "ds_read_b128 v[12:15], v102 offset:35840\n\t" \
  "s_waitcnt lgkmcnt(6)\n\t" \
  "v_smfmac_f32_16x16x64_bf16 v[76:79], v[52:55], v[184:191], v104\n\t" \
  "buffer_load_dwordx4 v[40:43], v100, %[ru], s47 offen offset:2048 sc1\n\t" \
  "v_smfmac_f32_16x16x64_bf16 v[80:83], v[52:55], v[192:199], v104\n\t" \
  "s_waitcnt lgkmcnt(4)\n\t" \
  "v_smfmac_f32_16x16x64_bf16 v[76:79], v[56:59], a[32:39], v104\n\t" \
  "s_waitcnt lgkmcnt(2)\n\t" \
  "v_smfmac_f32_16x16x64_bf16 v[76:79], v[60:63], v[0:7], v104\n\t" \
  "v_smfmac_f32_16x16x64_bf16 v[80:83], v[56:59], a[40:47], v104\n\t" \
  "s_waitcnt lgkmcnt(0)\n\t" \
  "v_smfmac_f32_16x16x64_bf16 v[80:83], v[60:63], v[8:15], v104\n\t" \
  "v_add_f32 v20, v76, v78\n\t" \
  "v_add_f32 v21, v77, v79\n\t" \
  "v_cvt_pk_bf16_f32 v20, v20, v21\n\t" \
  "ds_write_b32 v106, v20\n\t" \
  "s_nop 3\n\t" \
  "v_add_f32 v23, v80, v82\n\t" \
  "v_add_f32 v21, v81, v83\n\t" \
  "v_cvt_pk_bf16_f32 v21, v23, v21\n\t" \
  "ds_write_b32 v106, v21 offset:256\n\t" \
  "s_waitcnt lgkmcnt(0)\n\t" \
  "s_barrier\n\t" \
  "v_bfe_i32 v64, v114, 0, 1\n\t" \
  "v_and_b32 v64, 0xff800000, v64\n\t" \
  "v_bfe_i32 v65, v114, 1, 1\n\t" \
  "v_and_b32 v65, 0xff800000, v65\n\t" \
  "v_bfe_i32 v66, v114, 2, 1\n\t" \
  "v_and_b32 v66, 0xff800000, v66\n\t" \
  "v_bfe_i32 v67, v114, 3, 1\n\t" \
  "v_and_b32 v67, 0xff800000, v67\n\t" \
  "v_bfe_i32 v68, v114, 4, 1\n\t" \
  "v_and_b32 v68, 0xff800000, v68\n\t" \
  "v_bfe_i32 v69, v114, 5, 1\n\t" \
  "v_and_b32 v69, 0xff800000, v69\n\t" \
  "v_bfe_i32 v70, v114, 6, 1\n\t" \
  "v_and_b32 v70, 0xff800000, v70\n\t" \
  "v_bfe_i32 v71, v114, 7, 1\n\t" \
  "v_and_b32 v71, 0xff800000, v71\n\t" \
  "s_lshl_b32 s94, s45, 4\n\t" \
  "s_and_b32 s96, s44, 2\n\t" \
  "s_lshl_b32 s96, s96, 13\n\t" \
  "s_and_b32 s76, s44, 1\n\t" \
  "s_lshl_b32 s76, s76, 12\n\t" \
  "s_add_i32 s96, s96, s76\n\t" \
  "s_mov_b32 s95, 1\n\t" \
  "s_sleep 1\n\t" \
  "ds_read_b128 v[0:3], v101 offset:20480\n\t" \
  "ds_read_b128 v[4:7], v101 offset:21504\n\t" \
  "ds_read_b128 v[8:11], v101 offset:22528\n\t" \
  "ds_read_b128 v[12:15], v101 offset:23552\n\t" \
  "ds_read_b128 v[16:19], v101 offset:24576\n\t" \
  "ds_read_b128 v[20:23], v101 offset:25600\n\t" \
  "ds_read_b128 v[24:27], v101 offset:26624\n\t" \
  "ds_read_b128 v[28:31], v101 offset:27648\n\t" \
  "ds_read_b64_tr_b16 v[48:49], v108 offset:0\n\t" \
  "ds_read_b64_tr_b16 v[50:51], v108 offset:64\n\t" \
  "ds_read_b64_tr_b16 v[52:53], v108 offset:512\n\t" \
  "ds_read_b64_tr_b16 v[54:55], v108 offset:576\n\t" \
  "ds_read_b64_tr_b16 v[56:57], v108 offset:1024\n\t" \
  "ds_read_b64_tr_b16 v[58:59], v108 offset:1088\n\t" \
  "ds_read_b64_tr_b16 v[60:61], v108 offset:1536\n\t" \
  "ds_read_b64_tr_b16 v[62:63], v108 offset:1600\n\t" \
  "s_waitcnt vmcnt(3) lgkmcnt(8)\n\t" \
  "v_mfma_f32_16x16x32_bf16 v[64:67], v[0:3], v[84:87], v[64:67]\n\t" \
  "v_mfma_f32_16x16x32_bf16 v[68:71], v[16:19], v[84:87], v[68:71]\n\t" \
  "v_mfma_f32_16x16x32_bf16 v[64:67], v[4:7], v[88:91], v[64:67]\n\t" \
  "v_mfma_f32_16x16x32_bf16 v[68:71], v[20:23], v[88:91], v[68:71]\n\t" \
  "v_mfma_f32_16x16x32_bf16 v[64:67], v[8:11], v[92:95], v[64:67]\n\t" \
  "v_mfma_f32_16x16x32_bf16 v[68:71], v[24:27], v[92:95], v[68:71]\n\t" \
  "v_mfma_f32_16x16x32_bf16 v[64:67], v[12:15], v[96:99], v[64:67]\n\t" \
  "v_mfma_f32_16x16x32_bf16 v[68:71], v[28:31], v[96:99], v[68:71]\n\t" \
  "s_waitcnt lgkmcnt(0)\n\t" \
  "v_mfma_f32_16x16x32_bf16 v[72:75], v[48:51], v[84:87], 0\n\t" \
  "v_mfma_f32_16x16x32_bf16 v[72:75], v[52:55], v[88:91], v[72:75]\n\t" \
  "v_mfma_f32_16x16x32_bf16 v[72:75], v[56:59], v[92:95], v[72:75]\n\t" \
  "v_mfma_f32_16x16x32_bf16 v[72:75], v[60:63], v[96:99], v[72:75]\n\t" \
  "s_barrier\n\t" \
  "v_max3_f32 v76, v64, v65, v66\n\t" \
  "v_max3_f32 v77, v67, v68, v69\n\t" \
  "v_max3_f32 v76, v76, v70, v71\n\t" \
  "s_nop 4\n\t" \
  "v_max3_f32 v77, v77, v72, v73\n\t" \
  "v_max3_f32 v76, v76, v74, v75\n\t" \
  "v_max_f32 v76, v76, v77\n\t" \
  "v_mov_b32 v77, v76\n\t" \
  "s_nop 1\n\t" \
  "v_permlane16_swap_b32 v76, v77\n\t" \
  "v_max_f32 v76, v76, v77\n\t" \
  "v_mov_b32 v77, v76\n\t" \
  "s_nop 1\n\t" \
  "v_permlane32_swap_b32 v76, v77\n\t" \
  "v_max_f32 v76, v76, v77\n\t" \
  "v_mul_f32 v76, %[cs], v76\n\t" \
  "v_fma_f32 v64, v64, %[cs], -v76\n\t" \
  "v_fma_f32 v65, v65, %[cs], -v76\n\t" \
  "v_fma_f32 v66, v66, %[cs], -v76\n\t" \
  "v_fma_f32 v67, v67, %[cs], -v76\n\t" \
  "v_fma_f32 v68, v68, %[cs], -v76\n\t" \
  "v_fma_f32 v69, v69, %[cs], -v76\n\t" \
  "v_fma_f32 v70, v70, %[cs], -v76\n\t" \
  "v_fma_f32 v71, v71, %[cs], -v76\n\t" \
  "v_fma_f32 v72, v72, %[cs], -v76\n\t" \
  "v_fma_f32 v73, v73, %[cs], -v76\n\t" \
  "v_fma_f32 v74, v74, %[cs], -v76\n\t" \
  "v_fma_f32 v75, v75, %[cs], -v76\n\t" \
  "v_exp_f32 v64, v64\n\t" \
  "v_exp_f32 v65, v65\n\t" \
  "v_exp_f32 v66, v66\n\t" \
  "v_exp_f32 v67, v67\n\t" \
  "v_exp_f32 v68, v68\n\t" \
  "v_exp_f32 v69, v69\n\t" \
  "v_exp_f32 v70, v70\n\t" \
  "v_exp_f32 v71, v71\n\t" \
  "v_exp_f32 v72, v72\n\t" \
  "v_exp_f32 v73, v73\n\t" \
  "v_exp_f32 v74, v74\n\t" \
  "v_exp_f32 v75, v75\n\t" \
  "v_add_f32 v76, v72, v73\n\t" \
  "v_add_f32 v77, v74, v75\n\t" \
  "v_add_f32 v78, v64, v65\n\t" \
  "v_add_f32 v79, v66, v67\n\t" \
  "v_add_f32 v76, v76, v77\n\t" \
  "v_add_f32 v78, v78, v68\n\t" \
  "v_add_f32 v79, v79, v69\n\t" \
  "v_add_f32 v78, v78, v70\n\t" \
  "v_add_f32 v79, v79, v71\n\t" \
  "v_fma_f32 v76, v76, 0.5, v78\n\t" \
  "v_add_f32 v76, v76, v79\n\t" \
  "v_mov_b32 v77, v76\n\t" \
  "s_nop 1\n\t" \
  "v_permlane16_swap_b32 v76, v77\n\t" \
  "v_add_f32 v76, v76, v77\n\t" \
  "v_mov_b32 v77, v76\n\t" \
  "s_nop 1\n\t" \
  "v_permlane32_swap_b32 v76, v77\n\t" \
  "v_add_f32 v76, v76, v77\n\t" \
  "v_rcp_f32 v76, v76\n\t" \
  "s_nop 0\n\t" \
  "v_mul_f32 v64, v64, v76\n\t" \
  "v_mul_f32 v65, v65, v76\n\t" \
  "v_mul_f32 v66, v66, v76\n\t" \
  "v_mul_f32 v67, v67, v76\n\t" \
  "v_mul_f32 v68, v68, v76\n\t" \
  "v_mul_f32 v69, v69, v76\n\t" \
  "v_mul_f32 v70, v70, v76\n\t" \
  "v_mul_f32 v71, v71, v76\n\t" \
  "v_mul_f32 v72, v72, v76\n\t" \
  "v_mul_f32 v73, v73, v76\n\t" \
  "v_mul_f32 v74, v74, v76\n\t" \
  "v_mul_f32 v75, v75, v76\n\t" \
  "v_cvt_pk_bf16_f32 v0, v64, v68\n\t" \
  "v_cvt_pk_bf16_f32 v1, v65, v69\n\t" \
  "v_cvt_pk_bf16_f32 v2, v66, v70\n\t" \
  "v_cvt_pk_bf16_f32 v3, v67, v71\n\t" \
  "v_cvt_pk_bf16_f32 v4, v72, v73\n\t" \
  "v_cvt_pk_bf16_f32 v5, v74, v75\n\t" \
  "v_permlane32_swap_b32 v0, v2\n\t" \
  "v_permlane32_swap_b32 v1, v3\n\t" \
  "v_mov_b64 v[6:7], v[4:5]\n\t" \
  "s_mov_b32 exec_hi, 0\n\t" \
  "buffer_load_dword v118, v112, %[rh], s96 offen sc1\n\t" \
  "s_mov_b64 exec, -1\n\t" \
  "buffer_store_dwordx4 v[0:3], v110, %[rh], s94 offen sc1\n\t" \
  "v_permlane16_swap_b32 v4, v6\n\t" \
  "v_permlane16_swap_b32 v5, v7\n\t" \
  "buffer_store_dwordx4 v[4:7], v111, %[rh], s94 offen sc1\n\t" \
  "s_waitcnt vmcnt(3)\n\t" \
  "ds_write_b128 v101, v[32:35] offset:23552\n\t" \
  "ds_write_b128 v101, v[36:39] offset:24576\n\t" \
  "ds_write_b128 v101, v[40:43] offset:25600\n\t" \
  "s_add_i32 s77, s44, 2\n\t" \
  "s_min_u32 s77, s77, 63\n\t" \
  "v_cmp_gt_i32 vcc, s77, v119\n\t" \
  "s_cmp_lg_u64 vcc, 0\n\t" \
  "s_cselect_b32 s42, 32, 0\n\t" \
  "s_cmp_eq_u32 s44, s43\n\t" \
  "s_cselect_b32 s42, 32, s42\n\t" \
  "s_add_i32 s41, s56, 1950\n\t" \
  "s_sub_i32 s76, s92, s51\n\t" \
  "s_sub_i32 s77, s93, s53\n\t" \
  "s_cmp_eq_u32 s76, 0\n\t" \
  "s_cbranch_scc1 21f\n\t" \
  "v_cvt_f32_u32 v117, s76\n\t" \
  "v_cvt_f32_u32 v116, s77\n\t" \
  "v_rcp_f32 v117, v117\n\t" \
  "s_mov_b32 s51, s92\n\t" \
  "v_mul_f32 v117, v117, v116\n\t" \
  "v_max_f32 v117, 0x3f028f5c, v117\n\t" \
  "v_min_f32 v117, 0x3ffeb852, v117\n\t" \
  "v_rcp_f32 v116, v117\n\t" \
  "v_mul_f32 v117, 0x4f000000, v117\n\t" \
  "v_mul_f32 v116, 0x47800000, v116\n\t" \
  "v_cvt_u32_f32 v117, v117\n\t" \
  "v_cvt_u32_f32 v116, v116\n\t" \
  "s_mov_b32 s53, s93\n\t" \
  "v_readfirstlane_b32 s46, v117\n\t" \
  "v_readfirstlane_b32 s52, v116\n\t" \
  "21:\n\t" \
  "s_add_i32 s44, s44, 1\n\t" \
  "s_and_b32 s58, s58, 4\n\t" \
  "s_and_b32 s76, s44, 64\n\t" \
  "s_or_b32 s42, s42, s76\n\t" \
  "s_cbranch_scc0 20b\n\t" \
  "s_branch 99f\n\t" \
  "14:\n\t" \
  "s_sleep 3\n\t" \
  "s_add_u32 s59, s59, 0x10000\n\t" \
  "s_cmp_lt_u32 s59, 0x100000\n\t" \
  "s_cbranch_scc1 13b\n\t" \
  "s_mov_b64 exec, -1\n\t" \
  "s_mov_b32 s42, 16\n\t" \
  "s_branch 99f\n\t" \
  "8:\n\t" \
  "s_bitcmp1_b32 s58, 1\n\t" \
  "s_cbranch_scc1 4b\n\t" \
  "s_bitcmp1_b32 s58, 2\n\t" \
  "s_cbranch_scc1 5b\n\t" \
  "v_max3_u32 v116, v32, v33, v34\n\t" \
  "v_max3_u32 v117, v35, v36, v37\n\t" \
  "v_max3_u32 v116, v116, v38, v39\n\t" \
  "v_max_u32 v116, v116, v117\n\t" \
  "v_cmp_eq_u32 vcc, -1, v116\n\t" \
  "s_cbranch_vccz 5b\n\t" \
  "s_branch 0f\n\t" \
  "9:\n\t" \
  "s_bitcmp1_b32 s58, 2\n\t" \
  "s_cbranch_scc1 6b\n\t" \
  "v_max3_u32 v116, v40, v41, v42\n\t" \
  "v_max3_u32 v117, v43, v44, v45\n\t" \
  "v_max3_u32 v116, v116, v46, v47\n\t" \
  "v_max_u32 v116, v116, v117\n\t" \
  "v_cmp_eq_u32 vcc, -1, v116\n\t" \
  "s_cbranch_vccz 6b\n\t" \
  "s_or_b32 s58, s58, 2\n\t" \
  "0:\n\t" \
  "s_or_b32 s58, s58, 1\n\t" \
  "11:\n\t" \
  "buffer_load_dwordx4 v[32:35], v100, %[rh], s40 offen sc1\n\t" \
  "buffer_load_dwordx4 v[36:39], v100, %[rh], s76 offen sc1\n\t" \
  "buffer_load_dwordx4 v[40:43], v100, %[rh], s77 offen sc1\n\t" \
  "buffer_load_dwordx4 v[44:47], v100, %[rh], s78 offen sc1\n\t" \
  "s_waitcnt lgkmcnt(0)\n\t" \
  "s_branch 10b\n\t" \
  "17:\n\t" \
  "s_add_u32 s59, s59, 1\n\t" \
  "s_bitcmp1_b32 s58, 2\n\t" \
  "s_cbranch_scc1 3b\n\t" \
  "s_waitcnt vmcnt(0)\n\t" \
  "v_max_u32 v116, v32, v33\n\t" \
  "v_max3_u32 v116, v116, v34, v35\n\t" \
  "v_max3_u32 v116, v116, v36, v37\n\t" \
  "v_max3_u32 v116, v116, v38, v39\n\t" \
  "v_max3_u32 v116, v116, v40, v41\n\t" \
  "v_max3_u32 v116, v116, v42, v43\n\t" \
  "v_max3_u32 v116, v116, v44, v45\n\t" \
  "v_max3_u32 v116, v116, v46, v47\n\t" \
  "v_cmp_eq_u32 vcc, -1, v116\n\t" \
  "s_cbranch_vccz 3b\n\t" \
  "s_bitcmp1_b32 s59, 6\n\t" \
  "s_cbranch_scc1 12f\n\t" \
  "buffer_load_dwordx4 v[32:35], v100, %[rh], s40 offen sc1\n\t" \
  "buffer_load_dwordx4 v[36:39], v100, %[rh], s76 offen sc1\n\t" \
  "buffer_load_dwordx4 v[40:43], v100, %[rh], s77 offen sc1\n\t" \
  "buffer_load_dwordx4 v[44:47], v100, %[rh], s78 offen sc1\n\t" \
  "s_branch 17b\n\t" \
  "12:\n\t" \
  "s_mov_b32 s42, s58\n\t" \
  "s_branch 99f\n\t" \
  "93:\n\t" \
  "s_mov_b32 s58, s42\n\t" \
  "20:\n\t" \
  "s_and_b32 s76, s44, 7\n\t" \
  "s_lshl_b32 s76, s76, 15\n\t" \
  "s_add_i32 s40, s54, s76\n\t" \
  "s_and_b32 s76, s44, 3\n\t" \
  "s_lshl_b32 s45, s76, 14\n\t" \
  "s_add_i32 s47, s44, 1\n\t" \
  "s_min_u32 s47, s47, 63\n\t" \
  "s_lshr_b32 s76, s47, 1\n\t" \
  "s_lshl_b32 s47, s47, 20\n\t" \
  "s_add_i32 s47, s47, s55\n\t" \
  "s_bitcmp1_b64 %[sel], s76\n\t" \
  "s_cselect_b32 s76, 0x2000, 0\n\t" \
  "s_add_i32 s47, s47, s76\n\t" \
  "s_add_i32 s49, s44, -1\n\t" \
  "s_and_b32 s48, s49, 2\n\t" \
  "s_lshl_b32 s48, s48, 13\n\t" \
  "s_and_b32 s76, s49, 1\n\t" \
  "s_lshl_b32 s76, s76, 12\n\t" \
  "s_add_i32 s48, s48, s76\n\t" \
  "s_lshl_b32 s50, s53, 8\n\t" \
  "s_mov_b32 s59, 0\n\t" \
  "s_mov_b32 s92, s51\n\t" \
  "s_mov_b32 s93, s53\n\t" \
  "s_xor_b32 s76, s40, 0x400\n\t" \
  "s_xor_b32 s77, s40, 0x800\n\t" \
  "s_xor_b32 s78, s40, 0xc00\n\t" \
  "s_cmp_lg_u32 s58, 0\n\t" \
  "s_cbranch_scc1 11f\n\t" \
  "10:\n\t" \
  "ds_read_b128 v[48:51], v103 offset:32768\n\t" \
  "ds_read_b128 v[52:55], v103 offset:33792\n\t" \
  "ds_read_b128 v[56:59], v103 offset:34816\n\t" \
  "ds_read_b128 v[60:63], v103 offset:35840\n\t" \
  "ds_read_b128 v[16:19], v103 offset:49152\n\t" \
  "ds_read_b128 v[20:23], v103 offset:50176\n\t" \
  "ds_read_b128 v[24:27], v103 offset:51200\n\t" \
  "ds_read_b128 v[28:31], v103 offset:52224\n\t" \
  "ds_read_b128 v[84:87], v102 offset:16384\n\t" \
  "ds_read_b128 v[88:91], v102 offset:17408\n\t" \
  "ds_read_b128 v[92:95], v102 offset:18432\n\t" \
  "ds_read_b128 v[96:99], v102 offset:19456\n\t" \
  "s_waitcnt lgkmcnt(4)\n\t" \
  "ds_read_b128 v[0:3], v102 offset:49152\n\t" \
  "ds_read_b128 v[4:7], v102 offset:50176\n\t" \
  "ds_read_b128 v[8:11], v102 offset:51200\n\t" \
  "ds_read_b128 v[12:15], v102 offset:52224\n\t" \
  "s_bitcmp1_b32 s58, 1\n\t" \
  "s_cselect_b64 s[80:81], -1, 0\n\t" \
  "s_cmp_lg_u32 s58, 0\n\t" \
  "s_cbranch_scc1 17f\n\t" \
  "s_mov_b32 exec_hi, 0\n\t" \
  "16:\n\t" \
  "s_memtime vcc\n\t" \
  "s_waitcnt lgkmcnt(0)\n\t" \
  "s_sub_i32 s79, s41, vcc_lo\n\t" \
  "s_cmp_gt_i32 s79, 64\n\t" \
  "s_cbranch_scc1 16b\n\t" \
  "13:\n\t" \
  "buffer_load_dword v116, v112, %[rh], s48 offen sc1\n\t" \
  "s_waitcnt vmcnt(0)\n\t" \
  "15:\n\t" \
  "v_mov_b32 v117, 20000\n\t" \
  "v_cmp_ne_u32_sdwa vcc, v116, s49 src0_sel:BYTE_0 src1_sel:DWORD\n\t" \
  "v_subrev_u32 v116, s50, v116\n\t" \
  "v_ashrrev_i32 v116, 8, v116\n\t" \
  "s_nop 1\n\t" \
  "v_max_i32_dpp v116, v116, v116 quad_perm:[1,0,3,2] row_mask:0xf bank_mask:0xf\n\t" \
  "s_cbranch_vccnz 14f\n\t" \
  "s_nop 0\n\t" \
  "v_max_i32_dpp v116, v116, v116 quad_perm:[2,3,0,1] row_mask:0xf bank_mask:0xf\n\t" \
  "s_nop 1\n\t" \
  "v_max_i32_dpp v116, v116, v116 row_half_mirror row_mask:0xf bank_mask:0xf\n\t" \
  "s_nop 1\n\t" \
  "v_max_i32_dpp v116, v116, v116 row_mirror row_mask:0xf bank_mask:0xf\n\t" \
  "v_med3_i32 v116, v116, 0, v117\n\t" \
  "v_mul_u32_u24 v116, s52, v116\n\t" \
  "v_lshrrev_b32 v116, 16, v116\n\t" \
  "s_mov_b64 exec, -1\n\t" \
  "v_readfirstlane_b32 s84, v116\n\t" \
  "v_readlane_b32 s85, v116, 16\n\t" \
  "s_max_u32 s84, s84, s85\n\t" \
  "s_add_i32 s84, s84, s51\n\t" \
  "1:\n\t" \
  "s_memtime vcc\n\t" \
  "s_waitcnt lgkmcnt(0)\n\t" \
  "s_sub_i32 s79, s84, vcc_lo\n\t" \
  "s_cmp_gt_i32 s79, 128\n\t" \
  "s_cbranch_scc1 1b\n\t" \
  ".p2align 4\n\t" \
  "2:\n\t" \
  "s_sub_i32 s79, s79, 33\n\t" \
  "s_cmp_gt_i32 s79, 56\n\t" \
  "s_cbranch_scc1 2b\n\t" \
  "buffer_load_dwordx4 v[32:35], v100, %[rh], s40 offen sc1\n\t" \
  "s_nop 12\n\t" \
  "buffer_load_dwordx4 v[36:39], v100, %[rh], s76 offen sc1\n\t" \
  "s_nop 12\n\t" \
  "buffer_load_dwordx4 v[40:43], v100, %[rh], s77 offen sc1\n\t" \
  "s_nop 12\n\t" \
  "buffer_load_dwordx4 v[44:47], v100, %[rh], s78 offen sc1\n\t" \
  "s_memtime vcc\n\t" \
  "s_memrealtime s[82:83]\n\t" \
  "s_nop 4\n\t" \
  "s_memrealtime s[84:85]\n\t" \
  "s_nop 4\n\t" \
  "s_memrealtime s[56:57]\n\t" \
  "s_nop 4\n\t" \
  "s_memrealtime s[86:87]\n\t" \
  "s_nop 4\n\t" \
  "s_memrealtime s[88:89]\n\t" \
  "s_nop 4\n\t" \
  "s_memrealtime s[90:91]\n\t" \
  "s_mov_b32 s92, 0\n\t" \
  "s_waitcnt lgkmcnt(0)\n\t" \
  "s_cmp_eq_u32 s82, s84\n\t" \
  "s_addc_u32 s92, s92, 0\n\t" \
  "s_cmp_eq_u32 s82, s56\n\t" \
  "s_addc_u32 s92, s92, 0\n\t" \
  "s_cmp_eq_u32 s82, s86\n\t" \
  "s_addc_u32 s92, s92, 0\n\t" \
  "s_cmp_eq_u32 s82, s88\n\t" \
  "s_addc_u32 s92, s92, 0\n\t" \
  "s_cmp_eq_u32 s82, s90\n\t" \
  "s_addc_u32 s92, s92, 0\n\t" \
  "s_mul_i32 s92, s92, 24\n\t" \
  "s_add_i32 s92, s92, vcc_lo\n\t" \
  "s_lshl4_add_u32 s93, s82, 64\n\t" \
  "3:\n\t" \
  "v_mov_b64 v[68:69], 0\n\t" \
  "v_mov_b64 v[70:71], 0\n\t" \
  "v_mov_b64 v[72:73], 0\n\t" \
  "v_mov_b64 v[74:75], 0\n\t" \
  "v_mov_b64 v[76:77], 0\n\t" \
  "v_mov_b64 v[78:79], 0\n\t" \
  "v_mov_b64 v[80:81], 0\n\t" \
  "v_mov_b64 v[82:83], 0\n\t" \
  "s_waitcnt lgkmcnt(0)\n\t" \
  "s_waitcnt vmcnt(3)\n\t" \
  "v_smfmac_f32_16x16x64_bf16 v[68:71], v[32:35], v[152:159], v104\n\t" \
  "ds_write_b128 v101, v[32:35] offset:12288\n\t" \
  "v_smfmac_f32_16x16x64_bf16 v[72:75], v[32:35], v[160:167], v104\n\t" \
  "v_smfmac_f32_16x16x64_bf16 v[76:79], v[32:35], v[48:55], v104\n\t" \
  "v_smfmac_f32_16x16x64_bf16 v[80:83], v[32:35], v[56:63], v104\n\t" \
  "s_waitcnt vmcnt(2)\n\t" \
  "v_smfmac_f32_16x16x64_bf16 v[68:71], v[36:39], v[168:175], v104\n\t" \
  "ds_write_b128 v101, v[36:39] offset:13312\n\t" \
  "v_smfmac_f32_16x16x64_bf16 v[72:75], v[36:39], v[176:183], v104\n\t" \
  "v_smfmac_f32_16x16x64_bf16 v[76:79], v[36:39], v[184:191], v104\n\t" \
  "v_cmp_u_f32 vcc, v68, v69\n\t" \
  "v_smfmac_f32_16x16x64_bf16 v[80:83], v[36:39], v[192:199], v104\n\t" \
  "v_cmp_u_f32 s[82:83], v70, v71\n\t" \
  "s_waitcnt vmcnt(1)\n\t" \
  "v_smfmac_f32_16x16x64_bf16 v[68:71], v[40:43], v[16:23], v104\n\t" \
  "ds_write_b128 v101, v[40:43] offset:14336\n\t" \
  "v_smfmac_f32_16x16x64_bf16 v[72:75], v[40:43], v[24:31], v104\n\t" \
  "s_or_b64 vcc, vcc, s[82:83]\n\t" \
  "s_or_b64 vcc, vcc, s[80:81]\n\t" \
  "s_waitcnt lgkmcnt(1)\n\t" \
  "v_smfmac_f32_16x16x64_bf16 v[76:79], v[40:43], v[200:207], v104\n\t" \
  "s_cbranch_scc1 8f\n\t" \
  "5:\n\t" \
  "s_barrier\n\t" \
  "4:\n\t" \
  "s_memtime s[56:57]\n\t" \
  "ds_read_b128 v[48:51], v101 offset:0\n\t" \
  "v_smfmac_f32_16x16x64_bf16 v[80:83], v[40:43], v[208:215], v104\n\t" \
  "ds_read_b128 v[52:55], v101 offset:1024\n\t" \
  "ds_read_b128 v[56:59], v101 offset:4096\n\t" \
  "s_waitcnt vmcnt(0)\n\t" \
  "v_smfmac_f32_16x16x64_bf16 v[68:71], v[44:47], v[216:223], v104\n\t" \
  "v_smfmac_f32_16x16x64_bf16 v[72:75], v[44:47], v[224:231], v104\n\t" \
  "ds_write_b128 v101, v[44:47] offset:15360\n\t" \
  "v_smfmac_f32_16x16x64_bf16 v[76:79], v[44:47], v[232:239], v104\n\t" \
  "v_cmp_u_f32 vcc, v68, v69\n\t" \
  "v_smfmac_f32_16x16x64_bf16 v[80:83], v[44:47], v[240:247], v104\n\t" \
  "v_cmp_u_f32 s[82:83], v70, v71\n\t" \
  "s_waitcnt lgkmcnt(3)\n\t" \
  "v_smfmac_f32_16x16x64_bf16 v[68:71], v[48:51], a[0:7], v104\n\t" \
  "ds_read_b128 v[60:63], v101 offset:5120\n\t" \
  "ds_read_b128 v[64:67], v101 offset:8192\n\t" \
  "v_smfmac_f32_16x16x64_bf16 v[72:75], v[48:51], a[8:15], v104\n\t" \
  "s_or_b64 vcc, vcc, s[82:83]\n\t" \
  "s_waitcnt lgkmcnt(2)\n\t" \
  "v_smfmac_f32_16x16x64_bf16 v[68:71], v[52:55], a[16:23], v104\n\t" \
  "s_cbranch_scc1 9f\n\t" \
  "6:\n\t" \
  "s_barrier\n\t" \
  "v_smfmac_f32_16x16x64_bf16 v[72:75], v[52:55], a[24:31], v104\n\t" \
  "ds_read_b128 v[48:51], v101 offset:9216\n\t" \
  "v_smfmac_f32_16x16x64_bf16 v[68:71], v[56:59], a[96:103], v104\n\t" \
  "ds_read_b128 v[52:55], v101 offset:2048\n\t" \
  "v_smfmac_f32_16x16x64_bf16 v[72:75], v[56:59], a[104:111], v104\n\t" \
  "s_waitcnt lgkmcnt(3)\n\t" \
  "v_smfmac_f32_16x16x64_bf16 v[68:71], v[60:63], a[112:119], v104\n\t" \
  "ds_read_b128 v[56:59], v101 offset:3072\n\t" \
  "v_smfmac_f32_16x16x64_bf16 v[72:75], v[60:63], a[120:127], v104\n\t" \
  "s_waitcnt lgkmcnt(3)\n\t" \
  "v_smfmac_f32_16x16x64_bf16 v[68:71], v[64:67], a[192:199], v104\n\t" \
  "ds_read_b128 v[60:63], v101 offset:6144\n\t" \
  "v_smfmac_f32_16x16x64_bf16 v[72:75], v[64:67], a[200:207], v104\n\t" \
  "s_waitcnt lgkmcnt(3)\n\t" \
  "v_smfmac_f32_16x16x64_bf16 v[68:71], v[48:51], a[208:215], v104\n\t" \
  "ds_read_b128 v[64:67], v101 offset:7168\n\t" \
  "ds_read_b128 v[16:19], v103 offset:16384\n\t" \
  "global_load_dword v119, v109, %[kc] sc1\n\t" \
  "v_smfmac_f32_16x16x64_bf16 v[72:75], v[48:51], a[216:223], v104\n\t" \
  "s_waitcnt lgkmcnt(4)\n\t" \
  "v_smfmac_f32_16x16x64_bf16 v[68:71], v[52:55], v[84:91], v104\n\t" \
  "ds_read_b128 v[48:51], v101 offset:11264\n\t" \
  "ds_read_b128 v[20:23], v103 offset:17408\n\t" \
  "v_smfmac_f32_16x16x64_bf16 v[72:75], v[52:55], v[92:99], v104\n\t" \
  "s_waitcnt lgkmcnt(5)\n\t" \
  "v_smfmac_f32_16x16x64_bf16 v[68:71], v[56:59], a[64:71], v104\n\t" \
  "ds_read_b128 v[52:55], v101 offset:10240\n\t" \
  "ds_read_b128 v[24:27], v103 offset:18432\n\t" \
  "v_smfmac_f32_16x16x64_bf16 v[72:75], v[56:59], a[72:79], v104\n\t" \
  "ds_read_b128 v[28:31], v103 offset:19456\n\t" \
  "s_waitcnt lgkmcnt(7)\n\t" \
  "v_smfmac_f32_16x16x64_bf16 v[68:71], v[60:63], v[0:7], v104\n\t" \
  "v_smfmac_f32_16x16x64_bf16 v[72:75], v[60:63], v[8:15], v104\n\t" \
  "s_waitcnt lgkmcnt(6)\n\t" \
  "v_smfmac_f32_16x16x64_bf16 v[68:71], v[64:67], a[160:167], v104\n\t" \
  "ds_read_b128 v[0:3], v102 offset:0\n\t" \
  "v_smfmac_f32_16x16x64_bf16 v[72:75], v[64:67], a[168:175], v104\n\t" \
  "ds_read_b128 v[4:7], v102 offset:1024\n\t" \
  "s_waitcnt lgkmcnt(6)\n\t" \
  "v_smfmac_f32_16x16x64_bf16 v[68:71], v[48:51], v[120:127], v104\n\t" \
  "s_waitcnt lgkmcnt(4)\n\t" \
  "v_smfmac_f32_16x16x64_bf16 v[68:71], v[52:55], v[16:23], v104\n\t" \
  "ds_read_b128 v[8:11], v102 offset:2048\n\t" \
  "v_smfmac_f32_16x16x64_bf16 v[72:75], v[48:51], v[128:135], v104\n\t" \
  "ds_read_b128 v[12:15], v102 offset:3072\n\t" \
  "s_waitcnt lgkmcnt(4)\n\t" \
  "v_smfmac_f32_16x16x64_bf16 v[72:75], v[52:55], v[24:31], v104\n\t" \
  "v_add_f32 v44, v68, v70\n\t" \
  "v_smfmac_f32_16x16x64_bf16 v[76:79], v[56:59], a[80:87], v104\n\t" \
  "v_add_f32 v45, v69, v71\n\t" \
  "v_cvt_pk_bf16_f32 v44, v44, v45\n\t" \
  "v_smfmac_f32_16x16x64_bf16 v[80:83], v[56:59], a[88:95], v104\n\t" \
  "v_add_f32 v46, v72, v74\n\t" \
  "v_add_f32 v47, v73, v75\n\t" \
  "v_smfmac_f32_16x16x64_bf16 v[76:79], v[60:63], a[144:151], v104\n\t" \
  "v_cvt_pk_bf16_f32 v46, v46, v47\n\t" \
  "ds_write2st64_b32 v105, v44, v46 offset1:1\n\t" \
  "v_smfmac_f32_16x16x64_bf16 v[80:83], v[60:63], a[152:159], v104\n\t" \
  "ds_read_b128 v[56:59], v101 offset:0\n\t" \
  "v_smfmac_f32_16x16x64_bf16 v[76:79], v[64:67], a[176:183], v104\n\t" \
  "ds_read_b128 v[16:19], v102 offset:32768\n\t" \
  "v_smfmac_f32_16x16x64_bf16 v[80:83], v[64:67], a[184:191], v104\n\t" \
  "ds_read_b128 v[20:23], v102 offset:33792\n\t" \
  "v_smfmac_f32_16x16x64_bf16 v[76:79], v[48:51], v[136:143], v104\n\t" \
  "s_waitcnt lgkmcnt(3)\n\t" \
  "s_barrier\n\t" \
  "v_smfmac_f32_16x16x64_bf16 v[80:83], v[48:51], v[144:151], v104\n\t" \
  "v_smfmac_f32_16x16x64_bf16 v[76:79], v[52:55], a[240:247], v104\n\t" \
  "ds_read_b128 v[60:63], v101 offset:9216\n\t" \
  "ds_read_b128 v[24:27], v102 offset:34816\n\t" \
  "v_smfmac_f32_16x16x64_bf16 v[80:83], v[52:55], a[248:255], v104\n\t" \
  "ds_read_b128 v[64:67], v101 offset:2048\n\t" \
  "s_waitcnt lgkmcnt(5)\n\t" \
  "v_smfmac_f32_16x16x64_bf16 v[76:79], v[56:59], v[0:7], v104\n\t" \
  "ds_read_b128 v[48:51], v101 offset:4096\n\t" \
  "ds_read_b128 v[28:31], v102 offset:35840\n\t" \
  "v_smfmac_f32_16x16x64_bf16 v[80:83], v[56:59], v[8:15], v104\n\t" \
  "s_waitcnt lgkmcnt(4)\n\t" \
  "v_smfmac_f32_16x16x64_bf16 v[76:79], v[60:63], a[224:231], v104\n\t" \
  "ds_read_b128 v[52:55], v101 offset:1024\n\t" \
  "v_smfmac_f32_16x16x64_bf16 v[80:83], v[60:63], a[232:239], v104\n\t" \
  "ds_read_b128 v[0:3], v103 offset:0\n\t" \
  "buffer_load_dwordx4 v[32:35], v100, %[ru], s47 offen offset:0 sc1\n\t" \
  "s_waitcnt lgkmcnt(4)\n\t" \
  "v_smfmac_f32_16x16x64_bf16 v[76:79], v[64:67], a[48:55], v104\n\t" \
  "ds_read_b128 v[56:59], v101 offset:5120\n\t" \
  "v_smfmac_f32_16x16x64_bf16 v[80:83], v[64:67], a[56:63], v104\n\t" \
  "ds_read_b128 v[60:63], v101 offset:8192\n\t" \
  "ds_read_b128 v[4:7], v103 offset:1024\n\t" \
  "s_waitcnt lgkmcnt(6)\n\t" \
  "v_smfmac_f32_16x16x64_bf16 v[76:79], v[48:51], v[16:23], v104\n\t" \
  "ds_read_b128 v[8:11], v103 offset:2048\n\t" \
  "buffer_load_dwordx4 v[36:39], v100, %[ru], s47 offen offset:1024 sc1\n\t" \
  "s_waitcnt lgkmcnt(6)\n\t" \
  "v_smfmac_f32_16x16x64_bf16 v[80:83], v[48:51], v[24:31], v104\n\t" \
  "ds_read_b128 v[12:15], v103 offset:3072\n\t" \
  "s_waitcnt lgkmcnt(6)\n\t" \
  "v_smfmac_f32_16x16x64_bf16 v[76:79], v[52:55], a[32:39], v104\n\t" \
  "v_smfmac_f32_16x16x64_bf16 v[80:83], v[52:55], a[40:47], v104\n\t" \
  "s_waitcnt lgkmcnt(4)\n\t" \
  "v_smfmac_f32_16x16x64_bf16 v[76:79], v[56:59], a[128:135], v104\n\t" \
  "s_waitcnt lgkmcnt(2)\n\t" \
  "v_smfmac_f32_16x16x64_bf16 v[76:79], v[60:63], v[0:7], v104\n\t" \
  "v_smfmac_f32_16x16x64_bf16 v[80:83], v[56:59], a[136:143], v104\n\t" \
  "s_waitcnt lgkmcnt(0)\n\t" \
  "v_smfmac_f32_16x16x64_bf16 v[80:83], v[60:63], v[8:15], v104\n\t" \
  "v_add_f32 v20, v76, v78\n\t" \
  "v_add_f32 v21, v77, v79\n\t" \
  "v_cvt_pk_bf16_f32 v20, v20, v21\n\t" \
  "ds_write_b32 v106, v20\n\t" \
  "s_nop 3\n\t" \
  "v_add_f32 v23, v80, v82\n\t" \
  "v_add_f32 v21, v81, v83\n\t" \
  "v_cvt_pk_bf16_f32 v21, v23, v21\n\t" \
  "ds_write_b32 v106, v21 offset:256\n\t" \
  "s_waitcnt lgkmcnt(0)\n\t" \
  "s_barrier\n\t" \
  "s_waitcnt vmcnt(0)\n\t" \
  "s_barrier\n\t" \
  "ds_write_b128 v101, v[32:35] offset:26624\n\t" \
  "ds_write_b128 v101, v[36:39] offset:27648\n\t" \
  "s_add_i32 s77, s44, 2\n\t" \
  "s_min_u32 s77, s77, 63\n\t" \
  "v_cmp_gt_i32 vcc, s77, v119\n\t" \
  "s_cmp_lg_u64 vcc, 0\n\t" \
  "s_cselect_b32 s42, 32, 0\n\t" \
  "s_cmp_eq_u32 s44, s43\n\t" \
  "s_cselect_b32 s42, 32, s42\n\t" \
  "s_add_i32 s41, s56, 1950\n\t" \
  "s_sub_i32 s76, s92, s51\n\t" \
  "s_sub_i32 s77, s93, s53\n\t" \
  "s_cmp_eq_u32 s76, 0\n\t" \
  "s_cbranch_scc1 21f\n\t" \
  "v_cvt_f32_u32 v117, s76\n\t" \
  "v_cvt_f32_u32 v116, s77\n\t" \
  "v_rcp_f32 v117, v117\n\t" \
  "s_mov_b32 s51, s92\n\t" \
  "v_mul_f32 v117, v117, v116\n\t" \
  "v_max_f32 v117, 0x3f028f5c, v117\n\t" \
  "v_min_f32 v117, 0x3ffeb852, v117\n\t" \
  "v_rcp_f32 v116, v117\n\t" \
  "v_mul_f32 v117, 0x4f000000, v117\n\t" \
  "v_mul_f32 v116, 0x47800000, v116\n\t" \
  "v_cvt_u32_f32 v117, v117\n\t" \
  "v_cvt_u32_f32 v116, v116\n\t" \
  "s_mov_b32 s53, s93\n\t" \
  "v_readfirstlane_b32 s46, v117\n\t" \
  "v_readfirstlane_b32 s52, v116\n\t" \
  "21:\n\t" \
  "v_mov_b32 v0, -1\n\t" \
  "v_mov_b32 v1, -1\n\t" \
  "v_mov_b32 v2, -1\n\t" \
  "v_mov_b32 v3, -1\n\t" \
  "s_and_b32 s76, s49, 3\n\t" \
  "s_mul_i32 s76, s76, 0xc000\n\t" \
  "s_add_i32 s76, s76, 0x40000\n\t" \
  "buffer_store_dwordx4 v[0:3], v110, %[rh], s76 offen sc1\n\t" \
  "s_add_i32 s76, s44, 1\n\t" \
  "s_and_b32 s76, s76, 3\n\t" \
  "s_lshl_b32 s76, s76, 18\n\t" \
  "s_add_i32 s77, s76, 0x10000\n\t" \
  "buffer_store_dwordx4 v[0:3], v107, %[rh], s76 offen sc1\n\t" \
  "buffer_store_dwordx4 v[0:3], v107, %[rh], s77 offen sc1\n\t" \
  "buffer_store_dwordx4 v[0:3], v111, %[rh], s76 offen sc1\n\t" \
  "s_add_i32 s44, s44, 1\n\t" \
  "s_and_b32 s58, s58, 4\n\t" \
  "s_and_b32 s76, s44, 64\n\t" \
  "s_or_b32 s42, s42, s76\n\t" \
  "s_cbranch_scc0 20b\n\t" \
  "s_branch 99f\n\t" \
  "14:\n\t" \
  "s_sleep 3\n\t" \
  "s_add_u32 s59, s59, 0x10000\n\t" \
  "s_cmp_lt_u32 s59, 0x100000\n\t" \
  "s_cbranch_scc1 13b\n\t" \
  "s_mov_b64 exec, -1\n\t" \
  "s_mov_b32 s42, 16\n\t" \
  "s_branch 99f\n\t" \
  "8:\n\t" \
  "s_bitcmp1_b32 s58, 1\n\t" \
  "s_cbranch_scc1 4b\n\t" \
  "s_bitcmp1_b32 s58, 2\n\t" \
  "s_cbranch_scc1 5b\n\t" \
  "v_max3_u32 v116, v32, v33, v34\n\t" \
  "v_max3_u32 v117, v35, v36, v37\n\t" \
  "v_max3_u32 v116, v116, v38, v39\n\t" \
  "v_max_u32 v116, v116, v117\n\t" \
  "v_cmp_eq_u32 vcc, -1, v116\n\t" \
  "s_cbranch_vccz 5b\n\t" \
  "s_branch 0f\n\t" \
  "9:\n\t" \
  "s_bitcmp1_b32 s58, 2\n\t" \
  "s_cbranch_scc1 6b\n\t" \
  "v_max3_u32 v116, v40, v41, v42\n\t" \
  "v_max3_u32 v117, v43, v44, v45\n\t" \
  "v_max3_u32 v116, v116, v46, v47\n\t" \
  "v_max_u32 v116, v116, v117\n\t" \
  "v_cmp_eq_u32 vcc, -1, v116\n\t" \
  "s_cbranch_vccz 6b\n\t" \
  "s_or_b32 s58, s58, 2\n\t" \
  "0:\n\t" \
  "s_or_b32 s58, s58, 1\n\t" \
  "11:\n\t" \
  "buffer_load_dwordx4 v[32:35], v100, %[rh], s40 offen sc1\n\t" \
  "buffer_load_dwordx4 v[36:39], v100, %[rh], s76 offen sc1\n\t" \
  "buffer_load_dwordx4 v[40:43], v100, %[rh], s77 offen sc1\n\t" \
  "buffer_load_dwordx4 v[44:47], v100, %[rh], s78 offen sc1\n\t" \
  "s_waitcnt lgkmcnt(0)\n\t" \
  "s_branch 10b\n\t" \
  "17:\n\t" \
  "s_add_u32 s59, s59, 1\n\t" \
  "s_bitcmp1_b32 s58, 2\n\t" \
  "s_cbranch_scc1 3b\n\t" \
  "s_waitcnt vmcnt(0)\n\t" \
  "v_max_u32 v116, v32, v33\n\t" \
  "v_max3_u32 v116, v116, v34, v35\n\t" \
  "v_max3_u32 v116, v116, v36, v37\n\t" \
  "v_max3_u32 v116, v116, v38, v39\n\t" \
  "v_max3_u32 v116, v116, v40, v41\n\t" \
  "v_max3_u32 v116, v116, v42, v43\n\t" \
  "v_max3_u32 v116, v116, v44, v45\n\t" \
  "v_max3_u32 v116, v116, v46, v47\n\t" \
  "v_cmp_eq_u32 vcc, -1, v116\n\t" \
  "s_cbranch_vccz 3b\n\t" \
  "s_bitcmp1_b32 s59, 6\n\t" \
  "s_cbranch_scc1 12f\n\t" \
  "buffer_load_dwordx4 v[32:35], v100, %[rh], s40 offen sc1\n\t" \
  "buffer_load_dwordx4 v[36:39], v100, %[rh], s76 offen sc1\n\t" \
  "buffer_load_dwordx4 v[40:43], v100, %[rh], s77 offen sc1\n\t" \
  "buffer_load_dwordx4 v[44:47], v100, %[rh], s78 offen sc1\n\t" \
  "s_branch 17b\n\t" \
  "12:\n\t" \
  "s_mov_b32 s42, s58\n\t" \
  "s_branch 99f\n\t" \
  "99:\n\t" \
  "s_waitcnt vmcnt(0) lgkmcnt(0)\n\t" \
  ""
#define VO_BODY \
  "s_cmp_eq_u32 %[wv], 1\n\t" \
  "s_cbranch_scc1 91f\n\t" \
  "s_cmp_eq_u32 %[wv], 2\n\t" \
  "s_cbranch_scc1 92f\n\t" \
  "s_cmp_eq_u32 %[wv], 3\n\t" \
  "s_cbranch_scc1 93f\n\t" \
  "90:\n\t" \
  "s_mov_b32 s58, s42\n\t" \
  "20:\n\t" \
  "s_and_b32 s76, s44, 7\n\t" \
  "s_lshl_b32 s76, s76, 15\n\t" \
  "s_add_i32 s40, s54, s76\n\t" \
  "s_and_b32 s76, s44, 3\n\t" \
  "s_lshl_b32 s45, s76, 14\n\t" \
  "s_lshl_b32 s47, s76, 20\n\t" \
  "s_add_i32 s47, s47, s55\n\t" \
  "s_lshr_b32 s76, s47, 21\n\t" \
  "s_bitcmp1_b64 %[sel], s76\n\t" \
  "s_cselect_b32 s76, 0x2000, 0\n\t" \
  "s_add_i32 s47, s47, s76\n\t" \
  "s_add_i32 s49, s44, -1\n\t" \
  "s_and_b32 s48, s49, 2\n\t" \
  "s_lshl_b32 s48, s48, 13\n\t" \
  "s_and_b32 s76, s49, 1\n\t" \
  "s_lshl_b32 s76, s76, 12\n\t" \
  "s_add_i32 s48, s48, s76\n\t" \
  "s_lshl_b32 s50, s53, 8\n\t" \
  "s_mov_b32 s59, 0\n\t" \
  "s_mov_b32 s92, s51\n\t" \
  "s_mov_b32 s93, s53\n\t" \
  "s_xor_b32 s76, s40, 0x400\n\t" \
  "s_xor_b32 s77, s40, 0x800\n\t" \
  "s_xor_b32 s78, s40, 0xc00\n\t" \
  "s_cmp_lg_u32 s58, 0\n\t" \
  "s_cbranch_scc1 11f\n\t" \
  "10:\n\t" \
  "ds_read_b128 v[48:51], v102 offset:0\n\t" \
  "ds_read_b128 v[52:55], v102 offset:1024\n\t" \
  "ds_read_b128 v[56:59], v102 offset:2048\n\t" \
  "ds_read_b128 v[60:63], v102 offset:3072\n\t" \
  "ds_read_b128 v[0:3], v102 offset:16384\n\t" \
  "ds_read_b128 v[4:7], v102 offset:17408\n\t" \
  "ds_read_b128 v[8:11], v102 offset:18432\n\t" \
  "ds_read_b128 v[12:15], v102 offset:19456\n\t" \
  "ds_read_b128 v[16:19], v102 offset:32768\n\t" \
  "ds_read_b128 v[20:23], v102 offset:33792\n\t" \
  "ds_read_b128 v[24:27], v102 offset:34816\n\t" \
  "ds_read_b128 v[28:31], v102 offset:35840\n\t" \
  "s_waitcnt lgkmcnt(4)\n\t" \
  "ds_read_b128 v[80:83], v102 offset:49152\n\t" \
  "ds_read_b128 v[84:87], v102 offset:50176\n\t" \
  "ds_read_b128 v[88:91], v102 offset:51200\n\t" \
  "ds_read_b128 v[92:95], v102 offset:52224\n\t" \
  "s_bitcmp1_b32 s58, 1\n\t" \
  "s_cselect_b64 s[80:81], -1, 0\n\t" \
  "s_cmp_lg_u32 s58, 0\n\t" \
  "s_cbranch_scc1 17f\n\t" \
  "s_mov_b32 exec_hi, 0\n\t" \
  "16:\n\t" \
  "s_memtime vcc\n\t" \
  "s_waitcnt lgkmcnt(0)\n\t" \
  "s_sub_i32 s79, s41, vcc_lo\n\t" \
  "s_cmp_gt_i32 s79, 64\n\t" \
  "s_cbranch_scc1 16b\n\t" \
  "13:\n\t" \
  "buffer_load_dword v116, v112, %[rh], s48 offen sc1\n\t" \
  "s_waitcnt vmcnt(0)\n\t" \
  "15:\n\t" \
  "v_mov_b32 v117, 20000\n\t" \
  "v_cmp_ne_u32_sdwa vcc, v116, s49 src0_sel:BYTE_0 src1_sel:DWORD\n\t" \
  "v_subrev_u32 v116, s50, v116\n\t" \
  "v_ashrrev_i32 v116, 8, v116\n\t" \
  "s_nop 1\n\t" \
  "v_max_i32_dpp v116, v116, v116 quad_perm:[1,0,3,2] row_mask:0xf bank_mask:0xf\n\t" \
  "s_cbranch_vccnz 14f\n\t" \
  "s_nop 0\n\t" \
  "v_max_i32_dpp v116, v116, v116 quad_perm:[2,3,0,1] row_mask:0xf bank_mask:0xf\n\t" \
  "s_nop 1\n\t" \
  "v_max_i32_dpp v116, v116, v116 row_half_mirror row_mask:0xf bank_mask:0xf\n\t" \
  "s_nop 1\n\t" \
  "v_max_i32_dpp v116, v116, v116 row_mirror row_mask:0xf bank_mask:0xf\n\t" \
  "v_med3_i32 v116, v116, 0, v117\n\t" \
  "v_mul_u32_u24 v116, s52, v116\n\t" \
  "v_lshrrev_b32 v116, 16, v116\n\t" \
  "s_mov_b64 exec, -1\n\t" \
  "v_readfirstlane_b32 s84, v116\n\t" \
  "v_readlane_b32 s85, v116, 16\n\t" \
  "s_max_u32 s84, s84, s85\n\t" \
  "s_add_i32 s84, s84, s51\n\t" \
  "1:\n\t" \
  "s_memtime vcc\n\t" \
  "s_waitcnt lgkmcnt(0)\n\t" \
  "s_sub_i32 s79, s84, vcc_lo\n\t" \
  "s_cmp_gt_i32 s79, 128\n\t" \
  "s_cbranch_scc1 1b\n\t" \
  ".p2align 4\n\t" \
  "2:\n\t" \
  "s_sub_i32 s79, s79, 33\n\t" \
  "s_cmp_gt_i32 s79, 56\n\t" \
  "s_cbranch_scc1 2b\n\t" \
  "buffer_load_dwordx4 v[32:35], v100, %[rh], s40 offen sc1\n\t" \
  "s_nop 12\n\t" \
  "buffer_load_dwordx4 v[36:39], v100, %[rh], s76 offen sc1\n\t" \
  "s_nop 12\n\t" \
  "buffer_load_dwordx4 v[40:43], v100, %[rh], s77 offen sc1\n\t" \
  "s_nop 12\n\t" \
  "buffer_load_dwordx4 v[44:47], v100, %[rh], s78 offen sc1\n\t" \
  "s_memtime vcc\n\t" \
  "s_memrealtime s[82:83]\n\t" \
  "s_nop 4\n\t" \
  "s_memrealtime s[84:85]\n\t" \
  "s_nop 4\n\t" \
  "s_memrealtime s[56:57]\n\t" \
  "s_nop 4\n\t" \
  "s_memrealtime s[86:87]\n\t" \
  "s_nop 4\n\t" \
  "s_memrealtime s[88:89]\n\t" \
  "s_nop 4\n\t" \
  "s_memrealtime s[90:91]\n\t" \
  "s_mov_b32 s92, 0\n\t" \
  "s_waitcnt lgkmcnt(0)\n\t" \
  "s_cmp_eq_u32 s82, s84\n\t" \
  "s_addc_u32 s92, s92, 0\n\t" \
  "s_cmp_eq_u32 s82, s56\n\t" \
  "s_addc_u32 s92, s92, 0\n\t" \
  "s_cmp_eq_u32 s82, s86\n\t" \
  "s_addc_u32 s92, s92, 0\n\t" \
  "s_cmp_eq_u32 s82, s88\n\t" \
  "s_addc_u32 s92, s92, 0\n\t" \
  "s_cmp_eq_u32 s82, s90\n\t" \
  "s_addc_u32 s92, s92, 0\n\t" \
  "s_mul_i32 s92, s92, 24\n\t" \
  "s_add_i32 s92, s92, vcc_lo\n\t" \
  "s_lshl4_add_u32 s93, s82, 64\n\t" \
  "3:\n\t" \
  "v_mov_b64 v[72:73], 0\n\t" \
  "v_mov_b64 v[74:75], 0\n\t" \
  "v_mov_b64 v[76:77], 0\n\t" \
  "v_mov_b64 v[78:79], 0\n\t" \
  "s_waitcnt lgkmcnt(0)\n\t" \
  "s_waitcnt vmcnt(3)\n\t" \
  "v_smfmac_f32_16x16x64_bf16 v[72:75], v[32:35], v[48:55], v104\n\t" \
  "ds_write_b128 v101, v[32:35] offset:0\n\t" \
  "v_smfmac_f32_16x16x64_bf16 v[76:79], v[32:35], v[56:63], v104\n\t" \
  "s_waitcnt vmcnt(2)\n\t" \
  "ds_write_b128 v101, v[36:39] offset:1024\n\t" \
  "v_smfmac_f32_16x16x64_bf16 v[72:75], v[36:39], a[0:7], v104\n\t" \
  "v_smfmac_f32_16x16x64_bf16 v[76:79], v[36:39], a[8:15], v104\n\t" \
  "s_nop 3\n\t" \
  "v_cmp_u_f32 vcc, v72, v73\n\t" \
  "v_cmp_u_f32 s[82:83], v74, v75\n\t" \
  "s_waitcnt vmcnt(1)\n\t" \
  "v_smfmac_f32_16x16x64_bf16 v[72:75], v[40:43], a[16:23], v104\n\t" \
  "s_or_b64 vcc, vcc, s[82:83]\n\t" \
  "s_or_b64 vcc, vcc, s[80:81]\n\t" \
  "v_smfmac_f32_16x16x64_bf16 v[76:79], v[40:43], a[24:31], v104\n\t" \
  "s_waitcnt lgkmcnt(0)\n\t" \
  "s_cbranch_scc1 8f\n\t" \
  "5:\n\t" \
  "s_barrier\n\t" \
  "4:\n\t" \
  "s_memtime s[56:57]\n\t" \
  "ds_read_b128 v[48:51], v101 offset:4096\n\t" \
  "ds_read_b128 v[52:55], v101 offset:5120\n\t" \
  "ds_read_b128 v[56:59], v101 offset:9216\n\t" \
  "s_waitcnt vmcnt(0)\n\t" \
  "v_smfmac_f32_16x16x64_bf16 v[72:75], v[44:47], a[32:39], v104\n\t" \
  "ds_read_b128 v[60:63], v101 offset:8192\n\t" \
  "ds_read_b128 v[64:67], v101 offset:13312\n\t" \
  "v_smfmac_f32_16x16x64_bf16 v[76:79], v[44:47], a[40:47], v104\n\t" \
  "ds_read_b128 v[68:71], v101 offset:12288\n\t" \
  "ds_write_b128 v101, v[40:43] offset:2048\n\t" \
  "s_nop 1\n\t" \
  "v_cmp_u_f32 vcc, v72, v73\n\t" \
  "v_cmp_u_f32 s[82:83], v74, v75\n\t" \
  "s_waitcnt lgkmcnt(6)\n\t" \
  "v_smfmac_f32_16x16x64_bf16 v[72:75], v[48:51], v[0:7], v104\n\t" \
  "s_or_b64 vcc, vcc, s[82:83]\n\t" \
  "v_smfmac_f32_16x16x64_bf16 v[76:79], v[48:51], v[8:15], v104\n\t" \
  "s_cbranch_scc1 9f\n\t" \
  "6:\n\t" \
  "s_waitcnt lgkmcnt(5)\n\t" \
  "v_smfmac_f32_16x16x64_bf16 v[72:75], v[52:55], a[48:55], v104\n\t" \
  "ds_write_b128 v101, v[44:47] offset:3072\n\t" \
  "v_smfmac_f32_16x16x64_bf16 v[76:79], v[52:55], a[56:63], v104\n\t" \
  "v_mov_b64 v[40:41], 0\n\t" \
  "s_waitcnt lgkmcnt(5)\n\t" \
  "v_smfmac_f32_16x16x64_bf16 v[72:75], v[56:59], a[96:103], v104\n\t" \
  "v_mov_b64 v[42:43], 0\n\t" \
  "v_smfmac_f32_16x16x64_bf16 v[76:79], v[56:59], a[104:111], v104\n\t" \
  "s_waitcnt lgkmcnt(4)\n\t" \
  "v_smfmac_f32_16x16x64_bf16 v[72:75], v[60:63], v[16:23], v104\n\t" \
  "s_waitcnt lgkmcnt(0)\n\t" \
  "s_barrier\n\t" \
  "v_smfmac_f32_16x16x64_bf16 v[76:79], v[60:63], v[24:31], v104\n\t" \
  "ds_read_b128 v[48:51], v101 offset:6144\n\t" \
  "ds_read_b128 v[52:55], v101 offset:10240\n\t" \
  "v_smfmac_f32_16x16x64_bf16 v[72:75], v[64:67], a[144:151], v104\n\t" \
  "ds_read_b128 v[56:59], v101 offset:14336\n\t" \
  "ds_read_b128 v[60:63], v101 offset:7168\n\t" \
  "v_smfmac_f32_16x16x64_bf16 v[76:79], v[64:67], a[152:159], v104\n\t" \
  "ds_read_b128 v[32:35], v101 offset:11264\n\t" \
  "ds_read_b128 v[36:39], v101 offset:15360\n\t" \
  "v_smfmac_f32_16x16x64_bf16 v[72:75], v[68:71], v[80:87], v104\n\t" \
  "ds_read_b128 v[0:3], v103 offset:0\n\t" \
  "v_mov_b64 v[44:45], 0\n\t" \
  "v_smfmac_f32_16x16x64_bf16 v[76:79], v[68:71], v[88:95], v104\n\t" \
  "v_mov_b64 v[46:47], 0\n\t" \
  "s_waitcnt lgkmcnt(5)\n\t" \
  "v_smfmac_f32_16x16x64_bf16 v[72:75], v[48:51], a[64:71], v104\n\t" \
  "ds_read_b128 v[4:7], v103 offset:1024\n\t" \
  "v_mov_b64 v[80:81], 0\n\t" \
  "v_smfmac_f32_16x16x64_bf16 v[72:75], v[52:55], a[112:119], v104\n\t" \
  "v_mov_b64 v[82:83], 0\n\t" \
  "s_waitcnt lgkmcnt(2)\n\t" \
  "v_smfmac_f32_16x16x64_bf16 v[72:75], v[56:59], a[160:167], v104\n\t" \
  "ds_read_b128 v[8:11], v103 offset:2048\n\t" \
  "v_mov_b64 v[84:85], 0\n\t" \
  "v_smfmac_f32_16x16x64_bf16 v[72:75], v[60:63], a[80:87], v104\n\t" \
  "ds_read_b128 v[12:15], v103 offset:3072\n\t" \
  "v_mov_b64 v[86:87], 0\n\t" \
  "v_smfmac_f32_16x16x64_bf16 v[72:75], v[32:35], a[128:135], v104\n\t" \
  "ds_read_b128 v[16:19], v103 offset:16384\n\t" \
  "v_mov_b64 v[88:89], 0\n\t" \
  "v_smfmac_f32_16x16x64_bf16 v[72:75], v[36:39], a[176:183], v104\n\t" \
  "ds_read_b128 v[20:23], v103 offset:17408\n\t" \
  "v_mov_b64 v[90:91], 0\n\t" \
  "v_smfmac_f32_16x16x64_bf16 v[76:79], v[48:51], a[72:79], v104\n\t" \
  "ds_read_b128 v[24:27], v103 offset:18432\n\t" \
  "v_mov_b64 v[92:93], 0\n\t" \
  "v_smfmac_f32_16x16x64_bf16 v[76:79], v[52:55], a[120:127], v104\n\t" \
  "v_add_f32 v96, v72, v74\n\t" \
  "v_add_f32 v97, v73, v75\n\t" \
  "v_cvt_pk_bf16_f32 v96, v96, v97\n\t" \
  "ds_write_b32 v105, v96\n\t" \
  "v_smfmac_f32_16x16x64_bf16 v[76:79], v[56:59], a[168:175], v104\n\t" \
  "ds_read_b128 v[28:31], v103 offset:19456\n\t" \
  "v_mov_b64 v[94:95], 0\n\t" \
  "v_smfmac_f32_16x16x64_bf16 v[76:79], v[60:63], a[88:95], v104\n\t" \
  "v_smfmac_f32_16x16x64_bf16 v[76:79], v[32:35], a[136:143], v104\n\t" \
  "v_smfmac_f32_16x16x64_bf16 v[76:79], v[36:39], a[184:191], v104\n\t" \
  "s_waitcnt lgkmcnt(1)\n\t" \
  "s_barrier\n\t" \
  "ds_read_b64_tr_b16 v[96:97], v107 offset:0\n\t" \
  "ds_read_b64_tr_b16 v[98:99], v107 offset:64\n\t" \
  "ds_read_b64_tr_b16 v[56:57], v107 offset:1024\n\t" \
  "ds_read_b64_tr_b16 v[58:59], v107 offset:1088\n\t" \
  "s_nop 1\n\t" \
  "v_add_f32 v116, v76, v78\n\t" \
  "v_add_f32 v117, v77, v79\n\t" \
  "v_cvt_pk_bf16_f32 v116, v116, v117\n\t" \
  "ds_write_b32 v105, v116 offset:256\n\t" \
  "s_waitcnt lgkmcnt(3)\n\t" \
  "v_smfmac_f32_16x16x64_bf16 v[40:43], v[96:99], a[192:199], v106\n\t" \
  "v_smfmac_f32_16x16x64_bf16 v[44:47], v[96:99], a[224:231], v106\n\t" \
  "s_waitcnt lgkmcnt(0)\n\t" \
  "s_barrier\n\t" \
  "ds_read_b64_tr_b16 v[52:53], v107 offset:256\n\t" \
  "ds_read_b64_tr_b16 v[54:55], v107 offset:320\n\t" \
  "v_smfmac_f32_16x16x64_bf16 v[40:43], v[56:59], a[208:215], v106\n\t" \
  "ds_read_b64_tr_b16 v[60:61], v107 offset:1280\n\t" \
  "ds_read_b64_tr_b16 v[62:63], v107 offset:1344\n\t" \
  "v_smfmac_f32_16x16x64_bf16 v[44:47], v[56:59], v[0:7], v106\n\t" \
  "v_smfmac_f32_16x16x64_bf16 v[80:83], v[96:99], a[240:247], v106\n\t" \
  "v_mov_b64 v[32:33], 0\n\t" \
  "v_smfmac_f32_16x16x64_bf16 v[84:87], v[96:99], v[136:143], v106\n\t" \
  "v_mov_b64 v[34:35], 0\n\t" \
  "v_smfmac_f32_16x16x64_bf16 v[80:83], v[56:59], v[120:127], v106\n\t" \
  "v_mov_b64 v[36:37], 0\n\t" \
  "v_smfmac_f32_16x16x64_bf16 v[84:87], v[56:59], v[16:23], v106\n\t" \
  "s_waitcnt lgkmcnt(2)\n\t" \
  "v_smfmac_f32_16x16x64_bf16 v[40:43], v[52:55], a[200:207], v106\n\t" \
  "v_smfmac_f32_16x16x64_bf16 v[44:47], v[52:55], a[232:239], v106\n\t" \
  "s_xor_b32 s76, s47, 0x40000\n\t" \
  "s_waitcnt lgkmcnt(0)\n\t" \
  "v_smfmac_f32_16x16x64_bf16 v[40:43], v[60:63], a[216:223], v106\n\t" \
  "v_mov_b64 v[38:39], 0\n\t" \
  "s_xor_b32 s77, s47, 0x80000\n\t" \
  "v_smfmac_f32_16x16x64_bf16 v[44:47], v[60:63], v[8:15], v106\n\t" \
  "s_xor_b32 s78, s47, 0xc0000\n\t" \
  "v_smfmac_f32_16x16x64_bf16 v[80:83], v[52:55], a[248:255], v106\n\t" \
  "ds_read_b128 v[0:3], v103 offset:32768\n\t" \
  "v_cvt_pk_bf16_f32 v64, v40, v41\n\t" \
  "v_smfmac_f32_16x16x64_bf16 v[84:87], v[52:55], v[144:151], v106\n\t" \
  "ds_read_b128 v[4:7], v103 offset:33792\n\t" \
  "v_cvt_pk_bf16_f32 v65, v42, v43\n\t" \
  "v_smfmac_f32_16x16x64_bf16 v[80:83], v[60:63], v[128:135], v106\n\t" \
  "ds_read_b128 v[8:11], v103 offset:34816\n\t" \
  "v_cvt_pk_bf16_f32 v66, v44, v45\n\t" \
  "v_smfmac_f32_16x16x64_bf16 v[84:87], v[60:63], v[24:31], v106\n\t" \
  "ds_read_b128 v[12:15], v103 offset:35840\n\t" \
  "v_cvt_pk_bf16_f32 v67, v46, v47\n\t" \
  "v_smfmac_f32_16x16x64_bf16 v[88:91], v[96:99], v[152:159], v106\n\t" \
  "v_permlane16_swap_b32 v64, v66\n\t" \
  "v_cvt_pk_bf16_f32 v68, v80, v81\n\t" \
  "v_smfmac_f32_16x16x64_bf16 v[92:95], v[96:99], v[184:191], v106\n\t" \
  "v_permlane16_swap_b32 v65, v67\n\t" \
  "buffer_store_dwordx4 v[64:67], v108, %[ru], s47 offen sc1\n\t" \
  "v_cvt_pk_bf16_f32 v69, v82, v83\n\t" \
  "v_smfmac_f32_16x16x64_bf16 v[88:91], v[52:55], v[160:167], v106\n\t" \
  "v_cvt_pk_bf16_f32 v70, v84, v85\n\t" \
  "v_smfmac_f32_16x16x64_bf16 v[92:95], v[52:55], v[192:199], v106\n\t" \
  "v_cvt_pk_bf16_f32 v71, v86, v87\n\t" \
  "v_smfmac_f32_16x16x64_bf16 v[88:91], v[56:59], v[168:175], v106\n\t" \
  "ds_read_b128 v[16:19], v103 offset:49152\n\t" \
  "v_permlane16_swap_b32 v68, v70\n\t" \
  "s_waitcnt lgkmcnt(3)\n\t" \
  "v_smfmac_f32_16x16x64_bf16 v[92:95], v[56:59], v[0:7], v106\n\t" \
  "ds_read_b128 v[20:23], v103 offset:50176\n\t" \
  "v_permlane16_swap_b32 v69, v71\n\t" \
  "buffer_store_dwordx4 v[68:71], v108, %[ru], s76 offen sc1\n\t" \
  "v_smfmac_f32_16x16x64_bf16 v[88:91], v[60:63], v[176:183], v106\n\t" \
  "ds_read_b128 v[24:27], v103 offset:51200\n\t" \
  "s_waitcnt lgkmcnt(3)\n\t" \
  "v_smfmac_f32_16x16x64_bf16 v[92:95], v[60:63], v[8:15], v106\n\t" \
  "ds_read_b128 v[28:31], v103 offset:52224\n\t" \
  "v_smfmac_f32_16x16x64_bf16 v[32:35], v[96:99], v[200:207], v106\n\t" \
  "v_cvt_pk_bf16_f32 v72, v88, v89\n\t" \
  "v_smfmac_f32_16x16x64_bf16 v[36:39], v[96:99], v[232:239], v106\n\t" \
  "v_cvt_pk_bf16_f32 v73, v90, v91\n\t" \
  "v_smfmac_f32_16x16x64_bf16 v[32:35], v[52:55], v[208:215], v106\n\t" \
  "v_cvt_pk_bf16_f32 v74, v92, v93\n\t" \
  "v_smfmac_f32_16x16x64_bf16 v[36:39], v[52:55], v[240:247], v106\n\t" \
  "v_cvt_pk_bf16_f32 v75, v94, v95\n\t" \
  "v_smfmac_f32_16x16x64_bf16 v[32:35], v[56:59], v[216:223], v106\n\t" \
  "v_permlane16_swap_b32 v72, v74\n\t" \
  "v_smfmac_f32_16x16x64_bf16 v[32:35], v[60:63], v[224:231], v106\n\t" \
  "v_permlane16_swap_b32 v73, v75\n\t" \
  "buffer_store_dwordx4 v[72:75], v108, %[ru], s77 offen sc1\n\t" \
  "s_waitcnt lgkmcnt(2)\n\t" \
  "v_smfmac_f32_16x16x64_bf16 v[36:39], v[56:59], v[16:23], v106\n\t" \
  "s_waitcnt lgkmcnt(0)\n\t" \
  "v_smfmac_f32_16x16x64_bf16 v[36:39], v[60:63], v[24:31], v106\n\t" \
  "v_cvt_pk_bf16_f32 v76, v32, v33\n\t" \
  "v_cvt_pk_bf16_f32 v77, v34, v35\n\t" \
  "s_nop 5\n\t" \
  "v_cvt_pk_bf16_f32 v78, v36, v37\n\t" \
  "v_cvt_pk_bf16_f32 v79, v38, v39\n\t" \
  "s_nop 0\n\t" \
  "v_permlane16_swap_b32 v76, v78\n\t" \
  "v_permlane16_swap_b32 v77, v79\n\t" \
  "buffer_store_dwordx4 v[76:79], v108, %[ru], s78 offen sc1\n\t" \
  "s_memtime s[76:77]\n\t" \
  "s_mov_b32 s42, 0\n\t" \
  "s_waitcnt lgkmcnt(0)\n\t" \
  "s_mov_b32 s57, s76\n\t" \
  "s_add_i32 s41, s56, 1950\n\t" \
  "s_sub_i32 s76, s92, s51\n\t" \
  "s_sub_i32 s77, s93, s53\n\t" \
  "s_cmp_eq_u32 s76, 0\n\t" \
  "s_cbranch_scc1 21f\n\t" \
  "v_cvt_f32_u32 v117, s76\n\t" \
  "v_cvt_f32_u32 v116, s77\n\t" \
  "v_rcp_f32 v117, v117\n\t" \
  "s_mov_b32 s51, s92\n\t" \
  "v_mul_f32 v117, v117, v116\n\t" \
  "v_max_f32 v117, 0x3f028f5c, v117\n\t" \
  "v_min_f32 v117, 0x3ffeb852, v117\n\t" \
  "v_rcp_f32 v116, v117\n\t" \
  "v_mul_f32 v117, 0x4f000000, v117\n\t" \
  "v_mul_f32 v116, 0x47800000, v116\n\t" \
  "v_cvt_u32_f32 v117, v117\n\t" \
  "v_cvt_u32_f32 v116, v116\n\t" \
  "s_mov_b32 s53, s93\n\t" \
  "v_readfirstlane_b32 s46, v117\n\t" \
  "v_readfirstlane_b32 s52, v116\n\t" \
  "21:\n\t" \
  "s_add_i32 s44, s44, 1\n\t" \
  "s_and_b32 s58, s58, 4\n\t" \
  "s_and_b32 s76, s44, 64\n\t" \
  "s_or_b32 s42, s42, s76\n\t" \
  "s_cbranch_scc0 20b\n\t" \
  "s_branch 99f\n\t" \
  "14:\n\t" \
  "s_sleep 3\n\t" \
  "s_add_u32 s59, s59, 0x10000\n\t" \
  "s_cmp_lt_u32 s59, 0x100000\n\t" \
  "s_cbranch_scc1 13b\n\t" \
  "s_mov_b64 exec, -1\n\t" \
  "s_mov_b32 s42, 16\n\t" \
  "s_branch 99f\n\t" \
  "8:\n\t" \
  "s_bitcmp1_b32 s58, 1\n\t" \
  "s_cbranch_scc1 4b\n\t" \
  "s_bitcmp1_b32 s58, 2\n\t" \
  "s_cbranch_scc1 5b\n\t" \
  "v_max3_u32 v116, v32, v33, v34\n\t" \
  "v_max3_u32 v117, v35, v36, v37\n\t" \
  "v_max3_u32 v116, v116, v38, v39\n\t" \
  "v_max_u32 v116, v116, v117\n\t" \
  "v_cmp_eq_u32 vcc, -1, v116\n\t" \
  "s_cbranch_vccz 5b\n\t" \
  "s_branch 0f\n\t" \
  "9:\n\t" \
  "s_bitcmp1_b32 s58, 2\n\t" \
  "s_cbranch_scc1 6b\n\t" \
  "v_max3_u32 v116, v40, v41, v42\n\t" \
  "v_max3_u32 v117, v43, v44, v45\n\t" \
  "v_max3_u32 v116, v116, v46, v47\n\t" \
  "v_max_u32 v116, v116, v117\n\t" \
  "v_cmp_eq_u32 vcc, -1, v116\n\t" \
  "s_cbranch_vccz 6b\n\t" \
  "s_or_b32 s58, s58, 2\n\t" \
  "0:\n\t" \
  "s_or_b32 s58, s58, 1\n\t" \
  "11:\n\t" \
  "buffer_load_dwordx4 v[32:35], v100, %[rh], s40 offen sc1\n\t" \
  "buffer_load_dwordx4 v[36:39], v100, %[rh], s76 offen sc1\n\t" \
  "buffer_load_dwordx4 v[40:43], v100, %[rh], s77 offen sc1\n\t" \
  "buffer_load_dwordx4 v[44:47], v100, %[rh], s78 offen sc1\n\t" \
  "s_waitcnt lgkmcnt(0)\n\t" \
  "s_branch 10b\n\t" \
  "17:\n\t" \
  "s_add_u32 s59, s59, 1\n\t" \
  "s_bitcmp1_b32 s58, 2\n\t" \
  "s_cbranch_scc1 3b\n\t" \
  "s_waitcnt vmcnt(0)\n\t" \
  "v_max_u32 v116, v32, v33\n\t" \
  "v_max3_u32 v116, v116, v34, v35\n\t" \
  "v_max3_u32 v116, v116, v36, v37\n\t" \
  "v_max3_u32 v116, v116, v38, v39\n\t" \
  "v_max3_u32 v116, v116, v40, v41\n\t" \
  "v_max3_u32 v116, v116, v42, v43\n\t" \
  "v_max3_u32 v116, v116, v44, v45\n\t" \
  "v_max3_u32 v116, v116, v46, v47\n\t" \
  "v_cmp_eq_u32 vcc, -1, v116\n\t" \
  "s_cbranch_vccz 3b\n\t" \
  "s_bitcmp1_b32 s59, 6\n\t" \
  "s_cbranch_scc1 12f\n\t" \
  "buffer_load_dwordx4 v[32:35], v100, %[rh], s40 offen sc1\n\t" \
  "buffer_load_dwordx4 v[36:39], v100, %[rh], s76 offen sc1\n\t" \
  "buffer_load_dwordx4 v[40:43], v100, %[rh], s77 offen sc1\n\t" \
  "buffer_load_dwordx4 v[44:47], v100, %[rh], s78 offen sc1\n\t" \
  "s_branch 17b\n\t" \
  "12:\n\t" \
  "s_mov_b32 s42, s58\n\t" \
  "s_branch 99f\n\t" \
  "91:\n\t" \
  "s_mov_b32 s58, s42\n\t" \
  "20:\n\t" \
  "s_and_b32 s76, s44, 7\n\t" \
  "s_lshl_b32 s76, s76, 15\n\t" \
  "s_add_i32 s40, s54, s76\n\t" \
  "s_and_b32 s76, s44, 3\n\t" \
  "s_lshl_b32 s45, s76, 14\n\t" \
  "s_lshl_b32 s47, s76, 20\n\t" \
  "s_add_i32 s47, s47, s55\n\t" \
  "s_lshr_b32 s76, s47, 21\n\t" \
  "s_bitcmp1_b64 %[sel], s76\n\t" \
  "s_cselect_b32 s76, 0x2000, 0\n\t" \
  "s_add_i32 s47, s47, s76\n\t" \
  "s_add_i32 s49, s44, -1\n\t" \
  "s_and_b32 s48, s49, 2\n\t" \
  "s_lshl_b32 s48, s48, 13\n\t" \
  "s_and_b32 s76, s49, 1\n\t" \
  "s_lshl_b32 s76, s76, 12\n\t" \
  "s_add_i32 s48, s48, s76\n\t" \
  "s_lshl_b32 s50, s53, 8\n\t" \
  "s_mov_b32 s59, 0\n\t" \
  "s_mov_b32 s92, s51\n\t" \
  "s_mov_b32 s93, s53\n\t" \
  "s_xor_b32 s76, s40, 0x400\n\t" \
  "s_xor_b32 s77, s40, 0x800\n\t" \
  "s_xor_b32 s78, s40, 0xc00\n\t" \
  "s_cmp_lg_u32 s58, 0\n\t" \
  "s_cbranch_scc1 11f\n\t" \
  "10:\n\t" \
  "ds_read_b128 v[48:51], v102 offset:16384\n\t" \
  "ds_read_b128 v[52:55], v102 offset:17408\n\t" \
  "ds_read_b128 v[56:59], v102 offset:18432\n\t" \
  "ds_read_b128 v[60:63], v102 offset:19456\n\t" \
  "ds_read_b128 v[0:3], v102 offset:32768\n\t" \
  "ds_read_b128 v[4:7], v102 offset:33792\n\t" \
  "ds_read_b128 v[8:11], v102 offset:34816\n\t" \
  "ds_read_b128 v[12:15], v102 offset:35840\n\t" \
  "ds_read_b128 v[16:19], v102 offset:49152\n\t" \
  "ds_read_b128 v[20:23], v102 offset:50176\n\t" \
  "ds_read_b128 v[24:27], v102 offset:51200\n\t" \
  "ds_read_b128 v[28:31], v102 offset:52224\n\t" \
  "s_waitcnt lgkmcnt(4)\n\t" \
  "ds_read_b128 v[80:83], v102 offset:0\n\t" \
  "ds_read_b128 v[84:87], v102 offset:1024\n\t" \
  "ds_read_b128 v[88:91], v102 offset:2048\n\t" \
  "ds_read_b128 v[92:95], v102 offset:3072\n\t" \
  "s_bitcmp1_b32 s58, 1\n\t" \
  "s_cselect_b64 s[80:81], -1, 0\n\t" \
  "s_cmp_lg_u32 s58, 0\n\t" \
  "s_cbranch_scc1 17f\n\t" \
  "s_mov_b32 exec_hi, 0\n\t" \
  "16:\n\t" \
  "s_memtime vcc\n\t" \
  "s_waitcnt lgkmcnt(0)\n\t" \
  "s_sub_i32 s79, s41, vcc_lo\n\t" \
  "s_cmp_gt_i32 s79, 64\n\t" \
  "s_cbranch_scc1 16b\n\t" \
  "13:\n\t" \
  "buffer_load_dword v116, v112, %[rh], s48 offen sc1\n\t" \
  "s_waitcnt vmcnt(0)\n\t" \
  "15:\n\t" \
  "v_mov_b32 v117, 20000\n\t" \
  "v_cmp_ne_u32_sdwa vcc, v116, s49 src0_sel:BYTE_0 src1_sel:DWORD\n\t" \
  "v_subrev_u32 v116, s50, v116\n\t" \
  "v_ashrrev_i32 v116, 8, v116\n\t" \
  "s_nop 1\n\t" \
  "v_max_i32_dpp v116, v116, v116 quad_perm:[1,0,3,2] row_mask:0xf bank_mask:0xf\n\t" \
  "s_cbranch_vccnz 14f\n\t" \
  "s_nop 0\n\t" \
  "v_max_i32_dpp v116, v116, v116 quad_perm:[2,3,0,1] row_mask:0xf bank_mask:0xf\n\t" \
  "s_nop 1\n\t" \
  "v_max_i32_dpp v116, v116, v116 row_half_mirror row_mask:0xf bank_mask:0xf\n\t" \
  "s_nop 1\n\t" \
  "v_max_i32_dpp v116, v116, v116 row_mirror row_mask:0xf bank_mask:0xf\n\t" \
  "v_med3_i32 v116, v116, 0, v117\n\t" \
  "v_mul_u32_u24 v116, s52, v116\n\t" \
  "v_lshrrev_b32 v116, 16, v116\n\t" \
  "s_mov_b64 exec, -1\n\t" \
  "v_readfirstlane_b32 s84, v116\n\t" \
  "v_readlane_b32 s85, v116, 16\n\t" \
  "s_max_u32 s84, s84, s85\n\t" \
  "s_add_i32 s84, s84, s51\n\t" \
  "1:\n\t" \
  "s_memtime vcc\n\t" \
  "s_waitcnt lgkmcnt(0)\n\t" \
  "s_sub_i32 s79, s84, vcc_lo\n\t" \
  "s_cmp_gt_i32 s79, 128\n\t" \
  "s_cbranch_scc1 1b\n\t" \
  ".p2align 4\n\t" \
  "2:\n\t" \
  "s_sub_i32 s79, s79, 33\n\t" \
  "s_cmp_gt_i32 s79, 56\n\t" \
  "s_cbranch_scc1 2b\n\t" \
  "buffer_load_dwordx4 v[32:35], v100, %[rh], s40 offen sc1\n\t" \
  "s_nop 12\n\t" \
  "buffer_load_dwordx4 v[36:39], v100, %[rh], s76 offen sc1\n\t" \
  "s_nop 12\n\t" \
  "buffer_load_dwordx4 v[40:43], v100, %[rh], s77 offen sc1\n\t" \
  "s_nop 12\n\t" \
  "buffer_load_dwordx4 v[44:47], v100, %[rh], s78 offen sc1\n\t" \
  "s_memtime vcc\n\t" \
  "s_memrealtime s[82:83]\n\t" \
  "s_nop 4\n\t" \
  "s_memrealtime s[84:85]\n\t" \
  "s_nop 4\n\t" \
  "s_memrealtime s[56:57]\n\t" \
  "s_nop 4\n\t" \
  "s_memrealtime s[86:87]\n\t" \
  "s_nop 4\n\t" \
  "s_memrealtime s[88:89]\n\t" \
  "s_nop 4\n\t" \
  "s_memrealtime s[90:91]\n\t" \
  "s_mov_b32 s92, 0\n\t" \
  "s_waitcnt lgkmcnt(0)\n\t" \
  "s_cmp_eq_u32 s82, s84\n\t" \
  "s_addc_u32 s92, s92, 0\n\t" \
  "s_cmp_eq_u32 s82, s56\n\t" \
  "s_addc_u32 s92, s92, 0\n\t" \
  "s_cmp_eq_u32 s82, s86\n\t" \
  "s_addc_u32 s92, s92, 0\n\t" \
  "s_cmp_eq_u32 s82, s88\n\t" \
  "s_addc_u32 s92, s92, 0\n\t" \
  "s_cmp_eq_u32 s82, s90\n\t" \
  "s_addc_u32 s92, s92, 0\n\t" \
  "s_mul_i32 s92, s92, 24\n\t" \
  "s_add_i32 s92, s92, vcc_lo\n\t" \
  "s_lshl4_add_u32 s93, s82, 64\n\t" \
  "3:\n\t" \
  "v_mov_b64 v[72:73], 0\n\t" \
  "v_mov_b64 v[74:75], 0\n\t" \
  "v_mov_b64 v[76:77], 0\n\t" \
  "v_mov_b64 v[78:79], 0\n\t" \
  "s_waitcnt lgkmcnt(0)\n\t" \
  "s_waitcnt vmcnt(3)\n\t" \
  "v_smfmac_f32_16x16x64_bf16 v[72:75], v[32:35], v[48:55], v104\n\t" \
  "ds_write_b128 v101, v[32:35] offset:4096\n\t" \
  "v_smfmac_f32_16x16x64_bf16 v[76:79], v[32:35], v[56:63], v104\n\t" \
  "s_waitcnt vmcnt(2)\n\t" \
  "ds_write_b128 v101, v[36:39] offset:5120\n\t" \
  "v_smfmac_f32_16x16x64_bf16 v[72:75], v[36:39], a[48:55], v104\n\t" \
  "v_smfmac_f32_16x16x64_bf16 v[76:79], v[36:39], a[56:63], v104\n\t" \
  "s_nop 3\n\t" \
  "v_cmp_u_f32 vcc, v72, v73\n\t" \
  "v_cmp_u_f32 s[82:83], v74, v75\n\t" \
  "s_waitcnt vmcnt(1)\n\t" \
  "v_smfmac_f32_16x16x64_bf16 v[72:75], v[40:43], a[64:71], v104\n\t" \
  "s_or_b64 vcc, vcc, s[82:83]\n\t" \
  "s_or_b64 vcc, vcc, s[80:81]\n\t" \
  "v_smfmac_f32_16x16x64_bf16 v[76:79], v[40:43], a[72:79], v104\n\t" \
  "s_waitcnt lgkmcnt(0)\n\t" \
  "s_cbranch_scc1 8f\n\t" \
  "5:\n\t" \
  "s_barrier\n\t" \
  "4:\n\t" \
  "s_memtime s[56:57]\n\t" \
  "ds_read_b128 v[48:51], v101 offset:8192\n\t" \
  "ds_read_b128 v[52:55], v101 offset:9216\n\t" \
  "ds_read_b128 v[56:59], v101 offset:13312\n\t" \
  "s_waitcnt vmcnt(0)\n\t" \
  "v_smfmac_f32_16x16x64_bf16 v[72:75], v[44:47], a[80:87], v104\n\t" \
  "ds_read_b128 v[60:63], v101 offset:12288\n\t" \
  "ds_read_b128 v[64:67], v101 offset:1024\n\t" \
  "v_smfmac_f32_16x16x64_bf16 v[76:79], v[44:47], a[88:95], v104\n\t" \
  "ds_read_b128 v[68:71], v101 offset:0\n\t" \
  "ds_write_b128 v101, v[40:43] offset:6144\n\t" \
  "s_nop 1\n\t" \
  "v_cmp_u_f32 vcc, v72, v73\n\t" \
  "v_cmp_u_f32 s[82:83], v74, v75\n\t" \
  "s_waitcnt lgkmcnt(6)\n\t" \
  "v_smfmac_f32_16x16x64_bf16 v[72:75], v[48:51], v[0:7], v104\n\t" \
  "s_or_b64 vcc, vcc, s[82:83]\n\t" \
  "v_smfmac_f32_16x16x64_bf16 v[76:79], v[48:51], v[8:15], v104\n\t" \
  "s_cbranch_scc1 9f\n\t" \
  "6:\n\t" \
  "s_waitcnt lgkmcnt(5)\n\t" \
  "v_smfmac_f32_16x16x64_bf16 v[72:75], v[52:55], a[96:103], v104\n\t" \
  "ds_write_b128 v101, v[44:47] offset:7168\n\t" \
  "v_smfmac_f32_16x16x64_bf16 v[76:79], v[52:55], a[104:111], v104\n\t" \
  "v_mov_b64 v[40:41], 0\n\t" \
  "s_waitcnt lgkmcnt(5)\n\t" \
  "v_smfmac_f32_16x16x64_bf16 v[72:75], v[56:59], a[144:151], v104\n\t" \
  "v_mov_b64 v[42:43], 0\n\t" \
  "v_smfmac_f32_16x16x64_bf16 v[76:79], v[56:59], a[152:159], v104\n\t" \
  "s_waitcnt lgkmcnt(4)\n\t" \
  "v_smfmac_f32_16x16x64_bf16 v[72:75], v[60:63], v[16:23], v104\n\t" \
  "s_waitcnt lgkmcnt(0)\n\t" \
  "s_barrier\n\t" \
  "v_smfmac_f32_16x16x64_bf16 v[76:79], v[60:63], v[24:31], v104\n\t" \
  "ds_read_b128 v[48:51], v101 offset:10240\n\t" \
  "ds_read_b128 v[52:55], v101 offset:14336\n\t" \
  "v_smfmac_f32_16x16x64_bf16 v[72:75], v[64:67], a[0:7], v104\n\t" \
  "ds_read_b128 v[56:59], v101 offset:2048\n\t" \
  "ds_read_b128 v[60:63], v101 offset:11264\n\t" \
  "v_smfmac_f32_16x16x64_bf16 v[76:79], v[64:67], a[8:15], v104\n\t" \
  "ds_read_b128 v[32:35], v101 offset:15360\n\t" \
  "ds_read_b128 v[36:39], v101 offset:3072\n\t" \
  "v_smfmac_f32_16x16x64_bf16 v[72:75], v[68:71], v[80:87], v104\n\t" \
  "ds_read_b128 v[0:3], v103 offset:0\n\t" \
  "v_mov_b64 v[44:45], 0\n\t" \
  "v_smfmac_f32_16x16x64_bf16 v[76:79], v[68:71], v[88:95], v104\n\t" \
  "v_mov_b64 v[46:47], 0\n\t" \
  "s_waitcnt lgkmcnt(5)\n\t" \
  "v_smfmac_f32_16x16x64_bf16 v[72:75], v[48:51], a[112:119], v104\n\t" \
  "ds_read_b128 v[4:7], v103 offset:1024\n\t" \
  "v_mov_b64 v[80:81], 0\n\t" \
  "v_smfmac_f32_16x16x64_bf16 v[72:75], v[52:55], a[160:167], v104\n\t" \
  "v_mov_b64 v[82:83], 0\n\t" \
  "s_waitcnt lgkmcnt(2)\n\t" \
  "v_smfmac_f32_16x16x64_bf16 v[72:75], v[56:59], a[16:23], v104\n\t" \
  "ds_read_b128 v[8:11], v103 offset:2048\n\t" \
  "v_mov_b64 v[84:85], 0\n\t" \
  "v_smfmac_f32_16x16x64_bf16 v[72:75], v[60:63], a[128:135], v104\n\t" \
  "ds_read_b128 v[12:15], v103 offset:3072\n\t" \
  "v_mov_b64 v[86:87], 0\n\t" \
  "v_smfmac_f32_16x16x64_bf16 v[72:75], v[32:35], a[176:183], v104\n\t" \
  "ds_read_b128 v[16:19], v103 offset:16384\n\t" \
  "v_mov_b64 v[88:89], 0\n\t" \
  "v_smfmac_f32_16x16x64_bf16 v[72:75], v[36:39], a[32:39], v104\n\t" \
  "ds_read_b128 v[20:23], v103 offset:17408\n\t" \
  "v_mov_b64 v[90:91], 0\n\t" \
  "v_smfmac_f32_16x16x64_bf16 v[76:79], v[48:51], a[120:127], v104\n\t" \
  "ds_read_b128 v[24:27], v103 offset:18432\n\t" \
  "v_mov_b64 v[92:93], 0\n\t" \
  "v_smfmac_f32_16x16x64_bf16 v[76:79], v[52:55], a[168:175], v104\n\t" \
  "v_add_f32 v96, v72, v74\n\t" \
  "v_add_f32 v97, v73, v75\n\t" \
  "v_cvt_pk_bf16_f32 v96, v96, v97\n\t" \
  "ds_write_b32 v105, v96\n\t" \
  "v_smfmac_f32_16x16x64_bf16 v[76:79], v[56:59], a[24:31], v104\n\t" \
  "ds_read_b128 v[28:31], v103 offset:19456\n\t" \
  "v_mov_b64 v[94:95], 0\n\t" \
  "v_smfmac_f32_16x16x64_bf16 v[76:79], v[60:63], a[136:143], v104\n\t" \
  "v_smfmac_f32_16x16x64_bf16 v[76:79], v[32:35], a[184:191], v104\n\t" \
  "v_smfmac_f32_16x16x64_bf16 v[76:79], v[36:39], a[40:47], v104\n\t" \
  "s_waitcnt lgkmcnt(1)\n\t" \
  "s_barrier\n\t" \
  "ds_read_b64_tr_b16 v[96:97], v107 offset:0\n\t" \
  "ds_read_b64_tr_b16 v[98:99], v107 offset:64\n\t" \
  "ds_read_b64_tr_b16 v[56:57], v107 offset:1024\n\t" \
  "ds_read_b64_tr_b16 v[58:59], v107 offset:1088\n\t" \
  "s_nop 1\n\t" \
  "v_add_f32 v116, v76, v78\n\t" \
  "v_add_f32 v117, v77, v79\n\t" \
  "v_cvt_pk_bf16_f32 v116, v116, v117\n\t" \
  "ds_write_b32 v105, v116 offset:256\n\t" \
  "s_waitcnt lgkmcnt(3)\n\t" \
  "v_smfmac_f32_16x16x64_bf16 v[40:43], v[96:99], a[192:199], v106\n\t" \
  "v_smfmac_f32_16x16x64_bf16 v[44:47], v[96:99], a[224:231], v106\n\t" \
  "s_waitcnt lgkmcnt(0)\n\t" \
  "s_barrier\n\t" \
  "ds_read_b64_tr_b16 v[52:53], v107 offset:256\n\t" \
  "ds_read_b64_tr_b16 v[54:55], v107 offset:320\n\t" \
  "v_smfmac_f32_16x16x64_bf16 v[40:43], v[56:59], a[208:215], v106\n\t" \
  "ds_read_b64_tr_b16 v[60:61], v107 offset:1280\n\t" \
  "ds_read_b64_tr_b16 v[62:63], v107 offset:1344\n\t" \
  "v_smfmac_f32_16x16x64_bf16 v[44:47], v[56:59], v[0:7], v106\n\t" \
  "v_smfmac_f32_16x16x64_bf16 v[80:83], v[96:99], a[240:247], v106\n\t" \
  "v_mov_b64 v[32:33], 0\n\t" \
  "v_smfmac_f32_16x16x64_bf16 v[84:87], v[96:99], v[136:143], v106\n\t" \
  "v_mov_b64 v[34:35], 0\n\t" \
  "v_smfmac_f32_16x16x64_bf16 v[80:83], v[56:59], v[120:127], v106\n\t" \
  "v_mov_b64 v[36:37], 0\n\t" \
  "v_smfmac_f32_16x16x64_bf16 v[84:87], v[56:59], v[16:23], v106\n\t" \
  "s_waitcnt lgkmcnt(2)\n\t" \
  "v_smfmac_f32_16x16x64_bf16 v[40:43], v[52:55], a[200:207], v106\n\t" \
  "v_smfmac_f32_16x16x64_bf16 v[44:47], v[52:55], a[232:239], v106\n\t" \
  "s_xor_b32 s76, s47, 0x40000\n\t" \
  "s_waitcnt lgkmcnt(0)\n\t" \
  "v_smfmac_f32_16x16x64_bf16 v[40:43], v[60:63], a[216:223], v106\n\t" \
  "v_mov_b64 v[38:39], 0\n\t" \
  "s_xor_b32 s77, s47, 0x80000\n\t" \
  "v_smfmac_f32_16x16x64_bf16 v[44:47], v[60:63], v[8:15], v106\n\t" \
  "s_xor_b32 s78, s47, 0xc0000\n\t" \
  "v_smfmac_f32_16x16x64_bf16 v[80:83], v[52:55], a[248:255], v106\n\t" \
  "ds_read_b128 v[0:3], v103 offset:32768\n\t" \
  "v_cvt_pk_bf16_f32 v64, v40, v41\n\t" \
  "v_smfmac_f32_16x16x64_bf16 v[84:87], v[52:55], v[144:151], v106\n\t" \
  "ds_read_b128 v[4:7], v103 offset:33792\n\t" \
  "v_cvt_pk_bf16_f32 v65, v42, v43\n\t" \
  "v_smfmac_f32_16x16x64_bf16 v[80:83], v[60:63], v[128:135], v106\n\t" \
  "ds_read_b128 v[8:11], v103 offset:34816\n\t" \
  "v_cvt_pk_bf16_f32 v66, v44, v45\n\t" \
  "v_smfmac_f32_16x16x64_bf16 v[84:87], v[60:63], v[24:31], v106\n\t" \
  "ds_read_b128 v[12:15], v103 offset:35840\n\t" \
  "v_cvt_pk_bf16_f32 v67, v46, v47\n\t" \
  "v_smfmac_f32_16x16x64_bf16 v[88:91], v[96:99], v[152:159], v106\n\t" \
  "v_permlane16_swap_b32 v64, v66\n\t" \
  "v_cvt_pk_bf16_f32 v68, v80, v81\n\t" \
  "v_smfmac_f32_16x16x64_bf16 v[92:95], v[96:99], v[184:191], v106\n\t" \
  "v_permlane16_swap_b32 v65, v67\n\t" \
  "v_cvt_pk_bf16_f32 v69, v82, v83\n\t" \
  "v_smfmac_f32_16x16x64_bf16 v[88:91], v[52:55], v[160:167], v106\n\t" \
  "buffer_store_dwordx4 v[64:67], v108, %[ru], s47 offen sc1\n\t" \
  "v_cvt_pk_bf16_f32 v70, v84, v85\n\t" \
  "v_smfmac_f32_16x16x64_bf16 v[92:95], v[52:55], v[192:199], v106\n\t" \
  "v_cvt_pk_bf16_f32 v71, v86, v87\n\t" \
  "v_smfmac_f32_16x16x64_bf16 v[88:91], v[56:59], v[168:175], v106\n\t" \
  "ds_read_b128 v[16:19], v103 offset:49152\n\t" \
  "v_permlane16_swap_b32 v68, v70\n\t" \
  "s_waitcnt lgkmcnt(3)\n\t" \
  "v_smfmac_f32_16x16x64_bf16 v[92:95], v[56:59], v[0:7], v106\n\t" \
  "ds_read_b128 v[20:23], v103 offset:50176\n\t" \
  "v_permlane16_swap_b32 v69, v71\n\t" \
  "v_smfmac_f32_16x16x64_bf16 v[88:91], v[60:63], v[176:183], v106\n\t" \
  "ds_read_b128 v[24:27], v103 offset:51200\n\t" \
  "buffer_store_dwordx4 v[68:71], v108, %[ru], s76 offen sc1\n\t" \
  "s_waitcnt lgkmcnt(3)\n\t" \
  "v_smfmac_f32_16x16x64_bf16 v[92:95], v[60:63], v[8:15], v106\n\t" \
  "ds_read_b128 v[28:31], v103 offset:52224\n\t" \
  "v_smfmac_f32_16x16x64_bf16 v[32:35], v[96:99], v[200:207], v106\n\t" \
  "v_cvt_pk_bf16_f32 v72, v88, v89\n\t" \
  "v_smfmac_f32_16x16x64_bf16 v[36:39], v[96:99], v[232:239], v106\n\t" \
  "v_cvt_pk_bf16_f32 v73, v90, v91\n\t" \
  "v_smfmac_f32_16x16x64_bf16 v[32:35], v[52:55], v[208:215], v106\n\t" \
  "v_cvt_pk_bf16_f32 v74, v92, v93\n\t" \
  "v_smfmac_f32_16x16x64_bf16 v[36:39], v[52:55], v[240:247], v106\n\t" \
  "v_cvt_pk_bf16_f32 v75, v94, v95\n\t" \
  "v_smfmac_f32_16x16x64_bf16 v[32:35], v[56:59], v[216:223], v106\n\t" \
  "v_permlane16_swap_b32 v72, v74\n\t" \
  "v_smfmac_f32_16x16x64_bf16 v[32:35], v[60:63], v[224:231], v106\n\t" \
  "v_permlane16_swap_b32 v73, v75\n\t" \
  "s_waitcnt lgkmcnt(2)\n\t" \
  "v_smfmac_f32_16x16x64_bf16 v[36:39], v[56:59], v[16:23], v106\n\t" \
  "buffer_store_dwordx4 v[72:75], v108, %[ru], s77 offen sc1\n\t" \
  "s_waitcnt lgkmcnt(0)\n\t" \
  "v_smfmac_f32_16x16x64_bf16 v[36:39], v[60:63], v[24:31], v106\n\t" \
  "v_cvt_pk_bf16_f32 v76, v32, v33\n\t" \
  "v_cvt_pk_bf16_f32 v77, v34, v35\n\t" \
  "s_nop 5\n\t" \
  "v_cvt_pk_bf16_f32 v78, v36, v37\n\t" \
  "v_cvt_pk_bf16_f32 v79, v38, v39\n\t" \
  "s_nop 0\n\t" \
  "v_permlane16_swap_b32 v76, v78\n\t" \
  "v_permlane16_swap_b32 v77, v79\n\t" \
  "buffer_store_dwordx4 v[76:79], v108, %[ru], s78 offen sc1\n\t" \
  "s_memtime s[76:77]\n\t" \
  "s_mov_b32 s42, 0\n\t" \
  "s_waitcnt lgkmcnt(0)\n\t" \
  "s_mov_b32 s57, s76\n\t" \
  "s_add_i32 s41, s56, 1950\n\t" \
  "s_sub_i32 s76, s92, s51\n\t" \
  "s_sub_i32 s77, s93, s53\n\t" \
  "s_cmp_eq_u32 s76, 0\n\t" \
  "s_cbranch_scc1 21f\n\t" \
  "v_cvt_f32_u32 v117, s76\n\t" \
  "v_cvt_f32_u32 v116, s77\n\t" \
  "v_rcp_f32 v117, v117\n\t" \
  "s_mov_b32 s51, s92\n\t" \
  "v_mul_f32 v117, v117, v116\n\t" \
  "v_max_f32 v117, 0x3f028f5c, v117\n\t" \
  "v_min_f32 v117, 0x3ffeb852, v117\n\t" \
  "v_rcp_f32 v116, v117\n\t" \
  "v_mul_f32 v117, 0x4f000000, v117\n\t" \
  "v_mul_f32 v116, 0x47800000, v116\n\t" \
  "v_cvt_u32_f32 v117, v117\n\t" \
  "v_cvt_u32_f32 v116, v116\n\t" \
  "s_mov_b32 s53, s93\n\t" \
  "v_readfirstlane_b32 s46, v117\n\t" \
  "v_readfirstlane_b32 s52, v116\n\t" \
  "21:\n\t" \
  "s_add_i32 s44, s44, 1\n\t" \
  "s_and_b32 s58, s58, 4\n\t" \
  "s_and_b32 s76, s44, 64\n\t" \
  "s_or_b32 s42, s42, s76\n\t" \
  "s_cbranch_scc0 20b\n\t" \
  "s_branch 99f\n\t" \
  "14:\n\t" \
  "s_sleep 3\n\t" \
  "s_add_u32 s59, s59, 0x10000\n\t" \
  "s_cmp_lt_u32 s59, 0x100000\n\t" \
  "s_cbranch_scc1 13b\n\t" \
  "s_mov_b64 exec, -1\n\t" \
  "s_mov_b32 s42, 16\n\t" \
  "s_branch 99f\n\t" \
  "8:\n\t" \
  "s_bitcmp1_b32 s58, 1\n\t" \
  "s_cbranch_scc1 4b\n\t" \
  "s_bitcmp1_b32 s58, 2\n\t" \
  "s_cbranch_scc1 5b\n\t" \
  "v_max3_u32 v116, v32, v33, v34\n\t" \
  "v_max3_u32 v117, v35, v36, v37\n\t" \
  "v_max3_u32 v116, v116, v38, v39\n\t" \
  "v_max_u32 v116, v116, v117\n\t" \
  "v_cmp_eq_u32 vcc, -1, v116\n\t" \
  "s_cbranch_vccz 5b\n\t" \
  "s_branch 0f\n\t" \
  "9:\n\t" \
  "s_bitcmp1_b32 s58, 2\n\t" \
  "s_cbranch_scc1 6b\n\t" \
  "v_max3_u32 v116, v40, v41, v42\n\t" \
  "v_max3_u32 v117, v43, v44, v45\n\t" \
  "v_max3_u32 v116, v116, v46, v47\n\t" \
  "v_max_u32 v116, v116, v117\n\t" \
  "v_cmp_eq_u32 vcc, -1, v116\n\t" \
  "s_cbranch_vccz 6b\n\t" \
  "s_or_b32 s58, s58, 2\n\t" \
  "0:\n\t" \
  "s_or_b32 s58, s58, 1\n\t" \
  "11:\n\t" \
  "buffer_load_dwordx4 v[32:35], v100, %[rh], s40 offen sc1\n\t" \
  "buffer_load_dwordx4 v[36:39], v100, %[rh], s76 offen sc1\n\t" \
  "buffer_load_dwordx4 v[40:43], v100, %[rh], s77 offen sc1\n\t" \
  "buffer_load_dwordx4 v[44:47], v100, %[rh], s78 offen sc1\n\t" \
  "s_waitcnt lgkmcnt(0)\n\t" \
  "s_branch 10b\n\t" \
  "17:\n\t" \
  "s_add_u32 s59, s59, 1\n\t" \
  "s_bitcmp1_b32 s58, 2\n\t" \
  "s_cbranch_scc1 3b\n\t" \
  "s_waitcnt vmcnt(0)\n\t" \
  "v_max_u32 v116, v32, v33\n\t" \
  "v_max3_u32 v116, v116, v34, v35\n\t" \
  "v_max3_u32 v116, v116, v36, v37\n\t" \
  "v_max3_u32 v116, v116, v38, v39\n\t" \
  "v_max3_u32 v116, v116, v40, v41\n\t" \
  "v_max3_u32 v116, v116, v42, v43\n\t" \
  "v_max3_u32 v116, v116, v44, v45\n\t" \
  "v_max3_u32 v116, v116, v46, v47\n\t" \
  "v_cmp_eq_u32 vcc, -1, v116\n\t" \
  "s_cbranch_vccz 3b\n\t" \
  "s_bitcmp1_b32 s59, 6\n\t" \
  "s_cbranch_scc1 12f\n\t" \
  "buffer_load_dwordx4 v[32:35], v100, %[rh], s40 offen sc1\n\t" \
  "buffer_load_dwordx4 v[36:39], v100, %[rh], s76 offen sc1\n\t" \
  "buffer_load_dwordx4 v[40:43], v100, %[rh], s77 offen sc1\n\t" \
  "buffer_load_dwordx4 v[44:47], v100, %[rh], s78 offen sc1\n\t" \
  "s_branch 17b\n\t" \
  "12:\n\t" \
  "s_mov_b32 s42, s58\n\t" \
  "s_branch 99f\n\t" \
  "92:\n\t" \
  "s_mov_b32 s58, s42\n\t" \
  "20:\n\t" \
  "s_and_b32 s76, s44, 7\n\t" \
  "s_lshl_b32 s76, s76, 15\n\t" \
  "s_add_i32 s40, s54, s76\n\t" \
  "s_and_b32 s76, s44, 3\n\t" \
  "s_lshl_b32 s45, s76, 14\n\t" \
  "s_lshl_b32 s47, s76, 20\n\t" \
  "s_add_i32 s47, s47, s55\n\t" \
  "s_lshr_b32 s76, s47, 21\n\t" \
  "s_bitcmp1_b64 %[sel], s76\n\t" \
  "s_cselect_b32 s76, 0x2000, 0\n\t" \
  "s_add_i32 s47, s47, s76\n\t" \
  "s_add_i32 s49, s44, -1\n\t" \
  "s_and_b32 s48, s49, 2\n\t" \
  "s_lshl_b32 s48, s48, 13\n\t" \
  "s_and_b32 s76, s49, 1\n\t" \
  "s_lshl_b32 s76, s76, 12\n\t" \
  "s_add_i32 s48, s48, s76\n\t" \
  "s_lshl_b32 s50, s53, 8\n\t" \
  "s_mov_b32 s59, 0\n\t" \
  "s_mov_b32 s92, s51\n\t" \
  "s_mov_b32 s93, s53\n\t" \
  "s_xor_b32 s76, s40, 0x400\n\t" \
  "s_xor_b32 s77, s40, 0x800\n\t" \
  "s_xor_b32 s78, s40, 0xc00\n\t" \
  "s_cmp_lg_u32 s58, 0\n\t" \
  "s_cbranch_scc1 11f\n\t" \
  "10:\n\t" \
  "ds_read_b128 v[48:51], v102 offset:32768\n\t" \
  "ds_read_b128 v[52:55], v102 offset:33792\n\t" \
  "ds_read_b128 v[56:59], v102 offset:34816\n\t" \
  "ds_read_b128 v[60:63], v102 offset:35840\n\t" \
  "ds_read_b128 v[0:3], v102 offset:49152\n\t" \
  "ds_read_b128 v[4:7], v102 offset:50176\n\t" \
  "ds_read_b128 v[8:11], v102 offset:51200\n\t" \
  "ds_read_b128 v[12:15], v102 offset:52224\n\t" \
  "ds_read_b128 v[16:19], v102 offset:0\n\t" \
  "ds_read_b128 v[20:23], v102 offset:1024\n\t" \
  "ds_read_b128 v[24:27], v102 offset:2048\n\t" \
  "ds_read_b128 v[28:31], v102 offset:3072\n\t" \
  "s_waitcnt lgkmcnt(4)\n\t" \
  "ds_read_b128 v[80:83], v102 offset:16384\n\t" \
  "ds_read_b128 v[84:87], v102 offset:17408\n\t" \
  "ds_read_b128 v[88:91], v102 offset:18432\n\t" \
  "ds_read_b128 v[92:95], v102 offset:19456\n\t" \
  "s_bitcmp1_b32 s58, 1\n\t" \
  "s_cselect_b64 s[80:81], -1, 0\n\t" \
  "s_cmp_lg_u32 s58, 0\n\t" \
  "s_cbranch_scc1 17f\n\t" \
  "s_mov_b32 exec_hi, 0\n\t" \
  "16:\n\t" \
  "s_memtime vcc\n\t" \
  "s_waitcnt lgkmcnt(0)\n\t" \
  "s_sub_i32 s79, s41, vcc_lo\n\t" \
  "s_cmp_gt_i32 s79, 64\n\t" \
  "s_cbranch_scc1 16b\n\t" \
  "13:\n\t" \
  "buffer_load_dword v116, v112, %[rh], s48 offen sc1\n\t" \
  "s_waitcnt vmcnt(0)\n\t" \
  "15:\n\t" \
  "v_mov_b32 v117, 20000\n\t" \
  "v_cmp_ne_u32_sdwa vcc, v116, s49 src0_sel:BYTE_0 src1_sel:DWORD\n\t" \
  "v_subrev_u32 v116, s50, v116\n\t" \
  "v_ashrrev_i32 v116, 8, v116\n\t" \
  "s_nop 1\n\t" \
  "v_max_i32_dpp v116, v116, v116 quad_perm:[1,0,3,2] row_mask:0xf bank_mask:0xf\n\t" \
  "s_cbranch_vccnz 14f\n\t" \
  "s_nop 0\n\t" \
  "v_max_i32_dpp v116, v116, v116 quad_perm:[2,3,0,1] row_mask:0xf bank_mask:0xf\n\t" \
  "s_nop 1\n\t" \
  "v_max_i32_dpp v116, v116, v116 row_half_mirror row_mask:0xf bank_mask:0xf\n\t" \
  "s_nop 1\n\t" \
  "v_max_i32_dpp v116, v116, v116 row_mirror row_mask:0xf bank_mask:0xf\n\t" \
  "v_med3_i32 v116, v116, 0, v117\n\t" \
  "v_mul_u32_u24 v116, s52, v116\n\t" \
  "v_lshrrev_b32 v116, 16, v116\n\t" \
  "s_mov_b64 exec, -1\n\t" \
  "v_readfirstlane_b32 s84, v116\n\t" \
  "v_readlane_b32 s85, v116, 16\n\t" \
  "s_max_u32 s84, s84, s85\n\t" \
  "s_add_i32 s84, s84, s51\n\t" \
  "1:\n\t" \
  "s_memtime vcc\n\t" \
  "s_waitcnt lgkmcnt(0)\n\t" \
  "s_sub_i32 s79, s84, vcc_lo\n\t" \
  "s_cmp_gt_i32 s79, 128\n\t" \
  "s_cbranch_scc1 1b\n\t" \
  ".p2align 4\n\t" \
  "2:\n\t" \
  "s_sub_i32 s79, s79, 33\n\t" \
  "s_cmp_gt_i32 s79, 56\n\t" \
  "s_cbranch_scc1 2b\n\t" \
  "buffer_load_dwordx4 v[32:35], v100, %[rh], s40 offen sc1\n\t" \
  "s_nop 12\n\t" \
  "buffer_load_dwordx4 v[36:39], v100, %[rh], s76 offen sc1\n\t" \
  "s_nop 12\n\t" \
  "buffer_load_dwordx4 v[40:43], v100, %[rh], s77 offen sc1\n\t" \
  "s_nop 12\n\t" \
  "buffer_load_dwordx4 v[44:47], v100, %[rh], s78 offen sc1\n\t" \
  "s_memtime vcc\n\t" \
  "s_memrealtime s[82:83]\n\t" \
  "s_nop 4\n\t" \
  "s_memrealtime s[84:85]\n\t" \
  "s_nop 4\n\t" \
  "s_memrealtime s[56:57]\n\t" \
  "s_nop 4\n\t" \
  "s_memrealtime s[86:87]\n\t" \
  "s_nop 4\n\t" \
  "s_memrealtime s[88:89]\n\t" \
  "s_nop 4\n\t" \
  "s_memrealtime s[90:91]\n\t" \
  "s_mov_b32 s92, 0\n\t" \
  "s_waitcnt lgkmcnt(0)\n\t" \
  "s_cmp_eq_u32 s82, s84\n\t" \
  "s_addc_u32 s92, s92, 0\n\t" \
  "s_cmp_eq_u32 s82, s56\n\t" \
  "s_addc_u32 s92, s92, 0\n\t" \
  "s_cmp_eq_u32 s82, s86\n\t" \
  "s_addc_u32 s92, s92, 0\n\t" \
  "s_cmp_eq_u32 s82, s88\n\t" \
  "s_addc_u32 s92, s92, 0\n\t" \
  "s_cmp_eq_u32 s82, s90\n\t" \
  "s_addc_u32 s92, s92, 0\n\t" \
  "s_mul_i32 s92, s92, 24\n\t" \
  "s_add_i32 s92, s92, vcc_lo\n\t" \
  "s_lshl4_add_u32 s93, s82, 64\n\t" \
  "3:\n\t" \
  "v_mov_b64 v[72:73], 0\n\t" \
  "v_mov_b64 v[74:75], 0\n\t" \
  "v_mov_b64 v[76:77], 0\n\t" \
  "v_mov_b64 v[78:79], 0\n\t" \
  "s_waitcnt lgkmcnt(0)\n\t" \
  "s_waitcnt vmcnt(3)\n\t" \
  "v_smfmac_f32_16x16x64_bf16 v[72:75], v[32:35], v[48:55], v104\n\t" \
  "ds_write_b128 v101, v[32:35] offset:8192\n\t" \
  "v_smfmac_f32_16x16x64_bf16 v[76:79], v[32:35], v[56:63], v104\n\t" \
  "s_waitcnt vmcnt(2)\n\t" \
  "ds_write_b128 v101, v[36:39] offset:9216\n\t" \
  "v_smfmac_f32_16x16x64_bf16 v[72:75], v[36:39], a[96:103], v104\n\t" \
  "v_smfmac_f32_16x16x64_bf16 v[76:79], v[36:39], a[104:111], v104\n\t" \
  "s_nop 3\n\t" \
  "v_cmp_u_f32 vcc, v72, v73\n\t" \
  "v_cmp_u_f32 s[82:83], v74, v75\n\t" \
  "s_waitcnt vmcnt(1)\n\t" \
  "v_smfmac_f32_16x16x64_bf16 v[72:75], v[40:43], a[112:119], v104\n\t" \
  "s_or_b64 vcc, vcc, s[82:83]\n\t" \
  "s_or_b64 vcc, vcc, s[80:81]\n\t" \
  "v_smfmac_f32_16x16x64_bf16 v[76:79], v[40:43], a[120:127], v104\n\t" \
  "s_waitcnt lgkmcnt(0)\n\t" \
  "s_cbranch_scc1 8f\n\t" \
  "5:\n\t" \
  "s_barrier\n\t" \
  "4:\n\t" \
  "s_memtime s[56:57]\n\t" \
  "ds_read_b128 v[48:51], v101 offset:12288\n\t" \
  "ds_read_b128 v[52:55], v101 offset:13312\n\t" \
  "ds_read_b128 v[56:59], v101 offset:1024\n\t" \
  "s_waitcnt vmcnt(0)\n\t" \
  "v_smfmac_f32_16x16x64_bf16 v[72:75], v[44:47], a[128:135], v104\n\t" \
  "ds_read_b128 v[60:63], v101 offset:0\n\t" \
  "ds_read_b128 v[64:67], v101 offset:5120\n\t" \
  "v_smfmac_f32_16x16x64_bf16 v[76:79], v[44:47], a[136:143], v104\n\t" \
  "ds_read_b128 v[68:71], v101 offset:4096\n\t" \
  "ds_write_b128 v101, v[40:43] offset:10240\n\t" \
  "s_nop 1\n\t" \
  "v_cmp_u_f32 vcc, v72, v73\n\t" \
  "v_cmp_u_f32 s[82:83], v74, v75\n\t" \
  "s_waitcnt lgkmcnt(6)\n\t" \
  "v_smfmac_f32_16x16x64_bf16 v[72:75], v[48:51], v[0:7], v104\n\t" \
  "s_or_b64 vcc, vcc, s[82:83]\n\t" \
  "v_smfmac_f32_16x16x64_bf16 v[76:79], v[48:51], v[8:15], v104\n\t" \
  "s_cbranch_scc1 9f\n\t" \
  "6:\n\t" \
  "s_waitcnt lgkmcnt(5)\n\t" \
  "v_smfmac_f32_16x16x64_bf16 v[72:75], v[52:55], a[144:151], v104\n\t" \
  "ds_write_b128 v101, v[44:47] offset:11264\n\t" \
  "v_smfmac_f32_16x16x64_bf16 v[76:79], v[52:55], a[152:159], v104\n\t" \
  "v_mov_b64 v[40:41], 0\n\t" \
  "s_waitcnt lgkmcnt(5)\n\t" \
  "v_smfmac_f32_16x16x64_bf16 v[72:75], v[56:59], a[0:7], v104\n\t" \
  "v_mov_b64 v[42:43], 0\n\t" \
  "v_smfmac_f32_16x16x64_bf16 v[76:79], v[56:59], a[8:15], v104\n\t" \
  "s_waitcnt lgkmcnt(4)\n\t" \
  "v_smfmac_f32_16x16x64_bf16 v[72:75], v[60:63], v[16:23], v104\n\t" \
  "s_waitcnt lgkmcnt(0)\n\t" \
  "s_barrier\n\t" \
  "v_smfmac_f32_16x16x64_bf16 v[76:79], v[60:63], v[24:31], v104\n\t" \
  "ds_read_b128 v[48:51], v101 offset:14336\n\t" \
  "ds_read_b128 v[52:55], v101 offset:2048\n\t" \
  "v_smfmac_f32_16x16x64_bf16 v[72:75], v[64:67], a[48:55], v104\n\t" \
  "ds_read_b128 v[56:59], v101 offset:6144\n\t" \
  "ds_read_b128 v[60:63], v101 offset:15360\n\t" \
  "v_smfmac_f32_16x16x64_bf16 v[76:79], v[64:67], a[56:63], v104\n\t" \
  "ds_read_b128 v[32:35], v101 offset:3072\n\t" \
  "ds_read_b128 v[36:39], v101 offset:7168\n\t" \
  "v_smfmac_f32_16x16x64_bf16 v[72:75], v[68:71], v[80:87], v104\n\t" \
  "ds_read_b128 v[0:3], v103 offset:0\n\t" \
  "v_mov_b64 v[44:45], 0\n\t" \
  "v_smfmac_f32_16x16x64_bf16 v[76:79], v[68:71], v[88:95], v104\n\t" \
  "v_mov_b64 v[46:47], 0\n\t" \
  "s_waitcnt lgkmcnt(5)\n\t" \
  "v_smfmac_f32_16x16x64_bf16 v[72:75], v[48:51], a[160:167], v104\n\t" \
  "ds_read_b128 v[4:7], v103 offset:1024\n\t" \
  "v_mov_b64 v[80:81], 0\n\t" \
  "v_smfmac_f32_16x16x64_bf16 v[72:75], v[52:55], a[16:23], v104\n\t" \
  "v_mov_b64 v[82:83], 0\n\t" \
  "s_waitcnt lgkmcnt(2)\n\t" \
  "v_smfmac_f32_16x16x64_bf16 v[72:75], v[56:59], a[64:71], v104\n\t" \
  "ds_read_b128 v[8:11], v103 offset:2048\n\t" \
  "v_mov_b64 v[84:85], 0\n\t" \
  "v_smfmac_f32_16x16x64_bf16 v[72:75], v[60:63], a[176:183], v104\n\t" \
  "ds_read_b128 v[12:15], v103 offset:3072\n\t" \
  "v_mov_b64 v[86:87], 0\n\t" \
  "v_smfmac_f32_16x16x64_bf16 v[72:75], v[32:35], a[32:39], v104\n\t" \
  "ds_read_b128 v[16:19], v103 offset:16384\n\t" \
  "v_mov_b64 v[88:89], 0\n\t" \
  "v_smfmac_f32_16x16x64_bf16 v[72:75], v[36:39], a[80:87], v104\n\t" \
  "ds_read_b128 v[20:23], v103 offset:17408\n\t" \
  "v_mov_b64 v[90:91], 0\n\t" \
  "v_smfmac_f32_16x16x64_bf16 v[76:79], v[48:51], a[168:175], v104\n\t" \
  "ds_read_b128 v[24:27], v103 offset:18432\n\t" \
  "v_mov_b64 v[92:93], 0\n\t" \
  "v_smfmac_f32_16x16x64_bf16 v[76:79], v[52:55], a[24:31], v104\n\t" \
  "v_add_f32 v96, v72, v74\n\t" \
  "v_add_f32 v97, v73, v75\n\t" \
  "v_cvt_pk_bf16_f32 v96, v96, v97\n\t" \
  "ds_write_b32 v105, v96\n\t" \
  "v_smfmac_f32_16x16x64_bf16 v[76:79], v[56:59], a[72:79], v104\n\t" \
  "ds_read_b128 v[28:31], v103 offset:19456\n\t" \
  "v_mov_b64 v[94:95], 0\n\t" \
  "v_smfmac_f32_16x16x64_bf16 v[76:79], v[60:63], a[184:191], v104\n\t" \
  "v_smfmac_f32_16x16x64_bf16 v[76:79], v[32:35], a[40:47], v104\n\t" \
  "v_smfmac_f32_16x16x64_bf16 v[76:79], v[36:39], a[88:95], v104\n\t" \
  "s_waitcnt lgkmcnt(1)\n\t" \
  "s_barrier\n\t" \
  "ds_read_b64_tr_b16 v[96:97], v107 offset:0\n\t" \
  "ds_read_b64_tr_b16 v[98:99], v107 offset:64\n\t" \
  "ds_read_b64_tr_b16 v[56:57], v107 offset:1024\n\t" \
  "ds_read_b64_tr_b16 v[58:59], v107 offset:1088\n\t" \
  "s_nop 1\n\t" \
  "v_add_f32 v116, v76, v78\n\t" \
  "v_add_f32 v117, v77, v79\n\t" \
  "v_cvt_pk_bf16_f32 v116, v116, v117\n\t" \
  "ds_write_b32 v105, v116 offset:256\n\t" \
  "s_waitcnt lgkmcnt(3)\n\t" \
  "v_smfmac_f32_16x16x64_bf16 v[40:43], v[96:99], a[192:199], v106\n\t" \
  "v_smfmac_f32_16x16x64_bf16 v[44:47], v[96:99], a[224:231], v106\n\t" \
  "s_waitcnt lgkmcnt(0)\n\t" \
  "s_barrier\n\t" \
  "ds_read_b64_tr_b16 v[52:53], v107 offset:256\n\t" \
  "ds_read_b64_tr_b16 v[54:55], v107 offset:320\n\t" \
  "v_smfmac_f32_16x16x64_bf16 v[40:43], v[56:59], a[208:215], v106\n\t" \
  "ds_read_b64_tr_b16 v[60:61], v107 offset:1280\n\t" \
  "ds_read_b64_tr_b16 v[62:63], v107 offset:1344\n\t" \
  "v_smfmac_f32_16x16x64_bf16 v[44:47], v[56:59], v[0:7], v106\n\t" \
  "v_smfmac_f32_16x16x64_bf16 v[80:83], v[96:99], a[240:247], v106\n\t" \
  "v_mov_b64 v[32:33], 0\n\t" \
  "v_smfmac_f32_16x16x64_bf16 v[84:87], v[96:99], v[136:143], v106\n\t" \
  "v_mov_b64 v[34:35], 0\n\t" \
  "v_smfmac_f32_16x16x64_bf16 v[80:83], v[56:59], v[120:127], v106\n\t" \
  "v_mov_b64 v[36:37], 0\n\t" \
  "v_smfmac_f32_16x16x64_bf16 v[84:87], v[56:59], v[16:23], v106\n\t" \
  "s_waitcnt lgkmcnt(2)\n\t" \
  "v_smfmac_f32_16x16x64_bf16 v[40:43], v[52:55], a[200:207], v106\n\t" \
  "v_smfmac_f32_16x16x64_bf16 v[44:47], v[52:55], a[232:239], v106\n\t" \
  "s_xor_b32 s76, s47, 0x40000\n\t" \
  "s_waitcnt lgkmcnt(0)\n\t" \
  "v_smfmac_f32_16x16x64_bf16 v[40:43], v[60:63], a[216:223], v106\n\t" \
  "v_mov_b64 v[38:39], 0\n\t" \
  "s_xor_b32 s77, s47, 0x80000\n\t" \
  "v_smfmac_f32_16x16x64_bf16 v[44:47], v[60:63], v[8:15], v106\n\t" \
  "s_xor_b32 s78, s47, 0xc0000\n\t" \
  "v_smfmac_f32_16x16x64_bf16 v[80:83], v[52:55], a[248:255], v106\n\t" \
  "ds_read_b128 v[0:3], v103 offset:32768\n\t" \
  "v_cvt_pk_bf16_f32 v64, v40, v41\n\t" \
  "v_smfmac_f32_16x16x64_bf16 v[84:87], v[52:55], v[144:151], v106\n\t" \
  "ds_read_b128 v[4:7], v103 offset:33792\n\t" \
  "v_cvt_pk_bf16_f32 v65, v42, v43\n\t" \
  "v_smfmac_f32_16x16x64_bf16 v[80:83], v[60:63], v[128:135], v106\n\t" \
  "ds_read_b128 v[8:11], v103 offset:34816\n\t" \
  "v_cvt_pk_bf16_f32 v66, v44, v45\n\t" \
  "v_smfmac_f32_16x16x64_bf16 v[84:87], v[60:63], v[24:31], v106\n\t" \
  "ds_read_b128 v[12:15], v103 offset:35840\n\t" \
  "v_cvt_pk_bf16_f32 v67, v46, v47\n\t" \
  "v_smfmac_f32_16x16x64_bf16 v[88:91], v[96:99], v[152:159], v106\n\t" \
  "v_permlane16_swap_b32 v64, v66\n\t" \
  "v_cvt_pk_bf16_f32 v68, v80, v81\n\t" \
  "v_smfmac_f32_16x16x64_bf16 v[92:95], v[96:99], v[184:191], v106\n\t" \
  "v_permlane16_swap_b32 v65, v67\n\t" \
  "v_cvt_pk_bf16_f32 v69, v82, v83\n\t" \
  "v_smfmac_f32_16x16x64_bf16 v[88:91], v[52:55], v[160:167], v106\n\t" \
  "v_cvt_pk_bf16_f32 v70, v84, v85\n\t" \
  "v_smfmac_f32_16x16x64_bf16 v[92:95], v[52:55], v[192:199], v106\n\t" \
  "buffer_store_dwordx4 v[64:67], v108, %[ru], s47 offen sc1\n\t" \
  "v_cvt_pk_bf16_f32 v71, v86, v87\n\t" \
  "v_smfmac_f32_16x16x64_bf16 v[88:91], v[56:59], v[168:175], v106\n\t" \
  "ds_read_b128 v[16:19], v103 offset:49152\n\t" \
  "v_permlane16_swap_b32 v68, v70\n\t" \
  "s_waitcnt lgkmcnt(3)\n\t" \
  "v_smfmac_f32_16x16x64_bf16 v[92:95], v[56:59], v[0:7], v106\n\t" \
  "ds_read_b128 v[20:23], v103 offset:50176\n\t" \
  "v_permlane16_swap_b32 v69, v71\n\t" \
  "v_smfmac_f32_16x16x64_bf16 v[88:91], v[60:63], v[176:183], v106\n\t" \
  "ds_read_b128 v[24:27], v103 offset:51200\n\t" \
  "s_waitcnt lgkmcnt(3)\n\t" \
  "v_smfmac_f32_16x16x64_bf16 v[92:95], v[60:63], v[8:15], v106\n\t" \
  "ds_read_b128 v[28:31], v103 offset:52224\n\t" \
  "buffer_store_dwordx4 v[68:71], v108, %[ru], s76 offen sc1\n\t" \
  "v_smfmac_f32_16x16x64_bf16 v[32:35], v[96:99], v[200:207], v106\n\t" \
  "v_cvt_pk_bf16_f32 v72, v88, v89\n\t" \
  "v_smfmac_f32_16x16x64_bf16 v[36:39], v[96:99], v[232:239], v106\n\t" \
  "v_cvt_pk_bf16_f32 v73, v90, v91\n\t" \
  "v_smfmac_f32_16x16x64_bf16 v[32:35], v[52:55], v[208:215], v106\n\t" \
  "v_cvt_pk_bf16_f32 v74, v92, v93\n\t" \
  "v_smfmac_f32_16x16x64_bf16 v[36:39], v[52:55], v[240:247], v106\n\t" \
  "v_cvt_pk_bf16_f32 v75, v94, v95\n\t" \
  "v_smfmac_f32_16x16x64_bf16 v[32:35], v[56:59], v[216:223], v106\n\t" \
  "v_permlane16_swap_b32 v72, v74\n\t" \
  "v_smfmac_f32_16x16x64_bf16 v[32:35], v[60:63], v[224:231], v106\n\t" \
  "v_permlane16_swap_b32 v73, v75\n\t" \
  "s_waitcnt lgkmcnt(2)\n\t" \
  "v_smfmac_f32_16x16x64_bf16 v[36:39], v[56:59], v[16:23], v106\n\t" \
  "s_waitcnt lgkmcnt(0)\n\t" \
  "v_smfmac_f32_16x16x64_bf16 v[36:39], v[60:63], v[24:31], v106\n\t" \
  "buffer_store_dwordx4 v[72:75], v108, %[ru], s77 offen sc1\n\t" \
  "v_cvt_pk_bf16_f32 v76, v32, v33\n\t" \
  "v_cvt_pk_bf16_f32 v77, v34, v35\n\t" \
  "s_nop 5\n\t" \
  "v_cvt_pk_bf16_f32 v78, v36, v37\n\t" \
  "v_cvt_pk_bf16_f32 v79, v38, v39\n\t" \
  "s_nop 0\n\t" \
  "v_permlane16_swap_b32 v76, v78\n\t" \
  "v_permlane16_swap_b32 v77, v79\n\t" \
  "buffer_store_dwordx4 v[76:79], v108, %[ru], s78 offen sc1\n\t" \
  "s_memtime s[76:77]\n\t" \
  "s_mov_b32 s42, 0\n\t" \
  "s_waitcnt lgkmcnt(0)\n\t" \
  "s_mov_b32 s57, s76\n\t" \
  "s_add_i32 s41, s56, 1950\n\t" \
  "s_sub_i32 s76, s92, s51\n\t" \
  "s_sub_i32 s77, s93, s53\n\t" \
  "s_cmp_eq_u32 s76, 0\n\t" \
  "s_cbranch_scc1 21f\n\t" \
  "v_cvt_f32_u32 v117, s76\n\t" \
  "v_cvt_f32_u32 v116, s77\n\t" \
  "v_rcp_f32 v117, v117\n\t" \
  "s_mov_b32 s51, s92\n\t" \
  "v_mul_f32 v117, v117, v116\n\t" \
  "v_max_f32 v117, 0x3f028f5c, v117\n\t" \
  "v_min_f32 v117, 0x3ffeb852, v117\n\t" \
  "v_rcp_f32 v116, v117\n\t" \
  "v_mul_f32 v117, 0x4f000000, v117\n\t" \
  "v_mul_f32 v116, 0x47800000, v116\n\t" \
  "v_cvt_u32_f32 v117, v117\n\t" \
  "v_cvt_u32_f32 v116, v116\n\t" \
  "s_mov_b32 s53, s93\n\t" \
  "v_readfirstlane_b32 s46, v117\n\t" \
  "v_readfirstlane_b32 s52, v116\n\t" \
  "21:\n\t" \
  "s_add_i32 s44, s44, 1\n\t" \
  "s_and_b32 s58, s58, 4\n\t" \
  "s_and_b32 s76, s44, 64\n\t" \
  "s_or_b32 s42, s42, s76\n\t" \
  "s_cbranch_scc0 20b\n\t" \
  "s_branch 99f\n\t" \
  "14:\n\t" \
  "s_sleep 3\n\t" \
  "s_add_u32 s59, s59, 0x10000\n\t" \
  "s_cmp_lt_u32 s59, 0x100000\n\t" \
  "s_cbranch_scc1 13b\n\t" \
  "s_mov_b64 exec, -1\n\t" \
  "s_mov_b32 s42, 16\n\t" \
  "s_branch 99f\n\t" \
  "8:\n\t" \
  "s_bitcmp1_b32 s58, 1\n\t" \
  "s_cbranch_scc1 4b\n\t" \
  "s_bitcmp1_b32 s58, 2\n\t" \
  "s_cbranch_scc1 5b\n\t" \
  "v_max3_u32 v116, v32, v33, v34\n\t" \
  "v_max3_u32 v117, v35, v36, v37\n\t" \
  "v_max3_u32 v116, v116, v38, v39\n\t" \
  "v_max_u32 v116, v116, v117\n\t" \
  "v_cmp_eq_u32 vcc, -1, v116\n\t" \
  "s_cbranch_vccz 5b\n\t" \
  "s_branch 0f\n\t" \
  "9:\n\t" \
  "s_bitcmp1_b32 s58, 2\n\t" \
  "s_cbranch_scc1 6b\n\t" \
  "v_max3_u32 v116, v40, v41, v42\n\t" \
  "v_max3_u32 v117, v43, v44, v45\n\t" \
  "v_max3_u32 v116, v116, v46, v47\n\t" \
  "v_max_u32 v116, v116, v117\n\t" \
  "v_cmp_eq_u32 vcc, -1, v116\n\t" \
  "s_cbranch_vccz 6b\n\t" \
  "s_or_b32 s58, s58, 2\n\t" \
  "0:\n\t" \
  "s_or_b32 s58, s58, 1\n\t" \
  "11:\n\t" \
  "buffer_load_dwordx4 v[32:35], v100, %[rh], s40 offen sc1\n\t" \
  "buffer_load_dwordx4 v[36:39], v100, %[rh], s76 offen sc1\n\t" \
  "buffer_load_dwordx4 v[40:43], v100, %[rh], s77 offen sc1\n\t" \
  "buffer_load_dwordx4 v[44:47], v100, %[rh], s78 offen sc1\n\t" \
  "s_waitcnt lgkmcnt(0)\n\t" \
  "s_branch 10b\n\t" \
  "17:\n\t" \
  "s_add_u32 s59, s59, 1\n\t" \
  "s_bitcmp1_b32 s58, 2\n\t" \
  "s_cbranch_scc1 3b\n\t" \
  "s_waitcnt vmcnt(0)\n\t" \
  "v_max_u32 v116, v32, v33\n\t" \
  "v_max3_u32 v116, v116, v34, v35\n\t" \
  "v_max3_u32 v116, v116, v36, v37\n\t" \
  "v_max3_u32 v116, v116, v38, v39\n\t" \
  "v_max3_u32 v116, v116, v40, v41\n\t" \
  "v_max3_u32 v116, v116, v42, v43\n\t" \
  "v_max3_u32 v116, v116, v44, v45\n\t" \
  "v_max3_u32 v116, v116, v46, v47\n\t" \
  "v_cmp_eq_u32 vcc, -1, v116\n\t" \
  "s_cbranch_vccz 3b\n\t" \
  "s_bitcmp1_b32 s59, 6\n\t" \
  "s_cbranch_scc1 12f\n\t" \
  "buffer_load_dwordx4 v[32:35], v100, %[rh], s40 offen sc1\n\t" \
  "buffer_load_dwordx4 v[36:39], v100, %[rh], s76 offen sc1\n\t" \
  "buffer_load_dwordx4 v[40:43], v100, %[rh], s77 offen sc1\n\t" \
  "buffer_load_dwordx4 v[44:47], v100, %[rh], s78 offen sc1\n\t" \
  "s_branch 17b\n\t" \
  "12:\n\t" \
  "s_mov_b32 s42, s58\n\t" \
  "s_branch 99f\n\t" \
  "93:\n\t" \
  "s_mov_b32 s58, s42\n\t" \
  "20:\n\t" \
  "s_and_b32 s76, s44, 7\n\t" \
  "s_lshl_b32 s76, s76, 15\n\t" \
  "s_add_i32 s40, s54, s76\n\t" \
  "s_and_b32 s76, s44, 3\n\t" \
  "s_lshl_b32 s45, s76, 14\n\t" \
  "s_lshl_b32 s47, s76, 20\n\t" \
  "s_add_i32 s47, s47, s55\n\t" \
  "s_lshr_b32 s76, s47, 21\n\t" \
  "s_bitcmp1_b64 %[sel], s76\n\t" \
  "s_cselect_b32 s76, 0x2000, 0\n\t" \
  "s_add_i32 s47, s47, s76\n\t" \
  "s_add_i32 s49, s44, -1\n\t" \
  "s_and_b32 s48, s49, 2\n\t" \
  "s_lshl_b32 s48, s48, 13\n\t" \
  "s_and_b32 s76, s49, 1\n\t" \
  "s_lshl_b32 s76, s76, 12\n\t" \
  "s_add_i32 s48, s48, s76\n\t" \
  "s_lshl_b32 s50, s53, 8\n\t" \
  "s_mov_b32 s59, 0\n\t" \
  "s_mov_b32 s92, s51\n\t" \
  "s_mov_b32 s93, s53\n\t" \
  "s_xor_b32 s76, s40, 0x400\n\t" \
  "s_xor_b32 s77, s40, 0x800\n\t" \
  "s_xor_b32 s78, s40, 0xc00\n\t" \
  "s_cmp_lg_u32 s58, 0\n\t" \
  "s_cbranch_scc1 11f\n\t" \
  "10:\n\t" \
  "ds_read_b128 v[48:51], v102 offset:49152\n\t" \
  "ds_read_b128 v[52:55], v102 offset:50176\n\t" \
  "ds_read_b128 v[56:59], v102 offset:51200\n\t" \
  "ds_read_b128 v[60:63], v102 offset:52224\n\t" \
  "ds_read_b128 v[0:3], v102 offset:0\n\t" \
  "ds_read_b128 v[4:7], v102 offset:1024\n\t" \
  "ds_read_b128 v[8:11], v102 offset:2048\n\t" \
  "ds_read_b128 v[12:15], v102 offset:3072\n\t" \
  "ds_read_b128 v[16:19], v102 offset:16384\n\t" \
  "ds_read_b128 v[20:23], v102 offset:17408\n\t" \
  "ds_read_b128 v[24:27], v102 offset:18432\n\t" \
  "ds_read_b128 v[28:31], v102 offset:19456\n\t" \
  "s_waitcnt lgkmcnt(4)\n\t" \
  "ds_read_b128 v[80:83], v102 offset:32768\n\t" \
  "ds_read_b128 v[84:87], v102 offset:33792\n\t" \
  "ds_read_b128 v[88:91], v102 offset:34816\n\t" \
  "ds_read_b128 v[92:95], v102 offset:35840\n\t" \
  "s_bitcmp1_b32 s58, 1\n\t" \
  "s_cselect_b64 s[80:81], -1, 0\n\t" \
  "s_cmp_lg_u32 s58, 0\n\t" \
  "s_cbranch_scc1 17f\n\t" \
  "s_mov_b32 exec_hi, 0\n\t" \
  "16:\n\t" \
  "s_memtime vcc\n\t" \
  "s_waitcnt lgkmcnt(0)\n\t" \
  "s_sub_i32 s79, s41, vcc_lo\n\t" \
  "s_cmp_gt_i32 s79, 64\n\t" \
  "s_cbranch_scc1 16b\n\t" \
  "13:\n\t" \
  "buffer_load_dword v116, v112, %[rh], s48 offen sc1\n\t" \
  "s_waitcnt vmcnt(0)\n\t" \
  "15:\n\t" \
  "v_mov_b32 v117, 20000\n\t" \
  "v_cmp_ne_u32_sdwa vcc, v116, s49 src0_sel:BYTE_0 src1_sel:DWORD\n\t" \
  "v_subrev_u32 v116, s50, v116\n\t" \
  "v_ashrrev_i32 v116, 8, v116\n\t" \
  "s_nop 1\n\t" \
  "v_max_i32_dpp v116, v116, v116 quad_perm:[1,0,3,2] row_mask:0xf bank_mask:0xf\n\t" \
  "s_cbranch_vccnz 14f\n\t" \
  "s_nop 0\n\t" \
  "v_max_i32_dpp v116, v116, v116 quad_perm:[2,3,0,1] row_mask:0xf bank_mask:0xf\n\t" \
  "s_nop 1\n\t" \
  "v_max_i32_dpp v116, v116, v116 row_half_mirror row_mask:0xf bank_mask:0xf\n\t" \
  "s_nop 1\n\t" \
  "v_max_i32_dpp v116, v116, v116 row_mirror row_mask:0xf bank_mask:0xf\n\t" \
  "v_med3_i32 v116, v116, 0, v117\n\t" \
  "v_mul_u32_u24 v116, s52, v116\n\t" \
  "v_lshrrev_b32 v116, 16, v116\n\t" \
  "s_mov_b64 exec, -1\n\t" \
  "v_readfirstlane_b32 s84, v116\n\t" \
  "v_readlane_b32 s85, v116, 16\n\t" \
  "s_max_u32 s84, s84, s85\n\t" \
  "s_add_i32 s84, s84, s51\n\t" \
  "1:\n\t" \
  "s_memtime vcc\n\t" \
  "s_waitcnt lgkmcnt(0)\n\t" \
  "s_sub_i32 s79, s84, vcc_lo\n\t" \
  "s_cmp_gt_i32 s79, 128\n\t" \
  "s_cbranch_scc1 1b\n\t" \
  ".p2align 4\n\t" \
  "2:\n\t" \
  "s_sub_i32 s79, s79, 33\n\t" \
  "s_cmp_gt_i32 s79, 56\n\t" \
  "s_cbranch_scc1 2b\n\t" \
  "buffer_load_dwordx4 v[32:35], v100, %[rh], s40 offen sc1\n\t" \
  "s_nop 12\n\t" \
  "buffer_load_dwordx4 v[36:39], v100, %[rh], s76 offen sc1\n\t" \
  "s_nop 12\n\t" \
  "buffer_load_dwordx4 v[40:43], v100, %[rh], s77 offen sc1\n\t" \
  "s_nop 12\n\t" \
  "buffer_load_dwordx4 v[44:47], v100, %[rh], s78 offen sc1\n\t" \
  "s_memtime vcc\n\t" \
  "s_memrealtime s[82:83]\n\t" \
  "s_nop 4\n\t" \
  "s_memrealtime s[84:85]\n\t" \
  "s_nop 4\n\t" \
  "s_memrealtime s[56:57]\n\t" \
  "s_nop 4\n\t" \
  "s_memrealtime s[86:87]\n\t" \
  "s_nop 4\n\t" \
  "s_memrealtime s[88:89]\n\t" \
  "s_nop 4\n\t" \
  "s_memrealtime s[90:91]\n\t" \
  "s_mov_b32 s92, 0\n\t" \
  "s_waitcnt lgkmcnt(0)\n\t" \
  "s_cmp_eq_u32 s82, s84\n\t" \
  "s_addc_u32 s92, s92, 0\n\t" \
  "s_cmp_eq_u32 s82, s56\n\t" \
  "s_addc_u32 s92, s92, 0\n\t" \
  "s_cmp_eq_u32 s82, s86\n\t" \
  "s_addc_u32 s92, s92, 0\n\t" \
  "s_cmp_eq_u32 s82, s88\n\t" \
  "s_addc_u32 s92, s92, 0\n\t" \
  "s_cmp_eq_u32 s82, s90\n\t" \
  "s_addc_u32 s92, s92, 0\n\t" \
  "s_mul_i32 s92, s92, 24\n\t" \
  "s_add_i32 s92, s92, vcc_lo\n\t" \
  "s_lshl4_add_u32 s93, s82, 64\n\t" \
  "3:\n\t" \
  "v_mov_b64 v[72:73], 0\n\t" \
  "v_mov_b64 v[74:75], 0\n\t" \
  "v_mov_b64 v[76:77], 0\n\t" \
  "v_mov_b64 v[78:79], 0\n\t" \
  "s_waitcnt lgkmcnt(0)\n\t" \
  "s_waitcnt vmcnt(3)\n\t" \
  "v_smfmac_f32_16x16x64_bf16 v[72:75], v[32:35], v[48:55], v104\n\t" \
  "ds_write_b128 v101, v[32:35] offset:12288\n\t" \
  "v_smfmac_f32_16x16x64_bf16 v[76:79], v[32:35], v[56:63], v104\n\t" \
  "s_waitcnt vmcnt(2)\n\t" \
  "ds_write_b128 v101, v[36:39] offset:13312\n\t" \
  "v_smfmac_f32_16x16x64_bf16 v[72:75], v[36:39], a[144:151], v104\n\t" \
  "v_smfmac_f32_16x16x64_bf16 v[76:79], v[36:39], a[152:159], v104\n\t" \
  "s_nop 3\n\t" \
  "v_cmp_u_f32 vcc, v72, v73\n\t" \
  "v_cmp_u_f32 s[82:83], v74, v75\n\t" \
  "s_waitcnt vmcnt(1)\n\t" \
  "v_smfmac_f32_16x16x64_bf16 v[72:75], v[40:43], a[160:167], v104\n\t" \
  "s_or_b64 vcc, vcc, s[82:83]\n\t" \
  "s_or_b64 vcc, vcc, s[80:81]\n\t" \
  "v_smfmac_f32_16x16x64_bf16 v[76:79], v[40:43], a[168:175], v104\n\t" \
  "s_waitcnt lgkmcnt(0)\n\t" \
  "s_cbranch_scc1 8f\n\t" \
  "5:\n\t" \
  "s_barrier\n\t" \
  "4:\n\t" \
  "s_memtime s[56:57]\n\t" \
  "ds_read_b128 v[48:51], v101 offset:0\n\t" \
  "ds_read_b128 v[52:55], v101 offset:1024\n\t" \
  "ds_read_b128 v[56:59], v101 offset:5120\n\t" \
  "s_waitcnt vmcnt(0)\n\t" \
  "v_smfmac_f32_16x16x64_bf16 v[72:75], v[44:47], a[176:183], v104\n\t" \
  "ds_read_b128 v[60:63], v101 offset:4096\n\t" \
  "ds_read_b128 v[64:67], v101 offset:9216\n\t" \
  "v_smfmac_f32_16x16x64_bf16 v[76:79], v[44:47], a[184:191], v104\n\t" \
  "ds_read_b128 v[68:71], v101 offset:8192\n\t" \
  "ds_write_b128 v101, v[40:43] offset:14336\n\t" \
  "s_nop 1\n\t" \
  "v_cmp_u_f32 vcc, v72, v73\n\t" \
  "v_cmp_u_f32 s[82:83], v74, v75\n\t" \
  "s_waitcnt lgkmcnt(6)\n\t" \
  "v_smfmac_f32_16x16x64_bf16 v[72:75], v[48:51], v[0:7], v104\n\t" \
  "s_or_b64 vcc, vcc, s[82:83]\n\t" \
  "v_smfmac_f32_16x16x64_bf16 v[76:79], v[48:51], v[8:15], v104\n\t" \
  "s_cbranch_scc1 9f\n\t" \
  "6:\n\t" \
  "s_waitcnt lgkmcnt(5)\n\t" \
  "v_smfmac_f32_16x16x64_bf16 v[72:75], v[52:55], a[0:7], v104\n\t" \
  "ds_write_b128 v101, v[44:47] offset:15360\n\t" \
  "v_smfmac_f32_16x16x64_bf16 v[76:79], v[52:55], a[8:15], v104\n\t" \
  "v_mov_b64 v[40:41], 0\n\t" \
  "s_waitcnt lgkmcnt(5)\n\t" \
  "v_smfmac_f32_16x16x64_bf16 v[72:75], v[56:59], a[48:55], v104\n\t" \
  "v_mov_b64 v[42:43], 0\n\t" \
  "v_smfmac_f32_16x16x64_bf16 v[76:79], v[56:59], a[56:63], v104\n\t" \
  "s_waitcnt lgkmcnt(4)\n\t" \
  "v_smfmac_f32_16x16x64_bf16 v[72:75], v[60:63], v[16:23], v104\n\t" \
  "s_waitcnt lgkmcnt(0)\n\t" \
  "s_barrier\n\t" \
  "v_smfmac_f32_16x16x64_bf16 v[76:79], v[60:63], v[24:31], v104\n\t" \
  "ds_read_b128 v[48:51], v101 offset:2048\n\t" \
  "ds_read_b128 v[52:55], v101 offset:6144\n\t" \
  "v_smfmac_f32_16x16x64_bf16 v[72:75], v[64:67], a[96:103], v104\n\t" \
  "ds_read_b128 v[56:59], v101 offset:10240\n\t" \
  "ds_read_b128 v[60:63], v101 offset:3072\n\t" \
  "v_smfmac_f32_16x16x64_bf16 v[76:79], v[64:67], a[104:111], v104\n\t" \
  "ds_read_b128 v[32:35], v101 offset:7168\n\t" \
  "ds_read_b128 v[36:39], v101 offset:11264\n\t" \
  "v_smfmac_f32_16x16x64_bf16 v[72:75], v[68:71], v[80:87], v104\n\t" \
  "ds_read_b128 v[0:3], v103 offset:0\n\t" \
  "v_mov_b64 v[44:45], 0\n\t" \
  "v_smfmac_f32_16x16x64_bf16 v[76:79], v[68:71], v[88:95], v104\n\t" \
  "v_mov_b64 v[46:47], 0\n\t" \
  "s_waitcnt lgkmcnt(5)\n\t" \
  "v_smfmac_f32_16x16x64_bf16 v[72:75], v[48:51], a[16:23], v104\n\t" \
  "ds_read_b128 v[4:7], v103 offset:1024\n\t" \
  "v_mov_b64 v[80:81], 0\n\t" \
  "v_smfmac_f32_16x16x64_bf16 v[72:75], v[52:55], a[64:71], v104\n\t" \
  "v_mov_b64 v[82:83], 0\n\t" \
  "s_waitcnt lgkmcnt(2)\n\t" \
  "v_smfmac_f32_16x16x64_bf16 v[72:75], v[56:59], a[112:119], v104\n\t" \
  "ds_read_b128 v[8:11], v103 offset:2048\n\t" \
  "v_mov_b64 v[84:85], 0\n\t" \
  "v_smfmac_f32_16x16x64_bf16 v[72:75], v[60:63], a[32:39], v104\n\t" \
  "ds_read_b128 v[12:15], v103 offset:3072\n\t" \
  "v_mov_b64 v[86:87], 0\n\t" \
  "v_smfmac_f32_16x16x64_bf16 v[72:75], v[32:35], a[80:87], v104\n\t" \
  "ds_read_b128 v[16:19], v103 offset:16384\n\t" \
  "v_mov_b64 v[88:89], 0\n\t" \
  "v_smfmac_f32_16x16x64_bf16 v[72:75], v[36:39], a[128:135], v104\n\t" \
  "ds_read_b128 v[20:23], v103 offset:17408\n\t" \
  "v_mov_b64 v[90:91], 0\n\t" \
  "v_smfmac_f32_16x16x64_bf16 v[76:79], v[48:51], a[24:31], v104\n\t" \
  "ds_read_b128 v[24:27], v103 offset:18432\n\t" \
  "v_mov_b64 v[92:93], 0\n\t" \
  "v_smfmac_f32_16x16x64_bf16 v[76:79], v[52:55], a[72:79], v104\n\t" \
  "v_add_f32 v96, v72, v74\n\t" \
  "v_add_f32 v97, v73, v75\n\t" \
  "v_cvt_pk_bf16_f32 v96, v96, v97\n\t" \
  "ds_write_b32 v105, v96\n\t" \
  "v_smfmac_f32_16x16x64_bf16 v[76:79], v[56:59], a[120:127], v104\n\t" \
  "ds_read_b128 v[28:31], v103 offset:19456\n\t" \
  "v_mov_b64 v[94:95], 0\n\t" \
  "v_smfmac_f32_16x16x64_bf16 v[76:79], v[60:63], a[40:47], v104\n\t" \
  "v_smfmac_f32_16x16x64_bf16 v[76:79], v[32:35], a[88:95], v104\n\t" \
  "v_smfmac_f32_16x16x64_bf16 v[76:79], v[36:39], a[136:143], v104\n\t" \
  "s_waitcnt lgkmcnt(1)\n\t" \
  "s_barrier\n\t" \
  "ds_read_b64_tr_b16 v[96:97], v107 offset:0\n\t" \
  "ds_read_b64_tr_b16 v[98:99], v107 offset:64\n\t" \
  "ds_read_b64_tr_b16 v[56:57], v107 offset:1024\n\t" \
  "ds_read_b64_tr_b16 v[58:59], v107 offset:1088\n\t" \
  "s_nop 1\n\t" \
  "v_add_f32 v116, v76, v78\n\t" \
  "v_add_f32 v117, v77, v79\n\t" \
  "v_cvt_pk_bf16_f32 v116, v116, v117\n\t" \
  "ds_write_b32 v105, v116 offset:256\n\t" \
  "s_waitcnt lgkmcnt(3)\n\t" \
  "v_smfmac_f32_16x16x64_bf16 v[40:43], v[96:99], a[192:199], v106\n\t" \
  "v_smfmac_f32_16x16x64_bf16 v[44:47], v[96:99], a[224:231], v106\n\t" \
  "s_waitcnt lgkmcnt(0)\n\t" \
  "s_barrier\n\t" \
  "ds_read_b64_tr_b16 v[52:53], v107 offset:256\n\t" \
  "ds_read_b64_tr_b16 v[54:55], v107 offset:320\n\t" \
  "v_smfmac_f32_16x16x64_bf16 v[40:43], v[56:59], a[208:215], v106\n\t" \
  "ds_read_b64_tr_b16 v[60:61], v107 offset:1280\n\t" \
  "ds_read_b64_tr_b16 v[62:63], v107 offset:1344\n\t" \
  "v_smfmac_f32_16x16x64_bf16 v[44:47], v[56:59], v[0:7], v106\n\t" \
  "v_smfmac_f32_16x16x64_bf16 v[80:83], v[96:99], a[240:247], v106\n\t" \
  "v_mov_b64 v[32:33], 0\n\t" \
  "v_smfmac_f32_16x16x64_bf16 v[84:87], v[96:99], v[136:143], v106\n\t" \
  "v_mov_b64 v[34:35], 0\n\t" \
  "v_smfmac_f32_16x16x64_bf16 v[80:83], v[56:59], v[120:127], v106\n\t" \
  "v_mov_b64 v[36:37], 0\n\t" \
  "v_smfmac_f32_16x16x64_bf16 v[84:87], v[56:59], v[16:23], v106\n\t" \
  "s_waitcnt lgkmcnt(2)\n\t" \
  "v_smfmac_f32_16x16x64_bf16 v[40:43], v[52:55], a[200:207], v106\n\t" \
  "v_smfmac_f32_16x16x64_bf16 v[44:47], v[52:55], a[232:239], v106\n\t" \
  "s_xor_b32 s76, s47, 0x40000\n\t" \
  "s_waitcnt lgkmcnt(0)\n\t" \
  "v_smfmac_f32_16x16x64_bf16 v[40:43], v[60:63], a[216:223], v106\n\t" \
  "v_mov_b64 v[38:39], 0\n\t" \
  "s_xor_b32 s77, s47, 0x80000\n\t" \
  "v_smfmac_f32_16x16x64_bf16 v[44:47], v[60:63], v[8:15], v106\n\t" \
  "s_xor_b32 s78, s47, 0xc0000\n\t" \
  "v_smfmac_f32_16x16x64_bf16 v[80:83], v[52:55], a[248:255], v106\n\t" \
  "ds_read_b128 v[0:3], v103 offset:32768\n\t" \
  "v_cvt_pk_bf16_f32 v64, v40, v41\n\t" \
  "v_smfmac_f32_16x16x64_bf16 v[84:87], v[52:55], v[144:151], v106\n\t" \
  "ds_read_b128 v[4:7], v103 offset:33792\n\t" \
  "v_cvt_pk_bf16_f32 v65, v42, v43\n\t" \
  "v_smfmac_f32_16x16x64_bf16 v[80:83], v[60:63], v[128:135], v106\n\t" \
  "ds_read_b128 v[8:11], v103 offset:34816\n\t" \
  "v_cvt_pk_bf16_f32 v66, v44, v45\n\t" \
  "v_smfmac_f32_16x16x64_bf16 v[84:87], v[60:63], v[24:31], v106\n\t" \
  "ds_read_b128 v[12:15], v103 offset:35840\n\t" \
  "v_cvt_pk_bf16_f32 v67, v46, v47\n\t" \
  "v_smfmac_f32_16x16x64_bf16 v[88:91], v[96:99], v[152:159], v106\n\t" \
  "v_permlane16_swap_b32 v64, v66\n\t" \
  "v_cvt_pk_bf16_f32 v68, v80, v81\n\t" \
  "v_smfmac_f32_16x16x64_bf16 v[92:95], v[96:99], v[184:191], v106\n\t" \
  "v_permlane16_swap_b32 v65, v67\n\t" \
  "v_cvt_pk_bf16_f32 v69, v82, v83\n\t" \
  "v_smfmac_f32_16x16x64_bf16 v[88:91], v[52:55], v[160:167], v106\n\t" \
  "v_cvt_pk_bf16_f32 v70, v84, v85\n\t" \
  "v_smfmac_f32_16x16x64_bf16 v[92:95], v[52:55], v[192:199], v106\n\t" \
  "v_cvt_pk_bf16_f32 v71, v86, v87\n\t" \
  "v_smfmac_f32_16x16x64_bf16 v[88:91], v[56:59], v[168:175], v106\n\t" \
  "ds_read_b128 v[16:19], v103 offset:49152\n\t" \
  "buffer_store_dwordx4 v[64:67], v108, %[ru], s47 offen sc1\n\t" \
  "v_permlane16_swap_b32 v68, v70\n\t" \
  "s_waitcnt lgkmcnt(3)\n\t" \
  "v_smfmac_f32_16x16x64_bf16 v[92:95], v[56:59], v[0:7], v106\n\t" \
  "ds_read_b128 v[20:23], v103 offset:50176\n\t" \
  "v_permlane16_swap_b32 v69, v71\n\t" \
  "v_smfmac_f32_16x16x64_bf16 v[88:91], v[60:63], v[176:183], v106\n\t" \
  "ds_read_b128 v[24:27], v103 offset:51200\n\t" \
  "s_waitcnt lgkmcnt(3)\n\t" \
  "v_smfmac_f32_16x16x64_bf16 v[92:95], v[60:63], v[8:15], v106\n\t" \
  "ds_read_b128 v[28:31], v103 offset:52224\n\t" \
  "v_smfmac_f32_16x16x64_bf16 v[32:35], v[96:99], v[200:207], v106\n\t" \
  "buffer_store_dwordx4 v[68:71], v108, %[ru], s76 offen sc1\n\t" \
  "v_cvt_pk_bf16_f32 v72, v88, v89\n\t" \
  "v_smfmac_f32_16x16x64_bf16 v[36:39], v[96:99], v[232:239], v106\n\t" \
  "v_cvt_pk_bf16_f32 v73, v90, v91\n\t" \
  "v_smfmac_f32_16x16x64_bf16 v[32:35], v[52:55], v[208:215], v106\n\t" \
  "v_cvt_pk_bf16_f32 v74, v92, v93\n\t" \
  "v_smfmac_f32_16x16x64_bf16 v[36:39], v[52:55], v[240:247], v106\n\t" \
  "v_cvt_pk_bf16_f32 v75, v94, v95\n\t" \
  "v_smfmac_f32_16x16x64_bf16 v[32:35], v[56:59], v[216:223], v106\n\t" \
  "v_permlane16_swap_b32 v72, v74\n\t" \
  "v_smfmac_f32_16x16x64_bf16 v[32:35], v[60:63], v[224:231], v106\n\t" \
  "v_permlane16_swap_b32 v73, v75\n\t" \
  "s_waitcnt lgkmcnt(2)\n\t" \
  "v_smfmac_f32_16x16x64_bf16 v[36:39], v[56:59], v[16:23], v106\n\t" \
  "s_waitcnt lgkmcnt(0)\n\t" \
  "v_smfmac_f32_16x16x64_bf16 v[36:39], v[60:63], v[24:31], v106\n\t" \
  "buffer_store_dwordx4 v[72:75], v108, %[ru], s77 offen sc1\n\t" \
  "v_cvt_pk_bf16_f32 v76, v32, v33\n\t" \
  "v_cvt_pk_bf16_f32 v77, v34, v35\n\t" \
  "s_nop 5\n\t" \
  "v_cvt_pk_bf16_f32 v78, v36, v37\n\t" \
  "v_cvt_pk_bf16_f32 v79, v38, v39\n\t" \
  "s_nop 0\n\t" \
  "v_permlane16_swap_b32 v76, v78\n\t" \
  "v_permlane16_swap_b32 v77, v79\n\t" \
  "buffer_store_dwordx4 v[76:79], v108, %[ru], s78 offen sc1\n\t" \
  "s_memtime s[76:77]\n\t" \
  "s_mov_b32 s42, 0\n\t" \
  "s_waitcnt lgkmcnt(0)\n\t" \
  "s_mov_b32 s57, s76\n\t" \
  "s_add_i32 s41, s56, 1950\n\t" \
  "s_sub_i32 s76, s92, s51\n\t" \
  "s_sub_i32 s77, s93, s53\n\t" \
  "s_cmp_eq_u32 s76, 0\n\t" \
  "s_cbranch_scc1 21f\n\t" \
  "v_cvt_f32_u32 v117, s76\n\t" \
  "v_cvt_f32_u32 v116, s77\n\t" \
  "v_rcp_f32 v117, v117\n\t" \
  "s_mov_b32 s51, s92\n\t" \
  "v_mul_f32 v117, v117, v116\n\t" \
  "v_max_f32 v117, 0x3f028f5c, v117\n\t" \
  "v_min_f32 v117, 0x3ffeb852, v117\n\t" \
  "v_rcp_f32 v116, v117\n\t" \
  "v_mul_f32 v117, 0x4f000000, v117\n\t" \
  "v_mul_f32 v116, 0x47800000, v116\n\t" \
  "v_cvt_u32_f32 v117, v117\n\t" \
  "v_cvt_u32_f32 v116, v116\n\t" \
  "s_mov_b32 s53, s93\n\t" \
  "v_readfirstlane_b32 s46, v117\n\t" \
  "v_readfirstlane_b32 s52, v116\n\t" \
  "21:\n\t" \
  "s_add_i32 s44, s44, 1\n\t" \
  "s_and_b32 s58, s58, 4\n\t" \
  "s_and_b32 s76, s44, 64\n\t" \
  "s_or_b32 s42, s42, s76\n\t" \
  "s_cbranch_scc0 20b\n\t" \
  "s_branch 99f\n\t" \
  "14:\n\t" \
  "s_sleep 3\n\t" \
  "s_add_u32 s59, s59, 0x10000\n\t" \
  "s_cmp_lt_u32 s59, 0x100000\n\t" \
  "s_cbranch_scc1 13b\n\t" \
  "s_mov_b64 exec, -1\n\t" \
  "s_mov_b32 s42, 16\n\t" \
  "s_branch 99f\n\t" \
  "8:\n\t" \
  "s_bitcmp1_b32 s58, 1\n\t" \
  "s_cbranch_scc1 4b\n\t" \
  "s_bitcmp1_b32 s58, 2\n\t" \
  "s_cbranch_scc1 5b\n\t" \
  "v_max3_u32 v116, v32, v33, v34\n\t" \
  "v_max3_u32 v117, v35, v36, v37\n\t" \
  "v_max3_u32 v116, v116, v38, v39\n\t" \
  "v_max_u32 v116, v116, v117\n\t" \
  "v_cmp_eq_u32 vcc, -1, v116\n\t" \
  "s_cbranch_vccz 5b\n\t" \
  "s_branch 0f\n\t" \
  "9:\n\t" \
  "s_bitcmp1_b32 s58, 2\n\t" \
  "s_cbranch_scc1 6b\n\t" \
  "v_max3_u32 v116, v40, v41, v42\n\t" \
  "v_max3_u32 v117, v43, v44, v45\n\t" \
  "v_max3_u32 v116, v116, v46, v47\n\t" \
  "v_max_u32 v116, v116, v117\n\t" \
  "v_cmp_eq_u32 vcc, -1, v116\n\t" \
  "s_cbranch_vccz 6b\n\t" \
  "s_or_b32 s58, s58, 2\n\t" \
  "0:\n\t" \
  "s_or_b32 s58, s58, 1\n\t" \
  "11:\n\t" \
  "buffer_load_dwordx4 v[32:35], v100, %[rh], s40 offen sc1\n\t" \
  "buffer_load_dwordx4 v[36:39], v100, %[rh], s76 offen sc1\n\t" \
  "buffer_load_dwordx4 v[40:43], v100, %[rh], s77 offen sc1\n\t" \
  "buffer_load_dwordx4 v[44:47], v100, %[rh], s78 offen sc1\n\t" \
  "s_waitcnt lgkmcnt(0)\n\t" \
  "s_branch 10b\n\t" \
  "17:\n\t" \
  "s_add_u32 s59, s59, 1\n\t" \
  "s_bitcmp1_b32 s58, 2\n\t" \
  "s_cbranch_scc1 3b\n\t" \
  "s_waitcnt vmcnt(0)\n\t" \
  "v_max_u32 v116, v32, v33\n\t" \
  "v_max3_u32 v116, v116, v34, v35\n\t" \
  "v_max3_u32 v116, v116, v36, v37\n\t" \
  "v_max3_u32 v116, v116, v38, v39\n\t" \
  "v_max3_u32 v116, v116, v40, v41\n\t" \
  "v_max3_u32 v116, v116, v42, v43\n\t" \
  "v_max3_u32 v116, v116, v44, v45\n\t" \
  "v_max3_u32 v116, v116, v46, v47\n\t" \
  "v_cmp_eq_u32 vcc, -1, v116\n\t" \
  "s_cbranch_vccz 3b\n\t" \
  "s_bitcmp1_b32 s59, 6\n\t" \
  "s_cbranch_scc1 12f\n\t" \
  "buffer_load_dwordx4 v[32:35], v100, %[rh], s40 offen sc1\n\t" \
  "buffer_load_dwordx4 v[36:39], v100, %[rh], s76 offen sc1\n\t" \
  "buffer_load_dwordx4 v[40:43], v100, %[rh], s77 offen sc1\n\t" \
  "buffer_load_dwordx4 v[44:47], v100, %[rh], s78 offen sc1\n\t" \
  "s_branch 17b\n\t" \
  "12:\n\t" \
  "s_mov_b32 s42, s58\n\t" \
  "s_branch 99f\n\t" \
  "99:\n\t" \
  "s_waitcnt vmcnt(0) lgkmcnt(0)\n\t" \
  ".Lchain_last:\n\t" \
  ""
#define CB_BODY \
  "s_cmp_gt_u32 %[wv], 1\n\t" \
  "s_cbranch_scc1 91f\n\t" \
  "s_mov_b32 s58, s42\n\t" \
  "s_mov_b32 s59, 0\n\t" \
  "s_bitcmp1_b32 s58, 4\n\t" \
  "s_cbranch_scc1 26f\n\t" \
  "s_bitcmp1_b32 s58, 3\n\t" \
  "s_cbranch_scc1 34f\n\t" \
  "s_bitcmp1_b32 s58, 1\n\t" \
  "s_cbranch_scc1 18f\n\t" \
  "17:\n\t" \
  "19:\n\t" \
  "s_add_i32 s85, s40, -1\n\t" \
  "s_and_b32 s82, s85, 2\n\t" \
  "s_lshl_b32 s82, s82, 13\n\t" \
  "s_and_b32 s80, s85, 1\n\t" \
  "s_lshl_b32 s80, s80, 12\n\t" \
  "s_add_i32 s82, s82, s80\n\t" \
  "s_add_i32 s82, s82, 0x84000\n\t" \
  "s_mov_b32 exec_hi, 0\n\t" \
  "buffer_load_dword v184, v200, %[rh], s82 offen sc1\n\t" \
  "buffer_load_dword v186, v203, %[ru], 0 offen sc1\n\t" \
  "s_mov_b64 exec, -1\n\t" \
  "20:\n\t" \
  "s_and_b32 s80, s40, 3\n\t" \
  "s_mul_i32 s77, s80, 0xc000\n\t" \
  "s_add_i32 s77, s77, 0x40000\n\t" \
  "s_lshl_b32 s78, s80, 14\n\t" \
  "s_add_i32 s78, s78, 0x70000\n\t" \
  "s_mov_b32 s57, 0\n\t" \
  "s_add_i32 s96, s77, 0x1000\n\t" \
  "s_lshl_b32 s88, s80, 20\n\t" \
  "s_add_i32 s88, s88, s48\n\t" \
  "s_lshr_b32 s80, s88, 21\n\t" \
  "s_bitcmp1_b64 %[sel], s80\n\t" \
  "s_cselect_b32 s80, 0x2000, 0\n\t" \
  "s_add_i32 s88, s88, s80\n\t" \
  "s_xor_b32 s89, s88, 0x800\n\t" \
  "s_xor_b32 s90, s88, 0x1000\n\t" \
  "s_xor_b32 s91, s88, 0x1800\n\t" \
  "s_add_i32 s92, s77, s50\n\t" \
  "s_xor_b32 s93, s92, 0x100\n\t" \
  "s_xor_b32 s94, s92, 0x200\n\t" \
  "s_xor_b32 s95, s92, 0x300\n\t" \
  "s_add_i32 s97, s40, 1\n\t" \
  "s_and_b32 s97, s97, 7\n\t" \
  "s_lshl_b32 s97, s97, 15\n\t" \
  "s_add_i32 s97, s97, s49\n\t" \
  "s_memtime vcc\n\t" \
  "s_memrealtime s[86:87]\n\t" \
  "s_nop 4\n\t" \
  "s_memrealtime s[60:61]\n\t" \
  "s_nop 4\n\t" \
  "s_memrealtime s[62:63]\n\t" \
  "s_nop 4\n\t" \
  "s_memrealtime s[64:65]\n\t" \
  "s_nop 4\n\t" \
  "s_memrealtime s[66:67]\n\t" \
  "s_nop 4\n\t" \
  "s_memrealtime s[68:69]\n\t" \
  "s_mov_b32 s79, 0\n\t" \
  "s_waitcnt lgkmcnt(0)\n\t" \
  "s_cmp_eq_u32 s86, s60\n\t" \
  "s_addc_u32 s79, s79, 0\n\t" \
  "s_cmp_eq_u32 s86, s62\n\t" \
  "s_addc_u32 s79, s79, 0\n\t" \
  "s_cmp_eq_u32 s86, s64\n\t" \
  "s_addc_u32 s79, s79, 0\n\t" \
  "s_cmp_eq_u32 s86, s66\n\t" \
  "s_addc_u32 s79, s79, 0\n\t" \
  "s_cmp_eq_u32 s86, s68\n\t" \
  "s_addc_u32 s79, s79, 0\n\t" \
  "s_mul_i32 s79, s79, 24\n\t" \
  "s_add_i32 s79, s79, vcc_lo\n\t" \
  "s_lshl4_add_u32 s80, s86, 64\n\t" \
  "s_sub_i32 s86, s79, s41\n\t" \
  "s_sub_i32 s87, s80, s43\n\t" \
  "s_cmp_lt_u32 s86, 4000\n\t" \
  "s_cbranch_scc1 21f\n\t" \
  "v_cvt_f32_u32 v165, s86\n\t" \
  "v_cvt_f32_u32 v164, s87\n\t" \
  "v_rcp_f32 v165, v165\n\t" \
  "s_nop 0\n\t" \
  "v_mul_f32 v165, v165, v164\n\t" \
  "v_max_f32 v165, 0x3f028f5c, v165\n\t" \
  "v_min_f32 v165, 0x3ffeb852, v165\n\t" \
  "v_rcp_f32 v164, v165\n\t" \
  "v_mul_f32 v165, 0x4f000000, v165\n\t" \
  "v_mul_f32 v164, 0x47800000, v164\n\t" \
  "v_cvt_u32_f32 v165, v165\n\t" \
  "v_cvt_u32_f32 v164, v164\n\t" \
  "s_nop 0\n\t" \
  "v_readfirstlane_b32 s45, v165\n\t" \
  "v_readfirstlane_b32 s44, v164\n\t" \
  "21:\n\t" \
  "s_mov_b32 s41, s79\n\t" \
  "s_mov_b32 s43, s80\n\t" \
  "s_lshl_b32 s83, s43, 8\n\t" \
  "s_mov_b32 s84, 650\n\t" \
  "s_mov_b32 exec_hi, 0\n\t" \
  "s_waitcnt vmcnt(0)\n\t" \
  "v_cmp_ne_u32_sdwa vcc, v184, s85 src0_sel:BYTE_0 src1_sel:DWORD\n\t" \
  "v_subrev_u32 v184, s83, v184\n\t" \
  "v_ashrrev_i32 v184, 8, v184\n\t" \
  "s_cbranch_vccnz 22f\n\t" \
  "s_nop 0\n\t" \
  "v_max_i32_dpp v184, v184, v184 quad_perm:[1,0,3,2] row_mask:0xf bank_mask:0xf\n\t" \
  "s_nop 1\n\t" \
  "v_max_i32_dpp v184, v184, v184 quad_perm:[2,3,0,1] row_mask:0xf bank_mask:0xf\n\t" \
  "s_nop 1\n\t" \
  "v_max_i32_dpp v184, v184, v184 row_half_mirror row_mask:0xf bank_mask:0xf\n\t" \
  "s_nop 1\n\t" \
  "v_max_i32_dpp v184, v184, v184 row_mirror row_mask:0xf bank_mask:0xf\n\t" \
  "s_nop 0\n\t" \
  "v_readlane_b32 s81, v184, 0\n\t" \
  "v_readlane_b32 s82, v184, 16\n\t" \
  "s_max_i32 s81, s81, s82\n\t" \
  "s_add_i32 s81, s81, 1056\n\t" \
  "s_max_i32 s81, s81, 0\n\t" \
  "s_min_i32 s81, s81, 20000\n\t" \
  "s_mul_i32 s81, s81, s44\n\t" \
  "s_lshr_b32 s81, s81, 16\n\t" \
  "24:\n\t" \
  "s_add_i32 s81, s81, s41\n\t" \
  "1:\n\t" \
  "s_memtime vcc\n\t" \
  "s_waitcnt lgkmcnt(0)\n\t" \
  "s_sub_i32 s79, s81, vcc_lo\n\t" \
  "s_cmp_gt_i32 s79, 128\n\t" \
  "s_cbranch_scc1 1b\n\t" \
  ".p2align 4\n\t" \
  "2:\n\t" \
  "s_sub_i32 s79, s79, 33\n\t" \
  "s_cmp_gt_i32 s79, 56\n\t" \
  "s_cbranch_scc1 2b\n\t" \
  "buffer_load_dword v187, v200, %[rh], s78 offen sc1\n\t" \
  "s_memtime vcc\n\t" \
  "s_memrealtime s[86:87]\n\t" \
  "s_nop 4\n\t" \
  "s_memrealtime s[60:61]\n\t" \
  "s_nop 4\n\t" \
  "s_memrealtime s[62:63]\n\t" \
  "s_nop 4\n\t" \
  "s_memrealtime s[64:65]\n\t" \
  "s_nop 4\n\t" \
  "s_memrealtime s[66:67]\n\t" \
  "s_nop 4\n\t" \
  "s_memrealtime s[68:69]\n\t" \
  "s_mov_b32 s79, 0\n\t" \
  "s_waitcnt lgkmcnt(0)\n\t" \
  "s_cmp_eq_u32 s86, s60\n\t" \
  "s_addc_u32 s79, s79, 0\n\t" \
  "s_cmp_eq_u32 s86, s62\n\t" \
  "s_addc_u32 s79, s79, 0\n\t" \
  "s_cmp_eq_u32 s86, s64\n\t" \
  "s_addc_u32 s79, s79, 0\n\t" \
  "s_cmp_eq_u32 s86, s66\n\t" \
  "s_addc_u32 s79, s79, 0\n\t" \
  "s_cmp_eq_u32 s86, s68\n\t" \
  "s_addc_u32 s79, s79, 0\n\t" \
  "s_mul_i32 s79, s79, 24\n\t" \
  "s_add_i32 s79, s79, vcc_lo\n\t" \
  "s_lshl4_add_u32 s80, s86, 64\n\t" \
  "s_mov_b32 s41, s79\n\t" \
  "s_mov_b32 s43, s80\n\t" \
  "s_lshl_b32 s83, s43, 8\n\t" \
  "13:\n\t" \
  "s_waitcnt vmcnt(0)\n\t" \
  "s_memtime s[86:87]\n\t" \
  "v_cmp_ne_u32_sdwa vcc, v187, s40 src0_sel:BYTE_0 src1_sel:DWORD\n\t" \
  "v_subrev_u32 v184, s83, v187\n\t" \
  "v_ashrrev_i32 v184, 8, v184\n\t" \
  "s_nop 1\n\t" \
  "v_max_i32_dpp v184, v184, v184 quad_perm:[1,0,3,2] row_mask:0xf bank_mask:0xf\n\t" \
  "s_nop 1\n\t" \
  "v_max_i32_dpp v184, v184, v184 quad_perm:[2,3,0,1] row_mask:0xf bank_mask:0xf\n\t" \
  "s_cbranch_vccnz 14f\n\t" \
  "15:\n\t" \
  "s_nop 0\n\t" \
  "v_max_i32_dpp v184, v184, v184 row_half_mirror row_mask:0xf bank_mask:0xf\n\t" \
  "s_nop 1\n\t" \
  "v_max_i32_dpp v184, v184, v184 row_mirror row_mask:0xf bank_mask:0xf\n\t" \
  "s_mov_b64 exec, -1\n\t" \
  "v_readlane_b32 s57, v184, 0\n\t" \
  "v_readlane_b32 s82, v184, 16\n\t" \
  "s_waitcnt lgkmcnt(0)\n\t" \
  "s_sub_i32 s79, s41, s86\n\t" \
  "s_max_i32 s57, s57, s82\n\t" \
  "s_add_i32 s81, s57, 200\n\t" \
  "s_max_i32 s81, s81, 0\n\t" \
  "s_min_i32 s81, s81, 20000\n\t" \
  "s_mul_i32 s81, s81, s44\n\t" \
  "s_lshr_b32 s81, s81, 16\n\t" \
  "s_add_i32 s79, s79, s81\n\t" \
  "s_add_i32 s79, s79, -370\n\t" \
  ".p2align 4\n\t" \
  "3:\n\t" \
  "s_sub_i32 s79, s79, 33\n\t" \
  "s_cmp_gt_i32 s79, 56\n\t" \
  "s_cbranch_scc1 3b\n\t" \
  "buffer_load_dwordx4 v[72:75], v192, %[ru], s88 offen offset:0 sc1\n\t" \
  "s_nop 4\n\t" \
  "buffer_load_dwordx4 v[76:79], v192, %[ru], s88 offen offset:1024 sc1\n\t" \
  "s_nop 4\n\t" \
  "s_add_i32 s79, s79, 22\n\t" \
  ".p2align 4\n\t" \
  "4:\n\t" \
  "s_sub_i32 s79, s79, 33\n\t" \
  "s_cmp_gt_i32 s79, 56\n\t" \
  "s_cbranch_scc1 4b\n\t" \
  "buffer_load_dwordx4 v[80:83], v192, %[ru], s89 offen offset:0 sc1\n\t" \
  "s_nop 4\n\t" \
  "buffer_load_dwordx4 v[84:87], v192, %[ru], s89 offen offset:1024 sc1\n\t" \
  "s_nop 4\n\t" \
  "s_add_i32 s79, s79, 42\n\t" \
  ".p2align 4\n\t" \
  "5:\n\t" \
  "s_sub_i32 s79, s79, 33\n\t" \
  "s_cmp_gt_i32 s79, 56\n\t" \
  "s_cbranch_scc1 5b\n\t" \
  "buffer_load_dwordx4 v[88:91], v192, %[ru], s90 offen offset:0 sc1\n\t" \
  "buffer_load_dwordx4 v[92:95], v192, %[ru], s90 offen offset:1024 sc1\n\t" \
  "s_add_i32 s79, s79, 122\n\t" \
  ".p2align 4\n\t" \
  "9:\n\t" \
  "s_sub_i32 s79, s79, 33\n\t" \
  "s_cmp_gt_i32 s79, 56\n\t" \
  "s_cbranch_scc1 9b\n\t" \
  "buffer_load_dwordx4 v[104:107], v194, %[rh], s92 offen sc1\n\t" \
  "s_nop 4\n\t" \
  "buffer_load_dwordx4 v[108:111], v194, %[rh], s93 offen sc1\n\t" \
  "s_nop 4\n\t" \
  "buffer_load_dwordx4 v[112:115], v194, %[rh], s94 offen sc1\n\t" \
  "s_nop 4\n\t" \
  "buffer_load_dwordx4 v[116:119], v194, %[rh], s95 offen sc1\n\t" \
  "s_nop 4\n\t" \
  "buffer_load_dwordx4 v[96:99], v192, %[ru], s91 offen offset:0 sc1\n\t" \
  "s_nop 4\n\t" \
  "buffer_load_dwordx4 v[100:103], v192, %[ru], s91 offen offset:1024 sc1\n\t" \
  "s_nop 4\n\t" \
  "10:\n\t" \
  "s_waitcnt vmcnt(5)\n\t" \
  "v_mfma_f32_16x16x32_bf16 v[136:139], v[104:107], v[72:75], 0\n\t" \
  "v_mfma_f32_16x16x32_bf16 v[140:143], v[104:107], v[76:79], 0\n\t" \
  "s_waitcnt vmcnt(4)\n\t" \
  "v_mfma_f32_16x16x32_bf16 v[136:139], v[108:111], v[80:83], v[136:139]\n\t" \
  "v_mfma_f32_16x16x32_bf16 v[140:143], v[108:111], v[84:87], v[140:143]\n\t" \
  "s_waitcnt vmcnt(3)\n\t" \
  "v_mfma_f32_16x16x32_bf16 v[136:139], v[112:115], v[88:91], v[136:139]\n\t" \
  "v_mfma_f32_16x16x32_bf16 v[140:143], v[112:115], v[92:95], v[140:143]\n\t" \
  "s_waitcnt vmcnt(1)\n\t" \
  "v_mfma_f32_16x16x32_bf16 v[136:139], v[116:119], v[96:99], v[136:139]\n\t" \
  "s_waitcnt vmcnt(0)\n\t" \
  "v_mfma_f32_16x16x32_bf16 v[140:143], v[116:119], v[100:103], v[140:143]\n\t" \
  "s_barrier\n\t" \
  "ds_read2st64_b64 v[144:147], v195 offset1:1\n\t" \
  "s_nop 1\n\t" \
  "v_permlane32_swap_b32 v136, v138\n\t" \
  "v_permlane32_swap_b32 v137, v139\n\t" \
  "s_nop 1\n\t" \
  "v_permlane32_swap_b32 v140, v142\n\t" \
  "v_permlane32_swap_b32 v141, v143\n\t" \
  "v_add_f32 v136, v136, v142\n\t" \
  "v_add_f32 v137, v137, v143\n\t" \
  "s_waitcnt lgkmcnt(0)\n\t" \
  "v_add_f32 v136, v136, v144\n\t" \
  "v_add_f32 v137, v137, v145\n\t" \
  "v_add_f32 v136, v136, v146\n\t" \
  "v_add_f32 v137, v137, v147\n\t" \
  "v_cvt_pk_bf16_f32 v148, v136, v137\n\t" \
  "s_nop 1\n\t" \
  "v_mov_b32_dpp v149, v148 row_half_mirror row_mask:0xf bank_mask:0xf\n\t" \
  "v_perm_b32 v150, v148, v149, v205\n\t" \
  "v_cmpx_o_f16_sdwa vcc, v150, v150 src0_sel:WORD_0 src1_sel:WORD_1\n\t" \
  "6:\n\t" \
  "buffer_store_dword v150, v201, %[rh], s97 offen sc1\n\t" \
  "s_memtime s[82:83]\n\t" \
  "s_mov_b64 exec, -1\n\t" \
  "s_cmp_eq_u64 vcc, -1\n\t" \
  "s_cbranch_scc0 8f\n\t" \
  "v_mov_b32 v151, 0\n\t" \
  "5:\n\t" \
  "ds_write_b32 v206, v151\n\t" \
  "s_waitcnt lgkmcnt(0)\n\t" \
  "s_mov_b32 s56, s82\n\t" \
  "s_barrier\n\t" \
  "ds_read2_b32 v[148:149], v207 offset1:8\n\t" \
  "s_waitcnt lgkmcnt(0)\n\t" \
  "v_or_b32 v148, v148, v149\n\t" \
  "v_cmp_ne_u32 vcc, 0, v148\n\t" \
  "s_cbranch_vccnz 12f\n\t" \
  "s_and_b32 s58, s58, 4\n\t" \
  "25:\n\t" \
  "s_add_i32 s82, s40, 2\n\t" \
  "s_min_i32 s83, s82, 63\n\t" \
  "s_mov_b32 exec_hi, 0\n\t" \
  "v_cmp_gt_i32 vcc, s83, v186\n\t" \
  "s_mov_b64 exec, -1\n\t" \
  "s_and_b32 s81, s82, 15\n\t" \
  "s_lshl_b32 s81, s81, 22\n\t" \
  "s_add_i32 s81, s81, s51\n\t" \
  "s_lshr_b32 s80, s81, 21\n\t" \
  "s_bitcmp0_b64 %[sel], s80\n\t" \
  "s_cselect_b32 s80, 0x2000, 0\n\t" \
  "s_add_i32 s81, s81, s80\n\t" \
  "s_cbranch_vccnz 27f\n\t" \
  "28:\n\t" \
  "s_cmp_ge_i32 s40, 62\n\t" \
  "s_cbranch_scc1 37f\n\t" \
  "s_and_b32 s76, s40, 3\n\t" \
  "s_lshl_b32 s76, s76, 18\n\t" \
  "buffer_load_dwordx4 v[0:3], v210, %[ru], s81 offen offset:0 sc1\n\t" \
  "buffer_load_dwordx4 v[4:7], v210, %[ru], s81 offen offset:1024 sc1\n\t" \
  "s_add_i32 s81, s81, 0x20000\n\t" \
  "buffer_load_dwordx4 v[8:11], v210, %[ru], s81 offen offset:0 sc1\n\t" \
  "buffer_load_dwordx4 v[12:15], v210, %[ru], s81 offen offset:1024 sc1\n\t" \
  "s_add_i32 s81, s81, 0x20000\n\t" \
  "buffer_load_dwordx4 v[16:19], v210, %[ru], s81 offen offset:0 sc1\n\t" \
  "buffer_load_dwordx4 v[20:23], v210, %[ru], s81 offen offset:1024 sc1\n\t" \
  "s_add_i32 s81, s81, 0x20000\n\t" \
  "buffer_load_dwordx4 v[24:27], v210, %[ru], s81 offen offset:0 sc1\n\t" \
  "buffer_load_dwordx4 v[28:31], v210, %[ru], s81 offen offset:1024 sc1\n\t" \
  "s_add_i32 s81, s81, 0x20000\n\t" \
  "s_bitcmp1_b32 s58, 4\n\t" \
  "s_cbranch_scc1 29f\n\t" \
  "s_sub_i32 s80, s56, s41\n\t" \
  "s_mul_hi_u32 s80, s80, s45\n\t" \
  "s_lshl_b32 s80, s80, 1\n\t" \
  "s_sub_i32 s80, s80, s57\n\t" \
  "s_min_u32 s47, s47, s46\n\t" \
  "s_min_u32 s47, s47, s80\n\t" \
  "v_mov_b32 v188, s47\n\t" \
  "s_mov_b32 s47, s46\n\t" \
  "s_mov_b32 s46, s80\n\t" \
  "buffer_store_dword v188, v202, %[rh], s52 offen sc1\n\t" \
  "v_mov_b64 v[188:189], -1\n\t" \
  "v_mov_b64 v[190:191], -1\n\t" \
  "v_mov_b32 v187, s40\n\t" \
  "s_add_i32 s80, s40, -1\n\t" \
  "s_and_b32 s80, s80, 7\n\t" \
  "s_lshl_b32 s80, s80, 15\n\t" \
  "s_add_i32 s80, s80, s49\n\t" \
  "buffer_store_dwordx4 v[188:191], v208, %[rh], s80 offen sc1\n\t" \
  "buffer_store_dword v187, v209, %[ru], 0 offen sc1\n\t" \
  "ds_write_b128 v197, v[72:75] offset:16384\n\t" \
  "ds_write_b128 v197, v[76:79] offset:17408\n\t" \
  "ds_write_b128 v197, v[80:83] offset:18432\n\t" \
  "ds_write_b128 v197, v[84:87] offset:19456\n\t" \
  "ds_write_b128 v197, v[88:91] offset:20480\n\t" \
  "ds_write_b128 v197, v[92:95] offset:21504\n\t" \
  "ds_write_b128 v197, v[96:99] offset:22528\n\t" \
  "ds_write_b128 v197, v[100:103] offset:23552\n\t" \
  "29:\n\t" \
  "s_and_b32 s58, s58, 36\n\t" \
  "buffer_load_dwordx4 v[32:35], v210, %[ru], s81 offen offset:0 sc1\n\t" \
  "buffer_load_dwordx4 v[36:39], v210, %[ru], s81 offen offset:1024 sc1\n\t" \
  "s_add_i32 s81, s81, 0x20000\n\t" \
  "buffer_load_dwordx4 v[40:43], v210, %[ru], s81 offen offset:0 sc1\n\t" \
  "buffer_load_dwordx4 v[44:47], v210, %[ru], s81 offen offset:1024 sc1\n\t" \
  "s_add_i32 s81, s81, 0x20000\n\t" \
  "buffer_load_dwordx4 a[0:3], v211, %[rh], s76 offen offset:0 sc1\n\t" \
  "buffer_load_dwordx4 a[4:7], v211, %[rh], s76 offen offset:1024 sc1\n\t" \
  "s_waitcnt lgkmcnt(0)\n\t" \
  "s_barrier\n\t" \
  "buffer_load_dwordx4 v[48:51], v210, %[ru], s81 offen offset:0 sc1\n\t" \
  "buffer_load_dwordx4 v[52:55], v210, %[ru], s81 offen offset:1024 sc1\n\t" \
  "s_add_i32 s81, s81, 0x20000\n\t" \
  "buffer_load_dwordx4 v[56:59], v210, %[ru], s81 offen offset:0 sc1\n\t" \
  "buffer_load_dwordx4 v[60:63], v210, %[ru], s81 offen offset:1024 sc1\n\t" \
  "s_add_i32 s81, s81, 0x20000\n\t" \
  "buffer_load_dwordx4 a[8:11], v212, %[rh], s76 offen offset:0 sc1\n\t" \
  "buffer_load_dwordx4 a[12:15], v212, %[rh], s76 offen offset:1024 sc1\n\t" \
  "buffer_load_dwordx4 v[64:67], v210, %[ru], s81 offen offset:0 sc1\n\t" \
  "buffer_load_dwordx4 v[68:71], v210, %[ru], s81 offen offset:1024 sc1\n\t" \
  "s_add_i32 s81, s81, 0x20000\n\t" \
  "buffer_load_dwordx4 v[104:107], v210, %[ru], s81 offen offset:0 sc1\n\t" \
  "buffer_load_dwordx4 v[108:111], v210, %[ru], s81 offen offset:1024 sc1\n\t" \
  "s_add_i32 s81, s81, 0x20000\n\t" \
  "buffer_load_dwordx4 a[16:19], v213, %[rh], s76 offen offset:0 sc1\n\t" \
  "buffer_load_dwordx4 a[20:23], v213, %[rh], s76 offen offset:1024 sc1\n\t" \
  "ds_read_b128 v[160:163], v193 offset:0\n\t" \
  "ds_read_b128 v[164:167], v193 offset:256\n\t" \
  "ds_read_b128 v[168:171], v193 offset:2048\n\t" \
  "ds_read_b128 v[172:175], v193 offset:2304\n\t" \
  "ds_read_b128 v[176:179], v193 offset:4096\n\t" \
  "ds_read_b128 v[180:183], v193 offset:4352\n\t" \
  "s_waitcnt lgkmcnt(2)\n\t" \
  "v_pk_add_f32 v[160:161], v[160:161], v[168:169]\n\t" \
  "v_pk_add_f32 v[162:163], v[162:163], v[170:171]\n\t" \
  "v_pk_add_f32 v[164:165], v[164:165], v[172:173]\n\t" \
  "v_pk_add_f32 v[166:167], v[166:167], v[174:175]\n\t" \
  "s_waitcnt lgkmcnt(0)\n\t" \
  "v_pk_add_f32 v[160:161], v[160:161], v[176:177]\n\t" \
  "v_pk_add_f32 v[162:163], v[162:163], v[178:179]\n\t" \
  "v_pk_add_f32 v[164:165], v[164:165], v[180:181]\n\t" \
  "v_pk_add_f32 v[166:167], v[166:167], v[182:183]\n\t" \
  "v_cvt_pk_bf16_f32 v160, v160, v161\n\t" \
  "v_cvt_pk_bf16_f32 v161, v162, v163\n\t" \
  "v_cvt_pk_bf16_f32 v162, v164, v165\n\t" \
  "v_cvt_pk_bf16_f32 v163, v166, v167\n\t" \
  "buffer_load_dwordx4 v[112:115], v210, %[ru], s81 offen offset:0 sc1\n\t" \
  "buffer_load_dwordx4 v[116:119], v210, %[ru], s81 offen offset:1024 sc1\n\t" \
  "s_add_i32 s81, s81, 0x20000\n\t" \
  "buffer_load_dwordx4 v[120:123], v210, %[ru], s81 offen offset:0 sc1\n\t" \
  "buffer_load_dwordx4 v[124:127], v210, %[ru], s81 offen offset:1024 sc1\n\t" \
  "s_add_i32 s81, s81, 0x20000\n\t" \
  "buffer_load_dwordx4 a[24:27], v214, %[rh], s76 offen offset:0 sc1\n\t" \
  "buffer_load_dwordx4 a[28:31], v214, %[rh], s76 offen offset:1024 sc1\n\t" \
  "s_waitcnt vmcnt(28)\n\t" \
  "v_swap_b32 v1, v8\n\t" \
  "v_swap_b32 v3, v10\n\t" \
  "v_swap_b32 v5, v12\n\t" \
  "v_swap_b32 v7, v14\n\t" \
  "ds_write_b128 v204, v[0:3] offset:0\n\t" \
  "ds_write_b128 v204, v[8:11] offset:1024\n\t" \
  "ds_write_b128 v204, v[4:7] offset:2048\n\t" \
  "ds_write_b128 v204, v[12:15] offset:3072\n\t" \
  "buffer_load_dwordx4 v[128:131], v210, %[ru], s81 offen offset:0 sc1\n\t" \
  "buffer_load_dwordx4 v[132:135], v210, %[ru], s81 offen offset:1024 sc1\n\t" \
  "s_add_i32 s81, s81, 0x20000\n\t" \
  "buffer_load_dwordx4 v[136:139], v210, %[ru], s81 offen offset:0 sc1\n\t" \
  "buffer_load_dwordx4 v[140:143], v210, %[ru], s81 offen offset:1024 sc1\n\t" \
  "s_add_i32 s81, s81, 0x20000\n\t" \
  "s_waitcnt vmcnt(28)\n\t" \
  "v_swap_b32 v17, v24\n\t" \
  "v_swap_b32 v19, v26\n\t" \
  "v_swap_b32 v21, v28\n\t" \
  "v_swap_b32 v23, v30\n\t" \
  "ds_write_b128 v204, v[16:19] offset:4096\n\t" \
  "ds_write_b128 v204, v[24:27] offset:5120\n\t" \
  "ds_write_b128 v204, v[20:23] offset:6144\n\t" \
  "ds_write_b128 v204, v[28:31] offset:7168\n\t" \
  "buffer_load_dwordx4 v[144:147], v210, %[ru], s81 offen offset:0 sc1\n\t" \
  "buffer_load_dwordx4 v[148:151], v210, %[ru], s81 offen offset:1024 sc1\n\t" \
  "s_add_i32 s81, s81, 0x20000\n\t" \
  "buffer_load_dwordx4 v[152:155], v210, %[ru], s81 offen offset:0 sc1\n\t" \
  "buffer_load_dwordx4 v[156:159], v210, %[ru], s81 offen offset:1024 sc1\n\t" \
  "s_add_i32 s81, s81, 0x20000\n\t" \
  "s_waitcnt vmcnt(28)\n\t" \
  "v_swap_b32 v33, v40\n\t" \
  "v_swap_b32 v35, v42\n\t" \
  "v_swap_b32 v37, v44\n\t" \
  "v_swap_b32 v39, v46\n\t" \
  "ds_write_b128 v204, v[32:35] offset:8192\n\t" \
  "ds_write_b128 v204, v[40:43] offset:9216\n\t" \
  "ds_write_b128 v204, v[36:39] offset:10240\n\t" \
  "ds_write_b128 v204, v[44:47] offset:11264\n\t" \
  "s_waitcnt vmcnt(22)\n\t" \
  "v_swap_b32 v49, v56\n\t" \
  "v_swap_b32 v51, v58\n\t" \
  "v_swap_b32 v53, v60\n\t" \
  "v_swap_b32 v55, v62\n\t" \
  "ds_write_b128 v204, v[48:51] offset:12288\n\t" \
  "ds_write_b128 v204, v[56:59] offset:13312\n\t" \
  "ds_write_b128 v204, v[52:55] offset:14336\n\t" \
  "ds_write_b128 v204, v[60:63] offset:15360\n\t" \
  "s_waitcnt vmcnt(16)\n\t" \
  "v_swap_b32 v65, v104\n\t" \
  "v_swap_b32 v67, v106\n\t" \
  "v_swap_b32 v69, v108\n\t" \
  "v_swap_b32 v71, v110\n\t" \
  "ds_write_b128 v204, v[64:67] offset:16384\n\t" \
  "ds_write_b128 v204, v[104:107] offset:17408\n\t" \
  "ds_write_b128 v204, v[68:71] offset:18432\n\t" \
  "ds_write_b128 v204, v[108:111] offset:19456\n\t" \
  "s_waitcnt vmcnt(10)\n\t" \
  "v_swap_b32 v113, v120\n\t" \
  "v_swap_b32 v115, v122\n\t" \
  "v_swap_b32 v117, v124\n\t" \
  "v_swap_b32 v119, v126\n\t" \
  "ds_write_b128 v204, v[112:115] offset:20480\n\t" \
  "ds_write_b128 v204, v[120:123] offset:21504\n\t" \
  "ds_write_b128 v204, v[116:119] offset:22528\n\t" \
  "ds_write_b128 v204, v[124:127] offset:23552\n\t" \
  "s_waitcnt vmcnt(4)\n\t" \
  "v_swap_b32 v129, v136\n\t" \
  "v_swap_b32 v131, v138\n\t" \
  "v_swap_b32 v133, v140\n\t" \
  "v_swap_b32 v135, v142\n\t" \
  "ds_write_b128 v204, v[128:131] offset:24576\n\t" \
  "ds_write_b128 v204, v[136:139] offset:25600\n\t" \
  "ds_write_b128 v204, v[132:135] offset:26624\n\t" \
  "ds_write_b128 v204, v[140:143] offset:27648\n\t" \
  "s_waitcnt vmcnt(0)\n\t" \
  "v_swap_b32 v145, v152\n\t" \
  "v_swap_b32 v147, v154\n\t" \
  "v_swap_b32 v149, v156\n\t" \
  "v_swap_b32 v151, v158\n\t" \
  "ds_write_b128 v204, v[144:147] offset:28672\n\t" \
  "ds_write_b128 v204, v[152:155] offset:29696\n\t" \
  "ds_write_b128 v204, v[148:151] offset:30720\n\t" \
  "ds_write_b128 v204, v[156:159] offset:31744\n\t" \
  "s_waitcnt lgkmcnt(0)\n\t" \
  "s_cmp_lt_i32 s40, 0\n\t" \
  "s_cbranch_scc1 39f\n\t" \
  "38:\n\t" \
  "s_add_i32 s80, s40, -2\n\t" \
  "s_lshl_b32 s80, s80, 17\n\t" \
  "s_min_u32 s80, s80, 0x70000000\n\t" \
  "buffer_store_dwordx4 v[160:163], v196, %[ro], s80 offen nt\n\t" \
  "ds_read_b128 a[32:35], v198 offset:0\n\t" \
  "ds_read_b128 a[36:39], v198 offset:1024\n\t" \
  "ds_read_b128 a[40:43], v198 offset:2048\n\t" \
  "ds_read_b128 a[44:47], v198 offset:3072\n\t" \
  "ds_read_b128 a[48:51], v198 offset:4096\n\t" \
  "ds_read_b128 a[52:55], v198 offset:5120\n\t" \
  "ds_read_b128 a[56:59], v198 offset:6144\n\t" \
  "ds_read_b128 a[60:63], v198 offset:7168\n\t" \
  "ds_read_b128 a[64:67], v198 offset:8192\n\t" \
  "ds_read_b128 a[68:71], v198 offset:9216\n\t" \
  "ds_read_b128 a[72:75], v198 offset:10240\n\t" \
  "ds_read_b128 a[76:79], v198 offset:11264\n\t" \
  "ds_read_b128 a[80:83], v198 offset:12288\n\t" \
  "ds_read_b128 a[84:87], v198 offset:13312\n\t" \
  "ds_read_b128 a[88:91], v198 offset:14336\n\t" \
  "ds_read_b128 a[92:95], v198 offset:15360\n\t" \
  "s_waitcnt vmcnt(1) lgkmcnt(0)\n\t" \
  "31:\n\t" \
  "v_mfma_f32_16x16x32_bf16 v[168:171], a[32:35], a[0:3], 0\n\t" \
  "v_mfma_f32_16x16x32_bf16 v[172:175], a[64:67], a[0:3], 0\n\t" \
  "v_mfma_f32_16x16x32_bf16 v[168:171], a[36:39], a[4:7], v[168:171]\n\t" \
  "v_mfma_f32_16x16x32_bf16 v[172:175], a[68:71], a[4:7], v[172:175]\n\t" \
  "v_mfma_f32_16x16x32_bf16 v[168:171], a[40:43], a[8:11], v[168:171]\n\t" \
  "v_mfma_f32_16x16x32_bf16 v[172:175], a[72:75], a[8:11], v[172:175]\n\t" \
  "v_mfma_f32_16x16x32_bf16 v[168:171], a[44:47], a[12:15], v[168:171]\n\t" \
  "v_mfma_f32_16x16x32_bf16 v[172:175], a[76:79], a[12:15], v[172:175]\n\t" \
  "v_mfma_f32_16x16x32_bf16 v[168:171], a[48:51], a[16:19], v[168:171]\n\t" \
  "v_mfma_f32_16x16x32_bf16 v[172:175], a[80:83], a[16:19], v[172:175]\n\t" \
  "v_mfma_f32_16x16x32_bf16 v[168:171], a[52:55], a[20:23], v[168:171]\n\t" \
  "v_mfma_f32_16x16x32_bf16 v[172:175], a[84:87], a[20:23], v[172:175]\n\t" \
  "v_mfma_f32_16x16x32_bf16 v[168:171], a[56:59], a[24:27], v[168:171]\n\t" \
  "v_mfma_f32_16x16x32_bf16 v[172:175], a[88:91], a[24:27], v[172:175]\n\t" \
  "v_mfma_f32_16x16x32_bf16 v[168:171], a[60:63], a[28:31], v[168:171]\n\t" \
  "v_mfma_f32_16x16x32_bf16 v[172:175], a[92:95], a[28:31], v[172:175]\n\t" \
  "s_nop 7\n\t" \
  "39:\n\t" \
  "s_add_i32 s40, s40, 2\n\t" \
  "v_cmp_u_f32 vcc, v168, v168\n\t" \
  "s_cbranch_vccnz 32f\n\t" \
  "33:\n\t" \
  "s_barrier\n\t" \
  "s_add_i32 s85, s40, -1\n\t" \
  "s_and_b32 s82, s85, 2\n\t" \
  "s_lshl_b32 s82, s82, 13\n\t" \
  "s_and_b32 s80, s85, 1\n\t" \
  "s_lshl_b32 s80, s80, 12\n\t" \
  "s_add_i32 s82, s82, s80\n\t" \
  "s_add_i32 s82, s82, 0x84000\n\t" \
  "s_mov_b32 exec_hi, 0\n\t" \
  "buffer_load_dword v184, v200, %[rh], s82 offen sc1\n\t" \
  "buffer_load_dword v186, v203, %[ru], 0 offen sc1\n\t" \
  "s_mov_b64 exec, -1\n\t" \
  "ds_write_b128 v199, v[168:171] offset:0\n\t" \
  "ds_write_b128 v199, v[172:175] offset:1024\n\t" \
  "s_mov_b32 s59, 0\n\t" \
  "s_and_b32 s58, s58, 4\n\t" \
  "s_cmp_lt_i32 s40, 64\n\t" \
  "s_cbranch_scc1 20b\n\t" \
  "s_waitcnt lgkmcnt(0)\n\t" \
  "s_barrier\n\t" \
  "ds_read_b128 v[160:163], v193 offset:0\n\t" \
  "ds_read_b128 v[164:167], v193 offset:256\n\t" \
  "ds_read_b128 v[168:171], v193 offset:2048\n\t" \
  "ds_read_b128 v[172:175], v193 offset:2304\n\t" \
  "ds_read_b128 v[176:179], v193 offset:4096\n\t" \
  "ds_read_b128 v[180:183], v193 offset:4352\n\t" \
  "s_waitcnt lgkmcnt(2)\n\t" \
  "v_pk_add_f32 v[160:161], v[160:161], v[168:169]\n\t" \
  "v_pk_add_f32 v[162:163], v[162:163], v[170:171]\n\t" \
  "v_pk_add_f32 v[164:165], v[164:165], v[172:173]\n\t" \
  "v_pk_add_f32 v[166:167], v[166:167], v[174:175]\n\t" \
  "s_waitcnt lgkmcnt(0)\n\t" \
  "v_pk_add_f32 v[160:161], v[160:161], v[176:177]\n\t" \
  "v_pk_add_f32 v[162:163], v[162:163], v[178:179]\n\t" \
  "v_pk_add_f32 v[164:165], v[164:165], v[180:181]\n\t" \
  "v_pk_add_f32 v[166:167], v[166:167], v[182:183]\n\t" \
  "v_cvt_pk_bf16_f32 v160, v160, v161\n\t" \
  "v_cvt_pk_bf16_f32 v161, v162, v163\n\t" \
  "v_cvt_pk_bf16_f32 v162, v164, v165\n\t" \
  "v_cvt_pk_bf16_f32 v163, v166, v167\n\t" \
  "s_add_i32 s80, s40, -2\n\t" \
  "s_lshl_b32 s80, s80, 17\n\t" \
  "s_min_u32 s80, s80, 0x70000000\n\t" \
  "buffer_store_dwordx4 v[160:163], v196, %[ro], s80 offen nt\n\t" \
  "s_mov_b32 s58, 64\n\t" \
  "s_branch 7f\n\t" \
  "22:\n\t" \
  "s_mov_b32 s81, 0\n\t" \
  "s_cmp_eq_u32 s40, 0\n\t" \
  "s_cbranch_scc1 24b\n\t" \
  "s_bitcmp1_b32 s58, 2\n\t" \
  "s_cbranch_scc1 24b\n\t" \
  "s_mov_b64 exec, -1\n\t" \
  "s_sleep 10\n\t" \
  "s_add_u32 s59, s59, 0x10000\n\t" \
  "s_cmp_lt_u32 s59, 0x100000\n\t" \
  "s_cbranch_scc1 19b\n\t" \
  "s_or_b32 s58, s58, 1\n\t" \
  "s_branch 7f\n\t" \
  "14:\n\t" \
  "v_mov_b32 v184, 0\n\t" \
  "s_bitcmp1_b32 s58, 2\n\t" \
  "s_nop 0\n\t" \
  "s_cbranch_scc1 15b\n\t" \
  "s_mov_b32 s84, 950\n\t" \
  "s_add_u32 s59, s59, 0x10000\n\t" \
  "s_cmp_lt_u32 s59, 0x100000\n\t" \
  "s_mov_b64 exec, vcc\n\t" \
  "buffer_load_dword v187, v200, %[rh], s78 offen sc1\n\t" \
  "s_mov_b32 exec_lo, -1\n\t" \
  "s_cbranch_scc1 13b\n\t" \
  "s_mov_b64 exec, -1\n\t" \
  "s_or_b32 s58, s58, 1\n\t" \
  "s_branch 7f\n\t" \
  "27:\n\t" \
  "s_bitcmp1_b32 s58, 2\n\t" \
  "s_cbranch_scc1 28b\n\t" \
  "s_bitcmp1_b32 s58, 4\n\t" \
  "s_cbranch_scc1 23f\n\t" \
  "s_sub_i32 s80, s56, s41\n\t" \
  "s_mul_hi_u32 s80, s80, s45\n\t" \
  "s_lshl_b32 s80, s80, 1\n\t" \
  "s_sub_i32 s80, s80, s57\n\t" \
  "s_min_u32 s47, s47, s46\n\t" \
  "s_min_u32 s47, s47, s80\n\t" \
  "v_mov_b32 v188, s47\n\t" \
  "s_mov_b32 s47, s46\n\t" \
  "s_mov_b32 s46, s80\n\t" \
  "buffer_store_dword v188, v202, %[rh], s52 offen sc1\n\t" \
  "v_mov_b64 v[188:189], -1\n\t" \
  "v_mov_b64 v[190:191], -1\n\t" \
  "v_mov_b32 v187, s40\n\t" \
  "s_add_i32 s80, s40, -1\n\t" \
  "s_and_b32 s80, s80, 7\n\t" \
  "s_lshl_b32 s80, s80, 15\n\t" \
  "s_add_i32 s80, s80, s49\n\t" \
  "buffer_store_dwordx4 v[188:191], v208, %[rh], s80 offen sc1\n\t" \
  "buffer_store_dword v187, v209, %[ru], 0 offen sc1\n\t" \
  "ds_write_b128 v197, v[72:75] offset:16384\n\t" \
  "ds_write_b128 v197, v[76:79] offset:17408\n\t" \
  "ds_write_b128 v197, v[80:83] offset:18432\n\t" \
  "ds_write_b128 v197, v[84:87] offset:19456\n\t" \
  "ds_write_b128 v197, v[88:91] offset:20480\n\t" \
  "ds_write_b128 v197, v[92:95] offset:21504\n\t" \
  "ds_write_b128 v197, v[96:99] offset:22528\n\t" \
  "ds_write_b128 v197, v[100:103] offset:23552\n\t" \
  "s_or_b32 s58, s58, 16\n\t" \
  "23:\n\t" \
  "s_add_u32 s59, s59, 0x1000000\n\t" \
  "s_cmp_lt_u32 s59, 0x40000000\n\t" \
  "s_cbranch_scc1 26f\n\t" \
  "s_or_b32 s58, s58, 1\n\t" \
  "s_branch 7f\n\t" \
  "26:\n\t" \
  "buffer_load_dword v186, v203, %[ru], 0 offen sc1\n\t" \
  "s_waitcnt vmcnt(0)\n\t" \
  "s_branch 25b\n\t" \
  "37:\n\t" \
  "s_and_b32 s76, s40, 3\n\t" \
  "s_lshl_b32 s76, s76, 18\n\t" \
  "buffer_load_dwordx4 a[0:3], v211, %[rh], s76 offen offset:0 sc1\n\t" \
  "buffer_load_dwordx4 a[4:7], v211, %[rh], s76 offen offset:1024 sc1\n\t" \
  "buffer_load_dwordx4 a[8:11], v212, %[rh], s76 offen offset:0 sc1\n\t" \
  "buffer_load_dwordx4 a[12:15], v212, %[rh], s76 offen offset:1024 sc1\n\t" \
  "buffer_load_dwordx4 a[16:19], v213, %[rh], s76 offen offset:0 sc1\n\t" \
  "buffer_load_dwordx4 a[20:23], v213, %[rh], s76 offen offset:1024 sc1\n\t" \
  "buffer_load_dwordx4 a[24:27], v214, %[rh], s76 offen offset:0 sc1\n\t" \
  "buffer_load_dwordx4 a[28:31], v214, %[rh], s76 offen offset:1024 sc1\n\t" \
  "s_bitcmp1_b32 s58, 4\n\t" \
  "s_cbranch_scc1 29f\n\t" \
  "s_sub_i32 s80, s56, s41\n\t" \
  "s_mul_hi_u32 s80, s80, s45\n\t" \
  "s_lshl_b32 s80, s80, 1\n\t" \
  "s_sub_i32 s80, s80, s57\n\t" \
  "s_min_u32 s47, s47, s46\n\t" \
  "s_min_u32 s47, s47, s80\n\t" \
  "v_mov_b32 v188, s47\n\t" \
  "s_mov_b32 s47, s46\n\t" \
  "s_mov_b32 s46, s80\n\t" \
  "buffer_store_dword v188, v202, %[rh], s52 offen sc1\n\t" \
  "v_mov_b64 v[188:189], -1\n\t" \
  "v_mov_b64 v[190:191], -1\n\t" \
  "v_mov_b32 v187, s40\n\t" \
  "s_add_i32 s80, s40, -1\n\t" \
  "s_and_b32 s80, s80, 7\n\t" \
  "s_lshl_b32 s80, s80, 15\n\t" \
  "s_add_i32 s80, s80, s49\n\t" \
  "buffer_store_dwordx4 v[188:191], v208, %[rh], s80 offen sc1\n\t" \
  "buffer_store_dword v187, v209, %[ru], 0 offen sc1\n\t" \
  "ds_write_b128 v197, v[72:75] offset:16384\n\t" \
  "ds_write_b128 v197, v[76:79] offset:17408\n\t" \
  "ds_write_b128 v197, v[80:83] offset:18432\n\t" \
  "ds_write_b128 v197, v[84:87] offset:19456\n\t" \
  "ds_write_b128 v197, v[88:91] offset:20480\n\t" \
  "ds_write_b128 v197, v[92:95] offset:21504\n\t" \
  "ds_write_b128 v197, v[96:99] offset:22528\n\t" \
  "ds_write_b128 v197, v[100:103] offset:23552\n\t" \
  "29:\n\t" \
  "s_and_b32 s58, s58, 36\n\t" \
  "s_waitcnt lgkmcnt(0)\n\t" \
  "s_barrier\n\t" \
  "ds_read_b128 v[160:163], v193 offset:0\n\t" \
  "ds_read_b128 v[164:167], v193 offset:256\n\t" \
  "ds_read_b128 v[168:171], v193 offset:2048\n\t" \
  "ds_read_b128 v[172:175], v193 offset:2304\n\t" \
  "ds_read_b128 v[176:179], v193 offset:4096\n\t" \
  "ds_read_b128 v[180:183], v193 offset:4352\n\t" \
  "s_waitcnt lgkmcnt(2)\n\t" \
  "v_pk_add_f32 v[160:161], v[160:161], v[168:169]\n\t" \
  "v_pk_add_f32 v[162:163], v[162:163], v[170:171]\n\t" \
  "v_pk_add_f32 v[164:165], v[164:165], v[172:173]\n\t" \
  "v_pk_add_f32 v[166:167], v[166:167], v[174:175]\n\t" \
  "s_waitcnt lgkmcnt(0)\n\t" \
  "v_pk_add_f32 v[160:161], v[160:161], v[176:177]\n\t" \
  "v_pk_add_f32 v[162:163], v[162:163], v[178:179]\n\t" \
  "v_pk_add_f32 v[164:165], v[164:165], v[180:181]\n\t" \
  "v_pk_add_f32 v[166:167], v[166:167], v[182:183]\n\t" \
  "v_cvt_pk_bf16_f32 v160, v160, v161\n\t" \
  "v_cvt_pk_bf16_f32 v161, v162, v163\n\t" \
  "v_cvt_pk_bf16_f32 v162, v164, v165\n\t" \
  "v_cvt_pk_bf16_f32 v163, v166, v167\n\t" \
  "s_branch 38b\n\t" \
  "8:\n\t" \
  "s_mov_b64 vcc, -1\n\t" \
  "s_and_b32 s80, s58, 6\n\t" \
  "s_cbranch_scc1 6b\n\t" \
  "v_mov_b32 v151, 1\n\t" \
  "s_branch 5b\n\t" \
  "32:\n\t" \
  "s_and_b32 s80, s58, 36\n\t" \
  "s_cbranch_scc1 33b\n\t" \
  "s_add_i32 s40, s40, -2\n\t" \
  "34:\n\t" \
  "s_or_b32 s59, s59, 128\n\t" \
  "s_and_b32 s76, s40, 3\n\t" \
  "s_lshl_b32 s76, s76, 18\n\t" \
  "35:\n\t" \
  "buffer_load_dwordx4 a[0:3], v211, %[rh], s76 offen offset:0 sc1\n\t" \
  "buffer_load_dwordx4 a[4:7], v211, %[rh], s76 offen offset:1024 sc1\n\t" \
  "buffer_load_dwordx4 a[8:11], v212, %[rh], s76 offen offset:0 sc1\n\t" \
  "buffer_load_dwordx4 a[12:15], v212, %[rh], s76 offen offset:1024 sc1\n\t" \
  "buffer_load_dwordx4 a[16:19], v213, %[rh], s76 offen offset:0 sc1\n\t" \
  "buffer_load_dwordx4 a[20:23], v213, %[rh], s76 offen offset:1024 sc1\n\t" \
  "buffer_load_dwordx4 a[24:27], v214, %[rh], s76 offen offset:0 sc1\n\t" \
  "buffer_load_dwordx4 a[28:31], v214, %[rh], s76 offen offset:1024 sc1\n\t" \
  "s_waitcnt vmcnt(0)\n\t" \
  "s_bitcmp1_b32 s58, 2\n\t" \
  "s_cbranch_scc1 36f\n\t" \
  "v_accvgpr_read_b32 v0, a0\n\t" \
  "v_accvgpr_read_b32 v1, a1\n\t" \
  "v_accvgpr_read_b32 v2, a2\n\t" \
  "v_accvgpr_read_b32 v3, a3\n\t" \
  "v_accvgpr_read_b32 v4, a4\n\t" \
  "v_accvgpr_read_b32 v5, a5\n\t" \
  "v_accvgpr_read_b32 v6, a6\n\t" \
  "v_accvgpr_read_b32 v7, a7\n\t" \
  "v_accvgpr_read_b32 v8, a8\n\t" \
  "v_accvgpr_read_b32 v9, a9\n\t" \
  "v_accvgpr_read_b32 v10, a10\n\t" \
  "v_accvgpr_read_b32 v11, a11\n\t" \
  "v_accvgpr_read_b32 v12, a12\n\t" \
  "v_accvgpr_read_b32 v13, a13\n\t" \
  "v_accvgpr_read_b32 v14, a14\n\t" \
  "v_accvgpr_read_b32 v15, a15\n\t" \
  "v_accvgpr_read_b32 v16, a16\n\t" \
  "v_accvgpr_read_b32 v17, a17\n\t" \
  "v_accvgpr_read_b32 v18, a18\n\t" \
  "v_accvgpr_read_b32 v19, a19\n\t" \
  "v_accvgpr_read_b32 v20, a20\n\t" \
  "v_accvgpr_read_b32 v21, a21\n\t" \
  "v_accvgpr_read_b32 v22, a22\n\t" \
  "v_accvgpr_read_b32 v23, a23\n\t" \
  "v_accvgpr_read_b32 v24, a24\n\t" \
  "v_accvgpr_read_b32 v25, a25\n\t" \
  "v_accvgpr_read_b32 v26, a26\n\t" \
  "v_accvgpr_read_b32 v27, a27\n\t" \
  "v_accvgpr_read_b32 v28, a28\n\t" \
  "v_accvgpr_read_b32 v29, a29\n\t" \
  "v_accvgpr_read_b32 v30, a30\n\t" \
  "v_accvgpr_read_b32 v31, a31\n\t" \
  "v_max_u32 v148, v0, v1\n\t" \
  "v_max3_u32 v148, v148, v2, v3\n\t" \
  "v_max3_u32 v148, v148, v4, v5\n\t" \
  "v_max3_u32 v148, v148, v6, v7\n\t" \
  "v_max3_u32 v148, v148, v8, v9\n\t" \
  "v_max3_u32 v148, v148, v10, v11\n\t" \
  "v_max3_u32 v148, v148, v12, v13\n\t" \
  "v_max3_u32 v148, v148, v14, v15\n\t" \
  "v_max3_u32 v148, v148, v16, v17\n\t" \
  "v_max3_u32 v148, v148, v18, v19\n\t" \
  "v_max3_u32 v148, v148, v20, v21\n\t" \
  "v_max3_u32 v148, v148, v22, v23\n\t" \
  "v_max3_u32 v148, v148, v24, v25\n\t" \
  "v_max3_u32 v148, v148, v26, v27\n\t" \
  "v_max3_u32 v148, v148, v28, v29\n\t" \
  "v_max3_u32 v148, v148, v30, v31\n\t" \
  "v_cmp_eq_u32 vcc, -1, v148\n\t" \
  "s_cbranch_vccz 36f\n\t" \
  "s_add_u32 s59, s59, 0x1000000\n\t" \
  "s_cmp_lt_u32 s59, 0x10000000\n\t" \
  "s_cbranch_scc1 35b\n\t" \
  "s_or_b32 s58, s58, 9\n\t" \
  "s_branch 7f\n\t" \
  "36:\n\t" \
  "s_and_b32 s58, s58, 4\n\t" \
  "s_or_b32 s58, s58, 32\n\t" \
  "s_branch 31b\n\t" \
  "18:\n\t" \
  "v_mov_b32 v186, -1\n\t" \
  "s_and_b32 s80, s40, 3\n\t" \
  "s_mul_i32 s77, s80, 0xc000\n\t" \
  "s_add_i32 s77, s77, 0x40000\n\t" \
  "s_lshl_b32 s78, s80, 14\n\t" \
  "s_add_i32 s78, s78, 0x70000\n\t" \
  "s_mov_b32 s57, 0\n\t" \
  "s_add_i32 s96, s77, 0x1000\n\t" \
  "s_lshl_b32 s88, s80, 20\n\t" \
  "s_add_i32 s88, s88, s48\n\t" \
  "s_lshr_b32 s80, s88, 21\n\t" \
  "s_bitcmp1_b64 %[sel], s80\n\t" \
  "s_cselect_b32 s80, 0x2000, 0\n\t" \
  "s_add_i32 s88, s88, s80\n\t" \
  "s_xor_b32 s89, s88, 0x800\n\t" \
  "s_xor_b32 s90, s88, 0x1000\n\t" \
  "s_xor_b32 s91, s88, 0x1800\n\t" \
  "s_add_i32 s92, s77, s50\n\t" \
  "s_xor_b32 s93, s92, 0x100\n\t" \
  "s_xor_b32 s94, s92, 0x200\n\t" \
  "s_xor_b32 s95, s92, 0x300\n\t" \
  "s_add_i32 s97, s40, 1\n\t" \
  "s_and_b32 s97, s97, 7\n\t" \
  "s_lshl_b32 s97, s97, 15\n\t" \
  "s_add_i32 s97, s97, s49\n\t" \
  "12:\n\t" \
  "s_or_b32 s58, s58, 3\n\t" \
  "11:\n\t" \
  "buffer_load_dwordx4 v[72:75], v192, %[ru], s88 offen offset:0 sc1\n\t" \
  "buffer_load_dwordx4 v[76:79], v192, %[ru], s88 offen offset:1024 sc1\n\t" \
  "buffer_load_dwordx4 v[80:83], v192, %[ru], s89 offen offset:0 sc1\n\t" \
  "buffer_load_dwordx4 v[84:87], v192, %[ru], s89 offen offset:1024 sc1\n\t" \
  "buffer_load_dwordx4 v[88:91], v192, %[ru], s90 offen offset:0 sc1\n\t" \
  "buffer_load_dwordx4 v[92:95], v192, %[ru], s90 offen offset:1024 sc1\n\t" \
  "buffer_load_dwordx4 v[96:99], v192, %[ru], s91 offen offset:0 sc1\n\t" \
  "buffer_load_dwordx4 v[100:103], v192, %[ru], s91 offen offset:1024 sc1\n\t" \
  "buffer_load_dwordx4 v[104:107], v194, %[rh], s92 offen sc1\n\t" \
  "buffer_load_dwordx4 v[108:111], v194, %[rh], s93 offen sc1\n\t" \
  "buffer_load_dwordx4 v[112:115], v194, %[rh], s94 offen sc1\n\t" \
  "buffer_load_dwordx4 v[116:119], v194, %[rh], s95 offen sc1\n\t" \
  "s_waitcnt vmcnt(0)\n\t" \
  "s_add_u32 s59, s59, 1\n\t" \
  "s_bitcmp1_b32 s58, 2\n\t" \
  "s_cbranch_scc1 10b\n\t" \
  "v_max_u32 v148, v72, v73\n\t" \
  "v_max3_u32 v148, v148, v74, v75\n\t" \
  "v_max3_u32 v148, v148, v76, v77\n\t" \
  "v_max3_u32 v148, v148, v78, v79\n\t" \
  "v_max3_u32 v148, v148, v80, v81\n\t" \
  "v_max3_u32 v148, v148, v82, v83\n\t" \
  "v_max3_u32 v148, v148, v84, v85\n\t" \
  "v_max3_u32 v148, v148, v86, v87\n\t" \
  "v_max3_u32 v148, v148, v88, v89\n\t" \
  "v_max3_u32 v148, v148, v90, v91\n\t" \
  "v_max3_u32 v148, v148, v92, v93\n\t" \
  "v_max3_u32 v148, v148, v94, v95\n\t" \
  "v_max3_u32 v148, v148, v96, v97\n\t" \
  "v_max3_u32 v148, v148, v98, v99\n\t" \
  "v_max3_u32 v148, v148, v100, v101\n\t" \
  "v_max3_u32 v148, v148, v102, v103\n\t" \
  "v_max3_u32 v148, v148, v104, v105\n\t" \
  "v_max3_u32 v148, v148, v106, v107\n\t" \
  "v_max3_u32 v148, v148, v108, v109\n\t" \
  "v_max3_u32 v148, v148, v110, v111\n\t" \
  "v_max3_u32 v148, v148, v112, v113\n\t" \
  "v_max3_u32 v148, v148, v114, v115\n\t" \
  "v_max3_u32 v148, v148, v116, v117\n\t" \
  "v_max3_u32 v148, v148, v118, v119\n\t" \
  "v_cmp_eq_u32 vcc, -1, v148\n\t" \
  "s_cbranch_vccz 10b\n\t" \
  "s_bitcmp1_b32 s59, 6\n\t" \
  "s_cbranch_scc0 11b\n\t" \
  "7:\n\t" \
  "s_branch 92f\n\t" \
  "91:\n\t" \
  "s_mov_b32 s58, s42\n\t" \
  "s_mov_b32 s59, 0\n\t" \
  "s_bitcmp1_b32 s58, 4\n\t" \
  "s_cbranch_scc1 37f\n\t" \
  "s_bitcmp1_b32 s58, 3\n\t" \
  "s_cbranch_scc1 34f\n\t" \
  "s_bitcmp1_b32 s58, 1\n\t" \
  "s_cbranch_scc1 18f\n\t" \
  "17:\n\t" \
  "v_mov_b64 v[40:41], 0\n\t" \
  "v_mov_b64 v[42:43], 0\n\t" \
  "v_mov_b64 v[44:45], 0\n\t" \
  "v_mov_b64 v[46:47], 0\n\t" \
  "v_mov_b64 v[48:49], 0\n\t" \
  "v_mov_b64 v[50:51], 0\n\t" \
  "v_mov_b64 v[52:53], 0\n\t" \
  "v_mov_b64 v[54:55], 0\n\t" \
  "v_mov_b64 v[56:57], 0\n\t" \
  "v_mov_b64 v[58:59], 0\n\t" \
  "v_mov_b64 v[60:61], 0\n\t" \
  "v_mov_b64 v[62:63], 0\n\t" \
  "v_mov_b64 v[64:65], 0\n\t" \
  "v_mov_b64 v[66:67], 0\n\t" \
  "v_mov_b64 v[68:69], 0\n\t" \
  "v_mov_b64 v[70:71], 0\n\t" \
  "v_mov_b64 v[72:73], 0\n\t" \
  "v_mov_b64 v[74:75], 0\n\t" \
  "v_mov_b64 v[76:77], 0\n\t" \
  "v_mov_b64 v[78:79], 0\n\t" \
  "v_mov_b64 v[80:81], 0\n\t" \
  "v_mov_b64 v[82:83], 0\n\t" \
  "v_mov_b64 v[84:85], 0\n\t" \
  "v_mov_b64 v[86:87], 0\n\t" \
  "v_mov_b64 v[88:89], 0\n\t" \
  "v_mov_b64 v[90:91], 0\n\t" \
  "v_mov_b64 v[92:93], 0\n\t" \
  "v_mov_b64 v[94:95], 0\n\t" \
  "v_mov_b64 v[96:97], 0\n\t" \
  "v_mov_b64 v[98:99], 0\n\t" \
  "v_mov_b64 v[100:101], 0\n\t" \
  "v_mov_b64 v[102:103], 0\n\t" \
  "v_mov_b64 v[136:137], 0\n\t" \
  "v_mov_b64 v[138:139], 0\n\t" \
  "v_mov_b64 v[140:141], 0\n\t" \
  "v_mov_b64 v[142:143], 0\n\t" \
  "19:\n\t" \
  "s_add_i32 s85, s40, -1\n\t" \
  "s_and_b32 s82, s85, 2\n\t" \
  "s_lshl_b32 s82, s82, 13\n\t" \
  "s_and_b32 s80, s85, 1\n\t" \
  "s_lshl_b32 s80, s80, 12\n\t" \
  "s_add_i32 s82, s82, s80\n\t" \
  "s_add_i32 s82, s82, 0x84000\n\t" \
  "s_mov_b32 exec_hi, 0\n\t" \
  "buffer_load_dword v184, v200, %[rh], s82 offen sc1\n\t" \
  "buffer_load_dword v185, v200, %[rh], s52 offen sc1\n\t" \
  "s_mov_b64 exec, -1\n\t" \
  "20:\n\t" \
  "s_and_b32 s80, s40, 3\n\t" \
  "s_mul_i32 s77, s80, 0xc000\n\t" \
  "s_add_i32 s77, s77, 0x40000\n\t" \
  "s_lshl_b32 s78, s80, 14\n\t" \
  "s_add_i32 s78, s78, 0x70000\n\t" \
  "s_mov_b32 s57, 0\n\t" \
  "s_add_i32 s96, s77, 0x1000\n\t" \
  "s_and_b32 s98, s40, 2\n\t" \
  "s_lshl_b32 s98, s98, 13\n\t" \
  "s_and_b32 s80, s40, 1\n\t" \
  "s_lshl_b32 s80, s80, 12\n\t" \
  "s_add_i32 s98, s98, s80\n\t" \
  "s_add_i32 s98, s98, 0x84000\n\t" \
  "s_memtime vcc\n\t" \
  "s_memrealtime s[86:87]\n\t" \
  "s_nop 4\n\t" \
  "s_memrealtime s[60:61]\n\t" \
  "s_nop 4\n\t" \
  "s_memrealtime s[62:63]\n\t" \
  "s_nop 4\n\t" \
  "s_memrealtime s[64:65]\n\t" \
  "s_nop 4\n\t" \
  "s_memrealtime s[66:67]\n\t" \
  "s_nop 4\n\t" \
  "s_memrealtime s[68:69]\n\t" \
  "s_mov_b32 s79, 0\n\t" \
  "s_waitcnt lgkmcnt(0)\n\t" \
  "s_cmp_eq_u32 s86, s60\n\t" \
  "s_addc_u32 s79, s79, 0\n\t" \
  "s_cmp_eq_u32 s86, s62\n\t" \
  "s_addc_u32 s79, s79, 0\n\t" \
  "s_cmp_eq_u32 s86, s64\n\t" \
  "s_addc_u32 s79, s79, 0\n\t" \
  "s_cmp_eq_u32 s86, s66\n\t" \
  "s_addc_u32 s79, s79, 0\n\t" \
  "s_cmp_eq_u32 s86, s68\n\t" \
  "s_addc_u32 s79, s79, 0\n\t" \
  "s_mul_i32 s79, s79, 24\n\t" \
  "s_add_i32 s79, s79, vcc_lo\n\t" \
  "s_lshl4_add_u32 s80, s86, 64\n\t" \
  "s_sub_i32 s86, s79, s41\n\t" \
  "s_sub_i32 s87, s80, s43\n\t" \
  "s_cmp_lt_u32 s86, 4000\n\t" \
  "s_cbranch_scc1 21f\n\t" \
  "v_cvt_f32_u32 v165, s86\n\t" \
  "v_cvt_f32_u32 v164, s87\n\t" \
  "v_rcp_f32 v165, v165\n\t" \
  "s_nop 0\n\t" \
  "v_mul_f32 v165, v165, v164\n\t" \
  "v_max_f32 v165, 0x3f028f5c, v165\n\t" \
  "v_min_f32 v165, 0x3ffeb852, v165\n\t" \
  "v_rcp_f32 v164, v165\n\t" \
  "v_mul_f32 v165, 0x4f000000, v165\n\t" \
  "v_mul_f32 v164, 0x47800000, v164\n\t" \
  "v_cvt_u32_f32 v165, v165\n\t" \
  "v_cvt_u32_f32 v164, v164\n\t" \
  "s_nop 0\n\t" \
  "v_readfirstlane_b32 s45, v165\n\t" \
  "v_readfirstlane_b32 s44, v164\n\t" \
  "21:\n\t" \
  "s_mov_b32 s41, s79\n\t" \
  "s_mov_b32 s43, s80\n\t" \
  "s_lshl_b32 s83, s43, 8\n\t" \
  "s_mov_b32 s84, 650\n\t" \
  "s_mov_b32 exec_hi, 0\n\t" \
  "s_waitcnt vmcnt(0)\n\t" \
  "v_cmp_ne_u32_sdwa vcc, v184, s85 src0_sel:BYTE_0 src1_sel:DWORD\n\t" \
  "v_subrev_u32 v184, s83, v184\n\t" \
  "v_ashrrev_i32 v184, 8, v184\n\t" \
  "s_cbranch_vccnz 22f\n\t" \
  "s_nop 0\n\t" \
  "v_max_i32_dpp v184, v184, v184 quad_perm:[1,0,3,2] row_mask:0xf bank_mask:0xf\n\t" \
  "s_nop 1\n\t" \
  "v_max_i32_dpp v184, v184, v184 quad_perm:[2,3,0,1] row_mask:0xf bank_mask:0xf\n\t" \
  "s_nop 1\n\t" \
  "v_max_i32_dpp v184, v184, v184 row_half_mirror row_mask:0xf bank_mask:0xf\n\t" \
  "s_nop 1\n\t" \
  "v_max_i32_dpp v184, v184, v184 row_mirror row_mask:0xf bank_mask:0xf\n\t" \
  "s_nop 0\n\t" \
  "v_readlane_b32 s81, v184, 0\n\t" \
  "v_readlane_b32 s82, v184, 16\n\t" \
  "s_max_i32 s81, s81, s82\n\t" \
  "s_add_i32 s81, s81, 1056\n\t" \
  "s_max_i32 s81, s81, 0\n\t" \
  "s_min_i32 s81, s81, 20000\n\t" \
  "s_mul_i32 s81, s81, s44\n\t" \
  "s_lshr_b32 s81, s81, 16\n\t" \
  "24:\n\t" \
  "s_add_i32 s81, s81, s41\n\t" \
  "v_min_u32 v185, 1760, v185\n\t" \
  "s_nop 1\n\t" \
  "v_max_i32_dpp v185, v185, v185 quad_perm:[1,0,3,2] row_mask:0xf bank_mask:0xf\n\t" \
  "s_nop 1\n\t" \
  "v_max_i32_dpp v185, v185, v185 quad_perm:[2,3,0,1] row_mask:0xf bank_mask:0xf\n\t" \
  "s_nop 1\n\t" \
  "v_max_i32_dpp v185, v185, v185 row_half_mirror row_mask:0xf bank_mask:0xf\n\t" \
  "s_nop 1\n\t" \
  "v_max_i32_dpp v185, v185, v185 row_mirror row_mask:0xf bank_mask:0xf\n\t" \
  "s_nop 0\n\t" \
  "v_readlane_b32 s54, v185, 0\n\t" \
  "v_readlane_b32 s82, v185, 16\n\t" \
  "s_max_i32 s54, s54, s82\n\t" \
  "s_add_i32 s54, s54, 245\n\t" \
  "1:\n\t" \
  "s_memtime vcc\n\t" \
  "s_waitcnt lgkmcnt(0)\n\t" \
  "s_sub_i32 s79, s81, vcc_lo\n\t" \
  "s_cmp_gt_i32 s79, 128\n\t" \
  "s_cbranch_scc1 1b\n\t" \
  ".p2align 4\n\t" \
  "2:\n\t" \
  "s_sub_i32 s79, s79, 33\n\t" \
  "s_cmp_gt_i32 s79, 56\n\t" \
  "s_cbranch_scc1 2b\n\t" \
  "buffer_load_dword v187, v200, %[rh], s78 offen sc1\n\t" \
  "s_memtime vcc\n\t" \
  "s_memrealtime s[86:87]\n\t" \
  "s_nop 4\n\t" \
  "s_memrealtime s[60:61]\n\t" \
  "s_nop 4\n\t" \
  "s_memrealtime s[62:63]\n\t" \
  "s_nop 4\n\t" \
  "s_memrealtime s[64:65]\n\t" \
  "s_nop 4\n\t" \
  "s_memrealtime s[66:67]\n\t" \
  "s_nop 4\n\t" \
  "s_memrealtime s[68:69]\n\t" \
  "s_mov_b32 s79, 0\n\t" \
  "s_waitcnt lgkmcnt(0)\n\t" \
  "s_cmp_eq_u32 s86, s60\n\t" \
  "s_addc_u32 s79, s79, 0\n\t" \
  "s_cmp_eq_u32 s86, s62\n\t" \
  "s_addc_u32 s79, s79, 0\n\t" \
  "s_cmp_eq_u32 s86, s64\n\t" \
  "s_addc_u32 s79, s79, 0\n\t" \
  "s_cmp_eq_u32 s86, s66\n\t" \
  "s_addc_u32 s79, s79, 0\n\t" \
  "s_cmp_eq_u32 s86, s68\n\t" \
  "s_addc_u32 s79, s79, 0\n\t" \
  "s_mul_i32 s79, s79, 24\n\t" \
  "s_add_i32 s79, s79, vcc_lo\n\t" \
  "s_lshl4_add_u32 s80, s86, 64\n\t" \
  "s_mov_b32 s41, s79\n\t" \
  "s_mov_b32 s43, s80\n\t" \
  "s_lshl_b32 s83, s43, 8\n\t" \
  "s_add_i32 s54, s54, s43\n\t" \
  "13:\n\t" \
  "s_waitcnt vmcnt(0)\n\t" \
  "s_memtime s[86:87]\n\t" \
  "v_cmp_ne_u32_sdwa vcc, v187, s40 src0_sel:BYTE_0 src1_sel:DWORD\n\t" \
  "v_subrev_u32 v184, s83, v187\n\t" \
  "v_ashrrev_i32 v184, 8, v184\n\t" \
  "s_nop 1\n\t" \
  "v_max_i32_dpp v184, v184, v184 quad_perm:[1,0,3,2] row_mask:0xf bank_mask:0xf\n\t" \
  "s_nop 1\n\t" \
  "v_max_i32_dpp v184, v184, v184 quad_perm:[2,3,0,1] row_mask:0xf bank_mask:0xf\n\t" \
  "s_cbranch_vccnz 14f\n\t" \
  "15:\n\t" \
  "s_nop 0\n\t" \
  "v_max_i32_dpp v184, v184, v184 row_half_mirror row_mask:0xf bank_mask:0xf\n\t" \
  "s_nop 1\n\t" \
  "v_max_i32_dpp v184, v184, v184 row_mirror row_mask:0xf bank_mask:0xf\n\t" \
  "s_mov_b64 exec, -1\n\t" \
  "v_readlane_b32 s57, v184, 0\n\t" \
  "v_readlane_b32 s82, v184, 16\n\t" \
  "s_waitcnt lgkmcnt(0)\n\t" \
  "s_sub_i32 s79, s41, s86\n\t" \
  "s_max_i32 s57, s57, s82\n\t" \
  "s_add_i32 s81, s57, 200\n\t" \
  "s_max_i32 s81, s81, 0\n\t" \
  "s_min_i32 s81, s81, 20000\n\t" \
  "s_mul_i32 s81, s81, s44\n\t" \
  "s_lshr_b32 s81, s81, 16\n\t" \
  "s_add_i32 s79, s79, s81\n\t" \
  "s_sub_i32 s85, s84, s79\n\t" \
  "s_max_i32 s85, s85, 0\n\t" \
  "s_mul_hi_u32 s85, s85, s45\n\t" \
  "s_lshl1_add_u32 s85, s85, s57\n\t" \
  "s_add_i32 s85, s85, s54\n\t" \
  "s_lshl_b32 s85, s85, 8\n\t" \
  "s_or_b32 s85, s85, s40\n\t" \
  "v_mov_b32 v184, s85\n\t" \
  "buffer_store_dword v184, v202, %[rh], s98 offen sc1\n\t" \
  "s_add_i32 s79, s79, -640\n\t" \
  ".p2align 4\n\t" \
  "3:\n\t" \
  "s_sub_i32 s79, s79, 33\n\t" \
  "s_cmp_gt_i32 s79, 56\n\t" \
  "s_cbranch_scc1 3b\n\t" \
  "buffer_load_dwordx4 v[0:3], v193, %[rh], s77 offen offset:0 sc1\n\t" \
  "s_nop 4\n\t" \
  "buffer_load_dwordx4 v[4:7], v193, %[rh], s77 offen offset:1024 sc1\n\t" \
  "s_nop 4\n\t" \
  "buffer_load_dwordx4 v[8:11], v193, %[rh], s77 offen offset:2048 sc1\n\t" \
  "s_nop 4\n\t" \
  "buffer_load_dwordx4 v[12:15], v193, %[rh], s77 offen offset:3072 sc1\n\t" \
  "s_nop 4\n\t" \
  "buffer_load_dwordx4 v[16:19], v193, %[rh], s96 offen offset:0 sc1\n\t" \
  "s_nop 4\n\t" \
  "buffer_load_dwordx4 v[20:23], v193, %[rh], s96 offen offset:1024 sc1\n\t" \
  "s_nop 4\n\t" \
  "buffer_load_dwordx4 v[24:27], v193, %[rh], s96 offen offset:2048 sc1\n\t" \
  "s_nop 4\n\t" \
  "buffer_load_dwordx4 v[28:31], v193, %[rh], s96 offen offset:3072 sc1\n\t" \
  "s_add_i32 s79, s79, 272\n\t" \
  ".p2align 4\n\t" \
  "4:\n\t" \
  "s_sub_i32 s79, s79, 33\n\t" \
  "s_cmp_gt_i32 s79, 56\n\t" \
  "s_cbranch_scc1 4b\n\t" \
  "buffer_load_dwordx4 v[32:35], v195, %[rh], s77 offen offset:0 sc1\n\t" \
  "buffer_load_dwordx4 v[36:39], v195, %[rh], s77 offen offset:2048 sc1\n\t" \
  "10:\n\t" \
  "s_waitcnt vmcnt(9)\n\t" \
  "v_smfmac_f32_16x16x64_bf16 v[40:43], v[0:3], a[0:7], v203\n\t" \
  "v_smfmac_f32_16x16x64_bf16 v[56:59], v[0:3], a[8:15], v203\n\t" \
  "s_waitcnt vmcnt(8)\n\t" \
  "v_smfmac_f32_16x16x64_bf16 v[44:47], v[4:7], a[16:23], v203\n\t" \
  "v_cvt_pk_bf16_f32 v40, v40, v41\n\t" \
  "v_cvt_pk_bf16_f32 v41, v42, v43\n\t" \
  "v_smfmac_f32_16x16x64_bf16 v[60:63], v[4:7], a[24:31], v203\n\t" \
  "v_cvt_pk_bf16_f32 v56, v56, v57\n\t" \
  "v_cvt_pk_bf16_f32 v57, v58, v59\n\t" \
  "s_waitcnt vmcnt(7)\n\t" \
  "v_smfmac_f32_16x16x64_bf16 v[48:51], v[8:11], a[32:39], v203\n\t" \
  "v_cvt_pk_bf16_f32 v42, v44, v45\n\t" \
  "v_cvt_pk_bf16_f32 v43, v46, v47\n\t" \
  "v_smfmac_f32_16x16x64_bf16 v[64:67], v[8:11], a[40:47], v203\n\t" \
  "v_cvt_pk_bf16_f32 v58, v60, v61\n\t" \
  "v_cvt_pk_bf16_f32 v59, v62, v63\n\t" \
  "s_waitcnt vmcnt(6)\n\t" \
  "v_smfmac_f32_16x16x64_bf16 v[52:55], v[12:15], a[48:55], v203\n\t" \
  "v_cvt_pk_bf16_f32 v44, v48, v49\n\t" \
  "v_cvt_pk_bf16_f32 v45, v50, v51\n\t" \
  "v_smfmac_f32_16x16x64_bf16 v[68:71], v[12:15], a[56:63], v203\n\t" \
  "v_cvt_pk_bf16_f32 v60, v64, v65\n\t" \
  "v_cvt_pk_bf16_f32 v61, v66, v67\n\t" \
  "s_waitcnt vmcnt(5)\n\t" \
  "v_smfmac_f32_16x16x64_bf16 v[72:75], v[16:19], a[64:71], v203\n\t" \
  "v_cvt_pk_bf16_f32 v46, v52, v53\n\t" \
  "v_cvt_pk_bf16_f32 v47, v54, v55\n\t" \
  "v_smfmac_f32_16x16x64_bf16 v[88:91], v[16:19], a[72:79], v203\n\t" \
  "v_cvt_pk_bf16_f32 v62, v68, v69\n\t" \
  "v_cvt_pk_bf16_f32 v63, v70, v71\n\t" \
  "s_waitcnt vmcnt(4)\n\t" \
  "v_smfmac_f32_16x16x64_bf16 v[76:79], v[20:23], a[80:87], v203\n\t" \
  "v_cvt_pk_bf16_f32 v72, v72, v73\n\t" \
  "v_cvt_pk_bf16_f32 v73, v74, v75\n\t" \
  "v_smfmac_f32_16x16x64_bf16 v[92:95], v[20:23], a[88:95], v203\n\t" \
  "v_cvt_pk_bf16_f32 v88, v88, v89\n\t" \
  "v_cvt_pk_bf16_f32 v89, v90, v91\n\t" \
  "s_waitcnt vmcnt(3)\n\t" \
  "v_smfmac_f32_16x16x64_bf16 v[80:83], v[24:27], a[96:103], v203\n\t" \
  "v_cvt_pk_bf16_f32 v74, v76, v77\n\t" \
  "v_cvt_pk_bf16_f32 v75, v78, v79\n\t" \
  "v_smfmac_f32_16x16x64_bf16 v[96:99], v[24:27], a[104:111], v203\n\t" \
  "v_cvt_pk_bf16_f32 v90, v92, v93\n\t" \
  "v_cvt_pk_bf16_f32 v91, v94, v95\n\t" \
  "s_waitcnt vmcnt(2)\n\t" \
  "v_smfmac_f32_16x16x64_bf16 v[84:87], v[28:31], a[112:119], v203\n\t" \
  "v_cvt_pk_bf16_f32 v76, v80, v81\n\t" \
  "v_cvt_pk_bf16_f32 v77, v82, v83\n\t" \
  "v_smfmac_f32_16x16x64_bf16 v[100:103], v[28:31], a[120:127], v203\n\t" \
  "v_cvt_pk_bf16_f32 v92, v96, v97\n\t" \
  "v_cvt_pk_bf16_f32 v93, v98, v99\n\t" \
  "s_waitcnt vmcnt(1)\n\t" \
  "v_smfmac_f32_16x16x64_bf16 v[136:139], v[32:35], v[40:47], v204\n\t" \
  "v_cvt_pk_bf16_f32 v78, v84, v85\n\t" \
  "v_cvt_pk_bf16_f32 v79, v86, v87\n\t" \
  "v_smfmac_f32_16x16x64_bf16 v[140:143], v[32:35], v[56:63], v204\n\t" \
  "v_cvt_pk_bf16_f32 v94, v100, v101\n\t" \
  "v_cvt_pk_bf16_f32 v95, v102, v103\n\t" \
  "s_waitcnt vmcnt(0)\n\t" \
  "v_smfmac_f32_16x16x64_bf16 v[136:139], v[36:39], v[72:79], v204\n\t" \
  "v_smfmac_f32_16x16x64_bf16 v[140:143], v[36:39], v[88:95], v204\n\t" \
  "s_nop 3\n\t" \
  "v_permlane32_swap_b32 v136, v138\n\t" \
  "v_permlane32_swap_b32 v137, v139\n\t" \
  "v_add_f32 v136, v136, v138\n\t" \
  "v_add_f32 v137, v137, v139\n\t" \
  "v_permlane32_swap_b32 v140, v142\n\t" \
  "v_permlane32_swap_b32 v141, v143\n\t" \
  "v_add_f32 v138, v140, v142\n\t" \
  "v_add_f32 v139, v141, v143\n\t" \
  "ds_write2st64_b64 v197, v[136:137], v[138:139] offset1:2\n\t" \
  "s_waitcnt lgkmcnt(0)\n\t" \
  "s_barrier\n\t" \
  "s_barrier\n\t" \
  "ds_read2_b32 v[148:149], v207 offset1:8\n\t" \
  "s_waitcnt lgkmcnt(0)\n\t" \
  "v_or_b32 v148, v148, v149\n\t" \
  "v_cmp_ne_u32 vcc, 0, v148\n\t" \
  "s_cbranch_vccnz 12f\n\t" \
  "s_and_b32 s58, s58, 4\n\t" \
  "s_cmp_ge_i32 s40, 62\n\t" \
  "s_cbranch_scc1 26f\n\t" \
  "s_and_b32 s82, s40, 3\n\t" \
  "s_lshl_b32 s82, s82, 20\n\t" \
  "s_add_i32 s82, s82, s48\n\t" \
  "s_andn2_b32 s82, s82, 0x1800\n\t" \
  "s_lshr_b32 s80, s82, 21\n\t" \
  "s_bitcmp1_b64 %[sel], s80\n\t" \
  "s_cselect_b32 s80, 0x2000, 0\n\t" \
  "s_add_i32 s82, s82, s80\n\t" \
  "s_add_i32 s80, s82, 0x1000\n\t" \
  "v_mov_b64 v[188:189], -1\n\t" \
  "v_mov_b64 v[190:191], -1\n\t" \
  "s_sleep 8\n\t" \
  "buffer_store_dwordx4 v[188:191], v192, %[ru], s82 offen offset:0 sc1\n\t" \
  "buffer_store_dwordx4 v[188:191], v192, %[ru], s82 offen offset:1024 sc1\n\t" \
  "buffer_store_dwordx4 v[188:191], v192, %[ru], s82 offen offset:2048 sc1\n\t" \
  "buffer_store_dwordx4 v[188:191], v192, %[ru], s82 offen offset:3072 sc1\n\t" \
  "buffer_store_dwordx4 v[188:191], v192, %[ru], s80 offen offset:0 sc1\n\t" \
  "buffer_store_dwordx4 v[188:191], v192, %[ru], s80 offen offset:1024 sc1\n\t" \
  "buffer_store_dwordx4 v[188:191], v192, %[ru], s80 offen offset:2048 sc1\n\t" \
  "buffer_store_dwordx4 v[188:191], v192, %[ru], s80 offen offset:3072 sc1\n\t" \
  "26:\n\t" \
  "s_barrier\n\t" \
  "s_and_b32 s76, s40, 3\n\t" \
  "s_lshl_b32 s76, s76, 18\n\t" \
  "s_add_i32 s77, s76, 0x10000\n\t" \
  "s_add_i32 s78, s76, 0x1000\n\t" \
  "s_add_i32 s79, s76, 0x11000\n\t" \
  "31:\n\t" \
  "buffer_load_dwordx4 v[0:3], v210, %[rh], s76 offen offset:0 sc1\n\t" \
  "s_nop 10\n\t" \
  "buffer_load_dwordx4 v[4:7], v210, %[rh], s76 offen offset:1024 sc1\n\t" \
  "s_nop 10\n\t" \
  "buffer_load_dwordx4 v[8:11], v210, %[rh], s77 offen offset:0 sc1\n\t" \
  "s_nop 10\n\t" \
  "buffer_load_dwordx4 v[12:15], v210, %[rh], s77 offen offset:1024 sc1\n\t" \
  "s_nop 10\n\t" \
  "buffer_load_dwordx4 v[16:19], v210, %[rh], s76 offen offset:2048 sc1\n\t" \
  "s_nop 10\n\t" \
  "buffer_load_dwordx4 v[20:23], v210, %[rh], s76 offen offset:3072 sc1\n\t" \
  "s_nop 10\n\t" \
  "buffer_load_dwordx4 v[24:27], v210, %[rh], s77 offen offset:2048 sc1\n\t" \
  "s_nop 10\n\t" \
  "buffer_load_dwordx4 v[28:31], v210, %[rh], s77 offen offset:3072 sc1\n\t" \
  "s_nop 10\n\t" \
  "buffer_load_dwordx4 v[32:35], v210, %[rh], s78 offen offset:0 sc1\n\t" \
  "s_nop 10\n\t" \
  "buffer_load_dwordx4 v[36:39], v210, %[rh], s78 offen offset:1024 sc1\n\t" \
  "s_nop 10\n\t" \
  "buffer_load_dwordx4 v[40:43], v210, %[rh], s79 offen offset:0 sc1\n\t" \
  "s_nop 10\n\t" \
  "buffer_load_dwordx4 v[44:47], v210, %[rh], s79 offen offset:1024 sc1\n\t" \
  "s_nop 10\n\t" \
  "buffer_load_dwordx4 v[48:51], v210, %[rh], s78 offen offset:2048 sc1\n\t" \
  "s_nop 10\n\t" \
  "buffer_load_dwordx4 v[52:55], v210, %[rh], s78 offen offset:3072 sc1\n\t" \
  "s_nop 10\n\t" \
  "buffer_load_dwordx4 v[56:59], v210, %[rh], s79 offen offset:2048 sc1\n\t" \
  "s_nop 10\n\t" \
  "buffer_load_dwordx4 v[60:63], v210, %[rh], s79 offen offset:3072 sc1\n\t" \
  "s_nop 10\n\t" \
  "buffer_load_dwordx4 v[64:67], v211, %[rh], s76 offen offset:0 sc1\n\t" \
  "s_nop 10\n\t" \
  "buffer_load_dwordx4 v[68:71], v211, %[rh], s76 offen offset:1024 sc1\n\t" \
  "s_nop 10\n\t" \
  "buffer_load_dwordx4 v[72:75], v211, %[rh], s77 offen offset:0 sc1\n\t" \
  "s_nop 10\n\t" \
  "buffer_load_dwordx4 v[76:79], v211, %[rh], s77 offen offset:1024 sc1\n\t" \
  "s_nop 10\n\t" \
  "buffer_load_dwordx4 v[80:83], v211, %[rh], s76 offen offset:2048 sc1\n\t" \
  "s_nop 10\n\t" \
  "buffer_load_dwordx4 v[84:87], v211, %[rh], s76 offen offset:3072 sc1\n\t" \
  "s_nop 10\n\t" \
  "v_mov_b64 v[136:137], 0\n\t" \
  "v_mov_b64 v[138:139], 0\n\t" \
  "v_mov_b64 v[140:141], 0\n\t" \
  "v_mov_b64 v[142:143], 0\n\t" \
  "s_waitcnt vmcnt(20)\n\t" \
  "v_swap_b32 v1, v4\n\t" \
  "v_swap_b32 v3, v6\n\t" \
  "s_nop 1\n\t" \
  "v_mfma_f32_16x16x32_bf16 v[168:171], a[0:3], v[0:3], 0\n\t" \
  "buffer_load_dwordx4 v[88:91], v211, %[rh], s77 offen offset:2048 sc1\n\t" \
  "v_mfma_f32_16x16x32_bf16 v[172:175], a[8:11], v[0:3], 0\n\t" \
  "s_waitcnt vmcnt(19)\n\t" \
  "v_swap_b32 v9, v12\n\t" \
  "v_mfma_f32_16x16x32_bf16 v[168:171], a[4:7], v[4:7], v[168:171]\n\t" \
  "v_swap_b32 v11, v14\n\t" \
  "v_mfma_f32_16x16x32_bf16 v[172:175], a[12:15], v[4:7], v[172:175]\n\t" \
  "buffer_load_dwordx4 v[92:95], v211, %[rh], s77 offen offset:3072 sc1\n\t" \
  "v_mfma_f32_16x16x32_bf16 v[176:179], a[0:3], v[8:11], 0\n\t" \
  "buffer_load_dwordx4 v[96:99], v211, %[rh], s78 offen offset:0 sc1\n\t" \
  "v_mfma_f32_16x16x32_bf16 v[180:183], a[8:11], v[8:11], 0\n\t" \
  "s_waitcnt vmcnt(19)\n\t" \
  "v_swap_b32 v17, v20\n\t" \
  "v_mfma_f32_16x16x32_bf16 v[176:179], a[4:7], v[12:15], v[176:179]\n\t" \
  "v_swap_b32 v19, v22\n\t" \
  "v_mfma_f32_16x16x32_bf16 v[180:183], a[12:15], v[12:15], v[180:183]\n\t" \
  "buffer_load_dwordx4 v[100:103], v211, %[rh], s78 offen offset:1024 sc1\n\t" \
  "v_mfma_f32_16x16x32_bf16 v[168:171], a[16:19], v[16:19], v[168:171]\n\t" \
  "buffer_load_dwordx4 v[104:107], v211, %[rh], s79 offen offset:0 sc1\n\t" \
  "v_mfma_f32_16x16x32_bf16 v[172:175], a[24:27], v[16:19], v[172:175]\n\t" \
  "s_waitcnt vmcnt(19)\n\t" \
  "v_swap_b32 v25, v28\n\t" \
  "v_mfma_f32_16x16x32_bf16 v[168:171], a[20:23], v[20:23], v[168:171]\n\t" \
  "v_swap_b32 v27, v30\n\t" \
  "v_mfma_f32_16x16x32_bf16 v[172:175], a[28:31], v[20:23], v[172:175]\n\t" \
  "buffer_load_dwordx4 v[108:111], v211, %[rh], s79 offen offset:1024 sc1\n\t" \
  "v_mfma_f32_16x16x32_bf16 v[176:179], a[16:19], v[24:27], v[176:179]\n\t" \
  "buffer_load_dwordx4 v[112:115], v211, %[rh], s78 offen offset:2048 sc1\n\t" \
  "v_mfma_f32_16x16x32_bf16 v[180:183], a[24:27], v[24:27], v[180:183]\n\t" \
  "s_waitcnt vmcnt(19)\n\t" \
  "v_swap_b32 v33, v36\n\t" \
  "v_mfma_f32_16x16x32_bf16 v[176:179], a[20:23], v[28:31], v[176:179]\n\t" \
  "v_swap_b32 v35, v38\n\t" \
  "v_mfma_f32_16x16x32_bf16 v[180:183], a[28:31], v[28:31], v[180:183]\n\t" \
  "buffer_load_dwordx4 v[116:119], v211, %[rh], s78 offen offset:3072 sc1\n\t" \
  "v_mfma_f32_16x16x32_bf16 v[168:171], a[32:35], v[32:35], v[168:171]\n\t" \
  "buffer_load_dwordx4 v[120:123], v211, %[rh], s79 offen offset:2048 sc1\n\t" \
  "v_mfma_f32_16x16x32_bf16 v[172:175], a[40:43], v[32:35], v[172:175]\n\t" \
  "s_waitcnt vmcnt(19)\n\t" \
  "v_swap_b32 v41, v44\n\t" \
  "v_mfma_f32_16x16x32_bf16 v[168:171], a[36:39], v[36:39], v[168:171]\n\t" \
  "v_swap_b32 v43, v46\n\t" \
  "v_mfma_f32_16x16x32_bf16 v[172:175], a[44:47], v[36:39], v[172:175]\n\t" \
  "buffer_load_dwordx4 v[124:127], v211, %[rh], s79 offen offset:3072 sc1\n\t" \
  "v_mfma_f32_16x16x32_bf16 v[176:179], a[32:35], v[40:43], v[176:179]\n\t" \
  "v_mfma_f32_16x16x32_bf16 v[180:183], a[40:43], v[40:43], v[180:183]\n\t" \
  "s_waitcnt vmcnt(18)\n\t" \
  "v_swap_b32 v49, v52\n\t" \
  "v_mfma_f32_16x16x32_bf16 v[176:179], a[36:39], v[44:47], v[176:179]\n\t" \
  "v_swap_b32 v51, v54\n\t" \
  "v_mfma_f32_16x16x32_bf16 v[180:183], a[44:47], v[44:47], v[180:183]\n\t" \
  "v_mov_b64 v[40:41], 0\n\t" \
  "v_mov_b64 v[42:43], 0\n\t" \
  "v_mfma_f32_16x16x32_bf16 v[168:171], a[48:51], v[48:51], v[168:171]\n\t" \
  "v_mov_b64 v[44:45], 0\n\t" \
  "v_mov_b64 v[46:47], 0\n\t" \
  "v_mfma_f32_16x16x32_bf16 v[172:175], a[56:59], v[48:51], v[172:175]\n\t" \
  "s_waitcnt vmcnt(16)\n\t" \
  "v_swap_b32 v57, v60\n\t" \
  "v_mfma_f32_16x16x32_bf16 v[168:171], a[52:55], v[52:55], v[168:171]\n\t" \
  "v_swap_b32 v59, v62\n\t" \
  "v_mfma_f32_16x16x32_bf16 v[172:175], a[60:63], v[52:55], v[172:175]\n\t" \
  "v_mov_b64 v[48:49], 0\n\t" \
  "v_mov_b64 v[50:51], 0\n\t" \
  "v_mfma_f32_16x16x32_bf16 v[176:179], a[48:51], v[56:59], v[176:179]\n\t" \
  "v_mov_b64 v[52:53], 0\n\t" \
  "v_mov_b64 v[54:55], 0\n\t" \
  "v_mfma_f32_16x16x32_bf16 v[180:183], a[56:59], v[56:59], v[180:183]\n\t" \
  "s_waitcnt vmcnt(14)\n\t" \
  "v_swap_b32 v65, v68\n\t" \
  "v_mfma_f32_16x16x32_bf16 v[176:179], a[52:55], v[60:63], v[176:179]\n\t" \
  "v_swap_b32 v67, v70\n\t" \
  "v_mfma_f32_16x16x32_bf16 v[180:183], a[60:63], v[60:63], v[180:183]\n\t" \
  "v_mov_b64 v[56:57], 0\n\t" \
  "v_mov_b64 v[58:59], 0\n\t" \
  "v_mfma_f32_16x16x32_bf16 v[168:171], a[64:67], v[64:67], v[168:171]\n\t" \
  "v_mov_b64 v[60:61], 0\n\t" \
  "v_mov_b64 v[62:63], 0\n\t" \
  "v_mfma_f32_16x16x32_bf16 v[172:175], a[72:75], v[64:67], v[172:175]\n\t" \
  "s_waitcnt vmcnt(12)\n\t" \
  "v_swap_b32 v73, v76\n\t" \
  "v_mfma_f32_16x16x32_bf16 v[168:171], a[68:71], v[68:71], v[168:171]\n\t" \
  "v_swap_b32 v75, v78\n\t" \
  "v_mfma_f32_16x16x32_bf16 v[172:175], a[76:79], v[68:71], v[172:175]\n\t" \
  "v_mov_b64 v[64:65], 0\n\t" \
  "v_mov_b64 v[66:67], 0\n\t" \
  "v_mfma_f32_16x16x32_bf16 v[176:179], a[64:67], v[72:75], v[176:179]\n\t" \
  "v_mov_b64 v[68:69], 0\n\t" \
  "v_mov_b64 v[70:71], 0\n\t" \
  "v_mfma_f32_16x16x32_bf16 v[180:183], a[72:75], v[72:75], v[180:183]\n\t" \
  "s_waitcnt vmcnt(10)\n\t" \
  "v_swap_b32 v81, v84\n\t" \
  "v_mfma_f32_16x16x32_bf16 v[176:179], a[68:71], v[76:79], v[176:179]\n\t" \
  "v_swap_b32 v83, v86\n\t" \
  "v_mfma_f32_16x16x32_bf16 v[180:183], a[76:79], v[76:79], v[180:183]\n\t" \
  "v_mov_b64 v[72:73], 0\n\t" \
  "v_mov_b64 v[74:75], 0\n\t" \
  "v_mfma_f32_16x16x32_bf16 v[168:171], a[80:83], v[80:83], v[168:171]\n\t" \
  "v_mov_b64 v[76:77], 0\n\t" \
  "v_mov_b64 v[78:79], 0\n\t" \
  "v_mfma_f32_16x16x32_bf16 v[172:175], a[88:91], v[80:83], v[172:175]\n\t" \
  "s_waitcnt vmcnt(8)\n\t" \
  "v_swap_b32 v89, v92\n\t" \
  "v_mfma_f32_16x16x32_bf16 v[168:171], a[84:87], v[84:87], v[168:171]\n\t" \
  "v_swap_b32 v91, v94\n\t" \
  "v_mfma_f32_16x16x32_bf16 v[172:175], a[92:95], v[84:87], v[172:175]\n\t" \
  "v_mov_b64 v[80:81], 0\n\t" \
  "v_mov_b64 v[82:83], 0\n\t" \
  "v_mfma_f32_16x16x32_bf16 v[176:179], a[80:83], v[88:91], v[176:179]\n\t" \
  "v_mov_b64 v[84:85], 0\n\t" \
  "v_mov_b64 v[86:87], 0\n\t" \
  "v_mfma_f32_16x16x32_bf16 v[180:183], a[88:91], v[88:91], v[180:183]\n\t" \
  "s_waitcnt vmcnt(6)\n\t" \
  "v_swap_b32 v97, v100\n\t" \
  "v_mfma_f32_16x16x32_bf16 v[176:179], a[84:87], v[92:95], v[176:179]\n\t" \
  "v_swap_b32 v99, v102\n\t" \
  "v_mfma_f32_16x16x32_bf16 v[180:183], a[92:95], v[92:95], v[180:183]\n\t" \
  "v_mov_b64 v[88:89], 0\n\t" \
  "v_mov_b64 v[90:91], 0\n\t" \
  "v_mfma_f32_16x16x32_bf16 v[168:171], a[96:99], v[96:99], v[168:171]\n\t" \
  "v_mov_b64 v[92:93], 0\n\t" \
  "v_mov_b64 v[94:95], 0\n\t" \
  "v_mfma_f32_16x16x32_bf16 v[172:175], a[104:107], v[96:99], v[172:175]\n\t" \
  "s_waitcnt vmcnt(4)\n\t" \
  "v_swap_b32 v105, v108\n\t" \
  "v_mfma_f32_16x16x32_bf16 v[168:171], a[100:103], v[100:103], v[168:171]\n\t" \
  "v_swap_b32 v107, v110\n\t" \
  "v_mfma_f32_16x16x32_bf16 v[172:175], a[108:111], v[100:103], v[172:175]\n\t" \
  "v_mov_b64 v[96:97], 0\n\t" \
  "v_mov_b64 v[98:99], 0\n\t" \
  "v_mfma_f32_16x16x32_bf16 v[176:179], a[96:99], v[104:107], v[176:179]\n\t" \
  "v_mov_b64 v[100:101], 0\n\t" \
  "v_mov_b64 v[102:103], 0\n\t" \
  "v_mfma_f32_16x16x32_bf16 v[180:183], a[104:107], v[104:107], v[180:183]\n\t" \
  "s_waitcnt vmcnt(2)\n\t" \
  "v_swap_b32 v113, v116\n\t" \
  "v_mfma_f32_16x16x32_bf16 v[176:179], a[100:103], v[108:111], v[176:179]\n\t" \
  "v_swap_b32 v115, v118\n\t" \
  "v_mfma_f32_16x16x32_bf16 v[180:183], a[108:111], v[108:111], v[180:183]\n\t" \
  "s_nop 0\n\t" \
  "v_mfma_f32_16x16x32_bf16 v[168:171], a[112:115], v[112:115], v[168:171]\n\t" \
  "v_mfma_f32_16x16x32_bf16 v[172:175], a[120:123], v[112:115], v[172:175]\n\t" \
  "s_waitcnt vmcnt(0)\n\t" \
  "v_swap_b32 v121, v124\n\t" \
  "v_mfma_f32_16x16x32_bf16 v[168:171], a[116:119], v[116:119], v[168:171]\n\t" \
  "v_swap_b32 v123, v126\n\t" \
  "v_mfma_f32_16x16x32_bf16 v[172:175], a[124:127], v[116:119], v[172:175]\n\t" \
  "s_nop 0\n\t" \
  "v_mfma_f32_16x16x32_bf16 v[176:179], a[112:115], v[120:123], v[176:179]\n\t" \
  "v_mfma_f32_16x16x32_bf16 v[180:183], a[120:123], v[120:123], v[180:183]\n\t" \
  "v_mfma_f32_16x16x32_bf16 v[176:179], a[116:119], v[124:127], v[176:179]\n\t" \
  "v_mfma_f32_16x16x32_bf16 v[180:183], a[124:127], v[124:127], v[180:183]\n\t" \
  "s_nop 6\n\t" \
  "39:\n\t" \
  "s_add_i32 s40, s40, 2\n\t" \
  "v_cmp_u_f32 vcc, v168, v176\n\t" \
  "s_cbranch_vccnz 32f\n\t" \
  "33:\n\t" \
  "s_barrier\n\t" \
  "s_add_i32 s85, s40, -1\n\t" \
  "s_and_b32 s82, s85, 2\n\t" \
  "s_lshl_b32 s82, s82, 13\n\t" \
  "s_and_b32 s80, s85, 1\n\t" \
  "s_lshl_b32 s80, s80, 12\n\t" \
  "s_add_i32 s82, s82, s80\n\t" \
  "s_add_i32 s82, s82, 0x84000\n\t" \
  "s_mov_b32 exec_hi, 0\n\t" \
  "buffer_load_dword v184, v200, %[rh], s82 offen sc1\n\t" \
  "buffer_load_dword v185, v200, %[rh], s52 offen sc1\n\t" \
  "s_mov_b64 exec, -1\n\t" \
  "ds_read_b128 a[0:3], v194 offset:0\n\t" \
  "ds_read_b128 a[4:7], v194 offset:1024\n\t" \
  "ds_read_b128 a[8:11], v194 offset:2048\n\t" \
  "ds_read_b128 a[12:15], v194 offset:3072\n\t" \
  "ds_read_b128 a[16:19], v194 offset:4096\n\t" \
  "ds_read_b128 a[20:23], v194 offset:5120\n\t" \
  "ds_read_b128 a[24:27], v194 offset:6144\n\t" \
  "ds_read_b128 a[28:31], v194 offset:7168\n\t" \
  "ds_read_b128 a[32:35], v194 offset:8192\n\t" \
  "ds_read_b128 a[36:39], v194 offset:9216\n\t" \
  "ds_read_b128 a[40:43], v194 offset:10240\n\t" \
  "ds_read_b128 a[44:47], v194 offset:11264\n\t" \
  "ds_read_b128 a[48:51], v194 offset:12288\n\t" \
  "ds_read_b128 a[52:55], v194 offset:13312\n\t" \
  "ds_read_b128 a[56:59], v194 offset:14336\n\t" \
  "ds_read_b128 a[60:63], v194 offset:15360\n\t" \
  "ds_read_b128 a[64:67], v194 offset:16384\n\t" \
  "ds_read_b128 a[68:71], v194 offset:17408\n\t" \
  "ds_read_b128 a[72:75], v194 offset:18432\n\t" \
  "ds_read_b128 a[76:79], v194 offset:19456\n\t" \
  "ds_read_b128 a[80:83], v194 offset:20480\n\t" \
  "ds_read_b128 a[84:87], v194 offset:21504\n\t" \
  "ds_read_b128 a[88:91], v194 offset:22528\n\t" \
  "ds_read_b128 a[92:95], v194 offset:23552\n\t" \
  "ds_read_b128 a[96:99], v194 offset:24576\n\t" \
  "ds_read_b128 a[100:103], v194 offset:25600\n\t" \
  "ds_read_b128 a[104:107], v194 offset:26624\n\t" \
  "ds_read_b128 a[108:111], v194 offset:27648\n\t" \
  "ds_read_b128 a[112:115], v194 offset:28672\n\t" \
  "ds_read_b128 a[116:119], v194 offset:29696\n\t" \
  "ds_read_b128 a[120:123], v194 offset:30720\n\t" \
  "ds_read_b128 a[124:127], v194 offset:31744\n\t" \
  "ds_write_b128 v199, v[168:171] offset:0\n\t" \
  "ds_write_b128 v199, v[172:175] offset:1024\n\t" \
  "ds_write_b128 v199, v[176:179] offset:8192\n\t" \
  "ds_write_b128 v199, v[180:183] offset:9216\n\t" \
  "s_mov_b32 s59, 0\n\t" \
  "s_and_b32 s58, s58, 4\n\t" \
  "s_cmp_lt_i32 s40, 64\n\t" \
  "s_cbranch_scc1 20b\n\t" \
  "s_waitcnt lgkmcnt(0)\n\t" \
  "s_barrier\n\t" \
  "s_mov_b32 s58, 64\n\t" \
  "s_branch 7f\n\t" \
  "22:\n\t" \
  "s_mov_b32 s81, 0\n\t" \
  "s_cmp_eq_u32 s40, 0\n\t" \
  "s_cbranch_scc1 24b\n\t" \
  "s_bitcmp1_b32 s58, 2\n\t" \
  "s_cbranch_scc1 24b\n\t" \
  "s_mov_b64 exec, -1\n\t" \
  "s_sleep 10\n\t" \
  "s_add_u32 s59, s59, 0x10000\n\t" \
  "s_cmp_lt_u32 s59, 0x100000\n\t" \
  "s_cbranch_scc1 19b\n\t" \
  "s_or_b32 s58, s58, 1\n\t" \
  "s_branch 7f\n\t" \
  "14:\n\t" \
  "v_mov_b32 v184, 0\n\t" \
  "s_bitcmp1_b32 s58, 2\n\t" \
  "s_nop 0\n\t" \
  "s_cbranch_scc1 15b\n\t" \
  "s_mov_b32 s84, 950\n\t" \
  "s_add_u32 s59, s59, 0x10000\n\t" \
  "s_cmp_lt_u32 s59, 0x100000\n\t" \
  "s_mov_b64 exec, vcc\n\t" \
  "buffer_load_dword v187, v200, %[rh], s78 offen sc1\n\t" \
  "s_mov_b32 exec_lo, -1\n\t" \
  "s_cbranch_scc1 13b\n\t" \
  "s_mov_b64 exec, -1\n\t" \
  "s_or_b32 s58, s58, 1\n\t" \
  "s_branch 7f\n\t" \
  "37:\n\t" \
  "s_barrier\n\t" \
  "v_mov_b64 v[40:41], 0\n\t" \
  "v_mov_b64 v[42:43], 0\n\t" \
  "v_mov_b64 v[44:45], 0\n\t" \
  "v_mov_b64 v[46:47], 0\n\t" \
  "v_mov_b64 v[48:49], 0\n\t" \
  "v_mov_b64 v[50:51], 0\n\t" \
  "v_mov_b64 v[52:53], 0\n\t" \
  "v_mov_b64 v[54:55], 0\n\t" \
  "v_mov_b64 v[56:57], 0\n\t" \
  "v_mov_b64 v[58:59], 0\n\t" \
  "v_mov_b64 v[60:61], 0\n\t" \
  "v_mov_b64 v[62:63], 0\n\t" \
  "v_mov_b64 v[64:65], 0\n\t" \
  "v_mov_b64 v[66:67], 0\n\t" \
  "v_mov_b64 v[68:69], 0\n\t" \
  "v_mov_b64 v[70:71], 0\n\t" \
  "v_mov_b64 v[72:73], 0\n\t" \
  "v_mov_b64 v[74:75], 0\n\t" \
  "v_mov_b64 v[76:77], 0\n\t" \
  "v_mov_b64 v[78:79], 0\n\t" \
  "v_mov_b64 v[80:81], 0\n\t" \
  "v_mov_b64 v[82:83], 0\n\t" \
  "v_mov_b64 v[84:85], 0\n\t" \
  "v_mov_b64 v[86:87], 0\n\t" \
  "v_mov_b64 v[88:89], 0\n\t" \
  "v_mov_b64 v[90:91], 0\n\t" \
  "v_mov_b64 v[92:93], 0\n\t" \
  "v_mov_b64 v[94:95], 0\n\t" \
  "v_mov_b64 v[96:97], 0\n\t" \
  "v_mov_b64 v[98:99], 0\n\t" \
  "v_mov_b64 v[100:101], 0\n\t" \
  "v_mov_b64 v[102:103], 0\n\t" \
  "v_mov_b64 v[136:137], 0\n\t" \
  "v_mov_b64 v[138:139], 0\n\t" \
  "v_mov_b64 v[140:141], 0\n\t" \
  "v_mov_b64 v[142:143], 0\n\t" \
  "s_add_i32 s40, s40, 2\n\t" \
  "s_branch 33b\n\t" \
  "32:\n\t" \
  "s_and_b32 s80, s58, 36\n\t" \
  "s_cbranch_scc1 33b\n\t" \
  "s_add_i32 s40, s40, -2\n\t" \
  "34:\n\t" \
  "s_or_b32 s59, s59, 128\n\t" \
  "s_and_b32 s76, s40, 3\n\t" \
  "s_lshl_b32 s76, s76, 18\n\t" \
  "s_add_i32 s77, s76, 0x10000\n\t" \
  "s_add_i32 s78, s76, 0x1000\n\t" \
  "s_add_i32 s79, s76, 0x11000\n\t" \
  "35:\n\t" \
  "buffer_load_dwordx4 v[0:3], v210, %[rh], s76 offen offset:0 sc1\n\t" \
  "buffer_load_dwordx4 v[4:7], v210, %[rh], s76 offen offset:1024 sc1\n\t" \
  "buffer_load_dwordx4 v[8:11], v210, %[rh], s77 offen offset:0 sc1\n\t" \
  "buffer_load_dwordx4 v[12:15], v210, %[rh], s77 offen offset:1024 sc1\n\t" \
  "buffer_load_dwordx4 v[16:19], v210, %[rh], s76 offen offset:2048 sc1\n\t" \
  "buffer_load_dwordx4 v[20:23], v210, %[rh], s76 offen offset:3072 sc1\n\t" \
  "buffer_load_dwordx4 v[24:27], v210, %[rh], s77 offen offset:2048 sc1\n\t" \
  "buffer_load_dwordx4 v[28:31], v210, %[rh], s77 offen offset:3072 sc1\n\t" \
  "buffer_load_dwordx4 v[32:35], v210, %[rh], s78 offen offset:0 sc1\n\t" \
  "buffer_load_dwordx4 v[36:39], v210, %[rh], s78 offen offset:1024 sc1\n\t" \
  "buffer_load_dwordx4 v[40:43], v210, %[rh], s79 offen offset:0 sc1\n\t" \
  "buffer_load_dwordx4 v[44:47], v210, %[rh], s79 offen offset:1024 sc1\n\t" \
  "buffer_load_dwordx4 v[48:51], v210, %[rh], s78 offen offset:2048 sc1\n\t" \
  "buffer_load_dwordx4 v[52:55], v210, %[rh], s78 offen offset:3072 sc1\n\t" \
  "buffer_load_dwordx4 v[56:59], v210, %[rh], s79 offen offset:2048 sc1\n\t" \
  "buffer_load_dwordx4 v[60:63], v210, %[rh], s79 offen offset:3072 sc1\n\t" \
  "buffer_load_dwordx4 v[64:67], v211, %[rh], s76 offen offset:0 sc1\n\t" \
  "buffer_load_dwordx4 v[68:71], v211, %[rh], s76 offen offset:1024 sc1\n\t" \
  "buffer_load_dwordx4 v[72:75], v211, %[rh], s77 offen offset:0 sc1\n\t" \
  "buffer_load_dwordx4 v[76:79], v211, %[rh], s77 offen offset:1024 sc1\n\t" \
  "buffer_load_dwordx4 v[80:83], v211, %[rh], s76 offen offset:2048 sc1\n\t" \
  "buffer_load_dwordx4 v[84:87], v211, %[rh], s76 offen offset:3072 sc1\n\t" \
  "buffer_load_dwordx4 v[88:91], v211, %[rh], s77 offen offset:2048 sc1\n\t" \
  "buffer_load_dwordx4 v[92:95], v211, %[rh], s77 offen offset:3072 sc1\n\t" \
  "buffer_load_dwordx4 v[96:99], v211, %[rh], s78 offen offset:0 sc1\n\t" \
  "buffer_load_dwordx4 v[100:103], v211, %[rh], s78 offen offset:1024 sc1\n\t" \
  "buffer_load_dwordx4 v[104:107], v211, %[rh], s79 offen offset:0 sc1\n\t" \
  "buffer_load_dwordx4 v[108:111], v211, %[rh], s79 offen offset:1024 sc1\n\t" \
  "buffer_load_dwordx4 v[112:115], v211, %[rh], s78 offen offset:2048 sc1\n\t" \
  "buffer_load_dwordx4 v[116:119], v211, %[rh], s78 offen offset:3072 sc1\n\t" \
  "buffer_load_dwordx4 v[120:123], v211, %[rh], s79 offen offset:2048 sc1\n\t" \
  "buffer_load_dwordx4 v[124:127], v211, %[rh], s79 offen offset:3072 sc1\n\t" \
  "s_waitcnt vmcnt(0)\n\t" \
  "s_bitcmp1_b32 s58, 2\n\t" \
  "s_cbranch_scc1 36f\n\t" \
  "v_max_u32 v148, v0, v1\n\t" \
  "v_max3_u32 v148, v148, v2, v3\n\t" \
  "v_max3_u32 v148, v148, v4, v5\n\t" \
  "v_max3_u32 v148, v148, v6, v7\n\t" \
  "v_max3_u32 v148, v148, v8, v9\n\t" \
  "v_max3_u32 v148, v148, v10, v11\n\t" \
  "v_max3_u32 v148, v148, v12, v13\n\t" \
  "v_max3_u32 v148, v148, v14, v15\n\t" \
  "v_max3_u32 v148, v148, v16, v17\n\t" \
  "v_max3_u32 v148, v148, v18, v19\n\t" \
  "v_max3_u32 v148, v148, v20, v21\n\t" \
  "v_max3_u32 v148, v148, v22, v23\n\t" \
  "v_max3_u32 v148, v148, v24, v25\n\t" \
  "v_max3_u32 v148, v148, v26, v27\n\t" \
  "v_max3_u32 v148, v148, v28, v29\n\t" \
  "v_max3_u32 v148, v148, v30, v31\n\t" \
  "v_max3_u32 v148, v148, v32, v33\n\t" \
  "v_max3_u32 v148, v148, v34, v35\n\t" \
  "v_max3_u32 v148, v148, v36, v37\n\t" \
  "v_max3_u32 v148, v148, v38, v39\n\t" \
  "v_max3_u32 v148, v148, v40, v41\n\t" \
  "v_max3_u32 v148, v148, v42, v43\n\t" \
  "v_max3_u32 v148, v148, v44, v45\n\t" \
  "v_max3_u32 v148, v148, v46, v47\n\t" \
  "v_max3_u32 v148, v148, v48, v49\n\t" \
  "v_max3_u32 v148, v148, v50, v51\n\t" \
  "v_max3_u32 v148, v148, v52, v53\n\t" \
  "v_max3_u32 v148, v148, v54, v55\n\t" \
  "v_max3_u32 v148, v148, v56, v57\n\t" \
  "v_max3_u32 v148, v148, v58, v59\n\t" \
  "v_max3_u32 v148, v148, v60, v61\n\t" \
  "v_max3_u32 v148, v148, v62, v63\n\t" \
  "v_max3_u32 v148, v148, v64, v65\n\t" \
  "v_max3_u32 v148, v148, v66, v67\n\t" \
  "v_max3_u32 v148, v148, v68, v69\n\t" \
  "v_max3_u32 v148, v148, v70, v71\n\t" \
  "v_max3_u32 v148, v148, v72, v73\n\t" \
  "v_max3_u32 v148, v148, v74, v75\n\t" \
  "v_max3_u32 v148, v148, v76, v77\n\t" \
  "v_max3_u32 v148, v148, v78, v79\n\t" \
  "v_max3_u32 v148, v148, v80, v81\n\t" \
  "v_max3_u32 v148, v148, v82, v83\n\t" \
  "v_max3_u32 v148, v148, v84, v85\n\t" \
  "v_max3_u32 v148, v148, v86, v87\n\t" \
  "v_max3_u32 v148, v148, v88, v89\n\t" \
  "v_max3_u32 v148, v148, v90, v91\n\t" \
  "v_max3_u32 v148, v148, v92, v93\n\t" \
  "v_max3_u32 v148, v148, v94, v95\n\t" \
  "v_max3_u32 v148, v148, v96, v97\n\t" \
  "v_max3_u32 v148, v148, v98, v99\n\t" \
  "v_max3_u32 v148, v148, v100, v101\n\t" \
  "v_max3_u32 v148, v148, v102, v103\n\t" \
  "v_max3_u32 v148, v148, v104, v105\n\t" \
  "v_max3_u32 v148, v148, v106, v107\n\t" \
  "v_max3_u32 v148, v148, v108, v109\n\t" \
  "v_max3_u32 v148, v148, v110, v111\n\t" \
  "v_max3_u32 v148, v148, v112, v113\n\t" \
  "v_max3_u32 v148, v148, v114, v115\n\t" \
  "v_max3_u32 v148, v148, v116, v117\n\t" \
  "v_max3_u32 v148, v148, v118, v119\n\t" \
  "v_max3_u32 v148, v148, v120, v121\n\t" \
  "v_max3_u32 v148, v148, v122, v123\n\t" \
  "v_max3_u32 v148, v148, v124, v125\n\t" \
  "v_max3_u32 v148, v148, v126, v127\n\t" \
  "v_cmp_eq_u32 vcc, -1, v148\n\t" \
  "s_cbranch_vccz 36f\n\t" \
  "s_add_u32 s59, s59, 0x1000000\n\t" \
  "s_cmp_lt_u32 s59, 0x10000000\n\t" \
  "s_cbranch_scc1 35b\n\t" \
  "s_or_b32 s58, s58, 9\n\t" \
  "s_branch 7f\n\t" \
  "36:\n\t" \
  "s_and_b32 s58, s58, 4\n\t" \
  "s_or_b32 s58, s58, 32\n\t" \
  "s_branch 31b\n\t" \
  "18:\n\t" \
  "v_mov_b32 v186, -1\n\t" \
  "s_and_b32 s80, s40, 3\n\t" \
  "s_mul_i32 s77, s80, 0xc000\n\t" \
  "s_add_i32 s77, s77, 0x40000\n\t" \
  "s_lshl_b32 s78, s80, 14\n\t" \
  "s_add_i32 s78, s78, 0x70000\n\t" \
  "s_mov_b32 s57, 0\n\t" \
  "s_add_i32 s96, s77, 0x1000\n\t" \
  "s_and_b32 s98, s40, 2\n\t" \
  "s_lshl_b32 s98, s98, 13\n\t" \
  "s_and_b32 s80, s40, 1\n\t" \
  "s_lshl_b32 s80, s80, 12\n\t" \
  "s_add_i32 s98, s98, s80\n\t" \
  "s_add_i32 s98, s98, 0x84000\n\t" \
  "12:\n\t" \
  "s_or_b32 s58, s58, 3\n\t" \
  "11:\n\t" \
  "buffer_load_dwordx4 v[0:3], v193, %[rh], s77 offen offset:0 sc1\n\t" \
  "buffer_load_dwordx4 v[4:7], v193, %[rh], s77 offen offset:1024 sc1\n\t" \
  "buffer_load_dwordx4 v[8:11], v193, %[rh], s77 offen offset:2048 sc1\n\t" \
  "buffer_load_dwordx4 v[12:15], v193, %[rh], s77 offen offset:3072 sc1\n\t" \
  "buffer_load_dwordx4 v[16:19], v193, %[rh], s96 offen offset:0 sc1\n\t" \
  "buffer_load_dwordx4 v[20:23], v193, %[rh], s96 offen offset:1024 sc1\n\t" \
  "buffer_load_dwordx4 v[24:27], v193, %[rh], s96 offen offset:2048 sc1\n\t" \
  "buffer_load_dwordx4 v[28:31], v193, %[rh], s96 offen offset:3072 sc1\n\t" \
  "buffer_load_dwordx4 v[32:35], v195, %[rh], s77 offen offset:0 sc1\n\t" \
  "buffer_load_dwordx4 v[36:39], v195, %[rh], s77 offen offset:2048 sc1\n\t" \
  "v_mov_b64 v[40:41], 0\n\t" \
  "v_mov_b64 v[42:43], 0\n\t" \
  "v_mov_b64 v[44:45], 0\n\t" \
  "v_mov_b64 v[46:47], 0\n\t" \
  "v_mov_b64 v[48:49], 0\n\t" \
  "v_mov_b64 v[50:51], 0\n\t" \
  "v_mov_b64 v[52:53], 0\n\t" \
  "v_mov_b64 v[54:55], 0\n\t" \
  "v_mov_b64 v[56:57], 0\n\t" \
  "v_mov_b64 v[58:59], 0\n\t" \
  "v_mov_b64 v[60:61], 0\n\t" \
  "v_mov_b64 v[62:63], 0\n\t" \
  "v_mov_b64 v[64:65], 0\n\t" \
  "v_mov_b64 v[66:67], 0\n\t" \
  "v_mov_b64 v[68:69], 0\n\t" \
  "v_mov_b64 v[70:71], 0\n\t" \
  "v_mov_b64 v[72:73], 0\n\t" \
  "v_mov_b64 v[74:75], 0\n\t" \
  "v_mov_b64 v[76:77], 0\n\t" \
  "v_mov_b64 v[78:79], 0\n\t" \
  "v_mov_b64 v[80:81], 0\n\t" \
  "v_mov_b64 v[82:83], 0\n\t" \
  "v_mov_b64 v[84:85], 0\n\t" \
  "v_mov_b64 v[86:87], 0\n\t" \
  "v_mov_b64 v[88:89], 0\n\t" \
  "v_mov_b64 v[90:91], 0\n\t" \
  "v_mov_b64 v[92:93], 0\n\t" \
  "v_mov_b64 v[94:95], 0\n\t" \
  "v_mov_b64 v[96:97], 0\n\t" \
  "v_mov_b64 v[98:99], 0\n\t" \
  "v_mov_b64 v[100:101], 0\n\t" \
  "v_mov_b64 v[102:103], 0\n\t" \
  "v_mov_b64 v[136:137], 0\n\t" \
  "v_mov_b64 v[138:139], 0\n\t" \
  "v_mov_b64 v[140:141], 0\n\t" \
  "v_mov_b64 v[142:143], 0\n\t" \
  "s_waitcnt vmcnt(0)\n\t" \
  "s_add_u32 s59, s59, 1\n\t" \
  "s_bitcmp1_b32 s58, 2\n\t" \
  "s_cbranch_scc1 10b\n\t" \
  "v_max_u32 v148, v0, v1\n\t" \
  "v_max3_u32 v148, v148, v2, v3\n\t" \
  "v_max3_u32 v148, v148, v4, v5\n\t" \
  "v_max3_u32 v148, v148, v6, v7\n\t" \
  "v_max3_u32 v148, v148, v8, v9\n\t" \
  "v_max3_u32 v148, v148, v10, v11\n\t" \
  "v_max3_u32 v148, v148, v12, v13\n\t" \
  "v_max3_u32 v148, v148, v14, v15\n\t" \
  "v_max3_u32 v148, v148, v16, v17\n\t" \
  "v_max3_u32 v148, v148, v18, v19\n\t" \
  "v_max3_u32 v148, v148, v20, v21\n\t" \
  "v_max3_u32 v148, v148, v22, v23\n\t" \
  "v_max3_u32 v148, v148, v24, v25\n\t" \
  "v_max3_u32 v148, v148, v26, v27\n\t" \
  "v_max3_u32 v148, v148, v28, v29\n\t" \
  "v_max3_u32 v148, v148, v30, v31\n\t" \
  "v_max3_u32 v148, v148, v32, v33\n\t" \
  "v_max3_u32 v148, v148, v34, v35\n\t" \
  "v_max3_u32 v148, v148, v36, v37\n\t" \
  "v_max3_u32 v148, v148, v38, v39\n\t" \
  "v_cmp_eq_u32 vcc, -1, v148\n\t" \
  "s_cbranch_vccz 10b\n\t" \
  "s_bitcmp1_b32 s59, 6\n\t" \
  "s_cbranch_scc0 11b\n\t" \
  "7:\n\t" \
  "92:\n\t" \
  "s_mov_b32 s42, s58\n\t" \
  "s_waitcnt vmcnt(0) lgkmcnt(0)\n\t" \
  ""

#define V53 "v0", "v1", "v2", "v3", "v4", "v5", "v6", "v7", "v8", "v9", "v10", "v11", "v12", "v13", "v14", "v15", "v16", "v17", \
  "v18", "v19", "v20", "v21", "v22", "v23", "v24", "v25", "v26", "v27", "v28", "v29", "v30", "v31", "v32", "v33", "v34", "v35", \
  "v36", "v37", "v38", "v39", "v40", "v41", "v42", "v43", "v44", "v45", "v46", "v47", "v48", "v49", "v50", "v51", "v52"
#define V100 V53, "v53", "v54", "v55", "v56", "v57", "v58", "v59", "v60", "v61", "v62", "v63", "v64", "v65", "v66", "v67", "v68", "v69", "v70", \
  "v71", "v72", "v73", "v74", "v75", "v76", "v77", "v78", "v79", "v80", "v81", "v82", "v83", "v84", "v85", "v86", "v87", "v88", \
  "v89", "v90", "v91", "v116", "v117", "v118", "v119"
#define STEP_PV
#define CB_CL "s60", "s61", "s62", "s63", "s64", "s65", "s66", "s67", "s68", "s69",
#define STEPS(BODY, ...) asm volatile(BODY : "=&{v[92:99]}"(X), "+{s[40:55]}"(P) STEP_PV \
  : "{v[120:151]}"(W.v[0]), "{v[152:183]}"(W.v[1]), "{v[184:215]}"(W.v[2]), "{v[216:247]}"(W.v[3]), "{v[100:115]}"(C), [rh] "s"(rh), [wv] "s"(w), [ru] "s"(rum), [sel] "s"(sel) __VA_ARGS__ \
  : "memory", "vcc", "scc", "s56", "s57", "s58", "s59", "s76", "s77", "s78", "s79", "s80", "s81", "s82", "s83", "s84", "s85", "s86", "s87", "s88", "s89", \
    "s90", "s91", "s92", "s93", "s94", "s95", "s96", "s97", V100)
__device__ __forceinline__ void keep_a(const Wts& W) {
#pragma unroll
  for (int i = 0; i < 32; ++i) asm volatile("" :: "a"(W.a[i]));
}
__device__ __forceinline__ char* lds_base(char* smem, int off) { asm volatile("" : "+v"(off)); return smem + off; }
__device__ __forceinline__ u32x2 tr4(unsigned p) { return __builtin_bit_cast(u32x2, __builtin_amdgcn_ds_read_tr16_b64_v4i16((LAS bf16x4*)(uintptr_t)p)); }
__device__ __forceinline__ float rb1(float x) { return __uint_as_float(pk2(0.f, x)); }

__device__ __forceinline__ void steps_init(u32x16& P, int h, int w, unsigned place) {
#pragma unroll
  for (int i = 0; i < 16; ++i) P[i] = 0;
  P[2] = 1; P[3] = CDUR; P[14] = H_MEM + (w >> 1) * CHK + (w & 1) * 4096 + (h & RHO) * 1024; P[15] = place;
}
__device__ __forceinline__ void steps_enter(u32x16& P, Clk& ck, bool dead) {
  ck.sync();
  P[2] = (P[2] & 3u) | (dead ? 4u : 0u); P[6] = ck.c2t << 15; P[11] = ck.ce; P[12] = ck.t2c; P[13] = ck.te;
#pragma unroll
  for (int i = 0; i < 16; ++i) P[i] = __builtin_amdgcn_readfirstlane(P[i]);
}

__device__ __forceinline__ void role_qk(const ChainArgs& a, int h, char* smem, const int w, Gate& G) {
  int lane = opaque_tid(0);
  const int b = h; (void)b;
  u64* prof = a.prof; (void)prof;
  PDECL(L_PRF, 4);
  const float CS = 0.08838834764831845f * 1.4426950408889634f;
  GAS unsigned* abortw = (GAS unsigned*)(a.sync + SY_ABORT);
  bool dead = false;
  Wts W;
  Clk ck; ck.init();
  u32x4 gv;
  if (QK_D) while (__builtin_amdgcn_s_memrealtime() - G.entry < QK_D) __builtin_amdgcn_s_sleep(8);
  load_weights<0>(a, h, w, lane, smem, W, G, gv);
  if (!G.look(a.sync, gv)) return;
  PZERO;
  const Sel sel = G.sel;
  const __amdgpu_buffer_rsrc_t rh = rsrc_hot(a.sync, sel), rum = rsrc_u(a.sync);
  u32x4 k0[2];
  const bool two = start_wait(a.sync, abortw, lane, dead, CT_Q + ct_near(sel) + h, [&]() { for (int i = 0; i < 2; ++i) k0[i] = ld16(rum, x_slot(1, 0, h, sel) + (2 * w + i) * 1024 + lane * 16); });
  G.arrive(a.sync);
#pragma unroll
  for (int i = 0; i < 2; ++i) *(u32x4*)(smem + L_KX + (2 * w + i) * 1024 + lane * 16) = k0[i];
  auto pm_o = [&](int l) { return 2 * CHK + h * 256 + (l & 1) * 128 + (l >> 3) * 16 + (l & 6) * 2; };
  auto fac = [&](int l, int hf) { return ((l & 7) >> 1) == 2 * (l >> 5) + ((h >> 1) & 1) && ((l >> 3) & 1) == hf ? 1.f : 0.f; };
  u32x16 C, P;
  {
    const int r = lane & 15, g = lane >> 4;
    const unsigned l0 = (unsigned)(uintptr_t)(LAS char*)smem;
    C[0] = lane * 16; C[1] = l0 + L_MEM + lane * 16; C[2] = l0 + L_WTS + w * 4096 + lane * 16; C[3] = C[2] + 65536;
    C[4] = (lane & 2) ? 0xEEEE : 0x4444;
    C[5] = l0 + L_XCH + (16 * w + 2 * (r >> 2) + (r & 1)) * 32 + g * 8 + ((r >> 1) & 1) * 4;
    C[6] = l0 + L_XK + (32 * w + r) * 16 + g * 4;
    C[7] = w == 3 ? H_PX + (h >> 3) * CHK + (h & 7) * 1024 + lane * 16 : w ? (2 * h + 1) * CHK + (w - 1) * 4096 + lane * 16 : l0 + L_XCH + (8 * g + (r >> 2)) * 32 + (r & 3) * 8;
    C[8] = l0 + L_XK + (8 * g + (r >> 2)) * 16 + (r & 1) * 8;
    C[9] = w ? (lane & 1) * 4096 : (lane == 0 ? H_ANN + h * 128 : 0x7fffffffu);
    C[10] = w == 0 ? (h >> 4) * CHK + (h & 15) * 512 + ((lane >> 1) & 1) * 256 + (lane >> 5) * 128 + (lane & 1) * 64 + ((lane >> 3) & 3) * 16 + ((lane >> 2) & 1) * 8 :
            w < 3 ? H_PX + ((w - 1) * 4 + (h >> 3)) * CHK + (h & 7) * 1024 + lane * 16 :
            lane < 32 ? (h >> 4) * CHK + (h & 15) * 512 + lane * 16 : (lane < 48 ? 2 * CHK + h * 256 + (lane - 32) * 16 : 0x7fffffffu);
    C[11] = w == 0 ? pm_o(lane) : (lane < (w == 3 ? 32 : 16) ? H_PX + (w == 3 ? 8 + g : 7 + w) * CHK + hpos(h) * 256 + r * 16 : 0x7fffffffu);
    C[12] = H_TR + (lane & 31) * 128; C[13] = l0 + L_PRF + w * 1024 + (lane >> 6);
    unsigned mk = 0;
#pragma unroll
    for (int i = 0; i < 8; ++i) mk |= (16 * ((i >> 1) & 1) + 8 * (g & 1) + 4 * (g >> 1) + 2 * (i & 1) + (i >> 2) > 16 * (w - 1) + r ? 1u : 0u) << i;
    C[14] = w ? mk : __float_as_uint(fac(lane, 0)); C[15] = __float_as_uint(fac(lane, 1));
    asm volatile("" : "+v"(C));
  }
  SSTAMP(96 + h);
  steps_init(P, h, w, 2 * h * CHK + (3 * w - 3) * 1024);
  if (w) P[3] = two ? NSEG : 0;
  GAS unsigned* kcp = ctr(a, CT_Q + ct_near(sel) + h);
  unsigned spins = 0;
  while (true) {
    u32x8 X; u32x16 pv; (void)pv;
    steps_enter(P, ck, dead);
    STEPS(QK_BODY, , [cs] "s"(CS), [kc] "s"(kcp));
    keep_a(W);
    const unsigned res = __builtin_amdgcn_readfirstlane(P[2]), s = __builtin_amdgcn_readfirstlane(P[4]);
    lane = opaque_tid(0);
    if (res & 8u) {
      auto sw = [](float v, int k) { return __int_as_float(k == 0 ? __builtin_amdgcn_mov_dpp(__float_as_int(v), 0xB1, 0xf, 0xf, false) :
                                                           k == 1 ? __builtin_amdgcn_mov_dpp(__float_as_int(v), 0x4E, 0xf, 0xf, false) :
                                                                    __builtin_amdgcn_mov_dpp(__float_as_int(v), 0x141, 0xf, 0xf, false)); };
      float x[5], d = fmaxf(__uint_as_float(X[4]), 0.f);
      d = fmaxf(d, sw(d, 0)); d = fmaxf(d, sw(d, 1)); d = fmaxf(d, sw(d, 2));
#pragma unroll
      for (int i = 0; i < 5; ++i) x[i] = __builtin_amdgcn_exp2f(__uint_as_float(X[i]) - d);
      float z = ((x[0] + x[1]) + (x[2] + x[3])) + x[4];
      z += sw(z, 0); z += sw(z, 1); z += sw(z, 2);
      z = __builtin_amdgcn_rcpf(z);
      const float y = sw(x[4], 0), f = __builtin_amdgcn_exp2f(-d) * z;
      const unsigned pm = (lane & 1) ? pk2(fac(lane, 0) * f, fac(lane, 1) * f) : pk2(x[4] * z, y * z);
      if (!(__uint_as_float(X[5]) < 1.0e18f))
        __builtin_amdgcn_raw_buffer_store_b32(nosent(pm), rh, pm_o(lane), H_P + ((s - 1) & 3) * 3 * CHK, ST_AUX);
    }
    if (res & 64u) break;
    if (res & 32u) { ctr_wait(kcp, min(s + 2, (unsigned)NSEG), abortw, lane, dead); ctr_wait(kcp + 1024, min(s + 2, (unsigned)NSEG), abortw, lane, dead); }
    if ((res & 32u) && s == 1 && !two) {
      for (int i = 3 * w - 3; i < min(3 * w, 8); ++i) *(u32x4*)(smem + L_KX + i * 1024 + lane * 16) = ld16(rum, x_slot(1, 1, h, sel) + i * 1024 + lane * 16);
    }
    if (res & 17u) {
      spins |= 63u;
      if (dead || poll_fail(spins, abortw, lane)) dead = true;
    }
  }
  PDUMP(0);
}

__device__ __forceinline__ void role_vo(const ChainArgs& a, int h, char* smem, const int w, Gate& G) {
  int lane = opaque_tid(0);
  const int b = 32 + h; (void)b;
  u64* prof = a.prof; (void)prof;
  PDECL(L_PRF, 4);
  GAS unsigned* abortw = (GAS unsigned*)(a.sync + SY_ABORT);
  bool dead = false;
  Wts W;
  Clk ck; ck.init();
  u32x4 gv;
  load_weights<1>(a, h, w, lane, smem, W, G, gv);
  if (!G.look(a.sync, gv)) return;
  PZERO;
  const Sel sel = G.sel;
  start_wait(a.sync, abortw, lane, dead, -1, [] {});
  G.arrive(a.sync);
  const __amdgpu_buffer_rsrc_t rh = rsrc_hot(a.sync, sel), rum = rsrc_u(a.sync);
  u32x16 C, P;
  {
    const int r = lane & 15, g = lane >> 4;
    const unsigned l0 = (unsigned)(uintptr_t)(LAS char*)smem;
#pragma unroll
    for (int i = 0; i < 16; ++i) C[i] = H_TR + (lane & 31) * 128;
    C[0] = lane * 16; C[1] = l0 + L_MEM + lane * 16; C[2] = l0 + L_WTS + w * 4096 + lane * 16; C[3] = C[2] + 65536;
    C[4] = (lane & 2) ? 0xEEEE : 0x4444;
    C[5] = l0 + L_XCH + (32 * w + r) * 16 + g * 4;
    C[6] = (lane & 8) ? 0xEEEE : 0x4444;
    C[7] = l0 + L_XCH + (32 * (g >> 1) + 8 * (g & 1) + (r >> 2)) * 16 + (r & 1) * 8;
    C[8] = ((g & 1) * 8 + (g >> 1)) * CHK + r * 16;
    C[13] = l0 + L_PRF + w * 1024 + (lane >> 6);
    asm volatile("" : "+v"(C));
  }
  SSTAMP(128 + h);
  steps_init(P, h, w, SY_UM + (16 * (h & 3) + 2 * w) * CHK + hpos(h) * 256);
  unsigned spins = 0;
  while (true) {
    u32x8 X; u32x16 pv; (void)pv;
    steps_enter(P, ck, dead);
    STEPS(VO_BODY);
    keep_a(W);
    lane = opaque_tid(0);
    if (__builtin_amdgcn_readfirstlane(P[2]) & 64u) break;
    spins |= 63u;
    if (dead || poll_fail(spins, abortw, lane)) dead = true;
  }
  PDUMP(0);
}

__device__ __forceinline__ void calibrate(const ChainArgs& a, int cal, int slot, u64 entry) {
  const u32x4 rs = own_rsrc(a.sync, 64 * PAGE);
  unsigned best[2] = {1u << 30, 1u << 30};
  for (int i = 0;; ++i) {
    if (i >= CALN && ((min(best[0], best[1]) < CALT && max(best[0], best[1]) - min(best[0], best[1]) > 120u) || __builtin_amdgcn_s_memrealtime() - entry > CALM)) break;
#pragma unroll
    for (int hf = 0; hf < 2; ++hf) {
      u64 c0, c1;
      unsigned z = 0;
      asm volatile("s_mov_b64 exec, 1\n\ts_memtime %0\n\ts_waitcnt lgkmcnt(0)\n\tbuffer_atomic_or %2, off, %3, %4 sc0 sc1\n\ts_waitcnt vmcnt(0)\n\ts_memtime %1\n\ts_mov_b64 exec, -1\n\ts_waitcnt lgkmcnt(0)"
                   : "=&s"(c0), "=&s"(c1), "+v"(z) : "s"(rs), "s"(cal * PAGE + (slot >> 1) * CHK + hf * 8192 + (slot & 1) * 4096) : "memory");
      best[hf] = min(best[hf], (unsigned)(c1 - c0));
    }
  }
  const u64 hs = (__builtin_amdgcn_s_memrealtime() & ~1ull) | ((best[1] < best[0] ? 1u : 0u) ^ die_half());
  if (opaque_tid(0) == 0) __hip_atomic_store((GAS u64*)ctr(a, CT_CAL + (slot >> 1)) + (slot & 1), hs, __ATOMIC_RELAXED, __HIP_MEMORY_SCOPE_AGENT);
}
__device__ __forceinline__ u32x4 pk8(f32x4 a, f32x4 b) { return (u32x4){pk2(a[0], a[1]), pk2(a[2], a[3]), pk2(b[0], b[1]), pk2(b[2], b[3])}; }
__device__ __forceinline__ unsigned pk2v(float a, float b) { unsigned r; asm volatile("v_cvt_pk_bf16_f32 %0, %1, %2" : "=v"(r) : "v"(a), "v"(b)); return r; }
__device__ __forceinline__ u32x4 pk8v(f32x4 a, f32x4 b) { asm volatile("s_nop 7"); return (u32x4){pk2v(a[0], a[1]), pk2v(a[2], a[3]), pk2v(b[0], b[1]), pk2v(b[2], b[3])}; }
__device__ __forceinline__ void st16g(u16* p, u32x4 v) { asm volatile("global_store_dwordx4 %0, %1, off " OUTP "\n\ts_nop 1" :: "v"(p), "v"(v) : "memory"); }
__device__ __forceinline__ void role_cb(const ChainArgs& a, int cbi, char* smem, const int w, Gate& G) {
  int lane = opaque_tid(0);
  const int b = 64 + cbi; (void)b;
  const int par = cbi >> 5, ci = cbi & 31, J = ci >> 3;
  u64* prof = a.prof; (void)prof;
  PDECL(65536, 8); PZERO;
  GAS unsigned* abortw = (GAS unsigned*)(a.sync + SY_ABORT);
  bool dead = false;
  const int tw = 2 * ci + (w & 1);
  const unsigned mo = (tw >> 5) * CHK + (tw & 31) * 256;
  Sel sel = 0;
  auto start = [&]() {
    lane = opaque_tid(0);
    SSTAMP(0 + ci);
    unsigned m0 = 0;
    if (par == 0 && w < 2) {
      const u16* m = a.mem0 + (2 * (lane >> 4) + ((lane >> 2) & 1)) * DM + 16 * tw + (lane & 8);
      m0 = m[lane & 3] | (unsigned)m[7 - (lane & 3)] << 16;
    }
    if (!G.look(a.sync, (u32x4){0u, 0u, 0u, 0u})) return false;
    sel = G.sel;
    SSTAMP(64 + ci);
    const __amdgpu_buffer_rsrc_t rh = rsrc_hot(a.sync, sel), rum = rsrc_u(a.sync);
    const unsigned s1v = ~(unsigned)(lane >> 6);
    const u32x4 sv = {s1v, s1v, s1v, s1v};
    auto um = [&](int k) {
#pragma unroll
      for (int i = 0; i < 4; ++i) st16(rum, near_half(SY_UM + (64 * k + tw) * CHK, sel) + (w >> 1) * 4096 + i * 1024 + lane * 16, sv);
    };
    um(par);
    const unsigned dv = cbi == H_DLT / CHK ? K_DEF * TU : s1v;
#pragma unroll
    for (int i = 0; i < 2; ++i) st16(rh, cbi >= 2 && cbi < 45 ? (cbi < 35 ? cbi * CHK : H_PX + (cbi - 35) * CHK) + w * 2048 + i * 1024 + lane * 16 : 0x7fffffffu, (u32x4){dv, s1v, s1v, s1v});
    if (par == 0 && w < 2) st4(rh, H_MEM + mo + lane * 4, nosent(m0));
    asm volatile("s_waitcnt vmcnt(0)" ::: "memory");
    lds_barrier();
    if (w == 0 && lane == 0) ctr_add(a, CT_INIT + J, 1u);
    SSTAMP(256 + ci);
    G.arrive(a.sync);
    um(par + 2);
    {
      const __amdgpu_buffer_rsrc_t rx = mk_rsrc((void*)a.X, 4u << 20);
      u32x4 v[16];
#pragma unroll
      for (int i = 0; i < 16; ++i) v[i] = __builtin_amdgcn_raw_buffer_load_b128(rx, (unsigned)(cbi * 4096 + w * 1024 + lane * 16), (unsigned)(i * 262144), 0);
#pragma unroll
      for (int i = 0; i < 16; ++i) asm volatile("" :: "v"(v[i]));
    }
    SSTAMP(224 + ci);
    start_wait(a.sync, abortw, lane, dead, -1, [] {});
    return true;
  };
  if (!start()) return;
  Wts W;
#pragma unroll
  for (int i = 0; i < 32; ++i) asm volatile("" : "=a"(W.a[i]));
  u32x16 C, P; u32x8 C2;
  {
    const unsigned l0 = (unsigned)(uintptr_t)(LAS char*)smem, oor = 0x7fffffffu;
    const int r = lane & 7, t = (lane >> 3) & 1, g = lane >> 4, e = w & 1;
    const bool L = w < 2;
    C[0] = lane * 16;
    C[1] = L ? l0 + 32768 + e * 8192 + (lane & 2) * 512 + ((lane & 1) * 32 + (lane >> 2)) * 16 : e * CHK + t * 512 + (g & 1) * 256 + (r >> 2) * 128 + (g >> 1) * 64 + (r & 3) * 16;
    C[2] = L ? (4 * t + g) * 1024 + r * 16 : l0 + 81920 + e * 32768 + lane * 16;
    C[3] = L ? l0 + e * 1024 + lane * 8 : 2 * CHK + e * 4096 + (4 * (g >> 1) + 2 * t + (g & 1)) * 256 + 128 + r * 16;
    C[4] = ((16 * e + (lane >> 2)) * OUTW + DM + 32 * ci + 8 * (lane & 3)) * 2;
    C[5] = l0 + (L ? e * 8192 + lane * 16 : e * 512 + lane * 8); C[6] = l0 + 16384 + lane * 16; C[7] = l0 + 32768 + (L ? e * 8192 + 4096 : e * 2048) + lane * 16;
    C[8] = (lane & 31) * 128;
    C[9] = (8 * (g & 1) + 4 * (g >> 1) + 2 * t + (r >> 2)) * 16 + 4 * (r < 4 ? r : 7 - r);
    C[10] = (lane | e) ? oor : ci * 128;
    C[11] = L ? SY_CT + (CT_U + ct_near(sel) + 16 * e + (lane & 15)) * 128 : (t ? 0xEEEE : 0x4444); C[12] = L ? l0 + 81920 + e * 32768 + lane * 16 : ((lane & 2) ? 0xEEEE : 0x4444); C[13] = r < 4 ? 0x01000504u : 0x07060302u;
    C[14] = l0 + 49152 + 32 * e + (lane >> 6); C[15] = l0 + 49152 + (lane >> 6);
    C2[0] = L ? (lane < 16 ? lane * 16 : oor) : ux_tile(2 * ci) + lane * 16;
    C2[1] = lane == 0 && e && ci < 8 ? SY_CT + (CT_DONE + ct_near(~sel) + 8 * par + ci) * 128 : oor;
    C2[2] = (L ? ux_tile(2 * ci) : H_PX + 2 * e * CHK) + lane * 16;
#pragma unroll
    for (int n = 0; n < 4; ++n) C2[3 + n] = L ? H_PX + (8 + e) * CHK + (J ^ n) * 2048 + lane * 16 : C2[2] + CHK;
    C2[7] = l0 + 65536 + w * 2048 - 64 + (lane >> 6);
    asm volatile("" : "+v"(C), "+v"(C2));
#pragma unroll
    for (int i = 0; i < 16; ++i) P[i] = par - 2;
    P[2] = 48; P[6] = P[7] = K_DEF * TU; P[8] = SY_UM + tw * CHK + J * 2048; P[9] = H_MEM + mo; P[10] = 2 * CHK + J * 256; P[11] = e * 16 * UXH; P[12] = H_DLT + par * 4096;
  }
  Clk ck; ck.init();
  unsigned spins = 0;
  while (true) {
    u32x16 pv; (void)pv;
    unsigned res = __builtin_amdgcn_readfirstlane(P[2]);
    if (res & 64u) break;
    const u32x4 rh2 = own_rsrc(a.sync + near_half(SY_HOT, sel), H_BYTES - 8192), ra = own_rsrc(a.sync, SY_BYTES), ro = own_rsrc(a.out, S_LEN * OUTW * 2);
    if (res & 1u) {
      spins |= 63u;
      if (dead || poll_fail(spins, abortw, opaque_tid(0))) dead = true;
    }
    ck.sync();
    P[2] = (res & 58u) | (dead ? 4u : 0u); P[1] = ck.ce; P[3] = ck.te; P[4] = ck.t2c; P[5] = ck.c2t << 15;
#pragma unroll
    for (int i = 0; i < 16; ++i) P[i] = __builtin_amdgcn_readfirstlane(P[i]);
    asm volatile(CB_BODY : "+{s[40:55]}"(P) STEP_PV
                 : "{v[192:207]}"(C), "{v[208:215]}"(C2), [rh] "s"(rh2), [ru] "s"(ra), [ro] "s"(ro), [wv] "s"(w), [sel] "s"(sel)
                 : "memory", "vcc", "scc", CB_CL "s56", "s57", "s58", "s59", "s76", "s77", "s78", "s79", "s80", "s81", "s82", "s83", "s84", "s85", "s86", "s87", "s88", "s89", "s90", "s91", "s92", "s93", "s94", "s95", "s96", "s97", "s98", "s99", "s100", "s101", V53, "v53", "v54", "v55", "v56", "v57", "v58", "v59", "v60", "v61", "v62", "v63", "v64", "v65", "v66", "v67", "v68", "v69", "v70", "v71", "v72", "v73", "v74", "v75", "v76", "v77", "v78", "v79", "v80", "v81", "v82", "v83", "v84", "v85", "v86", "v87", "v88", "v89", "v90", "v91", "v92", "v93", "v94", "v95", "v96", "v97", "v98", "v99", "v100", "v101", "v102", "v103", "v104", "v105", "v106", "v107", "v108", "v109", "v110", "v111", "v112", "v113", "v114", "v115", "v116", "v117", "v118", "v119", "v120", "v121", "v122", "v123", "v124", "v125", "v126", "v127", "v128", "v129", "v130", "v131", "v132", "v133", "v134", "v135", "v136", "v137", "v138", "v139", "v140", "v141", "v142", "v143", "v144", "v145", "v146", "v147", "v148", "v149", "v150", "v151", "v152", "v153", "v154", "v155", "v156", "v157", "v158", "v159", "v160", "v161", "v162", "v163", "v164", "v165", "v166", "v167", "v168", "v169", "v170", "v171", "v172", "v173", "v174", "v175", "v176", "v177", "v178", "v179", "v180", "v181", "v182", "v183", "v184", "v185", "v186", "v187", "v188", "v189", "v190", "v191");
    keep_a(W);
  }
  PDUMP(2);
}

__device__ __forceinline__ void bg_drain() {
  asm volatile("s_waitcnt vmcnt(0)" ::: "memory");
  __builtin_amdgcn_s_barrier();
  asm volatile("" ::: "memory");
}
#define BG_SLP 16
#define A3_SLP 16
#define WAH 4
#define DSP 4
#define SIGQ 2
__device__ __forceinline__ void task_w(const ChainArgs& a, int kind, int h, char* smem, int wv, Gate& G) {
  const int lane = opaque_tid(0), w = wv, r = lane & 15, g = lane >> 4;
  const __amdgpu_buffer_rsrc_t rw = mk_rsrc(kind == 0 ? a.Wq : (kind == 1 ? a.Wk : a.Wv), 8u << 20), rx = mk_rsrc(a.X, 4u << 20);
  const unsigned lds0 = (unsigned)(uintptr_t)(LAS char*)smem + w * 4096;
  unsigned vx[2], vw[4];
#pragma unroll
  for (int j = 0; j < 2; ++j) {
    const int rr = 8 * j + (lane >> 3);
    const int tb = kind == 0 ? rr : (kind == 1 ? 16 * ((rr >> 1) & 1) + 8 * ((rr >> 2) & 1) + 4 * (rr >> 3) + 2 * (rr & 1) : 8 * (rr >> 2) + (rr & 3));
    vx[j] = tb * DM * 2 + ((lane & 7) ^ (rr >> 1)) * 16;
  }
#pragma unroll
  for (int j = 0; j < 4; ++j) { const int row = (w * 4 + j) * 4 + g; vw[j] = row * HD * 2 + (r ^ swz_b(row)) * 16; }
  const u64 wo = kind ? bg_order(1) : bg_order(0);
  auto xo = [&](int q) { return ((int)((wo >> (16 * q + 4 * w)) & 15) ^ (h & RHO)) * 128; };
  unsigned dT = (kind == 0 ? 16 : (kind == 1 ? 1 : 4)) * DM * 2;
  asm volatile("s_nop 4" : "+s"(dT));
  const unsigned vx0 = ((w & 1) ? vx[1] : vx[0]) + (w >> 1) * dT;
  auto st_w = [&](int n) {
#pragma unroll
    for (int j = 0; j < 4; ++j) dma16(rw, vw[j], (64 * ((int)((wo >> (4 * n)) & 15) ^ (h & RHO)) * HD + 128 * h) * 2, lds0 + (n & 7) * 16384 + j * 1024, W_NT & 16);
    dma16(rx, vx0, ((int)((wo >> (4 * n)) & 15) ^ (h & RHO)) * 128, lds0 - 3072 * w + 131072 + (n & 7) * 4096);
  };
  auto st_x = [&](int s, int b, int q, int j, bool pad) { dma16(rx, vx[j & 1], s * 65536 + (j >> 1) * dT + xo(q), lds0 + (b * 4 + q) * 16384 + j * 1024, false, pad); };
  if (KQ_D && kind < 2) while (__builtin_amdgcn_s_memrealtime() - G.entry < KQ_D) __builtin_amdgcn_s_sleep(8);
  __builtin_amdgcn_s_waitcnt(0x0F70);
  u32x4 W[32][2], gv;
  char* xb[3][2];
#pragma unroll
  for (int i = 0; i < 6; ++i) xb[i >> 1][i & 1] = lds_base(smem, (i >> 1) * 65536 + (r >> 3) * 1024 + (r & 7) * 128 + ((4 * (i & 1) + g) ^ (r >> 1)) * 16);
  f32x4 acc[2][4];
#pragma unroll
  for (int i = 0; i < 4; ++i) acc[1][i] = vzero();
#pragma unroll
  for (int n = 0; n < WAH; ++n) st_w(n);
#pragma unroll
  for (int n = 0; n < 16; ++n) {
    asm volatile("s_waitcnt vmcnt(%0)" :: "n"(4 * WAH - 4 + (n < 16 - WAH ? WAH - 1 : 15 - n)) : "memory");
    lds_barrier();
    u32x4 f[4], y[4];
#pragma unroll
    for (int i = 0; i < 4; ++i) {
#pragma unroll
      for (int q = 0; q < 2; ++q) {
        const int row = (i & 1) * 32 + 8 * g + 4 * q + (r >> 2);
        const u32x2 u = __builtin_bit_cast(u32x2, __builtin_amdgcn_ds_read_tr16_b64_v4i16((LAS bf16x4*)(smem + (n & 7) * 16384 + row * 256 + (((4 * w + (r & 3)) ^ swz_b(row)) * 16) + 8 * (i >> 1))));
        f[i][2 * q] = u[0]; f[i][2 * q + 1] = u[1];
      }
      y[i] = *(const u32x4*)(xb[2][i & 1] + (n & 7) * 4096 + (i >> 1) * 2048);
    }
    SB0;
    if (n == 16 - WAH) G.ask(a.sync, gv);
    if (n == 15) asm volatile("" : "+v"(gv));
    if (n + WAH < 16) st_w(n + WAH);
    else {
#pragma unroll
      for (int j = 0; j < 4; ++j) st_x(1 + ((n + WAH - 16) >> 2), (n + WAH - 16) >> 2, (n + WAH) & 3, j, true);
    }
#pragma unroll
    for (int i = 0; i < 8; ++i)
      asm volatile("s_nop 1\n\tv_mfma_f32_16x16x32_bf16 %0, %1, %2, %0" : "+v"(acc[1][i & 3]) : "v"(f[2 * (i & 1) + (i >> 2)]), "v"(y[(i & 2) + (i >> 2)]));
#pragma unroll
    for (int i = 0; i < 4; ++i) { asm volatile("" : "+a"(f[i])); W[2 * n + (i & 1)][i >> 1] = f[i]; }
  }
  if (!G.pass(a.sync, gv)) { bg_drain(); return; }
  SSTAMP(160 + h);
  const Sel sel = G.sel;
  const __amdgpu_buffer_rsrc_t ru = mk_rsrc(a.sync, SY_BYTES);
  const unsigned sgo = (w | lane) ? 0x7fffffffu : SY_CT + (kind == 2 ? CT_V + ct_near(~sel) : CT_Q + 32 * kind + ct_near(sel)) * 128 + h * 128;
  const u32x4 rx2 = own_rsrc(a.X, 4u << 20);
  unsigned vq[4][4];
#pragma unroll
  for (int i = 0; i < 16; ++i) { vq[i >> 2][i & 3] = vx[i & 1] + ((i >> 1) & 1) * dT + xo(i >> 2); asm volatile("" : "+v"(vq[i >> 2][i & 3])); }
  u32x4 xf[4][2], o;
  auto rd = [&](int b, int k, int T) { xf[k & 3][T] = *(const u32x4*)(xb[b][k & 1] + (k >> 1) * 4096 + T * 2048); };
  auto signal = [&](int s) {
    st4(ru, sgo, s);
    if (w == 0 && lane == 0 && s >= 0 && (s < 5 || (s & 7) == 0 || (s & 15) == 2 || s > 61)) {
    }
  };
#pragma unroll
  for (int T = 0; T < 2; ++T) st16s(pk8v(acc[1][2 * T], acc[1][2 * T + 1]), ru, lane * 16 + T * 4096, x_slot(kind, 0, h, sel) + w * 1024);
#pragma unroll
  for (int i = 4 * WAH; i < 28; ++i) st_x(1 + (i >> 4), i >> 4, (i >> 2) & 3, i & 3, true);
  wait_vm<26>();
  lds_barrier();
#pragma unroll
  for (int i = 0; i < 6; ++i) rd(0, i >> 1, i & 1);
  WST(kind == 2, 18);
  auto seg = [&](int s, int b) {
    const unsigned so = x_slot(kind, s - 1, h, sel) + w * 1024;
    WST(kind == 2 && s > 0 && s < 4, 18 + s);
#pragma unroll
    for (int k = 0; k < 32; ++k)
#pragma unroll
      for (int i = 0; i < 4; ++i) {
        if (k == 0) asm volatile("v_mfma_f32_16x16x32_bf16 %0, %1, %2, 0" : "=&v"(acc[b][i]) : "a"(W[k][i & 1]), "v"(xf[k & 3][i >> 1]));
        else asm volatile("v_mfma_f32_16x16x32_bf16 %0, %1, %2, %0" : "+v"(acc[b][i]) : "a"(W[k][i & 1]), "v"(xf[k & 3][i >> 1]));
        SB0;
        const int q = k >> 3, n = 4 * k + i;
        if ((k & 7) == 5 && i == 0) {
          if (q == SIGQ) wait_vm<4 * SIGQ>(); else wait_vm<20>();
          if (q == 1 && __builtin_expect(s - 2u < W_NOW, 0)) { wait_vm<4>(); __builtin_amdgcn_s_barrier(); signal(s - 1); }
          else if (q == 0 && b == 0 && __builtin_expect(s == 1, 0)) { wait_vm<32 - 4 * WAH>(); __builtin_amdgcn_s_barrier(); signal(0); }
          else __builtin_amdgcn_s_barrier();
          asm volatile("" ::: "memory");
        }
        if (i == 1 || i == 2) rd(k + 3 < 32 ? b : b ^ 1, (k + 3) & 31, i - 1);
        {
          const int m = (n + 128 - 23) & 127, j = (m & 31) / DSP, q0 = m >> 5;
          if (j < 4 && (m & 31) % DSP == 0) asm volatile("s_add_i32 m0, %0, %1" :: "s"(lds0), "n"(((b ^ (q0 == 0) ^ (n < 23)) * 4 + ((q0 + 3) & 3)) * 16384 + j * 1024) : "m0", "scc");
          if (j < 4 && (m & 31) % DSP == 1)
            asm volatile("buffer_load_dwordx4 %0, %1, %2 offen lds" :: "v"(vq[(q0 + 3) & 3][j & 3]), "s"(rx2), "s"((s + 2 - (q0 == 0) - (n < 23)) * 65536) : "memory");
        }
        if (k == 8 * SIGQ + 5 && i == 2) signal(s - 1);
        if (k >= 1 && k < 5 && (i == 0 || i == 3)) { const f32x4 x = acc[b ^ 1][k - 1]; o[2 * (~k & 1) + (i & 1)] = pk2v(x[2 * (i & 1)], x[2 * (i & 1) + 1]); }
        if ((k == 2 || k == 4) && i == 3) { __builtin_amdgcn_raw_buffer_store_b128(o, ru, (unsigned)(lane * 16 + (k >> 2) * 4096), so, ST_AUX); asm volatile("s_nop 0" :: "v"(o)); }
        SB0;
      }
  };
  for (int s = 1;; s += 2) {
    seg(s, 0);
    if (s == NSEG - 1) break;
    seg(s + 1, 1);
  }
#pragma unroll
  for (int T = 0; T < 2; ++T) st16s(pk8v(acc[0][2 * T], acc[0][2 * T + 1]), ru, lane * 16 + T * 4096, x_slot(kind, NSEG - 1, h, sel) + w * 1024);
  bg_drain();
  signal(NSEG - 1);
}
__device__ __forceinline__ float blo(unsigned d) { return __uint_as_float(d << 16); }
__device__ __forceinline__ float bhi(unsigned d) { return __uint_as_float(d & 0xffff0000u); }
constexpr int A2_CSTR = 4160;
constexpr int A2_UOFF = 4 * A2_CSTR + 16 * 80;
constexpr int A2_USTR = 80 * 80;
constexpr int A2_WLDS = 4 * A2_CSTR + 2 * A2_USTR;
__device__ __forceinline__ void a2_wave(const ChainArgs& a, int d0, int d1, char* smem, int w, Sel sel) {
  const int lane = opaque_tid(0);
  char* Wb = smem + 1024 + w * A2_WLDS;
  char* U = Wb + A2_UOFF;
  const __amdgpu_buffer_rsrc_t rf = mk_rsrc((void*)a.Assm, 4u << 20), rd_ = mk_rsrc((void*)a.AD, 2048);
  const __amdgpu_buffer_rsrc_t rkv = rsrc_u(a.sync);
  u32x4 hi[4], uv[2][4];
  u32x2 lo[4];
  unsigned dn;
  auto ld_h = [&](int d) {
#pragma unroll
    for (int i = 0; i < 4; ++i) {
      hi[i] = __builtin_amdgcn_raw_buffer_load_b128(rf, (unsigned)((255 - 64 * i - lane) * 16), (unsigned)(d * 4096), 2);
      lo[i] = __builtin_amdgcn_raw_buffer_load_b64(rf, i == 3 && lane == 63 ? 0x7fffffffu : (255 - 64 * i - lane) * 16 - 8, (unsigned)(d * 4096), 2);
    }
    dn = __builtin_amdgcn_raw_buffer_load_b16(rd_, 0, 2 * d, 0);
  };
  auto ld_u = [&](int d) {
#pragma unroll
    for (int i = 0; i < 8; ++i) uv[i & 1][i >> 1] = __builtin_amdgcn_raw_buffer_load_b128(rkv, (unsigned)max(((i >> 1) * 64 + lane) * 16 - 8 * (i & 1), 0), kv_row(d, sel), LD_AUX);
  };
  ld_h(d0);
  {
    bool dead = false;
    ctr_wait<BG_SLP>(ctr(a, CT_A1 + ct_near(~sel) + (d0 >> 8)), 64u, (GAS unsigned*)(a.sync + SY_ABORT), lane, dead);
  }
  ld_u(d0);
  if (lane < 12) *(f32x4*)(Wb + (lane / 3) * A2_CSTR + (256 + lane % 3) * 16) = vzero();
#pragma unroll
  for (int i = 0; i < 4; ++i) if (64 * (i & 1) + lane < 80) *(f32x4*)(U + (i >> 1) * A2_USTR - 16 * 80 + (64 * (i & 1) + lane) * 16) = vzero();
  const int r = lane & 15, g = lane >> 4;
#pragma unroll 1
  for (int d = d0; d < d1; d += 96) {
    u64 at[6]; (void)at;
    AST(0);
#pragma unroll
    for (int i = 0; i < 4; ++i) {
      const unsigned src[6] = {lo[i][0], lo[i][1], hi[i][0], hi[i][1], hi[i][2], hi[i][3]};
      unsigned f[10];
#pragma unroll
      for (int x = 0; x < 10; ++x) {
        if ((x & 1) == 0) f[x] = __builtin_amdgcn_alignbit(src[5 - x / 2], src[5 - x / 2], 16);
        else f[x] = __builtin_amdgcn_perm(src[5 - (x + 1) / 2], src[5 - (x - 1) / 2], 0x07060100u);
      }
#pragma unroll
      for (int m = 0; m < 4; ++m) *(u32x4*)(Wb + m * A2_CSTR + (64 * i + lane) * 16) = (u32x4){f[3 - m], f[5 - m], f[7 - m], f[9 - m]};
    }
    if (lane == 0) uv[1][0] = (u32x4){0u, 0u, uv[1][0][0], uv[1][0][1]};
#pragma unroll
    for (int i = 0; i < 8; ++i) {
      const int ch = (i >> 1) * 64 + lane;
      *(u32x4*)(U + (i & 1) * A2_USTR + (ch >> 2) * 80 + (ch & 3) * 16) = uv[i & 1][i >> 1];
    }
    const float Dd = blo(dn);
    asm volatile("" :: "v"(Dd) : "memory");
    AST(1);
    ld_h(d + 96 < d1 ? d + 96 : D_A); ld_u(d + 96 < d1 ? d + 96 : D_A);
    f32x4 acc[4][2];
#pragma unroll
    for (int i = 0; i < 8; ++i) acc[i >> 1][i & 1] = vzero();
    const char* pa = Wb + (r & 3) * A2_CSTR + 16 * (255 - (r >> 2) + g) - 3 * 1024 - 64;
    const char* pb = U + (r - 1) * 80 + 16 * g;
    bf16x8 fa[2][4], fb[2][8];
    auto rd = [&](int b, int n, int k) {
      if (n == 0 || n > 8) fa[b][n ? n - 8 : 0] = *(const bf16x8*)(pa + 1024 * (3 - (n ? n - 8 : 0)) + 64 * (1 - k));
      else fb[b][n - 1] = *(const bf16x8*)(pb + ((n - 1) & 1) * A2_USTR + 80 * (16 * ((n - 1) >> 1) + 1 - k));
    };
#pragma unroll
    for (int n = 0; n < 12; ++n) rd(0, n, 0);
    AST(2);
#pragma unroll 1
    for (int k = 0; k < 16; k += 2) {
#pragma unroll
      for (int b = 0; b < 2; ++b) {
        int q = 0;
#pragma unroll
        for (int j = 0; j < 4; ++j)
#pragma unroll
          for (int i = j; i < 4; ++i)
#pragma unroll
            for (int mt = 0; mt < 2; ++mt) {
              asm volatile("v_mfma_f32_16x16x32_bf16 %0, %1, %2, %0" : "+v"(acc[i][mt]) : "v"(fa[b][j]), "v"(fb[b][2 * (i - j) + mt]));
              SB0;
              if (q < 12) rd(b ^ 1, q, 1);
              if (q == 12) { pa -= 64; pb -= 80; }
              ++q;
              SB0;
            }
      }
    }
    AST(3);
    u32x4 kv[4];
#pragma unroll
    for (int i = 0; i < 4; ++i) kv[i] = *(const u32x4*)(U + (16 * i + r) * 80 + 16 * g);
    asm volatile("s_nop 7\n\ts_waitcnt vmcnt(0)" ::: "memory");
#pragma unroll
    for (int i = 0; i < 8; ++i) asm volatile("" : "+v"(acc[i >> 1][i & 1]));
    if (lane == 0 && d > d0) ctr_add(a, CT_A2 + ct_near(~sel) + ((d - 96) >> 6), 1u);
#pragma unroll
    for (int i = 0; i < 4; ++i) {
      const f32x4 x = acc[i][1], y = acc[i][0];
      st16s((u32x4){pk2(fmaf(blo(kv[i][0]), Dd, x[0]), fmaf(bhi(kv[i][0]), Dd, x[1])), pk2(fmaf(blo(kv[i][1]), Dd, x[2]), fmaf(bhi(kv[i][1]), Dd, x[3])),
                    pk2(fmaf(blo(kv[i][2]), Dd, y[0]), fmaf(bhi(kv[i][2]), Dd, y[1])), pk2(fmaf(blo(kv[i][3]), Dd, y[2]), fmaf(bhi(kv[i][3]), Dd, y[3]))},
            rkv, (unsigned)(i * 1024 + r * 64 + g * 16), kv_row(d, sel));
    }
    AST(4);
  }
  wait_vm<0>();
  if (lane == 0) ctr_add(a, CT_A2 + ct_near(~sel) + ((d0 + (d1 - 1 - d0) / 96 * 96) >> 6), 1u);
}
__device__ __forceinline__ void task_u(const ChainArgs& a, int h, char* smem, int wv, Gate& G) {
  const int lane = opaque_tid(0);
  const int w = wv;
  GAS unsigned* abortw = (GAS unsigned*)(a.sync + SY_ABORT);
  bool dead = false;
  char* sb = smem + w * 32768;
  const __amdgpu_buffer_rsrc_t rwo = mk_rsrc(a.Wo, 8u << 20);
  const unsigned lds0 = (unsigned)(uintptr_t)(LAS char*)sb;
  unsigned wo4[4];
#pragma unroll
  for (int i = 0; i < 4; ++i) { const int row = i * 4 + (lane >> 4); wo4[i] = row * DM * 2 + (((lane & 15) ^ swz_b(row)) * 16); }
  asm volatile("s_nop 4");
  auto stage = [&](int n) {
    const int p = (U_ORD >> (4 * n)) & 3;
#pragma unroll
    for (int i = 0; i < 16; ++i)
      dma16(rwo, wo4[i & 3], (unsigned)(((128 * h + 64 * (p & 1) + 16 * (i >> 2)) * DM + 256 * w + 128 * (p >> 1)) * 2), lds0 + (n & 1) * 16384 + i * 1024, W_NT & 2);
  };
  __builtin_amdgcn_s_waitcnt(0x0F70);
  u32x4 W[16][4];
  stage(0); stage(1);
#pragma unroll
  for (int n = 0; n < 4; ++n) {
    const int p = (U_ORD >> (4 * n)) & 3;
    if (n < 3) wait_vm<16>(); else wait_vm<0>();
#pragma unroll
    for (int t = 0; t < 8; ++t) {
      const u32x8 f = tr_frag(sb + (n & 1) * 16384, t, lane);
      u32x4 f0 = {f[0], f[1], f[2], f[3]}, f1 = {f[4], f[5], f[6], f[7]};
      asm volatile("" : "+a"(f0), "+a"(f1));
      W[8 * (p >> 1) + t][2 * (p & 1)] = f0; W[8 * (p >> 1) + t][2 * (p & 1) + 1] = f1;
    }
    asm volatile("s_waitcnt lgkmcnt(0)" ::: "memory");
    if (n + 2 < 4) stage(n + 2);
  }
  lds_barrier();
  if (!G.pass(a.sync)) return;
  SSTAMP(192 + h);
  const Sel sel = G.sel;
  const u32x4 ru = own_rsrc(a.sync, SY_BYTES);
  u32x4 V[2][8], pc[2], o[2];
  f32x4 c[2][2];
  unsigned cv, cw, vo[4], lk[2], sgo = (w | lane) ? 0x7fffffffu : SY_CT + (CT_U + ct_near(sel) + h) * 128, nd = (lane & 1) ? 3 - URING : 2;
#pragma unroll
  for (int i = 0; i < 4; ++i) { vo[i] = lane * 16 + ux_tile(16 * w + 4 * i); asm volatile("" : "+v"(vo[i])); }
#pragma unroll
  for (int i = 0; i < 2; ++i) lk[i] = SY_CT + (ct_near(~sel) + ((lane & 1) ? CT_DONE + 8 * i + (h & 7) : CT_V + h)) * 128;
  char* vl = lds_base(smem, lane * 16);
  const unsigned vl2 = lane * 16 + w * 2048;
  auto ld_v = [&](int s, bool pad = false) {
    if (pad) asm volatile("s_nop 4\n\tbuffer_load_dwordx4 %0, %2, %3, %4 offen sc1\n\tbuffer_load_dwordx4 %1, %2, %3, %4 offen offset:1024 sc1" : "=&v"(pc[0]), "=&v"(pc[1]) : "v"(vl2), "s"(ru), "s"(x_slot(2, s, h, sel)) : "memory");
    else asm volatile("buffer_load_dwordx4 %0, %2, %3, %4 offen sc1\n\tbuffer_load_dwordx4 %1, %2, %3, %4 offen offset:1024 sc1" : "=&v"(pc[0]), "=&v"(pc[1]) : "v"(vl2), "s"(ru), "s"(x_slot(2, s, h, sel)) : "memory");
  };
  auto signal = [&](int s) {
    asm volatile("buffer_store_dword %0, %1, %2, 0 offen sc1" :: "v"(s), "v"(sgo), "s"(ru) : "memory");
  };
  auto st_u = [&](int n, unsigned ub) { asm volatile("buffer_store_dwordx4 %0, %1, %2, %3 offen offset:%4 sc1" :: "v"(o[n & 1]), "v"(vo[n >> 2]), "s"(ru), "s"(ub), "n"((n & 3) * 1024) : "memory"); };
  auto fetch = [&](int s) {
    for (unsigned spins = 0;;) {
      asm volatile("buffer_load_dword %0, %1, %2, 0 offen sc1" : "=&v"(cw) : "v"(lk[0]), "s"(ru) : "memory");
      ld_v(s, true);
      asm volatile("s_waitcnt vmcnt(0)" : "+v"(pc[0]), "+v"(pc[1]), "+v"(cw) :: "memory");
      if (s && spins == 0) { __builtin_amdgcn_s_barrier(); signal(0); }
      if ((int)__builtin_amdgcn_readfirstlane(cw) >= s) break;
      __builtin_amdgcn_s_sleep(2);
      if (poll_fail(spins, abortw, lane)) { dead = true; break; }
    }
    WST(s == 0, 24);
#pragma unroll
    for (int i = 0; i < 2; ++i) *(u32x4*)(vl + 16384 + w * 2048 + i * 1024) = pc[i];
    lds_barrier();
#pragma unroll
    for (int i = 0; i < 8; ++i) V[s][i] = *(const u32x4*)(vl + 16384 + i * 1024);
  };
#pragma unroll
  for (int i = 0; i < 8; ++i) asm volatile("" : "=v"(V[0][i]));
  u64 bad = 0;
  unsigned up = 0x7f000000u;
  c[1][0] = c[1][1] = vzero();
  auto seg = [&](int s, int b) {
    const unsigned ub = s < 0 ? 0x7f000000u : ux_base(s, h, sel);
    WST(s >= 0 && s < 4, 25 + s);
#pragma unroll
    for (int n = 0; n < 16; ++n)
#pragma unroll
      for (int q = 0; q < 8; ++q) {
        const int kp = q >> 1, m = q & 1;
        if (kp == 0) asm volatile("v_mfma_f32_16x16x32_bf16 %0, %1, %2, 0" : "=&v"(c[n & 1][m]) : "v"(V[b][4 * m + kp]), "a"(W[n][kp]));
        else asm volatile("v_mfma_f32_16x16x32_bf16 %0, %1, %2, %0" : "+v"(c[n & 1][m]) : "v"(V[b][4 * m + kp]), "a"(W[n][kp]));
        SB0;
        if (q >= 2 && q < 6) { const f32x4 x = c[~n & 1][(q - 2) >> 1]; o[~n & 1][q - 2] = pk2v(x[2 * (q & 1)], x[2 * (q & 1) + 1]); }
        if (q == 6) st_u((n - 1) & 15, n ? ub : up);
        if (q == 5) asm volatile("" :: "v"(o[n & 1]));
        if (n == U_LD && q == 0) {
          if (__builtin_expect(bad != 0, 0)) {
            if (s <= U_NOW) {
              asm volatile("s_waitcnt vmcnt(%1)" : "+v"(cw) : "n"(U_LD - 2) : "memory");
              __builtin_amdgcn_s_barrier();
              signal(s - 1);
              bad = (int)__builtin_amdgcn_readfirstlane(cw) <= s;
            }
            if (bad & 1) ctr_wait<2>(ctr(a, CT_V + ct_near(~sel) + h), min(s + 2, NSEG), abortw, lane, dead);
            if (bad & 2) {
              ctr_wait<U_SLP>(ctr(a, CT_DONE + ct_near(~sel) + 8 * b + (h & 7)), max(s + 3 - URING, 0), abortw, lane, dead);
              for (int i = 0; i < U_STG * (h & 7) + U_ST0; ++i) __builtin_amdgcn_s_sleep(1);
            }
          }
          ld_v(min(s + 1u, NSEG - 1u));
        }
        if (n == 1 && q == 7) asm volatile("buffer_load_dword %0, %1, %2, 0 offen sc1" : "=&v"(cw) : "v"(s <= U_NOW ? lk[0] : 0x7fffffffu), "s"(ru) : "memory");
        if (n == U_LD && q == 7) asm volatile("buffer_load_dword %0, %1, %2, 0 offen sc1" : "=&v"(cv) : "v"(lk[b ^ 1]), "s"(ru) : "memory");
        if (n == U_SIG2 && q == 0) asm volatile("s_waitcnt vmcnt(%4)" : "+v"(pc[0]), "+v"(pc[1]), "+v"(cv), "+v"(cw) : "n"(U_SIG2 - U_LD - 1) : "memory");
        if (n == U_SIG2 && (q == 1 || q == 7)) *(u32x4*)(vl + (b ^ 1) * 8192 + w * 2048 + (q >> 2) * 1024) = pc[q >> 2];
        if (n == U_SIG2 + 1 && q == 0) { bad = __builtin_amdgcn_ballot_w64(s >= 0 && (int)cv < (s < U_NOW && lane > 1 ? NSEG : min(s + (int)nd, NSEG - 1))); asm volatile("" : "+s"(bad)); }
        if (n == U_SIG2 + 1 && q == 7) lds_barrier();
        if (n == U_SIG2 + 2 && q == 0) signal(s - 1);
        if ((n == U_SIG2 + 2 || n == U_SIG2 + 3) && (q & 1)) V[b ^ 1][4 * (n - U_SIG2 - 2) + (q >> 1)] = *(const u32x4*)(vl + (b ^ 1) * 8192 + (4 * (n - U_SIG2 - 2) + (q >> 1)) * 1024);
        SB0;
      }
    up = ub;
  };
  for (int s = -U_DRY; s < NSEG; s += 2) {
    if (__builtin_expect(s == 0, 0)) fetch(0);
    seg(s, 0);
    if (__builtin_expect(s == 0, 0)) {
      o[1] = pk8v(c[1][0], c[1][1]);
      st_u(15, up);
      fetch(1);
    }
    seg(s + 1, 1);
  }
  o[1] = pk8v(c[1][0], c[1][1]);
  st_u(15, up);
  bg_drain();
  signal(NSEG - 1);
}

__device__ __forceinline__ void task_a1(const ChainArgs& a, int dg, int t0, char* T, Sel sel) {
  const int lane = opaque_tid(0);
  const int d = dg * 64 + 8 * (lane & 7);
  const int tb = t0 + 8 * (lane >> 3);
  u32x4 kk[2][11], vv[2][8], sh[4];
#pragma unroll
  for (int i = 0; i < 4; ++i) sh[i] = *(const u32x4*)(a.Ashift + d * 4 + 8 * i);
  SB0;
#pragma unroll
  for (int gq = 0; gq < 2; ++gq) {
#pragma unroll
    for (int i = 0; i < 11; ++i) {
      const int t = tb + 64 * gq - 3 + i;
      kk[gq][i] = LDNT((const u32x4*)(a.Ak + (long)(t < 0 ? 0 : t) * D_A + d));
    }
#pragma unroll
    for (int i = 0; i < 8; ++i) vv[gq][i] = LDNT((const u32x4*)(a.Av + (long)(tb + 64 * gq + i) * D_A + d));
    SB0;
  }
#pragma unroll
  for (int i = 0; i < 3; ++i) if (tb == 0) kk[0][i] = (u32x4){0u, 0u, 0u, 0u};
#pragma unroll
  for (int gq = 0; gq < 2; ++gq) {
#pragma unroll
    for (int c = 0; c < 8; ++c) {
      const unsigned t01 = sh[c >> 1][2 * (c & 1)], t23 = sh[c >> 1][2 * (c & 1) + 1];
      const float s0 = blo(t01), s1 = bhi(t01), s2 = blo(t23), s3 = bhi(t23);
      float kf[11];
#pragma unroll
      for (int i = 0; i < 11; ++i) kf[i] = (c & 1) ? bhi(kk[gq][i][c >> 1]) : blo(kk[gq][i][c >> 1]);
      u32x4 o;
#pragma unroll
      for (int i = 0; i < 8; i += 2) {
        float r[2];
#pragma unroll
        for (int j = 0; j < 2; ++j)
          r[j] = fmaf(kf[i + j + 3], s0, fmaf(kf[i + j + 2], s1, fmaf(kf[i + j + 1], s2, kf[i + j] * s3))) * ((c & 1) ? bhi(vv[gq][i + j][c >> 1]) : blo(vv[gq][i + j][c >> 1]));
        o[i >> 1] = pk2(r[0], r[1]);
      }
      *(u32x4*)(T + (8 * (lane & 7) + c) * 256 + (((gq * 8 + (lane >> 3)) ^ (lane & 7)) * 16)) = o;
    }
    SB0;
  }
  const __amdgpu_buffer_rsrc_t rkv = rsrc_u(a.sync);
#pragma unroll
  for (int i = 0; i < 16; ++i) {
    const int row = 4 * i + (lane >> 4);
    const u32x4 o = *(const u32x4*)(T + row * 256 + (((lane & 15) ^ (row >> 3)) * 16));
    st16(rkv, kv_row(dg * 64, sel) + (row >> 1) * CHK + (row & 1) * 4096 + (t0 + (lane & 15) * 8) * 2, o);
  }
  wait_vm<0>();
  if (lane == 0) ctr_add(a, CT_A1 + ct_near(~sel) + (dg >> 2), 1u);
}

__device__ __forceinline__ void task_a3(const ChainArgs& a, int dg, int t0, char* T, Sel sel) {
  const int lane = opaque_tid(0);
  const int d = dg * 64 + 8 * (lane & 7);
  const int tb = t0 + 8 * (lane >> 3);
  const __amdgpu_buffer_rsrc_t rz = rsrc_u(a.sync);
  u32x4 qq[16], zz[8][2];
#pragma unroll
  for (int i = 0; i < 16; ++i) qq[i] = LDNT((const u32x4*)(a.Aq + (long)(tb + 64 * (i >> 3) + (i & 7)) * D_A + d));
  {
    u32x4 zr[16];
    bool dead = false;
    ctr_wait<A3_SLP>(ctr(a, CT_A2 + ct_near(~sel) + dg), 64u, (GAS unsigned*)(a.sync + SY_ABORT), lane, dead);
  #pragma unroll
    for (int i = 0; i < 16; ++i) zr[i] = ld16(rz, kv_row(dg * 64, sel) + (2 * i + (lane >> 5)) * CHK + ((lane >> 4) & 1) * 4096 + (t0 + (lane & 15) * 8) * 2);
#pragma unroll
    for (int i = 0; i < 16; ++i) {
      const int row = 4 * i + (lane >> 4);
      *(u32x4*)(T + row * 256 + (((lane & 15) ^ (row >> 3)) * 16)) = zr[i];
    }
  }
#pragma unroll
  for (int c = 0; c < 8; ++c)
#pragma unroll
    for (int gq = 0; gq < 2; ++gq) zz[c][gq] = *(const u32x4*)(T + (8 * (lane & 7) + c) * 256 + (((gq * 8 + (lane >> 3)) ^ (lane & 7)) * 16));
#pragma unroll
  for (int i = 0; i < 16; ++i) {
    u32x4 o;
#pragma unroll
    for (int c = 0; c < 8; c += 2) {
      const unsigned z0 = zz[c][i >> 3][(i >> 1) & 3], z1 = zz[c + 1][i >> 3][(i >> 1) & 3];
      o[c >> 1] = pk2(blo(qq[i][c >> 1]) * ((i & 1) ? bhi(z0) : blo(z0)), bhi(qq[i][c >> 1]) * ((i & 1) ? bhi(z1) : blo(z1)));
    }
    st16g(a.out + (long)(tb + 64 * (i >> 3) + (i & 7)) * OUTW + d, o);
  }
}

__device__ __forceinline__ void part_a(const ChainArgs& a, char* smem, int x, int wi, int w, Sel sel) {
  char* T = smem + 1024 + w * A2_WLDS;
  if (wi < 64) task_a1(a, 4 * x + (wi & 3), (wi >> 2) * 128, T, sel);
  a2_wave(a, 256 * x + wi, 256 * x + 256, smem, w, sel);
  while (true) {
    unsigned t = 0;
    if (opaque_tid(0) == 0) t = __hip_atomic_fetch_add(ctr(a, CT_TK + ct_near(~sel) + x), 1u, __ATOMIC_RELAXED, __HIP_MEMORY_SCOPE_AGENT) + 1u;
    t = __builtin_amdgcn_readfirstlane(t);
    if (t >= 64) break;
    task_a3(a, 4 * x + (t >> 4), (t & 15) * 128, T, sel);
  }
}

__global__ __launch_bounds__(256, 1) void chain_kernel(ChainArgs a) {
  extern __shared__ __attribute__((aligned(16))) char smem[];
  const int b = blockIdx.x;
  {
    u32x4 rc;
    asm volatile("s_getpc_b64 s[60:61]\n\t.Lchain_here:\n\ts_sub_u32 s60, s60, .Lchain_here-_Z12chain_kernel9ChainArgs\n\ts_subb_u32 s61, s61, 0\n\ts_and_b32 s61, s61, 0xffff\n\t"
                 "s_mov_b32 s62, .Lchain_end-_Z12chain_kernel9ChainArgs\n\ts_mov_b32 s63, 0x20000\n\t"
                 "buffer_load_dwordx4 a[252:255], %1, s[60:63], 0 offen\n\tbuffer_load_dwordx4 a[252:255], %1, s[60:63], s63 offen"
                 : "={s[60:63]}"(rc) : "v"((b >> 3) * 4096 + threadIdx.x * 16) : "scc", "a252", "a253", "a254", "a255");
  }
  int w = __builtin_amdgcn_readfirstlane(threadIdx.x >> 6);
  Gate G;
  G.entry = __builtin_amdgcn_s_memrealtime(); G.open = false; G.arr = w == 0 ? b & 7 : -1;
  G.rot = ((unsigned)__builtin_amdgcn_s_getreg(20 | (3 << 11)) - b + WRONG) & 7u;
  {
    volatile LAS unsigned* t = (volatile LAS unsigned*)smem;
    if (w == 0) { t[0] = (unsigned)G.entry; t[1] = (unsigned)(G.entry >> 32); }
    __syncthreads();
    G.entry = (u64)(unsigned)__builtin_amdgcn_readfirstlane(t[0]) | ((u64)(unsigned)__builtin_amdgcn_readfirstlane(t[1]) << 32);
    __syncthreads();
  }
  if (((b + G.rot) & 4) == 0 && b < 128) {
    const int c = (b >> 6) * 32 + 8 * ((b + G.rot) & 3) + ((b >> 3) & 7);
    if (w < 2) calibrate(a, w ? 32 + (0x3200 >> (4 * (c & 3)) & 3) : c, 2 * c + w, G.entry);
    __builtin_amdgcn_s_barrier();
  }
  if (b == 0) {
    const int lane = opaque_tid(0);
    const __amdgpu_buffer_rsrc_t rs = mk_rsrc(a.sync + SY_SET, 3 * CHK);
    st16(rs, CHK + w * 8192 + lane * 128, (u32x4){SENT, SENT, SENT, SENT});
    if (w == 0 && lane >= 2) st16(rs, lane * 128, (u32x4){SENT, SENT, SENT, SENT});
    if (w == 3) {
      u32x4 v;
      bool ok;
      do {
        v = ld16(mk_rsrc(a.sync + SY_CT + CT_CAL * 128, 8192), lane * 128);
        ok = ((u64)v[1] << 32 | v[0]) - G.entry + 2000u < (1ull << 31) && ((u64)v[3] << 32 | v[2]) - G.entry + 2000u < (1ull << 31);
      } while (!__all(ok) && __builtin_amdgcn_s_memrealtime() - G.entry < 2 * CALM);
      const u64 y = __builtin_amdgcn_ballot_w64(ok && (v[2] & 1u));
      if (lane == 32 || lane == 34 || lane == 35) v[0] = __builtin_popcountll(y & (lane == 32 ? 0x3333333333333333ull : 0x1111111111111111ull << (lane & 3))) > (lane == 32 ? 16 : 8);
      const u64 m = __builtin_amdgcn_ballot_w64(ok && (v[0] & 1u));
      if (lane == 0) __hip_atomic_store((GAS u64*)(a.sync + SY_SEL), m, __ATOMIC_RELAXED, __HIP_MEMORY_SCOPE_AGENT);
    }
    bg_drain();
    if (w == 0 && lane == 0) __hip_atomic_store((GAS u64*)(a.sync + SY_EPOCH), (__builtin_amdgcn_s_memrealtime() & ~7ull) | G.rot, __ATOMIC_RELAXED, __HIP_MEMORY_SCOPE_AGENT);
  }
  while (true) {
    const unsigned rot = G.rot;
    asm volatile("" : "+s"(w));
    const int x = (b + rot) & 7, j = b >> 3, h = 8 * (x & 3) + (j & 7), role = (x & 4) + (j >> 3);
    if ((role == 0 || role == 7) && w >= 2) {
      const int n = opaque_tid(w - 2);
      const unsigned x0 = __builtin_amdgcn_raw_buffer_load_b32(mk_rsrc(a.sync, 106u << 20), n * PAGE + 4096 + (j & 31) * 128, 0, 2);
      const unsigned x1 = __builtin_amdgcn_raw_buffer_load_b32(mk_rsrc(a.out, 8u << 20), n * PAGE + (j & 31) * 128, 0, 2);
      asm volatile("" :: "v"(x0), "v"(x1));
    }
    if (role < 2) role_cb(a, 32 * role + h, smem, w, G);
    else if (role == 2) role_qk(a, h, smem, w, G);
    else if (role == 3) role_vo(a, h, smem, w, G);
    else if (role == 4) task_u(a, h, smem, w, G);
    else task_w(a, 7 - role, h, smem, w, G);
    if (G.rot == rot && role > 4) part_a(a, smem, x & 3, 4 * (j - 8) + w, w, G.sel);
    if (G.rot == rot) break;
    __syncthreads();
  }
  asm volatile(".Lchain_end:");
}

extern "C" void kernel_launch(void* const* d_in, const int* in_sizes, int n_in,
                              void* d_out, int out_size, void* d_ws, size_t ws_size,
                              hipStream_t stream) {
  const u16* A_q = (const u16*)d_in[0];
  const u16* A_k = (const u16*)d_in[1];
  const u16* A_v = (const u16*)d_in[2];
  const u16* A_shift = (const u16*)d_in[3];
  const u16* A_ssmk = (const u16*)d_in[4];
  const u16* A_D = (const u16*)d_in[5];
  const u16* B_x = (const u16*)d_in[6];
  const u16* B_mem0 = (const u16*)d_in[7];
  const u16* B_Wq = (const u16*)d_in[8];
  const u16* B_Wk = (const u16*)d_in[9];
  const u16* B_Wv = (const u16*)d_in[10];
  const u16* B_Wo = (const u16*)d_in[11];
  u16* out = (u16*)d_out;
  char* ws = (char*)(((uintptr_t)d_ws + (2u << 20) - 1) & ~(uintptr_t)((2u << 20) - 1)) + WSOFF * MB;
  hipFuncSetAttribute((const void*)chain_kernel, hipFuncAttributeMaxDynamicSharedMemorySize, CHAIN_LDS);
  ChainArgs ca;
  ca.prof = (u64*)((char*)d_ws + PROF_AT);
  ca.mem0 = B_mem0; ca.Wq = B_Wq; ca.Wk = B_Wk; ca.Wv = B_Wv; ca.Wo = B_Wo; ca.X = B_x;
  ca.out = out; ca.sync = ws;
  ca.Aq = A_q; ca.Ak = A_k; ca.Av = A_v; ca.Ashift = A_shift; ca.Assm = A_ssmk; ca.AD = A_D;
  chain_kernel<<<256, 256, CHAIN_LDS, stream>>>(ca);
}
```

```cpp
#include <hip/hip_runtime.h>
#include <hip/hip_bf16.h>
#include <stdint.h>
#include <type_traits>

constexpr int S_LEN = 2048;
constexpr int D_A   = 1024;
constexpr int DM    = 1024;
constexpr int NH    = 32;
constexpr int DH    = 128;
constexpr int HD    = NH * DH;
constexpr int MEMR  = 8;
constexpr int SEG   = 32;
constexpr int NSEG  = S_LEN / SEG;
constexpr int OUTW  = 2048;

typedef unsigned short u16;
typedef unsigned long long u64;
typedef short bf16x8 __attribute__((ext_vector_type(8)));
typedef short bf16x4 __attribute__((ext_vector_type(4)));
typedef float f32x4 __attribute__((ext_vector_type(4)));
typedef float f32x16 __attribute__((ext_vector_type(16)));
typedef unsigned u32x4 __attribute__((ext_vector_type(4)));
typedef unsigned u32x2 __attribute__((ext_vector_type(2)));
#define LAS __attribute__((address_space(3)))
#define GAS __attribute__((address_space(1)))

constexpr size_t MB = 1u << 20;
constexpr size_t PROF_AT = 0;

__device__ __forceinline__ float bf2f(u16 b) { return __uint_as_float(((unsigned)b) << 16); }
__device__ __forceinline__ u16 f2bf(float f) { __bf16 b = (__bf16)f; return __builtin_bit_cast(u16, b); }
__device__ __forceinline__ float rbf(float f) { return bf2f(f2bf(f)); }
__device__ __forceinline__ unsigned pk2(float a, float b) {
  typedef float f2 __attribute__((ext_vector_type(2)));
  typedef __bf16 b2 __attribute__((ext_vector_type(2)));
  f2 v = {a, b}; b2 r = __builtin_convertvector(v, b2); return __builtin_bit_cast(unsigned, r);
}

__device__ __forceinline__ int opaque_tid(int wv) {
  int l;
  asm volatile("v_mbcnt_lo_u32_b32 %0, -1, 0\n\tv_mbcnt_hi_u32_b32 %0, -1, %0" : "=v"(l));
  return wv * 64 + l;
}
__device__ __forceinline__ int swz_b(int row) { return ((row & 3) << 2) | ((row >> 2) & 3); }

#define KNOB(name, v) constexpr int name = v
KNOB(K_DEF, 70);
KNOB(W_STEP, 33); KNOB(W_OFF, 56);
#define RHOV 3
KNOB(RHO, RHOV);
#define CDUR 1600
#define U_SLP 8
#define U_STG 0
#define U_ST0 0
#define WSOFF 122
#define START_SLP 3
#define QK_D 0
#define U_DRY 0
#define A1_0 20
#define A1_D 4
#define PSEG 1
#define CALN 3
#define CALM 500u
#define CALT 680u
#define KQ_D 500
#define W_NOW 4
#define U_SIG2 11
#define U_LD 5
#define U_NOW 6
#define WRONG 0
#define OUTP "nt"
#define LDNT(p) __builtin_nontemporal_load(p)
constexpr int CHK      = 16384;
constexpr int PAGE     = 2 << 20;
constexpr int SY_UM    = 34 * PAGE;
constexpr int SY_HOT   = 32 * PAGE;
constexpr int H_MEM    = 0;
constexpr int H_P      = 16 * CHK;
constexpr int H_ANN    = 28 * CHK;
constexpr int H_DLT    = 32 * CHK;
constexpr int H_TR     = 33 * CHK;
constexpr int H_PX     = 64 * CHK;
constexpr int H_BYTES  = 128 * CHK;
constexpr int URING    = 16;
constexpr int SY_SET   = 52 * PAGE;
constexpr int SY_EPOCH = SY_SET + 128;
constexpr int SY_ABORT = SY_SET + 256;
constexpr int SY_SEL   = SY_EPOCH + 8;
constexpr int SY_CT    = SY_SET + CHK;
constexpr int CT_Q = 0, CT_K = 32, CT_U = 128;
constexpr int CT_V = 0, CT_DONE = 32, CT_A2 = 48, CT_A1 = 128, CT_TK = 132;
constexpr int CT_ARR = -124, CT_INIT = -116, CT_CAL = 256;
constexpr int SY_KV    = 48 * PAGE;
constexpr int SY_BYTES = SY_CT + (CT_CAL + 64) * 128;
constexpr unsigned SENT = 0xFFFFFFFFu;
constexpr unsigned SPIN_CAP = 1u << 19;
constexpr int NLF = 16;
constexpr int L_WTS = 0;
constexpr int L_MEM = L_WTS + 4 * NLF * 2048;
constexpr int L_XCH = L_MEM + 16384;
constexpr int L_XK  = L_XCH + 2048;
constexpr int L_KX  = L_XCH + 4096;
constexpr int L_PRF = L_KX + 8192;
constexpr int XRS = 40;
constexpr int CHAIN_LDS = 160 * 1024;
static_assert(L_XCH + 128 * XRS <= L_PRF && L_PRF + 4096 <= CHAIN_LDS, "lds");

typedef __bf16 bfv8 __attribute__((ext_vector_type(8)));
typedef __bf16 bfv16 __attribute__((ext_vector_type(16)));
typedef unsigned u32x8 __attribute__((ext_vector_type(8)));
typedef unsigned u32x12 __attribute__((ext_vector_type(12)));
typedef unsigned u32x16 __attribute__((ext_vector_type(16)));
typedef unsigned u32x32 __attribute__((ext_vector_type(32)));
struct Wts { u32x8 a[32]; u32x32 v[4]; };

#define STAMPL(i) do {} while (0)
#define PVAL(i, v) do {} while (0)
#define PDECL(base, str) do {} while (0)
#define PZERO do {} while (0)
#define PDUMP(mask) do {} while (0)
#define TA(x) do {} while (0)
#define TB(x) TS(x)
#define TS(x) do {} while (0)
#define SSTAMP(i) do {} while (0)
#define WST(c, i) do {} while (0)
#define AST(i) do {} while (0)
struct ChainArgs {
  u64* prof;
  const u16* mem0; const u16* Wq; const u16* Wk; const u16* Wv; const u16* Wo; const u16* X;
  u16* out; char* sync;
  const u16* Aq; const u16* Ak; const u16* Av; const u16* Ashift; const u16* Assm; const u16* AD;
};
__device__ __forceinline__ GAS unsigned* ctr(const ChainArgs& a, int i) { return (GAS unsigned*)(a.sync + SY_CT + i * 128); }
__device__ __forceinline__ void ctr_add(const ChainArgs& a, int i, unsigned v) {
  __hip_atomic_fetch_add(ctr(a, i), v, __ATOMIC_RELAXED, __HIP_MEMORY_SCOPE_AGENT);
}

__device__ __forceinline__ __amdgpu_buffer_rsrc_t mk_rsrc(const void* p, unsigned bytes) {
  const u64 v = (u64)p;
  const unsigned lo = (unsigned)__builtin_amdgcn_readfirstlane((unsigned)v), hi = (unsigned)__builtin_amdgcn_readfirstlane((unsigned)(v >> 32));
  const u64 u = (u64)lo | ((u64)hi << 32);
  return __builtin_amdgcn_make_buffer_rsrc((void*)u, 0, bytes, 0x00020000);
}
__device__ __forceinline__ u32x4 own_rsrc(const void* p, unsigned bytes) {
  u32x4 r = {(unsigned)__builtin_amdgcn_readfirstlane((unsigned)(u64)p), (unsigned)__builtin_amdgcn_readfirstlane((unsigned)((u64)p >> 32)) & 0xffffu, bytes, 0x00020000u};
  asm volatile("s_nop 4" : "+s"(r));
  return r;
}
constexpr int LD_AUX = 16, ST_AUX = 16;
__device__ __forceinline__ u32x4 ld16(__amdgpu_buffer_rsrc_t rs, unsigned off) {
  return __builtin_amdgcn_raw_buffer_load_b128(rs, off, 0, LD_AUX);
}
__device__ __forceinline__ void st16(__amdgpu_buffer_rsrc_t rs, unsigned off, u32x4 v) {
  __builtin_amdgcn_raw_buffer_store_b128(v, rs, off, 0, ST_AUX);
}
__device__ __forceinline__ void st16s(u32x4 v, __amdgpu_buffer_rsrc_t rs, unsigned voff, unsigned soff) {
  __builtin_amdgcn_raw_buffer_store_b128(v, rs, voff, soff, ST_AUX);
  asm volatile("s_nop 1" :: "v"(v) : "memory");
}
__device__ __forceinline__ void st8(__amdgpu_buffer_rsrc_t rs, unsigned off, u32x2 v) {
  __builtin_amdgcn_raw_buffer_store_b64(v, rs, off, 0, ST_AUX);
}
__device__ __forceinline__ void st4(__amdgpu_buffer_rsrc_t rs, unsigned off, unsigned v) {
  __builtin_amdgcn_raw_buffer_store_b32(v, rs, off, 0, ST_AUX);
}
__device__ __forceinline__ unsigned ld4(__amdgpu_buffer_rsrc_t rs, unsigned off) {
  return __builtin_amdgcn_raw_buffer_load_b32(rs, off, 0, LD_AUX);
}
constexpr int TU = 16;
__device__ __forceinline__ unsigned cyc32() { return (unsigned)__builtin_amdgcn_s_memtime(); }
__device__ __forceinline__ unsigned to_sgpr(unsigned x) {
  unsigned r;
  asm volatile("s_nop 0\n\tv_readfirstlane_b32 %0, %1\n\ts_nop 4" : "=s"(r) : "v"(x));
  return r;
}
struct Clk {
  unsigned ce, te;
  unsigned c2t, t2c;
  __device__ __forceinline__ void edge(unsigned& c, unsigned& t) {
    u64 c0, r0, r1, r2, r3, r4, r5;
    asm volatile("s_memtime %0\n\ts_memrealtime %1\n\ts_nop 4\n\ts_memrealtime %2\n\ts_nop 4\n\ts_memrealtime %3\n\ts_nop 4\n\t"
                 "s_memrealtime %4\n\ts_nop 4\n\ts_memrealtime %5\n\ts_nop 4\n\ts_memrealtime %6\n\ts_waitcnt lgkmcnt(0)"
                 : "=&s"(c0), "=&s"(r0), "=&s"(r1), "=&s"(r2), "=&s"(r3), "=&s"(r4), "=&s"(r5) :: "memory");
    const unsigned x = (unsigned)r0;
    const unsigned n = ((unsigned)r1 == x) + ((unsigned)r2 == x) + ((unsigned)r3 == x) + ((unsigned)r4 == x) + ((unsigned)r5 == x);
    c = to_sgpr((unsigned)c0 + 24u * n); t = (x + 4u) * TU;
  }
  __device__ __forceinline__ void init() { edge(ce, te); c2t = 44000u; t2c = 97600u; }
  __device__ __forceinline__ void sync() {
    unsigned c, t;
    edge(c, t);
    set(c, t);
  }
  __device__ __forceinline__ void set(unsigned c, unsigned t) {
    if (c == ce) return;
    const float q = (float)(t - te) * __builtin_amdgcn_rcpf((float)(c - ce));
    const float qc = fminf(fmaxf(q, 0.51f), 1.99f);
    c2t = to_sgpr((unsigned)(qc * 65536.f));
    t2c = to_sgpr((unsigned)(__builtin_amdgcn_rcpf(qc) * 65536.f));
    ce = c; te = t;
  }
  __device__ __forceinline__ unsigned time(unsigned c) const { return te + ((min(c - ce, 30000u) * c2t) >> 16); }
  __device__ __forceinline__ unsigned now() const { return time(cyc32()); }
  __device__ __forceinline__ unsigned target(unsigned t) const {
    return to_sgpr(ce + (((unsigned)min(max((int)(t - te), 0), 20000) * t2c) >> 16));
  }
  __device__ __forceinline__ void wait(unsigned t) const { wait_c(target(t)); }
  __device__ __forceinline__ void wait_c(unsigned c) const {
    unsigned y;
    asm volatile("1:\n\ts_memtime vcc\n\ts_waitcnt lgkmcnt(0)\n\ts_sub_i32 %0, %1, vcc_lo\n\ts_cmp_gt_i32 %0, 128\n\ts_cbranch_scc1 1b\n\t"
                 "2:\n\ts_sub_i32 %0, %0, %2\n\ts_cmp_gt_i32 %0, %3\n\ts_cbranch_scc1 2b"
                 : "=&s"(y) : "s"(c), "n"(W_STEP), "n"(W_OFF) : "memory", "scc", "vcc");
  }
};
__device__ __forceinline__ unsigned nosent(unsigned d) { return min(d, SENT - 1u); }
__device__ __forceinline__ int arow(int row, int half) { return 4 * (row >> 1) + 2 * half + (row & 1); }
__device__ __forceinline__ unsigned umax4(u32x4 v) { return max(max(v[0], v[1]), max(v[2], v[3])); }
__device__ __forceinline__ void stamp_again(GAS u64* p) {
  __hip_atomic_store(p, (__builtin_amdgcn_s_memrealtime() & ~7ull) | (__hip_atomic_load(p, __ATOMIC_RELAXED, __HIP_MEMORY_SCOPE_AGENT) & 7ull),
                     __ATOMIC_RELAXED, __HIP_MEMORY_SCOPE_AGENT);
}
__device__ __forceinline__ bool poll_fail(unsigned& spins, GAS unsigned* abortw, int lane) {
  ++spins;
  if (spins > SPIN_CAP) {
    if (lane == 0) __hip_atomic_store(abortw, 1u, __ATOMIC_RELAXED, __HIP_MEMORY_SCOPE_AGENT);
    return true;
  }
  if ((spins & 511u) == 0u && lane == 0) {
    unsigned n = 0;
    for (int i = 0; i < 8; ++i) n += __hip_atomic_load(abortw + (SY_CT - SY_ABORT + (CT_ARR + i) * 128) / 4, __ATOMIC_RELAXED, __HIP_MEMORY_SCOPE_AGENT) + 1u;
    if (n < 256u) stamp_again((GAS u64*)(abortw - 32));
  }
  if ((spins & 63u) == 0u)
    return __builtin_amdgcn_readfirstlane(__hip_atomic_load(abortw, __ATOMIC_RELAXED, __HIP_MEMORY_SCOPE_AGENT)) != SENT;
  return false;
}
struct Gate {
  u64 entry, sel; int arr; unsigned rot; bool open;
  __device__ __forceinline__ bool look(char* sync, u32x4 v) {
    if (open) return true;
    for (unsigned spins = 1;; ++spins) {
#pragma unroll
      for (int i = 0; i < 4; ++i) v[i] = __builtin_amdgcn_readfirstlane(v[i]);
      if (((u64)v[1] << 32 | v[0]) - entry + 2000u < (1ull << 31) || spins > 4 * SPIN_CAP) break;
      if (spins > 1) __builtin_amdgcn_s_sleep(8);
      v = ld16(mk_rsrc(sync + SY_EPOCH, 16), 0);
    }
    asm volatile("" ::: "memory");
    sel = (u64)v[3] << 32 | v[2];
    const bool same = rot == (v[0] & 7u);
    rot = v[0] & 7u; open = true;
    return same;
  }
  __device__ __forceinline__ void ask(char* sync, u32x4& v) { asm volatile("s_nop 4\n\tbuffer_load_dwordx4 %0, off, %1, 0 sc1" : "=v"(v) : "s"(own_rsrc(sync + SY_EPOCH, 16)) : "memory"); }
  __device__ __forceinline__ void arrive(char* sync) {
    if (arr >= 0 && opaque_tid(0) == 0) __hip_atomic_fetch_add((GAS unsigned*)(sync + SY_CT + (CT_ARR + arr) * 128), 1u, __ATOMIC_RELAXED, __HIP_MEMORY_SCOPE_AGENT);
    arr = -1;
  }
  __device__ __forceinline__ bool pass(char* sync, u32x4 v = {0u, 0u, 0u, 0u}) { const bool r = look(sync, v); arrive(sync); return r; }
};
__device__ __forceinline__ unsigned ann_val(unsigned t, int s) { return (t << 8) | (unsigned)s; }
__device__ __forceinline__ unsigned tr_place(int s) { return (s & 2) * (CHK / 2) + (s & 1) * 4096; }
__device__ __forceinline__ unsigned die_half() { return ((unsigned)__builtin_amdgcn_s_getreg(20 | (3 << 11)) >> 2) & 1u; }
typedef u64 Sel;
template <class F>
__device__ __forceinline__ void start_wait(char* sync, GAS unsigned* abortw, int lane, bool& dead, int ci, unsigned n, F f) {
  unsigned spins = 0, v;
  const bool m = lane < 4 || ci < 0;
  while (true) {
    v = ld4(mk_rsrc(sync + SY_SET, SY_BYTES - SY_SET), lane < 6 ? CHK + (m ? CT_INIT + (lane & 3) : ci + 32 * (lane & 1)) * 128 : 0x7fffffffu) + 1u;
    asm volatile("" ::: "memory");
    f();
    if (__all(lane > 5 || v >= (m ? 16u : n))) break;
    __builtin_amdgcn_s_sleep(START_SLP);
    if (dead || poll_fail(spins, abortw, lane)) { dead = true; break; }
  }
}
__device__ __forceinline__ __amdgpu_buffer_rsrc_t rsrc_u(char* sync) { return mk_rsrc((void*)sync, SY_SET); }
__device__ __forceinline__ unsigned near_half(unsigned off, u64 sel) { return off + ((unsigned)(sel >> ((off >> 21) & 63)) & 1u) * 8192; }
__device__ __forceinline__ unsigned x_slot(int kind, int s, int h, Sel sel) {
  return kind == 2 ? near_half(32 * PAGE + (s * 32 + h) * CHK, ~sel) : near_half((s * 64 + 2 * h + 1 - kind) * CHK, sel);
}
__device__ __forceinline__ int ct_near(Sel sel) { return 64 * ((unsigned)(sel >> (SY_SET / PAGE)) & 1u); }
__device__ __forceinline__ unsigned kv_row(int d, Sel sel) { return near_half(SY_KV + (d >> 1) * CHK, ~sel) + (d & 1) * 4096; }
__device__ __forceinline__ int hpos(int h) { return (h & 3) * 8 + (h >> 2); }
constexpr unsigned UXH = 8 * CHK;
__device__ __forceinline__ unsigned ux_base(int s, int h, Sel sel) { return near_half(((s & (URING - 1)) * 32 + h) * UXH, ~sel); }
__device__ __forceinline__ constexpr unsigned ux_tile(int t) { return (t >> 3) * CHK + (t & 7) * 1024; }
__device__ __forceinline__ __amdgpu_buffer_rsrc_t rsrc_hot(char* sync, Sel sel) {
  return mk_rsrc((void*)(sync + near_half(SY_HOT, sel)), H_BYTES - 8192);
}
template <int SLP = 4>
__device__ __forceinline__ void ctr_wait(GAS unsigned* c, unsigned need, GAS unsigned* abortw, int lane, bool& dead) {
  unsigned spins = 0;
  while (true) {
    unsigned v = __builtin_amdgcn_readfirstlane(__hip_atomic_load(c, __ATOMIC_RELAXED, __HIP_MEMORY_SCOPE_AGENT)) + 1u;
    if (v >= need) break;
    __builtin_amdgcn_s_sleep(SLP);
    if (dead || poll_fail(spins, abortw, lane)) { dead = true; break; }
  }
}
template <int N> __device__ __forceinline__ void wait_vm() { asm volatile("s_waitcnt vmcnt(%0)" :: "n"(N) : "memory"); }
__device__ __forceinline__ void lds_barrier() {
  asm volatile("s_waitcnt lgkmcnt(0)" ::: "memory");
  __builtin_amdgcn_s_barrier();
  asm volatile("" ::: "memory");
}
__device__ __forceinline__ f32x4 vzero() { float z; asm volatile("v_mov_b32 %0, 0" : "=v"(z)); f32x4 r = {z, z, z, z}; asm volatile("" : "+v"(r)); return r; }
__device__ __forceinline__ f32x4 smf(u32x4 a, u32x8 b, f32x4 c, int idx) {
  return __builtin_amdgcn_smfmac_f32_16x16x64_bf16(__builtin_bit_cast(bfv8, a), __builtin_bit_cast(bfv16, b), c, idx, 0, 0);
}
__device__ __forceinline__ float red16max(float x) { auto r = __builtin_amdgcn_permlane16_swap(__float_as_uint(x), __float_as_uint(x), false, false); return fmaxf(__uint_as_float(r[0]), __uint_as_float(r[1])); }
__device__ __forceinline__ float red32max(float x) { auto r = __builtin_amdgcn_permlane32_swap(__float_as_uint(x), __float_as_uint(x), false, false); return fmaxf(__uint_as_float(r[0]), __uint_as_float(r[1])); }
__device__ __forceinline__ float red16add(float x) { auto r = __builtin_amdgcn_permlane16_swap(__float_as_uint(x), __float_as_uint(x), false, false); return __uint_as_float(r[0]) + __uint_as_float(r[1]); }
__device__ __forceinline__ float red32add(float x) { auto r = __builtin_amdgcn_permlane32_swap(__float_as_uint(x), __float_as_uint(x), false, false); return __uint_as_float(r[0]) + __uint_as_float(r[1]); }
constexpr bool blk_in_lds(int R, int b) {
  return R == 0 ? ((b & 7) == 1 || (b & 7) == 4) : (b < 16 ? (b & 3) == 0 : (b & 3) == 3);
}
constexpr int lds_index(int R, int b) { int n = 0; for (int i = 0; i < b; ++i) if (blk_in_lds(R, i)) ++n; return n; }
constexpr bool frag_in_lds(int R, int f) { return blk_in_lds(R, f >> 1); }
constexpr int frag_reg_slot(int R, int f) { int n = 0; for (int i = 0; i < f; ++i) if (!frag_in_lds(R, i)) ++n; return n; }

constexpr int m_block(int R, int m, int i) {
  return R == 0 ? 2 * (i < 12 ? (m ? i + i / 3 + 1 : i + (i + 1) / 3) : 4 * (i - 12) + 2 - 2 * m) + m : m ? 16 + 4 * (i & 3) + (i >> 2) : (i < 12 ? i + i / 3 + 1 : 4 * (i - 12));
}
#define QK_REV 0
#define W_BV 0xba9876543210fedcull
#define VO_ORD 3
constexpr int w_block(int R, int i) {
  if (R && VO_ORD == 2) return i < 12 ? m_block(1, 0, 11 - i) : m_block(1, 1, i - 12);
  if (R && VO_ORD == 3) return i & 1 ? m_block(1, 1, i >> 1) : m_block(1, 0, 11 - (i >> 1));
  if (!R && QK_REV) return m_block(0, i & 1, 11 - (i >> 1));
  return R && VO_ORD ? m_block(R, i < 12, i % 12) : m_block(R, i & 1, i >> 1);
}
#define W_BG 0xfedcba9876543210ull
#define W_NT 0
#define U_ORD 0x3210
constexpr u64 bg_order(int m) { u64 r = 0; for (int n = 0; n < 16; ++n) r |= (u64)(m_block(0, m > 0, ((m == 2 ? W_BV : W_BG) >> (4 * n)) & 15) >> 1) << (4 * n); return r; }
struct WSched { int ord[32]; int slot[32]; int issue[32]; int out[32]; int reg[32]; };
constexpr WSched make_wsched(int R) {
  WSched S{};
  int n = 0;
  for (int i = 0; i < 24; ++i) S.ord[n++] = w_block(R, i);
  for (int i = 0; i < n; ++i) S.reg[i] = frag_reg_slot(R, 2 * S.ord[i]);
  const int npure = n;
  for (int b = 0; b < 32; ++b) if (blk_in_lds(R, b)) S.ord[n++] = b;
  int seq[32] = {};
  int nissued = 0, next = 0;
  for (int i = 0; i < 10; ++i) { S.slot[i] = (i + 10 - npure % 10) % 10; S.issue[i] = -1; seq[i] = nissued++; }
  next = 10;
  S.out[0] = nissued - 1;
  for (int c = 0; c <= npure; ++c) {
    if (c >= 1) {
      const int k = S.slot[c - 1];
      if (next < npure) { S.slot[next] = k; S.issue[next] = c; seq[next] = nissued++; ++next; }
      else if (k < NLF / 2) { S.slot[npure + k] = k; S.issue[npure + k] = c; seq[npure + k] = nissued++; }
    }
    if (c + 1 < npure) S.out[c + 1] = nissued - 1 - seq[c + 1];
  }
  return S;
}
__device__ __forceinline__ int kcol(int k) { return 8 * ((k >> 1) & 1) + ((k & 1) ? 7 - (k >> 2) : k >> 2); }
template <bool PERM = false>
__device__ __forceinline__ u32x8 tr_frag(const char* sb, int tile, int lane) {
  u32x8 f;
#pragma unroll
  for (int q = 0; q < 4; ++q) {
    int row = (q >> 1) * 32 + 8 * (lane >> 4) + 4 * (q & 1) + ((lane & 15) >> 2);
    if (PERM) row = (row & ~15) + kcol(row & 15);
    int p = lane & 3;
    int lc = 2 * tile + (p >> 1);
    int addr = row * 256 + ((lc ^ swz_b(row)) * 16) + 8 * (p & 1);
    bf16x4 v = __builtin_amdgcn_ds_read_tr16_b64_v4i16((LAS bf16x4*)(sb + addr));
    u32x2 u = __builtin_bit_cast(u32x2, v);
    f[2 * q] = u[0]; f[2 * q + 1] = u[1];
  }
  return f;
}
__device__ __forceinline__ void dma16(__amdgpu_buffer_rsrc_t rs, unsigned voff, unsigned soff, unsigned lds, bool nt = false, bool pad = true) {
  if (nt) asm volatile("s_mov_b32 m0, %0\n\ts_nop 3\n\tbuffer_load_dwordx4 %1, %2, %3 offen nt lds" :: "s"(lds), "v"(voff), "s"(rs), "s"(soff) : "m0", "memory");
  else if (pad) asm volatile("s_mov_b32 m0, %0\n\ts_nop 3\n\tbuffer_load_dwordx4 %1, %2, %3 offen lds" :: "s"(lds), "v"(voff), "s"(rs), "s"(soff) : "m0", "memory");
  else asm volatile("s_mov_b32 m0, %0\n\ts_nop 0\n\tbuffer_load_dwordx4 %1, %2, %3 offen lds" :: "s"(lds), "v"(voff), "s"(rs), "s"(soff) : "m0", "memory");
}
__device__ __forceinline__ u32x8 tr_frag2(const char* sb, int tile, int f, int lane) {
  u32x8 r;
#pragma unroll
  for (int c = 0; c < 2; ++c)
#pragma unroll
    for (int t = 0; t < 2; ++t) {
      const int row = 32 * c + 16 * f + 4 * (lane >> 4) + ((lane & 15) >> 2);
      const int p = lane & 3, lc = 2 * (tile + t) + (p >> 1);
      const u32x2 u = __builtin_bit_cast(u32x2, __builtin_amdgcn_ds_read_tr16_b64_v4i16((LAS bf16x4*)(sb + row * 256 + ((lc ^ swz_b(row)) * 16) + 8 * (p & 1))));
      r[4 * c + t] = u[0]; r[4 * c + 2 + t] = u[1];
    }
  return r;
}
template <int ROLE>
__device__ __forceinline__ void load_weights(const ChainArgs& a, int h, int w, int lane, char* smem, Wts& W, Gate& G, u32x4& gv) {
#pragma unroll
  for (int i = 0; i < 4; ++i) asm volatile("" : "=v"(W.v[i]));
#pragma unroll
  for (int i = 0; i < 32; ++i) asm volatile("" : "=a"(W.a[i]));
  constexpr WSched S = make_wsched(ROLE);
  constexpr int NP = 32 - NLF / 2;
  const __amdgpu_buffer_rsrc_t r0 = mk_rsrc(ROLE == 0 ? a.Wq : a.Wv, 8u << 20), r1 = mk_rsrc(ROLE == 0 ? a.Wk : a.Wo, 8u << 20);
  const unsigned lds0 = (unsigned)(uintptr_t)(LAS char*)smem;
  unsigned vo[2][4];
#pragma unroll
  for (int j = 0; j < 4; ++j) {
    const int row = (w * 4 + j) * 4 + (lane >> 4), lc = (lane & 15) ^ swz_b(row);
    vo[0][j] = row * HD * 2 + lc * 16; vo[1][j] = row * DM * 2 + lc * 16;
  }
  asm volatile("s_nop 4");
  auto stage = [&](int i) {
    const int blk = S.ord[i];
    const bool second = ROLE == 0 ? (blk & 1) : blk >= 16, wo = ROLE == 1 && second;
    const unsigned so = wo ? (unsigned)(((128 * h + 64 * (blk & 1)) * DM + 128 * (((blk - 16) >> 1) ^ (2 * (h & 3)))) * 2)
                           : (unsigned)((64 * ((ROLE == 0 ? blk >> 1 : blk) ^ (h & RHO)) * HD + 128 * h) * 2);
#pragma unroll
    for (int j = 0; j < 4; ++j) dma16(second ? r1 : r0, vo[wo][j], so, lds0 + 16384 * S.slot[i] + (w * 4 + j) * 1024, W_NT & (ROLE ? 8 : 1));
  };
#pragma unroll
  for (int i = 0; i < 32; ++i) if (S.issue[i] == -1) stage(i);
#pragma unroll
  for (int c = 0; c < NP; ++c) {
    asm volatile("s_waitcnt vmcnt(%0)" :: "n"(4 * S.out[c]) : "memory");
    lds_barrier();
#pragma unroll
    for (int i = 0; i < 32; ++i) if (S.issue[i] == c) stage(i);
    const char* sb = smem + 16384 * S.slot[c];
#pragma unroll
    for (int t = 0; t < 2; ++t) {
      u32x8 f = (ROLE == 1 && S.ord[c] >= 16) ? tr_frag2(sb, 2 * w, t, lane) : tr_frag<true>(sb, 2 * w + t, lane);
      if (S.reg[c] < 32) {
#pragma unroll
        for (int i = 0; i < 8; ++i) asm volatile("v_accvgpr_write_b32 a[%1], %0" :: "v"(f[i]), "n"(8 * ((S.reg[c] + t) & 31) + i));
      }
      else {
#pragma unroll
        for (int i = 0; i < 8; ++i) W.v[(S.reg[c] >> 2) & 3][8 * ((S.reg[c] + t) & 3) + i] = f[i];
      }
    }
    if (S.reg[c] >= 32) asm volatile("" : "+v"(W.v[(S.reg[c] >> 2) & 3]));
  }
  static_assert(S.issue[31] < NP && S.issue[NP] < S.issue[31], "resident blocks are issued in slot order inside the loop");
  G.ask(a.sync, gv);
#pragma unroll
  for (int j = 0; j < NLF / 2; ++j) {
    asm volatile("s_waitcnt vmcnt(%0)" :: "n"(4 * (NLF / 2 - 1 - j) + 1) : "memory");
    lds_barrier();
    const bool two = ROLE == 1 && S.ord[NP + j] >= 16;
    u32x8 f0 = two ? tr_frag2(smem + 16384 * j, 2 * w, 0, lane) : tr_frag<true>(smem + 16384 * j, 2 * w, lane);
    u32x8 f1 = two ? tr_frag2(smem + 16384 * j, 2 * w, 1, lane) : tr_frag<true>(smem + 16384 * j, 2 * w + 1, lane);
    lds_barrier();
    char* d = smem + 16384 * j + w * 4096 + lane * 16;
    *(u32x4*)d = (u32x4){f0[0], f0[1], f0[2], f0[3]};
    *(u32x4*)(d + 1024) = (u32x4){f0[4], f0[5], f0[6], f0[7]};
    *(u32x4*)(d + 2048) = (u32x4){f1[0], f1[1], f1[2], f1[3]};
    *(u32x4*)(d + 3072) = (u32x4){f1[4], f1[5], f1[6], f1[7]};
  }
  asm volatile("s_waitcnt vmcnt(0)" : "+v"(gv) :: "memory");
  lds_barrier();
}
template <int... Is> struct iseq {};
template <int N, int... Is> struct mkseq : mkseq<N - 1, N - 1, Is...> {};
template <int... Is> struct mkseq<0, Is...> { typedef iseq<Is...> type; };

#define SB0 __builtin_amdgcn_sched_barrier(0)
#define CAT8(x, y) ((u32x8){x[0], x[1], x[2], x[3], y[0], y[1], y[2], y[3]})
template <class F, int... I>
__device__ __forceinline__ void for_slots(iseq<I...>, F f) { (f(std::integral_constant<int, I>{}), ...); }
#define QK_BODY \
  "s_cmp_eq_u32 %[wv], 1\n\t" \
  "s_cbranch_scc1 91f\n\t" \
  "s_cmp_eq_u32 %[wv], 2\n\t" \
  "s_cbranch_scc1 92f\n\t" \
  "s_cmp_eq_u32 %[wv], 3\n\t" \
  "s_cbranch_scc1 93f\n\t" \
  "90:\n\t" \
  "s_mov_b32 s58, s42\n\t" \
  "20:\n\t" \
  "s_and_b32 s76, s44, 7\n\t" \
  "s_lshl_b32 s76, s76, 15\n\t" \
  "s_add_i32 s40, s54, s76\n\t" \
  "s_and_b32 s76, s44, 3\n\t" \
  "s_lshl_b32 s45, s76, 14\n\t" \
  "s_mul_i32 s47, s76, 0xc000\n\t" \
  "s_add_i32 s47, s47, 0x40000\n\t" \
  "s_add_i32 s49, s44, -1\n\t" \
  "s_and_b32 s48, s49, 2\n\t" \
  "s_lshl_b32 s48, s48, 13\n\t" \
  "s_and_b32 s76, s49, 1\n\t" \
  "s_lshl_b32 s76, s76, 12\n\t" \
  "s_add_i32 s48, s48, s76\n\t" \
  "s_lshl_b32 s50, s53, 8\n\t" \
  "s_mov_b32 s59, 0\n\t" \
  "s_mov_b32 s92, s51\n\t" \
  "s_mov_b32 s93, s53\n\t" \
  "s_xor_b32 s76, s40, 0x400\n\t" \
  "s_xor_b32 s77, s40, 0x800\n\t" \
  "s_xor_b32 s78, s40, 0xc00\n\t" \
  "s_cmp_lg_u32 s58, 0\n\t" \
  "s_cbranch_scc1 11f\n\t" \
  "10:\n\t" \
  "ds_read_b128 v[48:51], v102 offset:0\n\t" \
  "ds_read_b128 v[52:55], v102 offset:1024\n\t" \
  "ds_read_b128 v[56:59], v102 offset:2048\n\t" \
  "ds_read_b128 v[60:63], v102 offset:3072\n\t" \
  "ds_read_b128 v[16:19], v102 offset:16384\n\t" \
  "ds_read_b128 v[20:23], v102 offset:17408\n\t" \
  "ds_read_b128 v[24:27], v102 offset:18432\n\t" \
  "ds_read_b128 v[28:31], v102 offset:19456\n\t" \
  "ds_read_b128 v[84:87], v102 offset:49152\n\t" \
  "ds_read_b128 v[88:91], v102 offset:50176\n\t" \
  "ds_read_b128 v[92:95], v102 offset:51200\n\t" \
  "ds_read_b128 v[96:99], v102 offset:52224\n\t" \
  "s_waitcnt lgkmcnt(4)\n\t" \
  "ds_read_b128 v[0:3], v103 offset:16384\n\t" \
  "ds_read_b128 v[4:7], v103 offset:17408\n\t" \
  "ds_read_b128 v[8:11], v103 offset:18432\n\t" \
  "ds_read_b128 v[12:15], v103 offset:19456\n\t" \
  "s_bitcmp1_b32 s58, 1\n\t" \
  "s_cselect_b64 s[80:81], -1, 0\n\t" \
  "s_cmp_lg_u32 s58, 0\n\t" \
  "s_cbranch_scc1 17f\n\t" \
  "s_mov_b32 exec_hi, 0\n\t" \
  "16:\n\t" \
  "s_memtime vcc\n\t" \
  "s_waitcnt lgkmcnt(0)\n\t" \
  "s_sub_i32 s79, s41, vcc_lo\n\t" \
  "s_cmp_gt_i32 s79, 64\n\t" \
  "s_cbranch_scc1 16b\n\t" \
  "13:\n\t" \
  "buffer_load_dword v116, v112, %[rh], s48 offen sc1\n\t" \
  "s_waitcnt vmcnt(0)\n\t" \
  "15:\n\t" \
  "v_mov_b32 v117, 20000\n\t" \
  "v_cmp_ne_u32_sdwa vcc, v116, s49 src0_sel:BYTE_0 src1_sel:DWORD\n\t" \
  "v_subrev_u32 v116, s50, v116\n\t" \
  "v_ashrrev_i32 v116, 8, v116\n\t" \
  "s_nop 1\n\t" \
  "v_max_i32_dpp v116, v116, v116 quad_perm:[1,0,3,2] row_mask:0xf bank_mask:0xf\n\t" \
  "s_cbranch_vccnz 14f\n\t" \
  "s_nop 0\n\t" \
  "v_max_i32_dpp v116, v116, v116 quad_perm:[2,3,0,1] row_mask:0xf bank_mask:0xf\n\t" \
  "s_nop 1\n\t" \
  "v_max_i32_dpp v116, v116, v116 row_half_mirror row_mask:0xf bank_mask:0xf\n\t" \
  "s_nop 1\n\t" \
  "v_max_i32_dpp v116, v116, v116 row_mirror row_mask:0xf bank_mask:0xf\n\t" \
  "v_med3_i32 v116, v116, 0, v117\n\t" \
  "v_mul_u32_u24 v116, s52, v116\n\t" \
  "v_lshrrev_b32 v116, 16, v116\n\t" \
  "s_mov_b64 exec, -1\n\t" \
  "v_readfirstlane_b32 s84, v116\n\t" \
  "v_readlane_b32 s85, v116, 16\n\t" \
  "s_max_u32 s84, s84, s85\n\t" \
  "s_add_i32 s84, s84, s51\n\t" \
  "1:\n\t" \
  "s_memtime vcc\n\t" \
  "s_waitcnt lgkmcnt(0)\n\t" \
  "s_sub_i32 s79, s84, vcc_lo\n\t" \
  "s_cmp_gt_i32 s79, 128\n\t" \
  "s_cbranch_scc1 1b\n\t" \
  ".p2align 4\n\t" \
  "2:\n\t" \
  "s_sub_i32 s79, s79, 33\n\t" \
  "s_cmp_gt_i32 s79, 56\n\t" \
  "s_cbranch_scc1 2b\n\t" \
  "buffer_load_dwordx4 v[32:35], v100, %[rh], s40 offen sc1\n\t" \
  "s_nop 12\n\t" \
  "buffer_load_dwordx4 v[36:39], v100, %[rh], s76 offen sc1\n\t" \
  "s_nop 12\n\t" \
  "buffer_load_dwordx4 v[40:43], v100, %[rh], s77 offen sc1\n\t" \
  "s_nop 12\n\t" \
  "buffer_load_dwordx4 v[44:47], v100, %[rh], s78 offen sc1\n\t" \
  "s_memtime vcc\n\t" \
  "s_memrealtime s[82:83]\n\t" \
  "s_nop 4\n\t" \
  "s_memrealtime s[84:85]\n\t" \
  "s_nop 4\n\t" \
  "s_memrealtime s[56:57]\n\t" \
  "s_nop 4\n\t" \
  "s_memrealtime s[86:87]\n\t" \
  "s_nop 4\n\t" \
  "s_memrealtime s[88:89]\n\t" \
  "s_nop 4\n\t" \
  "s_memrealtime s[90:91]\n\t" \
  "s_mov_b32 s92, 0\n\t" \
  "s_waitcnt lgkmcnt(0)\n\t" \
  "s_cmp_eq_u32 s82, s84\n\t" \
  "s_addc_u32 s92, s92, 0\n\t" \
  "s_cmp_eq_u32 s82, s56\n\t" \
  "s_addc_u32 s92, s92, 0\n\t" \
  "s_cmp_eq_u32 s82, s86\n\t" \
  "s_addc_u32 s92, s92, 0\n\t" \
  "s_cmp_eq_u32 s82, s88\n\t" \
  "s_addc_u32 s92, s92, 0\n\t" \
  "s_cmp_eq_u32 s82, s90\n\t" \
  "s_addc_u32 s92, s92, 0\n\t" \
  "s_mul_i32 s92, s92, 24\n\t" \
  "s_add_i32 s92, s92, vcc_lo\n\t" \
  "s_lshl4_add_u32 s93, s82, 64\n\t" \
  "3:\n\t" \
  "s_sub_i32 s84, s92, s43\n\t" \
  "s_lshl_b32 s85, s93, 8\n\t" \
  "s_or_b32 s85, s85, s44\n\t" \
  "v_mov_b32 v118, s85\n\t" \
  "v_mov_b64 v[68:69], 0\n\t" \
  "v_mov_b64 v[70:71], 0\n\t" \
  "v_mov_b64 v[72:73], 0\n\t" \
  "v_mov_b64 v[74:75], 0\n\t" \
  "v_mov_b64 v[76:77], 0\n\t" \
  "v_mov_b64 v[78:79], 0\n\t" \
  "v_mov_b64 v[80:81], 0\n\t" \
  "v_mov_b64 v[82:83], 0\n\t" \
  "s_waitcnt lgkmcnt(0)\n\t" \
  "s_waitcnt vmcnt(3)\n\t" \
  "v_smfmac_f32_16x16x64_bf16 v[68:71], v[32:35], a[0:7], v104\n\t" \
  "ds_write_b128 v101, v[32:35] offset:0\n\t" \
  "v_smfmac_f32_16x16x64_bf16 v[72:75], v[32:35], a[8:15], v104\n\t" \
  "v_smfmac_f32_16x16x64_bf16 v[76:79], v[32:35], v[48:55], v104\n\t" \
  "v_smfmac_f32_16x16x64_bf16 v[80:83], v[32:35], v[56:63], v104\n\t" \
  "s_waitcnt vmcnt(2)\n\t" \
  "v_smfmac_f32_16x16x64_bf16 v[68:71], v[36:39], a[16:23], v104\n\t" \
  "ds_write_b128 v101, v[36:39] offset:1024\n\t" \
  "v_smfmac_f32_16x16x64_bf16 v[72:75], v[36:39], a[24:31], v104\n\t" \
  "v_smfmac_f32_16x16x64_bf16 v[76:79], v[36:39], a[32:39], v104\n\t" \
  "v_cmp_u_f32 vcc, v68, v69\n\t" \
  "v_smfmac_f32_16x16x64_bf16 v[80:83], v[36:39], a[40:47], v104\n\t" \
  "v_cmp_u_f32 s[82:83], v70, v71\n\t" \
  "s_waitcnt vmcnt(1)\n\t" \
  "v_smfmac_f32_16x16x64_bf16 v[68:71], v[40:43], v[16:23], v104\n\t" \
  "ds_write_b128 v101, v[40:43] offset:2048\n\t" \
  "v_smfmac_f32_16x16x64_bf16 v[72:75], v[40:43], v[24:31], v104\n\t" \
  "s_or_b64 vcc, vcc, s[82:83]\n\t" \
  "s_or_b64 vcc, vcc, s[80:81]\n\t" \
  "s_waitcnt lgkmcnt(1)\n\t" \
  "v_smfmac_f32_16x16x64_bf16 v[76:79], v[40:43], a[48:55], v104\n\t" \
  "s_cbranch_scc1 8f\n\t" \
  "5:\n\t" \
  "s_barrier\n\t" \
  "4:\n\t" \
  "s_memtime s[56:57]\n\t" \
  "ds_read_b128 v[48:51], v101 offset:4096\n\t" \
  "v_smfmac_f32_16x16x64_bf16 v[80:83], v[40:43], a[56:63], v104\n\t" \
  "ds_read_b128 v[52:55], v101 offset:5120\n\t" \
  "ds_read_b128 v[56:59], v101 offset:8192\n\t" \
  "s_waitcnt vmcnt(0)\n\t" \
  "v_smfmac_f32_16x16x64_bf16 v[68:71], v[44:47], a[64:71], v104\n\t" \
  "ds_write_b128 v101, v[44:47] offset:3072\n\t" \
  "v_smfmac_f32_16x16x64_bf16 v[72:75], v[44:47], a[72:79], v104\n\t" \
  "v_smfmac_f32_16x16x64_bf16 v[76:79], v[44:47], a[80:87], v104\n\t" \
  "v_cmp_u_f32 vcc, v68, v69\n\t" \
  "v_smfmac_f32_16x16x64_bf16 v[80:83], v[44:47], a[88:95], v104\n\t" \
  "v_cmp_u_f32 s[82:83], v70, v71\n\t" \
  "s_waitcnt lgkmcnt(3)\n\t" \
  "v_smfmac_f32_16x16x64_bf16 v[68:71], v[48:51], a[96:103], v104\n\t" \
  "ds_read_b128 v[60:63], v101 offset:9216\n\t" \
  "ds_read_b128 v[64:67], v101 offset:12288\n\t" \
  "v_smfmac_f32_16x16x64_bf16 v[72:75], v[48:51], a[104:111], v104\n\t" \
  "s_or_b64 vcc, vcc, s[82:83]\n\t" \
  "s_waitcnt lgkmcnt(2)\n\t" \
  "v_smfmac_f32_16x16x64_bf16 v[68:71], v[52:55], a[112:119], v104\n\t" \
  "s_cbranch_scc1 9f\n\t" \
  "6:\n\t" \
  "s_barrier\n\t" \
  "v_smfmac_f32_16x16x64_bf16 v[72:75], v[52:55], a[120:127], v104\n\t" \
  "s_waitcnt lgkmcnt(0)\n\t" \
  "ds_read_b128 v[48:51], v101 offset:13312\n\t" \
  "v_smfmac_f32_16x16x64_bf16 v[68:71], v[56:59], a[192:199], v104\n\t" \
  "ds_read_b128 v[52:55], v101 offset:6144\n\t" \
  "s_sub_i32 s79, s56, s84\n\t" \
  "s_mul_hi_u32 s79, s79, s46\n\t" \
  "v_smfmac_f32_16x16x64_bf16 v[72:75], v[56:59], a[200:207], v104\n\t" \
  "v_lshl_add_u32 v116, s79, 9, v118\n\t" \
  "buffer_store_dword v116, v109, %[rh], s45 offen sc1\n\t" \
  "v_smfmac_f32_16x16x64_bf16 v[68:71], v[60:63], a[208:215], v104\n\t" \
  "ds_read_b128 v[56:59], v101 offset:7168\n\t" \
  "v_smfmac_f32_16x16x64_bf16 v[72:75], v[60:63], a[216:223], v104\n\t" \
  "v_smfmac_f32_16x16x64_bf16 v[68:71], v[64:67], v[152:159], v104\n\t" \
  "ds_read_b128 v[60:63], v101 offset:10240\n\t" \
  "v_smfmac_f32_16x16x64_bf16 v[72:75], v[64:67], v[160:167], v104\n\t" \
  "s_waitcnt lgkmcnt(3)\n\t" \
  "v_smfmac_f32_16x16x64_bf16 v[68:71], v[48:51], v[168:175], v104\n\t" \
  "ds_read_b128 v[64:67], v101 offset:11264\n\t" \
  "ds_read_b128 v[16:19], v103 offset:49152\n\t" \
  "v_smfmac_f32_16x16x64_bf16 v[72:75], v[48:51], v[176:183], v104\n\t" \
  "s_waitcnt lgkmcnt(4)\n\t" \
  "v_smfmac_f32_16x16x64_bf16 v[68:71], v[52:55], v[84:91], v104\n\t" \
  "ds_read_b128 v[48:51], v101 offset:15360\n\t" \
  "ds_read_b128 v[20:23], v103 offset:50176\n\t" \
  "v_smfmac_f32_16x16x64_bf16 v[72:75], v[52:55], v[92:99], v104\n\t" \
  "s_waitcnt lgkmcnt(5)\n\t" \
  "v_smfmac_f32_16x16x64_bf16 v[68:71], v[56:59], a[160:167], v104\n\t" \
  "ds_read_b128 v[52:55], v101 offset:14336\n\t" \
  "ds_read_b128 v[24:27], v103 offset:51200\n\t" \
  "v_smfmac_f32_16x16x64_bf16 v[72:75], v[56:59], a[168:175], v104\n\t" \
  "ds_read_b128 v[28:31], v103 offset:52224\n\t" \
  "s_waitcnt lgkmcnt(7)\n\t" \
  "v_smfmac_f32_16x16x64_bf16 v[68:71], v[60:63], v[0:7], v104\n\t" \
  "ds_read_b128 v[32:35], v101 offset:20480\n\t" \
  "v_smfmac_f32_16x16x64_bf16 v[72:75], v[60:63], v[8:15], v104\n\t" \
  "ds_read_b128 v[36:39], v101 offset:21504\n\t" \
  "v_mov_b64 v[92:93], 0\n\t" \
  "s_waitcnt lgkmcnt(8)\n\t" \
  "v_smfmac_f32_16x16x64_bf16 v[68:71], v[64:67], v[120:127], v104\n\t" \
  "ds_read_b128 v[0:3], v102 offset:32768\n\t" \
  "ds_read_b128 v[40:43], v101 offset:22528\n\t" \
  "v_smfmac_f32_16x16x64_bf16 v[72:75], v[64:67], v[128:135], v104\n\t" \
  "ds_read_b128 v[4:7], v102 offset:33792\n\t" \
  "s_waitcnt lgkmcnt(9)\n\t" \
  "v_smfmac_f32_16x16x64_bf16 v[68:71], v[48:51], v[216:223], v104\n\t" \
  "ds_read_b128 v[44:47], v101 offset:23552\n\t" \
  "v_mov_b64 v[94:95], 0\n\t" \
  "s_waitcnt lgkmcnt(8)\n\t" \
  "v_smfmac_f32_16x16x64_bf16 v[68:71], v[52:55], v[16:23], v104\n\t" \
  "ds_read_b128 v[8:11], v102 offset:34816\n\t" \
  "v_smfmac_f32_16x16x64_bf16 v[72:75], v[48:51], v[224:231], v104\n\t" \
  "ds_read_b128 v[12:15], v102 offset:35840\n\t" \
  "s_waitcnt lgkmcnt(8)\n\t" \
  "v_smfmac_f32_16x16x64_bf16 v[72:75], v[52:55], v[24:31], v104\n\t" \
  "v_add_f32 v96, v68, v70\n\t" \
  "v_smfmac_f32_16x16x64_bf16 v[76:79], v[56:59], a[176:183], v104\n\t" \
  "v_add_f32 v97, v69, v71\n\t" \
  "v_cvt_pk_bf16_f32 v96, v96, v97\n\t" \
  "v_smfmac_f32_16x16x64_bf16 v[80:83], v[56:59], a[184:191], v104\n\t" \
  "v_add_f32 v98, v72, v74\n\t" \
  "v_add_f32 v99, v73, v75\n\t" \
  "v_smfmac_f32_16x16x64_bf16 v[76:79], v[60:63], a[240:247], v104\n\t" \
  "v_cvt_pk_bf16_f32 v98, v98, v99\n\t" \
  "ds_write2st64_b32 v105, v96, v98 offset1:1\n\t" \
  "v_smfmac_f32_16x16x64_bf16 v[80:83], v[60:63], a[248:255], v104\n\t" \
  "ds_read_b128 v[56:59], v101 offset:4096\n\t" \
  "ds_read_b128 v[16:19], v101 offset:24576\n\t" \
  "v_mov_b64 v[68:69], 0\n\t" \
  "v_smfmac_f32_16x16x64_bf16 v[76:79], v[64:67], v[136:143], v104\n\t" \
  "ds_read_b128 v[20:23], v101 offset:25600\n\t" \
  "v_mov_b64 v[70:71], 0\n\t" \
  "v_smfmac_f32_16x16x64_bf16 v[80:83], v[64:67], v[144:151], v104\n\t" \
  "ds_read_b128 v[24:27], v101 offset:26624\n\t" \
  "v_smfmac_f32_16x16x64_bf16 v[76:79], v[48:51], v[232:239], v104\n\t" \
  "s_waitcnt lgkmcnt(4)\n\t" \
  "s_barrier\n\t" \
  "ds_read_b64_tr_b16 v[84:85], v107 offset:0\n\t" \
  "v_smfmac_f32_16x16x64_bf16 v[80:83], v[48:51], v[240:247], v104\n\t" \
  "ds_read_b64_tr_b16 v[86:87], v107 offset:128\n\t" \
  "ds_read_b64_tr_b16 v[88:89], v107 offset:1024\n\t" \
  "v_smfmac_f32_16x16x64_bf16 v[76:79], v[52:55], v[200:207], v104\n\t" \
  "ds_read_b128 v[60:63], v101 offset:13312\n\t" \
  "ds_read_b64_tr_b16 v[90:91], v107 offset:1152\n\t" \
  "v_smfmac_f32_16x16x64_bf16 v[80:83], v[52:55], v[208:215], v104\n\t" \
  "ds_read_b128 v[64:67], v101 offset:6144\n\t" \
  "ds_read_b128 v[28:31], v101 offset:27648\n\t" \
  "s_waitcnt lgkmcnt(10)\n\t" \
  "v_smfmac_f32_16x16x64_bf16 v[76:79], v[56:59], v[0:7], v104\n\t" \
  "ds_read_b128 v[48:51], v101 offset:8192\n\t" \
  "s_waitcnt lgkmcnt(6)\n\t" \
  "v_smfmac_f32_16x16x64_bf16 v[92:95], v[84:87], v[32:39], v104\n\t" \
  "ds_read_b128 v[0:3], v103 offset:0\n\t" \
  "s_waitcnt lgkmcnt(4)\n\t" \
  "v_smfmac_f32_16x16x64_bf16 v[92:95], v[88:91], v[40:47], v104\n\t" \
  "ds_read_b128 v[4:7], v103 offset:1024\n\t" \
  "v_smfmac_f32_16x16x64_bf16 v[68:71], v[84:87], v[16:23], v104\n\t" \
  "s_waitcnt lgkmcnt(3)\n\t" \
  "v_smfmac_f32_16x16x64_bf16 v[68:71], v[88:91], v[24:31], v104\n\t" \
  "v_add_f32 v92, v92, v94\n\t" \
  "v_add_f32 v94, v93, v95\n\t" \
  "v_smfmac_f32_16x16x64_bf16 v[80:83], v[56:59], v[8:15], v104\n\t" \
  "ds_read_b128 v[8:11], v103 offset:2048\n\t" \
  "v_mov_b32 v97, v94\n\t" \
  "v_mov_b32_dpp v94, v92 row_ror:8 row_mask:0xf bank_mask:0x3\n\t" \
  "s_nop 0\n\t" \
  "v_mov_b32_dpp v92, v97 row_ror:8 row_mask:0xf bank_mask:0xc\n\t" \
  "v_smfmac_f32_16x16x64_bf16 v[76:79], v[60:63], v[184:191], v104\n\t" \
  "ds_read_b128 v[52:55], v101 offset:5120\n\t" \
  "ds_read_b128 v[12:15], v103 offset:3072\n\t" \
  "v_add_f32 v93, v68, v70\n\t" \
  "v_add_f32 v95, v69, v71\n\t" \
  "v_max_f32 v98, v92, v94\n\t" \
  "v_mov_b32 v97, v95\n\t" \
  "v_mov_b32_dpp v95, v93 row_ror:8 row_mask:0xf bank_mask:0x3\n\t" \
  "v_smfmac_f32_16x16x64_bf16 v[80:83], v[60:63], v[192:199], v104\n\t" \
  "v_mov_b32_dpp v93, v97 row_ror:8 row_mask:0xf bank_mask:0xc\n\t" \
  "v_max3_f32 v98, v98, v93, v95\n\t" \
  "v_smfmac_f32_16x16x64_bf16 v[76:79], v[64:67], a[144:151], v104\n\t" \
  "ds_read_b128 v[56:59], v101 offset:9216\n\t" \
  "ds_read_b128 v[32:35], v103 offset:32768\n\t" \
  "v_max_f32_dpp v98, v98, v98 quad_perm:[1,0,3,2] row_mask:0xf bank_mask:0xf\n\t" \
  "v_smfmac_f32_16x16x64_bf16 v[80:83], v[64:67], a[152:159], v104\n\t" \
  "ds_read_b128 v[60:63], v101 offset:12288\n\t" \
  "ds_read_b128 v[36:39], v103 offset:33792\n\t" \
  "v_max_f32_dpp v98, v98, v98 quad_perm:[2,3,0,1] row_mask:0xf bank_mask:0xf\n\t" \
  "s_waitcnt lgkmcnt(7)\n\t" \
  "v_smfmac_f32_16x16x64_bf16 v[76:79], v[48:51], v[0:7], v104\n\t" \
  "ds_read_b128 v[40:43], v103 offset:34816\n\t" \
  "v_max_f32_dpp v98, v98, v98 row_half_mirror row_mask:0xf bank_mask:0xf\n\t" \
  "v_mul_f32 v98, %[cs], v98\n\t" \
  "v_fma_f32 v92, v92, %[cs], -v98\n\t" \
  "v_fma_f32 v93, v93, %[cs], -v98\n\t" \
  "v_fma_f32 v94, v94, %[cs], -v98\n\t" \
  "s_waitcnt lgkmcnt(5)\n\t" \
  "v_smfmac_f32_16x16x64_bf16 v[80:83], v[48:51], v[8:15], v104\n\t" \
  "ds_read_b128 v[44:47], v103 offset:35840\n\t" \
  "v_fma_f32 v95, v95, %[cs], -v98\n\t" \
  "v_exp_f32 v72, v92\n\t" \
  "v_exp_f32 v73, v93\n\t" \
  "v_exp_f32 v74, v94\n\t" \
  "v_exp_f32 v75, v95\n\t" \
  "v_cvt_pk_bf16_f32 v68, v72, v73\n\t" \
  "v_cvt_pk_bf16_f32 v69, v74, v75\n\t" \
  "buffer_store_dwordx2 v[68:69], v110, %[rh], s47 offen sc1\n\t" \
  "v_smfmac_f32_16x16x64_bf16 v[76:79], v[52:55], a[128:135], v104\n\t" \
  "v_smfmac_f32_16x16x64_bf16 v[80:83], v[52:55], a[136:143], v104\n\t" \
  "s_waitcnt lgkmcnt(5)\n\t" \
  "v_smfmac_f32_16x16x64_bf16 v[76:79], v[56:59], a[224:231], v104\n\t" \
  "s_waitcnt lgkmcnt(2)\n\t" \
  "v_smfmac_f32_16x16x64_bf16 v[76:79], v[60:63], v[32:39], v104\n\t" \
  "v_smfmac_f32_16x16x64_bf16 v[80:83], v[56:59], a[232:239], v104\n\t" \
  "s_waitcnt lgkmcnt(0)\n\t" \
  "v_smfmac_f32_16x16x64_bf16 v[80:83], v[60:63], v[40:47], v104\n\t" \
  "v_add_f32 v20, v76, v78\n\t" \
  "v_add_f32 v21, v77, v79\n\t" \
  "v_cvt_pk_bf16_f32 v20, v20, v21\n\t" \
  "ds_write_b32 v106, v20\n\t" \
  "s_nop 0\n\t" \
  "s_nop 0\n\t" \
  "s_nop 0\n\t" \
  "s_nop 0\n\t" \
  "v_add_f32 v23, v80, v82\n\t" \
  "v_add_f32 v21, v81, v83\n\t" \
  "v_cvt_pk_bf16_f32 v21, v23, v21\n\t" \
  "ds_write_b32 v106, v21 offset:256\n\t" \
  "s_waitcnt lgkmcnt(0)\n\t" \
  "s_barrier\n\t" \
  "ds_read_b64_tr_b16 v[4:5], v108\n\t" \
  "ds_read_b64_tr_b16 v[6:7], v108 offset:64\n\t" \
  "ds_read_b64_tr_b16 v[8:9], v108 offset:512\n\t" \
  "ds_read_b64_tr_b16 v[10:11], v108 offset:576\n\t" \
  "ds_read_b64_tr_b16 v[12:13], v108 offset:1024\n\t" \
  "ds_read_b64_tr_b16 v[14:15], v108 offset:1088\n\t" \
  "ds_read_b64_tr_b16 v[16:17], v108 offset:1536\n\t" \
  "ds_read_b64_tr_b16 v[18:19], v108 offset:1600\n\t" \
  "v_mov_b64 v[0:1], 0\n\t" \
  "v_mov_b64 v[2:3], 0\n\t" \
  "v_add_f32 v29, v72, v73\n\t" \
  "v_add_f32 v29, v29, v74\n\t" \
  "s_waitcnt lgkmcnt(4)\n\t" \
  "v_smfmac_f32_16x16x64_bf16 v[0:3], v[84:87], v[4:11], v104\n\t" \
  "s_waitcnt lgkmcnt(0)\n\t" \
  "v_smfmac_f32_16x16x64_bf16 v[0:3], v[88:91], v[12:19], v104\n\t" \
  "v_add_f32 v29, v29, v75\n\t" \
  "s_mov_b32 s76, 0xaaaaaaaa\n\t" \
  "s_mov_b32 s77, s76\n\t" \
  "s_nop 4\n\t" \
  "v_add_f32 v96, v0, v2\n\t" \
  "v_add_f32_dpp v96, v1, v3 quad_perm:[0,1,2,3] row_mask:0xf bank_mask:0xc\n\t" \
  "v_fma_f32 v96, v96, %[cs], -v98\n\t" \
  "v_exp_f32 v24, v96\n\t" \
  "s_nop 0\n\t" \
  "v_add_f32 v29, v29, v24\n\t" \
  "s_nop 0\n\t" \
  "v_mov_b32_dpp v25, v24 quad_perm:[1,0,3,2] row_mask:0xf bank_mask:0xf\n\t" \
  "v_add_f32_dpp v29, v29, v29 quad_perm:[1,0,3,2] row_mask:0xf bank_mask:0xf\n\t" \
  "v_cndmask_b32_e64 v24, v24, v114, s[76:77]\n\t" \
  "v_cndmask_b32_e64 v25, v25, v115, s[76:77]\n\t" \
  "v_add_f32_dpp v29, v29, v29 quad_perm:[2,3,0,1] row_mask:0xf bank_mask:0xf\n\t" \
  "s_nop 1\n\t" \
  "v_add_f32_dpp v29, v29, v29 row_half_mirror row_mask:0xf bank_mask:0xf\n\t" \
  "v_rcp_f32 v26, v29\n\t" \
  "v_cmpx_gt_f32 vcc, 0x5d5e0b6b, v29\n\t" \
  "v_mul_f32 v24, v24, v26\n\t" \
  "v_mul_f32 v25, v25, v26\n\t" \
  "v_cvt_pk_bf16_f32 v24, v24, v25\n\t" \
  "buffer_store_dword v24, v111, %[rh], s47 offen sc1\n\t" \
  "s_memtime s[76:77]\n\t" \
  "s_mov_b64 exec, -1\n\t" \
  "v_mov_b32 v97, v29\n\t" \
  "s_cmp_eq_u64 vcc, -1\n\t" \
  "s_cselect_b32 s42, 0, 8\n\t" \
  "s_waitcnt lgkmcnt(0)\n\t" \
  "s_mov_b32 s57, s76\n\t" \
  "s_barrier\n\t" \
  "s_add_i32 s41, s56, 1950\n\t" \
  "s_sub_i32 s76, s57, s56\n\t" \
  "s_add_i32 s77, s43, 8\n\t" \
  "s_min_u32 s76, s76, s77\n\t" \
  "s_sub_i32 s43, s43, 4\n\t" \
  "s_max_u32 s43, s43, s76\n\t" \
  "s_add_i32 s76, s45, 0x4000\n\t" \
  "s_and_b32 s76, s76, 0xc000\n\t" \
  "v_mov_b32 v116, -1\n\t" \
  "buffer_store_dword v116, v109, %[rh], s76 offen sc1\n\t" \
  "s_sub_i32 s76, s92, s51\n\t" \
  "s_sub_i32 s77, s93, s53\n\t" \
  "s_cmp_eq_u32 s76, 0\n\t" \
  "s_cbranch_scc1 21f\n\t" \
  "v_cvt_f32_u32 v117, s76\n\t" \
  "v_cvt_f32_u32 v116, s77\n\t" \
  "v_rcp_f32 v117, v117\n\t" \
  "s_mov_b32 s51, s92\n\t" \
  "v_mul_f32 v117, v117, v116\n\t" \
  "v_max_f32 v117, 0x3f028f5c, v117\n\t" \
  "v_min_f32 v117, 0x3ffeb852, v117\n\t" \
  "v_rcp_f32 v116, v117\n\t" \
  "v_mul_f32 v117, 0x4f000000, v117\n\t" \
  "v_mul_f32 v116, 0x47800000, v116\n\t" \
  "v_cvt_u32_f32 v117, v117\n\t" \
  "v_cvt_u32_f32 v116, v116\n\t" \
  "s_mov_b32 s53, s93\n\t" \
  "v_readfirstlane_b32 s46, v117\n\t" \
  "v_readfirstlane_b32 s52, v116\n\t" \
  "21:\n\t" \
  "s_add_i32 s44, s44, 1\n\t" \
  "s_and_b32 s58, s58, 4\n\t" \
  "s_and_b32 s76, s44, 64\n\t" \
  "s_or_b32 s42, s42, s76\n\t" \
  "s_cbranch_scc0 20b\n\t" \
  "s_branch 99f\n\t" \
  "14:\n\t" \
  "s_sleep 3\n\t" \
  "s_add_u32 s59, s59, 0x10000\n\t" \
  "s_cmp_lt_u32 s59, 0x100000\n\t" \
  "s_cbranch_scc1 13b\n\t" \
  "s_mov_b64 exec, -1\n\t" \
  "s_mov_b32 s42, 16\n\t" \
  "s_branch 99f\n\t" \
  "8:\n\t" \
  "s_bitcmp1_b32 s58, 1\n\t" \
  "s_cbranch_scc1 4b\n\t" \
  "s_bitcmp1_b32 s58, 2\n\t" \
  "s_cbranch_scc1 5b\n\t" \
  "v_max3_u32 v116, v32, v33, v34\n\t" \
  "v_max3_u32 v117, v35, v36, v37\n\t" \
  "v_max3_u32 v116, v116, v38, v39\n\t" \
  "v_max_u32 v116, v116, v117\n\t" \
  "v_cmp_eq_u32 vcc, -1, v116\n\t" \
  "s_cbranch_vccz 5b\n\t" \
  "s_branch 0f\n\t" \
  "9:\n\t" \
  "s_bitcmp1_b32 s58, 2\n\t" \
  "s_cbranch_scc1 6b\n\t" \
  "v_max3_u32 v116, v40, v41, v42\n\t" \
  "v_max3_u32 v117, v43, v44, v45\n\t" \
  "v_max3_u32 v116, v116, v46, v47\n\t" \
  "v_max_u32 v116, v116, v117\n\t" \
  "v_cmp_eq_u32 vcc, -1, v116\n\t" \
  "s_cbranch_vccz 6b\n\t" \
  "s_or_b32 s58, s58, 2\n\t" \
  "0:\n\t" \
  "s_or_b32 s58, s58, 1\n\t" \
  "11:\n\t" \
  "buffer_load_dwordx4 v[32:35], v100, %[rh], s40 offen sc1\n\t" \
  "buffer_load_dwordx4 v[36:39], v100, %[rh], s76 offen sc1\n\t" \
  "buffer_load_dwordx4 v[40:43], v100, %[rh], s77 offen sc1\n\t" \
  "buffer_load_dwordx4 v[44:47], v100, %[rh], s78 offen sc1\n\t" \
  "s_waitcnt lgkmcnt(0)\n\t" \
  "s_branch 10b\n\t" \
  "17:\n\t" \
  "s_add_u32 s59, s59, 1\n\t" \
  "s_bitcmp1_b32 s58, 2\n\t" \
  "s_cbranch_scc1 3b\n\t" \
  "s_waitcnt vmcnt(0)\n\t" \
  "v_max_u32 v116, v32, v33\n\t" \
  "v_max3_u32 v116, v116, v34, v35\n\t" \
  "v_max3_u32 v116, v116, v36, v37\n\t" \
  "v_max3_u32 v116, v116, v38, v39\n\t" \
  "v_max3_u32 v116, v116, v40, v41\n\t" \
  "v_max3_u32 v116, v116, v42, v43\n\t" \
  "v_max3_u32 v116, v116, v44, v45\n\t" \
  "v_max3_u32 v116, v116, v46, v47\n\t" \
  "v_cmp_eq_u32 vcc, -1, v116\n\t" \
  "s_cbranch_vccz 3b\n\t" \
  "s_bitcmp1_b32 s59, 6\n\t" \
  "s_cbranch_scc1 12f\n\t" \
  "buffer_load_dwordx4 v[32:35], v100, %[rh], s40 offen sc1\n\t" \
  "buffer_load_dwordx4 v[36:39], v100, %[rh], s76 offen sc1\n\t" \
  "buffer_load_dwordx4 v[40:43], v100, %[rh], s77 offen sc1\n\t" \
  "buffer_load_dwordx4 v[44:47], v100, %[rh], s78 offen sc1\n\t" \
  "s_branch 17b\n\t" \
  "12:\n\t" \
  "s_mov_b32 s42, s58\n\t" \
  "s_branch 99f\n\t" \
  "91:\n\t" \
  "s_mov_b32 s58, s42\n\t" \
  "s_mov_b32 s95, 0\n\t" \
  "20:\n\t" \
  "s_and_b32 s76, s44, 7\n\t" \
  "s_lshl_b32 s76, s76, 15\n\t" \
  "s_add_i32 s40, s54, s76\n\t" \
  "s_and_b32 s76, s44, 3\n\t" \
  "s_lshl_b32 s45, s76, 14\n\t" \
  "s_add_i32 s47, s44, 1\n\t" \
  "s_min_u32 s47, s47, 63\n\t" \
  "s_lshr_b32 s76, s47, 1\n\t" \
  "s_lshl_b32 s47, s47, 20\n\t" \
  "s_add_i32 s47, s47, s55\n\t" \
  "s_bitcmp1_b64 %[sel], s76\n\t" \
  "s_cselect_b32 s76, 0x2000, 0\n\t" \
  "s_add_i32 s47, s47, s76\n\t" \
  "s_lshr_b32 s76, s44, 1\n\t" \
  "s_lshl_b32 s94, s44, 20\n\t" \
  "s_bitcmp1_b64 %[sel], s76\n\t" \
  "s_cselect_b32 s76, 0x2000, 0\n\t" \
  "s_add_i32 s94, s94, s76\n\t" \
  "s_add_i32 s49, s44, -1\n\t" \
  "s_and_b32 s48, s49, 2\n\t" \
  "s_lshl_b32 s48, s48, 13\n\t" \
  "s_and_b32 s76, s49, 1\n\t" \
  "s_lshl_b32 s76, s76, 12\n\t" \
  "s_add_i32 s48, s48, s76\n\t" \
  "s_lshl_b32 s50, s53, 8\n\t" \
  "s_mov_b32 s59, 0\n\t" \
  "s_mov_b32 s92, s51\n\t" \
  "s_mov_b32 s93, s53\n\t" \
  "s_xor_b32 s76, s40, 0x400\n\t" \
  "s_xor_b32 s77, s40, 0x800\n\t" \
  "s_xor_b32 s78, s40, 0xc00\n\t" \
  "s_cmp_lg_u32 s58, 0\n\t" \
  "s_cbranch_scc1 11f\n\t" \
  "10:\n\t" \
  "ds_read_b128 v[48:51], v102 offset:32768\n\t" \
  "ds_read_b128 v[52:55], v102 offset:33792\n\t" \
  "ds_read_b128 v[56:59], v102 offset:34816\n\t" \
  "ds_read_b128 v[60:63], v102 offset:35840\n\t" \
  "ds_read_b128 v[16:19], v102 offset:49152\n\t" \
  "ds_read_b128 v[20:23], v102 offset:50176\n\t" \
  "ds_read_b128 v[24:27], v102 offset:51200\n\t" \
  "ds_read_b128 v[28:31], v102 offset:52224\n\t" \
  "ds_read_b128 v[84:87], v103 offset:16384\n\t" \
  "ds_read_b128 v[88:91], v103 offset:17408\n\t" \
  "ds_read_b128 v[92:95], v103 offset:18432\n\t" \
  "ds_read_b128 v[96:99], v103 offset:19456\n\t" \
  "s_waitcnt lgkmcnt(4)\n\t" \
  "ds_read_b128 v[0:3], v103 offset:49152\n\t" \
  "ds_read_b128 v[4:7], v103 offset:50176\n\t" \
  "ds_read_b128 v[8:11], v103 offset:51200\n\t" \
  "ds_read_b128 v[12:15], v103 offset:52224\n\t" \
  "s_bitcmp1_b32 s58, 1\n\t" \
  "s_cselect_b64 s[80:81], -1, 0\n\t" \
  "s_cmp_lg_u32 s58, 0\n\t" \
  "s_cbranch_scc1 17f\n\t" \
  "s_mov_b32 exec_hi, 0\n\t" \
  "s_cmp_eq_u32 s95, 0\n\t" \
  "s_cbranch_scc1 16f\n\t" \
  "s_waitcnt vmcnt(2)\n\t" \
  "v_mov_b32 v116, v118\n\t" \
  "s_branch 15f\n\t" \
  "16:\n\t" \
  "s_memtime vcc\n\t" \
  "s_waitcnt lgkmcnt(0)\n\t" \
  "s_sub_i32 s79, s41, vcc_lo\n\t" \
  "s_cmp_gt_i32 s79, 64\n\t" \
  "s_cbranch_scc1 16b\n\t" \
  "13:\n\t" \
  "buffer_load_dword v116, v112, %[rh], s48 offen sc1\n\t" \
  "s_waitcnt vmcnt(0)\n\t" \
  "15:\n\t" \
  "v_mov_b32 v117, 20000\n\t" \
  "v_cmp_ne_u32_sdwa vcc, v116, s49 src0_sel:BYTE_0 src1_sel:DWORD\n\t" \
  "v_subrev_u32 v116, s50, v116\n\t" \
  "v_ashrrev_i32 v116, 8, v116\n\t" \
  "s_nop 1\n\t" \
  "v_max_i32_dpp v116, v116, v116 quad_perm:[1,0,3,2] row_mask:0xf bank_mask:0xf\n\t" \
  "s_cbranch_vccnz 14f\n\t" \
  "s_nop 0\n\t" \
  "v_max_i32_dpp v116, v116, v116 quad_perm:[2,3,0,1] row_mask:0xf bank_mask:0xf\n\t" \
  "s_nop 1\n\t" \
  "v_max_i32_dpp v116, v116, v116 row_half_mirror row_mask:0xf bank_mask:0xf\n\t" \
  "s_nop 1\n\t" \
  "v_max_i32_dpp v116, v116, v116 row_mirror row_mask:0xf bank_mask:0xf\n\t" \
  "v_med3_i32 v116, v116, 0, v117\n\t" \
  "v_mul_u32_u24 v116, s52, v116\n\t" \
  "v_lshrrev_b32 v116, 16, v116\n\t" \
  "s_mov_b64 exec, -1\n\t" \
  "v_readfirstlane_b32 s84, v116\n\t" \
  "v_readlane_b32 s85, v116, 16\n\t" \
  "s_max_u32 s84, s84, s85\n\t" \
  "s_add_i32 s84, s84, s51\n\t" \
  "1:\n\t" \
  "s_memtime vcc\n\t" \
  "s_waitcnt lgkmcnt(0)\n\t" \
  "s_sub_i32 s79, s84, vcc_lo\n\t" \
  "s_cmp_gt_i32 s79, 128\n\t" \
  "s_cbranch_scc1 1b\n\t" \
  ".p2align 4\n\t" \
  "2:\n\t" \
  "s_sub_i32 s79, s79, 33\n\t" \
  "s_cmp_gt_i32 s79, 56\n\t" \
  "s_cbranch_scc1 2b\n\t" \
  "buffer_load_dwordx4 v[32:35], v100, %[rh], s40 offen sc1\n\t" \
  "s_nop 12\n\t" \
  "buffer_load_dwordx4 v[36:39], v100, %[rh], s76 offen sc1\n\t" \
  "s_nop 12\n\t" \
  "buffer_load_dwordx4 v[40:43], v100, %[rh], s77 offen sc1\n\t" \
  "s_nop 12\n\t" \
  "buffer_load_dwordx4 v[44:47], v100, %[rh], s78 offen sc1\n\t" \
  "s_memtime vcc\n\t" \
  "s_memrealtime s[82:83]\n\t" \
  "s_nop 4\n\t" \
  "s_memrealtime s[84:85]\n\t" \
  "s_nop 4\n\t" \
  "s_memrealtime s[56:57]\n\t" \
  "s_nop 4\n\t" \
  "s_memrealtime s[86:87]\n\t" \
  "s_nop 4\n\t" \
  "s_memrealtime s[88:89]\n\t" \
  "s_nop 4\n\t" \
  "s_memrealtime s[90:91]\n\t" \
  "s_mov_b32 s92, 0\n\t" \
  "s_waitcnt lgkmcnt(0)\n\t" \
  "s_cmp_eq_u32 s82, s84\n\t" \
  "s_addc_u32 s92, s92, 0\n\t" \
  "s_cmp_eq_u32 s82, s56\n\t" \
  "s_addc_u32 s92, s92, 0\n\t" \
  "s_cmp_eq_u32 s82, s86\n\t" \
  "s_addc_u32 s92, s92, 0\n\t" \
  "s_cmp_eq_u32 s82, s88\n\t" \
  "s_addc_u32 s92, s92, 0\n\t" \
  "s_cmp_eq_u32 s82, s90\n\t" \
  "s_addc_u32 s92, s92, 0\n\t" \
  "s_mul_i32 s92, s92, 24\n\t" \
  "s_add_i32 s92, s92, vcc_lo\n\t" \
  "s_lshl4_add_u32 s93, s82, 64\n\t" \
  "3:\n\t" \
  "v_mov_b64 v[68:69], 0\n\t" \
  "v_mov_b64 v[70:71], 0\n\t" \
  "v_mov_b64 v[72:73], 0\n\t" \
  "v_mov_b64 v[74:75], 0\n\t" \
  "v_mov_b64 v[76:77], 0\n\t" \
  "v_mov_b64 v[78:79], 0\n\t" \
  "v_mov_b64 v[80:81], 0\n\t" \
  "v_mov_b64 v[82:83], 0\n\t" \
  "s_waitcnt lgkmcnt(0)\n\t" \
  "s_waitcnt vmcnt(3)\n\t" \
  "v_smfmac_f32_16x16x64_bf16 v[68:71], v[32:35], a[96:103], v104\n\t" \
  "ds_write_b128 v101, v[32:35] offset:4096\n\t" \
  "v_smfmac_f32_16x16x64_bf16 v[72:75], v[32:35], a[104:111], v104\n\t" \
  "v_smfmac_f32_16x16x64_bf16 v[76:79], v[32:35], v[48:55], v104\n\t" \
  "v_smfmac_f32_16x16x64_bf16 v[80:83], v[32:35], v[56:63], v104\n\t" \
  "s_waitcnt vmcnt(2)\n\t" \
  "v_smfmac_f32_16x16x64_bf16 v[68:71], v[36:39], a[112:119], v104\n\t" \
  "ds_write_b128 v101, v[36:39] offset:5120\n\t" \
  "v_smfmac_f32_16x16x64_bf16 v[72:75], v[36:39], a[120:127], v104\n\t" \
  "v_smfmac_f32_16x16x64_bf16 v[76:79], v[36:39], a[128:135], v104\n\t" \
  "v_cmp_u_f32 vcc, v68, v69\n\t" \
  "v_smfmac_f32_16x16x64_bf16 v[80:83], v[36:39], a[136:143], v104\n\t" \
  "v_cmp_u_f32 s[82:83], v70, v71\n\t" \
  "s_waitcnt vmcnt(1)\n\t" \
  "v_smfmac_f32_16x16x64_bf16 v[68:71], v[40:43], v[16:23], v104\n\t" \
  "ds_write_b128 v101, v[40:43] offset:6144\n\t" \
  "v_smfmac_f32_16x16x64_bf16 v[72:75], v[40:43], v[24:31], v104\n\t" \
  "s_or_b64 vcc, vcc, s[82:83]\n\t" \
  "s_or_b64 vcc, vcc, s[80:81]\n\t" \
  "s_waitcnt lgkmcnt(1)\n\t" \
  "v_smfmac_f32_16x16x64_bf16 v[76:79], v[40:43], a[144:151], v104\n\t" \
  "s_cbranch_scc1 8f\n\t" \
  "5:\n\t" \
  "s_barrier\n\t" \
  "4:\n\t" \
  "s_memtime s[56:57]\n\t" \
  "ds_read_b128 v[48:51], v101 offset:8192\n\t" \
  "v_smfmac_f32_16x16x64_bf16 v[80:83], v[40:43], a[152:159], v104\n\t" \
  "ds_read_b128 v[52:55], v101 offset:9216\n\t" \
  "ds_read_b128 v[56:59], v101 offset:12288\n\t" \
  "s_waitcnt vmcnt(0)\n\t" \
  "v_smfmac_f32_16x16x64_bf16 v[68:71], v[44:47], a[160:167], v104\n\t" \
  "ds_write_b128 v101, v[44:47] offset:7168\n\t" \
  "v_smfmac_f32_16x16x64_bf16 v[72:75], v[44:47], a[168:175], v104\n\t" \
  "v_smfmac_f32_16x16x64_bf16 v[76:79], v[44:47], a[176:183], v104\n\t" \
  "v_cmp_u_f32 vcc, v68, v69\n\t" \
  "v_smfmac_f32_16x16x64_bf16 v[80:83], v[44:47], a[184:191], v104\n\t" \
  "v_cmp_u_f32 s[82:83], v70, v71\n\t" \
  "s_waitcnt lgkmcnt(3)\n\t" \
  "v_smfmac_f32_16x16x64_bf16 v[68:71], v[48:51], a[192:199], v104\n\t" \
  "ds_read_b128 v[60:63], v101 offset:13312\n\t" \
  "ds_read_b128 v[64:67], v101 offset:0\n\t" \
  "v_smfmac_f32_16x16x64_bf16 v[72:75], v[48:51], a[200:207], v104\n\t" \
  "s_or_b64 vcc, vcc, s[82:83]\n\t" \
  "s_waitcnt lgkmcnt(2)\n\t" \
  "v_smfmac_f32_16x16x64_bf16 v[68:71], v[52:55], a[208:215], v104\n\t" \
  "s_cbranch_scc1 9f\n\t" \
  "6:\n\t" \
  "s_barrier\n\t" \
  "v_smfmac_f32_16x16x64_bf16 v[72:75], v[52:55], a[216:223], v104\n\t" \
  "ds_read_b128 v[48:51], v101 offset:1024\n\t" \
  "v_smfmac_f32_16x16x64_bf16 v[68:71], v[56:59], v[152:159], v104\n\t" \
  "ds_read_b128 v[52:55], v101 offset:10240\n\t" \
  "v_smfmac_f32_16x16x64_bf16 v[72:75], v[56:59], v[160:167], v104\n\t" \
  "s_waitcnt lgkmcnt(3)\n\t" \
  "v_smfmac_f32_16x16x64_bf16 v[68:71], v[60:63], v[168:175], v104\n\t" \
  "ds_read_b128 v[56:59], v101 offset:11264\n\t" \
  "v_smfmac_f32_16x16x64_bf16 v[72:75], v[60:63], v[176:183], v104\n\t" \
  "s_waitcnt lgkmcnt(3)\n\t" \
  "v_smfmac_f32_16x16x64_bf16 v[68:71], v[64:67], a[0:7], v104\n\t" \
  "ds_read_b128 v[60:63], v101 offset:14336\n\t" \
  "v_smfmac_f32_16x16x64_bf16 v[72:75], v[64:67], a[8:15], v104\n\t" \
  "s_waitcnt lgkmcnt(3)\n\t" \
  "v_smfmac_f32_16x16x64_bf16 v[68:71], v[48:51], a[16:23], v104\n\t" \
  "ds_read_b128 v[64:67], v101 offset:15360\n\t" \
  "ds_read_b128 v[16:19], v102 offset:16384\n\t" \
  "v_smfmac_f32_16x16x64_bf16 v[72:75], v[48:51], a[24:31], v104\n\t" \
  "s_waitcnt lgkmcnt(4)\n\t" \
  "v_smfmac_f32_16x16x64_bf16 v[68:71], v[52:55], v[84:91], v104\n\t" \
  "ds_read_b128 v[48:51], v101 offset:3072\n\t" \
  "ds_read_b128 v[20:23], v102 offset:17408\n\t" \
  "v_smfmac_f32_16x16x64_bf16 v[72:75], v[52:55], v[92:99], v104\n\t" \
  "s_waitcnt lgkmcnt(5)\n\t" \
  "v_smfmac_f32_16x16x64_bf16 v[68:71], v[56:59], v[120:127], v104\n\t" \
  "ds_read_b128 v[52:55], v101 offset:2048\n\t" \
  "ds_read_b128 v[24:27], v102 offset:18432\n\t" \
  "v_smfmac_f32_16x16x64_bf16 v[72:75], v[56:59], v[128:135], v104\n\t" \
  "ds_read_b128 v[28:31], v102 offset:19456\n\t" \
  "buffer_load_dwordx4 v[84:87], v107, %[ru], s94 offen offset:0 sc1\n\t" \
  "s_waitcnt lgkmcnt(7)\n\t" \
  "v_smfmac_f32_16x16x64_bf16 v[68:71], v[60:63], v[0:7], v104\n\t" \
  "v_smfmac_f32_16x16x64_bf16 v[72:75], v[60:63], v[8:15], v104\n\t" \
  "buffer_load_dwordx4 v[88:91], v107, %[ru], s94 offen offset:1024 sc1\n\t" \
  "s_waitcnt lgkmcnt(6)\n\t" \
  "v_smfmac_f32_16x16x64_bf16 v[68:71], v[64:67], v[216:223], v104\n\t" \
  "ds_read_b128 v[0:3], v103 offset:0\n\t" \
  "v_smfmac_f32_16x16x64_bf16 v[72:75], v[64:67], v[224:231], v104\n\t" \
  "ds_read_b128 v[4:7], v103 offset:1024\n\t" \
  "buffer_load_dwordx4 v[92:95], v107, %[ru], s94 offen offset:2048 sc1\n\t" \
  "s_waitcnt lgkmcnt(6)\n\t" \
  "v_smfmac_f32_16x16x64_bf16 v[68:71], v[48:51], a[64:71], v104\n\t" \
  "s_waitcnt lgkmcnt(4)\n\t" \
  "v_smfmac_f32_16x16x64_bf16 v[68:71], v[52:55], v[16:23], v104\n\t" \
  "ds_read_b128 v[8:11], v103 offset:2048\n\t" \
  "buffer_load_dwordx4 v[96:99], v107, %[ru], s94 offen offset:3072 sc1\n\t" \
  "v_smfmac_f32_16x16x64_bf16 v[72:75], v[48:51], a[72:79], v104\n\t" \
  "ds_read_b128 v[12:15], v103 offset:3072\n\t" \
  "s_waitcnt lgkmcnt(4)\n\t" \
  "v_smfmac_f32_16x16x64_bf16 v[72:75], v[52:55], v[24:31], v104\n\t" \
  "v_add_f32 v44, v68, v70\n\t" \
  "v_smfmac_f32_16x16x64_bf16 v[76:79], v[56:59], v[136:143], v104\n\t" \
  "v_add_f32 v45, v69, v71\n\t" \
  "v_cvt_pk_bf16_f32 v44, v44, v45\n\t" \
  "v_smfmac_f32_16x16x64_bf16 v[80:83], v[56:59], v[144:151], v104\n\t" \
  "v_add_f32 v46, v72, v74\n\t" \
  "v_add_f32 v47, v73, v75\n\t" \
  "v_smfmac_f32_16x16x64_bf16 v[76:79], v[60:63], v[200:207], v104\n\t" \
  "v_cvt_pk_bf16_f32 v46, v46, v47\n\t" \
  "ds_write2st64_b32 v105, v44, v46 offset1:1\n\t" \
  "v_smfmac_f32_16x16x64_bf16 v[80:83], v[60:63], v[208:215], v104\n\t" \
  "ds_read_b128 v[56:59], v101 offset:8192\n\t" \
  "v_smfmac_f32_16x16x64_bf16 v[76:79], v[64:67], v[232:239], v104\n\t" \
  "ds_read_b128 v[16:19], v103 offset:32768\n\t" \
  "v_smfmac_f32_16x16x64_bf16 v[80:83], v[64:67], v[240:247], v104\n\t" \
  "ds_read_b128 v[20:23], v103 offset:33792\n\t" \
  "v_smfmac_f32_16x16x64_bf16 v[76:79], v[48:51], a[80:87], v104\n\t" \
  "s_waitcnt lgkmcnt(3)\n\t" \
  "s_barrier\n\t" \
  "v_smfmac_f32_16x16x64_bf16 v[80:83], v[48:51], a[88:95], v104\n\t" \
  "global_load_dword v119, v109, %[kc] sc1\n\t" \
  "v_smfmac_f32_16x16x64_bf16 v[76:79], v[52:55], a[48:55], v104\n\t" \
  "ds_read_b128 v[60:63], v101 offset:1024\n\t" \
  "ds_read_b128 v[24:27], v103 offset:34816\n\t" \
  "v_smfmac_f32_16x16x64_bf16 v[80:83], v[52:55], a[56:63], v104\n\t" \
  "ds_read_b128 v[64:67], v101 offset:10240\n\t" \
  "s_waitcnt lgkmcnt(5)\n\t" \
  "v_smfmac_f32_16x16x64_bf16 v[76:79], v[56:59], v[0:7], v104\n\t" \
  "ds_read_b128 v[48:51], v101 offset:12288\n\t" \
  "ds_read_b128 v[28:31], v103 offset:35840\n\t" \
  "v_smfmac_f32_16x16x64_bf16 v[80:83], v[56:59], v[8:15], v104\n\t" \
  "buffer_load_dwordx4 v[32:35], v100, %[ru], s47 offen offset:0 sc1\n\t" \
  "s_waitcnt lgkmcnt(4)\n\t" \
  "v_smfmac_f32_16x16x64_bf16 v[76:79], v[60:63], a[32:39], v104\n\t" \
  "ds_read_b128 v[52:55], v101 offset:9216\n\t" \
  "v_smfmac_f32_16x16x64_bf16 v[80:83], v[60:63], a[40:47], v104\n\t" \
  "ds_read_b128 v[0:3], v102 offset:0\n\t" \
  "s_waitcnt lgkmcnt(4)\n\t" \
  "v_smfmac_f32_16x16x64_bf16 v[76:79], v[64:67], a[240:247], v104\n\t" \
  "ds_read_b128 v[56:59], v101 offset:13312\n\t" \
  "buffer_load_dwordx4 v[36:39], v100, %[ru], s47 offen offset:1024 sc1\n\t" \
  "v_smfmac_f32_16x16x64_bf16 v[80:83], v[64:67], a[248:255], v104\n\t" \
  "ds_read_b128 v[60:63], v101 offset:0\n\t" \
  "ds_read_b128 v[4:7], v102 offset:1024\n\t" \
  "s_waitcnt lgkmcnt(6)\n\t" \
  "v_smfmac_f32_16x16x64_bf16 v[76:79], v[48:51], v[16:23], v104\n\t" \
  "ds_read_b128 v[8:11], v102 offset:2048\n\t" \
  "s_waitcnt lgkmcnt(6)\n\t" \
  "v_smfmac_f32_16x16x64_bf16 v[80:83], v[48:51], v[24:31], v104\n\t" \
  "ds_read_b128 v[12:15], v102 offset:3072\n\t" \
  "buffer_load_dwordx4 v[40:43], v100, %[ru], s47 offen offset:2048 sc1\n\t" \
  "s_waitcnt lgkmcnt(6)\n\t" \
  "v_smfmac_f32_16x16x64_bf16 v[76:79], v[52:55], a[224:231], v104\n\t" \
  "v_smfmac_f32_16x16x64_bf16 v[80:83], v[52:55], a[232:239], v104\n\t" \
  "s_waitcnt lgkmcnt(4)\n\t" \
  "v_smfmac_f32_16x16x64_bf16 v[76:79], v[56:59], v[184:191], v104\n\t" \
  "s_waitcnt lgkmcnt(2)\n\t" \
  "v_smfmac_f32_16x16x64_bf16 v[76:79], v[60:63], v[0:7], v104\n\t" \
  "v_smfmac_f32_16x16x64_bf16 v[80:83], v[56:59], v[192:199], v104\n\t" \
  "s_waitcnt lgkmcnt(0)\n\t" \
  "v_smfmac_f32_16x16x64_bf16 v[80:83], v[60:63], v[8:15], v104\n\t" \
  "v_add_f32 v20, v76, v78\n\t" \
  "v_add_f32 v21, v77, v79\n\t" \
  "v_cvt_pk_bf16_f32 v20, v20, v21\n\t" \
  "ds_write_b32 v106, v20\n\t" \
  "s_nop 3\n\t" \
  "v_add_f32 v23, v80, v82\n\t" \
  "v_add_f32 v21, v81, v83\n\t" \
  "v_cvt_pk_bf16_f32 v21, v23, v21\n\t" \
  "ds_write_b32 v106, v21 offset:256\n\t" \
  "s_waitcnt lgkmcnt(0)\n\t" \
  "s_barrier\n\t" \
  "v_bfe_i32 v64, v114, 0, 1\n\t" \
  "v_and_b32 v64, 0xff800000, v64\n\t" \
  "v_bfe_i32 v65, v114, 1, 1\n\t" \
  "v_and_b32 v65, 0xff800000, v65\n\t" \
  "v_bfe_i32 v66, v114, 2, 1\n\t" \
  "v_and_b32 v66, 0xff800000, v66\n\t" \
  "v_bfe_i32 v67, v114, 3, 1\n\t" \
  "v_and_b32 v67, 0xff800000, v67\n\t" \
  "v_bfe_i32 v68, v114, 4, 1\n\t" \
  "v_and_b32 v68, 0xff800000, v68\n\t" \
  "v_bfe_i32 v69, v114, 5, 1\n\t" \
  "v_and_b32 v69, 0xff800000, v69\n\t" \
  "v_bfe_i32 v70, v114, 6, 1\n\t" \
  "v_and_b32 v70, 0xff800000, v70\n\t" \
  "v_bfe_i32 v71, v114, 7, 1\n\t" \
  "v_and_b32 v71, 0xff800000, v71\n\t" \
  "s_lshl_b32 s94, s45, 4\n\t" \
  "s_and_b32 s96, s44, 2\n\t" \
  "s_lshl_b32 s96, s96, 13\n\t" \
  "s_and_b32 s76, s44, 1\n\t" \
  "s_lshl_b32 s76, s76, 12\n\t" \
  "s_add_i32 s96, s96, s76\n\t" \
  "s_mov_b32 s95, 1\n\t" \
  "s_sleep 1\n\t" \
  "ds_read_b128 v[0:3], v101 offset:20480\n\t" \
  "ds_read_b128 v[4:7], v101 offset:21504\n\t" \
  "ds_read_b128 v[8:11], v101 offset:22528\n\t" \
  "ds_read_b128 v[12:15], v101 offset:23552\n\t" \
  "ds_read_b128 v[16:19], v101 offset:24576\n\t" \
  "ds_read_b128 v[20:23], v101 offset:25600\n\t" \
  "ds_read_b128 v[24:27], v101 offset:26624\n\t" \
  "ds_read_b128 v[28:31], v101 offset:27648\n\t" \
  "ds_read_b64_tr_b16 v[48:49], v108 offset:0\n\t" \
  "ds_read_b64_tr_b16 v[50:51], v108 offset:64\n\t" \
  "ds_read_b64_tr_b16 v[52:53], v108 offset:512\n\t" \
  "ds_read_b64_tr_b16 v[54:55], v108 offset:576\n\t" \
  "ds_read_b64_tr_b16 v[56:57], v108 offset:1024\n\t" \
  "ds_read_b64_tr_b16 v[58:59], v108 offset:1088\n\t" \
  "ds_read_b64_tr_b16 v[60:61], v108 offset:1536\n\t" \
  "ds_read_b64_tr_b16 v[62:63], v108 offset:1600\n\t" \
  "s_waitcnt vmcnt(4) lgkmcnt(8)\n\t" \
  "v_mfma_f32_16x16x32_bf16 v[64:67], v[0:3], v[84:87], v[64:67]\n\t" \
  "v_mfma_f32_16x16x32_bf16 v[68:71], v[16:19], v[84:87], v[68:71]\n\t" \
  "v_mfma_f32_16x16x32_bf16 v[64:67], v[4:7], v[88:91], v[64:67]\n\t" \
  "v_mfma_f32_16x16x32_bf16 v[68:71], v[20:23], v[88:91], v[68:71]\n\t" \
  "v_mfma_f32_16x16x32_bf16 v[64:67], v[8:11], v[92:95], v[64:67]\n\t" \
  "v_mfma_f32_16x16x32_bf16 v[68:71], v[24:27], v[92:95], v[68:71]\n\t" \
  "v_mfma_f32_16x16x32_bf16 v[64:67], v[12:15], v[96:99], v[64:67]\n\t" \
  "v_mfma_f32_16x16x32_bf16 v[68:71], v[28:31], v[96:99], v[68:71]\n\t" \
  "s_waitcnt lgkmcnt(0)\n\t" \
  "v_mfma_f32_16x16x32_bf16 v[72:75], v[48:51], v[84:87], 0\n\t" \
  "v_mfma_f32_16x16x32_bf16 v[72:75], v[52:55], v[88:91], v[72:75]\n\t" \
  "v_mfma_f32_16x16x32_bf16 v[72:75], v[56:59], v[92:95], v[72:75]\n\t" \
  "v_mfma_f32_16x16x32_bf16 v[72:75], v[60:63], v[96:99], v[72:75]\n\t" \
  "s_barrier\n\t" \
  "v_max3_f32 v76, v64, v65, v66\n\t" \
  "v_max3_f32 v77, v67, v68, v69\n\t" \
  "v_max3_f32 v76, v76, v70, v71\n\t" \
  "s_nop 4\n\t" \
  "v_max3_f32 v77, v77, v72, v73\n\t" \
  "v_max3_f32 v76, v76, v74, v75\n\t" \
  "v_max_f32 v76, v76, v77\n\t" \
  "v_mov_b32 v77, v76\n\t" \
  "s_nop 1\n\t" \
  "v_permlane16_swap_b32 v76, v77\n\t" \
  "v_max_f32 v76, v76, v77\n\t" \
  "v_mov_b32 v77, v76\n\t" \
  "s_nop 1\n\t" \
  "v_permlane32_swap_b32 v76, v77\n\t" \
  "v_max_f32 v76, v76, v77\n\t" \
  "v_mul_f32 v76, %[cs], v76\n\t" \
  "v_fma_f32 v64, v64, %[cs], -v76\n\t" \
  "v_fma_f32 v65, v65, %[cs], -v76\n\t" \
  "v_fma_f32 v66, v66, %[cs], -v76\n\t" \
  "v_fma_f32 v67, v67, %[cs], -v76\n\t" \
  "v_fma_f32 v68, v68, %[cs], -v76\n\t" \
  "v_fma_f32 v69, v69, %[cs], -v76\n\t" \
  "v_fma_f32 v70, v70, %[cs], -v76\n\t" \
  "v_fma_f32 v71, v71, %[cs], -v76\n\t" \
  "v_fma_f32 v72, v72, %[cs], -v76\n\t" \
  "v_fma_f32 v73, v73, %[cs], -v76\n\t" \
  "v_fma_f32 v74, v74, %[cs], -v76\n\t" \
  "v_fma_f32 v75, v75, %[cs], -v76\n\t" \
  "v_exp_f32 v64, v64\n\t" \
  "v_exp_f32 v65, v65\n\t" \
  "v_exp_f32 v66, v66\n\t" \
  "v_exp_f32 v67, v67\n\t" \
  "v_exp_f32 v68, v68\n\t" \
  "v_exp_f32 v69, v69\n\t" \
  "v_exp_f32 v70, v70\n\t" \
  "v_exp_f32 v71, v71\n\t" \
  "v_exp_f32 v72, v72\n\t" \
  "v_exp_f32 v73, v73\n\t" \
  "v_exp_f32 v74, v74\n\t" \
  "v_exp_f32 v75, v75\n\t" \
  "v_add_f32 v76, v72, v73\n\t" \
  "v_add_f32 v77, v74, v75\n\t" \
  "v_add_f32 v78, v64, v65\n\t" \
  "v_add_f32 v79, v66, v67\n\t" \
  "v_add_f32 v76, v76, v77\n\t" \
  "v_add_f32 v78, v78, v68\n\t" \
  "v_add_f32 v79, v79, v69\n\t" \
  "v_add_f32 v78, v78, v70\n\t" \
  "v_add_f32 v79, v79, v71\n\t" \
  "v_fma_f32 v76, v76, 0.5, v78\n\t" \
  "v_add_f32 v76, v76, v79\n\t" \
  "v_mov_b32 v77, v76\n\t" \
  "s_nop 1\n\t" \
  "v_permlane16_swap_b32 v76, v77\n\t" \
  "v_add_f32 v76, v76, v77\n\t" \
  "v_mov_b32 v77, v76\n\t" \
  "s_nop 1\n\t" \
  "v_permlane32_swap_b32 v76, v77\n\t" \
  "v_add_f32 v76, v76, v77\n\t" \
  "v_rcp_f32 v76, v76\n\t" \
  "s_nop 0\n\t" \
  "v_mul_f32 v64, v64, v76\n\t" \
  "v_mul_f32 v65, v65, v76\n\t" \
  "v_mul_f32 v66, v66, v76\n\t" \
  "v_mul_f32 v67, v67, v76\n\t" \
  "v_mul_f32 v68, v68, v76\n\t" \
  "v_mul_f32 v69, v69, v76\n\t" \
  "v_mul_f32 v70, v70, v76\n\t" \
  "v_mul_f32 v71, v71, v76\n\t" \
  "v_mul_f32 v72, v72, v76\n\t" \
  "v_mul_f32 v73, v73, v76\n\t" \
  "v_mul_f32 v74, v74, v76\n\t" \
  "v_mul_f32 v75, v75, v76\n\t" \
  "v_cvt_pk_bf16_f32 v0, v64, v68\n\t" \
  "v_cvt_pk_bf16_f32 v1, v65, v69\n\t" \
  "v_cvt_pk_bf16_f32 v2, v66, v70\n\t" \
  "v_cvt_pk_bf16_f32 v3, v67, v71\n\t" \
  "v_cvt_pk_bf16_f32 v4, v72, v73\n\t" \
  "v_cvt_pk_bf16_f32 v5, v74, v75\n\t" \
  "v_permlane32_swap_b32 v0, v2\n\t" \
  "v_permlane32_swap_b32 v1, v3\n\t" \
  "v_mov_b64 v[6:7], v[4:5]\n\t" \
  "s_mov_b32 exec_hi, 0\n\t" \
  "buffer_load_dword v118, v112, %[rh], s96 offen sc1\n\t" \
  "s_mov_b64 exec, -1\n\t" \
  "buffer_store_dwordx4 v[0:3], v110, %[rh], s94 offen sc1\n\t" \
  "v_permlane16_swap_b32 v4, v6\n\t" \
  "v_permlane16_swap_b32 v5, v7\n\t" \
  "buffer_store_dwordx4 v[4:7], v111, %[rh], s94 offen sc1\n\t" \
  "s_waitcnt vmcnt(3)\n\t" \
  "ds_write_b128 v101, v[32:35] offset:20480\n\t" \
  "ds_write_b128 v101, v[36:39] offset:21504\n\t" \
  "ds_write_b128 v101, v[40:43] offset:22528\n\t" \
  "s_add_i32 s77, s44, 1\n\t" \
  "s_min_u32 s77, s77, 63\n\t" \
  "v_cmp_gt_i32 vcc, s77, v119\n\t" \
  "s_cmp_lg_u64 vcc, 0\n\t" \
  "s_cselect_b32 s42, 32, 0\n\t" \
  "s_add_i32 s41, s56, 1950\n\t" \
  "s_sub_i32 s76, s92, s51\n\t" \
  "s_sub_i32 s77, s93, s53\n\t" \
  "s_cmp_eq_u32 s76, 0\n\t" \
  "s_cbranch_scc1 21f\n\t" \
  "v_cvt_f32_u32 v117, s76\n\t" \
  "v_cvt_f32_u32 v116, s77\n\t" \
  "v_rcp_f32 v117, v117\n\t" \
  "s_mov_b32 s51, s92\n\t" \
  "v_mul_f32 v117, v117, v116\n\t" \
  "v_max_f32 v117, 0x3f028f5c, v117\n\t" \
  "v_min_f32 v117, 0x3ffeb852, v117\n\t" \
  "v_rcp_f32 v116, v117\n\t" \
  "v_mul_f32 v117, 0x4f000000, v117\n\t" \
  "v_mul_f32 v116, 0x47800000, v116\n\t" \
  "v_cvt_u32_f32 v117, v117\n\t" \
  "v_cvt_u32_f32 v116, v116\n\t" \
  "s_mov_b32 s53, s93\n\t" \
  "v_readfirstlane_b32 s46, v117\n\t" \
  "v_readfirstlane_b32 s52, v116\n\t" \
  "21:\n\t" \
  "s_add_i32 s44, s44, 1\n\t" \
  "s_and_b32 s58, s58, 4\n\t" \
  "s_and_b32 s76, s44, 64\n\t" \
  "s_or_b32 s42, s42, s76\n\t" \
  "s_cbranch_scc0 20b\n\t" \
  "s_branch 99f\n\t" \
  "14:\n\t" \
  "s_sleep 3\n\t" \
  "s_add_u32 s59, s59, 0x10000\n\t" \
  "s_cmp_lt_u32 s59, 0x100000\n\t" \
  "s_cbranch_scc1 13b\n\t" \
  "s_mov_b64 exec, -1\n\t" \
  "s_mov_b32 s42, 16\n\t" \
  "s_branch 99f\n\t" \
  "8:\n\t" \
  "s_bitcmp1_b32 s58, 1\n\t" \
  "s_cbranch_scc1 4b\n\t" \
  "s_bitcmp1_b32 s58, 2\n\t" \
  "s_cbranch_scc1 5b\n\t" \
  "v_max3_u32 v116, v32, v33, v34\n\t" \
  "v_max3_u32 v117, v35, v36, v37\n\t" \
  "v_max3_u32 v116, v116, v38, v39\n\t" \
  "v_max_u32 v116, v116, v117\n\t" \
  "v_cmp_eq_u32 vcc, -1, v116\n\t" \
  "s_cbranch_vccz 5b\n\t" \
  "s_branch 0f\n\t" \
  "9:\n\t" \
  "s_bitcmp1_b32 s58, 2\n\t" \
  "s_cbranch_scc1 6b\n\t" \
  "v_max3_u32 v116, v40, v41, v42\n\t" \
  "v_max3_u32 v117, v43, v44, v45\n\t" \
  "v_max3_u32 v116, v116, v46, v47\n\t" \
  "v_max_u32 v116, v116, v117\n\t" \
  "v_cmp_eq_u32 vcc, -1, v116\n\t" \
  "s_cbranch_vccz 6b\n\t" \
  "s_or_b32 s58, s58, 2\n\t" \
  "0:\n\t" \
  "s_or_b32 s58, s58, 1\n\t" \
  "11:\n\t" \
  "buffer_load_dwordx4 v[32:35], v100, %[rh], s40 offen sc1\n\t" \
  "buffer_load_dwordx4 v[36:39], v100, %[rh], s76 offen sc1\n\t" \
  "buffer_load_dwordx4 v[40:43], v100, %[rh], s77 offen sc1\n\t" \
  "buffer_load_dwordx4 v[44:47], v100, %[rh], s78 offen sc1\n\t" \
  "s_waitcnt lgkmcnt(0)\n\t" \
  "s_branch 10b\n\t" \
  "17:\n\t" \
  "s_add_u32 s59, s59, 1\n\t" \
  "s_bitcmp1_b32 s58, 2\n\t" \
  "s_cbranch_scc1 3b\n\t" \
  "s_waitcnt vmcnt(0)\n\t" \
  "v_max_u32 v116, v32, v33\n\t" \
  "v_max3_u32 v116, v116, v34, v35\n\t" \
  "v_max3_u32 v116, v116, v36, v37\n\t" \
  "v_max3_u32 v116, v116, v38, v39\n\t" \
  "v_max3_u32 v116, v116, v40, v41\n\t" \
  "v_max3_u32 v116, v116, v42, v43\n\t" \
  "v_max3_u32 v116, v116, v44, v45\n\t" \
  "v_max3_u32 v116, v116, v46, v47\n\t" \
  "v_cmp_eq_u32 vcc, -1, v116\n\t" \
  "s_cbranch_vccz 3b\n\t" \
  "s_bitcmp1_b32 s59, 6\n\t" \
  "s_cbranch_scc1 12f\n\t" \
  "buffer_load_dwordx4 v[32:35], v100, %[rh], s40 offen sc1\n\t" \
  "buffer_load_dwordx4 v[36:39], v100, %[rh], s76 offen sc1\n\t" \
  "buffer_load_dwordx4 v[40:43], v100, %[rh], s77 offen sc1\n\t" \
  "buffer_load_dwordx4 v[44:47], v100, %[rh], s78 offen sc1\n\t" \
  "s_branch 17b\n\t" \
  "12:\n\t" \
  "s_mov_b32 s42, s58\n\t" \
  "s_branch 99f\n\t" \
  "92:\n\t" \
  "s_mov_b32 s58, s42\n\t" \
  "s_mov_b32 s95, 0\n\t" \
  "20:\n\t" \
  "s_and_b32 s76, s44, 7\n\t" \
  "s_lshl_b32 s76, s76, 15\n\t" \
  "s_add_i32 s40, s54, s76\n\t" \
  "s_and_b32 s76, s44, 3\n\t" \
  "s_lshl_b32 s45, s76, 14\n\t" \
  "s_add_i32 s47, s44, 1\n\t" \
  "s_min_u32 s47, s47, 63\n\t" \
  "s_lshr_b32 s76, s47, 1\n\t" \
  "s_lshl_b32 s47, s47, 20\n\t" \
  "s_add_i32 s47, s47, s55\n\t" \
  "s_bitcmp1_b64 %[sel], s76\n\t" \
  "s_cselect_b32 s76, 0x2000, 0\n\t" \
  "s_add_i32 s47, s47, s76\n\t" \
  "s_lshr_b32 s76, s44, 1\n\t" \
  "s_lshl_b32 s94, s44, 20\n\t" \
  "s_bitcmp1_b64 %[sel], s76\n\t" \
  "s_cselect_b32 s76, 0x2000, 0\n\t" \
  "s_add_i32 s94, s94, s76\n\t" \
  "s_add_i32 s49, s44, -1\n\t" \
  "s_and_b32 s48, s49, 2\n\t" \
  "s_lshl_b32 s48, s48, 13\n\t" \
  "s_and_b32 s76, s49, 1\n\t" \
  "s_lshl_b32 s76, s76, 12\n\t" \
  "s_add_i32 s48, s48, s76\n\t" \
  "s_lshl_b32 s50, s53, 8\n\t" \
  "s_mov_b32 s59, 0\n\t" \
  "s_mov_b32 s92, s51\n\t" \
  "s_mov_b32 s93, s53\n\t" \
  "s_xor_b32 s76, s40, 0x400\n\t" \
  "s_xor_b32 s77, s40, 0x800\n\t" \
  "s_xor_b32 s78, s40, 0xc00\n\t" \
  "s_cmp_lg_u32 s58, 0\n\t" \
  "s_cbranch_scc1 11f\n\t" \
  "10:\n\t" \
  "ds_read_b128 v[48:51], v103 offset:0\n\t" \
  "ds_read_b128 v[52:55], v103 offset:1024\n\t" \
  "ds_read_b128 v[56:59], v103 offset:2048\n\t" \
  "ds_read_b128 v[60:63], v103 offset:3072\n\t" \
  "ds_read_b128 v[16:19], v103 offset:16384\n\t" \
  "ds_read_b128 v[20:23], v103 offset:17408\n\t" \
  "ds_read_b128 v[24:27], v103 offset:18432\n\t" \
  "ds_read_b128 v[28:31], v103 offset:19456\n\t" \
  "ds_read_b128 v[84:87], v103 offset:49152\n\t" \
  "ds_read_b128 v[88:91], v103 offset:50176\n\t" \
  "ds_read_b128 v[92:95], v103 offset:51200\n\t" \
  "ds_read_b128 v[96:99], v103 offset:52224\n\t" \
  "s_waitcnt lgkmcnt(4)\n\t" \
  "ds_read_b128 v[0:3], v102 offset:16384\n\t" \
  "ds_read_b128 v[4:7], v102 offset:17408\n\t" \
  "ds_read_b128 v[8:11], v102 offset:18432\n\t" \
  "ds_read_b128 v[12:15], v102 offset:19456\n\t" \
  "s_bitcmp1_b32 s58, 1\n\t" \
  "s_cselect_b64 s[80:81], -1, 0\n\t" \
  "s_cmp_lg_u32 s58, 0\n\t" \
  "s_cbranch_scc1 17f\n\t" \
  "s_mov_b32 exec_hi, 0\n\t" \
  "s_cmp_eq_u32 s95, 0\n\t" \
  "s_cbranch_scc1 16f\n\t" \
  "s_waitcnt vmcnt(2)\n\t" \
  "v_mov_b32 v116, v118\n\t" \
  "s_branch 15f\n\t" \
  "16:\n\t" \
  "s_memtime vcc\n\t" \
  "s_waitcnt lgkmcnt(0)\n\t" \
  "s_sub_i32 s79, s41, vcc_lo\n\t" \
  "s_cmp_gt_i32 s79, 64\n\t" \
  "s_cbranch_scc1 16b\n\t" \
  "13:\n\t" \
  "buffer_load_dword v116, v112, %[rh], s48 offen sc1\n\t" \
  "s_waitcnt vmcnt(0)\n\t" \
  "15:\n\t" \
  "v_mov_b32 v117, 20000\n\t" \
  "v_cmp_ne_u32_sdwa vcc, v116, s49 src0_sel:BYTE_0 src1_sel:DWORD\n\t" \
  "v_subrev_u32 v116, s50, v116\n\t" \
  "v_ashrrev_i32 v116, 8, v116\n\t" \
  "s_nop 1\n\t" \
  "v_max_i32_dpp v116, v116, v116 quad_perm:[1,0,3,2] row_mask:0xf bank_mask:0xf\n\t" \
  "s_cbranch_vccnz 14f\n\t" \
  "s_nop 0\n\t" \
  "v_max_i32_dpp v116, v116, v116 quad_perm:[2,3,0,1] row_mask:0xf bank_mask:0xf\n\t" \
  "s_nop 1\n\t" \
  "v_max_i32_dpp v116, v116, v116 row_half_mirror row_mask:0xf bank_mask:0xf\n\t" \
  "s_nop 1\n\t" \
  "v_max_i32_dpp v116, v116, v116 row_mirror row_mask:0xf bank_mask:0xf\n\t" \
  "v_med3_i32 v116, v116, 0, v117\n\t" \
  "v_mul_u32_u24 v116, s52, v116\n\t" \
  "v_lshrrev_b32 v116, 16, v116\n\t" \
  "s_mov_b64 exec, -1\n\t" \
  "v_readfirstlane_b32 s84, v116\n\t" \
  "v_readlane_b32 s85, v116, 16\n\t" \
  "s_max_u32 s84, s84, s85\n\t" \
  "s_add_i32 s84, s84, s51\n\t" \
  "1:\n\t" \
  "s_memtime vcc\n\t" \
  "s_waitcnt lgkmcnt(0)\n\t" \
  "s_sub_i32 s79, s84, vcc_lo\n\t" \
  "s_cmp_gt_i32 s79, 128\n\t" \
  "s_cbranch_scc1 1b\n\t" \
  ".p2align 4\n\t" \
  "2:\n\t" \
  "s_sub_i32 s79, s79, 33\n\t" \
  "s_cmp_gt_i32 s79, 56\n\t" \
  "s_cbranch_scc1 2b\n\t" \
  "buffer_load_dwordx4 v[32:35], v100, %[rh], s40 offen sc1\n\t" \
  "s_nop 12\n\t" \
  "buffer_load_dwordx4 v[36:39], v100, %[rh], s76 offen sc1\n\t" \
  "s_nop 12\n\t" \
  "buffer_load_dwordx4 v[40:43], v100, %[rh], s77 offen sc1\n\t" \
  "s_nop 12\n\t" \
  "buffer_load_dwordx4 v[44:47], v100, %[rh], s78 offen sc1\n\t" \
  "s_memtime vcc\n\t" \
  "s_memrealtime s[82:83]\n\t" \
  "s_nop 4\n\t" \
  "s_memrealtime s[84:85]\n\t" \
  "s_nop 4\n\t" \
  "s_memrealtime s[56:57]\n\t" \
  "s_nop 4\n\t" \
  "s_memrealtime s[86:87]\n\t" \
  "s_nop 4\n\t" \
  "s_memrealtime s[88:89]\n\t" \
  "s_nop 4\n\t" \
  "s_memrealtime s[90:91]\n\t" \
  "s_mov_b32 s92, 0\n\t" \
  "s_waitcnt lgkmcnt(0)\n\t" \
  "s_cmp_eq_u32 s82, s84\n\t" \
  "s_addc_u32 s92, s92, 0\n\t" \
  "s_cmp_eq_u32 s82, s56\n\t" \
  "s_addc_u32 s92, s92, 0\n\t" \
  "s_cmp_eq_u32 s82, s86\n\t" \
  "s_addc_u32 s92, s92, 0\n\t" \
  "s_cmp_eq_u32 s82, s88\n\t" \
  "s_addc_u32 s92, s92, 0\n\t" \
  "s_cmp_eq_u32 s82, s90\n\t" \
  "s_addc_u32 s92, s92, 0\n\t" \
  "s_mul_i32 s92, s92, 24\n\t" \
  "s_add_i32 s92, s92, vcc_lo\n\t" \
  "s_lshl4_add_u32 s93, s82, 64\n\t" \
  "3:\n\t" \
  "v_mov_b64 v[68:69], 0\n\t" \
  "v_mov_b64 v[70:71], 0\n\t" \
  "v_mov_b64 v[72:73], 0\n\t" \
  "v_mov_b64 v[74:75], 0\n\t" \
  "v_mov_b64 v[76:77], 0\n\t" \
  "v_mov_b64 v[78:79], 0\n\t" \
  "v_mov_b64 v[80:81], 0\n\t" \
  "v_mov_b64 v[82:83], 0\n\t" \
  "s_waitcnt lgkmcnt(0)\n\t" \
  "s_waitcnt vmcnt(3)\n\t" \
  "v_smfmac_f32_16x16x64_bf16 v[68:71], v[32:35], a[192:199], v104\n\t" \
  "ds_write_b128 v101, v[32:35] offset:8192\n\t" \
  "v_smfmac_f32_16x16x64_bf16 v[72:75], v[32:35], a[200:207], v104\n\t" \
  "v_smfmac_f32_16x16x64_bf16 v[76:79], v[32:35], v[48:55], v104\n\t" \
  "v_smfmac_f32_16x16x64_bf16 v[80:83], v[32:35], v[56:63], v104\n\t" \
  "s_waitcnt vmcnt(2)\n\t" \
  "v_smfmac_f32_16x16x64_bf16 v[68:71], v[36:39], a[208:215], v104\n\t" \
  "ds_write_b128 v101, v[36:39] offset:9216\n\t" \
  "v_smfmac_f32_16x16x64_bf16 v[72:75], v[36:39], a[216:223], v104\n\t" \
  "v_smfmac_f32_16x16x64_bf16 v[76:79], v[36:39], a[224:231], v104\n\t" \
  "v_cmp_u_f32 vcc, v68, v69\n\t" \
  "v_smfmac_f32_16x16x64_bf16 v[80:83], v[36:39], a[232:239], v104\n\t" \
  "v_cmp_u_f32 s[82:83], v70, v71\n\t" \
  "s_waitcnt vmcnt(1)\n\t" \
  "v_smfmac_f32_16x16x64_bf16 v[68:71], v[40:43], v[16:23], v104\n\t" \
  "ds_write_b128 v101, v[40:43] offset:10240\n\t" \
  "v_smfmac_f32_16x16x64_bf16 v[72:75], v[40:43], v[24:31], v104\n\t" \
  "s_or_b64 vcc, vcc, s[82:83]\n\t" \
  "s_or_b64 vcc, vcc, s[80:81]\n\t" \
  "s_waitcnt lgkmcnt(1)\n\t" \
  "v_smfmac_f32_16x16x64_bf16 v[76:79], v[40:43], a[240:247], v104\n\t" \
  "s_cbranch_scc1 8f\n\t" \
  "5:\n\t" \
  "s_barrier\n\t" \
  "4:\n\t" \
  "s_memtime s[56:57]\n\t" \
  "ds_read_b128 v[48:51], v101 offset:12288\n\t" \
  "v_smfmac_f32_16x16x64_bf16 v[80:83], v[40:43], a[248:255], v104\n\t" \
  "ds_read_b128 v[52:55], v101 offset:13312\n\t" \
  "ds_read_b128 v[56:59], v101 offset:0\n\t" \
  "s_waitcnt vmcnt(0)\n\t" \
  "v_smfmac_f32_16x16x64_bf16 v[68:71], v[44:47], v[120:127], v104\n\t" \
  "v_smfmac_f32_16x16x64_bf16 v[72:75], v[44:47], v[128:135], v104\n\t" \
  "ds_write_b128 v101, v[44:47] offset:11264\n\t" \
  "v_smfmac_f32_16x16x64_bf16 v[76:79], v[44:47], v[136:143], v104\n\t" \
  "v_cmp_u_f32 vcc, v68, v69\n\t" \
  "v_smfmac_f32_16x16x64_bf16 v[80:83], v[44:47], v[144:151], v104\n\t" \
  "v_cmp_u_f32 s[82:83], v70, v71\n\t" \
  "s_waitcnt lgkmcnt(3)\n\t" \
  "v_smfmac_f32_16x16x64_bf16 v[68:71], v[48:51], v[152:159], v104\n\t" \
  "ds_read_b128 v[60:63], v101 offset:1024\n\t" \
  "ds_read_b128 v[64:67], v101 offset:4096\n\t" \
  "v_smfmac_f32_16x16x64_bf16 v[72:75], v[48:51], v[160:167], v104\n\t" \
  "s_or_b64 vcc, vcc, s[82:83]\n\t" \
  "s_waitcnt lgkmcnt(2)\n\t" \
  "v_smfmac_f32_16x16x64_bf16 v[68:71], v[52:55], v[168:175], v104\n\t" \
  "s_cbranch_scc1 9f\n\t" \
  "6:\n\t" \
  "s_barrier\n\t" \
  "v_smfmac_f32_16x16x64_bf16 v[72:75], v[52:55], v[176:183], v104\n\t" \
  "ds_read_b128 v[48:51], v101 offset:5120\n\t" \
  "v_smfmac_f32_16x16x64_bf16 v[68:71], v[56:59], a[0:7], v104\n\t" \
  "ds_read_b128 v[52:55], v101 offset:14336\n\t" \
  "v_smfmac_f32_16x16x64_bf16 v[72:75], v[56:59], a[8:15], v104\n\t" \
  "s_waitcnt lgkmcnt(3)\n\t" \
  "v_smfmac_f32_16x16x64_bf16 v[68:71], v[60:63], a[16:23], v104\n\t" \
  "ds_read_b128 v[56:59], v101 offset:15360\n\t" \
  "v_smfmac_f32_16x16x64_bf16 v[72:75], v[60:63], a[24:31], v104\n\t" \
  "s_waitcnt lgkmcnt(3)\n\t" \
  "v_smfmac_f32_16x16x64_bf16 v[68:71], v[64:67], a[96:103], v104\n\t" \
  "ds_read_b128 v[60:63], v101 offset:2048\n\t" \
  "v_smfmac_f32_16x16x64_bf16 v[72:75], v[64:67], a[104:111], v104\n\t" \
  "s_waitcnt lgkmcnt(3)\n\t" \
  "v_smfmac_f32_16x16x64_bf16 v[68:71], v[48:51], a[112:119], v104\n\t" \
  "ds_read_b128 v[64:67], v101 offset:3072\n\t" \
  "ds_read_b128 v[16:19], v102 offset:49152\n\t" \
  "v_smfmac_f32_16x16x64_bf16 v[72:75], v[48:51], a[120:127], v104\n\t" \
  "s_waitcnt lgkmcnt(4)\n\t" \
  "v_smfmac_f32_16x16x64_bf16 v[68:71], v[52:55], v[84:91], v104\n\t" \
  "ds_read_b128 v[48:51], v101 offset:7168\n\t" \
  "ds_read_b128 v[20:23], v102 offset:50176\n\t" \
  "v_smfmac_f32_16x16x64_bf16 v[72:75], v[52:55], v[92:99], v104\n\t" \
  "s_waitcnt lgkmcnt(5)\n\t" \
  "v_smfmac_f32_16x16x64_bf16 v[68:71], v[56:59], v[216:223], v104\n\t" \
  "ds_read_b128 v[52:55], v101 offset:6144\n\t" \
  "ds_read_b128 v[24:27], v102 offset:51200\n\t" \
  "v_smfmac_f32_16x16x64_bf16 v[72:75], v[56:59], v[224:231], v104\n\t" \
  "ds_read_b128 v[28:31], v102 offset:52224\n\t" \
  "s_waitcnt lgkmcnt(7)\n\t" \
  "v_smfmac_f32_16x16x64_bf16 v[68:71], v[60:63], v[0:7], v104\n\t" \
  "buffer_load_dwordx4 v[84:87], v107, %[ru], s94 offen offset:0 sc1\n\t" \
  "v_smfmac_f32_16x16x64_bf16 v[72:75], v[60:63], v[8:15], v104\n\t" \
  "s_waitcnt lgkmcnt(6)\n\t" \
  "v_smfmac_f32_16x16x64_bf16 v[68:71], v[64:67], a[64:71], v104\n\t" \
  "ds_read_b128 v[0:3], v103 offset:32768\n\t" \
  "buffer_load_dwordx4 v[88:91], v107, %[ru], s94 offen offset:1024 sc1\n\t" \
  "v_smfmac_f32_16x16x64_bf16 v[72:75], v[64:67], a[72:79], v104\n\t" \
  "ds_read_b128 v[4:7], v103 offset:33792\n\t" \
  "s_waitcnt lgkmcnt(6)\n\t" \
  "v_smfmac_f32_16x16x64_bf16 v[68:71], v[48:51], a[160:167], v104\n\t" \
  "buffer_load_dwordx4 v[92:95], v107, %[ru], s94 offen offset:2048 sc1\n\t" \
  "s_waitcnt lgkmcnt(4)\n\t" \
  "v_smfmac_f32_16x16x64_bf16 v[68:71], v[52:55], v[16:23], v104\n\t" \
  "ds_read_b128 v[8:11], v103 offset:34816\n\t" \
  "v_smfmac_f32_16x16x64_bf16 v[72:75], v[48:51], a[168:175], v104\n\t" \
  "ds_read_b128 v[12:15], v103 offset:35840\n\t" \
  "buffer_load_dwordx4 v[96:99], v107, %[ru], s94 offen offset:3072 sc1\n\t" \
  "s_waitcnt lgkmcnt(4)\n\t" \
  "v_smfmac_f32_16x16x64_bf16 v[72:75], v[52:55], v[24:31], v104\n\t" \
  "v_add_f32 v44, v68, v70\n\t" \
  "v_smfmac_f32_16x16x64_bf16 v[76:79], v[56:59], v[232:239], v104\n\t" \
  "v_add_f32 v45, v69, v71\n\t" \
  "v_cvt_pk_bf16_f32 v44, v44, v45\n\t" \
  "v_smfmac_f32_16x16x64_bf16 v[80:83], v[56:59], v[240:247], v104\n\t" \
  "v_add_f32 v46, v72, v74\n\t" \
  "v_add_f32 v47, v73, v75\n\t" \
  "v_smfmac_f32_16x16x64_bf16 v[76:79], v[60:63], a[48:55], v104\n\t" \
  "v_cvt_pk_bf16_f32 v46, v46, v47\n\t" \
  "ds_write2st64_b32 v105, v44, v46 offset1:1\n\t" \
  "v_smfmac_f32_16x16x64_bf16 v[80:83], v[60:63], a[56:63], v104\n\t" \
  "ds_read_b128 v[56:59], v101 offset:12288\n\t" \
  "v_smfmac_f32_16x16x64_bf16 v[76:79], v[64:67], a[80:87], v104\n\t" \
  "ds_read_b128 v[16:19], v102 offset:0\n\t" \
  "v_smfmac_f32_16x16x64_bf16 v[80:83], v[64:67], a[88:95], v104\n\t" \
  "ds_read_b128 v[20:23], v102 offset:1024\n\t" \
  "v_smfmac_f32_16x16x64_bf16 v[76:79], v[48:51], a[176:183], v104\n\t" \
  "s_waitcnt lgkmcnt(3)\n\t" \
  "s_barrier\n\t" \
  "v_smfmac_f32_16x16x64_bf16 v[80:83], v[48:51], a[184:191], v104\n\t" \
  "v_smfmac_f32_16x16x64_bf16 v[76:79], v[52:55], a[144:151], v104\n\t" \
  "ds_read_b128 v[60:63], v101 offset:5120\n\t" \
  "ds_read_b128 v[24:27], v102 offset:2048\n\t" \
  "global_load_dword v119, v109, %[kc] sc1\n\t" \
  "v_smfmac_f32_16x16x64_bf16 v[80:83], v[52:55], a[152:159], v104\n\t" \
  "ds_read_b128 v[64:67], v101 offset:14336\n\t" \
  "s_waitcnt lgkmcnt(5)\n\t" \
  "v_smfmac_f32_16x16x64_bf16 v[76:79], v[56:59], v[0:7], v104\n\t" \
  "ds_read_b128 v[48:51], v101 offset:0\n\t" \
  "ds_read_b128 v[28:31], v102 offset:3072\n\t" \
  "v_smfmac_f32_16x16x64_bf16 v[80:83], v[56:59], v[8:15], v104\n\t" \
  "s_waitcnt lgkmcnt(4)\n\t" \
  "v_smfmac_f32_16x16x64_bf16 v[76:79], v[60:63], a[128:135], v104\n\t" \
  "ds_read_b128 v[52:55], v101 offset:13312\n\t" \
  "buffer_load_dwordx4 v[32:35], v100, %[ru], s47 offen offset:0 sc1\n\t" \
  "v_smfmac_f32_16x16x64_bf16 v[80:83], v[60:63], a[136:143], v104\n\t" \
  "ds_read_b128 v[0:3], v102 offset:32768\n\t" \
  "s_waitcnt lgkmcnt(4)\n\t" \
  "v_smfmac_f32_16x16x64_bf16 v[76:79], v[64:67], v[200:207], v104\n\t" \
  "ds_read_b128 v[56:59], v101 offset:1024\n\t" \
  "v_smfmac_f32_16x16x64_bf16 v[80:83], v[64:67], v[208:215], v104\n\t" \
  "ds_read_b128 v[60:63], v101 offset:4096\n\t" \
  "ds_read_b128 v[4:7], v102 offset:33792\n\t" \
  "buffer_load_dwordx4 v[36:39], v100, %[ru], s47 offen offset:1024 sc1\n\t" \
  "s_waitcnt lgkmcnt(6)\n\t" \
  "v_smfmac_f32_16x16x64_bf16 v[76:79], v[48:51], v[16:23], v104\n\t" \
  "ds_read_b128 v[8:11], v102 offset:34816\n\t" \
  "s_waitcnt lgkmcnt(6)\n\t" \
  "v_smfmac_f32_16x16x64_bf16 v[80:83], v[48:51], v[24:31], v104\n\t" \
  "ds_read_b128 v[12:15], v102 offset:35840\n\t" \
  "s_waitcnt lgkmcnt(6)\n\t" \
  "v_smfmac_f32_16x16x64_bf16 v[76:79], v[52:55], v[184:191], v104\n\t" \
  "buffer_load_dwordx4 v[40:43], v100, %[ru], s47 offen offset:2048 sc1\n\t" \
  "v_smfmac_f32_16x16x64_bf16 v[80:83], v[52:55], v[192:199], v104\n\t" \
  "s_waitcnt lgkmcnt(4)\n\t" \
  "v_smfmac_f32_16x16x64_bf16 v[76:79], v[56:59], a[32:39], v104\n\t" \
  "s_waitcnt lgkmcnt(2)\n\t" \
  "v_smfmac_f32_16x16x64_bf16 v[76:79], v[60:63], v[0:7], v104\n\t" \
  "v_smfmac_f32_16x16x64_bf16 v[80:83], v[56:59], a[40:47], v104\n\t" \
  "s_waitcnt lgkmcnt(0)\n\t" \
  "v_smfmac_f32_16x16x64_bf16 v[80:83], v[60:63], v[8:15], v104\n\t" \
  "v_add_f32 v20, v76, v78\n\t" \
  "v_add_f32 v21, v77, v79\n\t" \
  "v_cvt_pk_bf16_f32 v20, v20, v21\n\t" \
  "ds_write_b32 v106, v20\n\t" \
  "s_nop 3\n\t" \
  "v_add_f32 v23, v80, v82\n\t" \
  "v_add_f32 v21, v81, v83\n\t" \
  "v_cvt_pk_bf16_f32 v21, v23, v21\n\t" \
  "ds_write_b32 v106, v21 offset:256\n\t" \
  "s_waitcnt lgkmcnt(0)\n\t" \
  "s_barrier\n\t" \
  "v_bfe_i32 v64, v114, 0, 1\n\t" \
  "v_and_b32 v64, 0xff800000, v64\n\t" \
  "v_bfe_i32 v65, v114, 1, 1\n\t" \
  "v_and_b32 v65, 0xff800000, v65\n\t" \
  "v_bfe_i32 v66, v114, 2, 1\n\t" \
  "v_and_b32 v66, 0xff800000, v66\n\t" \
  "v_bfe_i32 v67, v114, 3, 1\n\t" \
  "v_and_b32 v67, 0xff800000, v67\n\t" \
  "v_bfe_i32 v68, v114, 4, 1\n\t" \
  "v_and_b32 v68, 0xff800000, v68\n\t" \
  "v_bfe_i32 v69, v114, 5, 1\n\t" \
  "v_and_b32 v69, 0xff800000, v69\n\t" \
  "v_bfe_i32 v70, v114, 6, 1\n\t" \
  "v_and_b32 v70, 0xff800000, v70\n\t" \
  "v_bfe_i32 v71, v114, 7, 1\n\t" \
  "v_and_b32 v71, 0xff800000, v71\n\t" \
  "s_lshl_b32 s94, s45, 4\n\t" \
  "s_and_b32 s96, s44, 2\n\t" \
  "s_lshl_b32 s96, s96, 13\n\t" \
  "s_and_b32 s76, s44, 1\n\t" \
  "s_lshl_b32 s76, s76, 12\n\t" \
  "s_add_i32 s96, s96, s76\n\t" \
  "s_mov_b32 s95, 1\n\t" \
  "s_sleep 1\n\t" \
  "ds_read_b128 v[0:3], v101 offset:20480\n\t" \
  "ds_read_b128 v[4:7], v101 offset:21504\n\t" \
  "ds_read_b128 v[8:11], v101 offset:22528\n\t" \
  "ds_read_b128 v[12:15], v101 offset:23552\n\t" \
  "ds_read_b128 v[16:19], v101 offset:24576\n\t" \
  "ds_read_b128 v[20:23], v101 offset:25600\n\t" \
  "ds_read_b128 v[24:27], v101 offset:26624\n\t" \
  "ds_read_b128 v[28:31], v101 offset:27648\n\t" \
  "ds_read_b64_tr_b16 v[48:49], v108 offset:0\n\t" \
  "ds_read_b64_tr_b16 v[50:51], v108 offset:64\n\t" \
  "ds_read_b64_tr_b16 v[52:53], v108 offset:512\n\t" \
  "ds_read_b64_tr_b16 v[54:55], v108 offset:576\n\t" \
  "ds_read_b64_tr_b16 v[56:57], v108 offset:1024\n\t" \
  "ds_read_b64_tr_b16 v[58:59], v108 offset:1088\n\t" \
  "ds_read_b64_tr_b16 v[60:61], v108 offset:1536\n\t" \
  "ds_read_b64_tr_b16 v[62:63], v108 offset:1600\n\t" \
  "s_waitcnt vmcnt(4) lgkmcnt(8)\n\t" \
  "v_mfma_f32_16x16x32_bf16 v[64:67], v[0:3], v[84:87], v[64:67]\n\t" \
  "v_mfma_f32_16x16x32_bf16 v[68:71], v[16:19], v[84:87], v[68:71]\n\t" \
  "v_mfma_f32_16x16x32_bf16 v[64:67], v[4:7], v[88:91], v[64:67]\n\t" \
  "v_mfma_f32_16x16x32_bf16 v[68:71], v[20:23], v[88:91], v[68:71]\n\t" \
  "v_mfma_f32_16x16x32_bf16 v[64:67], v[8:11], v[92:95], v[64:67]\n\t" \
  "v_mfma_f32_16x16x32_bf16 v[68:71], v[24:27], v[92:95], v[68:71]\n\t" \
  "v_mfma_f32_16x16x32_bf16 v[64:67], v[12:15], v[96:99], v[64:67]\n\t" \
  "v_mfma_f32_16x16x32_bf16 v[68:71], v[28:31], v[96:99], v[68:71]\n\t" \
  "s_waitcnt lgkmcnt(0)\n\t" \
  "v_mfma_f32_16x16x32_bf16 v[72:75], v[48:51], v[84:87], 0\n\t" \
  "v_mfma_f32_16x16x32_bf16 v[72:75], v[52:55], v[88:91], v[72:75]\n\t" \
  "v_mfma_f32_16x16x32_bf16 v[72:75], v[56:59], v[92:95], v[72:75]\n\t" \
  "v_mfma_f32_16x16x32_bf16 v[72:75], v[60:63], v[96:99], v[72:75]\n\t" \
  "s_barrier\n\t" \
  "v_max3_f32 v76, v64, v65, v66\n\t" \
  "v_max3_f32 v77, v67, v68, v69\n\t" \
  "v_max3_f32 v76, v76, v70, v71\n\t" \
  "s_nop 4\n\t" \
  "v_max3_f32 v77, v77, v72, v73\n\t" \
  "v_max3_f32 v76, v76, v74, v75\n\t" \
  "v_max_f32 v76, v76, v77\n\t" \
  "v_mov_b32 v77, v76\n\t" \
  "s_nop 1\n\t" \
  "v_permlane16_swap_b32 v76, v77\n\t" \
  "v_max_f32 v76, v76, v77\n\t" \
  "v_mov_b32 v77, v76\n\t" \
  "s_nop 1\n\t" \
  "v_permlane32_swap_b32 v76, v77\n\t" \
  "v_max_f32 v76, v76, v77\n\t" \
  "v_mul_f32 v76, %[cs], v76\n\t" \
  "v_fma_f32 v64, v64, %[cs], -v76\n\t" \
  "v_fma_f32 v65, v65, %[cs], -v76\n\t" \
  "v_fma_f32 v66, v66, %[cs], -v76\n\t" \
  "v_fma_f32 v67, v67, %[cs], -v76\n\t" \
  "v_fma_f32 v68, v68, %[cs], -v76\n\t" \
  "v_fma_f32 v69, v69, %[cs], -v76\n\t" \
  "v_fma_f32 v70, v70, %[cs], -v76\n\t" \
  "v_fma_f32 v71, v71, %[cs], -v76\n\t" \
  "v_fma_f32 v72, v72, %[cs], -v76\n\t" \
  "v_fma_f32 v73, v73, %[cs], -v76\n\t" \
  "v_fma_f32 v74, v74, %[cs], -v76\n\t" \
  "v_fma_f32 v75, v75, %[cs], -v76\n\t" \
  "v_exp_f32 v64, v64\n\t" \
  "v_exp_f32 v65, v65\n\t" \
  "v_exp_f32 v66, v66\n\t" \
  "v_exp_f32 v67, v67\n\t" \
  "v_exp_f32 v68, v68\n\t" \
  "v_exp_f32 v69, v69\n\t" \
  "v_exp_f32 v70, v70\n\t" \
  "v_exp_f32 v71, v71\n\t" \
  "v_exp_f32 v72, v72\n\t" \
  "v_exp_f32 v73, v73\n\t" \
  "v_exp_f32 v74, v74\n\t" \
  "v_exp_f32 v75, v75\n\t" \
  "v_add_f32 v76, v72, v73\n\t" \
  "v_add_f32 v77, v74, v75\n\t" \
  "v_add_f32 v78, v64, v65\n\t" \
  "v_add_f32 v79, v66, v67\n\t" \
  "v_add_f32 v76, v76, v77\n\t" \
  "v_add_f32 v78, v78, v68\n\t" \
  "v_add_f32 v79, v79, v69\n\t" \
  "v_add_f32 v78, v78, v70\n\t" \
  "v_add_f32 v79, v79, v71\n\t" \
  "v_fma_f32 v76, v76, 0.5, v78\n\t" \
  "v_add_f32 v76, v76, v79\n\t" \
  "v_mov_b32 v77, v76\n\t" \
  "s_nop 1\n\t" \
  "v_permlane16_swap_b32 v76, v77\n\t" \
  "v_add_f32 v76, v76, v77\n\t" \
  "v_mov_b32 v77, v76\n\t" \
  "s_nop 1\n\t" \
  "v_permlane32_swap_b32 v76, v77\n\t" \
  "v_add_f32 v76, v76, v77\n\t" \
  "v_rcp_f32 v76, v76\n\t" \
  "s_nop 0\n\t" \
  "v_mul_f32 v64, v64, v76\n\t" \
  "v_mul_f32 v65, v65, v76\n\t" \
  "v_mul_f32 v66, v66, v76\n\t" \
  "v_mul_f32 v67, v67, v76\n\t" \
  "v_mul_f32 v68, v68, v76\n\t" \
  "v_mul_f32 v69, v69, v76\n\t" \
  "v_mul_f32 v70, v70, v76\n\t" \
  "v_mul_f32 v71, v71, v76\n\t" \
  "v_mul_f32 v72, v72, v76\n\t" \
  "v_mul_f32 v73, v73, v76\n\t" \
  "v_mul_f32 v74, v74, v76\n\t" \
  "v_mul_f32 v75, v75, v76\n\t" \
  "v_cvt_pk_bf16_f32 v0, v64, v68\n\t" \
  "v_cvt_pk_bf16_f32 v1, v65, v69\n\t" \
  "v_cvt_pk_bf16_f32 v2, v66, v70\n\t" \
  "v_cvt_pk_bf16_f32 v3, v67, v71\n\t" \
  "v_cvt_pk_bf16_f32 v4, v72, v73\n\t" \
  "v_cvt_pk_bf16_f32 v5, v74, v75\n\t" \
  "v_permlane32_swap_b32 v0, v2\n\t" \
  "v_permlane32_swap_b32 v1, v3\n\t" \
  "v_mov_b64 v[6:7], v[4:5]\n\t" \
  "s_mov_b32 exec_hi, 0\n\t" \
  "buffer_load_dword v118, v112, %[rh], s96 offen sc1\n\t" \
  "s_mov_b64 exec, -1\n\t" \
  "buffer_store_dwordx4 v[0:3], v110, %[rh], s94 offen sc1\n\t" \
  "v_permlane16_swap_b32 v4, v6\n\t" \
  "v_permlane16_swap_b32 v5, v7\n\t" \
  "buffer_store_dwordx4 v[4:7], v111, %[rh], s94 offen sc1\n\t" \
  "s_waitcnt vmcnt(3)\n\t" \
  "ds_write_b128 v101, v[32:35] offset:23552\n\t" \
  "ds_write_b128 v101, v[36:39] offset:24576\n\t" \
  "ds_write_b128 v101, v[40:43] offset:25600\n\t" \
  "s_add_i32 s77, s44, 1\n\t" \
  "s_min_u32 s77, s77, 63\n\t" \
  "v_cmp_gt_i32 vcc, s77, v119\n\t" \
  "s_cmp_lg_u64 vcc, 0\n\t" \
  "s_cselect_b32 s42, 32, 0\n\t" \
  "s_add_i32 s41, s56, 1950\n\t" \
  "s_sub_i32 s76, s92, s51\n\t" \
  "s_sub_i32 s77, s93, s53\n\t" \
  "s_cmp_eq_u32 s76, 0\n\t" \
  "s_cbranch_scc1 21f\n\t" \
  "v_cvt_f32_u32 v117, s76\n\t" \
  "v_cvt_f32_u32 v116, s77\n\t" \
  "v_rcp_f32 v117, v117\n\t" \
  "s_mov_b32 s51, s92\n\t" \
  "v_mul_f32 v117, v117, v116\n\t" \
  "v_max_f32 v117, 0x3f028f5c, v117\n\t" \
  "v_min_f32 v117, 0x3ffeb852, v117\n\t" \
  "v_rcp_f32 v116, v117\n\t" \
  "v_mul_f32 v117, 0x4f000000, v117\n\t" \
  "v_mul_f32 v116, 0x47800000, v116\n\t" \
  "v_cvt_u32_f32 v117, v117\n\t" \
  "v_cvt_u32_f32 v116, v116\n\t" \
  "s_mov_b32 s53, s93\n\t" \
  "v_readfirstlane_b32 s46, v117\n\t" \
  "v_readfirstlane_b32 s52, v116\n\t" \
  "21:\n\t" \
  "s_add_i32 s44, s44, 1\n\t" \
  "s_and_b32 s58, s58, 4\n\t" \
  "s_and_b32 s76, s44, 64\n\t" \
  "s_or_b32 s42, s42, s76\n\t" \
  "s_cbranch_scc0 20b\n\t" \
  "s_branch 99f\n\t" \
  "14:\n\t" \
  "s_sleep 3\n\t" \
  "s_add_u32 s59, s59, 0x10000\n\t" \
  "s_cmp_lt_u32 s59, 0x100000\n\t" \
  "s_cbranch_scc1 13b\n\t" \
  "s_mov_b64 exec, -1\n\t" \
  "s_mov_b32 s42, 16\n\t" \
  "s_branch 99f\n\t" \
  "8:\n\t" \
  "s_bitcmp1_b32 s58, 1\n\t" \
  "s_cbranch_scc1 4b\n\t" \
  "s_bitcmp1_b32 s58, 2\n\t" \
  "s_cbranch_scc1 5b\n\t" \
  "v_max3_u32 v116, v32, v33, v34\n\t" \
  "v_max3_u32 v117, v35, v36, v37\n\t" \
  "v_max3_u32 v116, v116, v38, v39\n\t" \
  "v_max_u32 v116, v116, v117\n\t" \
  "v_cmp_eq_u32 vcc, -1, v116\n\t" \
  "s_cbranch_vccz 5b\n\t" \
  "s_branch 0f\n\t" \
  "9:\n\t" \
  "s_bitcmp1_b32 s58, 2\n\t" \
  "s_cbranch_scc1 6b\n\t" \
  "v_max3_u32 v116, v40, v41, v42\n\t" \
  "v_max3_u32 v117, v43, v44, v45\n\t" \
  "v_max3_u32 v116, v116, v46, v47\n\t" \
  "v_max_u32 v116, v116, v117\n\t" \
  "v_cmp_eq_u32 vcc, -1, v116\n\t" \
  "s_cbranch_vccz 6b\n\t" \
  "s_or_b32 s58, s58, 2\n\t" \
  "0:\n\t" \
  "s_or_b32 s58, s58, 1\n\t" \
  "11:\n\t" \
  "buffer_load_dwordx4 v[32:35], v100, %[rh], s40 offen sc1\n\t" \
  "buffer_load_dwordx4 v[36:39], v100, %[rh], s76 offen sc1\n\t" \
  "buffer_load_dwordx4 v[40:43], v100, %[rh], s77 offen sc1\n\t" \
  "buffer_load_dwordx4 v[44:47], v100, %[rh], s78 offen sc1\n\t" \
  "s_waitcnt lgkmcnt(0)\n\t" \
  "s_branch 10b\n\t" \
  "17:\n\t" \
  "s_add_u32 s59, s59, 1\n\t" \
  "s_bitcmp1_b32 s58, 2\n\t" \
  "s_cbranch_scc1 3b\n\t" \
  "s_waitcnt vmcnt(0)\n\t" \
  "v_max_u32 v116, v32, v33\n\t" \
  "v_max3_u32 v116, v116, v34, v35\n\t" \
  "v_max3_u32 v116, v116, v36, v37\n\t" \
  "v_max3_u32 v116, v116, v38, v39\n\t" \
  "v_max3_u32 v116, v116, v40, v41\n\t" \
  "v_max3_u32 v116, v116, v42, v43\n\t" \
  "v_max3_u32 v116, v116, v44, v45\n\t" \
  "v_max3_u32 v116, v116, v46, v47\n\t" \
  "v_cmp_eq_u32 vcc, -1, v116\n\t" \
  "s_cbranch_vccz 3b\n\t" \
  "s_bitcmp1_b32 s59, 6\n\t" \
  "s_cbranch_scc1 12f\n\t" \
  "buffer_load_dwordx4 v[32:35], v100, %[rh], s40 offen sc1\n\t" \
  "buffer_load_dwordx4 v[36:39], v100, %[rh], s76 offen sc1\n\t" \
  "buffer_load_dwordx4 v[40:43], v100, %[rh], s77 offen sc1\n\t" \
  "buffer_load_dwordx4 v[44:47], v100, %[rh], s78 offen sc1\n\t" \
  "s_branch 17b\n\t" \
  "12:\n\t" \
  "s_mov_b32 s42, s58\n\t" \
  "s_branch 99f\n\t" \
  "93:\n\t" \
  "s_mov_b32 s58, s42\n\t" \
  "20:\n\t" \
  "s_and_b32 s76, s44, 7\n\t" \
  "s_lshl_b32 s76, s76, 15\n\t" \
  "s_add_i32 s40, s54, s76\n\t" \
  "s_and_b32 s76, s44, 3\n\t" \
  "s_lshl_b32 s45, s76, 14\n\t" \
  "s_add_i32 s47, s44, 1\n\t" \
  "s_min_u32 s47, s47, 63\n\t" \
  "s_lshr_b32 s76, s47, 1\n\t" \
  "s_lshl_b32 s47, s47, 20\n\t" \
  "s_add_i32 s47, s47, s55\n\t" \
  "s_bitcmp1_b64 %[sel], s76\n\t" \
  "s_cselect_b32 s76, 0x2000, 0\n\t" \
  "s_add_i32 s47, s47, s76\n\t" \
  "s_add_i32 s49, s44, -1\n\t" \
  "s_and_b32 s48, s49, 2\n\t" \
  "s_lshl_b32 s48, s48, 13\n\t" \
  "s_and_b32 s76, s49, 1\n\t" \
  "s_lshl_b32 s76, s76, 12\n\t" \
  "s_add_i32 s48, s48, s76\n\t" \
  "s_lshl_b32 s50, s53, 8\n\t" \
  "s_mov_b32 s59, 0\n\t" \
  "s_mov_b32 s92, s51\n\t" \
  "s_mov_b32 s93, s53\n\t" \
  "s_xor_b32 s76, s40, 0x400\n\t" \
  "s_xor_b32 s77, s40, 0x800\n\t" \
  "s_xor_b32 s78, s40, 0xc00\n\t" \
  "s_cmp_lg_u32 s58, 0\n\t" \
  "s_cbranch_scc1 11f\n\t" \
  "10:\n\t" \
  "ds_read_b128 v[48:51], v103 offset:32768\n\t" \
  "ds_read_b128 v[52:55], v103 offset:33792\n\t" \
  "ds_read_b128 v[56:59], v103 offset:34816\n\t" \
  "ds_read_b128 v[60:63], v103 offset:35840\n\t" \
  "ds_read_b128 v[16:19], v103 offset:49152\n\t" \
  "ds_read_b128 v[20:23], v103 offset:50176\n\t" \
  "ds_read_b128 v[24:27], v103 offset:51200\n\t" \
  "ds_read_b128 v[28:31], v103 offset:52224\n\t" \
  "ds_read_b128 v[84:87], v102 offset:16384\n\t" \
  "ds_read_b128 v[88:91], v102 offset:17408\n\t" \
  "ds_read_b128 v[92:95], v102 offset:18432\n\t" \
  "ds_read_b128 v[96:99], v102 offset:19456\n\t" \
  "s_waitcnt lgkmcnt(4)\n\t" \
  "ds_read_b128 v[0:3], v102 offset:49152\n\t" \
  "ds_read_b128 v[4:7], v102 offset:50176\n\t" \
  "ds_read_b128 v[8:11], v102 offset:51200\n\t" \
  "ds_read_b128 v[12:15], v102 offset:52224\n\t" \
  "s_bitcmp1_b32 s58, 1\n\t" \
  "s_cselect_b64 s[80:81], -1, 0\n\t" \
  "s_cmp_lg_u32 s58, 0\n\t" \
  "s_cbranch_scc1 17f\n\t" \
  "s_mov_b32 exec_hi, 0\n\t" \
  "16:\n\t" \
  "s_memtime vcc\n\t" \
  "s_waitcnt lgkmcnt(0)\n\t" \
  "s_sub_i32 s79, s41, vcc_lo\n\t" \
  "s_cmp_gt_i32 s79, 64\n\t" \
  "s_cbranch_scc1 16b\n\t" \
  "13:\n\t" \
  "buffer_load_dword v116, v112, %[rh], s48 offen sc1\n\t" \
  "s_waitcnt vmcnt(0)\n\t" \
  "15:\n\t" \
  "v_mov_b32 v117, 20000\n\t" \
  "v_cmp_ne_u32_sdwa vcc, v116, s49 src0_sel:BYTE_0 src1_sel:DWORD\n\t" \
  "v_subrev_u32 v116, s50, v116\n\t" \
  "v_ashrrev_i32 v116, 8, v116\n\t" \
  "s_nop 1\n\t" \
  "v_max_i32_dpp v116, v116, v116 quad_perm:[1,0,3,2] row_mask:0xf bank_mask:0xf\n\t" \
  "s_cbranch_vccnz 14f\n\t" \
  "s_nop 0\n\t" \
  "v_max_i32_dpp v116, v116, v116 quad_perm:[2,3,0,1] row_mask:0xf bank_mask:0xf\n\t" \
  "s_nop 1\n\t" \
  "v_max_i32_dpp v116, v116, v116 row_half_mirror row_mask:0xf bank_mask:0xf\n\t" \
  "s_nop 1\n\t" \
  "v_max_i32_dpp v116, v116, v116 row_mirror row_mask:0xf bank_mask:0xf\n\t" \
  "v_med3_i32 v116, v116, 0, v117\n\t" \
  "v_mul_u32_u24 v116, s52, v116\n\t" \
  "v_lshrrev_b32 v116, 16, v116\n\t" \
  "s_mov_b64 exec, -1\n\t" \
  "v_readfirstlane_b32 s84, v116\n\t" \
  "v_readlane_b32 s85, v116, 16\n\t" \
  "s_max_u32 s84, s84, s85\n\t" \
  "s_add_i32 s84, s84, s51\n\t" \
  "1:\n\t" \
  "s_memtime vcc\n\t" \
  "s_waitcnt lgkmcnt(0)\n\t" \
  "s_sub_i32 s79, s84, vcc_lo\n\t" \
  "s_cmp_gt_i32 s79, 128\n\t" \
  "s_cbranch_scc1 1b\n\t" \
  ".p2align 4\n\t" \
  "2:\n\t" \
  "s_sub_i32 s79, s79, 33\n\t" \
  "s_cmp_gt_i32 s79, 56\n\t" \
  "s_cbranch_scc1 2b\n\t" \
  "buffer_load_dwordx4 v[32:35], v100, %[rh], s40 offen sc1\n\t" \
  "s_nop 12\n\t" \
  "buffer_load_dwordx4 v[36:39], v100, %[rh], s76 offen sc1\n\t" \
  "s_nop 12\n\t" \
  "buffer_load_dwordx4 v[40:43], v100, %[rh], s77 offen sc1\n\t" \
  "s_nop 12\n\t" \
  "buffer_load_dwordx4 v[44:47], v100, %[rh], s78 offen sc1\n\t" \
  "s_memtime vcc\n\t" \
  "s_memrealtime s[82:83]\n\t" \
  "s_nop 4\n\t" \
  "s_memrealtime s[84:85]\n\t" \
  "s_nop 4\n\t" \
  "s_memrealtime s[56:57]\n\t" \
  "s_nop 4\n\t" \
  "s_memrealtime s[86:87]\n\t" \
  "s_nop 4\n\t" \
  "s_memrealtime s[88:89]\n\t" \
  "s_nop 4\n\t" \
  "s_memrealtime s[90:91]\n\t" \
  "s_mov_b32 s92, 0\n\t" \
  "s_waitcnt lgkmcnt(0)\n\t" \
  "s_cmp_eq_u32 s82, s84\n\t" \
  "s_addc_u32 s92, s92, 0\n\t" \
  "s_cmp_eq_u32 s82, s56\n\t" \
  "s_addc_u32 s92, s92, 0\n\t" \
  "s_cmp_eq_u32 s82, s86\n\t" \
  "s_addc_u32 s92, s92, 0\n\t" \
  "s_cmp_eq_u32 s82, s88\n\t" \
  "s_addc_u32 s92, s92, 0\n\t" \
  "s_cmp_eq_u32 s82, s90\n\t" \
  "s_addc_u32 s92, s92, 0\n\t" \
  "s_mul_i32 s92, s92, 24\n\t" \
  "s_add_i32 s92, s92, vcc_lo\n\t" \
  "s_lshl4_add_u32 s93, s82, 64\n\t" \
  "3:\n\t" \
  "v_mov_b64 v[68:69], 0\n\t" \
  "v_mov_b64 v[70:71], 0\n\t" \
  "v_mov_b64 v[72:73], 0\n\t" \
  "v_mov_b64 v[74:75], 0\n\t" \
  "v_mov_b64 v[76:77], 0\n\t" \
  "v_mov_b64 v[78:79], 0\n\t" \
  "v_mov_b64 v[80:81], 0\n\t" \
  "v_mov_b64 v[82:83], 0\n\t" \
  "s_waitcnt lgkmcnt(0)\n\t" \
  "s_waitcnt vmcnt(3)\n\t" \
  "v_smfmac_f32_16x16x64_bf16 v[68:71], v[32:35], v[152:159], v104\n\t" \
  "ds_write_b128 v101, v[32:35] offset:12288\n\t" \
  "v_smfmac_f32_16x16x64_bf16 v[72:75], v[32:35], v[160:167], v104\n\t" \
  "v_smfmac_f32_16x16x64_bf16 v[76:79], v[32:35], v[48:55], v104\n\t" \
  "v_smfmac_f32_16x16x64_bf16 v[80:83], v[32:35], v[56:63], v104\n\t" \
  "s_waitcnt vmcnt(2)\n\t" \
  "v_smfmac_f32_16x16x64_bf16 v[68:71], v[36:39], v[168:175], v104\n\t" \
  "ds_write_b128 v101, v[36:39] offset:13312\n\t" \
  "v_smfmac_f32_16x16x64_bf16 v[72:75], v[36:39], v[176:183], v104\n\t" \
  "v_smfmac_f32_16x16x64_bf16 v[76:79], v[36:39], v[184:191], v104\n\t" \
  "v_cmp_u_f32 vcc, v68, v69\n\t" \
  "v_smfmac_f32_16x16x64_bf16 v[80:83], v[36:39], v[192:199], v104\n\t" \
  "v_cmp_u_f32 s[82:83], v70, v71\n\t" \
  "s_waitcnt vmcnt(1)\n\t" \
  "v_smfmac_f32_16x16x64_bf16 v[68:71], v[40:43], v[16:23], v104\n\t" \
  "ds_write_b128 v101, v[40:43] offset:14336\n\t" \
  "v_smfmac_f32_16x16x64_bf16 v[72:75], v[40:43], v[24:31], v104\n\t" \
  "s_or_b64 vcc, vcc, s[82:83]\n\t" \
  "s_or_b64 vcc, vcc, s[80:81]\n\t" \
  "s_waitcnt lgkmcnt(1)\n\t" \
  "v_smfmac_f32_16x16x64_bf16 v[76:79], v[40:43], v[200:207], v104\n\t" \
  "s_cbranch_scc1 8f\n\t" \
  "5:\n\t" \
  "s_barrier\n\t" \
  "4:\n\t" \
  "s_memtime s[56:57]\n\t" \
  "ds_read_b128 v[48:51], v101 offset:0\n\t" \
  "v_smfmac_f32_16x16x64_bf16 v[80:83], v[40:43], v[208:215], v104\n\t" \
  "ds_read_b128 v[52:55], v101 offset:1024\n\t" \
  "ds_read_b128 v[56:59], v101 offset:4096\n\t" \
  "s_waitcnt vmcnt(0)\n\t" \
  "v_smfmac_f32_16x16x64_bf16 v[68:71], v[44:47], v[216:223], v104\n\t" \
  "v_smfmac_f32_16x16x64_bf16 v[72:75], v[44:47], v[224:231], v104\n\t" \
  "ds_write_b128 v101, v[44:47] offset:15360\n\t" \
  "v_smfmac_f32_16x16x64_bf16 v[76:79], v[44:47], v[232:239], v104\n\t" \
  "v_cmp_u_f32 vcc, v68, v69\n\t" \
  "v_smfmac_f32_16x16x64_bf16 v[80:83], v[44:47], v[240:247], v104\n\t" \
  "v_cmp_u_f32 s[82:83], v70, v71\n\t" \
  "s_waitcnt lgkmcnt(3)\n\t" \
  "v_smfmac_f32_16x16x64_bf16 v[68:71], v[48:51], a[0:7], v104\n\t" \
  "ds_read_b128 v[60:63], v101 offset:5120\n\t" \
  "ds_read_b128 v[64:67], v101 offset:8192\n\t" \
  "v_smfmac_f32_16x16x64_bf16 v[72:75], v[48:51], a[8:15], v104\n\t" \
  "s_or_b64 vcc, vcc, s[82:83]\n\t" \
  "s_waitcnt lgkmcnt(2)\n\t" \
  "v_smfmac_f32_16x16x64_bf16 v[68:71], v[52:55], a[16:23], v104\n\t" \
  "s_cbranch_scc1 9f\n\t" \
  "6:\n\t" \
  "s_barrier\n\t" \
  "v_smfmac_f32_16x16x64_bf16 v[72:75], v[52:55], a[24:31], v104\n\t" \
  "ds_read_b128 v[48:51], v101 offset:9216\n\t" \
  "v_smfmac_f32_16x16x64_bf16 v[68:71], v[56:59], a[96:103], v104\n\t" \
  "ds_read_b128 v[52:55], v101 offset:2048\n\t" \
  "v_smfmac_f32_16x16x64_bf16 v[72:75], v[56:59], a[104:111], v104\n\t" \
  "s_waitcnt lgkmcnt(3)\n\t" \
  "v_smfmac_f32_16x16x64_bf16 v[68:71], v[60:63], a[112:119], v104\n\t" \
  "ds_read_b128 v[56:59], v101 offset:3072\n\t" \
  "v_smfmac_f32_16x16x64_bf16 v[72:75], v[60:63], a[120:127], v104\n\t" \
  "s_waitcnt lgkmcnt(3)\n\t" \
  "v_smfmac_f32_16x16x64_bf16 v[68:71], v[64:67], a[192:199], v104\n\t" \
  "ds_read_b128 v[60:63], v101 offset:6144\n\t" \
  "v_smfmac_f32_16x16x64_bf16 v[72:75], v[64:67], a[200:207], v104\n\t" \
  "s_waitcnt lgkmcnt(3)\n\t" \
  "v_smfmac_f32_16x16x64_bf16 v[68:71], v[48:51], a[208:215], v104\n\t" \
  "ds_read_b128 v[64:67], v101 offset:7168\n\t" \
  "ds_read_b128 v[16:19], v103 offset:16384\n\t" \
  "v_smfmac_f32_16x16x64_bf16 v[72:75], v[48:51], a[216:223], v104\n\t" \
  "s_waitcnt lgkmcnt(4)\n\t" \
  "v_smfmac_f32_16x16x64_bf16 v[68:71], v[52:55], v[84:91], v104\n\t" \
  "ds_read_b128 v[48:51], v101 offset:11264\n\t" \
  "ds_read_b128 v[20:23], v103 offset:17408\n\t" \
  "v_smfmac_f32_16x16x64_bf16 v[72:75], v[52:55], v[92:99], v104\n\t" \
  "s_waitcnt lgkmcnt(5)\n\t" \
  "v_smfmac_f32_16x16x64_bf16 v[68:71], v[56:59], a[64:71], v104\n\t" \
  "ds_read_b128 v[52:55], v101 offset:10240\n\t" \
  "ds_read_b128 v[24:27], v103 offset:18432\n\t" \
  "v_smfmac_f32_16x16x64_bf16 v[72:75], v[56:59], a[72:79], v104\n\t" \
  "ds_read_b128 v[28:31], v103 offset:19456\n\t" \
  "s_waitcnt lgkmcnt(7)\n\t" \
  "v_smfmac_f32_16x16x64_bf16 v[68:71], v[60:63], v[0:7], v104\n\t" \
  "v_smfmac_f32_16x16x64_bf16 v[72:75], v[60:63], v[8:15], v104\n\t" \
  "s_waitcnt lgkmcnt(6)\n\t" \
  "v_smfmac_f32_16x16x64_bf16 v[68:71], v[64:67], a[160:167], v104\n\t" \
  "ds_read_b128 v[0:3], v102 offset:0\n\t" \
  "v_smfmac_f32_16x16x64_bf16 v[72:75], v[64:67], a[168:175], v104\n\t" \
  "ds_read_b128 v[4:7], v102 offset:1024\n\t" \
  "s_waitcnt lgkmcnt(6)\n\t" \
  "v_smfmac_f32_16x16x64_bf16 v[68:71], v[48:51], v[120:127], v104\n\t" \
  "s_waitcnt lgkmcnt(4)\n\t" \
  "v_smfmac_f32_16x16x64_bf16 v[68:71], v[52:55], v[16:23], v104\n\t" \
  "ds_read_b128 v[8:11], v102 offset:2048\n\t" \
  "v_smfmac_f32_16x16x64_bf16 v[72:75], v[48:51], v[128:135], v104\n\t" \
  "ds_read_b128 v[12:15], v102 offset:3072\n\t" \
  "s_waitcnt lgkmcnt(4)\n\t" \
  "v_smfmac_f32_16x16x64_bf16 v[72:75], v[52:55], v[24:31], v104\n\t" \
  "v_add_f32 v44, v68, v70\n\t" \
  "v_smfmac_f32_16x16x64_bf16 v[76:79], v[56:59], a[80:87], v104\n\t" \
  "v_add_f32 v45, v69, v71\n\t" \
  "v_cvt_pk_bf16_f32 v44, v44, v45\n\t" \
  "v_smfmac_f32_16x16x64_bf16 v[80:83], v[56:59], a[88:95], v104\n\t" \
  "v_add_f32 v46, v72, v74\n\t" \
  "v_add_f32 v47, v73, v75\n\t" \
  "v_smfmac_f32_16x16x64_bf16 v[76:79], v[60:63], a[144:151], v104\n\t" \
  "v_cvt_pk_bf16_f32 v46, v46, v47\n\t" \
  "ds_write2st64_b32 v105, v44, v46 offset1:1\n\t" \
  "v_smfmac_f32_16x16x64_bf16 v[80:83], v[60:63], a[152:159], v104\n\t" \
  "ds_read_b128 v[56:59], v101 offset:0\n\t" \
  "v_smfmac_f32_16x16x64_bf16 v[76:79], v[64:67], a[176:183], v104\n\t" \
  "ds_read_b128 v[16:19], v102 offset:32768\n\t" \
  "v_smfmac_f32_16x16x64_bf16 v[80:83], v[64:67], a[184:191], v104\n\t" \
  "ds_read_b128 v[20:23], v102 offset:33792\n\t" \
  "v_smfmac_f32_16x16x64_bf16 v[76:79], v[48:51], v[136:143], v104\n\t" \
  "s_waitcnt lgkmcnt(3)\n\t" \
  "s_barrier\n\t" \
  "v_smfmac_f32_16x16x64_bf16 v[80:83], v[48:51], v[144:151], v104\n\t" \
  "v_smfmac_f32_16x16x64_bf16 v[76:79], v[52:55], a[240:247], v104\n\t" \
  "ds_read_b128 v[60:63], v101 offset:9216\n\t" \
  "ds_read_b128 v[24:27], v102 offset:34816\n\t" \
  "v_smfmac_f32_16x16x64_bf16 v[80:83], v[52:55], a[248:255], v104\n\t" \
  "ds_read_b128 v[64:67], v101 offset:2048\n\t" \
  "global_load_dword v119, v109, %[kc] sc1\n\t" \
  "s_waitcnt lgkmcnt(5)\n\t" \
  "v_smfmac_f32_16x16x64_bf16 v[76:79], v[56:59], v[0:7], v104\n\t" \
  "ds_read_b128 v[48:51], v101 offset:4096\n\t" \
  "ds_read_b128 v[28:31], v102 offset:35840\n\t" \
  "v_smfmac_f32_16x16x64_bf16 v[80:83], v[56:59], v[8:15], v104\n\t" \
  "s_waitcnt lgkmcnt(4)\n\t" \
  "v_smfmac_f32_16x16x64_bf16 v[76:79], v[60:63], a[224:231], v104\n\t" \
  "ds_read_b128 v[52:55], v101 offset:1024\n\t" \
  "v_smfmac_f32_16x16x64_bf16 v[80:83], v[60:63], a[232:239], v104\n\t" \
  "ds_read_b128 v[0:3], v103 offset:0\n\t" \
  "buffer_load_dwordx4 v[32:35], v100, %[ru], s47 offen offset:0 sc1\n\t" \
  "s_waitcnt lgkmcnt(4)\n\t" \
  "v_smfmac_f32_16x16x64_bf16 v[76:79], v[64:67], a[48:55], v104\n\t" \
  "ds_read_b128 v[56:59], v101 offset:5120\n\t" \
  "v_smfmac_f32_16x16x64_bf16 v[80:83], v[64:67], a[56:63], v104\n\t" \
  "ds_read_b128 v[60:63], v101 offset:8192\n\t" \
  "ds_read_b128 v[4:7], v103 offset:1024\n\t" \
  "s_waitcnt lgkmcnt(6)\n\t" \
  "v_smfmac_f32_16x16x64_bf16 v[76:79], v[48:51], v[16:23], v104\n\t" \
  "ds_read_b128 v[8:11], v103 offset:2048\n\t" \
  "buffer_load_dwordx4 v[36:39], v100, %[ru], s47 offen offset:1024 sc1\n\t" \
  "s_waitcnt lgkmcnt(6)\n\t" \
  "v_smfmac_f32_16x16x64_bf16 v[80:83], v[48:51], v[24:31], v104\n\t" \
  "ds_read_b128 v[12:15], v103 offset:3072\n\t" \
  "s_waitcnt lgkmcnt(6)\n\t" \
  "v_smfmac_f32_16x16x64_bf16 v[76:79], v[52:55], a[32:39], v104\n\t" \
  "v_smfmac_f32_16x16x64_bf16 v[80:83], v[52:55], a[40:47], v104\n\t" \
  "s_waitcnt lgkmcnt(4)\n\t" \
  "v_smfmac_f32_16x16x64_bf16 v[76:79], v[56:59], a[128:135], v104\n\t" \
  "s_waitcnt lgkmcnt(2)\n\t" \
  "v_smfmac_f32_16x16x64_bf16 v[76:79], v[60:63], v[0:7], v104\n\t" \
  "v_smfmac_f32_16x16x64_bf16 v[80:83], v[56:59], a[136:143], v104\n\t" \
  "s_waitcnt lgkmcnt(0)\n\t" \
  "v_smfmac_f32_16x16x64_bf16 v[80:83], v[60:63], v[8:15], v104\n\t" \
  "v_add_f32 v20, v76, v78\n\t" \
  "v_add_f32 v21, v77, v79\n\t" \
  "v_cvt_pk_bf16_f32 v20, v20, v21\n\t" \
  "ds_write_b32 v106, v20\n\t" \
  "s_nop 3\n\t" \
  "v_add_f32 v23, v80, v82\n\t" \
  "v_add_f32 v21, v81, v83\n\t" \
  "v_cvt_pk_bf16_f32 v21, v23, v21\n\t" \
  "ds_write_b32 v106, v21 offset:256\n\t" \
  "s_waitcnt lgkmcnt(0)\n\t" \
  "s_barrier\n\t" \
  "s_waitcnt vmcnt(0)\n\t" \
  "s_barrier\n\t" \
  "ds_write_b128 v101, v[32:35] offset:26624\n\t" \
  "ds_write_b128 v101, v[36:39] offset:27648\n\t" \
  "s_add_i32 s77, s44, 1\n\t" \
  "s_min_u32 s77, s77, 63\n\t" \
  "v_cmp_gt_i32 vcc, s77, v119\n\t" \
  "s_cmp_lg_u64 vcc, 0\n\t" \
  "s_cselect_b32 s42, 32, 0\n\t" \
  "s_add_i32 s41, s56, 1950\n\t" \
  "s_sub_i32 s76, s92, s51\n\t" \
  "s_sub_i32 s77, s93, s53\n\t" \
  "s_cmp_eq_u32 s76, 0\n\t" \
  "s_cbranch_scc1 21f\n\t" \
  "v_cvt_f32_u32 v117, s76\n\t" \
  "v_cvt_f32_u32 v116, s77\n\t" \
  "v_rcp_f32 v117, v117\n\t" \
  "s_mov_b32 s51, s92\n\t" \
  "v_mul_f32 v117, v117, v116\n\t" \
  "v_max_f32 v117, 0x3f028f5c, v117\n\t" \
  "v_min_f32 v117, 0x3ffeb852, v117\n\t" \
  "v_rcp_f32 v116, v117\n\t" \
  "v_mul_f32 v117, 0x4f000000, v117\n\t" \
  "v_mul_f32 v116, 0x47800000, v116\n\t" \
  "v_cvt_u32_f32 v117, v117\n\t" \
  "v_cvt_u32_f32 v116, v116\n\t" \
  "s_mov_b32 s53, s93\n\t" \
  "v_readfirstlane_b32 s46, v117\n\t" \
  "v_readfirstlane_b32 s52, v116\n\t" \
  "21:\n\t" \
  "v_mov_b32 v0, -1\n\t" \
  "v_mov_b32 v1, -1\n\t" \
  "v_mov_b32 v2, -1\n\t" \
  "v_mov_b32 v3, -1\n\t" \
  "s_and_b32 s76, s49, 3\n\t" \
  "s_mul_i32 s76, s76, 0xc000\n\t" \
  "s_add_i32 s76, s76, 0x40000\n\t" \
  "buffer_store_dwordx4 v[0:3], v110, %[rh], s76 offen sc1\n\t" \
  "s_add_i32 s76, s44, 1\n\t" \
  "s_and_b32 s76, s76, 3\n\t" \
  "s_lshl_b32 s76, s76, 18\n\t" \
  "s_add_i32 s77, s76, 0x10000\n\t" \
  "buffer_store_dwordx4 v[0:3], v107, %[rh], s76 offen sc1\n\t" \
  "buffer_store_dwordx4 v[0:3], v107, %[rh], s77 offen sc1\n\t" \
  "buffer_store_dwordx4 v[0:3], v111, %[rh], s76 offen sc1\n\t" \
  "s_add_i32 s44, s44, 1\n\t" \
  "s_and_b32 s58, s58, 4\n\t" \
  "s_and_b32 s76, s44, 64\n\t" \
  "s_or_b32 s42, s42, s76\n\t" \
  "s_cbranch_scc0 20b\n\t" \
  "s_branch 99f\n\t" \
  "14:\n\t" \
  "s_sleep 3\n\t" \
  "s_add_u32 s59, s59, 0x10000\n\t" \
  "s_cmp_lt_u32 s59, 0x100000\n\t" \
  "s_cbranch_scc1 13b\n\t" \
  "s_mov_b64 exec, -1\n\t" \
  "s_mov_b32 s42, 16\n\t" \
  "s_branch 99f\n\t" \
  "8:\n\t" \
  "s_bitcmp1_b32 s58, 1\n\t" \
  "s_cbranch_scc1 4b\n\t" \
  "s_bitcmp1_b32 s58, 2\n\t" \
  "s_cbranch_scc1 5b\n\t" \
  "v_max3_u32 v116, v32, v33, v34\n\t" \
  "v_max3_u32 v117, v35, v36, v37\n\t" \
  "v_max3_u32 v116, v116, v38, v39\n\t" \
  "v_max_u32 v116, v116, v117\n\t" \
  "v_cmp_eq_u32 vcc, -1, v116\n\t" \
  "s_cbranch_vccz 5b\n\t" \
  "s_branch 0f\n\t" \
  "9:\n\t" \
  "s_bitcmp1_b32 s58, 2\n\t" \
  "s_cbranch_scc1 6b\n\t" \
  "v_max3_u32 v116, v40, v41, v42\n\t" \
  "v_max3_u32 v117, v43, v44, v45\n\t" \
  "v_max3_u32 v116, v116, v46, v47\n\t" \
  "v_max_u32 v116, v116, v117\n\t" \
  "v_cmp_eq_u32 vcc, -1, v116\n\t" \
  "s_cbranch_vccz 6b\n\t" \
  "s_or_b32 s58, s58, 2\n\t" \
  "0:\n\t" \
  "s_or_b32 s58, s58, 1\n\t" \
  "11:\n\t" \
  "buffer_load_dwordx4 v[32:35], v100, %[rh], s40 offen sc1\n\t" \
  "buffer_load_dwordx4 v[36:39], v100, %[rh], s76 offen sc1\n\t" \
  "buffer_load_dwordx4 v[40:43], v100, %[rh], s77 offen sc1\n\t" \
  "buffer_load_dwordx4 v[44:47], v100, %[rh], s78 offen sc1\n\t" \
  "s_waitcnt lgkmcnt(0)\n\t" \
  "s_branch 10b\n\t" \
  "17:\n\t" \
  "s_add_u32 s59, s59, 1\n\t" \
  "s_bitcmp1_b32 s58, 2\n\t" \
  "s_cbranch_scc1 3b\n\t" \
  "s_waitcnt vmcnt(0)\n\t" \
  "v_max_u32 v116, v32, v33\n\t" \
  "v_max3_u32 v116, v116, v34, v35\n\t" \
  "v_max3_u32 v116, v116, v36, v37\n\t" \
  "v_max3_u32 v116, v116, v38, v39\n\t" \
  "v_max3_u32 v116, v116, v40, v41\n\t" \
  "v_max3_u32 v116, v116, v42, v43\n\t" \
  "v_max3_u32 v116, v116, v44, v45\n\t" \
  "v_max3_u32 v116, v116, v46, v47\n\t" \
  "v_cmp_eq_u32 vcc, -1, v116\n\t" \
  "s_cbranch_vccz 3b\n\t" \
  "s_bitcmp1_b32 s59, 6\n\t" \
  "s_cbranch_scc1 12f\n\t" \
  "buffer_load_dwordx4 v[32:35], v100, %[rh], s40 offen sc1\n\t" \
  "buffer_load_dwordx4 v[36:39], v100, %[rh], s76 offen sc1\n\t" \
  "buffer_load_dwordx4 v[40:43], v100, %[rh], s77 offen sc1\n\t" \
  "buffer_load_dwordx4 v[44:47], v100, %[rh], s78 offen sc1\n\t" \
  "s_branch 17b\n\t" \
  "12:\n\t" \
  "s_mov_b32 s42, s58\n\t" \
  "s_branch 99f\n\t" \
  "99:\n\t" \
  "s_waitcnt vmcnt(0) lgkmcnt(0)\n\t" \
  ""
#define VO_BODY \
  "s_cmp_eq_u32 %[wv], 1\n\t" \
  "s_cbranch_scc1 91f\n\t" \
  "s_cmp_eq_u32 %[wv], 2\n\t" \
  "s_cbranch_scc1 92f\n\t" \
  "s_cmp_eq_u32 %[wv], 3\n\t" \
  "s_cbranch_scc1 93f\n\t" \
  "90:\n\t" \
  "s_mov_b32 s58, s42\n\t" \
  "20:\n\t" \
  "s_and_b32 s76, s44, 7\n\t" \
  "s_lshl_b32 s76, s76, 15\n\t" \
  "s_add_i32 s40, s54, s76\n\t" \
  "s_and_b32 s76, s44, 3\n\t" \
  "s_lshl_b32 s45, s76, 14\n\t" \
  "s_lshl_b32 s47, s76, 20\n\t" \
  "s_add_i32 s47, s47, s55\n\t" \
  "s_lshr_b32 s76, s47, 21\n\t" \
  "s_bitcmp1_b64 %[sel], s76\n\t" \
  "s_cselect_b32 s76, 0x2000, 0\n\t" \
  "s_add_i32 s47, s47, s76\n\t" \
  "s_add_i32 s49, s44, -1\n\t" \
  "s_and_b32 s48, s49, 2\n\t" \
  "s_lshl_b32 s48, s48, 13\n\t" \
  "s_and_b32 s76, s49, 1\n\t" \
  "s_lshl_b32 s76, s76, 12\n\t" \
  "s_add_i32 s48, s48, s76\n\t" \
  "s_lshl_b32 s50, s53, 8\n\t" \
  "s_mov_b32 s59, 0\n\t" \
  "s_mov_b32 s92, s51\n\t" \
  "s_mov_b32 s93, s53\n\t" \
  "s_xor_b32 s76, s40, 0x400\n\t" \
  "s_xor_b32 s77, s40, 0x800\n\t" \
  "s_xor_b32 s78, s40, 0xc00\n\t" \
  "s_cmp_lg_u32 s58, 0\n\t" \
  "s_cbranch_scc1 11f\n\t" \
  "10:\n\t" \
  "ds_read_b128 v[48:51], v102 offset:0\n\t" \
  "ds_read_b128 v[52:55], v102 offset:1024\n\t" \
  "ds_read_b128 v[56:59], v102 offset:2048\n\t" \
  "ds_read_b128 v[60:63], v102 offset:3072\n\t" \
  "ds_read_b128 v[0:3], v102 offset:16384\n\t" \
  "ds_read_b128 v[4:7], v102 offset:17408\n\t" \
  "ds_read_b128 v[8:11], v102 offset:18432\n\t" \
  "ds_read_b128 v[12:15], v102 offset:19456\n\t" \
  "ds_read_b128 v[16:19], v102 offset:32768\n\t" \
  "ds_read_b128 v[20:23], v102 offset:33792\n\t" \
  "ds_read_b128 v[24:27], v102 offset:34816\n\t" \
  "ds_read_b128 v[28:31], v102 offset:35840\n\t" \
  "s_waitcnt lgkmcnt(4)\n\t" \
  "ds_read_b128 v[80:83], v102 offset:49152\n\t" \
  "ds_read_b128 v[84:87], v102 offset:50176\n\t" \
  "ds_read_b128 v[88:91], v102 offset:51200\n\t" \
  "ds_read_b128 v[92:95], v102 offset:52224\n\t" \
  "s_bitcmp1_b32 s58, 1\n\t" \
  "s_cselect_b64 s[80:81], -1, 0\n\t" \
  "s_cmp_lg_u32 s58, 0\n\t" \
  "s_cbranch_scc1 17f\n\t" \
  "s_mov_b32 exec_hi, 0\n\t" \
  "16:\n\t" \
  "s_memtime vcc\n\t" \
  "s_waitcnt lgkmcnt(0)\n\t" \
  "s_sub_i32 s79, s41, vcc_lo\n\t" \
  "s_cmp_gt_i32 s79, 64\n\t" \
  "s_cbranch_scc1 16b\n\t" \
  "13:\n\t" \
  "buffer_load_dword v116, v112, %[rh], s48 offen sc1\n\t" \
  "s_waitcnt vmcnt(0)\n\t" \
  "15:\n\t" \
  "v_mov_b32 v117, 20000\n\t" \
  "v_cmp_ne_u32_sdwa vcc, v116, s49 src0_sel:BYTE_0 src1_sel:DWORD\n\t" \
  "v_subrev_u32 v116, s50, v116\n\t" \
  "v_ashrrev_i32 v116, 8, v116\n\t" \
  "s_nop 1\n\t" \
  "v_max_i32_dpp v116, v116, v116 quad_perm:[1,0,3,2] row_mask:0xf bank_mask:0xf\n\t" \
  "s_cbranch_vccnz 14f\n\t" \
  "s_nop 0\n\t" \
  "v_max_i32_dpp v116, v116, v116 quad_perm:[2,3,0,1] row_mask:0xf bank_mask:0xf\n\t" \
  "s_nop 1\n\t" \
  "v_max_i32_dpp v116, v116, v116 row_half_mirror row_mask:0xf bank_mask:0xf\n\t" \
  "s_nop 1\n\t" \
  "v_max_i32_dpp v116, v116, v116 row_mirror row_mask:0xf bank_mask:0xf\n\t" \
  "v_med3_i32 v116, v116, 0, v117\n\t" \
  "v_mul_u32_u24 v116, s52, v116\n\t" \
  "v_lshrrev_b32 v116, 16, v116\n\t" \
  "s_mov_b64 exec, -1\n\t" \
  "v_readfirstlane_b32 s84, v116\n\t" \
  "v_readlane_b32 s85, v116, 16\n\t" \
  "s_max_u32 s84, s84, s85\n\t" \
  "s_add_i32 s84, s84, s51\n\t" \
  "1:\n\t" \
  "s_memtime vcc\n\t" \
  "s_waitcnt lgkmcnt(0)\n\t" \
  "s_sub_i32 s79, s84, vcc_lo\n\t" \
  "s_cmp_gt_i32 s79, 128\n\t" \
  "s_cbranch_scc1 1b\n\t" \
  ".p2align 4\n\t" \
  "2:\n\t" \
  "s_sub_i32 s79, s79, 33\n\t" \
  "s_cmp_gt_i32 s79, 56\n\t" \
  "s_cbranch_scc1 2b\n\t" \
  "buffer_load_dwordx4 v[32:35], v100, %[rh], s40 offen sc1\n\t" \
  "s_nop 12\n\t" \
  "buffer_load_dwordx4 v[36:39], v100, %[rh], s76 offen sc1\n\t" \
  "s_nop 12\n\t" \
  "buffer_load_dwordx4 v[40:43], v100, %[rh], s77 offen sc1\n\t" \
  "s_nop 12\n\t" \
  "buffer_load_dwordx4 v[44:47], v100, %[rh], s78 offen sc1\n\t" \
  "s_memtime vcc\n\t" \
  "s_memrealtime s[82:83]\n\t" \
  "s_nop 4\n\t" \
  "s_memrealtime s[84:85]\n\t" \
  "s_nop 4\n\t" \
  "s_memrealtime s[56:57]\n\t" \
  "s_nop 4\n\t" \
  "s_memrealtime s[86:87]\n\t" \
  "s_nop 4\n\t" \
  "s_memrealtime s[88:89]\n\t" \
  "s_nop 4\n\t" \
  "s_memrealtime s[90:91]\n\t" \
  "s_mov_b32 s92, 0\n\t" \
  "s_waitcnt lgkmcnt(0)\n\t" \
  "s_cmp_eq_u32 s82, s84\n\t" \
  "s_addc_u32 s92, s92, 0\n\t" \
  "s_cmp_eq_u32 s82, s56\n\t" \
  "s_addc_u32 s92, s92, 0\n\t" \
  "s_cmp_eq_u32 s82, s86\n\t" \
  "s_addc_u32 s92, s92, 0\n\t" \
  "s_cmp_eq_u32 s82, s88\n\t" \
  "s_addc_u32 s92, s92, 0\n\t" \
  "s_cmp_eq_u32 s82, s90\n\t" \
  "s_addc_u32 s92, s92, 0\n\t" \
  "s_mul_i32 s92, s92, 24\n\t" \
  "s_add_i32 s92, s92, vcc_lo\n\t" \
  "s_lshl4_add_u32 s93, s82, 64\n\t" \
  "3:\n\t" \
  "v_mov_b64 v[72:73], 0\n\t" \
  "v_mov_b64 v[74:75], 0\n\t" \
  "v_mov_b64 v[76:77], 0\n\t" \
  "v_mov_b64 v[78:79], 0\n\t" \
  "s_waitcnt lgkmcnt(0)\n\t" \
  "s_waitcnt vmcnt(3)\n\t" \
  "v_smfmac_f32_16x16x64_bf16 v[72:75], v[32:35], v[48:55], v104\n\t" \
  "ds_write_b128 v101, v[32:35] offset:0\n\t" \
  "v_smfmac_f32_16x16x64_bf16 v[76:79], v[32:35], v[56:63], v104\n\t" \
  "s_waitcnt vmcnt(2)\n\t" \
  "ds_write_b128 v101, v[36:39] offset:1024\n\t" \
  "v_smfmac_f32_16x16x64_bf16 v[72:75], v[36:39], a[0:7], v104\n\t" \
  "v_smfmac_f32_16x16x64_bf16 v[76:79], v[36:39], a[8:15], v104\n\t" \
  "s_nop 3\n\t" \
  "v_cmp_u_f32 vcc, v72, v73\n\t" \
  "v_cmp_u_f32 s[82:83], v74, v75\n\t" \
  "s_waitcnt vmcnt(1)\n\t" \
  "v_smfmac_f32_16x16x64_bf16 v[72:75], v[40:43], a[16:23], v104\n\t" \
  "s_or_b64 vcc, vcc, s[82:83]\n\t" \
  "s_or_b64 vcc, vcc, s[80:81]\n\t" \
  "v_smfmac_f32_16x16x64_bf16 v[76:79], v[40:43], a[24:31], v104\n\t" \
  "s_waitcnt lgkmcnt(0)\n\t" \
  "s_cbranch_scc1 8f\n\t" \
  "5:\n\t" \
  "s_barrier\n\t" \
  "4:\n\t" \
  "s_memtime s[56:57]\n\t" \
  "ds_read_b128 v[48:51], v101 offset:4096\n\t" \
  "ds_read_b128 v[52:55], v101 offset:5120\n\t" \
  "ds_read_b128 v[56:59], v101 offset:9216\n\t" \
  "s_waitcnt vmcnt(0)\n\t" \
  "v_smfmac_f32_16x16x64_bf16 v[72:75], v[44:47], a[32:39], v104\n\t" \
  "ds_read_b128 v[60:63], v101 offset:8192\n\t" \
  "ds_read_b128 v[64:67], v101 offset:13312\n\t" \
  "v_smfmac_f32_16x16x64_bf16 v[76:79], v[44:47], a[40:47], v104\n\t" \
  "ds_read_b128 v[68:71], v101 offset:12288\n\t" \
  "ds_write_b128 v101, v[40:43] offset:2048\n\t" \
  "s_nop 1\n\t" \
  "v_cmp_u_f32 vcc, v72, v73\n\t" \
  "v_cmp_u_f32 s[82:83], v74, v75\n\t" \
  "s_waitcnt lgkmcnt(6)\n\t" \
  "v_smfmac_f32_16x16x64_bf16 v[72:75], v[48:51], v[0:7], v104\n\t" \
  "s_or_b64 vcc, vcc, s[82:83]\n\t" \
  "v_smfmac_f32_16x16x64_bf16 v[76:79], v[48:51], v[8:15], v104\n\t" \
  "s_cbranch_scc1 9f\n\t" \
  "6:\n\t" \
  "s_waitcnt lgkmcnt(5)\n\t" \
  "v_smfmac_f32_16x16x64_bf16 v[72:75], v[52:55], a[48:55], v104\n\t" \
  "ds_write_b128 v101, v[44:47] offset:3072\n\t" \
  "v_smfmac_f32_16x16x64_bf16 v[76:79], v[52:55], a[56:63], v104\n\t" \
  "v_mov_b64 v[40:41], 0\n\t" \
  "s_waitcnt lgkmcnt(5)\n\t" \
  "v_smfmac_f32_16x16x64_bf16 v[72:75], v[56:59], a[96:103], v104\n\t" \
  "v_mov_b64 v[42:43], 0\n\t" \
  "v_smfmac_f32_16x16x64_bf16 v[76:79], v[56:59], a[104:111], v104\n\t" \
  "s_waitcnt lgkmcnt(4)\n\t" \
  "v_smfmac_f32_16x16x64_bf16 v[72:75], v[60:63], v[16:23], v104\n\t" \
  "s_waitcnt lgkmcnt(0)\n\t" \
  "s_barrier\n\t" \
  "v_smfmac_f32_16x16x64_bf16 v[76:79], v[60:63], v[24:31], v104\n\t" \
  "ds_read_b128 v[48:51], v101 offset:6144\n\t" \
  "ds_read_b128 v[52:55], v101 offset:10240\n\t" \
  "v_smfmac_f32_16x16x64_bf16 v[72:75], v[64:67], a[144:151], v104\n\t" \
  "ds_read_b128 v[56:59], v101 offset:14336\n\t" \
  "ds_read_b128 v[60:63], v101 offset:7168\n\t" \
  "v_smfmac_f32_16x16x64_bf16 v[76:79], v[64:67], a[152:159], v104\n\t" \
  "ds_read_b128 v[32:35], v101 offset:11264\n\t" \
  "ds_read_b128 v[36:39], v101 offset:15360\n\t" \
  "v_smfmac_f32_16x16x64_bf16 v[72:75], v[68:71], v[80:87], v104\n\t" \
  "ds_read_b128 v[0:3], v103 offset:0\n\t" \
  "v_mov_b64 v[44:45], 0\n\t" \
  "v_smfmac_f32_16x16x64_bf16 v[76:79], v[68:71], v[88:95], v104\n\t" \
  "v_mov_b64 v[46:47], 0\n\t" \
  "s_waitcnt lgkmcnt(5)\n\t" \
  "v_smfmac_f32_16x16x64_bf16 v[72:75], v[48:51], a[64:71], v104\n\t" \
  "ds_read_b128 v[4:7], v103 offset:1024\n\t" \
  "v_mov_b64 v[80:81], 0\n\t" \
  "v_smfmac_f32_16x16x64_bf16 v[72:75], v[52:55], a[112:119], v104\n\t" \
  "v_mov_b64 v[82:83], 0\n\t" \
  "s_waitcnt lgkmcnt(2)\n\t" \
  "v_smfmac_f32_16x16x64_bf16 v[72:75], v[56:59], a[160:167], v104\n\t" \
  "ds_read_b128 v[8:11], v103 offset:2048\n\t" \
  "v_mov_b64 v[84:85], 0\n\t" \
  "v_smfmac_f32_16x16x64_bf16 v[72:75], v[60:63], a[80:87], v104\n\t" \
  "ds_read_b128 v[12:15], v103 offset:3072\n\t" \
  "v_mov_b64 v[86:87], 0\n\t" \
  "v_smfmac_f32_16x16x64_bf16 v[72:75], v[32:35], a[128:135], v104\n\t" \
  "ds_read_b128 v[16:19], v103 offset:16384\n\t" \
  "v_mov_b64 v[88:89], 0\n\t" \
  "v_smfmac_f32_16x16x64_bf16 v[72:75], v[36:39], a[176:183], v104\n\t" \
  "ds_read_b128 v[20:23], v103 offset:17408\n\t" \
  "v_mov_b64 v[90:91], 0\n\t" \
  "v_smfmac_f32_16x16x64_bf16 v[76:79], v[48:51], a[72:79], v104\n\t" \
  "ds_read_b128 v[24:27], v103 offset:18432\n\t" \
  "v_mov_b64 v[92:93], 0\n\t" \
  "v_smfmac_f32_16x16x64_bf16 v[76:79], v[52:55], a[120:127], v104\n\t" \
  "v_add_f32 v96, v72, v74\n\t" \
  "v_add_f32 v97, v73, v75\n\t" \
  "v_cvt_pk_bf16_f32 v96, v96, v97\n\t" \
  "ds_write_b32 v105, v96\n\t" \
  "v_smfmac_f32_16x16x64_bf16 v[76:79], v[56:59], a[168:175], v104\n\t" \
  "ds_read_b128 v[28:31], v103 offset:19456\n\t" \
  "v_mov_b64 v[94:95], 0\n\t" \
  "v_smfmac_f32_16x16x64_bf16 v[76:79], v[60:63], a[88:95], v104\n\t" \
  "v_smfmac_f32_16x16x64_bf16 v[76:79], v[32:35], a[136:143], v104\n\t" \
  "v_smfmac_f32_16x16x64_bf16 v[76:79], v[36:39], a[184:191], v104\n\t" \
  "s_waitcnt lgkmcnt(1)\n\t" \
  "s_barrier\n\t" \
  "ds_read_b64_tr_b16 v[96:97], v107 offset:0\n\t" \
  "ds_read_b64_tr_b16 v[98:99], v107 offset:64\n\t" \
  "ds_read_b64_tr_b16 v[56:57], v107 offset:1024\n\t" \
  "ds_read_b64_tr_b16 v[58:59], v107 offset:1088\n\t" \
  "s_nop 1\n\t" \
  "v_add_f32 v116, v76, v78\n\t" \
  "v_add_f32 v117, v77, v79\n\t" \
  "v_cvt_pk_bf16_f32 v116, v116, v117\n\t" \
  "ds_write_b32 v105, v116 offset:256\n\t" \
  "s_waitcnt lgkmcnt(3)\n\t" \
  "v_smfmac_f32_16x16x64_bf16 v[40:43], v[96:99], a[192:199], v106\n\t" \
  "v_smfmac_f32_16x16x64_bf16 v[44:47], v[96:99], a[224:231], v106\n\t" \
  "s_waitcnt lgkmcnt(0)\n\t" \
  "s_barrier\n\t" \
  "ds_read_b64_tr_b16 v[52:53], v107 offset:256\n\t" \
  "ds_read_b64_tr_b16 v[54:55], v107 offset:320\n\t" \
  "v_smfmac_f32_16x16x64_bf16 v[40:43], v[56:59], a[208:215], v106\n\t" \
  "ds_read_b64_tr_b16 v[60:61], v107 offset:1280\n\t" \
  "ds_read_b64_tr_b16 v[62:63], v107 offset:1344\n\t" \
  "v_smfmac_f32_16x16x64_bf16 v[44:47], v[56:59], v[0:7], v106\n\t" \
  "v_smfmac_f32_16x16x64_bf16 v[80:83], v[96:99], a[240:247], v106\n\t" \
  "v_mov_b64 v[32:33], 0\n\t" \
  "v_smfmac_f32_16x16x64_bf16 v[84:87], v[96:99], v[136:143], v106\n\t" \
  "v_mov_b64 v[34:35], 0\n\t" \
  "v_smfmac_f32_16x16x64_bf16 v[80:83], v[56:59], v[120:127], v106\n\t" \
  "v_mov_b64 v[36:37], 0\n\t" \
  "v_smfmac_f32_16x16x64_bf16 v[84:87], v[56:59], v[16:23], v106\n\t" \
  "s_waitcnt lgkmcnt(2)\n\t" \
  "v_smfmac_f32_16x16x64_bf16 v[40:43], v[52:55], a[200:207], v106\n\t" \
  "v_smfmac_f32_16x16x64_bf16 v[44:47], v[52:55], a[232:239], v106\n\t" \
  "s_xor_b32 s76, s47, 0x40000\n\t" \
  "s_waitcnt lgkmcnt(0)\n\t" \
  "v_smfmac_f32_16x16x64_bf16 v[40:43], v[60:63], a[216:223], v106\n\t" \
  "v_mov_b64 v[38:39], 0\n\t" \
  "s_xor_b32 s77, s47, 0x80000\n\t" \
  "v_smfmac_f32_16x16x64_bf16 v[44:47], v[60:63], v[8:15], v106\n\t" \
  "s_xor_b32 s78, s47, 0xc0000\n\t" \
  "v_smfmac_f32_16x16x64_bf16 v[80:83], v[52:55], a[248:255], v106\n\t" \
  "ds_read_b128 v[0:3], v103 offset:32768\n\t" \
  "v_cvt_pk_bf16_f32 v64, v40, v41\n\t" \
  "v_smfmac_f32_16x16x64_bf16 v[84:87], v[52:55], v[144:151], v106\n\t" \
  "ds_read_b128 v[4:7], v103 offset:33792\n\t" \
  "v_cvt_pk_bf16_f32 v65, v42, v43\n\t" \
  "v_smfmac_f32_16x16x64_bf16 v[80:83], v[60:63], v[128:135], v106\n\t" \
  "ds_read_b128 v[8:11], v103 offset:34816\n\t" \
  "v_cvt_pk_bf16_f32 v66, v44, v45\n\t" \
  "v_smfmac_f32_16x16x64_bf16 v[84:87], v[60:63], v[24:31], v106\n\t" \
  "ds_read_b128 v[12:15], v103 offset:35840\n\t" \
  "v_cvt_pk_bf16_f32 v67, v46, v47\n\t" \
  "v_smfmac_f32_16x16x64_bf16 v[88:91], v[96:99], v[152:159], v106\n\t" \
  "v_permlane16_swap_b32 v64, v66\n\t" \
  "v_cvt_pk_bf16_f32 v68, v80, v81\n\t" \
  "v_smfmac_f32_16x16x64_bf16 v[92:95], v[96:99], v[184:191], v106\n\t" \
  "v_permlane16_swap_b32 v65, v67\n\t" \
  "buffer_store_dwordx4 v[64:67], v108, %[ru], s47 offen sc1\n\t" \
  "v_cvt_pk_bf16_f32 v69, v82, v83\n\t" \
  "v_smfmac_f32_16x16x64_bf16 v[88:91], v[52:55], v[160:167], v106\n\t" \
  "v_cvt_pk_bf16_f32 v70, v84, v85\n\t" \
  "v_smfmac_f32_16x16x64_bf16 v[92:95], v[52:55], v[192:199], v106\n\t" \
  "v_cvt_pk_bf16_f32 v71, v86, v87\n\t" \
  "v_smfmac_f32_16x16x64_bf16 v[88:91], v[56:59], v[168:175], v106\n\t" \
  "ds_read_b128 v[16:19], v103 offset:49152\n\t" \
  "v_permlane16_swap_b32 v68, v70\n\t" \
  "s_waitcnt lgkmcnt(3)\n\t" \
  "v_smfmac_f32_16x16x64_bf16 v[92:95], v[56:59], v[0:7], v106\n\t" \
  "ds_read_b128 v[20:23], v103 offset:50176\n\t" \
  "v_permlane16_swap_b32 v69, v71\n\t" \
  "buffer_store_dwordx4 v[68:71], v108, %[ru], s76 offen sc1\n\t" \
  "v_smfmac_f32_16x16x64_bf16 v[88:91], v[60:63], v[176:183], v106\n\t" \
  "ds_read_b128 v[24:27], v103 offset:51200\n\t" \
  "s_waitcnt lgkmcnt(3)\n\t" \
  "v_smfmac_f32_16x16x64_bf16 v[92:95], v[60:63], v[8:15], v106\n\t" \
  "ds_read_b128 v[28:31], v103 offset:52224\n\t" \
  "v_smfmac_f32_16x16x64_bf16 v[32:35], v[96:99], v[200:207], v106\n\t" \
  "v_cvt_pk_bf16_f32 v72, v88, v89\n\t" \
  "v_smfmac_f32_16x16x64_bf16 v[36:39], v[96:99], v[232:239], v106\n\t" \
  "v_cvt_pk_bf16_f32 v73, v90, v91\n\t" \
  "v_smfmac_f32_16x16x64_bf16 v[32:35], v[52:55], v[208:215], v106\n\t" \
  "v_cvt_pk_bf16_f32 v74, v92, v93\n\t" \
  "v_smfmac_f32_16x16x64_bf16 v[36:39], v[52:55], v[240:247], v106\n\t" \
  "v_cvt_pk_bf16_f32 v75, v94, v95\n\t" \
  "v_smfmac_f32_16x16x64_bf16 v[32:35], v[56:59], v[216:223], v106\n\t" \
  "v_permlane16_swap_b32 v72, v74\n\t" \
  "v_smfmac_f32_16x16x64_bf16 v[32:35], v[60:63], v[224:231], v106\n\t" \
  "v_permlane16_swap_b32 v73, v75\n\t" \
  "buffer_store_dwordx4 v[72:75], v108, %[ru], s77 offen sc1\n\t" \
  "s_waitcnt lgkmcnt(2)\n\t" \
  "v_smfmac_f32_16x16x64_bf16 v[36:39], v[56:59], v[16:23], v106\n\t" \
  "s_waitcnt lgkmcnt(0)\n\t" \
  "v_smfmac_f32_16x16x64_bf16 v[36:39], v[60:63], v[24:31], v106\n\t" \
  "v_cvt_pk_bf16_f32 v76, v32, v33\n\t" \
  "v_cvt_pk_bf16_f32 v77, v34, v35\n\t" \
  "s_nop 5\n\t" \
  "v_cvt_pk_bf16_f32 v78, v36, v37\n\t" \
  "v_cvt_pk_bf16_f32 v79, v38, v39\n\t" \
  "s_nop 0\n\t" \
  "v_permlane16_swap_b32 v76, v78\n\t" \
  "v_permlane16_swap_b32 v77, v79\n\t" \
  "buffer_store_dwordx4 v[76:79], v108, %[ru], s78 offen sc1\n\t" \
  "s_memtime s[76:77]\n\t" \
  "s_mov_b32 s42, 0\n\t" \
  "s_waitcnt lgkmcnt(0)\n\t" \
  "s_mov_b32 s57, s76\n\t" \
  "s_add_i32 s41, s56, 1950\n\t" \
  "s_sub_i32 s76, s92, s51\n\t" \
  "s_sub_i32 s77, s93, s53\n\t" \
  "s_cmp_eq_u32 s76, 0\n\t" \
  "s_cbranch_scc1 21f\n\t" \
  "v_cvt_f32_u32 v117, s76\n\t" \
  "v_cvt_f32_u32 v116, s77\n\t" \
  "v_rcp_f32 v117, v117\n\t" \
  "s_mov_b32 s51, s92\n\t" \
  "v_mul_f32 v117, v117, v116\n\t" \
  "v_max_f32 v117, 0x3f028f5c, v117\n\t" \
  "v_min_f32 v117, 0x3ffeb852, v117\n\t" \
  "v_rcp_f32 v116, v117\n\t" \
  "v_mul_f32 v117, 0x4f000000, v117\n\t" \
  "v_mul_f32 v116, 0x47800000, v116\n\t" \
  "v_cvt_u32_f32 v117, v117\n\t" \
  "v_cvt_u32_f32 v116, v116\n\t" \
  "s_mov_b32 s53, s93\n\t" \
  "v_readfirstlane_b32 s46, v117\n\t" \
  "v_readfirstlane_b32 s52, v116\n\t" \
  "21:\n\t" \
  "s_add_i32 s44, s44, 1\n\t" \
  "s_and_b32 s58, s58, 4\n\t" \
  "s_and_b32 s76, s44, 64\n\t" \
  "s_or_b32 s42, s42, s76\n\t" \
  "s_cbranch_scc0 20b\n\t" \
  "s_branch 99f\n\t" \
  "14:\n\t" \
  "s_sleep 3\n\t" \
  "s_add_u32 s59, s59, 0x10000\n\t" \
  "s_cmp_lt_u32 s59, 0x100000\n\t" \
  "s_cbranch_scc1 13b\n\t" \
  "s_mov_b64 exec, -1\n\t" \
  "s_mov_b32 s42, 16\n\t" \
  "s_branch 99f\n\t" \
  "8:\n\t" \
  "s_bitcmp1_b32 s58, 1\n\t" \
  "s_cbranch_scc1 4b\n\t" \
  "s_bitcmp1_b32 s58, 2\n\t" \
  "s_cbranch_scc1 5b\n\t" \
  "v_max3_u32 v116, v32, v33, v34\n\t" \
  "v_max3_u32 v117, v35, v36, v37\n\t" \
  "v_max3_u32 v116, v116, v38, v39\n\t" \
  "v_max_u32 v116, v116, v117\n\t" \
  "v_cmp_eq_u32 vcc, -1, v116\n\t" \
  "s_cbranch_vccz 5b\n\t" \
  "s_branch 0f\n\t" \
  "9:\n\t" \
  "s_bitcmp1_b32 s58, 2\n\t" \
  "s_cbranch_scc1 6b\n\t" \
  "v_max3_u32 v116, v40, v41, v42\n\t" \
  "v_max3_u32 v117, v43, v44, v45\n\t" \
  "v_max3_u32 v116, v116, v46, v47\n\t" \
  "v_max_u32 v116, v116, v117\n\t" \
  "v_cmp_eq_u32 vcc, -1, v116\n\t" \
  "s_cbranch_vccz 6b\n\t" \
  "s_or_b32 s58, s58, 2\n\t" \
  "0:\n\t" \
  "s_or_b32 s58, s58, 1\n\t" \
  "11:\n\t" \
  "buffer_load_dwordx4 v[32:35], v100, %[rh], s40 offen sc1\n\t" \
  "buffer_load_dwordx4 v[36:39], v100, %[rh], s76 offen sc1\n\t" \
  "buffer_load_dwordx4 v[40:43], v100, %[rh], s77 offen sc1\n\t" \
  "buffer_load_dwordx4 v[44:47], v100, %[rh], s78 offen sc1\n\t" \
  "s_waitcnt lgkmcnt(0)\n\t" \
  "s_branch 10b\n\t" \
  "17:\n\t" \
  "s_add_u32 s59, s59, 1\n\t" \
  "s_bitcmp1_b32 s58, 2\n\t" \
  "s_cbranch_scc1 3b\n\t" \
  "s_waitcnt vmcnt(0)\n\t" \
  "v_max_u32 v116, v32, v33\n\t" \
  "v_max3_u32 v116, v116, v34, v35\n\t" \
  "v_max3_u32 v116, v116, v36, v37\n\t" \
  "v_max3_u32 v116, v116, v38, v39\n\t" \
  "v_max3_u32 v116, v116, v40, v41\n\t" \
  "v_max3_u32 v116, v116, v42, v43\n\t" \
  "v_max3_u32 v116, v116, v44, v45\n\t" \
  "v_max3_u32 v116, v116, v46, v47\n\t" \
  "v_cmp_eq_u32 vcc, -1, v116\n\t" \
  "s_cbranch_vccz 3b\n\t" \
  "s_bitcmp1_b32 s59, 6\n\t" \
  "s_cbranch_scc1 12f\n\t" \
  "buffer_load_dwordx4 v[32:35], v100, %[rh], s40 offen sc1\n\t" \
  "buffer_load_dwordx4 v[36:39], v100, %[rh], s76 offen sc1\n\t" \
  "buffer_load_dwordx4 v[40:43], v100, %[rh], s77 offen sc1\n\t" \
  "buffer_load_dwordx4 v[44:47], v100, %[rh], s78 offen sc1\n\t" \
  "s_branch 17b\n\t" \
  "12:\n\t" \
  "s_mov_b32 s42, s58\n\t" \
  "s_branch 99f\n\t" \
  "91:\n\t" \
  "s_mov_b32 s58, s42\n\t" \
  "20:\n\t" \
  "s_and_b32 s76, s44, 7\n\t" \
  "s_lshl_b32 s76, s76, 15\n\t" \
  "s_add_i32 s40, s54, s76\n\t" \
  "s_and_b32 s76, s44, 3\n\t" \
  "s_lshl_b32 s45, s76, 14\n\t" \
  "s_lshl_b32 s47, s76, 20\n\t" \
  "s_add_i32 s47, s47, s55\n\t" \
  "s_lshr_b32 s76, s47, 21\n\t" \
  "s_bitcmp1_b64 %[sel], s76\n\t" \
  "s_cselect_b32 s76, 0x2000, 0\n\t" \
  "s_add_i32 s47, s47, s76\n\t" \
  "s_add_i32 s49, s44, -1\n\t" \
  "s_and_b32 s48, s49, 2\n\t" \
  "s_lshl_b32 s48, s48, 13\n\t" \
  "s_and_b32 s76, s49, 1\n\t" \
  "s_lshl_b32 s76, s76, 12\n\t" \
  "s_add_i32 s48, s48, s76\n\t" \
  "s_lshl_b32 s50, s53, 8\n\t" \
  "s_mov_b32 s59, 0\n\t" \
  "s_mov_b32 s92, s51\n\t" \
  "s_mov_b32 s93, s53\n\t" \
  "s_xor_b32 s76, s40, 0x400\n\t" \
  "s_xor_b32 s77, s40, 0x800\n\t" \
  "s_xor_b32 s78, s40, 0xc00\n\t" \
  "s_cmp_lg_u32 s58, 0\n\t" \
  "s_cbranch_scc1 11f\n\t" \
  "10:\n\t" \
  "ds_read_b128 v[48:51], v102 offset:16384\n\t" \
  "ds_read_b128 v[52:55], v102 offset:17408\n\t" \
  "ds_read_b128 v[56:59], v102 offset:18432\n\t" \
  "ds_read_b128 v[60:63], v102 offset:19456\n\t" \
  "ds_read_b128 v[0:3], v102 offset:32768\n\t" \
  "ds_read_b128 v[4:7], v102 offset:33792\n\t" \
  "ds_read_b128 v[8:11], v102 offset:34816\n\t" \
  "ds_read_b128 v[12:15], v102 offset:35840\n\t" \
  "ds_read_b128 v[16:19], v102 offset:49152\n\t" \
  "ds_read_b128 v[20:23], v102 offset:50176\n\t" \
  "ds_read_b128 v[24:27], v102 offset:51200\n\t" \
  "ds_read_b128 v[28:31], v102 offset:52224\n\t" \
  "s_waitcnt lgkmcnt(4)\n\t" \
  "ds_read_b128 v[80:83], v102 offset:0\n\t" \
  "ds_read_b128 v[84:87], v102 offset:1024\n\t" \
  "ds_read_b128 v[88:91], v102 offset:2048\n\t" \
  "ds_read_b128 v[92:95], v102 offset:3072\n\t" \
  "s_bitcmp1_b32 s58, 1\n\t" \
  "s_cselect_b64 s[80:81], -1, 0\n\t" \
  "s_cmp_lg_u32 s58, 0\n\t" \
  "s_cbranch_scc1 17f\n\t" \
  "s_mov_b32 exec_hi, 0\n\t" \
  "16:\n\t" \
  "s_memtime vcc\n\t" \
  "s_waitcnt lgkmcnt(0)\n\t" \
  "s_sub_i32 s79, s41, vcc_lo\n\t" \
  "s_cmp_gt_i32 s79, 64\n\t" \
  "s_cbranch_scc1 16b\n\t" \
  "13:\n\t" \
  "buffer_load_dword v116, v112, %[rh], s48 offen sc1\n\t" \
  "s_waitcnt vmcnt(0)\n\t" \
  "15:\n\t" \
  "v_mov_b32 v117, 20000\n\t" \
  "v_cmp_ne_u32_sdwa vcc, v116, s49 src0_sel:BYTE_0 src1_sel:DWORD\n\t" \
  "v_subrev_u32 v116, s50, v116\n\t" \
  "v_ashrrev_i32 v116, 8, v116\n\t" \
  "s_nop 1\n\t" \
  "v_max_i32_dpp v116, v116, v116 quad_perm:[1,0,3,2] row_mask:0xf bank_mask:0xf\n\t" \
  "s_cbranch_vccnz 14f\n\t" \
  "s_nop 0\n\t" \
  "v_max_i32_dpp v116, v116, v116 quad_perm:[2,3,0,1] row_mask:0xf bank_mask:0xf\n\t" \
  "s_nop 1\n\t" \
  "v_max_i32_dpp v116, v116, v116 row_half_mirror row_mask:0xf bank_mask:0xf\n\t" \
  "s_nop 1\n\t" \
  "v_max_i32_dpp v116, v116, v116 row_mirror row_mask:0xf bank_mask:0xf\n\t" \
  "v_med3_i32 v116, v116, 0, v117\n\t" \
  "v_mul_u32_u24 v116, s52, v116\n\t" \
  "v_lshrrev_b32 v116, 16, v116\n\t" \
  "s_mov_b64 exec, -1\n\t" \
  "v_readfirstlane_b32 s84, v116\n\t" \
  "v_readlane_b32 s85, v116, 16\n\t" \
  "s_max_u32 s84, s84, s85\n\t" \
  "s_add_i32 s84, s84, s51\n\t" \
  "1:\n\t" \
  "s_memtime vcc\n\t" \
  "s_waitcnt lgkmcnt(0)\n\t" \
  "s_sub_i32 s79, s84, vcc_lo\n\t" \
  "s_cmp_gt_i32 s79, 128\n\t" \
  "s_cbranch_scc1 1b\n\t" \
  ".p2align 4\n\t" \
  "2:\n\t" \
  "s_sub_i32 s79, s79, 33\n\t" \
  "s_cmp_gt_i32 s79, 56\n\t" \
  "s_cbranch_scc1 2b\n\t" \
  "buffer_load_dwordx4 v[32:35], v100, %[rh], s40 offen sc1\n\t" \
  "s_nop 12\n\t" \
  "buffer_load_dwordx4 v[36:39], v100, %[rh], s76 offen sc1\n\t" \
  "s_nop 12\n\t" \
  "buffer_load_dwordx4 v[40:43], v100, %[rh], s77 offen sc1\n\t" \
  "s_nop 12\n\t" \
  "buffer_load_dwordx4 v[44:47], v100, %[rh], s78 offen sc1\n\t" \
  "s_memtime vcc\n\t" \
  "s_memrealtime s[82:83]\n\t" \
  "s_nop 4\n\t" \
  "s_memrealtime s[84:85]\n\t" \
  "s_nop 4\n\t" \
  "s_memrealtime s[56:57]\n\t" \
  "s_nop 4\n\t" \
  "s_memrealtime s[86:87]\n\t" \
  "s_nop 4\n\t" \
  "s_memrealtime s[88:89]\n\t" \
  "s_nop 4\n\t" \
  "s_memrealtime s[90:91]\n\t" \
  "s_mov_b32 s92, 0\n\t" \
  "s_waitcnt lgkmcnt(0)\n\t" \
  "s_cmp_eq_u32 s82, s84\n\t" \
  "s_addc_u32 s92, s92, 0\n\t" \
  "s_cmp_eq_u32 s82, s56\n\t" \
  "s_addc_u32 s92, s92, 0\n\t" \
  "s_cmp_eq_u32 s82, s86\n\t" \
  "s_addc_u32 s92, s92, 0\n\t" \
  "s_cmp_eq_u32 s82, s88\n\t" \
  "s_addc_u32 s92, s92, 0\n\t" \
  "s_cmp_eq_u32 s82, s90\n\t" \
  "s_addc_u32 s92, s92, 0\n\t" \
  "s_mul_i32 s92, s92, 24\n\t" \
  "s_add_i32 s92, s92, vcc_lo\n\t" \
  "s_lshl4_add_u32 s93, s82, 64\n\t" \
  "3:\n\t" \
  "v_mov_b64 v[72:73], 0\n\t" \
  "v_mov_b64 v[74:75], 0\n\t" \
  "v_mov_b64 v[76:77], 0\n\t" \
  "v_mov_b64 v[78:79], 0\n\t" \
  "s_waitcnt lgkmcnt(0)\n\t" \
  "s_waitcnt vmcnt(3)\n\t" \
  "v_smfmac_f32_16x16x64_bf16 v[72:75], v[32:35], v[48:55], v104\n\t" \
  "ds_write_b128 v101, v[32:35] offset:4096\n\t" \
  "v_smfmac_f32_16x16x64_bf16 v[76:79], v[32:35], v[56:63], v104\n\t" \
  "s_waitcnt vmcnt(2)\n\t" \
  "ds_write_b128 v101, v[36:39] offset:5120\n\t" \
  "v_smfmac_f32_16x16x64_bf16 v[72:75], v[36:39], a[48:55], v104\n\t" \
  "v_smfmac_f32_16x16x64_bf16 v[76:79], v[36:39], a[56:63], v104\n\t" \
  "s_nop 3\n\t" \
  "v_cmp_u_f32 vcc, v72, v73\n\t" \
  "v_cmp_u_f32 s[82:83], v74, v75\n\t" \
  "s_waitcnt vmcnt(1)\n\t" \
  "v_smfmac_f32_16x16x64_bf16 v[72:75], v[40:43], a[64:71], v104\n\t" \
  "s_or_b64 vcc, vcc, s[82:83]\n\t" \
  "s_or_b64 vcc, vcc, s[80:81]\n\t" \
  "v_smfmac_f32_16x16x64_bf16 v[76:79], v[40:43], a[72:79], v104\n\t" \
  "s_waitcnt lgkmcnt(0)\n\t" \
  "s_cbranch_scc1 8f\n\t" \
  "5:\n\t" \
  "s_barrier\n\t" \
  "4:\n\t" \
  "s_memtime s[56:57]\n\t" \
  "ds_read_b128 v[48:51], v101 offset:8192\n\t" \
  "ds_read_b128 v[52:55], v101 offset:9216\n\t" \
  "ds_read_b128 v[56:59], v101 offset:13312\n\t" \
  "s_waitcnt vmcnt(0)\n\t" \
  "v_smfmac_f32_16x16x64_bf16 v[72:75], v[44:47], a[80:87], v104\n\t" \
  "ds_read_b128 v[60:63], v101 offset:12288\n\t" \
  "ds_read_b128 v[64:67], v101 offset:1024\n\t" \
  "v_smfmac_f32_16x16x64_bf16 v[76:79], v[44:47], a[88:95], v104\n\t" \
  "ds_read_b128 v[68:71], v101 offset:0\n\t" \
  "ds_write_b128 v101, v[40:43] offset:6144\n\t" \
  "s_nop 1\n\t" \
  "v_cmp_u_f32 vcc, v72, v73\n\t" \
  "v_cmp_u_f32 s[82:83], v74, v75\n\t" \
  "s_waitcnt lgkmcnt(6)\n\t" \
  "v_smfmac_f32_16x16x64_bf16 v[72:75], v[48:51], v[0:7], v104\n\t" \
  "s_or_b64 vcc, vcc, s[82:83]\n\t" \
  "v_smfmac_f32_16x16x64_bf16 v[76:79], v[48:51], v[8:15], v104\n\t" \
  "s_cbranch_scc1 9f\n\t" \
  "6:\n\t" \
  "s_waitcnt lgkmcnt(5)\n\t" \
  "v_smfmac_f32_16x16x64_bf16 v[72:75], v[52:55], a[96:103], v104\n\t" \
  "ds_write_b128 v101, v[44:47] offset:7168\n\t" \
  "v_smfmac_f32_16x16x64_bf16 v[76:79], v[52:55], a[104:111], v104\n\t" \
  "v_mov_b64 v[40:41], 0\n\t" \
  "s_waitcnt lgkmcnt(5)\n\t" \
  "v_smfmac_f32_16x16x64_bf16 v[72:75], v[56:59], a[144:151], v104\n\t" \
  "v_mov_b64 v[42:43], 0\n\t" \
  "v_smfmac_f32_16x16x64_bf16 v[76:79], v[56:59], a[152:159], v104\n\t" \
  "s_waitcnt lgkmcnt(4)\n\t" \
  "v_smfmac_f32_16x16x64_bf16 v[72:75], v[60:63], v[16:23], v104\n\t" \
  "s_waitcnt lgkmcnt(0)\n\t" \
  "s_barrier\n\t" \
  "v_smfmac_f32_16x16x64_bf16 v[76:79], v[60:63], v[24:31], v104\n\t" \
  "ds_read_b128 v[48:51], v101 offset:10240\n\t" \
  "ds_read_b128 v[52:55], v101 offset:14336\n\t" \
  "v_smfmac_f32_16x16x64_bf16 v[72:75], v[64:67], a[0:7], v104\n\t" \
  "ds_read_b128 v[56:59], v101 offset:2048\n\t" \
  "ds_read_b128 v[60:63], v101 offset:11264\n\t" \
  "v_smfmac_f32_16x16x64_bf16 v[76:79], v[64:67], a[8:15], v104\n\t" \
  "ds_read_b128 v[32:35], v101 offset:15360\n\t" \
  "ds_read_b128 v[36:39], v101 offset:3072\n\t" \
  "v_smfmac_f32_16x16x64_bf16 v[72:75], v[68:71], v[80:87], v104\n\t" \
  "ds_read_b128 v[0:3], v103 offset:0\n\t" \
  "v_mov_b64 v[44:45], 0\n\t" \
  "v_smfmac_f32_16x16x64_bf16 v[76:79], v[68:71], v[88:95], v104\n\t" \
  "v_mov_b64 v[46:47], 0\n\t" \
  "s_waitcnt lgkmcnt(5)\n\t" \
  "v_smfmac_f32_16x16x64_bf16 v[72:75], v[48:51], a[112:119], v104\n\t" \
  "ds_read_b128 v[4:7], v103 offset:1024\n\t" \
  "v_mov_b64 v[80:81], 0\n\t" \
  "v_smfmac_f32_16x16x64_bf16 v[72:75], v[52:55], a[160:167], v104\n\t" \
  "v_mov_b64 v[82:83], 0\n\t" \
  "s_waitcnt lgkmcnt(2)\n\t" \
  "v_smfmac_f32_16x16x64_bf16 v[72:75], v[56:59], a[16:23], v104\n\t" \
  "ds_read_b128 v[8:11], v103 offset:2048\n\t" \
  "v_mov_b64 v[84:85], 0\n\t" \
  "v_smfmac_f32_16x16x64_bf16 v[72:75], v[60:63], a[128:135], v104\n\t" \
  "ds_read_b128 v[12:15], v103 offset:3072\n\t" \
  "v_mov_b64 v[86:87], 0\n\t" \
  "v_smfmac_f32_16x16x64_bf16 v[72:75], v[32:35], a[176:183], v104\n\t" \
  "ds_read_b128 v[16:19], v103 offset:16384\n\t" \
  "v_mov_b64 v[88:89], 0\n\t" \
  "v_smfmac_f32_16x16x64_bf16 v[72:75], v[36:39], a[32:39], v104\n\t" \
  "ds_read_b128 v[20:23], v103 offset:17408\n\t" \
  "v_mov_b64 v[90:91], 0\n\t" \
  "v_smfmac_f32_16x16x64_bf16 v[76:79], v[48:51], a[120:127], v104\n\t" \
  "ds_read_b128 v[24:27], v103 offset:18432\n\t" \
  "v_mov_b64 v[92:93], 0\n\t" \
  "v_smfmac_f32_16x16x64_bf16 v[76:79], v[52:55], a[168:175], v104\n\t" \
  "v_add_f32 v96, v72, v74\n\t" \
  "v_add_f32 v97, v73, v75\n\t" \
  "v_cvt_pk_bf16_f32 v96, v96, v97\n\t" \
  "ds_write_b32 v105, v96\n\t" \
  "v_smfmac_f32_16x16x64_bf16 v[76:79], v[56:59], a[24:31], v104\n\t" \
  "ds_read_b128 v[28:31], v103 offset:19456\n\t" \
  "v_mov_b64 v[94:95], 0\n\t" \
  "v_smfmac_f32_16x16x64_bf16 v[76:79], v[60:63], a[136:143], v104\n\t" \
  "v_smfmac_f32_16x16x64_bf16 v[76:79], v[32:35], a[184:191], v104\n\t" \
  "v_smfmac_f32_16x16x64_bf16 v[76:79], v[36:39], a[40:47], v104\n\t" \
  "s_waitcnt lgkmcnt(1)\n\t" \
  "s_barrier\n\t" \
  "ds_read_b64_tr_b16 v[96:97], v107 offset:0\n\t" \
  "ds_read_b64_tr_b16 v[98:99], v107 offset:64\n\t" \
  "ds_read_b64_tr_b16 v[56:57], v107 offset:1024\n\t" \
  "ds_read_b64_tr_b16 v[58:59], v107 offset:1088\n\t" \
  "s_nop 1\n\t" \
  "v_add_f32 v116, v76, v78\n\t" \
  "v_add_f32 v117, v77, v79\n\t" \
  "v_cvt_pk_bf16_f32 v116, v116, v117\n\t" \
  "ds_write_b32 v105, v116 offset:256\n\t" \
  "s_waitcnt lgkmcnt(3)\n\t" \
  "v_smfmac_f32_16x16x64_bf16 v[40:43], v[96:99], a[192:199], v106\n\t" \
  "v_smfmac_f32_16x16x64_bf16 v[44:47], v[96:99], a[224:231], v106\n\t" \
  "s_waitcnt lgkmcnt(0)\n\t" \
  "s_barrier\n\t" \
  "ds_read_b64_tr_b16 v[52:53], v107 offset:256\n\t" \
  "ds_read_b64_tr_b16 v[54:55], v107 offset:320\n\t" \
  "v_smfmac_f32_16x16x64_bf16 v[40:43], v[56:59], a[208:215], v106\n\t" \
  "ds_read_b64_tr_b16 v[60:61], v107 offset:1280\n\t" \
  "ds_read_b64_tr_b16 v[62:63], v107 offset:1344\n\t" \
  "v_smfmac_f32_16x16x64_bf16 v[44:47], v[56:59], v[0:7], v106\n\t" \
  "v_smfmac_f32_16x16x64_bf16 v[80:83], v[96:99], a[240:247], v106\n\t" \
  "v_mov_b64 v[32:33], 0\n\t" \
  "v_smfmac_f32_16x16x64_bf16 v[84:87], v[96:99], v[136:143], v106\n\t" \
  "v_mov_b64 v[34:35], 0\n\t" \
  "v_smfmac_f32_16x16x64_bf16 v[80:83], v[56:59], v[120:127], v106\n\t" \
  "v_mov_b64 v[36:37], 0\n\t" \
  "v_smfmac_f32_16x16x64_bf16 v[84:87], v[56:59], v[16:23], v106\n\t" \
  "s_waitcnt lgkmcnt(2)\n\t" \
  "v_smfmac_f32_16x16x64_bf16 v[40:43], v[52:55], a[200:207], v106\n\t" \
  "v_smfmac_f32_16x16x64_bf16 v[44:47], v[52:55], a[232:239], v106\n\t" \
  "s_xor_b32 s76, s47, 0x40000\n\t" \
  "s_waitcnt lgkmcnt(0)\n\t" \
  "v_smfmac_f32_16x16x64_bf16 v[40:43], v[60:63], a[216:223], v106\n\t" \
  "v_mov_b64 v[38:39], 0\n\t" \
  "s_xor_b32 s77, s47, 0x80000\n\t" \
  "v_smfmac_f32_16x16x64_bf16 v[44:47], v[60:63], v[8:15], v106\n\t" \
  "s_xor_b32 s78, s47, 0xc0000\n\t" \
  "v_smfmac_f32_16x16x64_bf16 v[80:83], v[52:55], a[248:255], v106\n\t" \
  "ds_read_b128 v[0:3], v103 offset:32768\n\t" \
  "v_cvt_pk_bf16_f32 v64, v40, v41\n\t" \
  "v_smfmac_f32_16x16x64_bf16 v[84:87], v[52:55], v[144:151], v106\n\t" \
  "ds_read_b128 v[4:7], v103 offset:33792\n\t" \
  "v_cvt_pk_bf16_f32 v65, v42, v43\n\t" \
  "v_smfmac_f32_16x16x64_bf16 v[80:83], v[60:63], v[128:135], v106\n\t" \
  "ds_read_b128 v[8:11], v103 offset:34816\n\t" \
  "v_cvt_pk_bf16_f32 v66, v44, v45\n\t" \
  "v_smfmac_f32_16x16x64_bf16 v[84:87], v[60:63], v[24:31], v106\n\t" \
  "ds_read_b128 v[12:15], v103 offset:35840\n\t" \
  "v_cvt_pk_bf16_f32 v67, v46, v47\n\t" \
  "v_smfmac_f32_16x16x64_bf16 v[88:91], v[96:99], v[152:159], v106\n\t" \
  "v_permlane16_swap_b32 v64, v66\n\t" \
  "v_cvt_pk_bf16_f32 v68, v80, v81\n\t" \
  "v_smfmac_f32_16x16x64_bf16 v[92:95], v[96:99], v[184:191], v106\n\t" \
  "v_permlane16_swap_b32 v65, v67\n\t" \
  "v_cvt_pk_bf16_f32 v69, v82, v83\n\t" \
  "v_smfmac_f32_16x16x64_bf16 v[88:91], v[52:55], v[160:167], v106\n\t" \
  "buffer_store_dwordx4 v[64:67], v108, %[ru], s47 offen sc1\n\t" \
  "v_cvt_pk_bf16_f32 v70, v84, v85\n\t" \
  "v_smfmac_f32_16x16x64_bf16 v[92:95], v[52:55], v[192:199], v106\n\t" \
  "v_cvt_pk_bf16_f32 v71, v86, v87\n\t" \
  "v_smfmac_f32_16x16x64_bf16 v[88:91], v[56:59], v[168:175], v106\n\t" \
  "ds_read_b128 v[16:19], v103 offset:49152\n\t" \
  "v_permlane16_swap_b32 v68, v70\n\t" \
  "s_waitcnt lgkmcnt(3)\n\t" \
  "v_smfmac_f32_16x16x64_bf16 v[92:95], v[56:59], v[0:7], v106\n\t" \
  "ds_read_b128 v[20:23], v103 offset:50176\n\t" \
  "v_permlane16_swap_b32 v69, v71\n\t" \
  "v_smfmac_f32_16x16x64_bf16 v[88:91], v[60:63], v[176:183], v106\n\t" \
  "ds_read_b128 v[24:27], v103 offset:51200\n\t" \
  "buffer_store_dwordx4 v[68:71], v108, %[ru], s76 offen sc1\n\t" \
  "s_waitcnt lgkmcnt(3)\n\t" \
  "v_smfmac_f32_16x16x64_bf16 v[92:95], v[60:63], v[8:15], v106\n\t" \
  "ds_read_b128 v[28:31], v103 offset:52224\n\t" \
  "v_smfmac_f32_16x16x64_bf16 v[32:35], v[96:99], v[200:207], v106\n\t" \
  "v_cvt_pk_bf16_f32 v72, v88, v89\n\t" \
  "v_smfmac_f32_16x16x64_bf16 v[36:39], v[96:99], v[232:239], v106\n\t" \
  "v_cvt_pk_bf16_f32 v73, v90, v91\n\t" \
  "v_smfmac_f32_16x16x64_bf16 v[32:35], v[52:55], v[208:215], v106\n\t" \
  "v_cvt_pk_bf16_f32 v74, v92, v93\n\t" \
  "v_smfmac_f32_16x16x64_bf16 v[36:39], v[52:55], v[240:247], v106\n\t" \
  "v_cvt_pk_bf16_f32 v75, v94, v95\n\t" \
  "v_smfmac_f32_16x16x64_bf16 v[32:35], v[56:59], v[216:223], v106\n\t" \
  "v_permlane16_swap_b32 v72, v74\n\t" \
  "v_smfmac_f32_16x16x64_bf16 v[32:35], v[60:63], v[224:231], v106\n\t" \
  "v_permlane16_swap_b32 v73, v75\n\t" \
  "s_waitcnt lgkmcnt(2)\n\t" \
  "v_smfmac_f32_16x16x64_bf16 v[36:39], v[56:59], v[16:23], v106\n\t" \
  "buffer_store_dwordx4 v[72:75], v108, %[ru], s77 offen sc1\n\t" \
  "s_waitcnt lgkmcnt(0)\n\t" \
  "v_smfmac_f32_16x16x64_bf16 v[36:39], v[60:63], v[24:31], v106\n\t" \
  "v_cvt_pk_bf16_f32 v76, v32, v33\n\t" \
  "v_cvt_pk_bf16_f32 v77, v34, v35\n\t" \
  "s_nop 5\n\t" \
  "v_cvt_pk_bf16_f32 v78, v36, v37\n\t" \
  "v_cvt_pk_bf16_f32 v79, v38, v39\n\t" \
  "s_nop 0\n\t" \
  "v_permlane16_swap_b32 v76, v78\n\t" \
  "v_permlane16_swap_b32 v77, v79\n\t" \
  "buffer_store_dwordx4 v[76:79], v108, %[ru], s78 offen sc1\n\t" \
  "s_memtime s[76:77]\n\t" \
  "s_mov_b32 s42, 0\n\t" \
  "s_waitcnt lgkmcnt(0)\n\t" \
  "s_mov_b32 s57, s76\n\t" \
  "s_add_i32 s41, s56, 1950\n\t" \
  "s_sub_i32 s76, s92, s51\n\t" \
  "s_sub_i32 s77, s93, s53\n\t" \
  "s_cmp_eq_u32 s76, 0\n\t" \
  "s_cbranch_scc1 21f\n\t" \
  "v_cvt_f32_u32 v117, s76\n\t" \
  "v_cvt_f32_u32 v116, s77\n\t" \
  "v_rcp_f32 v117, v117\n\t" \
  "s_mov_b32 s51, s92\n\t" \
  "v_mul_f32 v117, v117, v116\n\t" \
  "v_max_f32 v117, 0x3f028f5c, v117\n\t" \
  "v_min_f32 v117, 0x3ffeb852, v117\n\t" \
  "v_rcp_f32 v116, v117\n\t" \
  "v_mul_f32 v117, 0x4f000000, v117\n\t" \
  "v_mul_f32 v116, 0x47800000, v116\n\t" \
  "v_cvt_u32_f32 v117, v117\n\t" \
  "v_cvt_u32_f32 v116, v116\n\t" \
  "s_mov_b32 s53, s93\n\t" \
  "v_readfirstlane_b32 s46, v117\n\t" \
  "v_readfirstlane_b32 s52, v116\n\t" \
  "21:\n\t" \
  "s_add_i32 s44, s44, 1\n\t" \
  "s_and_b32 s58, s58, 4\n\t" \
  "s_and_b32 s76, s44, 64\n\t" \
  "s_or_b32 s42, s42, s76\n\t" \
  "s_cbranch_scc0 20b\n\t" \
  "s_branch 99f\n\t" \
  "14:\n\t" \
  "s_sleep 3\n\t" \
  "s_add_u32 s59, s59, 0x10000\n\t" \
  "s_cmp_lt_u32 s59, 0x100000\n\t" \
  "s_cbranch_scc1 13b\n\t" \
  "s_mov_b64 exec, -1\n\t" \
  "s_mov_b32 s42, 16\n\t" \
  "s_branch 99f\n\t" \
  "8:\n\t" \
  "s_bitcmp1_b32 s58, 1\n\t" \
  "s_cbranch_scc1 4b\n\t" \
  "s_bitcmp1_b32 s58, 2\n\t" \
  "s_cbranch_scc1 5b\n\t" \
  "v_max3_u32 v116, v32, v33, v34\n\t" \
  "v_max3_u32 v117, v35, v36, v37\n\t" \
  "v_max3_u32 v116, v116, v38, v39\n\t" \
  "v_max_u32 v116, v116, v117\n\t" \
  "v_cmp_eq_u32 vcc, -1, v116\n\t" \
  "s_cbranch_vccz 5b\n\t" \
  "s_branch 0f\n\t" \
  "9:\n\t" \
  "s_bitcmp1_b32 s58, 2\n\t" \
  "s_cbranch_scc1 6b\n\t" \
  "v_max3_u32 v116, v40, v41, v42\n\t" \
  "v_max3_u32 v117, v43, v44, v45\n\t" \
  "v_max3_u32 v116, v116, v46, v47\n\t" \
  "v_max_u32 v116, v116, v117\n\t" \
  "v_cmp_eq_u32 vcc, -1, v116\n\t" \
  "s_cbranch_vccz 6b\n\t" \
  "s_or_b32 s58, s58, 2\n\t" \
  "0:\n\t" \
  "s_or_b32 s58, s58, 1\n\t" \
  "11:\n\t" \
  "buffer_load_dwordx4 v[32:35], v100, %[rh], s40 offen sc1\n\t" \
  "buffer_load_dwordx4 v[36:39], v100, %[rh], s76 offen sc1\n\t" \
  "buffer_load_dwordx4 v[40:43], v100, %[rh], s77 offen sc1\n\t" \
  "buffer_load_dwordx4 v[44:47], v100, %[rh], s78 offen sc1\n\t" \
  "s_waitcnt lgkmcnt(0)\n\t" \
  "s_branch 10b\n\t" \
  "17:\n\t" \
  "s_add_u32 s59, s59, 1\n\t" \
  "s_bitcmp1_b32 s58, 2\n\t" \
  "s_cbranch_scc1 3b\n\t" \
  "s_waitcnt vmcnt(0)\n\t" \
  "v_max_u32 v116, v32, v33\n\t" \
  "v_max3_u32 v116, v116, v34, v35\n\t" \
  "v_max3_u32 v116, v116, v36, v37\n\t" \
  "v_max3_u32 v116, v116, v38, v39\n\t" \
  "v_max3_u32 v116, v116, v40, v41\n\t" \
  "v_max3_u32 v116, v116, v42, v43\n\t" \
  "v_max3_u32 v116, v116, v44, v45\n\t" \
  "v_max3_u32 v116, v116, v46, v47\n\t" \
  "v_cmp_eq_u32 vcc, -1, v116\n\t" \
  "s_cbranch_vccz 3b\n\t" \
  "s_bitcmp1_b32 s59, 6\n\t" \
  "s_cbranch_scc1 12f\n\t" \
  "buffer_load_dwordx4 v[32:35], v100, %[rh], s40 offen sc1\n\t" \
  "buffer_load_dwordx4 v[36:39], v100, %[rh], s76 offen sc1\n\t" \
  "buffer_load_dwordx4 v[40:43], v100, %[rh], s77 offen sc1\n\t" \
  "buffer_load_dwordx4 v[44:47], v100, %[rh], s78 offen sc1\n\t" \
  "s_branch 17b\n\t" \
  "12:\n\t" \
  "s_mov_b32 s42, s58\n\t" \
  "s_branch 99f\n\t" \
  "92:\n\t" \
  "s_mov_b32 s58, s42\n\t" \
  "20:\n\t" \
  "s_and_b32 s76, s44, 7\n\t" \
  "s_lshl_b32 s76, s76, 15\n\t" \
  "s_add_i32 s40, s54, s76\n\t" \
  "s_and_b32 s76, s44, 3\n\t" \
  "s_lshl_b32 s45, s76, 14\n\t" \
  "s_lshl_b32 s47, s76, 20\n\t" \
  "s_add_i32 s47, s47, s55\n\t" \
  "s_lshr_b32 s76, s47, 21\n\t" \
  "s_bitcmp1_b64 %[sel], s76\n\t" \
  "s_cselect_b32 s76, 0x2000, 0\n\t" \
  "s_add_i32 s47, s47, s76\n\t" \
  "s_add_i32 s49, s44, -1\n\t" \
  "s_and_b32 s48, s49, 2\n\t" \
  "s_lshl_b32 s48, s48, 13\n\t" \
  "s_and_b32 s76, s49, 1\n\t" \
  "s_lshl_b32 s76, s76, 12\n\t" \
  "s_add_i32 s48, s48, s76\n\t" \
  "s_lshl_b32 s50, s53, 8\n\t" \
  "s_mov_b32 s59, 0\n\t" \
  "s_mov_b32 s92, s51\n\t" \
  "s_mov_b32 s93, s53\n\t" \
  "s_xor_b32 s76, s40, 0x400\n\t" \
  "s_xor_b32 s77, s40, 0x800\n\t" \
  "s_xor_b32 s78, s40, 0xc00\n\t" \
  "s_cmp_lg_u32 s58, 0\n\t" \
  "s_cbranch_scc1 11f\n\t" \
  "10:\n\t" \
  "ds_read_b128 v[48:51], v102 offset:32768\n\t" \
  "ds_read_b128 v[52:55], v102 offset:33792\n\t" \
  "ds_read_b128 v[56:59], v102 offset:34816\n\t" \
  "ds_read_b128 v[60:63], v102 offset:35840\n\t" \
  "ds_read_b128 v[0:3], v102 offset:49152\n\t" \
  "ds_read_b128 v[4:7], v102 offset:50176\n\t" \
  "ds_read_b128 v[8:11], v102 offset:51200\n\t" \
  "ds_read_b128 v[12:15], v102 offset:52224\n\t" \
  "ds_read_b128 v[16:19], v102 offset:0\n\t" \
  "ds_read_b128 v[20:23], v102 offset:1024\n\t" \
  "ds_read_b128 v[24:27], v102 offset:2048\n\t" \
  "ds_read_b128 v[28:31], v102 offset:3072\n\t" \
  "s_waitcnt lgkmcnt(4)\n\t" \
  "ds_read_b128 v[80:83], v102 offset:16384\n\t" \
  "ds_read_b128 v[84:87], v102 offset:17408\n\t" \
  "ds_read_b128 v[88:91], v102 offset:18432\n\t" \
  "ds_read_b128 v[92:95], v102 offset:19456\n\t" \
  "s_bitcmp1_b32 s58, 1\n\t" \
  "s_cselect_b64 s[80:81], -1, 0\n\t" \
  "s_cmp_lg_u32 s58, 0\n\t" \
  "s_cbranch_scc1 17f\n\t" \
  "s_mov_b32 exec_hi, 0\n\t" \
  "16:\n\t" \
  "s_memtime vcc\n\t" \
  "s_waitcnt lgkmcnt(0)\n\t" \
  "s_sub_i32 s79, s41, vcc_lo\n\t" \
  "s_cmp_gt_i32 s79, 64\n\t" \
  "s_cbranch_scc1 16b\n\t" \
  "13:\n\t" \
  "buffer_load_dword v116, v112, %[rh], s48 offen sc1\n\t" \
  "s_waitcnt vmcnt(0)\n\t" \
  "15:\n\t" \
  "v_mov_b32 v117, 20000\n\t" \
  "v_cmp_ne_u32_sdwa vcc, v116, s49 src0_sel:BYTE_0 src1_sel:DWORD\n\t" \
  "v_subrev_u32 v116, s50, v116\n\t" \
  "v_ashrrev_i32 v116, 8, v116\n\t" \
  "s_nop 1\n\t" \
  "v_max_i32_dpp v116, v116, v116 quad_perm:[1,0,3,2] row_mask:0xf bank_mask:0xf\n\t" \
  "s_cbranch_vccnz 14f\n\t" \
  "s_nop 0\n\t" \
  "v_max_i32_dpp v116, v116, v116 quad_perm:[2,3,0,1] row_mask:0xf bank_mask:0xf\n\t" \
  "s_nop 1\n\t" \
  "v_max_i32_dpp v116, v116, v116 row_half_mirror row_mask:0xf bank_mask:0xf\n\t" \
  "s_nop 1\n\t" \
  "v_max_i32_dpp v116, v116, v116 row_mirror row_mask:0xf bank_mask:0xf\n\t" \
  "v_med3_i32 v116, v116, 0, v117\n\t" \
  "v_mul_u32_u24 v116, s52, v116\n\t" \
  "v_lshrrev_b32 v116, 16, v116\n\t" \
  "s_mov_b64 exec, -1\n\t" \
  "v_readfirstlane_b32 s84, v116\n\t" \
  "v_readlane_b32 s85, v116, 16\n\t" \
  "s_max_u32 s84, s84, s85\n\t" \
  "s_add_i32 s84, s84, s51\n\t" \
  "1:\n\t" \
  "s_memtime vcc\n\t" \
  "s_waitcnt lgkmcnt(0)\n\t" \
  "s_sub_i32 s79, s84, vcc_lo\n\t" \
  "s_cmp_gt_i32 s79, 128\n\t" \
  "s_cbranch_scc1 1b\n\t" \
  ".p2align 4\n\t" \
  "2:\n\t" \
  "s_sub_i32 s79, s79, 33\n\t" \
  "s_cmp_gt_i32 s79, 56\n\t" \
  "s_cbranch_scc1 2b\n\t" \
  "buffer_load_dwordx4 v[32:35], v100, %[rh], s40 offen sc1\n\t" \
  "s_nop 12\n\t" \
  "buffer_load_dwordx4 v[36:39], v100, %[rh], s76 offen sc1\n\t" \
  "s_nop 12\n\t" \
  "buffer_load_dwordx4 v[40:43], v100, %[rh], s77 offen sc1\n\t" \
  "s_nop 12\n\t" \
  "buffer_load_dwordx4 v[44:47], v100, %[rh], s78 offen sc1\n\t" \
  "s_memtime vcc\n\t" \
  "s_memrealtime s[82:83]\n\t" \
  "s_nop 4\n\t" \
  "s_memrealtime s[84:85]\n\t" \
  "s_nop 4\n\t" \
  "s_memrealtime s[56:57]\n\t" \
  "s_nop 4\n\t" \
  "s_memrealtime s[86:87]\n\t" \
  "s_nop 4\n\t" \
  "s_memrealtime s[88:89]\n\t" \
  "s_nop 4\n\t" \
  "s_memrealtime s[90:91]\n\t" \
  "s_mov_b32 s92, 0\n\t" \
  "s_waitcnt lgkmcnt(0)\n\t" \
  "s_cmp_eq_u32 s82, s84\n\t" \
  "s_addc_u32 s92, s92, 0\n\t" \
  "s_cmp_eq_u32 s82, s56\n\t" \
  "s_addc_u32 s92, s92, 0\n\t" \
  "s_cmp_eq_u32 s82, s86\n\t" \
  "s_addc_u32 s92, s92, 0\n\t" \
  "s_cmp_eq_u32 s82, s88\n\t" \
  "s_addc_u32 s92, s92, 0\n\t" \
  "s_cmp_eq_u32 s82, s90\n\t" \
  "s_addc_u32 s92, s92, 0\n\t" \
  "s_mul_i32 s92, s92, 24\n\t" \
  "s_add_i32 s92, s92, vcc_lo\n\t" \
  "s_lshl4_add_u32 s93, s82, 64\n\t" \
  "3:\n\t" \
  "v_mov_b64 v[72:73], 0\n\t" \
  "v_mov_b64 v[74:75], 0\n\t" \
  "v_mov_b64 v[76:77], 0\n\t" \
  "v_mov_b64 v[78:79], 0\n\t" \
  "s_waitcnt lgkmcnt(0)\n\t" \
  "s_waitcnt vmcnt(3)\n\t" \
  "v_smfmac_f32_16x16x64_bf16 v[72:75], v[32:35], v[48:55], v104\n\t" \
  "ds_write_b128 v101, v[32:35] offset:8192\n\t" \
  "v_smfmac_f32_16x16x64_bf16 v[76:79], v[32:35], v[56:63], v104\n\t" \
  "s_waitcnt vmcnt(2)\n\t" \
  "ds_write_b128 v101, v[36:39] offset:9216\n\t" \
  "v_smfmac_f32_16x16x64_bf16 v[72:75], v[36:39], a[96:103], v104\n\t" \
  "v_smfmac_f32_16x16x64_bf16 v[76:79], v[36:39], a[104:111], v104\n\t" \
  "s_nop 3\n\t" \
  "v_cmp_u_f32 vcc, v72, v73\n\t" \
  "v_cmp_u_f32 s[82:83], v74, v75\n\t" \
  "s_waitcnt vmcnt(1)\n\t" \
  "v_smfmac_f32_16x16x64_bf16 v[72:75], v[40:43], a[112:119], v104\n\t" \
  "s_or_b64 vcc, vcc, s[82:83]\n\t" \
  "s_or_b64 vcc, vcc, s[80:81]\n\t" \
  "v_smfmac_f32_16x16x64_bf16 v[76:79], v[40:43], a[120:127], v104\n\t" \
  "s_waitcnt lgkmcnt(0)\n\t" \
  "s_cbranch_scc1 8f\n\t" \
  "5:\n\t" \
  "s_barrier\n\t" \
  "4:\n\t" \
  "s_memtime s[56:57]\n\t" \
  "ds_read_b128 v[48:51], v101 offset:12288\n\t" \
  "ds_read_b128 v[52:55], v101 offset:13312\n\t" \
  "ds_read_b128 v[56:59], v101 offset:1024\n\t" \
  "s_waitcnt vmcnt(0)\n\t" \
  "v_smfmac_f32_16x16x64_bf16 v[72:75], v[44:47], a[128:135], v104\n\t" \
  "ds_read_b128 v[60:63], v101 offset:0\n\t" \
  "ds_read_b128 v[64:67], v101 offset:5120\n\t" \
  "v_smfmac_f32_16x16x64_bf16 v[76:79], v[44:47], a[136:143], v104\n\t" \
  "ds_read_b128 v[68:71], v101 offset:4096\n\t" \
  "ds_write_b128 v101, v[40:43] offset:10240\n\t" \
  "s_nop 1\n\t" \
  "v_cmp_u_f32 vcc, v72, v73\n\t" \
  "v_cmp_u_f32 s[82:83], v74, v75\n\t" \
  "s_waitcnt lgkmcnt(6)\n\t" \
  "v_smfmac_f32_16x16x64_bf16 v[72:75], v[48:51], v[0:7], v104\n\t" \
  "s_or_b64 vcc, vcc, s[82:83]\n\t" \
  "v_smfmac_f32_16x16x64_bf16 v[76:79], v[48:51], v[8:15], v104\n\t" \
  "s_cbranch_scc1 9f\n\t" \
  "6:\n\t" \
  "s_waitcnt lgkmcnt(5)\n\t" \
  "v_smfmac_f32_16x16x64_bf16 v[72:75], v[52:55], a[144:151], v104\n\t" \
  "ds_write_b128 v101, v[44:47] offset:11264\n\t" \
  "v_smfmac_f32_16x16x64_bf16 v[76:79], v[52:55], a[152:159], v104\n\t" \
  "v_mov_b64 v[40:41], 0\n\t" \
  "s_waitcnt lgkmcnt(5)\n\t" \
  "v_smfmac_f32_16x16x64_bf16 v[72:75], v[56:59], a[0:7], v104\n\t" \
  "v_mov_b64 v[42:43], 0\n\t" \
  "v_smfmac_f32_16x16x64_bf16 v[76:79], v[56:59], a[8:15], v104\n\t" \
  "s_waitcnt lgkmcnt(4)\n\t" \
  "v_smfmac_f32_16x16x64_bf16 v[72:75], v[60:63], v[16:23], v104\n\t" \
  "s_waitcnt lgkmcnt(0)\n\t" \
  "s_barrier\n\t" \
  "v_smfmac_f32_16x16x64_bf16 v[76:79], v[60:63], v[24:31], v104\n\t" \
  "ds_read_b128 v[48:51], v101 offset:14336\n\t" \
  "ds_read_b128 v[52:55], v101 offset:2048\n\t" \
  "v_smfmac_f32_16x16x64_bf16 v[72:75], v[64:67], a[48:55], v104\n\t" \
  "ds_read_b128 v[56:59], v101 offset:6144\n\t" \
  "ds_read_b128 v[60:63], v101 offset:15360\n\t" \
  "v_smfmac_f32_16x16x64_bf16 v[76:79], v[64:67], a[56:63], v104\n\t" \
  "ds_read_b128 v[32:35], v101 offset:3072\n\t" \
  "ds_read_b128 v[36:39], v101 offset:7168\n\t" \
  "v_smfmac_f32_16x16x64_bf16 v[72:75], v[68:71], v[80:87], v104\n\t" \
  "ds_read_b128 v[0:3], v103 offset:0\n\t" \
  "v_mov_b64 v[44:45], 0\n\t" \
  "v_smfmac_f32_16x16x64_bf16 v[76:79], v[68:71], v[88:95], v104\n\t" \
  "v_mov_b64 v[46:47], 0\n\t" \
  "s_waitcnt lgkmcnt(5)\n\t" \
  "v_smfmac_f32_16x16x64_bf16 v[72:75], v[48:51], a[160:167], v104\n\t" \
  "ds_read_b128 v[4:7], v103 offset:1024\n\t" \
  "v_mov_b64 v[80:81], 0\n\t" \
  "v_smfmac_f32_16x16x64_bf16 v[72:75], v[52:55], a[16:23], v104\n\t" \
  "v_mov_b64 v[82:83], 0\n\t" \
  "s_waitcnt lgkmcnt(2)\n\t" \
  "v_smfmac_f32_16x16x64_bf16 v[72:75], v[56:59], a[64:71], v104\n\t" \
  "ds_read_b128 v[8:11], v103 offset:2048\n\t" \
  "v_mov_b64 v[84:85], 0\n\t" \
  "v_smfmac_f32_16x16x64_bf16 v[72:75], v[60:63], a[176:183], v104\n\t" \
  "ds_read_b128 v[12:15], v103 offset:3072\n\t" \
  "v_mov_b64 v[86:87], 0\n\t" \
  "v_smfmac_f32_16x16x64_bf16 v[72:75], v[32:35], a[32:39], v104\n\t" \
  "ds_read_b128 v[16:19], v103 offset:16384\n\t" \
  "v_mov_b64 v[88:89], 0\n\t" \
  "v_smfmac_f32_16x16x64_bf16 v[72:75], v[36:39], a[80:87], v104\n\t" \
  "ds_read_b128 v[20:23], v103 offset:17408\n\t" \
  "v_mov_b64 v[90:91], 0\n\t" \
  "v_smfmac_f32_16x16x64_bf16 v[76:79], v[48:51], a[168:175], v104\n\t" \
  "ds_read_b128 v[24:27], v103 offset:18432\n\t" \
  "v_mov_b64 v[92:93], 0\n\t" \
  "v_smfmac_f32_16x16x64_bf16 v[76:79], v[52:55], a[24:31], v104\n\t" \
  "v_add_f32 v96, v72, v74\n\t" \
  "v_add_f32 v97, v73, v75\n\t" \
  "v_cvt_pk_bf16_f32 v96, v96, v97\n\t" \
  "ds_write_b32 v105, v96\n\t" \
  "v_smfmac_f32_16x16x64_bf16 v[76:79], v[56:59], a[72:79], v104\n\t" \
  "ds_read_b128 v[28:31], v103 offset:19456\n\t" \
  "v_mov_b64 v[94:95], 0\n\t" \
  "v_smfmac_f32_16x16x64_bf16 v[76:79], v[60:63], a[184:191], v104\n\t" \
  "v_smfmac_f32_16x16x64_bf16 v[76:79], v[32:35], a[40:47], v104\n\t" \
  "v_smfmac_f32_16x16x64_bf16 v[76:79], v[36:39], a[88:95], v104\n\t" \
  "s_waitcnt lgkmcnt(1)\n\t" \
  "s_barrier\n\t" \
  "ds_read_b64_tr_b16 v[96:97], v107 offset:0\n\t" \
  "ds_read_b64_tr_b16 v[98:99], v107 offset:64\n\t" \
  "ds_read_b64_tr_b16 v[56:57], v107 offset:1024\n\t" \
  "ds_read_b64_tr_b16 v[58:59], v107 offset:1088\n\t" \
  "s_nop 1\n\t" \
  "v_add_f32 v116, v76, v78\n\t" \
  "v_add_f32 v117, v77, v79\n\t" \
  "v_cvt_pk_bf16_f32 v116, v116, v117\n\t" \
  "ds_write_b32 v105, v116 offset:256\n\t" \
  "s_waitcnt lgkmcnt(3)\n\t" \
  "v_smfmac_f32_16x16x64_bf16 v[40:43], v[96:99], a[192:199], v106\n\t" \
  "v_smfmac_f32_16x16x64_bf16 v[44:47], v[96:99], a[224:231], v106\n\t" \
  "s_waitcnt lgkmcnt(0)\n\t" \
  "s_barrier\n\t" \
  "ds_read_b64_tr_b16 v[52:53], v107 offset:256\n\t" \
  "ds_read_b64_tr_b16 v[54:55], v107 offset:320\n\t" \
  "v_smfmac_f32_16x16x64_bf16 v[40:43], v[56:59], a[208:215], v106\n\t" \
  "ds_read_b64_tr_b16 v[60:61], v107 offset:1280\n\t" \
  "ds_read_b64_tr_b16 v[62:63], v107 offset:1344\n\t" \
  "v_smfmac_f32_16x16x64_bf16 v[44:47], v[56:59], v[0:7], v106\n\t" \
  "v_smfmac_f32_16x16x64_bf16 v[80:83], v[96:99], a[240:247], v106\n\t" \
  "v_mov_b64 v[32:33], 0\n\t" \
  "v_smfmac_f32_16x16x64_bf16 v[84:87], v[96:99], v[136:143], v106\n\t" \
  "v_mov_b64 v[34:35], 0\n\t" \
  "v_smfmac_f32_16x16x64_bf16 v[80:83], v[56:59], v[120:127], v106\n\t" \
  "v_mov_b64 v[36:37], 0\n\t" \
  "v_smfmac_f32_16x16x64_bf16 v[84:87], v[56:59], v[16:23], v106\n\t" \
  "s_waitcnt lgkmcnt(2)\n\t" \
  "v_smfmac_f32_16x16x64_bf16 v[40:43], v[52:55], a[200:207], v106\n\t" \
  "v_smfmac_f32_16x16x64_bf16 v[44:47], v[52:55], a[232:239], v106\n\t" \
  "s_xor_b32 s76, s47, 0x40000\n\t" \
  "s_waitcnt lgkmcnt(0)\n\t" \
  "v_smfmac_f32_16x16x64_bf16 v[40:43], v[60:63], a[216:223], v106\n\t" \
  "v_mov_b64 v[38:39], 0\n\t" \
  "s_xor_b32 s77, s47, 0x80000\n\t" \
  "v_smfmac_f32_16x16x64_bf16 v[44:47], v[60:63], v[8:15], v106\n\t" \
  "s_xor_b32 s78, s47, 0xc0000\n\t" \
  "v_smfmac_f32_16x16x64_bf16 v[80:83], v[52:55], a[248:255], v106\n\t" \
  "ds_read_b128 v[0:3], v103 offset:32768\n\t" \
  "v_cvt_pk_bf16_f32 v64, v40, v41\n\t" \
  "v_smfmac_f32_16x16x64_bf16 v[84:87], v[52:55], v[144:151], v106\n\t" \
  "ds_read_b128 v[4:7], v103 offset:33792\n\t" \
  "v_cvt_pk_bf16_f32 v65, v42, v43\n\t" \
  "v_smfmac_f32_16x16x64_bf16 v[80:83], v[60:63], v[128:135], v106\n\t" \
  "ds_read_b128 v[8:11], v103 offset:34816\n\t" \
  "v_cvt_pk_bf16_f32 v66, v44, v45\n\t" \
  "v_smfmac_f32_16x16x64_bf16 v[84:87], v[60:63], v[24:31], v106\n\t" \
  "ds_read_b128 v[12:15], v103 offset:35840\n\t" \
  "v_cvt_pk_bf16_f32 v67, v46, v47\n\t" \
  "v_smfmac_f32_16x16x64_bf16 v[88:91], v[96:99], v[152:159], v106\n\t" \
  "v_permlane16_swap_b32 v64, v66\n\t" \
  "v_cvt_pk_bf16_f32 v68, v80, v81\n\t" \
  "v_smfmac_f32_16x16x64_bf16 v[92:95], v[96:99], v[184:191], v106\n\t" \
  "v_permlane16_swap_b32 v65, v67\n\t" \
  "v_cvt_pk_bf16_f32 v69, v82, v83\n\t" \
  "v_smfmac_f32_16x16x64_bf16 v[88:91], v[52:55], v[160:167], v106\n\t" \
  "v_cvt_pk_bf16_f32 v70, v84, v85\n\t" \
  "v_smfmac_f32_16x16x64_bf16 v[92:95], v[52:55], v[192:199], v106\n\t" \
  "buffer_store_dwordx4 v[64:67], v108, %[ru], s47 offen sc1\n\t" \
  "v_cvt_pk_bf16_f32 v71, v86, v87\n\t" \
  "v_smfmac_f32_16x16x64_bf16 v[88:91], v[56:59], v[168:175], v106\n\t" \
  "ds_read_b128 v[16:19], v103 offset:49152\n\t" \
  "v_permlane16_swap_b32 v68, v70\n\t" \
  "s_waitcnt lgkmcnt(3)\n\t" \
  "v_smfmac_f32_16x16x64_bf16 v[92:95], v[56:59], v[0:7], v106\n\t" \
  "ds_read_b128 v[20:23], v103 offset:50176\n\t" \
  "v_permlane16_swap_b32 v69, v71\n\t" \
  "v_smfmac_f32_16x16x64_bf16 v[88:91], v[60:63], v[176:183], v106\n\t" \
  "ds_read_b128 v[24:27], v103 offset:51200\n\t" \
  "s_waitcnt lgkmcnt(3)\n\t" \
  "v_smfmac_f32_16x16x64_bf16 v[92:95], v[60:63], v[8:15], v106\n\t" \
  "ds_read_b128 v[28:31], v103 offset:52224\n\t" \
  "buffer_store_dwordx4 v[68:71], v108, %[ru], s76 offen sc1\n\t" \
  "v_smfmac_f32_16x16x64_bf16 v[32:35], v[96:99], v[200:207], v106\n\t" \
  "v_cvt_pk_bf16_f32 v72, v88, v89\n\t" \
  "v_smfmac_f32_16x16x64_bf16 v[36:39], v[96:99], v[232:239], v106\n\t" \
  "v_cvt_pk_bf16_f32 v73, v90, v91\n\t" \
  "v_smfmac_f32_16x16x64_bf16 v[32:35], v[52:55], v[208:215], v106\n\t" \
  "v_cvt_pk_bf16_f32 v74, v92, v93\n\t" \
  "v_smfmac_f32_16x16x64_bf16 v[36:39], v[52:55], v[240:247], v106\n\t" \
  "v_cvt_pk_bf16_f32 v75, v94, v95\n\t" \
  "v_smfmac_f32_16x16x64_bf16 v[32:35], v[56:59], v[216:223], v106\n\t" \
  "v_permlane16_swap_b32 v72, v74\n\t" \
  "v_smfmac_f32_16x16x64_bf16 v[32:35], v[60:63], v[224:231], v106\n\t" \
  "v_permlane16_swap_b32 v73, v75\n\t" \
  "s_waitcnt lgkmcnt(2)\n\t" \
  "v_smfmac_f32_16x16x64_bf16 v[36:39], v[56:59], v[16:23], v106\n\t" \
  "s_waitcnt lgkmcnt(0)\n\t" \
  "v_smfmac_f32_16x16x64_bf16 v[36:39], v[60:63], v[24:31], v106\n\t" \
  "buffer_store_dwordx4 v[72:75], v108, %[ru], s77 offen sc1\n\t" \
  "v_cvt_pk_bf16_f32 v76, v32, v33\n\t" \
  "v_cvt_pk_bf16_f32 v77, v34, v35\n\t" \
  "s_nop 5\n\t" \
  "v_cvt_pk_bf16_f32 v78, v36, v37\n\t" \
  "v_cvt_pk_bf16_f32 v79, v38, v39\n\t" \
  "s_nop 0\n\t" \
  "v_permlane16_swap_b32 v76, v78\n\t" \
  "v_permlane16_swap_b32 v77, v79\n\t" \
  "buffer_store_dwordx4 v[76:79], v108, %[ru], s78 offen sc1\n\t" \
  "s_memtime s[76:77]\n\t" \
  "s_mov_b32 s42, 0\n\t" \
  "s_waitcnt lgkmcnt(0)\n\t" \
  "s_mov_b32 s57, s76\n\t" \
  "s_add_i32 s41, s56, 1950\n\t" \
  "s_sub_i32 s76, s92, s51\n\t" \
  "s_sub_i32 s77, s93, s53\n\t" \
  "s_cmp_eq_u32 s76, 0\n\t" \
  "s_cbranch_scc1 21f\n\t" \
  "v_cvt_f32_u32 v117, s76\n\t" \
  "v_cvt_f32_u32 v116, s77\n\t" \
  "v_rcp_f32 v117, v117\n\t" \
  "s_mov_b32 s51, s92\n\t" \
  "v_mul_f32 v117, v117, v116\n\t" \
  "v_max_f32 v117, 0x3f028f5c, v117\n\t" \
  "v_min_f32 v117, 0x3ffeb852, v117\n\t" \
  "v_rcp_f32 v116, v117\n\t" \
  "v_mul_f32 v117, 0x4f000000, v117\n\t" \
  "v_mul_f32 v116, 0x47800000, v116\n\t" \
  "v_cvt_u32_f32 v117, v117\n\t" \
  "v_cvt_u32_f32 v116, v116\n\t" \
  "s_mov_b32 s53, s93\n\t" \
  "v_readfirstlane_b32 s46, v117\n\t" \
  "v_readfirstlane_b32 s52, v116\n\t" \
  "21:\n\t" \
  "s_add_i32 s44, s44, 1\n\t" \
  "s_and_b32 s58, s58, 4\n\t" \
  "s_and_b32 s76, s44, 64\n\t" \
  "s_or_b32 s42, s42, s76\n\t" \
  "s_cbranch_scc0 20b\n\t" \
  "s_branch 99f\n\t" \
  "14:\n\t" \
  "s_sleep 3\n\t" \
  "s_add_u32 s59, s59, 0x10000\n\t" \
  "s_cmp_lt_u32 s59, 0x100000\n\t" \
  "s_cbranch_scc1 13b\n\t" \
  "s_mov_b64 exec, -1\n\t" \
  "s_mov_b32 s42, 16\n\t" \
  "s_branch 99f\n\t" \
  "8:\n\t" \
  "s_bitcmp1_b32 s58, 1\n\t" \
  "s_cbranch_scc1 4b\n\t" \
  "s_bitcmp1_b32 s58, 2\n\t" \
  "s_cbranch_scc1 5b\n\t" \
  "v_max3_u32 v116, v32, v33, v34\n\t" \
  "v_max3_u32 v117, v35, v36, v37\n\t" \
  "v_max3_u32 v116, v116, v38, v39\n\t" \
  "v_max_u32 v116, v116, v117\n\t" \
  "v_cmp_eq_u32 vcc, -1, v116\n\t" \
  "s_cbranch_vccz 5b\n\t" \
  "s_branch 0f\n\t" \
  "9:\n\t" \
  "s_bitcmp1_b32 s58, 2\n\t" \
  "s_cbranch_scc1 6b\n\t" \
  "v_max3_u32 v116, v40, v41, v42\n\t" \
  "v_max3_u32 v117, v43, v44, v45\n\t" \
  "v_max3_u32 v116, v116, v46, v47\n\t" \
  "v_max_u32 v116, v116, v117\n\t" \
  "v_cmp_eq_u32 vcc, -1, v116\n\t" \
  "s_cbranch_vccz 6b\n\t" \
  "s_or_b32 s58, s58, 2\n\t" \
  "0:\n\t" \
  "s_or_b32 s58, s58, 1\n\t" \
  "11:\n\t" \
  "buffer_load_dwordx4 v[32:35], v100, %[rh], s40 offen sc1\n\t" \
  "buffer_load_dwordx4 v[36:39], v100, %[rh], s76 offen sc1\n\t" \
  "buffer_load_dwordx4 v[40:43], v100, %[rh], s77 offen sc1\n\t" \
  "buffer_load_dwordx4 v[44:47], v100, %[rh], s78 offen sc1\n\t" \
  "s_waitcnt lgkmcnt(0)\n\t" \
  "s_branch 10b\n\t" \
  "17:\n\t" \
  "s_add_u32 s59, s59, 1\n\t" \
  "s_bitcmp1_b32 s58, 2\n\t" \
  "s_cbranch_scc1 3b\n\t" \
  "s_waitcnt vmcnt(0)\n\t" \
  "v_max_u32 v116, v32, v33\n\t" \
  "v_max3_u32 v116, v116, v34, v35\n\t" \
  "v_max3_u32 v116, v116, v36, v37\n\t" \
  "v_max3_u32 v116, v116, v38, v39\n\t" \
  "v_max3_u32 v116, v116, v40, v41\n\t" \
  "v_max3_u32 v116, v116, v42, v43\n\t" \
  "v_max3_u32 v116, v116, v44, v45\n\t" \
  "v_max3_u32 v116, v116, v46, v47\n\t" \
  "v_cmp_eq_u32 vcc, -1, v116\n\t" \
  "s_cbranch_vccz 3b\n\t" \
  "s_bitcmp1_b32 s59, 6\n\t" \
  "s_cbranch_scc1 12f\n\t" \
  "buffer_load_dwordx4 v[32:35], v100, %[rh], s40 offen sc1\n\t" \
  "buffer_load_dwordx4 v[36:39], v100, %[rh], s76 offen sc1\n\t" \
  "buffer_load_dwordx4 v[40:43], v100, %[rh], s77 offen sc1\n\t" \
  "buffer_load_dwordx4 v[44:47], v100, %[rh], s78 offen sc1\n\t" \
  "s_branch 17b\n\t" \
  "12:\n\t" \
  "s_mov_b32 s42, s58\n\t" \
  "s_branch 99f\n\t" \
  "93:\n\t" \
  "s_mov_b32 s58, s42\n\t" \
  "20:\n\t" \
  "s_and_b32 s76, s44, 7\n\t" \
  "s_lshl_b32 s76, s76, 15\n\t" \
  "s_add_i32 s40, s54, s76\n\t" \
  "s_and_b32 s76, s44, 3\n\t" \
  "s_lshl_b32 s45, s76, 14\n\t" \
  "s_lshl_b32 s47, s76, 20\n\t" \
  "s_add_i32 s47, s47, s55\n\t" \
  "s_lshr_b32 s76, s47, 21\n\t" \
  "s_bitcmp1_b64 %[sel], s76\n\t" \
  "s_cselect_b32 s76, 0x2000, 0\n\t" \
  "s_add_i32 s47, s47, s76\n\t" \
  "s_add_i32 s49, s44, -1\n\t" \
  "s_and_b32 s48, s49, 2\n\t" \
  "s_lshl_b32 s48, s48, 13\n\t" \
  "s_and_b32 s76, s49, 1\n\t" \
  "s_lshl_b32 s76, s76, 12\n\t" \
  "s_add_i32 s48, s48, s76\n\t" \
  "s_lshl_b32 s50, s53, 8\n\t" \
  "s_mov_b32 s59, 0\n\t" \
  "s_mov_b32 s92, s51\n\t" \
  "s_mov_b32 s93, s53\n\t" \
  "s_xor_b32 s76, s40, 0x400\n\t" \
  "s_xor_b32 s77, s40, 0x800\n\t" \
  "s_xor_b32 s78, s40, 0xc00\n\t" \
  "s_cmp_lg_u32 s58, 0\n\t" \
  "s_cbranch_scc1 11f\n\t" \
  "10:\n\t" \
  "ds_read_b128 v[48:51], v102 offset:49152\n\t" \
  "ds_read_b128 v[52:55], v102 offset:50176\n\t" \
  "ds_read_b128 v[56:59], v102 offset:51200\n\t" \
  "ds_read_b128 v[60:63], v102 offset:52224\n\t" \
  "ds_read_b128 v[0:3], v102 offset:0\n\t" \
  "ds_read_b128 v[4:7], v102 offset:1024\n\t" \
  "ds_read_b128 v[8:11], v102 offset:2048\n\t" \
  "ds_read_b128 v[12:15], v102 offset:3072\n\t" \
  "ds_read_b128 v[16:19], v102 offset:16384\n\t" \
  "ds_read_b128 v[20:23], v102 offset:17408\n\t" \
  "ds_read_b128 v[24:27], v102 offset:18432\n\t" \
  "ds_read_b128 v[28:31], v102 offset:19456\n\t" \
  "s_waitcnt lgkmcnt(4)\n\t" \
  "ds_read_b128 v[80:83], v102 offset:32768\n\t" \
  "ds_read_b128 v[84:87], v102 offset:33792\n\t" \
  "ds_read_b128 v[88:91], v102 offset:34816\n\t" \
  "ds_read_b128 v[92:95], v102 offset:35840\n\t" \
  "s_bitcmp1_b32 s58, 1\n\t" \
  "s_cselect_b64 s[80:81], -1, 0\n\t" \
  "s_cmp_lg_u32 s58, 0\n\t" \
  "s_cbranch_scc1 17f\n\t" \
  "s_mov_b32 exec_hi, 0\n\t" \
  "16:\n\t" \
  "s_memtime vcc\n\t" \
  "s_waitcnt lgkmcnt(0)\n\t" \
  "s_sub_i32 s79, s41, vcc_lo\n\t" \
  "s_cmp_gt_i32 s79, 64\n\t" \
  "s_cbranch_scc1 16b\n\t" \
  "13:\n\t" \
  "buffer_load_dword v116, v112, %[rh], s48 offen sc1\n\t" \
  "s_waitcnt vmcnt(0)\n\t" \
  "15:\n\t" \
  "v_mov_b32 v117, 20000\n\t" \
  "v_cmp_ne_u32_sdwa vcc, v116, s49 src0_sel:BYTE_0 src1_sel:DWORD\n\t" \
  "v_subrev_u32 v116, s50, v116\n\t" \
  "v_ashrrev_i32 v116, 8, v116\n\t" \
  "s_nop 1\n\t" \
  "v_max_i32_dpp v116, v116, v116 quad_perm:[1,0,3,2] row_mask:0xf bank_mask:0xf\n\t" \
  "s_cbranch_vccnz 14f\n\t" \
  "s_nop 0\n\t" \
  "v_max_i32_dpp v116, v116, v116 quad_perm:[2,3,0,1] row_mask:0xf bank_mask:0xf\n\t" \
  "s_nop 1\n\t" \
  "v_max_i32_dpp v116, v116, v116 row_half_mirror row_mask:0xf bank_mask:0xf\n\t" \
  "s_nop 1\n\t" \
  "v_max_i32_dpp v116, v116, v116 row_mirror row_mask:0xf bank_mask:0xf\n\t" \
  "v_med3_i32 v116, v116, 0, v117\n\t" \
  "v_mul_u32_u24 v116, s52, v116\n\t" \
  "v_lshrrev_b32 v116, 16, v116\n\t" \
  "s_mov_b64 exec, -1\n\t" \
  "v_readfirstlane_b32 s84, v116\n\t" \
  "v_readlane_b32 s85, v116, 16\n\t" \
  "s_max_u32 s84, s84, s85\n\t" \
  "s_add_i32 s84, s84, s51\n\t" \
  "1:\n\t" \
  "s_memtime vcc\n\t" \
  "s_waitcnt lgkmcnt(0)\n\t" \
  "s_sub_i32 s79, s84, vcc_lo\n\t" \
  "s_cmp_gt_i32 s79, 128\n\t" \
  "s_cbranch_scc1 1b\n\t" \
  ".p2align 4\n\t" \
  "2:\n\t" \
  "s_sub_i32 s79, s79, 33\n\t" \
  "s_cmp_gt_i32 s79, 56\n\t" \
  "s_cbranch_scc1 2b\n\t" \
  "buffer_load_dwordx4 v[32:35], v100, %[rh], s40 offen sc1\n\t" \
  "s_nop 12\n\t" \
  "buffer_load_dwordx4 v[36:39], v100, %[rh], s76 offen sc1\n\t" \
  "s_nop 12\n\t" \
  "buffer_load_dwordx4 v[40:43], v100, %[rh], s77 offen sc1\n\t" \
  "s_nop 12\n\t" \
  "buffer_load_dwordx4 v[44:47], v100, %[rh], s78 offen sc1\n\t" \
  "s_memtime vcc\n\t" \
  "s_memrealtime s[82:83]\n\t" \
  "s_nop 4\n\t" \
  "s_memrealtime s[84:85]\n\t" \
  "s_nop 4\n\t" \
  "s_memrealtime s[56:57]\n\t" \
  "s_nop 4\n\t" \
  "s_memrealtime s[86:87]\n\t" \
  "s_nop 4\n\t" \
  "s_memrealtime s[88:89]\n\t" \
  "s_nop 4\n\t" \
  "s_memrealtime s[90:91]\n\t" \
  "s_mov_b32 s92, 0\n\t" \
  "s_waitcnt lgkmcnt(0)\n\t" \
  "s_cmp_eq_u32 s82, s84\n\t" \
  "s_addc_u32 s92, s92, 0\n\t" \
  "s_cmp_eq_u32 s82, s56\n\t" \
  "s_addc_u32 s92, s92, 0\n\t" \
  "s_cmp_eq_u32 s82, s86\n\t" \
  "s_addc_u32 s92, s92, 0\n\t" \
  "s_cmp_eq_u32 s82, s88\n\t" \
  "s_addc_u32 s92, s92, 0\n\t" \
  "s_cmp_eq_u32 s82, s90\n\t" \
  "s_addc_u32 s92, s92, 0\n\t" \
  "s_mul_i32 s92, s92, 24\n\t" \
  "s_add_i32 s92, s92, vcc_lo\n\t" \
  "s_lshl4_add_u32 s93, s82, 64\n\t" \
  "3:\n\t" \
  "v_mov_b64 v[72:73], 0\n\t" \
  "v_mov_b64 v[74:75], 0\n\t" \
  "v_mov_b64 v[76:77], 0\n\t" \
  "v_mov_b64 v[78:79], 0\n\t" \
  "s_waitcnt lgkmcnt(0)\n\t" \
  "s_waitcnt vmcnt(3)\n\t" \
  "v_smfmac_f32_16x16x64_bf16 v[72:75], v[32:35], v[48:55], v104\n\t" \
  "ds_write_b128 v101, v[32:35] offset:12288\n\t" \
  "v_smfmac_f32_16x16x64_bf16 v[76:79], v[32:35], v[56:63], v104\n\t" \
  "s_waitcnt vmcnt(2)\n\t" \
  "ds_write_b128 v101, v[36:39] offset:13312\n\t" \
  "v_smfmac_f32_16x16x64_bf16 v[72:75], v[36:39], a[144:151], v104\n\t" \
  "v_smfmac_f32_16x16x64_bf16 v[76:79], v[36:39], a[152:159], v104\n\t" \
  "s_nop 3\n\t" \
  "v_cmp_u_f32 vcc, v72, v73\n\t" \
  "v_cmp_u_f32 s[82:83], v74, v75\n\t" \
  "s_waitcnt vmcnt(1)\n\t" \
  "v_smfmac_f32_16x16x64_bf16 v[72:75], v[40:43], a[160:167], v104\n\t" \
  "s_or_b64 vcc, vcc, s[82:83]\n\t" \
  "s_or_b64 vcc, vcc, s[80:81]\n\t" \
  "v_smfmac_f32_16x16x64_bf16 v[76:79], v[40:43], a[168:175], v104\n\t" \
  "s_waitcnt lgkmcnt(0)\n\t" \
  "s_cbranch_scc1 8f\n\t" \
  "5:\n\t" \
  "s_barrier\n\t" \
  "4:\n\t" \
  "s_memtime s[56:57]\n\t" \
  "ds_read_b128 v[48:51], v101 offset:0\n\t" \
  "ds_read_b128 v[52:55], v101 offset:1024\n\t" \
  "ds_read_b128 v[56:59], v101 offset:5120\n\t" \
  "s_waitcnt vmcnt(0)\n\t" \
  "v_smfmac_f32_16x16x64_bf16 v[72:75], v[44:47], a[176:183], v104\n\t" \
  "ds_read_b128 v[60:63], v101 offset:4096\n\t" \
  "ds_read_b128 v[64:67], v101 offset:9216\n\t" \
  "v_smfmac_f32_16x16x64_bf16 v[76:79], v[44:47], a[184:191], v104\n\t" \
  "ds_read_b128 v[68:71], v101 offset:8192\n\t" \
  "ds_write_b128 v101, v[40:43] offset:14336\n\t" \
  "s_nop 1\n\t" \
  "v_cmp_u_f32 vcc, v72, v73\n\t" \
  "v_cmp_u_f32 s[82:83], v74, v75\n\t" \
  "s_waitcnt lgkmcnt(6)\n\t" \
  "v_smfmac_f32_16x16x64_bf16 v[72:75], v[48:51], v[0:7], v104\n\t" \
  "s_or_b64 vcc, vcc, s[82:83]\n\t" \
  "v_smfmac_f32_16x16x64_bf16 v[76:79], v[48:51], v[8:15], v104\n\t" \
  "s_cbranch_scc1 9f\n\t" \
  "6:\n\t" \
  "s_waitcnt lgkmcnt(5)\n\t" \
  "v_smfmac_f32_16x16x64_bf16 v[72:75], v[52:55], a[0:7], v104\n\t" \
  "ds_write_b128 v101, v[44:47] offset:15360\n\t" \
  "v_smfmac_f32_16x16x64_bf16 v[76:79], v[52:55], a[8:15], v104\n\t" \
  "v_mov_b64 v[40:41], 0\n\t" \
  "s_waitcnt lgkmcnt(5)\n\t" \
  "v_smfmac_f32_16x16x64_bf16 v[72:75], v[56:59], a[48:55], v104\n\t" \
  "v_mov_b64 v[42:43], 0\n\t" \
  "v_smfmac_f32_16x16x64_bf16 v[76:79], v[56:59], a[56:63], v104\n\t" \
  "s_waitcnt lgkmcnt(4)\n\t" \
  "v_smfmac_f32_16x16x64_bf16 v[72:75], v[60:63], v[16:23], v104\n\t" \
  "s_waitcnt lgkmcnt(0)\n\t" \
  "s_barrier\n\t" \
  "v_smfmac_f32_16x16x64_bf16 v[76:79], v[60:63], v[24:31], v104\n\t" \
  "ds_read_b128 v[48:51], v101 offset:2048\n\t" \
  "ds_read_b128 v[52:55], v101 offset:6144\n\t" \
  "v_smfmac_f32_16x16x64_bf16 v[72:75], v[64:67], a[96:103], v104\n\t" \
  "ds_read_b128 v[56:59], v101 offset:10240\n\t" \
  "ds_read_b128 v[60:63], v101 offset:3072\n\t" \
  "v_smfmac_f32_16x16x64_bf16 v[76:79], v[64:67], a[104:111], v104\n\t" \
  "ds_read_b128 v[32:35], v101 offset:7168\n\t" \
  "ds_read_b128 v[36:39], v101 offset:11264\n\t" \
  "v_smfmac_f32_16x16x64_bf16 v[72:75], v[68:71], v[80:87], v104\n\t" \
  "ds_read_b128 v[0:3], v103 offset:0\n\t" \
  "v_mov_b64 v[44:45], 0\n\t" \
  "v_smfmac_f32_16x16x64_bf16 v[76:79], v[68:71], v[88:95], v104\n\t" \
  "v_mov_b64 v[46:47], 0\n\t" \
  "s_waitcnt lgkmcnt(5)\n\t" \
  "v_smfmac_f32_16x16x64_bf16 v[72:75], v[48:51], a[16:23], v104\n\t" \
  "ds_read_b128 v[4:7], v103 offset:1024\n\t" \
  "v_mov_b64 v[80:81], 0\n\t" \
  "v_smfmac_f32_16x16x64_bf16 v[72:75], v[52:55], a[64:71], v104\n\t" \
  "v_mov_b64 v[82:83], 0\n\t" \
  "s_waitcnt lgkmcnt(2)\n\t" \
  "v_smfmac_f32_16x16x64_bf16 v[72:75], v[56:59], a[112:119], v104\n\t" \
  "ds_read_b128 v[8:11], v103 offset:2048\n\t" \
  "v_mov_b64 v[84:85], 0\n\t" \
  "v_smfmac_f32_16x16x64_bf16 v[72:75], v[60:63], a[32:39], v104\n\t" \
  "ds_read_b128 v[12:15], v103 offset:3072\n\t" \
  "v_mov_b64 v[86:87], 0\n\t" \
  "v_smfmac_f32_16x16x64_bf16 v[72:75], v[32:35], a[80:87], v104\n\t" \
  "ds_read_b128 v[16:19], v103 offset:16384\n\t" \
  "v_mov_b64 v[88:89], 0\n\t" \
  "v_smfmac_f32_16x16x64_bf16 v[72:75], v[36:39], a[128:135], v104\n\t" \
  "ds_read_b128 v[20:23], v103 offset:17408\n\t" \
  "v_mov_b64 v[90:91], 0\n\t" \
  "v_smfmac_f32_16x16x64_bf16 v[76:79], v[48:51], a[24:31], v104\n\t" \
  "ds_read_b128 v[24:27], v103 offset:18432\n\t" \
  "v_mov_b64 v[92:93], 0\n\t" \
  "v_smfmac_f32_16x16x64_bf16 v[76:79], v[52:55], a[72:79], v104\n\t" \
  "v_add_f32 v96, v72, v74\n\t" \
  "v_add_f32 v97, v73, v75\n\t" \
  "v_cvt_pk_bf16_f32 v96, v96, v97\n\t" \
  "ds_write_b32 v105, v96\n\t" \
  "v_smfmac_f32_16x16x64_bf16 v[76:79], v[56:59], a[120:127], v104\n\t" \
  "ds_read_b128 v[28:31], v103 offset:19456\n\t" \
  "v_mov_b64 v[94:95], 0\n\t" \
  "v_smfmac_f32_16x16x64_bf16 v[76:79], v[60:63], a[40:47], v104\n\t" \
  "v_smfmac_f32_16x16x64_bf16 v[76:79], v[32:35], a[88:95], v104\n\t" \
  "v_smfmac_f32_16x16x64_bf16 v[76:79], v[36:39], a[136:143], v104\n\t" \
  "s_waitcnt lgkmcnt(1)\n\t" \
  "s_barrier\n\t" \
  "ds_read_b64_tr_b16 v[96:97], v107 offset:0\n\t" \
  "ds_read_b64_tr_b16 v[98:99], v107 offset:64\n\t" \
  "ds_read_b64_tr_b16 v[56:57], v107 offset:1024\n\t" \
  "ds_read_b64_tr_b16 v[58:59], v107 offset:1088\n\t" \
  "s_nop 1\n\t" \
  "v_add_f32 v116, v76, v78\n\t" \
  "v_add_f32 v117, v77, v79\n\t" \
  "v_cvt_pk_bf16_f32 v116, v116, v117\n\t" \
  "ds_write_b32 v105, v116 offset:256\n\t" \
  "s_waitcnt lgkmcnt(3)\n\t" \
  "v_smfmac_f32_16x16x64_bf16 v[40:43], v[96:99], a[192:199], v106\n\t" \
  "v_smfmac_f32_16x16x64_bf16 v[44:47], v[96:99], a[224:231], v106\n\t" \
  "s_waitcnt lgkmcnt(0)\n\t" \
  "s_barrier\n\t" \
  "ds_read_b64_tr_b16 v[52:53], v107 offset:256\n\t" \
  "ds_read_b64_tr_b16 v[54:55], v107 offset:320\n\t" \
  "v_smfmac_f32_16x16x64_bf16 v[40:43], v[56:59], a[208:215], v106\n\t" \
  "ds_read_b64_tr_b16 v[60:61], v107 offset:1280\n\t" \
  "ds_read_b64_tr_b16 v[62:63], v107 offset:1344\n\t" \
  "v_smfmac_f32_16x16x64_bf16 v[44:47], v[56:59], v[0:7], v106\n\t" \
  "v_smfmac_f32_16x16x64_bf16 v[80:83], v[96:99], a[240:247], v106\n\t" \
  "v_mov_b64 v[32:33], 0\n\t" \
  "v_smfmac_f32_16x16x64_bf16 v[84:87], v[96:99], v[136:143], v106\n\t" \
  "v_mov_b64 v[34:35], 0\n\t" \
  "v_smfmac_f32_16x16x64_bf16 v[80:83], v[56:59], v[120:127], v106\n\t" \
  "v_mov_b64 v[36:37], 0\n\t" \
  "v_smfmac_f32_16x16x64_bf16 v[84:87], v[56:59], v[16:23], v106\n\t" \
  "s_waitcnt lgkmcnt(2)\n\t" \
  "v_smfmac_f32_16x16x64_bf16 v[40:43], v[52:55], a[200:207], v106\n\t" \
  "v_smfmac_f32_16x16x64_bf16 v[44:47], v[52:55], a[232:239], v106\n\t" \
  "s_xor_b32 s76, s47, 0x40000\n\t" \
  "s_waitcnt lgkmcnt(0)\n\t" \
  "v_smfmac_f32_16x16x64_bf16 v[40:43], v[60:63], a[216:223], v106\n\t" \
  "v_mov_b64 v[38:39], 0\n\t" \
  "s_xor_b32 s77, s47, 0x80000\n\t" \
  "v_smfmac_f32_16x16x64_bf16 v[44:47], v[60:63], v[8:15], v106\n\t" \
  "s_xor_b32 s78, s47, 0xc0000\n\t" \
  "v_smfmac_f32_16x16x64_bf16 v[80:83], v[52:55], a[248:255], v106\n\t" \
  "ds_read_b128 v[0:3], v103 offset:32768\n\t" \
  "v_cvt_pk_bf16_f32 v64, v40, v41\n\t" \
  "v_smfmac_f32_16x16x64_bf16 v[84:87], v[52:55], v[144:151], v106\n\t" \
  "ds_read_b128 v[4:7], v103 offset:33792\n\t" \
  "v_cvt_pk_bf16_f32 v65, v42, v43\n\t" \
  "v_smfmac_f32_16x16x64_bf16 v[80:83], v[60:63], v[128:135], v106\n\t" \
  "ds_read_b128 v[8:11], v103 offset:34816\n\t" \
  "v_cvt_pk_bf16_f32 v66, v44, v45\n\t" \
  "v_smfmac_f32_16x16x64_bf16 v[84:87], v[60:63], v[24:31], v106\n\t" \
  "ds_read_b128 v[12:15], v103 offset:35840\n\t" \
  "v_cvt_pk_bf16_f32 v67, v46, v47\n\t" \
  "v_smfmac_f32_16x16x64_bf16 v[88:91], v[96:99], v[152:159], v106\n\t" \
  "v_permlane16_swap_b32 v64, v66\n\t" \
  "v_cvt_pk_bf16_f32 v68, v80, v81\n\t" \
  "v_smfmac_f32_16x16x64_bf16 v[92:95], v[96:99], v[184:191], v106\n\t" \
  "v_permlane16_swap_b32 v65, v67\n\t" \
  "v_cvt_pk_bf16_f32 v69, v82, v83\n\t" \
  "v_smfmac_f32_16x16x64_bf16 v[88:91], v[52:55], v[160:167], v106\n\t" \
  "v_cvt_pk_bf16_f32 v70, v84, v85\n\t" \
  "v_smfmac_f32_16x16x64_bf16 v[92:95], v[52:55], v[192:199], v106\n\t" \
  "v_cvt_pk_bf16_f32 v71, v86, v87\n\t" \
  "v_smfmac_f32_16x16x64_bf16 v[88:91], v[56:59], v[168:175], v106\n\t" \
  "ds_read_b128 v[16:19], v103 offset:49152\n\t" \
  "buffer_store_dwordx4 v[64:67], v108, %[ru], s47 offen sc1\n\t" \
  "v_permlane16_swap_b32 v68, v70\n\t" \
  "s_waitcnt lgkmcnt(3)\n\t" \
  "v_smfmac_f32_16x16x64_bf16 v[92:95], v[56:59], v[0:7], v106\n\t" \
  "ds_read_b128 v[20:23], v103 offset:50176\n\t" \
  "v_permlane16_swap_b32 v69, v71\n\t" \
  "v_smfmac_f32_16x16x64_bf16 v[88:91], v[60:63], v[176:183], v106\n\t" \
  "ds_read_b128 v[24:27], v103 offset:51200\n\t" \
  "s_waitcnt lgkmcnt(3)\n\t" \
  "v_smfmac_f32_16x16x64_bf16 v[92:95], v[60:63], v[8:15], v106\n\t" \
  "ds_read_b128 v[28:31], v103 offset:52224\n\t" \
  "v_smfmac_f32_16x16x64_bf16 v[32:35], v[96:99], v[200:207], v106\n\t" \
  "buffer_store_dwordx4 v[68:71], v108, %[ru], s76 offen sc1\n\t" \
  "v_cvt_pk_bf16_f32 v72, v88, v89\n\t" \
  "v_smfmac_f32_16x16x64_bf16 v[36:39], v[96:99], v[232:239], v106\n\t" \
  "v_cvt_pk_bf16_f32 v73, v90, v91\n\t" \
  "v_smfmac_f32_16x16x64_bf16 v[32:35], v[52:55], v[208:215], v106\n\t" \
  "v_cvt_pk_bf16_f32 v74, v92, v93\n\t" \
  "v_smfmac_f32_16x16x64_bf16 v[36:39], v[52:55], v[240:247], v106\n\t" \
  "v_cvt_pk_bf16_f32 v75, v94, v95\n\t" \
  "v_smfmac_f32_16x16x64_bf16 v[32:35], v[56:59], v[216:223], v106\n\t" \
  "v_permlane16_swap_b32 v72, v74\n\t" \
  "v_smfmac_f32_16x16x64_bf16 v[32:35], v[60:63], v[224:231], v106\n\t" \
  "v_permlane16_swap_b32 v73, v75\n\t" \
  "s_waitcnt lgkmcnt(2)\n\t" \
  "v_smfmac_f32_16x16x64_bf16 v[36:39], v[56:59], v[16:23], v106\n\t" \
  "s_waitcnt lgkmcnt(0)\n\t" \
  "v_smfmac_f32_16x16x64_bf16 v[36:39], v[60:63], v[24:31], v106\n\t" \
  "buffer_store_dwordx4 v[72:75], v108, %[ru], s77 offen sc1\n\t" \
  "v_cvt_pk_bf16_f32 v76, v32, v33\n\t" \
  "v_cvt_pk_bf16_f32 v77, v34, v35\n\t" \
  "s_nop 5\n\t" \
  "v_cvt_pk_bf16_f32 v78, v36, v37\n\t" \
  "v_cvt_pk_bf16_f32 v79, v38, v39\n\t" \
  "s_nop 0\n\t" \
  "v_permlane16_swap_b32 v76, v78\n\t" \
  "v_permlane16_swap_b32 v77, v79\n\t" \
  "buffer_store_dwordx4 v[76:79], v108, %[ru], s78 offen sc1\n\t" \
  "s_memtime s[76:77]\n\t" \
  "s_mov_b32 s42, 0\n\t" \
  "s_waitcnt lgkmcnt(0)\n\t" \
  "s_mov_b32 s57, s76\n\t" \
  "s_add_i32 s41, s56, 1950\n\t" \
  "s_sub_i32 s76, s92, s51\n\t" \
  "s_sub_i32 s77, s93, s53\n\t" \
  "s_cmp_eq_u32 s76, 0\n\t" \
  "s_cbranch_scc1 21f\n\t" \
  "v_cvt_f32_u32 v117, s76\n\t" \
  "v_cvt_f32_u32 v116, s77\n\t" \
  "v_rcp_f32 v117, v117\n\t" \
  "s_mov_b32 s51, s92\n\t" \
  "v_mul_f32 v117, v117, v116\n\t" \
  "v_max_f32 v117, 0x3f028f5c, v117\n\t" \
  "v_min_f32 v117, 0x3ffeb852, v117\n\t" \
  "v_rcp_f32 v116, v117\n\t" \
  "v_mul_f32 v117, 0x4f000000, v117\n\t" \
  "v_mul_f32 v116, 0x47800000, v116\n\t" \
  "v_cvt_u32_f32 v117, v117\n\t" \
  "v_cvt_u32_f32 v116, v116\n\t" \
  "s_mov_b32 s53, s93\n\t" \
  "v_readfirstlane_b32 s46, v117\n\t" \
  "v_readfirstlane_b32 s52, v116\n\t" \
  "21:\n\t" \
  "s_add_i32 s44, s44, 1\n\t" \
  "s_and_b32 s58, s58, 4\n\t" \
  "s_and_b32 s76, s44, 64\n\t" \
  "s_or_b32 s42, s42, s76\n\t" \
  "s_cbranch_scc0 20b\n\t" \
  "s_branch 99f\n\t" \
  "14:\n\t" \
  "s_sleep 3\n\t" \
  "s_add_u32 s59, s59, 0x10000\n\t" \
  "s_cmp_lt_u32 s59, 0x100000\n\t" \
  "s_cbranch_scc1 13b\n\t" \
  "s_mov_b64 exec, -1\n\t" \
  "s_mov_b32 s42, 16\n\t" \
  "s_branch 99f\n\t" \
  "8:\n\t" \
  "s_bitcmp1_b32 s58, 1\n\t" \
  "s_cbranch_scc1 4b\n\t" \
  "s_bitcmp1_b32 s58, 2\n\t" \
  "s_cbranch_scc1 5b\n\t" \
  "v_max3_u32 v116, v32, v33, v34\n\t" \
  "v_max3_u32 v117, v35, v36, v37\n\t" \
  "v_max3_u32 v116, v116, v38, v39\n\t" \
  "v_max_u32 v116, v116, v117\n\t" \
  "v_cmp_eq_u32 vcc, -1, v116\n\t" \
  "s_cbranch_vccz 5b\n\t" \
  "s_branch 0f\n\t" \
  "9:\n\t" \
  "s_bitcmp1_b32 s58, 2\n\t" \
  "s_cbranch_scc1 6b\n\t" \
  "v_max3_u32 v116, v40, v41, v42\n\t" \
  "v_max3_u32 v117, v43, v44, v45\n\t" \
  "v_max3_u32 v116, v116, v46, v47\n\t" \
  "v_max_u32 v116, v116, v117\n\t" \
  "v_cmp_eq_u32 vcc, -1, v116\n\t" \
  "s_cbranch_vccz 6b\n\t" \
  "s_or_b32 s58, s58, 2\n\t" \
  "0:\n\t" \
  "s_or_b32 s58, s58, 1\n\t" \
  "11:\n\t" \
  "buffer_load_dwordx4 v[32:35], v100, %[rh], s40 offen sc1\n\t" \
  "buffer_load_dwordx4 v[36:39], v100, %[rh], s76 offen sc1\n\t" \
  "buffer_load_dwordx4 v[40:43], v100, %[rh], s77 offen sc1\n\t" \
  "buffer_load_dwordx4 v[44:47], v100, %[rh], s78 offen sc1\n\t" \
  "s_waitcnt lgkmcnt(0)\n\t" \
  "s_branch 10b\n\t" \
  "17:\n\t" \
  "s_add_u32 s59, s59, 1\n\t" \
  "s_bitcmp1_b32 s58, 2\n\t" \
  "s_cbranch_scc1 3b\n\t" \
  "s_waitcnt vmcnt(0)\n\t" \
  "v_max_u32 v116, v32, v33\n\t" \
  "v_max3_u32 v116, v116, v34, v35\n\t" \
  "v_max3_u32 v116, v116, v36, v37\n\t" \
  "v_max3_u32 v116, v116, v38, v39\n\t" \
  "v_max3_u32 v116, v116, v40, v41\n\t" \
  "v_max3_u32 v116, v116, v42, v43\n\t" \
  "v_max3_u32 v116, v116, v44, v45\n\t" \
  "v_max3_u32 v116, v116, v46, v47\n\t" \
  "v_cmp_eq_u32 vcc, -1, v116\n\t" \
  "s_cbranch_vccz 3b\n\t" \
  "s_bitcmp1_b32 s59, 6\n\t" \
  "s_cbranch_scc1 12f\n\t" \
  "buffer_load_dwordx4 v[32:35], v100, %[rh], s40 offen sc1\n\t" \
  "buffer_load_dwordx4 v[36:39], v100, %[rh], s76 offen sc1\n\t" \
  "buffer_load_dwordx4 v[40:43], v100, %[rh], s77 offen sc1\n\t" \
  "buffer_load_dwordx4 v[44:47], v100, %[rh], s78 offen sc1\n\t" \
  "s_branch 17b\n\t" \
  "12:\n\t" \
  "s_mov_b32 s42, s58\n\t" \
  "s_branch 99f\n\t" \
  "99:\n\t" \
  "s_waitcnt vmcnt(0) lgkmcnt(0)\n\t" \
  ".Lchain_last:\n\t" \
  ""
#define CB_BODY \
  "s_cmp_gt_u32 %[wv], 1\n\t" \
  "s_cbranch_scc1 91f\n\t" \
  "s_mov_b32 s58, s42\n\t" \
  "s_mov_b32 s59, 0\n\t" \
  "s_bitcmp1_b32 s58, 4\n\t" \
  "s_cbranch_scc1 26f\n\t" \
  "s_bitcmp1_b32 s58, 3\n\t" \
  "s_cbranch_scc1 34f\n\t" \
  "s_bitcmp1_b32 s58, 1\n\t" \
  "s_cbranch_scc1 18f\n\t" \
  "17:\n\t" \
  "19:\n\t" \
  "s_add_i32 s85, s40, -1\n\t" \
  "s_and_b32 s82, s85, 2\n\t" \
  "s_lshl_b32 s82, s82, 13\n\t" \
  "s_and_b32 s80, s85, 1\n\t" \
  "s_lshl_b32 s80, s80, 12\n\t" \
  "s_add_i32 s82, s82, s80\n\t" \
  "s_add_i32 s82, s82, 0x84000\n\t" \
  "s_mov_b32 exec_hi, 0\n\t" \
  "buffer_load_dword v184, v200, %[rh], s82 offen sc1\n\t" \
  "buffer_load_dword v186, v203, %[ru], 0 offen sc1\n\t" \
  "s_mov_b64 exec, -1\n\t" \
  "20:\n\t" \
  "s_and_b32 s80, s40, 3\n\t" \
  "s_mul_i32 s77, s80, 0xc000\n\t" \
  "s_add_i32 s77, s77, 0x40000\n\t" \
  "s_lshl_b32 s78, s80, 14\n\t" \
  "s_add_i32 s78, s78, 0x70000\n\t" \
  "s_mov_b32 s57, 0\n\t" \
  "s_add_i32 s96, s77, 0x1000\n\t" \
  "s_lshl_b32 s88, s80, 20\n\t" \
  "s_add_i32 s88, s88, s48\n\t" \
  "s_lshr_b32 s80, s88, 21\n\t" \
  "s_bitcmp1_b64 %[sel], s80\n\t" \
  "s_cselect_b32 s80, 0x2000, 0\n\t" \
  "s_add_i32 s88, s88, s80\n\t" \
  "s_xor_b32 s89, s88, 0x800\n\t" \
  "s_xor_b32 s90, s88, 0x1000\n\t" \
  "s_xor_b32 s91, s88, 0x1800\n\t" \
  "s_add_i32 s92, s77, s50\n\t" \
  "s_xor_b32 s93, s92, 0x100\n\t" \
  "s_xor_b32 s94, s92, 0x200\n\t" \
  "s_xor_b32 s95, s92, 0x300\n\t" \
  "s_add_i32 s97, s40, 1\n\t" \
  "s_and_b32 s97, s97, 7\n\t" \
  "s_lshl_b32 s97, s97, 15\n\t" \
  "s_add_i32 s97, s97, s49\n\t" \
  "s_memtime vcc\n\t" \
  "s_memrealtime s[86:87]\n\t" \
  "s_nop 4\n\t" \
  "s_memrealtime s[60:61]\n\t" \
  "s_nop 4\n\t" \
  "s_memrealtime s[62:63]\n\t" \
  "s_nop 4\n\t" \
  "s_memrealtime s[64:65]\n\t" \
  "s_nop 4\n\t" \
  "s_memrealtime s[66:67]\n\t" \
  "s_nop 4\n\t" \
  "s_memrealtime s[68:69]\n\t" \
  "s_mov_b32 s79, 0\n\t" \
  "s_waitcnt lgkmcnt(0)\n\t" \
  "s_cmp_eq_u32 s86, s60\n\t" \
  "s_addc_u32 s79, s79, 0\n\t" \
  "s_cmp_eq_u32 s86, s62\n\t" \
  "s_addc_u32 s79, s79, 0\n\t" \
  "s_cmp_eq_u32 s86, s64\n\t" \
  "s_addc_u32 s79, s79, 0\n\t" \
  "s_cmp_eq_u32 s86, s66\n\t" \
  "s_addc_u32 s79, s79, 0\n\t" \
  "s_cmp_eq_u32 s86, s68\n\t" \
  "s_addc_u32 s79, s79, 0\n\t" \
  "s_mul_i32 s79, s79, 24\n\t" \
  "s_add_i32 s79, s79, vcc_lo\n\t" \
  "s_lshl4_add_u32 s80, s86, 64\n\t" \
  "s_sub_i32 s86, s79, s41\n\t" \
  "s_sub_i32 s87, s80, s43\n\t" \
  "s_cmp_lt_u32 s86, 4000\n\t" \
  "s_cbranch_scc1 21f\n\t" \
  "v_cvt_f32_u32 v165, s86\n\t" \
  "v_cvt_f32_u32 v164, s87\n\t" \
  "v_rcp_f32 v165, v165\n\t" \
  "s_nop 0\n\t" \
  "v_mul_f32 v165, v165, v164\n\t" \
  "v_max_f32 v165, 0x3f028f5c, v165\n\t" \
  "v_min_f32 v165, 0x3ffeb852, v165\n\t" \
  "v_rcp_f32 v164, v165\n\t" \
  "v_mul_f32 v165, 0x4f000000, v165\n\t" \
  "v_mul_f32 v164, 0x47800000, v164\n\t" \
  "v_cvt_u32_f32 v165, v165\n\t" \
  "v_cvt_u32_f32 v164, v164\n\t" \
  "s_nop 0\n\t" \
  "v_readfirstlane_b32 s45, v165\n\t" \
  "v_readfirstlane_b32 s44, v164\n\t" \
  "21:\n\t" \
  "s_mov_b32 s41, s79\n\t" \
  "s_mov_b32 s43, s80\n\t" \
  "s_lshl_b32 s83, s43, 8\n\t" \
  "s_mov_b32 s84, 650\n\t" \
  "s_mov_b32 exec_hi, 0\n\t" \
  "s_waitcnt vmcnt(0)\n\t" \
  "v_cmp_ne_u32_sdwa vcc, v184, s85 src0_sel:BYTE_0 src1_sel:DWORD\n\t" \
  "v_subrev_u32 v184, s83, v184\n\t" \
  "v_ashrrev_i32 v184, 8, v184\n\t" \
  "s_cbranch_vccnz 22f\n\t" \
  "s_nop 0\n\t" \
  "v_max_i32_dpp v184, v184, v184 quad_perm:[1,0,3,2] row_mask:0xf bank_mask:0xf\n\t" \
  "s_nop 1\n\t" \
  "v_max_i32_dpp v184, v184, v184 quad_perm:[2,3,0,1] row_mask:0xf bank_mask:0xf\n\t" \
  "s_nop 1\n\t" \
  "v_max_i32_dpp v184, v184, v184 row_half_mirror row_mask:0xf bank_mask:0xf\n\t" \
  "s_nop 1\n\t" \
  "v_max_i32_dpp v184, v184, v184 row_mirror row_mask:0xf bank_mask:0xf\n\t" \
  "s_nop 0\n\t" \
  "v_readlane_b32 s81, v184, 0\n\t" \
  "v_readlane_b32 s82, v184, 16\n\t" \
  "s_max_i32 s81, s81, s82\n\t" \
  "s_add_i32 s81, s81, 1056\n\t" \
  "s_max_i32 s81, s81, 0\n\t" \
  "s_min_i32 s81, s81, 20000\n\t" \
  "s_mul_i32 s81, s81, s44\n\t" \
  "s_lshr_b32 s81, s81, 16\n\t" \
  "24:\n\t" \
  "s_add_i32 s81, s81, s41\n\t" \
  "1:\n\t" \
  "s_memtime vcc\n\t" \
  "s_waitcnt lgkmcnt(0)\n\t" \
  "s_sub_i32 s79, s81, vcc_lo\n\t" \
  "s_cmp_gt_i32 s79, 128\n\t" \
  "s_cbranch_scc1 1b\n\t" \
  ".p2align 4\n\t" \
  "2:\n\t" \
  "s_sub_i32 s79, s79, 33\n\t" \
  "s_cmp_gt_i32 s79, 56\n\t" \
  "s_cbranch_scc1 2b\n\t" \
  "buffer_load_dword v187, v200, %[rh], s78 offen sc1\n\t" \
  "s_memtime vcc\n\t" \
  "s_memrealtime s[86:87]\n\t" \
  "s_nop 4\n\t" \
  "s_memrealtime s[60:61]\n\t" \
  "s_nop 4\n\t" \
  "s_memrealtime s[62:63]\n\t" \
  "s_nop 4\n\t" \
  "s_memrealtime s[64:65]\n\t" \
  "s_nop 4\n\t" \
  "s_memrealtime s[66:67]\n\t" \
  "s_nop 4\n\t" \
  "s_memrealtime s[68:69]\n\t" \
  "s_mov_b32 s79, 0\n\t" \
  "s_waitcnt lgkmcnt(0)\n\t" \
  "s_cmp_eq_u32 s86, s60\n\t" \
  "s_addc_u32 s79, s79, 0\n\t" \
  "s_cmp_eq_u32 s86, s62\n\t" \
  "s_addc_u32 s79, s79, 0\n\t" \
  "s_cmp_eq_u32 s86, s64\n\t" \
  "s_addc_u32 s79, s79, 0\n\t" \
  "s_cmp_eq_u32 s86, s66\n\t" \
  "s_addc_u32 s79, s79, 0\n\t" \
  "s_cmp_eq_u32 s86, s68\n\t" \
  "s_addc_u32 s79, s79, 0\n\t" \
  "s_mul_i32 s79, s79, 24\n\t" \
  "s_add_i32 s79, s79, vcc_lo\n\t" \
  "s_lshl4_add_u32 s80, s86, 64\n\t" \
  "s_mov_b32 s41, s79\n\t" \
  "s_mov_b32 s43, s80\n\t" \
  "s_lshl_b32 s83, s43, 8\n\t" \
  "13:\n\t" \
  "s_waitcnt vmcnt(0)\n\t" \
  "s_memtime s[86:87]\n\t" \
  "v_cmp_ne_u32_sdwa vcc, v187, s40 src0_sel:BYTE_0 src1_sel:DWORD\n\t" \
  "v_subrev_u32 v184, s83, v187\n\t" \
  "v_ashrrev_i32 v184, 8, v184\n\t" \
  "s_nop 1\n\t" \
  "v_max_i32_dpp v184, v184, v184 quad_perm:[1,0,3,2] row_mask:0xf bank_mask:0xf\n\t" \
  "s_nop 1\n\t" \
  "v_max_i32_dpp v184, v184, v184 quad_perm:[2,3,0,1] row_mask:0xf bank_mask:0xf\n\t" \
  "s_cbranch_vccnz 14f\n\t" \
  "15:\n\t" \
  "s_nop 0\n\t" \
  "v_max_i32_dpp v184, v184, v184 row_half_mirror row_mask:0xf bank_mask:0xf\n\t" \
  "s_nop 1\n\t" \
  "v_max_i32_dpp v184, v184, v184 row_mirror row_mask:0xf bank_mask:0xf\n\t" \
  "s_mov_b64 exec, -1\n\t" \
  "v_readlane_b32 s57, v184, 0\n\t" \
  "v_readlane_b32 s82, v184, 16\n\t" \
  "s_waitcnt lgkmcnt(0)\n\t" \
  "s_sub_i32 s79, s41, s86\n\t" \
  "s_max_i32 s57, s57, s82\n\t" \
  "s_add_i32 s81, s57, 200\n\t" \
  "s_max_i32 s81, s81, 0\n\t" \
  "s_min_i32 s81, s81, 20000\n\t" \
  "s_mul_i32 s81, s81, s44\n\t" \
  "s_lshr_b32 s81, s81, 16\n\t" \
  "s_add_i32 s79, s79, s81\n\t" \
  "s_add_i32 s79, s79, -370\n\t" \
  ".p2align 4\n\t" \
  "3:\n\t" \
  "s_sub_i32 s79, s79, 33\n\t" \
  "s_cmp_gt_i32 s79, 56\n\t" \
  "s_cbranch_scc1 3b\n\t" \
  "buffer_load_dwordx4 v[72:75], v192, %[ru], s88 offen offset:0 sc1\n\t" \
  "s_nop 4\n\t" \
  "buffer_load_dwordx4 v[76:79], v192, %[ru], s88 offen offset:1024 sc1\n\t" \
  "s_nop 4\n\t" \
  "s_add_i32 s79, s79, 22\n\t" \
  ".p2align 4\n\t" \
  "4:\n\t" \
  "s_sub_i32 s79, s79, 33\n\t" \
  "s_cmp_gt_i32 s79, 56\n\t" \
  "s_cbranch_scc1 4b\n\t" \
  "buffer_load_dwordx4 v[80:83], v192, %[ru], s89 offen offset:0 sc1\n\t" \
  "s_nop 4\n\t" \
  "buffer_load_dwordx4 v[84:87], v192, %[ru], s89 offen offset:1024 sc1\n\t" \
  "s_nop 4\n\t" \
  "s_add_i32 s79, s79, 42\n\t" \
  ".p2align 4\n\t" \
  "5:\n\t" \
  "s_sub_i32 s79, s79, 33\n\t" \
  "s_cmp_gt_i32 s79, 56\n\t" \
  "s_cbranch_scc1 5b\n\t" \
  "buffer_load_dwordx4 v[88:91], v192, %[ru], s90 offen offset:0 sc1\n\t" \
  "buffer_load_dwordx4 v[92:95], v192, %[ru], s90 offen offset:1024 sc1\n\t" \
  "s_add_i32 s79, s79, 122\n\t" \
  ".p2align 4\n\t" \
  "9:\n\t" \
  "s_sub_i32 s79, s79, 33\n\t" \
  "s_cmp_gt_i32 s79, 56\n\t" \
  "s_cbranch_scc1 9b\n\t" \
  "buffer_load_dwordx4 v[104:107], v194, %[rh], s92 offen sc1\n\t" \
  "s_nop 4\n\t" \
  "buffer_load_dwordx4 v[108:111], v194, %[rh], s93 offen sc1\n\t" \
  "s_nop 4\n\t" \
  "buffer_load_dwordx4 v[112:115], v194, %[rh], s94 offen sc1\n\t" \
  "s_nop 4\n\t" \
  "buffer_load_dwordx4 v[116:119], v194, %[rh], s95 offen sc1\n\t" \
  "s_nop 4\n\t" \
  "buffer_load_dwordx4 v[96:99], v192, %[ru], s91 offen offset:0 sc1\n\t" \
  "s_nop 4\n\t" \
  "buffer_load_dwordx4 v[100:103], v192, %[ru], s91 offen offset:1024 sc1\n\t" \
  "s_nop 4\n\t" \
  "10:\n\t" \
  "s_waitcnt vmcnt(5)\n\t" \
  "v_mfma_f32_16x16x32_bf16 v[136:139], v[104:107], v[72:75], 0\n\t" \
  "v_mfma_f32_16x16x32_bf16 v[140:143], v[104:107], v[76:79], 0\n\t" \
  "s_waitcnt vmcnt(4)\n\t" \
  "v_mfma_f32_16x16x32_bf16 v[136:139], v[108:111], v[80:83], v[136:139]\n\t" \
  "v_mfma_f32_16x16x32_bf16 v[140:143], v[108:111], v[84:87], v[140:143]\n\t" \
  "s_waitcnt vmcnt(3)\n\t" \
  "v_mfma_f32_16x16x32_bf16 v[136:139], v[112:115], v[88:91], v[136:139]\n\t" \
  "v_mfma_f32_16x16x32_bf16 v[140:143], v[112:115], v[92:95], v[140:143]\n\t" \
  "s_waitcnt vmcnt(1)\n\t" \
  "v_mfma_f32_16x16x32_bf16 v[136:139], v[116:119], v[96:99], v[136:139]\n\t" \
  "s_waitcnt vmcnt(0)\n\t" \
  "v_mfma_f32_16x16x32_bf16 v[140:143], v[116:119], v[100:103], v[140:143]\n\t" \
  "s_barrier\n\t" \
  "ds_read2st64_b64 v[144:147], v195 offset1:1\n\t" \
  "s_nop 1\n\t" \
  "v_permlane32_swap_b32 v136, v138\n\t" \
  "v_permlane32_swap_b32 v137, v139\n\t" \
  "s_nop 1\n\t" \
  "v_permlane32_swap_b32 v140, v142\n\t" \
  "v_permlane32_swap_b32 v141, v143\n\t" \
  "v_add_f32 v136, v136, v142\n\t" \
  "v_add_f32 v137, v137, v143\n\t" \
  "s_waitcnt lgkmcnt(0)\n\t" \
  "v_add_f32 v136, v136, v144\n\t" \
  "v_add_f32 v137, v137, v145\n\t" \
  "v_add_f32 v136, v136, v146\n\t" \
  "v_add_f32 v137, v137, v147\n\t" \
  "v_cvt_pk_bf16_f32 v148, v136, v137\n\t" \
  "s_nop 1\n\t" \
  "v_mov_b32_dpp v149, v148 row_half_mirror row_mask:0xf bank_mask:0xf\n\t" \
  "v_perm_b32 v150, v148, v149, v205\n\t" \
  "v_cmpx_o_f16_sdwa vcc, v150, v150 src0_sel:WORD_0 src1_sel:WORD_1\n\t" \
  "6:\n\t" \
  "buffer_store_dword v150, v201, %[rh], s97 offen sc1\n\t" \
  "s_memtime s[82:83]\n\t" \
  "s_mov_b64 exec, -1\n\t" \
  "s_cmp_eq_u64 vcc, -1\n\t" \
  "s_cbranch_scc0 8f\n\t" \
  "v_mov_b32 v151, 0\n\t" \
  "5:\n\t" \
  "ds_write_b32 v206, v151\n\t" \
  "s_waitcnt lgkmcnt(0)\n\t" \
  "s_mov_b32 s56, s82\n\t" \
  "s_barrier\n\t" \
  "ds_read2_b32 v[148:149], v207 offset1:8\n\t" \
  "s_waitcnt lgkmcnt(0)\n\t" \
  "v_or_b32 v148, v148, v149\n\t" \
  "v_cmp_ne_u32 vcc, 0, v148\n\t" \
  "s_cbranch_vccnz 12f\n\t" \
  "s_and_b32 s58, s58, 4\n\t" \
  "25:\n\t" \
  "s_add_i32 s82, s40, 2\n\t" \
  "s_min_i32 s83, s82, 63\n\t" \
  "s_mov_b32 exec_hi, 0\n\t" \
  "v_cmp_gt_i32 vcc, s83, v186\n\t" \
  "s_mov_b64 exec, -1\n\t" \
  "s_and_b32 s81, s82, 15\n\t" \
  "s_lshl_b32 s81, s81, 22\n\t" \
  "s_add_i32 s81, s81, s51\n\t" \
  "s_lshr_b32 s80, s81, 21\n\t" \
  "s_bitcmp0_b64 %[sel], s80\n\t" \
  "s_cselect_b32 s80, 0x2000, 0\n\t" \
  "s_add_i32 s81, s81, s80\n\t" \
  "s_cbranch_vccnz 27f\n\t" \
  "28:\n\t" \
  "s_cmp_ge_i32 s40, 62\n\t" \
  "s_cbranch_scc1 37f\n\t" \
  "s_and_b32 s76, s40, 3\n\t" \
  "s_lshl_b32 s76, s76, 18\n\t" \
  "buffer_load_dwordx4 v[0:3], v210, %[ru], s81 offen offset:0 sc1\n\t" \
  "buffer_load_dwordx4 v[4:7], v210, %[ru], s81 offen offset:1024 sc1\n\t" \
  "s_add_i32 s81, s81, 0x20000\n\t" \
  "buffer_load_dwordx4 v[8:11], v210, %[ru], s81 offen offset:0 sc1\n\t" \
  "buffer_load_dwordx4 v[12:15], v210, %[ru], s81 offen offset:1024 sc1\n\t" \
  "s_add_i32 s81, s81, 0x20000\n\t" \
  "buffer_load_dwordx4 v[16:19], v210, %[ru], s81 offen offset:0 sc1\n\t" \
  "buffer_load_dwordx4 v[20:23], v210, %[ru], s81 offen offset:1024 sc1\n\t" \
  "s_add_i32 s81, s81, 0x20000\n\t" \
  "buffer_load_dwordx4 v[24:27], v210, %[ru], s81 offen offset:0 sc1\n\t" \
  "buffer_load_dwordx4 v[28:31], v210, %[ru], s81 offen offset:1024 sc1\n\t" \
  "s_add_i32 s81, s81, 0x20000\n\t" \
  "s_bitcmp1_b32 s58, 4\n\t" \
  "s_cbranch_scc1 29f\n\t" \
  "s_sub_i32 s80, s56, s41\n\t" \
  "s_mul_hi_u32 s80, s80, s45\n\t" \
  "s_lshl_b32 s80, s80, 1\n\t" \
  "s_sub_i32 s80, s80, s57\n\t" \
  "s_min_u32 s47, s47, s46\n\t" \
  "s_min_u32 s47, s47, s80\n\t" \
  "v_mov_b32 v188, s47\n\t" \
  "s_mov_b32 s47, s46\n\t" \
  "s_mov_b32 s46, s80\n\t" \
  "buffer_store_dword v188, v202, %[rh], s52 offen sc1\n\t" \
  "v_mov_b64 v[188:189], -1\n\t" \
  "v_mov_b64 v[190:191], -1\n\t" \
  "v_mov_b32 v187, s40\n\t" \
  "s_add_i32 s80, s40, -1\n\t" \
  "s_and_b32 s80, s80, 7\n\t" \
  "s_lshl_b32 s80, s80, 15\n\t" \
  "s_add_i32 s80, s80, s49\n\t" \
  "buffer_store_dwordx4 v[188:191], v208, %[rh], s80 offen sc1\n\t" \
  "buffer_store_dword v187, v209, %[ru], 0 offen sc1\n\t" \
  "ds_write_b128 v197, v[72:75] offset:16384\n\t" \
  "ds_write_b128 v197, v[76:79] offset:17408\n\t" \
  "ds_write_b128 v197, v[80:83] offset:18432\n\t" \
  "ds_write_b128 v197, v[84:87] offset:19456\n\t" \
  "ds_write_b128 v197, v[88:91] offset:20480\n\t" \
  "ds_write_b128 v197, v[92:95] offset:21504\n\t" \
  "ds_write_b128 v197, v[96:99] offset:22528\n\t" \
  "ds_write_b128 v197, v[100:103] offset:23552\n\t" \
  "29:\n\t" \
  "s_and_b32 s58, s58, 36\n\t" \
  "buffer_load_dwordx4 v[32:35], v210, %[ru], s81 offen offset:0 sc1\n\t" \
  "buffer_load_dwordx4 v[36:39], v210, %[ru], s81 offen offset:1024 sc1\n\t" \
  "s_add_i32 s81, s81, 0x20000\n\t" \
  "buffer_load_dwordx4 v[40:43], v210, %[ru], s81 offen offset:0 sc1\n\t" \
  "buffer_load_dwordx4 v[44:47], v210, %[ru], s81 offen offset:1024 sc1\n\t" \
  "s_add_i32 s81, s81, 0x20000\n\t" \
  "buffer_load_dwordx4 a[0:3], v211, %[rh], s76 offen offset:0 sc1\n\t" \
  "buffer_load_dwordx4 a[4:7], v211, %[rh], s76 offen offset:1024 sc1\n\t" \
  "s_waitcnt lgkmcnt(0)\n\t" \
  "s_barrier\n\t" \
  "buffer_load_dwordx4 v[48:51], v210, %[ru], s81 offen offset:0 sc1\n\t" \
  "buffer_load_dwordx4 v[52:55], v210, %[ru], s81 offen offset:1024 sc1\n\t" \
  "s_add_i32 s81, s81, 0x20000\n\t" \
  "buffer_load_dwordx4 v[56:59], v210, %[ru], s81 offen offset:0 sc1\n\t" \
  "buffer_load_dwordx4 v[60:63], v210, %[ru], s81 offen offset:1024 sc1\n\t" \
  "s_add_i32 s81, s81, 0x20000\n\t" \
  "buffer_load_dwordx4 a[8:11], v212, %[rh], s76 offen offset:0 sc1\n\t" \
  "buffer_load_dwordx4 a[12:15], v212, %[rh], s76 offen offset:1024 sc1\n\t" \
  "buffer_load_dwordx4 v[64:67], v210, %[ru], s81 offen offset:0 sc1\n\t" \
  "buffer_load_dwordx4 v[68:71], v210, %[ru], s81 offen offset:1024 sc1\n\t" \
  "s_add_i32 s81, s81, 0x20000\n\t" \
  "buffer_load_dwordx4 v[104:107], v210, %[ru], s81 offen offset:0 sc1\n\t" \
  "buffer_load_dwordx4 v[108:111], v210, %[ru], s81 offen offset:1024 sc1\n\t" \
  "s_add_i32 s81, s81, 0x20000\n\t" \
  "buffer_load_dwordx4 a[16:19], v213, %[rh], s76 offen offset:0 sc1\n\t" \
  "buffer_load_dwordx4 a[20:23], v213, %[rh], s76 offen offset:1024 sc1\n\t" \
  "ds_read_b128 v[160:163], v193 offset:0\n\t" \
  "ds_read_b128 v[164:167], v193 offset:256\n\t" \
  "ds_read_b128 v[168:171], v193 offset:2048\n\t" \
  "ds_read_b128 v[172:175], v193 offset:2304\n\t" \
  "ds_read_b128 v[176:179], v193 offset:4096\n\t" \
  "ds_read_b128 v[180:183], v193 offset:4352\n\t" \
  "s_waitcnt lgkmcnt(2)\n\t" \
  "v_pk_add_f32 v[160:161], v[160:161], v[168:169]\n\t" \
  "v_pk_add_f32 v[162:163], v[162:163], v[170:171]\n\t" \
  "v_pk_add_f32 v[164:165], v[164:165], v[172:173]\n\t" \
  "v_pk_add_f32 v[166:167], v[166:167], v[174:175]\n\t" \
  "s_waitcnt lgkmcnt(0)\n\t" \
  "v_pk_add_f32 v[160:161], v[160:161], v[176:177]\n\t" \
  "v_pk_add_f32 v[162:163], v[162:163], v[178:179]\n\t" \
  "v_pk_add_f32 v[164:165], v[164:165], v[180:181]\n\t" \
  "v_pk_add_f32 v[166:167], v[166:167], v[182:183]\n\t" \
  "v_cvt_pk_bf16_f32 v160, v160, v161\n\t" \
  "v_cvt_pk_bf16_f32 v161, v162, v163\n\t" \
  "v_cvt_pk_bf16_f32 v162, v164, v165\n\t" \
  "v_cvt_pk_bf16_f32 v163, v166, v167\n\t" \
  "buffer_load_dwordx4 v[112:115], v210, %[ru], s81 offen offset:0 sc1\n\t" \
  "buffer_load_dwordx4 v[116:119], v210, %[ru], s81 offen offset:1024 sc1\n\t" \
  "s_add_i32 s81, s81, 0x20000\n\t" \
  "buffer_load_dwordx4 v[120:123], v210, %[ru], s81 offen offset:0 sc1\n\t" \
  "buffer_load_dwordx4 v[124:127], v210, %[ru], s81 offen offset:1024 sc1\n\t" \
  "s_add_i32 s81, s81, 0x20000\n\t" \
  "buffer_load_dwordx4 a[24:27], v214, %[rh], s76 offen offset:0 sc1\n\t" \
  "buffer_load_dwordx4 a[28:31], v214, %[rh], s76 offen offset:1024 sc1\n\t" \
  "s_waitcnt vmcnt(28)\n\t" \
  "v_swap_b32 v1, v8\n\t" \
  "v_swap_b32 v3, v10\n\t" \
  "v_swap_b32 v5, v12\n\t" \
  "v_swap_b32 v7, v14\n\t" \
  "ds_write_b128 v204, v[0:3] offset:0\n\t" \
  "ds_write_b128 v204, v[8:11] offset:1024\n\t" \
  "ds_write_b128 v204, v[4:7] offset:2048\n\t" \
  "ds_write_b128 v204, v[12:15] offset:3072\n\t" \
  "buffer_load_dwordx4 v[128:131], v210, %[ru], s81 offen offset:0 sc1\n\t" \
  "buffer_load_dwordx4 v[132:135], v210, %[ru], s81 offen offset:1024 sc1\n\t" \
  "s_add_i32 s81, s81, 0x20000\n\t" \
  "buffer_load_dwordx4 v[136:139], v210, %[ru], s81 offen offset:0 sc1\n\t" \
  "buffer_load_dwordx4 v[140:143], v210, %[ru], s81 offen offset:1024 sc1\n\t" \
  "s_add_i32 s81, s81, 0x20000\n\t" \
  "s_waitcnt vmcnt(28)\n\t" \
  "v_swap_b32 v17, v24\n\t" \
  "v_swap_b32 v19, v26\n\t" \
  "v_swap_b32 v21, v28\n\t" \
  "v_swap_b32 v23, v30\n\t" \
  "ds_write_b128 v204, v[16:19] offset:4096\n\t" \
  "ds_write_b128 v204, v[24:27] offset:5120\n\t" \
  "ds_write_b128 v204, v[20:23] offset:6144\n\t" \
  "ds_write_b128 v204, v[28:31] offset:7168\n\t" \
  "buffer_load_dwordx4 v[144:147], v210, %[ru], s81 offen offset:0 sc1\n\t" \
  "buffer_load_dwordx4 v[148:151], v210, %[ru], s81 offen offset:1024 sc1\n\t" \
  "s_add_i32 s81, s81, 0x20000\n\t" \
  "buffer_load_dwordx4 v[152:155], v210, %[ru], s81 offen offset:0 sc1\n\t" \
  "buffer_load_dwordx4 v[156:159], v210, %[ru], s81 offen offset:1024 sc1\n\t" \
  "s_add_i32 s81, s81, 0x20000\n\t" \
  "s_waitcnt vmcnt(28)\n\t" \
  "v_swap_b32 v33, v40\n\t" \
  "v_swap_b32 v35, v42\n\t" \
  "v_swap_b32 v37, v44\n\t" \
  "v_swap_b32 v39, v46\n\t" \
  "ds_write_b128 v204, v[32:35] offset:8192\n\t" \
  "ds_write_b128 v204, v[40:43] offset:9216\n\t" \
  "ds_write_b128 v204, v[36:39] offset:10240\n\t" \
  "ds_write_b128 v204, v[44:47] offset:11264\n\t" \
  "s_waitcnt vmcnt(22)\n\t" \
  "v_swap_b32 v49, v56\n\t" \
  "v_swap_b32 v51, v58\n\t" \
  "v_swap_b32 v53, v60\n\t" \
  "v_swap_b32 v55, v62\n\t" \
  "ds_write_b128 v204, v[48:51] offset:12288\n\t" \
  "ds_write_b128 v204, v[56:59] offset:13312\n\t" \
  "ds_write_b128 v204, v[52:55] offset:14336\n\t" \
  "ds_write_b128 v204, v[60:63] offset:15360\n\t" \
  "s_waitcnt vmcnt(16)\n\t" \
  "v_swap_b32 v65, v104\n\t" \
  "v_swap_b32 v67, v106\n\t" \
  "v_swap_b32 v69, v108\n\t" \
  "v_swap_b32 v71, v110\n\t" \
  "ds_write_b128 v204, v[64:67] offset:16384\n\t" \
  "ds_write_b128 v204, v[104:107] offset:17408\n\t" \
  "ds_write_b128 v204, v[68:71] offset:18432\n\t" \
  "ds_write_b128 v204, v[108:111] offset:19456\n\t" \
  "s_waitcnt vmcnt(10)\n\t" \
  "v_swap_b32 v113, v120\n\t" \
  "v_swap_b32 v115, v122\n\t" \
  "v_swap_b32 v117, v124\n\t" \
  "v_swap_b32 v119, v126\n\t" \
  "ds_write_b128 v204, v[112:115] offset:20480\n\t" \
  "ds_write_b128 v204, v[120:123] offset:21504\n\t" \
  "ds_write_b128 v204, v[116:119] offset:22528\n\t" \
  "ds_write_b128 v204, v[124:127] offset:23552\n\t" \
  "s_waitcnt vmcnt(4)\n\t" \
  "v_swap_b32 v129, v136\n\t" \
  "v_swap_b32 v131, v138\n\t" \
  "v_swap_b32 v133, v140\n\t" \
  "v_swap_b32 v135, v142\n\t" \
  "ds_write_b128 v204, v[128:131] offset:24576\n\t" \
  "ds_write_b128 v204, v[136:139] offset:25600\n\t" \
  "ds_write_b128 v204, v[132:135] offset:26624\n\t" \
  "ds_write_b128 v204, v[140:143] offset:27648\n\t" \
  "s_waitcnt vmcnt(0)\n\t" \
  "v_swap_b32 v145, v152\n\t" \
  "v_swap_b32 v147, v154\n\t" \
  "v_swap_b32 v149, v156\n\t" \
  "v_swap_b32 v151, v158\n\t" \
  "ds_write_b128 v204, v[144:147] offset:28672\n\t" \
  "ds_write_b128 v204, v[152:155] offset:29696\n\t" \
  "ds_write_b128 v204, v[148:151] offset:30720\n\t" \
  "ds_write_b128 v204, v[156:159] offset:31744\n\t" \
  "s_waitcnt lgkmcnt(0)\n\t" \
  "s_cmp_lt_i32 s40, 0\n\t" \
  "s_cbranch_scc1 39f\n\t" \
  "38:\n\t" \
  "s_add_i32 s80, s40, -2\n\t" \
  "s_lshl_b32 s80, s80, 17\n\t" \
  "s_min_u32 s80, s80, 0x70000000\n\t" \
  "buffer_store_dwordx4 v[160:163], v196, %[ro], s80 offen nt\n\t" \
  "ds_read_b128 a[32:35], v198 offset:0\n\t" \
  "ds_read_b128 a[36:39], v198 offset:1024\n\t" \
  "ds_read_b128 a[40:43], v198 offset:2048\n\t" \
  "ds_read_b128 a[44:47], v198 offset:3072\n\t" \
  "ds_read_b128 a[48:51], v198 offset:4096\n\t" \
  "ds_read_b128 a[52:55], v198 offset:5120\n\t" \
  "ds_read_b128 a[56:59], v198 offset:6144\n\t" \
  "ds_read_b128 a[60:63], v198 offset:7168\n\t" \
  "ds_read_b128 a[64:67], v198 offset:8192\n\t" \
  "ds_read_b128 a[68:71], v198 offset:9216\n\t" \
  "ds_read_b128 a[72:75], v198 offset:10240\n\t" \
  "ds_read_b128 a[76:79], v198 offset:11264\n\t" \
  "ds_read_b128 a[80:83], v198 offset:12288\n\t" \
  "ds_read_b128 a[84:87], v198 offset:13312\n\t" \
  "ds_read_b128 a[88:91], v198 offset:14336\n\t" \
  "ds_read_b128 a[92:95], v198 offset:15360\n\t" \
  "s_waitcnt vmcnt(1) lgkmcnt(0)\n\t" \
  "31:\n\t" \
  "v_mfma_f32_16x16x32_bf16 v[168:171], a[32:35], a[0:3], 0\n\t" \
  "v_mfma_f32_16x16x32_bf16 v[172:175], a[64:67], a[0:3], 0\n\t" \
  "v_mfma_f32_16x16x32_bf16 v[168:171], a[36:39], a[4:7], v[168:171]\n\t" \
  "v_mfma_f32_16x16x32_bf16 v[172:175], a[68:71], a[4:7], v[172:175]\n\t" \
  "v_mfma_f32_16x16x32_bf16 v[168:171], a[40:43], a[8:11], v[168:171]\n\t" \
  "v_mfma_f32_16x16x32_bf16 v[172:175], a[72:75], a[8:11], v[172:175]\n\t" \
  "v_mfma_f32_16x16x32_bf16 v[168:171], a[44:47], a[12:15], v[168:171]\n\t" \
  "v_mfma_f32_16x16x32_bf16 v[172:175], a[76:79], a[12:15], v[172:175]\n\t" \
  "v_mfma_f32_16x16x32_bf16 v[168:171], a[48:51], a[16:19], v[168:171]\n\t" \
  "v_mfma_f32_16x16x32_bf16 v[172:175], a[80:83], a[16:19], v[172:175]\n\t" \
  "v_mfma_f32_16x16x32_bf16 v[168:171], a[52:55], a[20:23], v[168:171]\n\t" \
  "v_mfma_f32_16x16x32_bf16 v[172:175], a[84:87], a[20:23], v[172:175]\n\t" \
  "v_mfma_f32_16x16x32_bf16 v[168:171], a[56:59], a[24:27], v[168:171]\n\t" \
  "v_mfma_f32_16x16x32_bf16 v[172:175], a[88:91], a[24:27], v[172:175]\n\t" \
  "v_mfma_f32_16x16x32_bf16 v[168:171], a[60:63], a[28:31], v[168:171]\n\t" \
  "v_mfma_f32_16x16x32_bf16 v[172:175], a[92:95], a[28:31], v[172:175]\n\t" \
  "s_nop 7\n\t" \
  "39:\n\t" \
  "s_add_i32 s40, s40, 2\n\t" \
  "v_cmp_u_f32 vcc, v168, v168\n\t" \
  "s_cbranch_vccnz 32f\n\t" \
  "33:\n\t" \
  "s_barrier\n\t" \
  "s_add_i32 s85, s40, -1\n\t" \
  "s_and_b32 s82, s85, 2\n\t" \
  "s_lshl_b32 s82, s82, 13\n\t" \
  "s_and_b32 s80, s85, 1\n\t" \
  "s_lshl_b32 s80, s80, 12\n\t" \
  "s_add_i32 s82, s82, s80\n\t" \
  "s_add_i32 s82, s82, 0x84000\n\t" \
  "s_mov_b32 exec_hi, 0\n\t" \
  "buffer_load_dword v184, v200, %[rh], s82 offen sc1\n\t" \
  "buffer_load_dword v186, v203, %[ru], 0 offen sc1\n\t" \
  "s_mov_b64 exec, -1\n\t" \
  "ds_write_b128 v199, v[168:171] offset:0\n\t" \
  "ds_write_b128 v199, v[172:175] offset:1024\n\t" \
  "s_mov_b32 s59, 0\n\t" \
  "s_and_b32 s58, s58, 4\n\t" \
  "s_cmp_lt_i32 s40, 64\n\t" \
  "s_cbranch_scc1 20b\n\t" \
  "s_waitcnt lgkmcnt(0)\n\t" \
  "s_barrier\n\t" \
  "ds_read_b128 v[160:163], v193 offset:0\n\t" \
  "ds_read_b128 v[164:167], v193 offset:256\n\t" \
  "ds_read_b128 v[168:171], v193 offset:2048\n\t" \
  "ds_read_b128 v[172:175], v193 offset:2304\n\t" \
  "ds_read_b128 v[176:179], v193 offset:4096\n\t" \
  "ds_read_b128 v[180:183], v193 offset:4352\n\t" \
  "s_waitcnt lgkmcnt(2)\n\t" \
  "v_pk_add_f32 v[160:161], v[160:161], v[168:169]\n\t" \
  "v_pk_add_f32 v[162:163], v[162:163], v[170:171]\n\t" \
  "v_pk_add_f32 v[164:165], v[164:165], v[172:173]\n\t" \
  "v_pk_add_f32 v[166:167], v[166:167], v[174:175]\n\t" \
  "s_waitcnt lgkmcnt(0)\n\t" \
  "v_pk_add_f32 v[160:161], v[160:161], v[176:177]\n\t" \
  "v_pk_add_f32 v[162:163], v[162:163], v[178:179]\n\t" \
  "v_pk_add_f32 v[164:165], v[164:165], v[180:181]\n\t" \
  "v_pk_add_f32 v[166:167], v[166:167], v[182:183]\n\t" \
  "v_cvt_pk_bf16_f32 v160, v160, v161\n\t" \
  "v_cvt_pk_bf16_f32 v161, v162, v163\n\t" \
  "v_cvt_pk_bf16_f32 v162, v164, v165\n\t" \
  "v_cvt_pk_bf16_f32 v163, v166, v167\n\t" \
  "s_add_i32 s80, s40, -2\n\t" \
  "s_lshl_b32 s80, s80, 17\n\t" \
  "s_min_u32 s80, s80, 0x70000000\n\t" \
  "buffer_store_dwordx4 v[160:163], v196, %[ro], s80 offen nt\n\t" \
  "s_mov_b32 s58, 64\n\t" \
  "s_branch 7f\n\t" \
  "22:\n\t" \
  "s_mov_b32 s81, 0\n\t" \
  "s_cmp_eq_u32 s40, 0\n\t" \
  "s_cbranch_scc1 24b\n\t" \
  "s_bitcmp1_b32 s58, 2\n\t" \
  "s_cbranch_scc1 24b\n\t" \
  "s_mov_b64 exec, -1\n\t" \
  "s_sleep 10\n\t" \
  "s_add_u32 s59, s59, 0x10000\n\t" \
  "s_cmp_lt_u32 s59, 0x100000\n\t" \
  "s_cbranch_scc1 19b\n\t" \
  "s_or_b32 s58, s58, 1\n\t" \
  "s_branch 7f\n\t" \
  "14:\n\t" \
  "v_mov_b32 v184, 0\n\t" \
  "s_bitcmp1_b32 s58, 2\n\t" \
  "s_nop 0\n\t" \
  "s_cbranch_scc1 15b\n\t" \
  "s_mov_b32 s84, 950\n\t" \
  "s_add_u32 s59, s59, 0x10000\n\t" \
  "s_cmp_lt_u32 s59, 0x100000\n\t" \
  "s_mov_b64 exec, vcc\n\t" \
  "buffer_load_dword v187, v200, %[rh], s78 offen sc1\n\t" \
  "s_mov_b32 exec_lo, -1\n\t" \
  "s_cbranch_scc1 13b\n\t" \
  "s_mov_b64 exec, -1\n\t" \
  "s_or_b32 s58, s58, 1\n\t" \
  "s_branch 7f\n\t" \
  "27:\n\t" \
  "s_bitcmp1_b32 s58, 2\n\t" \
  "s_cbranch_scc1 28b\n\t" \
  "s_bitcmp1_b32 s58, 4\n\t" \
  "s_cbranch_scc1 23f\n\t" \
  "s_sub_i32 s80, s56, s41\n\t" \
  "s_mul_hi_u32 s80, s80, s45\n\t" \
  "s_lshl_b32 s80, s80, 1\n\t" \
  "s_sub_i32 s80, s80, s57\n\t" \
  "s_min_u32 s47, s47, s46\n\t" \
  "s_min_u32 s47, s47, s80\n\t" \
  "v_mov_b32 v188, s47\n\t" \
  "s_mov_b32 s47, s46\n\t" \
  "s_mov_b32 s46, s80\n\t" \
  "buffer_store_dword v188, v202, %[rh], s52 offen sc1\n\t" \
  "v_mov_b64 v[188:189], -1\n\t" \
  "v_mov_b64 v[190:191], -1\n\t" \
  "v_mov_b32 v187, s40\n\t" \
  "s_add_i32 s80, s40, -1\n\t" \
  "s_and_b32 s80, s80, 7\n\t" \
  "s_lshl_b32 s80, s80, 15\n\t" \
  "s_add_i32 s80, s80, s49\n\t" \
  "buffer_store_dwordx4 v[188:191], v208, %[rh], s80 offen sc1\n\t" \
  "buffer_store_dword v187, v209, %[ru], 0 offen sc1\n\t" \
  "ds_write_b128 v197, v[72:75] offset:16384\n\t" \
  "ds_write_b128 v197, v[76:79] offset:17408\n\t" \
  "ds_write_b128 v197, v[80:83] offset:18432\n\t" \
  "ds_write_b128 v197, v[84:87] offset:19456\n\t" \
  "ds_write_b128 v197, v[88:91] offset:20480\n\t" \
  "ds_write_b128 v197, v[92:95] offset:21504\n\t" \
  "ds_write_b128 v197, v[96:99] offset:22528\n\t" \
  "ds_write_b128 v197, v[100:103] offset:23552\n\t" \
  "s_or_b32 s58, s58, 16\n\t" \
  "23:\n\t" \
  "s_add_u32 s59, s59, 0x1000000\n\t" \
  "s_cmp_lt_u32 s59, 0x40000000\n\t" \
  "s_cbranch_scc1 26f\n\t" \
  "s_or_b32 s58, s58, 1\n\t" \
  "s_branch 7f\n\t" \
  "26:\n\t" \
  "buffer_load_dword v186, v203, %[ru], 0 offen sc1\n\t" \
  "s_waitcnt vmcnt(0)\n\t" \
  "s_branch 25b\n\t" \
  "37:\n\t" \
  "s_and_b32 s76, s40, 3\n\t" \
  "s_lshl_b32 s76, s76, 18\n\t" \
  "buffer_load_dwordx4 a[0:3], v211, %[rh], s76 offen offset:0 sc1\n\t" \
  "buffer_load_dwordx4 a[4:7], v211, %[rh], s76 offen offset:1024 sc1\n\t" \
  "buffer_load_dwordx4 a[8:11], v212, %[rh], s76 offen offset:0 sc1\n\t" \
  "buffer_load_dwordx4 a[12:15], v212, %[rh], s76 offen offset:1024 sc1\n\t" \
  "buffer_load_dwordx4 a[16:19], v213, %[rh], s76 offen offset:0 sc1\n\t" \
  "buffer_load_dwordx4 a[20:23], v213, %[rh], s76 offen offset:1024 sc1\n\t" \
  "buffer_load_dwordx4 a[24:27], v214, %[rh], s76 offen offset:0 sc1\n\t" \
  "buffer_load_dwordx4 a[28:31], v214, %[rh], s76 offen offset:1024 sc1\n\t" \
  "s_bitcmp1_b32 s58, 4\n\t" \
  "s_cbranch_scc1 29f\n\t" \
  "s_sub_i32 s80, s56, s41\n\t" \
  "s_mul_hi_u32 s80, s80, s45\n\t" \
  "s_lshl_b32 s80, s80, 1\n\t" \
  "s_sub_i32 s80, s80, s57\n\t" \
  "s_min_u32 s47, s47, s46\n\t" \
  "s_min_u32 s47, s47, s80\n\t" \
  "v_mov_b32 v188, s47\n\t" \
  "s_mov_b32 s47, s46\n\t" \
  "s_mov_b32 s46, s80\n\t" \
  "buffer_store_dword v188, v202, %[rh], s52 offen sc1\n\t" \
  "v_mov_b64 v[188:189], -1\n\t" \
  "v_mov_b64 v[190:191], -1\n\t" \
  "v_mov_b32 v187, s40\n\t" \
  "s_add_i32 s80, s40, -1\n\t" \
  "s_and_b32 s80, s80, 7\n\t" \
  "s_lshl_b32 s80, s80, 15\n\t" \
  "s_add_i32 s80, s80, s49\n\t" \
  "buffer_store_dwordx4 v[188:191], v208, %[rh], s80 offen sc1\n\t" \
  "buffer_store_dword v187, v209, %[ru], 0 offen sc1\n\t" \
  "ds_write_b128 v197, v[72:75] offset:16384\n\t" \
  "ds_write_b128 v197, v[76:79] offset:17408\n\t" \
  "ds_write_b128 v197, v[80:83] offset:18432\n\t" \
  "ds_write_b128 v197, v[84:87] offset:19456\n\t" \
  "ds_write_b128 v197, v[88:91] offset:20480\n\t" \
  "ds_write_b128 v197, v[92:95] offset:21504\n\t" \
  "ds_write_b128 v197, v[96:99] offset:22528\n\t" \
  "ds_write_b128 v197, v[100:103] offset:23552\n\t" \
  "29:\n\t" \
  "s_and_b32 s58, s58, 36\n\t" \
  "s_waitcnt lgkmcnt(0)\n\t" \
  "s_barrier\n\t" \
  "ds_read_b128 v[160:163], v193 offset:0\n\t" \
  "ds_read_b128 v[164:167], v193 offset:256\n\t" \
  "ds_read_b128 v[168:171], v193 offset:2048\n\t" \
  "ds_read_b128 v[172:175], v193 offset:2304\n\t" \
  "ds_read_b128 v[176:179], v193 offset:4096\n\t" \
  "ds_read_b128 v[180:183], v193 offset:4352\n\t" \
  "s_waitcnt lgkmcnt(2)\n\t" \
  "v_pk_add_f32 v[160:161], v[160:161], v[168:169]\n\t" \
  "v_pk_add_f32 v[162:163], v[162:163], v[170:171]\n\t" \
  "v_pk_add_f32 v[164:165], v[164:165], v[172:173]\n\t" \
  "v_pk_add_f32 v[166:167], v[166:167], v[174:175]\n\t" \
  "s_waitcnt lgkmcnt(0)\n\t" \
  "v_pk_add_f32 v[160:161], v[160:161], v[176:177]\n\t" \
  "v_pk_add_f32 v[162:163], v[162:163], v[178:179]\n\t" \
  "v_pk_add_f32 v[164:165], v[164:165], v[180:181]\n\t" \
  "v_pk_add_f32 v[166:167], v[166:167], v[182:183]\n\t" \
  "v_cvt_pk_bf16_f32 v160, v160, v161\n\t" \
  "v_cvt_pk_bf16_f32 v161, v162, v163\n\t" \
  "v_cvt_pk_bf16_f32 v162, v164, v165\n\t" \
  "v_cvt_pk_bf16_f32 v163, v166, v167\n\t" \
  "s_branch 38b\n\t" \
  "8:\n\t" \
  "s_mov_b64 vcc, -1\n\t" \
  "s_and_b32 s80, s58, 6\n\t" \
  "s_cbranch_scc1 6b\n\t" \
  "v_mov_b32 v151, 1\n\t" \
  "s_branch 5b\n\t" \
  "32:\n\t" \
  "s_and_b32 s80, s58, 36\n\t" \
  "s_cbranch_scc1 33b\n\t" \
  "s_add_i32 s40, s40, -2\n\t" \
  "34:\n\t" \
  "s_or_b32 s59, s59, 128\n\t" \
  "s_and_b32 s76, s40, 3\n\t" \
  "s_lshl_b32 s76, s76, 18\n\t" \
  "35:\n\t" \
  "buffer_load_dwordx4 a[0:3], v211, %[rh], s76 offen offset:0 sc1\n\t" \
  "buffer_load_dwordx4 a[4:7], v211, %[rh], s76 offen offset:1024 sc1\n\t" \
  "buffer_load_dwordx4 a[8:11], v212, %[rh], s76 offen offset:0 sc1\n\t" \
  "buffer_load_dwordx4 a[12:15], v212, %[rh], s76 offen offset:1024 sc1\n\t" \
  "buffer_load_dwordx4 a[16:19], v213, %[rh], s76 offen offset:0 sc1\n\t" \
  "buffer_load_dwordx4 a[20:23], v213, %[rh], s76 offen offset:1024 sc1\n\t" \
  "buffer_load_dwordx4 a[24:27], v214, %[rh], s76 offen offset:0 sc1\n\t" \
  "buffer_load_dwordx4 a[28:31], v214, %[rh], s76 offen offset:1024 sc1\n\t" \
  "s_waitcnt vmcnt(0)\n\t" \
  "s_bitcmp1_b32 s58, 2\n\t" \
  "s_cbranch_scc1 36f\n\t" \
  "v_accvgpr_read_b32 v0, a0\n\t" \
  "v_accvgpr_read_b32 v1, a1\n\t" \
  "v_accvgpr_read_b32 v2, a2\n\t" \
  "v_accvgpr_read_b32 v3, a3\n\t" \
  "v_accvgpr_read_b32 v4, a4\n\t" \
  "v_accvgpr_read_b32 v5, a5\n\t" \
  "v_accvgpr_read_b32 v6, a6\n\t" \
  "v_accvgpr_read_b32 v7, a7\n\t" \
  "v_accvgpr_read_b32 v8, a8\n\t" \
  "v_accvgpr_read_b32 v9, a9\n\t" \
  "v_accvgpr_read_b32 v10, a10\n\t" \
  "v_accvgpr_read_b32 v11, a11\n\t" \
  "v_accvgpr_read_b32 v12, a12\n\t" \
  "v_accvgpr_read_b32 v13, a13\n\t" \
  "v_accvgpr_read_b32 v14, a14\n\t" \
  "v_accvgpr_read_b32 v15, a15\n\t" \
  "v_accvgpr_read_b32 v16, a16\n\t" \
  "v_accvgpr_read_b32 v17, a17\n\t" \
  "v_accvgpr_read_b32 v18, a18\n\t" \
  "v_accvgpr_read_b32 v19, a19\n\t" \
  "v_accvgpr_read_b32 v20, a20\n\t" \
  "v_accvgpr_read_b32 v21, a21\n\t" \
  "v_accvgpr_read_b32 v22, a22\n\t" \
  "v_accvgpr_read_b32 v23, a23\n\t" \
  "v_accvgpr_read_b32 v24, a24\n\t" \
  "v_accvgpr_read_b32 v25, a25\n\t" \
  "v_accvgpr_read_b32 v26, a26\n\t" \
  "v_accvgpr_read_b32 v27, a27\n\t" \
  "v_accvgpr_read_b32 v28, a28\n\t" \
  "v_accvgpr_read_b32 v29, a29\n\t" \
  "v_accvgpr_read_b32 v30, a30\n\t" \
  "v_accvgpr_read_b32 v31, a31\n\t" \
  "v_max_u32 v148, v0, v1\n\t" \
  "v_max3_u32 v148, v148, v2, v3\n\t" \
  "v_max3_u32 v148, v148, v4, v5\n\t" \
  "v_max3_u32 v148, v148, v6, v7\n\t" \
  "v_max3_u32 v148, v148, v8, v9\n\t" \
  "v_max3_u32 v148, v148, v10, v11\n\t" \
  "v_max3_u32 v148, v148, v12, v13\n\t" \
  "v_max3_u32 v148, v148, v14, v15\n\t" \
  "v_max3_u32 v148, v148, v16, v17\n\t" \
  "v_max3_u32 v148, v148, v18, v19\n\t" \
  "v_max3_u32 v148, v148, v20, v21\n\t" \
  "v_max3_u32 v148, v148, v22, v23\n\t" \
  "v_max3_u32 v148, v148, v24, v25\n\t" \
  "v_max3_u32 v148, v148, v26, v27\n\t" \
  "v_max3_u32 v148, v148, v28, v29\n\t" \
  "v_max3_u32 v148, v148, v30, v31\n\t" \
  "v_cmp_eq_u32 vcc, -1, v148\n\t" \
  "s_cbranch_vccz 36f\n\t" \
  "s_add_u32 s59, s59, 0x1000000\n\t" \
  "s_cmp_lt_u32 s59, 0x10000000\n\t" \
  "s_cbranch_scc1 35b\n\t" \
  "s_or_b32 s58, s58, 9\n\t" \
  "s_branch 7f\n\t" \
  "36:\n\t" \
  "s_and_b32 s58, s58, 4\n\t" \
  "s_or_b32 s58, s58, 32\n\t" \
  "s_branch 31b\n\t" \
  "18:\n\t" \
  "v_mov_b32 v186, -1\n\t" \
  "s_and_b32 s80, s40, 3\n\t" \
  "s_mul_i32 s77, s80, 0xc000\n\t" \
  "s_add_i32 s77, s77, 0x40000\n\t" \
  "s_lshl_b32 s78, s80, 14\n\t" \
  "s_add_i32 s78, s78, 0x70000\n\t" \
  "s_mov_b32 s57, 0\n\t" \
  "s_add_i32 s96, s77, 0x1000\n\t" \
  "s_lshl_b32 s88, s80, 20\n\t" \
  "s_add_i32 s88, s88, s48\n\t" \
  "s_lshr_b32 s80, s88, 21\n\t" \
  "s_bitcmp1_b64 %[sel], s80\n\t" \
  "s_cselect_b32 s80, 0x2000, 0\n\t" \
  "s_add_i32 s88, s88, s80\n\t" \
  "s_xor_b32 s89, s88, 0x800\n\t" \
  "s_xor_b32 s90, s88, 0x1000\n\t" \
  "s_xor_b32 s91, s88, 0x1800\n\t" \
  "s_add_i32 s92, s77, s50\n\t" \
  "s_xor_b32 s93, s92, 0x100\n\t" \
  "s_xor_b32 s94, s92, 0x200\n\t" \
  "s_xor_b32 s95, s92, 0x300\n\t" \
  "s_add_i32 s97, s40, 1\n\t" \
  "s_and_b32 s97, s97, 7\n\t" \
  "s_lshl_b32 s97, s97, 15\n\t" \
  "s_add_i32 s97, s97, s49\n\t" \
  "12:\n\t" \
  "s_or_b32 s58, s58, 3\n\t" \
  "11:\n\t" \
  "buffer_load_dwordx4 v[72:75], v192, %[ru], s88 offen offset:0 sc1\n\t" \
  "buffer_load_dwordx4 v[76:79], v192, %[ru], s88 offen offset:1024 sc1\n\t" \
  "buffer_load_dwordx4 v[80:83], v192, %[ru], s89 offen offset:0 sc1\n\t" \
  "buffer_load_dwordx4 v[84:87], v192, %[ru], s89 offen offset:1024 sc1\n\t" \
  "buffer_load_dwordx4 v[88:91], v192, %[ru], s90 offen offset:0 sc1\n\t" \
  "buffer_load_dwordx4 v[92:95], v192, %[ru], s90 offen offset:1024 sc1\n\t" \
  "buffer_load_dwordx4 v[96:99], v192, %[ru], s91 offen offset:0 sc1\n\t" \
  "buffer_load_dwordx4 v[100:103], v192, %[ru], s91 offen offset:1024 sc1\n\t" \
  "buffer_load_dwordx4 v[104:107], v194, %[rh], s92 offen sc1\n\t" \
  "buffer_load_dwordx4 v[108:111], v194, %[rh], s93 offen sc1\n\t" \
  "buffer_load_dwordx4 v[112:115], v194, %[rh], s94 offen sc1\n\t" \
  "buffer_load_dwordx4 v[116:119], v194, %[rh], s95 offen sc1\n\t" \
  "s_waitcnt vmcnt(0)\n\t" \
  "s_add_u32 s59, s59, 1\n\t" \
  "s_bitcmp1_b32 s58, 2\n\t" \
  "s_cbranch_scc1 10b\n\t" \
  "v_max_u32 v148, v72, v73\n\t" \
  "v_max3_u32 v148, v148, v74, v75\n\t" \
  "v_max3_u32 v148, v148, v76, v77\n\t" \
  "v_max3_u32 v148, v148, v78, v79\n\t" \
  "v_max3_u32 v148, v148, v80, v81\n\t" \
  "v_max3_u32 v148, v148, v82, v83\n\t" \
  "v_max3_u32 v148, v148, v84, v85\n\t" \
  "v_max3_u32 v148, v148, v86, v87\n\t" \
  "v_max3_u32 v148, v148, v88, v89\n\t" \
  "v_max3_u32 v148, v148, v90, v91\n\t" \
  "v_max3_u32 v148, v148, v92, v93\n\t" \
  "v_max3_u32 v148, v148, v94, v95\n\t" \
  "v_max3_u32 v148, v148, v96, v97\n\t" \
  "v_max3_u32 v148, v148, v98, v99\n\t" \
  "v_max3_u32 v148, v148, v100, v101\n\t" \
  "v_max3_u32 v148, v148, v102, v103\n\t" \
  "v_max3_u32 v148, v148, v104, v105\n\t" \
  "v_max3_u32 v148, v148, v106, v107\n\t" \
  "v_max3_u32 v148, v148, v108, v109\n\t" \
  "v_max3_u32 v148, v148, v110, v111\n\t" \
  "v_max3_u32 v148, v148, v112, v113\n\t" \
  "v_max3_u32 v148, v148, v114, v115\n\t" \
  "v_max3_u32 v148, v148, v116, v117\n\t" \
  "v_max3_u32 v148, v148, v118, v119\n\t" \
  "v_cmp_eq_u32 vcc, -1, v148\n\t" \
  "s_cbranch_vccz 10b\n\t" \
  "s_bitcmp1_b32 s59, 6\n\t" \
  "s_cbranch_scc0 11b\n\t" \
  "7:\n\t" \
  "s_branch 92f\n\t" \
  "91:\n\t" \
  "s_mov_b32 s58, s42\n\t" \
  "s_mov_b32 s59, 0\n\t" \
  "s_bitcmp1_b32 s58, 4\n\t" \
  "s_cbranch_scc1 37f\n\t" \
  "s_bitcmp1_b32 s58, 3\n\t" \
  "s_cbranch_scc1 34f\n\t" \
  "s_bitcmp1_b32 s58, 1\n\t" \
  "s_cbranch_scc1 18f\n\t" \
  "17:\n\t" \
  "v_mov_b64 v[40:41], 0\n\t" \
  "v_mov_b64 v[42:43], 0\n\t" \
  "v_mov_b64 v[44:45], 0\n\t" \
  "v_mov_b64 v[46:47], 0\n\t" \
  "v_mov_b64 v[48:49], 0\n\t" \
  "v_mov_b64 v[50:51], 0\n\t" \
  "v_mov_b64 v[52:53], 0\n\t" \
  "v_mov_b64 v[54:55], 0\n\t" \
  "v_mov_b64 v[56:57], 0\n\t" \
  "v_mov_b64 v[58:59], 0\n\t" \
  "v_mov_b64 v[60:61], 0\n\t" \
  "v_mov_b64 v[62:63], 0\n\t" \
  "v_mov_b64 v[64:65], 0\n\t" \
  "v_mov_b64 v[66:67], 0\n\t" \
  "v_mov_b64 v[68:69], 0\n\t" \
  "v_mov_b64 v[70:71], 0\n\t" \
  "v_mov_b64 v[72:73], 0\n\t" \
  "v_mov_b64 v[74:75], 0\n\t" \
  "v_mov_b64 v[76:77], 0\n\t" \
  "v_mov_b64 v[78:79], 0\n\t" \
  "v_mov_b64 v[80:81], 0\n\t" \
  "v_mov_b64 v[82:83], 0\n\t" \
  "v_mov_b64 v[84:85], 0\n\t" \
  "v_mov_b64 v[86:87], 0\n\t" \
  "v_mov_b64 v[88:89], 0\n\t" \
  "v_mov_b64 v[90:91], 0\n\t" \
  "v_mov_b64 v[92:93], 0\n\t" \
  "v_mov_b64 v[94:95], 0\n\t" \
  "v_mov_b64 v[96:97], 0\n\t" \
  "v_mov_b64 v[98:99], 0\n\t" \
  "v_mov_b64 v[100:101], 0\n\t" \
  "v_mov_b64 v[102:103], 0\n\t" \
  "v_mov_b64 v[136:137], 0\n\t" \
  "v_mov_b64 v[138:139], 0\n\t" \
  "v_mov_b64 v[140:141], 0\n\t" \
  "v_mov_b64 v[142:143], 0\n\t" \
  "19:\n\t" \
  "s_add_i32 s85, s40, -1\n\t" \
  "s_and_b32 s82, s85, 2\n\t" \
  "s_lshl_b32 s82, s82, 13\n\t" \
  "s_and_b32 s80, s85, 1\n\t" \
  "s_lshl_b32 s80, s80, 12\n\t" \
  "s_add_i32 s82, s82, s80\n\t" \
  "s_add_i32 s82, s82, 0x84000\n\t" \
  "s_mov_b32 exec_hi, 0\n\t" \
  "buffer_load_dword v184, v200, %[rh], s82 offen sc1\n\t" \
  "buffer_load_dword v185, v200, %[rh], s52 offen sc1\n\t" \
  "s_mov_b64 exec, -1\n\t" \
  "20:\n\t" \
  "s_and_b32 s80, s40, 3\n\t" \
  "s_mul_i32 s77, s80, 0xc000\n\t" \
  "s_add_i32 s77, s77, 0x40000\n\t" \
  "s_lshl_b32 s78, s80, 14\n\t" \
  "s_add_i32 s78, s78, 0x70000\n\t" \
  "s_mov_b32 s57, 0\n\t" \
  "s_add_i32 s96, s77, 0x1000\n\t" \
  "s_and_b32 s98, s40, 2\n\t" \
  "s_lshl_b32 s98, s98, 13\n\t" \
  "s_and_b32 s80, s40, 1\n\t" \
  "s_lshl_b32 s80, s80, 12\n\t" \
  "s_add_i32 s98, s98, s80\n\t" \
  "s_add_i32 s98, s98, 0x84000\n\t" \
  "s_memtime vcc\n\t" \
  "s_memrealtime s[86:87]\n\t" \
  "s_nop 4\n\t" \
  "s_memrealtime s[60:61]\n\t" \
  "s_nop 4\n\t" \
  "s_memrealtime s[62:63]\n\t" \
  "s_nop 4\n\t" \
  "s_memrealtime s[64:65]\n\t" \
  "s_nop 4\n\t" \
  "s_memrealtime s[66:67]\n\t" \
  "s_nop 4\n\t" \
  "s_memrealtime s[68:69]\n\t" \
  "s_mov_b32 s79, 0\n\t" \
  "s_waitcnt lgkmcnt(0)\n\t" \
  "s_cmp_eq_u32 s86, s60\n\t" \
  "s_addc_u32 s79, s79, 0\n\t" \
  "s_cmp_eq_u32 s86, s62\n\t" \
  "s_addc_u32 s79, s79, 0\n\t" \
  "s_cmp_eq_u32 s86, s64\n\t" \
  "s_addc_u32 s79, s79, 0\n\t" \
  "s_cmp_eq_u32 s86, s66\n\t" \
  "s_addc_u32 s79, s79, 0\n\t" \
  "s_cmp_eq_u32 s86, s68\n\t" \
  "s_addc_u32 s79, s79, 0\n\t" \
  "s_mul_i32 s79, s79, 24\n\t" \
  "s_add_i32 s79, s79, vcc_lo\n\t" \
  "s_lshl4_add_u32 s80, s86, 64\n\t" \
  "s_sub_i32 s86, s79, s41\n\t" \
  "s_sub_i32 s87, s80, s43\n\t" \
  "s_cmp_lt_u32 s86, 4000\n\t" \
  "s_cbranch_scc1 21f\n\t" \
  "v_cvt_f32_u32 v165, s86\n\t" \
  "v_cvt_f32_u32 v164, s87\n\t" \
  "v_rcp_f32 v165, v165\n\t" \
  "s_nop 0\n\t" \
  "v_mul_f32 v165, v165, v164\n\t" \
  "v_max_f32 v165, 0x3f028f5c, v165\n\t" \
  "v_min_f32 v165, 0x3ffeb852, v165\n\t" \
  "v_rcp_f32 v164, v165\n\t" \
  "v_mul_f32 v165, 0x4f000000, v165\n\t" \
  "v_mul_f32 v164, 0x47800000, v164\n\t" \
  "v_cvt_u32_f32 v165, v165\n\t" \
  "v_cvt_u32_f32 v164, v164\n\t" \
  "s_nop 0\n\t" \
  "v_readfirstlane_b32 s45, v165\n\t" \
  "v_readfirstlane_b32 s44, v164\n\t" \
  "21:\n\t" \
  "s_mov_b32 s41, s79\n\t" \
  "s_mov_b32 s43, s80\n\t" \
  "s_lshl_b32 s83, s43, 8\n\t" \
  "s_mov_b32 s84, 650\n\t" \
  "s_mov_b32 exec_hi, 0\n\t" \
  "s_waitcnt vmcnt(0)\n\t" \
  "v_cmp_ne_u32_sdwa vcc, v184, s85 src0_sel:BYTE_0 src1_sel:DWORD\n\t" \
  "v_subrev_u32 v184, s83, v184\n\t" \
  "v_ashrrev_i32 v184, 8, v184\n\t" \
  "s_cbranch_vccnz 22f\n\t" \
  "s_nop 0\n\t" \
  "v_max_i32_dpp v184, v184, v184 quad_perm:[1,0,3,2] row_mask:0xf bank_mask:0xf\n\t" \
  "s_nop 1\n\t" \
  "v_max_i32_dpp v184, v184, v184 quad_perm:[2,3,0,1] row_mask:0xf bank_mask:0xf\n\t" \
  "s_nop 1\n\t" \
  "v_max_i32_dpp v184, v184, v184 row_half_mirror row_mask:0xf bank_mask:0xf\n\t" \
  "s_nop 1\n\t" \
  "v_max_i32_dpp v184, v184, v184 row_mirror row_mask:0xf bank_mask:0xf\n\t" \
  "s_nop 0\n\t" \
  "v_readlane_b32 s81, v184, 0\n\t" \
  "v_readlane_b32 s82, v184, 16\n\t" \
  "s_max_i32 s81, s81, s82\n\t" \
  "s_add_i32 s81, s81, 1056\n\t" \
  "s_max_i32 s81, s81, 0\n\t" \
  "s_min_i32 s81, s81, 20000\n\t" \
  "s_mul_i32 s81, s81, s44\n\t" \
  "s_lshr_b32 s81, s81, 16\n\t" \
  "24:\n\t" \
  "s_add_i32 s81, s81, s41\n\t" \
  "v_min_u32 v185, 1760, v185\n\t" \
  "s_nop 1\n\t" \
  "v_max_i32_dpp v185, v185, v185 quad_perm:[1,0,3,2] row_mask:0xf bank_mask:0xf\n\t" \
  "s_nop 1\n\t" \
  "v_max_i32_dpp v185, v185, v185 quad_perm:[2,3,0,1] row_mask:0xf bank_mask:0xf\n\t" \
  "s_nop 1\n\t" \
  "v_max_i32_dpp v185, v185, v185 row_half_mirror row_mask:0xf bank_mask:0xf\n\t" \
  "s_nop 1\n\t" \
  "v_max_i32_dpp v185, v185, v185 row_mirror row_mask:0xf bank_mask:0xf\n\t" \
  "s_nop 0\n\t" \
  "v_readlane_b32 s54, v185, 0\n\t" \
  "v_readlane_b32 s82, v185, 16\n\t" \
  "s_max_i32 s54, s54, s82\n\t" \
  "s_add_i32 s54, s54, 245\n\t" \
  "1:\n\t" \
  "s_memtime vcc\n\t" \
  "s_waitcnt lgkmcnt(0)\n\t" \
  "s_sub_i32 s79, s81, vcc_lo\n\t" \
  "s_cmp_gt_i32 s79, 128\n\t" \
  "s_cbranch_scc1 1b\n\t" \
  ".p2align 4\n\t" \
  "2:\n\t" \
  "s_sub_i32 s79, s79, 33\n\t" \
  "s_cmp_gt_i32 s79, 56\n\t" \
  "s_cbranch_scc1 2b\n\t" \
  "buffer_load_dword v187, v200, %[rh], s78 offen sc1\n\t" \
  "s_memtime vcc\n\t" \
  "s_memrealtime s[86:87]\n\t" \
  "s_nop 4\n\t" \
  "s_memrealtime s[60:61]\n\t" \
  "s_nop 4\n\t" \
  "s_memrealtime s[62:63]\n\t" \
  "s_nop 4\n\t" \
  "s_memrealtime s[64:65]\n\t" \
  "s_nop 4\n\t" \
  "s_memrealtime s[66:67]\n\t" \
  "s_nop 4\n\t" \
  "s_memrealtime s[68:69]\n\t" \
  "s_mov_b32 s79, 0\n\t" \
  "s_waitcnt lgkmcnt(0)\n\t" \
  "s_cmp_eq_u32 s86, s60\n\t" \
  "s_addc_u32 s79, s79, 0\n\t" \
  "s_cmp_eq_u32 s86, s62\n\t" \
  "s_addc_u32 s79, s79, 0\n\t" \
  "s_cmp_eq_u32 s86, s64\n\t" \
  "s_addc_u32 s79, s79, 0\n\t" \
  "s_cmp_eq_u32 s86, s66\n\t" \
  "s_addc_u32 s79, s79, 0\n\t" \
  "s_cmp_eq_u32 s86, s68\n\t" \
  "s_addc_u32 s79, s79, 0\n\t" \
  "s_mul_i32 s79, s79, 24\n\t" \
  "s_add_i32 s79, s79, vcc_lo\n\t" \
  "s_lshl4_add_u32 s80, s86, 64\n\t" \
  "s_mov_b32 s41, s79\n\t" \
  "s_mov_b32 s43, s80\n\t" \
  "s_lshl_b32 s83, s43, 8\n\t" \
  "s_add_i32 s54, s54, s43\n\t" \
  "13:\n\t" \
  "s_waitcnt vmcnt(0)\n\t" \
  "s_memtime s[86:87]\n\t" \
  "v_cmp_ne_u32_sdwa vcc, v187, s40 src0_sel:BYTE_0 src1_sel:DWORD\n\t" \
  "v_subrev_u32 v184, s83, v187\n\t" \
  "v_ashrrev_i32 v184, 8, v184\n\t" \
  "s_nop 1\n\t" \
  "v_max_i32_dpp v184, v184, v184 quad_perm:[1,0,3,2] row_mask:0xf bank_mask:0xf\n\t" \
  "s_nop 1\n\t" \
  "v_max_i32_dpp v184, v184, v184 quad_perm:[2,3,0,1] row_mask:0xf bank_mask:0xf\n\t" \
  "s_cbranch_vccnz 14f\n\t" \
  "15:\n\t" \
  "s_nop 0\n\t" \
  "v_max_i32_dpp v184, v184, v184 row_half_mirror row_mask:0xf bank_mask:0xf\n\t" \
  "s_nop 1\n\t" \
  "v_max_i32_dpp v184, v184, v184 row_mirror row_mask:0xf bank_mask:0xf\n\t" \
  "s_mov_b64 exec, -1\n\t" \
  "v_readlane_b32 s57, v184, 0\n\t" \
  "v_readlane_b32 s82, v184, 16\n\t" \
  "s_waitcnt lgkmcnt(0)\n\t" \
  "s_sub_i32 s79, s41, s86\n\t" \
  "s_max_i32 s57, s57, s82\n\t" \
  "s_add_i32 s81, s57, 200\n\t" \
  "s_max_i32 s81, s81, 0\n\t" \
  "s_min_i32 s81, s81, 20000\n\t" \
  "s_mul_i32 s81, s81, s44\n\t" \
  "s_lshr_b32 s81, s81, 16\n\t" \
  "s_add_i32 s79, s79, s81\n\t" \
  "s_sub_i32 s85, s84, s79\n\t" \
  "s_max_i32 s85, s85, 0\n\t" \
  "s_mul_hi_u32 s85, s85, s45\n\t" \
  "s_lshl1_add_u32 s85, s85, s57\n\t" \
  "s_add_i32 s85, s85, s54\n\t" \
  "s_lshl_b32 s85, s85, 8\n\t" \
  "s_or_b32 s85, s85, s40\n\t" \
  "v_mov_b32 v184, s85\n\t" \
  "buffer_store_dword v184, v202, %[rh], s98 offen sc1\n\t" \
  "s_add_i32 s79, s79, -640\n\t" \
  ".p2align 4\n\t" \
  "3:\n\t" \
  "s_sub_i32 s79, s79, 33\n\t" \
  "s_cmp_gt_i32 s79, 56\n\t" \
  "s_cbranch_scc1 3b\n\t" \
  "buffer_load_dwordx4 v[0:3], v193, %[rh], s77 offen offset:0 sc1\n\t" \
  "s_nop 4\n\t" \
  "buffer_load_dwordx4 v[4:7], v193, %[rh], s77 offen offset:1024 sc1\n\t" \
  "s_nop 4\n\t" \
  "buffer_load_dwordx4 v[8:11], v193, %[rh], s77 offen offset:2048 sc1\n\t" \
  "s_nop 4\n\t" \
  "buffer_load_dwordx4 v[12:15], v193, %[rh], s77 offen offset:3072 sc1\n\t" \
  "s_nop 4\n\t" \
  "buffer_load_dwordx4 v[16:19], v193, %[rh], s96 offen offset:0 sc1\n\t" \
  "s_nop 4\n\t" \
  "buffer_load_dwordx4 v[20:23], v193, %[rh], s96 offen offset:1024 sc1\n\t" \
  "s_nop 4\n\t" \
  "buffer_load_dwordx4 v[24:27], v193, %[rh], s96 offen offset:2048 sc1\n\t" \
  "s_nop 4\n\t" \
  "buffer_load_dwordx4 v[28:31], v193, %[rh], s96 offen offset:3072 sc1\n\t" \
  "s_add_i32 s79, s79, 272\n\t" \
  ".p2align 4\n\t" \
  "4:\n\t" \
  "s_sub_i32 s79, s79, 33\n\t" \
  "s_cmp_gt_i32 s79, 56\n\t" \
  "s_cbranch_scc1 4b\n\t" \
  "buffer_load_dwordx4 v[32:35], v195, %[rh], s77 offen offset:0 sc1\n\t" \
  "buffer_load_dwordx4 v[36:39], v195, %[rh], s77 offen offset:2048 sc1\n\t" \
  "10:\n\t" \
  "s_waitcnt vmcnt(9)\n\t" \
  "v_smfmac_f32_16x16x64_bf16 v[40:43], v[0:3], a[0:7], v203\n\t" \
  "v_smfmac_f32_16x16x64_bf16 v[56:59], v[0:3], a[8:15], v203\n\t" \
  "s_waitcnt vmcnt(8)\n\t" \
  "v_smfmac_f32_16x16x64_bf16 v[44:47], v[4:7], a[16:23], v203\n\t" \
  "v_cvt_pk_bf16_f32 v40, v40, v41\n\t" \
  "v_cvt_pk_bf16_f32 v41, v42, v43\n\t" \
  "v_smfmac_f32_16x16x64_bf16 v[60:63], v[4:7], a[24:31], v203\n\t" \
  "v_cvt_pk_bf16_f32 v56, v56, v57\n\t" \
  "v_cvt_pk_bf16_f32 v57, v58, v59\n\t" \
  "s_waitcnt vmcnt(7)\n\t" \
  "v_smfmac_f32_16x16x64_bf16 v[48:51], v[8:11], a[32:39], v203\n\t" \
  "v_cvt_pk_bf16_f32 v42, v44, v45\n\t" \
  "v_cvt_pk_bf16_f32 v43, v46, v47\n\t" \
  "v_smfmac_f32_16x16x64_bf16 v[64:67], v[8:11], a[40:47], v203\n\t" \
  "v_cvt_pk_bf16_f32 v58, v60, v61\n\t" \
  "v_cvt_pk_bf16_f32 v59, v62, v63\n\t" \
  "s_waitcnt vmcnt(6)\n\t" \
  "v_smfmac_f32_16x16x64_bf16 v[52:55], v[12:15], a[48:55], v203\n\t" \
  "v_cvt_pk_bf16_f32 v44, v48, v49\n\t" \
  "v_cvt_pk_bf16_f32 v45, v50, v51\n\t" \
  "v_smfmac_f32_16x16x64_bf16 v[68:71], v[12:15], a[56:63], v203\n\t" \
  "v_cvt_pk_bf16_f32 v60, v64, v65\n\t" \
  "v_cvt_pk_bf16_f32 v61, v66, v67\n\t" \
  "s_waitcnt vmcnt(5)\n\t" \
  "v_smfmac_f32_16x16x64_bf16 v[72:75], v[16:19], a[64:71], v203\n\t" \
  "v_cvt_pk_bf16_f32 v46, v52, v53\n\t" \
  "v_cvt_pk_bf16_f32 v47, v54, v55\n\t" \
  "v_smfmac_f32_16x16x64_bf16 v[88:91], v[16:19], a[72:79], v203\n\t" \
  "v_cvt_pk_bf16_f32 v62, v68, v69\n\t" \
  "v_cvt_pk_bf16_f32 v63, v70, v71\n\t" \
  "s_waitcnt vmcnt(4)\n\t" \
  "v_smfmac_f32_16x16x64_bf16 v[76:79], v[20:23], a[80:87], v203\n\t" \
  "v_cvt_pk_bf16_f32 v72, v72, v73\n\t" \
  "v_cvt_pk_bf16_f32 v73, v74, v75\n\t" \
  "v_smfmac_f32_16x16x64_bf16 v[92:95], v[20:23], a[88:95], v203\n\t" \
  "v_cvt_pk_bf16_f32 v88, v88, v89\n\t" \
  "v_cvt_pk_bf16_f32 v89, v90, v91\n\t" \
  "s_waitcnt vmcnt(3)\n\t" \
  "v_smfmac_f32_16x16x64_bf16 v[80:83], v[24:27], a[96:103], v203\n\t" \
  "v_cvt_pk_bf16_f32 v74, v76, v77\n\t" \
  "v_cvt_pk_bf16_f32 v75, v78, v79\n\t" \
  "v_smfmac_f32_16x16x64_bf16 v[96:99], v[24:27], a[104:111], v203\n\t" \
  "v_cvt_pk_bf16_f32 v90, v92, v93\n\t" \
  "v_cvt_pk_bf16_f32 v91, v94, v95\n\t" \
  "s_waitcnt vmcnt(2)\n\t" \
  "v_smfmac_f32_16x16x64_bf16 v[84:87], v[28:31], a[112:119], v203\n\t" \
  "v_cvt_pk_bf16_f32 v76, v80, v81\n\t" \
  "v_cvt_pk_bf16_f32 v77, v82, v83\n\t" \
  "v_smfmac_f32_16x16x64_bf16 v[100:103], v[28:31], a[120:127], v203\n\t" \
  "v_cvt_pk_bf16_f32 v92, v96, v97\n\t" \
  "v_cvt_pk_bf16_f32 v93, v98, v99\n\t" \
  "s_waitcnt vmcnt(1)\n\t" \
  "v_smfmac_f32_16x16x64_bf16 v[136:139], v[32:35], v[40:47], v204\n\t" \
  "v_cvt_pk_bf16_f32 v78, v84, v85\n\t" \
  "v_cvt_pk_bf16_f32 v79, v86, v87\n\t" \
  "v_smfmac_f32_16x16x64_bf16 v[140:143], v[32:35], v[56:63], v204\n\t" \
  "v_cvt_pk_bf16_f32 v94, v100, v101\n\t" \
  "v_cvt_pk_bf16_f32 v95, v102, v103\n\t" \
  "s_waitcnt vmcnt(0)\n\t" \
  "v_smfmac_f32_16x16x64_bf16 v[136:139], v[36:39], v[72:79], v204\n\t" \
  "v_smfmac_f32_16x16x64_bf16 v[140:143], v[36:39], v[88:95], v204\n\t" \
  "s_nop 3\n\t" \
  "v_permlane32_swap_b32 v136, v138\n\t" \
  "v_permlane32_swap_b32 v137, v139\n\t" \
  "v_add_f32 v136, v136, v138\n\t" \
  "v_add_f32 v137, v137, v139\n\t" \
  "v_permlane32_swap_b32 v140, v142\n\t" \
  "v_permlane32_swap_b32 v141, v143\n\t" \
  "v_add_f32 v138, v140, v142\n\t" \
  "v_add_f32 v139, v141, v143\n\t" \
  "ds_write2st64_b64 v197, v[136:137], v[138:139] offset1:2\n\t" \
  "s_waitcnt lgkmcnt(0)\n\t" \
  "s_barrier\n\t" \
  "s_barrier\n\t" \
  "ds_read2_b32 v[148:149], v207 offset1:8\n\t" \
  "s_waitcnt lgkmcnt(0)\n\t" \
  "v_or_b32 v148, v148, v149\n\t" \
  "v_cmp_ne_u32 vcc, 0, v148\n\t" \
  "s_cbranch_vccnz 12f\n\t" \
  "s_and_b32 s58, s58, 4\n\t" \
  "s_cmp_ge_i32 s40, 62\n\t" \
  "s_cbranch_scc1 26f\n\t" \
  "s_and_b32 s82, s40, 3\n\t" \
  "s_lshl_b32 s82, s82, 20\n\t" \
  "s_add_i32 s82, s82, s48\n\t" \
  "s_andn2_b32 s82, s82, 0x1800\n\t" \
  "s_lshr_b32 s80, s82, 21\n\t" \
  "s_bitcmp1_b64 %[sel], s80\n\t" \
  "s_cselect_b32 s80, 0x2000, 0\n\t" \
  "s_add_i32 s82, s82, s80\n\t" \
  "s_add_i32 s80, s82, 0x1000\n\t" \
  "v_mov_b64 v[188:189], -1\n\t" \
  "v_mov_b64 v[190:191], -1\n\t" \
  "s_sleep 8\n\t" \
  "buffer_store_dwordx4 v[188:191], v192, %[ru], s82 offen offset:0 sc1\n\t" \
  "buffer_store_dwordx4 v[188:191], v192, %[ru], s82 offen offset:1024 sc1\n\t" \
  "buffer_store_dwordx4 v[188:191], v192, %[ru], s82 offen offset:2048 sc1\n\t" \
  "buffer_store_dwordx4 v[188:191], v192, %[ru], s82 offen offset:3072 sc1\n\t" \
  "buffer_store_dwordx4 v[188:191], v192, %[ru], s80 offen offset:0 sc1\n\t" \
  "buffer_store_dwordx4 v[188:191], v192, %[ru], s80 offen offset:1024 sc1\n\t" \
  "buffer_store_dwordx4 v[188:191], v192, %[ru], s80 offen offset:2048 sc1\n\t" \
  "buffer_store_dwordx4 v[188:191], v192, %[ru], s80 offen offset:3072 sc1\n\t" \
  "26:\n\t" \
  "s_barrier\n\t" \
  "s_and_b32 s76, s40, 3\n\t" \
  "s_lshl_b32 s76, s76, 18\n\t" \
  "s_add_i32 s77, s76, 0x10000\n\t" \
  "s_add_i32 s78, s76, 0x1000\n\t" \
  "s_add_i32 s79, s76, 0x11000\n\t" \
  "31:\n\t" \
  "buffer_load_dwordx4 v[0:3], v210, %[rh], s76 offen offset:0 sc1\n\t" \
  "s_nop 10\n\t" \
  "buffer_load_dwordx4 v[4:7], v210, %[rh], s76 offen offset:1024 sc1\n\t" \
  "s_nop 10\n\t" \
  "buffer_load_dwordx4 v[8:11], v210, %[rh], s77 offen offset:0 sc1\n\t" \
  "s_nop 10\n\t" \
  "buffer_load_dwordx4 v[12:15], v210, %[rh], s77 offen offset:1024 sc1\n\t" \
  "s_nop 10\n\t" \
  "buffer_load_dwordx4 v[16:19], v210, %[rh], s76 offen offset:2048 sc1\n\t" \
  "s_nop 10\n\t" \
  "buffer_load_dwordx4 v[20:23], v210, %[rh], s76 offen offset:3072 sc1\n\t" \
  "s_nop 10\n\t" \
  "buffer_load_dwordx4 v[24:27], v210, %[rh], s77 offen offset:2048 sc1\n\t" \
  "s_nop 10\n\t" \
  "buffer_load_dwordx4 v[28:31], v210, %[rh], s77 offen offset:3072 sc1\n\t" \
  "s_nop 10\n\t" \
  "buffer_load_dwordx4 v[32:35], v210, %[rh], s78 offen offset:0 sc1\n\t" \
  "s_nop 10\n\t" \
  "buffer_load_dwordx4 v[36:39], v210, %[rh], s78 offen offset:1024 sc1\n\t" \
  "s_nop 10\n\t" \
  "buffer_load_dwordx4 v[40:43], v210, %[rh], s79 offen offset:0 sc1\n\t" \
  "s_nop 10\n\t" \
  "buffer_load_dwordx4 v[44:47], v210, %[rh], s79 offen offset:1024 sc1\n\t" \
  "s_nop 10\n\t" \
  "buffer_load_dwordx4 v[48:51], v210, %[rh], s78 offen offset:2048 sc1\n\t" \
  "s_nop 10\n\t" \
  "buffer_load_dwordx4 v[52:55], v210, %[rh], s78 offen offset:3072 sc1\n\t" \
  "s_nop 10\n\t" \
  "buffer_load_dwordx4 v[56:59], v210, %[rh], s79 offen offset:2048 sc1\n\t" \
  "s_nop 10\n\t" \
  "buffer_load_dwordx4 v[60:63], v210, %[rh], s79 offen offset:3072 sc1\n\t" \
  "s_nop 10\n\t" \
  "buffer_load_dwordx4 v[64:67], v211, %[rh], s76 offen offset:0 sc1\n\t" \
  "s_nop 10\n\t" \
  "buffer_load_dwordx4 v[68:71], v211, %[rh], s76 offen offset:1024 sc1\n\t" \
  "s_nop 10\n\t" \
  "buffer_load_dwordx4 v[72:75], v211, %[rh], s77 offen offset:0 sc1\n\t" \
  "s_nop 10\n\t" \
  "buffer_load_dwordx4 v[76:79], v211, %[rh], s77 offen offset:1024 sc1\n\t" \
  "s_nop 10\n\t" \
  "buffer_load_dwordx4 v[80:83], v211, %[rh], s76 offen offset:2048 sc1\n\t" \
  "s_nop 10\n\t" \
  "buffer_load_dwordx4 v[84:87], v211, %[rh], s76 offen offset:3072 sc1\n\t" \
  "s_nop 10\n\t" \
  "v_mov_b64 v[136:137], 0\n\t" \
  "v_mov_b64 v[138:139], 0\n\t" \
  "v_mov_b64 v[140:141], 0\n\t" \
  "v_mov_b64 v[142:143], 0\n\t" \
  "s_waitcnt vmcnt(20)\n\t" \
  "v_swap_b32 v1, v4\n\t" \
  "v_swap_b32 v3, v6\n\t" \
  "s_nop 1\n\t" \
  "v_mfma_f32_16x16x32_bf16 v[168:171], a[0:3], v[0:3], 0\n\t" \
  "buffer_load_dwordx4 v[88:91], v211, %[rh], s77 offen offset:2048 sc1\n\t" \
  "v_mfma_f32_16x16x32_bf16 v[172:175], a[8:11], v[0:3], 0\n\t" \
  "s_waitcnt vmcnt(19)\n\t" \
  "v_swap_b32 v9, v12\n\t" \
  "v_mfma_f32_16x16x32_bf16 v[168:171], a[4:7], v[4:7], v[168:171]\n\t" \
  "v_swap_b32 v11, v14\n\t" \
  "v_mfma_f32_16x16x32_bf16 v[172:175], a[12:15], v[4:7], v[172:175]\n\t" \
  "buffer_load_dwordx4 v[92:95], v211, %[rh], s77 offen offset:3072 sc1\n\t" \
  "v_mfma_f32_16x16x32_bf16 v[176:179], a[0:3], v[8:11], 0\n\t" \
  "buffer_load_dwordx4 v[96:99], v211, %[rh], s78 offen offset:0 sc1\n\t" \
  "v_mfma_f32_16x16x32_bf16 v[180:183], a[8:11], v[8:11], 0\n\t" \
  "s_waitcnt vmcnt(19)\n\t" \
  "v_swap_b32 v17, v20\n\t" \
  "v_mfma_f32_16x16x32_bf16 v[176:179], a[4:7], v[12:15], v[176:179]\n\t" \
  "v_swap_b32 v19, v22\n\t" \
  "v_mfma_f32_16x16x32_bf16 v[180:183], a[12:15], v[12:15], v[180:183]\n\t" \
  "buffer_load_dwordx4 v[100:103], v211, %[rh], s78 offen offset:1024 sc1\n\t" \
  "v_mfma_f32_16x16x32_bf16 v[168:171], a[16:19], v[16:19], v[168:171]\n\t" \
  "buffer_load_dwordx4 v[104:107], v211, %[rh], s79 offen offset:0 sc1\n\t" \
  "v_mfma_f32_16x16x32_bf16 v[172:175], a[24:27], v[16:19], v[172:175]\n\t" \
  "s_waitcnt vmcnt(19)\n\t" \
  "v_swap_b32 v25, v28\n\t" \
  "v_mfma_f32_16x16x32_bf16 v[168:171], a[20:23], v[20:23], v[168:171]\n\t" \
  "v_swap_b32 v27, v30\n\t" \
  "v_mfma_f32_16x16x32_bf16 v[172:175], a[28:31], v[20:23], v[172:175]\n\t" \
  "buffer_load_dwordx4 v[108:111], v211, %[rh], s79 offen offset:1024 sc1\n\t" \
  "v_mfma_f32_16x16x32_bf16 v[176:179], a[16:19], v[24:27], v[176:179]\n\t" \
  "buffer_load_dwordx4 v[112:115], v211, %[rh], s78 offen offset:2048 sc1\n\t" \
  "v_mfma_f32_16x16x32_bf16 v[180:183], a[24:27], v[24:27], v[180:183]\n\t" \
  "s_waitcnt vmcnt(19)\n\t" \
  "v_swap_b32 v33, v36\n\t" \
  "v_mfma_f32_16x16x32_bf16 v[176:179], a[20:23], v[28:31], v[176:179]\n\t" \
  "v_swap_b32 v35, v38\n\t" \
  "v_mfma_f32_16x16x32_bf16 v[180:183], a[28:31], v[28:31], v[180:183]\n\t" \
  "buffer_load_dwordx4 v[116:119], v211, %[rh], s78 offen offset:3072 sc1\n\t" \
  "v_mfma_f32_16x16x32_bf16 v[168:171], a[32:35], v[32:35], v[168:171]\n\t" \
  "buffer_load_dwordx4 v[120:123], v211, %[rh], s79 offen offset:2048 sc1\n\t" \
  "v_mfma_f32_16x16x32_bf16 v[172:175], a[40:43], v[32:35], v[172:175]\n\t" \
  "s_waitcnt vmcnt(19)\n\t" \
  "v_swap_b32 v41, v44\n\t" \
  "v_mfma_f32_16x16x32_bf16 v[168:171], a[36:39], v[36:39], v[168:171]\n\t" \
  "v_swap_b32 v43, v46\n\t" \
  "v_mfma_f32_16x16x32_bf16 v[172:175], a[44:47], v[36:39], v[172:175]\n\t" \
  "buffer_load_dwordx4 v[124:127], v211, %[rh], s79 offen offset:3072 sc1\n\t" \
  "v_mfma_f32_16x16x32_bf16 v[176:179], a[32:35], v[40:43], v[176:179]\n\t" \
  "v_mfma_f32_16x16x32_bf16 v[180:183], a[40:43], v[40:43], v[180:183]\n\t" \
  "s_waitcnt vmcnt(18)\n\t" \
  "v_swap_b32 v49, v52\n\t" \
  "v_mfma_f32_16x16x32_bf16 v[176:179], a[36:39], v[44:47], v[176:179]\n\t" \
  "v_swap_b32 v51, v54\n\t" \
  "v_mfma_f32_16x16x32_bf16 v[180:183], a[44:47], v[44:47], v[180:183]\n\t" \
  "v_mov_b64 v[40:41], 0\n\t" \
  "v_mov_b64 v[42:43], 0\n\t" \
  "v_mfma_f32_16x16x32_bf16 v[168:171], a[48:51], v[48:51], v[168:171]\n\t" \
  "v_mov_b64 v[44:45], 0\n\t" \
  "v_mov_b64 v[46:47], 0\n\t" \
  "v_mfma_f32_16x16x32_bf16 v[172:175], a[56:59], v[48:51], v[172:175]\n\t" \
  "s_waitcnt vmcnt(16)\n\t" \
  "v_swap_b32 v57, v60\n\t" \
  "v_mfma_f32_16x16x32_bf16 v[168:171], a[52:55], v[52:55], v[168:171]\n\t" \
  "v_swap_b32 v59, v62\n\t" \
  "v_mfma_f32_16x16x32_bf16 v[172:175], a[60:63], v[52:55], v[172:175]\n\t" \
  "v_mov_b64 v[48:49], 0\n\t" \
  "v_mov_b64 v[50:51], 0\n\t" \
  "v_mfma_f32_16x16x32_bf16 v[176:179], a[48:51], v[56:59], v[176:179]\n\t" \
  "v_mov_b64 v[52:53], 0\n\t" \
  "v_mov_b64 v[54:55], 0\n\t" \
  "v_mfma_f32_16x16x32_bf16 v[180:183], a[56:59], v[56:59], v[180:183]\n\t" \
  "s_waitcnt vmcnt(14)\n\t" \
  "v_swap_b32 v65, v68\n\t" \
  "v_mfma_f32_16x16x32_bf16 v[176:179], a[52:55], v[60:63], v[176:179]\n\t" \
  "v_swap_b32 v67, v70\n\t" \
  "v_mfma_f32_16x16x32_bf16 v[180:183], a[60:63], v[60:63], v[180:183]\n\t" \
  "v_mov_b64 v[56:57], 0\n\t" \
  "v_mov_b64 v[58:59], 0\n\t" \
  "v_mfma_f32_16x16x32_bf16 v[168:171], a[64:67], v[64:67], v[168:171]\n\t" \
  "v_mov_b64 v[60:61], 0\n\t" \
  "v_mov_b64 v[62:63], 0\n\t" \
  "v_mfma_f32_16x16x32_bf16 v[172:175], a[72:75], v[64:67], v[172:175]\n\t" \
  "s_waitcnt vmcnt(12)\n\t" \
  "v_swap_b32 v73, v76\n\t" \
  "v_mfma_f32_16x16x32_bf16 v[168:171], a[68:71], v[68:71], v[168:171]\n\t" \
  "v_swap_b32 v75, v78\n\t" \
  "v_mfma_f32_16x16x32_bf16 v[172:175], a[76:79], v[68:71], v[172:175]\n\t" \
  "v_mov_b64 v[64:65], 0\n\t" \
  "v_mov_b64 v[66:67], 0\n\t" \
  "v_mfma_f32_16x16x32_bf16 v[176:179], a[64:67], v[72:75], v[176:179]\n\t" \
  "v_mov_b64 v[68:69], 0\n\t" \
  "v_mov_b64 v[70:71], 0\n\t" \
  "v_mfma_f32_16x16x32_bf16 v[180:183], a[72:75], v[72:75], v[180:183]\n\t" \
  "s_waitcnt vmcnt(10)\n\t" \
  "v_swap_b32 v81, v84\n\t" \
  "v_mfma_f32_16x16x32_bf16 v[176:179], a[68:71], v[76:79], v[176:179]\n\t" \
  "v_swap_b32 v83, v86\n\t" \
  "v_mfma_f32_16x16x32_bf16 v[180:183], a[76:79], v[76:79], v[180:183]\n\t" \
  "v_mov_b64 v[72:73], 0\n\t" \
  "v_mov_b64 v[74:75], 0\n\t" \
  "v_mfma_f32_16x16x32_bf16 v[168:171], a[80:83], v[80:83], v[168:171]\n\t" \
  "v_mov_b64 v[76:77], 0\n\t" \
  "v_mov_b64 v[78:79], 0\n\t" \
  "v_mfma_f32_16x16x32_bf16 v[172:175], a[88:91], v[80:83], v[172:175]\n\t" \
  "s_waitcnt vmcnt(8)\n\t" \
  "v_swap_b32 v89, v92\n\t" \
  "v_mfma_f32_16x16x32_bf16 v[168:171], a[84:87], v[84:87], v[168:171]\n\t" \
  "v_swap_b32 v91, v94\n\t" \
  "v_mfma_f32_16x16x32_bf16 v[172:175], a[92:95], v[84:87], v[172:175]\n\t" \
  "v_mov_b64 v[80:81], 0\n\t" \
  "v_mov_b64 v[82:83], 0\n\t" \
  "v_mfma_f32_16x16x32_bf16 v[176:179], a[80:83], v[88:91], v[176:179]\n\t" \
  "v_mov_b64 v[84:85], 0\n\t" \
  "v_mov_b64 v[86:87], 0\n\t" \
  "v_mfma_f32_16x16x32_bf16 v[180:183], a[88:91], v[88:91], v[180:183]\n\t" \
  "s_waitcnt vmcnt(6)\n\t" \
  "v_swap_b32 v97, v100\n\t" \
  "v_mfma_f32_16x16x32_bf16 v[176:179], a[84:87], v[92:95], v[176:179]\n\t" \
  "v_swap_b32 v99, v102\n\t" \
  "v_mfma_f32_16x16x32_bf16 v[180:183], a[92:95], v[92:95], v[180:183]\n\t" \
  "v_mov_b64 v[88:89], 0\n\t" \
  "v_mov_b64 v[90:91], 0\n\t" \
  "v_mfma_f32_16x16x32_bf16 v[168:171], a[96:99], v[96:99], v[168:171]\n\t" \
  "v_mov_b64 v[92:93], 0\n\t" \
  "v_mov_b64 v[94:95], 0\n\t" \
  "v_mfma_f32_16x16x32_bf16 v[172:175], a[104:107], v[96:99], v[172:175]\n\t" \
  "s_waitcnt vmcnt(4)\n\t" \
  "v_swap_b32 v105, v108\n\t" \
  "v_mfma_f32_16x16x32_bf16 v[168:171], a[100:103], v[100:103], v[168:171]\n\t" \
  "v_swap_b32 v107, v110\n\t" \
  "v_mfma_f32_16x16x32_bf16 v[172:175], a[108:111], v[100:103], v[172:175]\n\t" \
  "v_mov_b64 v[96:97], 0\n\t" \
  "v_mov_b64 v[98:99], 0\n\t" \
  "v_mfma_f32_16x16x32_bf16 v[176:179], a[96:99], v[104:107], v[176:179]\n\t" \
  "v_mov_b64 v[100:101], 0\n\t" \
  "v_mov_b64 v[102:103], 0\n\t" \
  "v_mfma_f32_16x16x32_bf16 v[180:183], a[104:107], v[104:107], v[180:183]\n\t" \
  "s_waitcnt vmcnt(2)\n\t" \
  "v_swap_b32 v113, v116\n\t" \
  "v_mfma_f32_16x16x32_bf16 v[176:179], a[100:103], v[108:111], v[176:179]\n\t" \
  "v_swap_b32 v115, v118\n\t" \
  "v_mfma_f32_16x16x32_bf16 v[180:183], a[108:111], v[108:111], v[180:183]\n\t" \
  "s_nop 0\n\t" \
  "v_mfma_f32_16x16x32_bf16 v[168:171], a[112:115], v[112:115], v[168:171]\n\t" \
  "v_mfma_f32_16x16x32_bf16 v[172:175], a[120:123], v[112:115], v[172:175]\n\t" \
  "s_waitcnt vmcnt(0)\n\t" \
  "v_swap_b32 v121, v124\n\t" \
  "v_mfma_f32_16x16x32_bf16 v[168:171], a[116:119], v[116:119], v[168:171]\n\t" \
  "v_swap_b32 v123, v126\n\t" \
  "v_mfma_f32_16x16x32_bf16 v[172:175], a[124:127], v[116:119], v[172:175]\n\t" \
  "s_nop 0\n\t" \
  "v_mfma_f32_16x16x32_bf16 v[176:179], a[112:115], v[120:123], v[176:179]\n\t" \
  "v_mfma_f32_16x16x32_bf16 v[180:183], a[120:123], v[120:123], v[180:183]\n\t" \
  "v_mfma_f32_16x16x32_bf16 v[176:179], a[116:119], v[124:127], v[176:179]\n\t" \
  "v_mfma_f32_16x16x32_bf16 v[180:183], a[124:127], v[124:127], v[180:183]\n\t" \
  "s_nop 6\n\t" \
  "39:\n\t" \
  "s_add_i32 s40, s40, 2\n\t" \
  "v_cmp_u_f32 vcc, v168, v176\n\t" \
  "s_cbranch_vccnz 32f\n\t" \
  "33:\n\t" \
  "s_barrier\n\t" \
  "s_add_i32 s85, s40, -1\n\t" \
  "s_and_b32 s82, s85, 2\n\t" \
  "s_lshl_b32 s82, s82, 13\n\t" \
  "s_and_b32 s80, s85, 1\n\t" \
  "s_lshl_b32 s80, s80, 12\n\t" \
  "s_add_i32 s82, s82, s80\n\t" \
  "s_add_i32 s82, s82, 0x84000\n\t" \
  "s_mov_b32 exec_hi, 0\n\t" \
  "buffer_load_dword v184, v200, %[rh], s82 offen sc1\n\t" \
  "buffer_load_dword v185, v200, %[rh], s52 offen sc1\n\t" \
  "s_mov_b64 exec, -1\n\t" \
  "ds_read_b128 a[0:3], v194 offset:0\n\t" \
  "ds_read_b128 a[4:7], v194 offset:1024\n\t" \
  "ds_read_b128 a[8:11], v194 offset:2048\n\t" \
  "ds_read_b128 a[12:15], v194 offset:3072\n\t" \
  "ds_read_b128 a[16:19], v194 offset:4096\n\t" \
  "ds_read_b128 a[20:23], v194 offset:5120\n\t" \
  "ds_read_b128 a[24:27], v194 offset:6144\n\t" \
  "ds_read_b128 a[28:31], v194 offset:7168\n\t" \
  "ds_read_b128 a[32:35], v194 offset:8192\n\t" \
  "ds_read_b128 a[36:39], v194 offset:9216\n\t" \
  "ds_read_b128 a[40:43], v194 offset:10240\n\t" \
  "ds_read_b128 a[44:47], v194 offset:11264\n\t" \
  "ds_read_b128 a[48:51], v194 offset:12288\n\t" \
  "ds_read_b128 a[52:55], v194 offset:13312\n\t" \
  "ds_read_b128 a[56:59], v194 offset:14336\n\t" \
  "ds_read_b128 a[60:63], v194 offset:15360\n\t" \
  "ds_read_b128 a[64:67], v194 offset:16384\n\t" \
  "ds_read_b128 a[68:71], v194 offset:17408\n\t" \
  "ds_read_b128 a[72:75], v194 offset:18432\n\t" \
  "ds_read_b128 a[76:79], v194 offset:19456\n\t" \
  "ds_read_b128 a[80:83], v194 offset:20480\n\t" \
  "ds_read_b128 a[84:87], v194 offset:21504\n\t" \
  "ds_read_b128 a[88:91], v194 offset:22528\n\t" \
  "ds_read_b128 a[92:95], v194 offset:23552\n\t" \
  "ds_read_b128 a[96:99], v194 offset:24576\n\t" \
  "ds_read_b128 a[100:103], v194 offset:25600\n\t" \
  "ds_read_b128 a[104:107], v194 offset:26624\n\t" \
  "ds_read_b128 a[108:111], v194 offset:27648\n\t" \
  "ds_read_b128 a[112:115], v194 offset:28672\n\t" \
  "ds_read_b128 a[116:119], v194 offset:29696\n\t" \
  "ds_read_b128 a[120:123], v194 offset:30720\n\t" \
  "ds_read_b128 a[124:127], v194 offset:31744\n\t" \
  "ds_write_b128 v199, v[168:171] offset:0\n\t" \
  "ds_write_b128 v199, v[172:175] offset:1024\n\t" \
  "ds_write_b128 v199, v[176:179] offset:8192\n\t" \
  "ds_write_b128 v199, v[180:183] offset:9216\n\t" \
  "s_mov_b32 s59, 0\n\t" \
  "s_and_b32 s58, s58, 4\n\t" \
  "s_cmp_lt_i32 s40, 64\n\t" \
  "s_cbranch_scc1 20b\n\t" \
  "s_waitcnt lgkmcnt(0)\n\t" \
  "s_barrier\n\t" \
  "s_mov_b32 s58, 64\n\t" \
  "s_branch 7f\n\t" \
  "22:\n\t" \
  "s_mov_b32 s81, 0\n\t" \
  "s_cmp_eq_u32 s40, 0\n\t" \
  "s_cbranch_scc1 24b\n\t" \
  "s_bitcmp1_b32 s58, 2\n\t" \
  "s_cbranch_scc1 24b\n\t" \
  "s_mov_b64 exec, -1\n\t" \
  "s_sleep 10\n\t" \
  "s_add_u32 s59, s59, 0x10000\n\t" \
  "s_cmp_lt_u32 s59, 0x100000\n\t" \
  "s_cbranch_scc1 19b\n\t" \
  "s_or_b32 s58, s58, 1\n\t" \
  "s_branch 7f\n\t" \
  "14:\n\t" \
  "v_mov_b32 v184, 0\n\t" \
  "s_bitcmp1_b32 s58, 2\n\t" \
  "s_nop 0\n\t" \
  "s_cbranch_scc1 15b\n\t" \
  "s_mov_b32 s84, 950\n\t" \
  "s_add_u32 s59, s59, 0x10000\n\t" \
  "s_cmp_lt_u32 s59, 0x100000\n\t" \
  "s_mov_b64 exec, vcc\n\t" \
  "buffer_load_dword v187, v200, %[rh], s78 offen sc1\n\t" \
  "s_mov_b32 exec_lo, -1\n\t" \
  "s_cbranch_scc1 13b\n\t" \
  "s_mov_b64 exec, -1\n\t" \
  "s_or_b32 s58, s58, 1\n\t" \
  "s_branch 7f\n\t" \
  "37:\n\t" \
  "s_barrier\n\t" \
  "v_mov_b64 v[40:41], 0\n\t" \
  "v_mov_b64 v[42:43], 0\n\t" \
  "v_mov_b64 v[44:45], 0\n\t" \
  "v_mov_b64 v[46:47], 0\n\t" \
  "v_mov_b64 v[48:49], 0\n\t" \
  "v_mov_b64 v[50:51], 0\n\t" \
  "v_mov_b64 v[52:53], 0\n\t" \
  "v_mov_b64 v[54:55], 0\n\t" \
  "v_mov_b64 v[56:57], 0\n\t" \
  "v_mov_b64 v[58:59], 0\n\t" \
  "v_mov_b64 v[60:61], 0\n\t" \
  "v_mov_b64 v[62:63], 0\n\t" \
  "v_mov_b64 v[64:65], 0\n\t" \
  "v_mov_b64 v[66:67], 0\n\t" \
  "v_mov_b64 v[68:69], 0\n\t" \
  "v_mov_b64 v[70:71], 0\n\t" \
  "v_mov_b64 v[72:73], 0\n\t" \
  "v_mov_b64 v[74:75], 0\n\t" \
  "v_mov_b64 v[76:77], 0\n\t" \
  "v_mov_b64 v[78:79], 0\n\t" \
  "v_mov_b64 v[80:81], 0\n\t" \
  "v_mov_b64 v[82:83], 0\n\t" \
  "v_mov_b64 v[84:85], 0\n\t" \
  "v_mov_b64 v[86:87], 0\n\t" \
  "v_mov_b64 v[88:89], 0\n\t" \
  "v_mov_b64 v[90:91], 0\n\t" \
  "v_mov_b64 v[92:93], 0\n\t" \
  "v_mov_b64 v[94:95], 0\n\t" \
  "v_mov_b64 v[96:97], 0\n\t" \
  "v_mov_b64 v[98:99], 0\n\t" \
  "v_mov_b64 v[100:101], 0\n\t" \
  "v_mov_b64 v[102:103], 0\n\t" \
  "v_mov_b64 v[136:137], 0\n\t" \
  "v_mov_b64 v[138:139], 0\n\t" \
  "v_mov_b64 v[140:141], 0\n\t" \
  "v_mov_b64 v[142:143], 0\n\t" \
  "s_add_i32 s40, s40, 2\n\t" \
  "s_branch 33b\n\t" \
  "32:\n\t" \
  "s_and_b32 s80, s58, 36\n\t" \
  "s_cbranch_scc1 33b\n\t" \
  "s_add_i32 s40, s40, -2\n\t" \
  "34:\n\t" \
  "s_or_b32 s59, s59, 128\n\t" \
  "s_and_b32 s76, s40, 3\n\t" \
  "s_lshl_b32 s76, s76, 18\n\t" \
  "s_add_i32 s77, s76, 0x10000\n\t" \
  "s_add_i32 s78, s76, 0x1000\n\t" \
  "s_add_i32 s79, s76, 0x11000\n\t" \
  "35:\n\t" \
  "buffer_load_dwordx4 v[0:3], v210, %[rh], s76 offen offset:0 sc1\n\t" \
  "buffer_load_dwordx4 v[4:7], v210, %[rh], s76 offen offset:1024 sc1\n\t" \
  "buffer_load_dwordx4 v[8:11], v210, %[rh], s77 offen offset:0 sc1\n\t" \
  "buffer_load_dwordx4 v[12:15], v210, %[rh], s77 offen offset:1024 sc1\n\t" \
  "buffer_load_dwordx4 v[16:19], v210, %[rh], s76 offen offset:2048 sc1\n\t" \
  "buffer_load_dwordx4 v[20:23], v210, %[rh], s76 offen offset:3072 sc1\n\t" \
  "buffer_load_dwordx4 v[24:27], v210, %[rh], s77 offen offset:2048 sc1\n\t" \
  "buffer_load_dwordx4 v[28:31], v210, %[rh], s77 offen offset:3072 sc1\n\t" \
  "buffer_load_dwordx4 v[32:35], v210, %[rh], s78 offen offset:0 sc1\n\t" \
  "buffer_load_dwordx4 v[36:39], v210, %[rh], s78 offen offset:1024 sc1\n\t" \
  "buffer_load_dwordx4 v[40:43], v210, %[rh], s79 offen offset:0 sc1\n\t" \
  "buffer_load_dwordx4 v[44:47], v210, %[rh], s79 offen offset:1024 sc1\n\t" \
  "buffer_load_dwordx4 v[48:51], v210, %[rh], s78 offen offset:2048 sc1\n\t" \
  "buffer_load_dwordx4 v[52:55], v210, %[rh], s78 offen offset:3072 sc1\n\t" \
  "buffer_load_dwordx4 v[56:59], v210, %[rh], s79 offen offset:2048 sc1\n\t" \
  "buffer_load_dwordx4 v[60:63], v210, %[rh], s79 offen offset:3072 sc1\n\t" \
  "buffer_load_dwordx4 v[64:67], v211, %[rh], s76 offen offset:0 sc1\n\t" \
  "buffer_load_dwordx4 v[68:71], v211, %[rh], s76 offen offset:1024 sc1\n\t" \
  "buffer_load_dwordx4 v[72:75], v211, %[rh], s77 offen offset:0 sc1\n\t" \
  "buffer_load_dwordx4 v[76:79], v211, %[rh], s77 offen offset:1024 sc1\n\t" \
  "buffer_load_dwordx4 v[80:83], v211, %[rh], s76 offen offset:2048 sc1\n\t" \
  "buffer_load_dwordx4 v[84:87], v211, %[rh], s76 offen offset:3072 sc1\n\t" \
  "buffer_load_dwordx4 v[88:91], v211, %[rh], s77 offen offset:2048 sc1\n\t" \
  "buffer_load_dwordx4 v[92:95], v211, %[rh], s77 offen offset:3072 sc1\n\t" \
  "buffer_load_dwordx4 v[96:99], v211, %[rh], s78 offen offset:0 sc1\n\t" \
  "buffer_load_dwordx4 v[100:103], v211, %[rh], s78 offen offset:1024 sc1\n\t" \
  "buffer_load_dwordx4 v[104:107], v211, %[rh], s79 offen offset:0 sc1\n\t" \
  "buffer_load_dwordx4 v[108:111], v211, %[rh], s79 offen offset:1024 sc1\n\t" \
  "buffer_load_dwordx4 v[112:115], v211, %[rh], s78 offen offset:2048 sc1\n\t" \
  "buffer_load_dwordx4 v[116:119], v211, %[rh], s78 offen offset:3072 sc1\n\t" \
  "buffer_load_dwordx4 v[120:123], v211, %[rh], s79 offen offset:2048 sc1\n\t" \
  "buffer_load_dwordx4 v[124:127], v211, %[rh], s79 offen offset:3072 sc1\n\t" \
  "s_waitcnt vmcnt(0)\n\t" \
  "s_bitcmp1_b32 s58, 2\n\t" \
  "s_cbranch_scc1 36f\n\t" \
  "v_max_u32 v148, v0, v1\n\t" \
  "v_max3_u32 v148, v148, v2, v3\n\t" \
  "v_max3_u32 v148, v148, v4, v5\n\t" \
  "v_max3_u32 v148, v148, v6, v7\n\t" \
  "v_max3_u32 v148, v148, v8, v9\n\t" \
  "v_max3_u32 v148, v148, v10, v11\n\t" \
  "v_max3_u32 v148, v148, v12, v13\n\t" \
  "v_max3_u32 v148, v148, v14, v15\n\t" \
  "v_max3_u32 v148, v148, v16, v17\n\t" \
  "v_max3_u32 v148, v148, v18, v19\n\t" \
  "v_max3_u32 v148, v148, v20, v21\n\t" \
  "v_max3_u32 v148, v148, v22, v23\n\t" \
  "v_max3_u32 v148, v148, v24, v25\n\t" \
  "v_max3_u32 v148, v148, v26, v27\n\t" \
  "v_max3_u32 v148, v148, v28, v29\n\t" \
  "v_max3_u32 v148, v148, v30, v31\n\t" \
  "v_max3_u32 v148, v148, v32, v33\n\t" \
  "v_max3_u32 v148, v148, v34, v35\n\t" \
  "v_max3_u32 v148, v148, v36, v37\n\t" \
  "v_max3_u32 v148, v148, v38, v39\n\t" \
  "v_max3_u32 v148, v148, v40, v41\n\t" \
  "v_max3_u32 v148, v148, v42, v43\n\t" \
  "v_max3_u32 v148, v148, v44, v45\n\t" \
  "v_max3_u32 v148, v148, v46, v47\n\t" \
  "v_max3_u32 v148, v148, v48, v49\n\t" \
  "v_max3_u32 v148, v148, v50, v51\n\t" \
  "v_max3_u32 v148, v148, v52, v53\n\t" \
  "v_max3_u32 v148, v148, v54, v55\n\t" \
  "v_max3_u32 v148, v148, v56, v57\n\t" \
  "v_max3_u32 v148, v148, v58, v59\n\t" \
  "v_max3_u32 v148, v148, v60, v61\n\t" \
  "v_max3_u32 v148, v148, v62, v63\n\t" \
  "v_max3_u32 v148, v148, v64, v65\n\t" \
  "v_max3_u32 v148, v148, v66, v67\n\t" \
  "v_max3_u32 v148, v148, v68, v69\n\t" \
  "v_max3_u32 v148, v148, v70, v71\n\t" \
  "v_max3_u32 v148, v148, v72, v73\n\t" \
  "v_max3_u32 v148, v148, v74, v75\n\t" \
  "v_max3_u32 v148, v148, v76, v77\n\t" \
  "v_max3_u32 v148, v148, v78, v79\n\t" \
  "v_max3_u32 v148, v148, v80, v81\n\t" \
  "v_max3_u32 v148, v148, v82, v83\n\t" \
  "v_max3_u32 v148, v148, v84, v85\n\t" \
  "v_max3_u32 v148, v148, v86, v87\n\t" \
  "v_max3_u32 v148, v148, v88, v89\n\t" \
  "v_max3_u32 v148, v148, v90, v91\n\t" \
  "v_max3_u32 v148, v148, v92, v93\n\t" \
  "v_max3_u32 v148, v148, v94, v95\n\t" \
  "v_max3_u32 v148, v148, v96, v97\n\t" \
  "v_max3_u32 v148, v148, v98, v99\n\t" \
  "v_max3_u32 v148, v148, v100, v101\n\t" \
  "v_max3_u32 v148, v148, v102, v103\n\t" \
  "v_max3_u32 v148, v148, v104, v105\n\t" \
  "v_max3_u32 v148, v148, v106, v107\n\t" \
  "v_max3_u32 v148, v148, v108, v109\n\t" \
  "v_max3_u32 v148, v148, v110, v111\n\t" \
  "v_max3_u32 v148, v148, v112, v113\n\t" \
  "v_max3_u32 v148, v148, v114, v115\n\t" \
  "v_max3_u32 v148, v148, v116, v117\n\t" \
  "v_max3_u32 v148, v148, v118, v119\n\t" \
  "v_max3_u32 v148, v148, v120, v121\n\t" \
  "v_max3_u32 v148, v148, v122, v123\n\t" \
  "v_max3_u32 v148, v148, v124, v125\n\t" \
  "v_max3_u32 v148, v148, v126, v127\n\t" \
  "v_cmp_eq_u32 vcc, -1, v148\n\t" \
  "s_cbranch_vccz 36f\n\t" \
  "s_add_u32 s59, s59, 0x1000000\n\t" \
  "s_cmp_lt_u32 s59, 0x10000000\n\t" \
  "s_cbranch_scc1 35b\n\t" \
  "s_or_b32 s58, s58, 9\n\t" \
  "s_branch 7f\n\t" \
  "36:\n\t" \
  "s_and_b32 s58, s58, 4\n\t" \
  "s_or_b32 s58, s58, 32\n\t" \
  "s_branch 31b\n\t" \
  "18:\n\t" \
  "v_mov_b32 v186, -1\n\t" \
  "s_and_b32 s80, s40, 3\n\t" \
  "s_mul_i32 s77, s80, 0xc000\n\t" \
  "s_add_i32 s77, s77, 0x40000\n\t" \
  "s_lshl_b32 s78, s80, 14\n\t" \
  "s_add_i32 s78, s78, 0x70000\n\t" \
  "s_mov_b32 s57, 0\n\t" \
  "s_add_i32 s96, s77, 0x1000\n\t" \
  "s_and_b32 s98, s40, 2\n\t" \
  "s_lshl_b32 s98, s98, 13\n\t" \
  "s_and_b32 s80, s40, 1\n\t" \
  "s_lshl_b32 s80, s80, 12\n\t" \
  "s_add_i32 s98, s98, s80\n\t" \
  "s_add_i32 s98, s98, 0x84000\n\t" \
  "12:\n\t" \
  "s_or_b32 s58, s58, 3\n\t" \
  "11:\n\t" \
  "buffer_load_dwordx4 v[0:3], v193, %[rh], s77 offen offset:0 sc1\n\t" \
  "buffer_load_dwordx4 v[4:7], v193, %[rh], s77 offen offset:1024 sc1\n\t" \
  "buffer_load_dwordx4 v[8:11], v193, %[rh], s77 offen offset:2048 sc1\n\t" \
  "buffer_load_dwordx4 v[12:15], v193, %[rh], s77 offen offset:3072 sc1\n\t" \
  "buffer_load_dwordx4 v[16:19], v193, %[rh], s96 offen offset:0 sc1\n\t" \
  "buffer_load_dwordx4 v[20:23], v193, %[rh], s96 offen offset:1024 sc1\n\t" \
  "buffer_load_dwordx4 v[24:27], v193, %[rh], s96 offen offset:2048 sc1\n\t" \
  "buffer_load_dwordx4 v[28:31], v193, %[rh], s96 offen offset:3072 sc1\n\t" \
  "buffer_load_dwordx4 v[32:35], v195, %[rh], s77 offen offset:0 sc1\n\t" \
  "buffer_load_dwordx4 v[36:39], v195, %[rh], s77 offen offset:2048 sc1\n\t" \
  "v_mov_b64 v[40:41], 0\n\t" \
  "v_mov_b64 v[42:43], 0\n\t" \
  "v_mov_b64 v[44:45], 0\n\t" \
  "v_mov_b64 v[46:47], 0\n\t" \
  "v_mov_b64 v[48:49], 0\n\t" \
  "v_mov_b64 v[50:51], 0\n\t" \
  "v_mov_b64 v[52:53], 0\n\t" \
  "v_mov_b64 v[54:55], 0\n\t" \
  "v_mov_b64 v[56:57], 0\n\t" \
  "v_mov_b64 v[58:59], 0\n\t" \
  "v_mov_b64 v[60:61], 0\n\t" \
  "v_mov_b64 v[62:63], 0\n\t" \
  "v_mov_b64 v[64:65], 0\n\t" \
  "v_mov_b64 v[66:67], 0\n\t" \
  "v_mov_b64 v[68:69], 0\n\t" \
  "v_mov_b64 v[70:71], 0\n\t" \
  "v_mov_b64 v[72:73], 0\n\t" \
  "v_mov_b64 v[74:75], 0\n\t" \
  "v_mov_b64 v[76:77], 0\n\t" \
  "v_mov_b64 v[78:79], 0\n\t" \
  "v_mov_b64 v[80:81], 0\n\t" \
  "v_mov_b64 v[82:83], 0\n\t" \
  "v_mov_b64 v[84:85], 0\n\t" \
  "v_mov_b64 v[86:87], 0\n\t" \
  "v_mov_b64 v[88:89], 0\n\t" \
  "v_mov_b64 v[90:91], 0\n\t" \
  "v_mov_b64 v[92:93], 0\n\t" \
  "v_mov_b64 v[94:95], 0\n\t" \
  "v_mov_b64 v[96:97], 0\n\t" \
  "v_mov_b64 v[98:99], 0\n\t" \
  "v_mov_b64 v[100:101], 0\n\t" \
  "v_mov_b64 v[102:103], 0\n\t" \
  "v_mov_b64 v[136:137], 0\n\t" \
  "v_mov_b64 v[138:139], 0\n\t" \
  "v_mov_b64 v[140:141], 0\n\t" \
  "v_mov_b64 v[142:143], 0\n\t" \
  "s_waitcnt vmcnt(0)\n\t" \
  "s_add_u32 s59, s59, 1\n\t" \
  "s_bitcmp1_b32 s58, 2\n\t" \
  "s_cbranch_scc1 10b\n\t" \
  "v_max_u32 v148, v0, v1\n\t" \
  "v_max3_u32 v148, v148, v2, v3\n\t" \
  "v_max3_u32 v148, v148, v4, v5\n\t" \
  "v_max3_u32 v148, v148, v6, v7\n\t" \
  "v_max3_u32 v148, v148, v8, v9\n\t" \
  "v_max3_u32 v148, v148, v10, v11\n\t" \
  "v_max3_u32 v148, v148, v12, v13\n\t" \
  "v_max3_u32 v148, v148, v14, v15\n\t" \
  "v_max3_u32 v148, v148, v16, v17\n\t" \
  "v_max3_u32 v148, v148, v18, v19\n\t" \
  "v_max3_u32 v148, v148, v20, v21\n\t" \
  "v_max3_u32 v148, v148, v22, v23\n\t" \
  "v_max3_u32 v148, v148, v24, v25\n\t" \
  "v_max3_u32 v148, v148, v26, v27\n\t" \
  "v_max3_u32 v148, v148, v28, v29\n\t" \
  "v_max3_u32 v148, v148, v30, v31\n\t" \
  "v_max3_u32 v148, v148, v32, v33\n\t" \
  "v_max3_u32 v148, v148, v34, v35\n\t" \
  "v_max3_u32 v148, v148, v36, v37\n\t" \
  "v_max3_u32 v148, v148, v38, v39\n\t" \
  "v_cmp_eq_u32 vcc, -1, v148\n\t" \
  "s_cbranch_vccz 10b\n\t" \
  "s_bitcmp1_b32 s59, 6\n\t" \
  "s_cbranch_scc0 11b\n\t" \
  "7:\n\t" \
  "92:\n\t" \
  "s_mov_b32 s42, s58\n\t" \
  "s_waitcnt vmcnt(0) lgkmcnt(0)\n\t" \
  ""

#define V53 "v0", "v1", "v2", "v3", "v4", "v5", "v6", "v7", "v8", "v9", "v10", "v11", "v12", "v13", "v14", "v15", "v16", "v17", \
  "v18", "v19", "v20", "v21", "v22", "v23", "v24", "v25", "v26", "v27", "v28", "v29", "v30", "v31", "v32", "v33", "v34", "v35", \
  "v36", "v37", "v38", "v39", "v40", "v41", "v42", "v43", "v44", "v45", "v46", "v47", "v48", "v49", "v50", "v51", "v52"
#define V100 V53, "v53", "v54", "v55", "v56", "v57", "v58", "v59", "v60", "v61", "v62", "v63", "v64", "v65", "v66", "v67", "v68", "v69", "v70", \
  "v71", "v72", "v73", "v74", "v75", "v76", "v77", "v78", "v79", "v80", "v81", "v82", "v83", "v84", "v85", "v86", "v87", "v88", \
  "v89", "v90", "v91", "v116", "v117", "v118", "v119"
#define STEP_PV
#define CB_CL "s60", "s61", "s62", "s63", "s64", "s65", "s66", "s67", "s68", "s69",
#define STEPS(BODY, ...) asm volatile(BODY : "=&{v[92:99]}"(X), "+{s[40:55]}"(P) STEP_PV \
  : "{v[120:151]}"(W.v[0]), "{v[152:183]}"(W.v[1]), "{v[184:215]}"(W.v[2]), "{v[216:247]}"(W.v[3]), "{v[100:115]}"(C), [rh] "s"(rh), [wv] "s"(w), [ru] "s"(rum), [sel] "s"(sel) __VA_ARGS__ \
  : "memory", "vcc", "scc", "s56", "s57", "s58", "s59", "s76", "s77", "s78", "s79", "s80", "s81", "s82", "s83", "s84", "s85", "s86", "s87", "s88", "s89", \
    "s90", "s91", "s92", "s93", "s94", "s95", "s96", "s97", V100)
__device__ __forceinline__ void keep_a(const Wts& W) {
#pragma unroll
  for (int i = 0; i < 32; ++i) asm volatile("" :: "a"(W.a[i]));
}
__device__ __forceinline__ char* lds_base(char* smem, int off) { asm volatile("" : "+v"(off)); return smem + off; }
__device__ __forceinline__ u32x2 tr4(unsigned p) { return __builtin_bit_cast(u32x2, __builtin_amdgcn_ds_read_tr16_b64_v4i16((LAS bf16x4*)(uintptr_t)p)); }
__device__ __forceinline__ float rb1(float x) { return __uint_as_float(pk2(0.f, x)); }

__device__ __forceinline__ void steps_init(u32x16& P, int h, int w, unsigned place) {
#pragma unroll
  for (int i = 0; i < 16; ++i) P[i] = 0;
  P[2] = 1; P[3] = CDUR; P[14] = H_MEM + (w >> 1) * CHK + (w & 1) * 4096 + (h & RHO) * 1024; P[15] = place;
}
__device__ __forceinline__ void steps_enter(u32x16& P, Clk& ck, bool dead) {
  ck.sync();
  P[2] = (P[2] & 3u) | (dead ? 4u : 0u); P[6] = ck.c2t << 15; P[11] = ck.ce; P[12] = ck.t2c; P[13] = ck.te;
#pragma unroll
  for (int i = 0; i < 16; ++i) P[i] = __builtin_amdgcn_readfirstlane(P[i]);
}

__device__ __forceinline__ void role_qk(const ChainArgs& a, int h, char* smem, const int w, Gate& G) {
  int lane = opaque_tid(0);
  const int b = h; (void)b;
  u64* prof = a.prof; (void)prof;
  PDECL(L_PRF, 4);
  const float CS = 0.08838834764831845f * 1.4426950408889634f;
  GAS unsigned* abortw = (GAS unsigned*)(a.sync + SY_ABORT);
  bool dead = false;
  Wts W;
  Clk ck; ck.init();
  u32x4 gv;
  if (QK_D) while (__builtin_amdgcn_s_memrealtime() - G.entry < QK_D) __builtin_amdgcn_s_sleep(8);
  load_weights<0>(a, h, w, lane, smem, W, G, gv);
  if (!G.look(a.sync, gv)) return;
  PZERO;
  const Sel sel = G.sel;
  const __amdgpu_buffer_rsrc_t rh = rsrc_hot(a.sync, sel), rum = rsrc_u(a.sync);
  auto pm_o = [&](int l) { return 2 * CHK + h * 256 + (l & 1) * 128 + (l >> 3) * 16 + (l & 6) * 2; };
  auto fac = [&](int l, int hf) { return ((l & 7) >> 1) == 2 * (l >> 5) + ((h >> 1) & 1) && ((l >> 3) & 1) == hf ? 1.f : 0.f; };
  u32x16 C, P;
  {
    const int r = lane & 15, g = lane >> 4;
    const unsigned l0 = (unsigned)(uintptr_t)(LAS char*)smem;
    C[0] = lane * 16; C[1] = l0 + L_MEM + lane * 16; C[2] = l0 + L_WTS + w * 4096 + lane * 16; C[3] = C[2] + 65536;
    C[4] = (lane & 2) ? 0xEEEE : 0x4444;
    C[5] = l0 + L_XCH + (16 * w + 2 * (r >> 2) + (r & 1)) * 32 + g * 8 + ((r >> 1) & 1) * 4;
    C[6] = l0 + L_XK + (32 * w + r) * 16 + g * 4;
    C[7] = w == 3 ? H_PX + (h >> 3) * CHK + (h & 7) * 1024 + lane * 16 : w ? (2 * h + 1) * CHK + (w - 1) * 4096 + lane * 16 : l0 + L_XCH + (8 * g + (r >> 2)) * 32 + (r & 3) * 8;
    C[8] = l0 + L_XK + (8 * g + (r >> 2)) * 16 + (r & 1) * 8;
    C[9] = w ? (lane & 1) * 4096 : (lane == 0 ? H_ANN + h * 128 : 0x7fffffffu);
    C[10] = w == 0 ? (h >> 4) * CHK + (h & 15) * 512 + ((lane >> 1) & 1) * 256 + (lane >> 5) * 128 + (lane & 1) * 64 + ((lane >> 3) & 3) * 16 + ((lane >> 2) & 1) * 8 :
            w < 3 ? H_PX + ((w - 1) * 4 + (h >> 3)) * CHK + (h & 7) * 1024 + lane * 16 :
            lane < 32 ? (h >> 4) * CHK + (h & 15) * 512 + lane * 16 : (lane < 48 ? 2 * CHK + h * 256 + (lane - 32) * 16 : 0x7fffffffu);
    C[11] = w == 0 ? pm_o(lane) : (lane < (w == 3 ? 32 : 16) ? H_PX + (w == 3 ? 8 + g : 7 + w) * CHK + hpos(h) * 256 + r * 16 : 0x7fffffffu);
    C[12] = H_TR + (lane & 31) * 128; C[13] = l0 + L_PRF + w * 1024 + (lane >> 6);
    unsigned mk = 0;
#pragma unroll
    for (int i = 0; i < 8; ++i) mk |= (16 * ((i >> 1) & 1) + 8 * (g & 1) + 4 * (g >> 1) + 2 * (i & 1) + (i >> 2) > 16 * (w - 1) + r ? 1u : 0u) << i;
    C[14] = w ? mk : __float_as_uint(fac(lane, 0)); C[15] = __float_as_uint(fac(lane, 1));
    asm volatile("" : "+v"(C));
  }
  steps_init(P, h, w, 2 * h * CHK + (3 * w - 3) * 1024);
  u32x4 k0[2];
  start_wait(a.sync, abortw, lane, dead, CT_Q + ct_near(sel) + h, 1u, [&]() { for (int i = 0; i < 2; ++i) k0[i] = ld16(rum, x_slot(1, 0, h, sel) + (2 * w + i) * 1024 + lane * 16); });
  G.arrive(a.sync);
#pragma unroll
  for (int i = 0; i < 2; ++i) *(u32x4*)(smem + L_KX + (2 * w + i) * 1024 + lane * 16) = k0[i];
  SSTAMP(96 + h);
  GAS unsigned* kcp = ctr(a, CT_Q + ct_near(sel) + h);
  unsigned spins = 0;
  while (true) {
    u32x8 X; u32x16 pv; (void)pv;
    steps_enter(P, ck, dead);
    STEPS(QK_BODY, , [cs] "s"(CS), [kc] "s"(kcp));
    keep_a(W);
    const unsigned res = __builtin_amdgcn_readfirstlane(P[2]), s = __builtin_amdgcn_readfirstlane(P[4]);
    lane = opaque_tid(0);
    if (res & 8u) {
      auto sw = [](float v, int k) { return __int_as_float(k == 0 ? __builtin_amdgcn_mov_dpp(__float_as_int(v), 0xB1, 0xf, 0xf, false) :
                                                           k == 1 ? __builtin_amdgcn_mov_dpp(__float_as_int(v), 0x4E, 0xf, 0xf, false) :
                                                                    __builtin_amdgcn_mov_dpp(__float_as_int(v), 0x141, 0xf, 0xf, false)); };
      float x[5], d = fmaxf(__uint_as_float(X[4]), 0.f);
      d = fmaxf(d, sw(d, 0)); d = fmaxf(d, sw(d, 1)); d = fmaxf(d, sw(d, 2));
#pragma unroll
      for (int i = 0; i < 5; ++i) x[i] = __builtin_amdgcn_exp2f(__uint_as_float(X[i]) - d);
      float z = ((x[0] + x[1]) + (x[2] + x[3])) + x[4];
      z += sw(z, 0); z += sw(z, 1); z += sw(z, 2);
      z = __builtin_amdgcn_rcpf(z);
      const float y = sw(x[4], 0), f = __builtin_amdgcn_exp2f(-d) * z;
      const unsigned pm = (lane & 1) ? pk2(fac(lane, 0) * f, fac(lane, 1) * f) : pk2(x[4] * z, y * z);
      if (!(__uint_as_float(X[5]) < 1.0e18f))
        __builtin_amdgcn_raw_buffer_store_b32(nosent(pm), rh, pm_o(lane), H_P + ((s - 1) & 3) * 3 * CHK, ST_AUX);
    }
    if (res & 64u) break;
    if (res & 32u) {
      u32x4 k[3];
      start_wait(a.sync, abortw, lane, dead, CT_Q + ct_near(sel) + h, s + 1, [&]() { for (int i = 0; i < 3; ++i) k[i] = ld16(rum, x_slot(1, s, h, sel) + min(3 * w - 3 + i, 7) * 1024 + lane * 16); });
#pragma unroll
      for (int i = 0; i < 3; ++i) *(u32x4*)(smem + L_KX + min(3 * w - 3 + i, 7) * 1024 + lane * 16) = k[i];
    }
    if (res & 17u) {
      spins |= 63u;
      if (dead || poll_fail(spins, abortw, lane)) dead = true;
    }
  }
  PDUMP(0);
}

__device__ __forceinline__ void role_vo(const ChainArgs& a, int h, char* smem, const int w, Gate& G) {
  int lane = opaque_tid(0);
  const int b = 32 + h; (void)b;
  u64* prof = a.prof; (void)prof;
  PDECL(L_PRF, 4);
  GAS unsigned* abortw = (GAS unsigned*)(a.sync + SY_ABORT);
  bool dead = false;
  Wts W;
  Clk ck; ck.init();
  u32x4 gv;
  load_weights<1>(a, h, w, lane, smem, W, G, gv);
  if (!G.look(a.sync, gv)) return;
  PZERO;
  const Sel sel = G.sel;
  const __amdgpu_buffer_rsrc_t rh = rsrc_hot(a.sync, sel), rum = rsrc_u(a.sync);
  u32x16 C, P;
  {
    const int r = lane & 15, g = lane >> 4;
    const unsigned l0 = (unsigned)(uintptr_t)(LAS char*)smem;
#pragma unroll
    for (int i = 0; i < 16; ++i) C[i] = H_TR + (lane & 31) * 128;
    C[0] = lane * 16; C[1] = l0 + L_MEM + lane * 16; C[2] = l0 + L_WTS + w * 4096 + lane * 16; C[3] = C[2] + 65536;
    C[4] = (lane & 2) ? 0xEEEE : 0x4444;
    C[5] = l0 + L_XCH + (32 * w + r) * 16 + g * 4;
    C[6] = (lane & 8) ? 0xEEEE : 0x4444;
    C[7] = l0 + L_XCH + (32 * (g >> 1) + 8 * (g & 1) + (r >> 2)) * 16 + (r & 1) * 8;
    C[8] = ((g & 1) * 8 + (g >> 1)) * CHK + r * 16;
    C[13] = l0 + L_PRF + w * 1024 + (lane >> 6);
    asm volatile("" : "+v"(C));
  }
  start_wait(a.sync, abortw, lane, dead, -1, 0u, [] {});
  G.arrive(a.sync);
  SSTAMP(128 + h);
  steps_init(P, h, w, SY_UM + (16 * (h & 3) + 2 * w) * CHK + hpos(h) * 256);
  unsigned spins = 0;
  while (true) {
    u32x8 X; u32x16 pv; (void)pv;
    steps_enter(P, ck, dead);
    STEPS(VO_BODY);
    keep_a(W);
    lane = opaque_tid(0);
    if (__builtin_amdgcn_readfirstlane(P[2]) & 64u) break;
    spins |= 63u;
    if (dead || poll_fail(spins, abortw, lane)) dead = true;
  }
  PDUMP(0);
}

__device__ __forceinline__ void calibrate(const ChainArgs& a, int cal, int slot, u64 entry) {
  const u32x4 rs = own_rsrc(a.sync, 64 * PAGE);
  const unsigned so = cal * PAGE + (slot >> 1) * CHK + (slot & 1) * 4096;
  unsigned best[2] = {1u << 30, 1u << 30};
  bool sure;
  {
    unsigned z = 0;
    asm volatile("s_mov_b64 exec, 0xffff\n\tbuffer_atomic_or %0, %1, %2, %3 offen sc0 sc1\n\ts_mov_b64 exec, -1\n\ts_waitcnt vmcnt(0)"
                 : "+v"(z) : "v"((opaque_tid(0) & 8) * 1024 + (opaque_tid(0) & 7) * 256), "s"(rs), "s"(so) : "memory");
  }
  for (int i = 0;; ++i) {
    sure = min(best[0], best[1]) < CALT && max(best[0], best[1]) - min(best[0], best[1]) > 110u;
    if (i >= CALN && (sure || __builtin_amdgcn_s_memrealtime() - entry > CALM)) break;
#pragma unroll
    for (int hf = 0; hf < 2; ++hf) {
      u64 c0, c1;
      unsigned z = 0;
      asm volatile("s_mov_b64 exec, 1\n\ts_memtime %0\n\ts_waitcnt lgkmcnt(0)\n\tbuffer_atomic_or %2, off, %3, %4 sc0 sc1\n\ts_waitcnt vmcnt(0)\n\ts_memtime %1\n\ts_mov_b64 exec, -1\n\ts_waitcnt lgkmcnt(0)"
                   : "=&s"(c0), "=&s"(c1), "+v"(z) : "s"(rs), "s"(so + hf * 8192 + (i & 7) * 256) : "memory");
      best[hf] = min(best[hf], (unsigned)(c1 - c0));
    }
  }
  const u64 hs = (__builtin_amdgcn_s_memrealtime() & ~3ull) | (sure ? 2u : 0u) | ((best[1] < best[0] ? 1u : 0u) ^ die_half());
  if (opaque_tid(0) == 0) __hip_atomic_store((GAS u64*)ctr(a, CT_CAL + (slot >> 1)) + (slot & 1), hs, __ATOMIC_RELAXED, __HIP_MEMORY_SCOPE_AGENT);
}
__device__ __forceinline__ u32x4 pk8(f32x4 a, f32x4 b) { return (u32x4){pk2(a[0], a[1]), pk2(a[2], a[3]), pk2(b[0], b[1]), pk2(b[2], b[3])}; }
__device__ __forceinline__ unsigned pk2v(float a, float b) { unsigned r; asm volatile("v_cvt_pk_bf16_f32 %0, %1, %2" : "=v"(r) : "v"(a), "v"(b)); return r; }
__device__ __forceinline__ u32x4 pk8v(f32x4 a, f32x4 b) { asm volatile("s_nop 7"); return (u32x4){pk2v(a[0], a[1]), pk2v(a[2], a[3]), pk2v(b[0], b[1]), pk2v(b[2], b[3])}; }
__device__ __forceinline__ void st16g(u16* p, u32x4 v) { asm volatile("global_store_dwordx4 %0, %1, off " OUTP "\n\ts_nop 1" :: "v"(p), "v"(v) : "memory"); }
__device__ __forceinline__ void role_cb(const ChainArgs& a, int cbi, char* smem, const int w, Gate& G) {
  int lane = opaque_tid(0);
  const int b = 64 + cbi; (void)b;
  const int par = cbi >> 5, ci = cbi & 31, J = ci >> 3;
  u64* prof = a.prof; (void)prof;
  PDECL(65536, 8); PZERO;
  GAS unsigned* abortw = (GAS unsigned*)(a.sync + SY_ABORT);
  bool dead = false;
  const int tw = 2 * ci + (w & 1);
  const unsigned mo = (tw >> 5) * CHK + (tw & 31) * 256;
  Sel sel = 0;
  auto start = [&]() {
    lane = opaque_tid(0);
    SSTAMP(0 + ci);
    unsigned m0 = 0;
    if (par == 0 && w < 2) {
      const u16* m = a.mem0 + (2 * (lane >> 4) + ((lane >> 2) & 1)) * DM + 16 * tw + (lane & 8);
      m0 = m[lane & 3] | (unsigned)m[7 - (lane & 3)] << 16;
    }
    if (!G.look(a.sync, (u32x4){0u, 0u, 0u, 0u})) return false;
    sel = G.sel;
    SSTAMP(64 + ci);
    const __amdgpu_buffer_rsrc_t rh = rsrc_hot(a.sync, sel), rum = rsrc_u(a.sync);
    const unsigned s1v = ~(unsigned)(lane >> 6);
    const u32x4 sv = {s1v, s1v, s1v, s1v};
    auto um = [&](int k) {
#pragma unroll
      for (int i = 0; i < 4; ++i) st16(rum, near_half(SY_UM + (64 * k + tw) * CHK, sel) + (w >> 1) * 4096 + i * 1024 + lane * 16, sv);
    };
    um(par);
    const unsigned dv = cbi == H_DLT / CHK ? K_DEF * TU : s1v;
#pragma unroll
    for (int i = 0; i < 2; ++i) st16(rh, cbi >= 2 && cbi < 45 ? (cbi < 35 ? cbi * CHK : H_PX + (cbi - 35) * CHK) + w * 2048 + i * 1024 + lane * 16 : 0x7fffffffu, (u32x4){dv, s1v, s1v, s1v});
    if (par == 0 && w < 2) st4(rh, H_MEM + mo + lane * 4, nosent(m0));
    asm volatile("s_waitcnt vmcnt(0)" ::: "memory");
    lds_barrier();
    if (w == 0 && lane == 0) ctr_add(a, CT_INIT + J, 1u);
    SSTAMP(256 + ci);
    G.arrive(a.sync);
    um(par + 2);
    {
      const __amdgpu_buffer_rsrc_t rx = mk_rsrc((void*)a.X, 4u << 20);
      u32x4 v[16];
#pragma unroll
      for (int i = 0; i < 16; ++i) v[i] = __builtin_amdgcn_raw_buffer_load_b128(rx, (unsigned)(cbi * 4096 + w * 1024 + lane * 16), (unsigned)(i * 262144), 0);
#pragma unroll
      for (int i = 0; i < 16; ++i) asm volatile("" :: "v"(v[i]));
    }
    SSTAMP(224 + ci);
    start_wait(a.sync, abortw, lane, dead, -1, 0u, [] {});
    return true;
  };
  if (!start()) return;
  Wts W;
#pragma unroll
  for (int i = 0; i < 32; ++i) asm volatile("" : "=a"(W.a[i]));
  u32x16 C, P; u32x8 C2;
  {
    const unsigned l0 = (unsigned)(uintptr_t)(LAS char*)smem, oor = 0x7fffffffu;
    const int r = lane & 7, t = (lane >> 3) & 1, g = lane >> 4, e = w & 1;
    const bool L = w < 2;
    C[0] = lane * 16;
    C[1] = L ? l0 + 32768 + e * 8192 + (lane & 2) * 512 + ((lane & 1) * 32 + (lane >> 2)) * 16 : e * CHK + t * 512 + (g & 1) * 256 + (r >> 2) * 128 + (g >> 1) * 64 + (r & 3) * 16;
    C[2] = L ? (4 * t + g) * 1024 + r * 16 : l0 + 81920 + e * 32768 + lane * 16;
    C[3] = L ? l0 + e * 1024 + lane * 8 : 2 * CHK + e * 4096 + (4 * (g >> 1) + 2 * t + (g & 1)) * 256 + 128 + r * 16;
    C[4] = ((16 * e + (lane >> 2)) * OUTW + DM + 32 * ci + 8 * (lane & 3)) * 2;
    C[5] = l0 + (L ? e * 8192 + lane * 16 : e * 512 + lane * 8); C[6] = l0 + 16384 + lane * 16; C[7] = l0 + 32768 + (L ? e * 8192 + 4096 : e * 2048) + lane * 16;
    C[8] = (lane & 31) * 128;
    C[9] = (8 * (g & 1) + 4 * (g >> 1) + 2 * t + (r >> 2)) * 16 + 4 * (r < 4 ? r : 7 - r);
    C[10] = (lane | e) ? oor : ci * 128;
    C[11] = L ? SY_CT + (CT_U + ct_near(sel) + 16 * e + (lane & 15)) * 128 : (t ? 0xEEEE : 0x4444); C[12] = L ? l0 + 81920 + e * 32768 + lane * 16 : ((lane & 2) ? 0xEEEE : 0x4444); C[13] = r < 4 ? 0x01000504u : 0x07060302u;
    C[14] = l0 + 49152 + 32 * e + (lane >> 6); C[15] = l0 + 49152 + (lane >> 6);
    C2[0] = L ? (lane < 16 ? lane * 16 : oor) : ux_tile(2 * ci) + lane * 16;
    C2[1] = lane == 0 && e && ci < 8 ? SY_CT + (CT_DONE + ct_near(~sel) + 8 * par + ci) * 128 : oor;
    C2[2] = (L ? ux_tile(2 * ci) : H_PX + 2 * e * CHK) + lane * 16;
#pragma unroll
    for (int n = 0; n < 4; ++n) C2[3 + n] = L ? H_PX + (8 + e) * CHK + (J ^ n) * 2048 + lane * 16 : C2[2] + CHK;
    C2[7] = l0 + 65536 + w * 2048 - 64 + (lane >> 6);
    asm volatile("" : "+v"(C), "+v"(C2));
#pragma unroll
    for (int i = 0; i < 16; ++i) P[i] = par - 2;
    P[2] = 48; P[6] = P[7] = K_DEF * TU; P[8] = SY_UM + tw * CHK + J * 2048; P[9] = H_MEM + mo; P[10] = 2 * CHK + J * 256; P[11] = e * 16 * UXH; P[12] = H_DLT + par * 4096;
  }
  Clk ck; ck.init();
  unsigned spins = 0;
  while (true) {
    u32x16 pv; (void)pv;
    unsigned res = __builtin_amdgcn_readfirstlane(P[2]);
    if (res & 64u) break;
    const u32x4 rh2 = own_rsrc(a.sync + near_half(SY_HOT, sel), H_BYTES - 8192), ra = own_rsrc(a.sync, SY_BYTES), ro = own_rsrc(a.out, S_LEN * OUTW * 2);
    if (res & 1u) {
      spins |= 63u;
      if (dead || poll_fail(spins, abortw, opaque_tid(0))) dead = true;
    }
    ck.sync();
    P[2] = (res & 58u) | (dead ? 4u : 0u); P[1] = ck.ce; P[3] = ck.te; P[4] = ck.t2c; P[5] = ck.c2t << 15;
#pragma unroll
    for (int i = 0; i < 16; ++i) P[i] = __builtin_amdgcn_readfirstlane(P[i]);
    asm volatile(CB_BODY : "+{s[40:55]}"(P) STEP_PV
                 : "{v[192:207]}"(C), "{v[208:215]}"(C2), [rh] "s"(rh2), [ru] "s"(ra), [ro] "s"(ro), [wv] "s"(w), [sel] "s"(sel)
                 : "memory", "vcc", "scc", CB_CL "s56", "s57", "s58", "s59", "s76", "s77", "s78", "s79", "s80", "s81", "s82", "s83", "s84", "s85", "s86", "s87", "s88", "s89", "s90", "s91", "s92", "s93", "s94", "s95", "s96", "s97", "s98", "s99", "s100", "s101", V53, "v53", "v54", "v55", "v56", "v57", "v58", "v59", "v60", "v61", "v62", "v63", "v64", "v65", "v66", "v67", "v68", "v69", "v70", "v71", "v72", "v73", "v74", "v75", "v76", "v77", "v78", "v79", "v80", "v81", "v82", "v83", "v84", "v85", "v86", "v87", "v88", "v89", "v90", "v91", "v92", "v93", "v94", "v95", "v96", "v97", "v98", "v99", "v100", "v101", "v102", "v103", "v104", "v105", "v106", "v107", "v108", "v109", "v110", "v111", "v112", "v113", "v114", "v115", "v116", "v117", "v118", "v119", "v120", "v121", "v122", "v123", "v124", "v125", "v126", "v127", "v128", "v129", "v130", "v131", "v132", "v133", "v134", "v135", "v136", "v137", "v138", "v139", "v140", "v141", "v142", "v143", "v144", "v145", "v146", "v147", "v148", "v149", "v150", "v151", "v152", "v153", "v154", "v155", "v156", "v157", "v158", "v159", "v160", "v161", "v162", "v163", "v164", "v165", "v166", "v167", "v168", "v169", "v170", "v171", "v172", "v173", "v174", "v175", "v176", "v177", "v178", "v179", "v180", "v181", "v182", "v183", "v184", "v185", "v186", "v187", "v188", "v189", "v190", "v191");
    keep_a(W);
  }
  PDUMP(2);
}

__device__ __forceinline__ void bg_drain() {
  asm volatile("s_waitcnt vmcnt(0)" ::: "memory");
  __builtin_amdgcn_s_barrier();
  asm volatile("" ::: "memory");
}
#define BG_SLP 16
#define A3_SLP 48
__device__ __forceinline__ float blo(unsigned d) { return __uint_as_float(d << 16); }
__device__ __forceinline__ float bhi(unsigned d) { return __uint_as_float(d & 0xffff0000u); }
struct A1 {
  u32x4 kk[11], vv[8], sh[4];
  __device__ __forceinline__ void load(const ChainArgs& a, int x, int n) {
    const int lane = opaque_tid(0);
    const int d = (4 * x + (n & 3)) * 64 + 8 * (lane & 7), tb = (n >> 2) * 64 + 8 * (lane >> 3);
    const __amdgpu_buffer_rsrc_t rk = mk_rsrc(a.Ak, 4u << 20), rv = mk_rsrc(a.Av, 4u << 20), rs = mk_rsrc(a.Ashift, 8192);
#pragma unroll
    for (int i = 0; i < 4; ++i) sh[i] = __builtin_amdgcn_raw_buffer_load_b128(rs, d * 8 + 16 * i, 0, 0);
#pragma unroll
    for (int i = 0; i < 11; ++i) kk[i] = __builtin_amdgcn_raw_buffer_load_b128(rk, tb - 3 + i < 0 ? 0x7fffffffu : ((tb - 3 + i) * D_A + d) * 2, 0, 2);
#pragma unroll
    for (int i = 0; i < 8; ++i) vv[i] = __builtin_amdgcn_raw_buffer_load_b128(rv, ((tb + i) * D_A + d) * 2, 0, 2);
  }
  __device__ __forceinline__ void finish(const ChainArgs& a, int x, int n, char* T, Sel sel) {
    const int lane = opaque_tid(0);
#pragma unroll
    for (int c = 0; c < 8; ++c) {
      const unsigned t01 = sh[c >> 1][2 * (c & 1)], t23 = sh[c >> 1][2 * (c & 1) + 1];
      const float s0 = blo(t01), s1 = bhi(t01), s2 = blo(t23), s3 = bhi(t23);
      float kf[11];
#pragma unroll
      for (int i = 0; i < 11; ++i) kf[i] = (c & 1) ? bhi(kk[i][c >> 1]) : blo(kk[i][c >> 1]);
      u32x4 o;
#pragma unroll
      for (int i = 0; i < 8; i += 2) {
        float r[2];
#pragma unroll
        for (int j = 0; j < 2; ++j)
          r[j] = fmaf(kf[i + j + 3], s0, fmaf(kf[i + j + 2], s1, fmaf(kf[i + j + 1], s2, kf[i + j] * s3))) * ((c & 1) ? bhi(vv[i + j][c >> 1]) : blo(vv[i + j][c >> 1]));
        o[i >> 1] = pk2(r[0], r[1]);
      }
      *(u32x4*)(T + (8 * (lane & 7) + c) * 128 + (((lane >> 3) ^ (lane & 7)) * 16)) = o;
    }
    const __amdgpu_buffer_rsrc_t rkv = rsrc_u(a.sync);
#pragma unroll
    for (int i = 0; i < 8; ++i)
      st16(rkv, kv_row((4 * x + (n & 3)) * 64, sel) + (4 * i + (lane >> 4)) * CHK + ((lane >> 3) & 1) * 4096 + (n >> 2) * 128 + (lane & 7) * 16,
           *(const u32x4*)(T + (8 * i + (lane >> 3)) * 128 + (((lane & 7) ^ i) * 16)));
  }
};
#define WAH 4
#define DSP 4
#define SIGQ 2
__device__ __forceinline__ void task_w(const ChainArgs& a, int kind, int h, char* smem, int wv, Gate& G) {
  const int lane = opaque_tid(0), w = wv, r = lane & 15, g = lane >> 4;
  const __amdgpu_buffer_rsrc_t rw = mk_rsrc(kind == 0 ? a.Wq : (kind == 1 ? a.Wk : a.Wv), 8u << 20), rx = mk_rsrc(a.X, 4u << 20);
  const unsigned lds0 = (unsigned)(uintptr_t)(LAS char*)smem + w * 4096;
  unsigned vx[2], vw[4];
#pragma unroll
  for (int j = 0; j < 2; ++j) {
    const int rr = 8 * j + (lane >> 3);
    const int tb = kind == 0 ? rr : (kind == 1 ? 16 * ((rr >> 1) & 1) + 8 * ((rr >> 2) & 1) + 4 * (rr >> 3) + 2 * (rr & 1) : 8 * (rr >> 2) + (rr & 3));
    vx[j] = tb * DM * 2 + ((lane & 7) ^ (rr >> 1)) * 16;
  }
#pragma unroll
  for (int j = 0; j < 4; ++j) { const int row = (w * 4 + j) * 4 + g; vw[j] = row * HD * 2 + (r ^ swz_b(row)) * 16; }
  const u64 wo = kind == 2 ? bg_order(2) : (kind ? bg_order(1) : bg_order(0));
  auto xo = [&](int q) { return ((int)((wo >> (16 * q + 4 * w)) & 15) ^ (h & RHO)) * 128; };
  unsigned dT = (kind == 0 ? 16 : (kind == 1 ? 1 : 4)) * DM * 2;
  asm volatile("s_nop 4" : "+s"(dT));
  const unsigned vx0 = ((w & 1) ? vx[1] : vx[0]) + (w >> 1) * dT;
  auto st_w = [&](int n) {
#pragma unroll
    for (int j = 0; j < 4; ++j) dma16(rw, vw[j], (64 * ((int)((wo >> (4 * n)) & 15) ^ (h & RHO)) * HD + 128 * h) * 2, lds0 + (n & 7) * 16384 + j * 1024, W_NT & 16);
    dma16(rx, vx0, ((int)((wo >> (4 * n)) & 15) ^ (h & RHO)) * 128, lds0 - 3072 * w + 131072 + (n & 7) * 4096);
  };
  auto st_x = [&](int s, int b, int q, int j, bool pad) { dma16(rx, vx[j & 1], s * 65536 + (j >> 1) * dT + xo(q), lds0 + (b * 4 + q) * 16384 + j * 1024, false, pad); };
  if (KQ_D && kind < 2) while (__builtin_amdgcn_s_memrealtime() - G.entry < KQ_D) __builtin_amdgcn_s_sleep(8);
  __builtin_amdgcn_s_waitcnt(0x0F70);
  u32x4 W[32][2], gv;
  char* xb[3][2];
#pragma unroll
  for (int i = 0; i < 6; ++i) xb[i >> 1][i & 1] = lds_base(smem, (i >> 1) * 65536 + (r >> 3) * 1024 + (r & 7) * 128 + ((4 * (i & 1) + g) ^ (r >> 1)) * 16);
  f32x4 acc[2][4];
#pragma unroll
  for (int i = 0; i < 4; ++i) acc[1][i] = vzero();
#pragma unroll
  for (int n = 0; n < WAH; ++n) st_w(n);
#pragma unroll
  for (int n = 0; n < 16; ++n) {
    asm volatile("s_waitcnt vmcnt(%0)" :: "n"(4 * WAH - 4 + (n < 16 - WAH ? WAH - 1 : 15 - n)) : "memory");
    lds_barrier();
    u32x4 f[4], y[4];
#pragma unroll
    for (int i = 0; i < 4; ++i) {
#pragma unroll
      for (int q = 0; q < 2; ++q) {
        const int row = (i & 1) * 32 + 8 * g + 4 * q + (r >> 2);
        const u32x2 u = __builtin_bit_cast(u32x2, __builtin_amdgcn_ds_read_tr16_b64_v4i16((LAS bf16x4*)(smem + (n & 7) * 16384 + row * 256 + (((4 * w + (r & 3)) ^ swz_b(row)) * 16) + 8 * (i >> 1))));
        f[i][2 * q] = u[0]; f[i][2 * q + 1] = u[1];
      }
      y[i] = *(const u32x4*)(xb[2][i & 1] + (n & 7) * 4096 + (i >> 1) * 2048);
    }
    SB0;
    if (n == 16 - WAH) G.ask(a.sync, gv);
    if (n == 15) asm volatile("" : "+v"(gv));
    if (n + WAH < 16) st_w(n + WAH);
    else {
#pragma unroll
      for (int j = 0; j < 4; ++j) st_x(1 + ((n + WAH - 16) >> 2), (n + WAH - 16) >> 2, (n + WAH) & 3, j, true);
    }
#pragma unroll
    for (int i = 0; i < 8; ++i)
      asm volatile("s_nop 1\n\tv_mfma_f32_16x16x32_bf16 %0, %1, %2, %0" : "+v"(acc[1][i & 3]) : "v"(f[2 * (i & 1) + (i >> 2)]), "v"(y[(i & 2) + (i >> 2)]));
#pragma unroll
    for (int i = 0; i < 4; ++i) { asm volatile("" : "+a"(f[i])); W[2 * n + (i & 1)][i >> 1] = f[i]; }
  }
  if (!G.pass(a.sync, gv)) { bg_drain(); return; }
  SSTAMP(160 + h);
  const Sel sel = G.sel;
  const __amdgpu_buffer_rsrc_t ru = mk_rsrc(a.sync, SY_BYTES);
  const unsigned sgo = (w | lane) ? 0x7fffffffu : SY_CT + (kind == 2 ? CT_V + ct_near(~sel) : CT_Q + 32 * kind + ct_near(sel)) * 128 + h * 128;
  const u32x4 rx2 = own_rsrc(a.X, 4u << 20);
  unsigned vq[4][4];
#pragma unroll
  for (int i = 0; i < 16; ++i) { vq[i >> 2][i & 3] = vx[i & 1] + ((i >> 1) & 1) * dT + xo(i >> 2); asm volatile("" : "+v"(vq[i >> 2][i & 3])); }
  u32x4 xf[4][2], o;
  auto rd = [&](int b, int k, int T) { xf[k & 3][T] = *(const u32x4*)(xb[b][k & 1] + (k >> 1) * 4096 + T * 2048); };
  auto signal = [&](int s) {
    st4(ru, sgo, s);
    if (w == 0 && lane == 0 && s >= 0 && (s < PSEG || (s & 7) == 0 || ((s & 15) == 2 && PSEG > 2) || s > 61)) {
    }
  };
#pragma unroll
  for (int T = 0; T < 2; ++T) st16s(pk8v(acc[1][2 * T], acc[1][2 * T + 1]), ru, lane * 16 + T * 4096, x_slot(kind, 0, h, sel) + w * 1024);
#pragma unroll
  for (int i = 4 * WAH; i < 28; ++i) st_x(1 + (i >> 4), i >> 4, (i >> 2) & 3, i & 3, true);
  wait_vm<26>();
  lds_barrier();
#pragma unroll
  for (int i = 0; i < 6; ++i) rd(0, i >> 1, i & 1);
  WST(kind == 2, 18);
  auto seg = [&](int s, int b) {
    const unsigned so = x_slot(kind, s - 1, h, sel) + w * 1024;
    WST(kind == 2 && s > 0 && s < 4, 18 + s);
#pragma unroll
    for (int k = 0; k < 32; ++k)
#pragma unroll
      for (int i = 0; i < 4; ++i) {
        if (k == 0) asm volatile("v_mfma_f32_16x16x32_bf16 %0, %1, %2, 0" : "=&v"(acc[b][i]) : "a"(W[k][i & 1]), "v"(xf[k & 3][i >> 1]));
        else asm volatile("v_mfma_f32_16x16x32_bf16 %0, %1, %2, %0" : "+v"(acc[b][i]) : "a"(W[k][i & 1]), "v"(xf[k & 3][i >> 1]));
        SB0;
        const int q = k >> 3, n = 4 * k + i;
        if ((k & 7) == 5 && i == 0) {
          if (q == SIGQ) wait_vm<4 * SIGQ>(); else wait_vm<20>();
          if (q == 1 && __builtin_expect(s - 2u < W_NOW, 0)) { wait_vm<4>(); __builtin_amdgcn_s_barrier(); signal(s - 1); }
          else if (q == 0 && b == 0 && __builtin_expect(s == 1, 0)) { wait_vm<32 - 4 * WAH>(); __builtin_amdgcn_s_barrier(); signal(0); }
          else __builtin_amdgcn_s_barrier();
          asm volatile("" ::: "memory");
        }
        if (i == 1 || i == 2) rd(k + 3 < 32 ? b : b ^ 1, (k + 3) & 31, i - 1);
        {
          const int m = (n + 128 - 23) & 127, j = (m & 31) / DSP, q0 = m >> 5;
          if (j < 4 && (m & 31) % DSP == 0) asm volatile("s_add_i32 m0, %0, %1" :: "s"(lds0), "n"(((b ^ (q0 == 0) ^ (n < 23)) * 4 + ((q0 + 3) & 3)) * 16384 + j * 1024) : "m0", "scc");
          if (j < 4 && (m & 31) % DSP == 1)
            asm volatile("buffer_load_dwordx4 %0, %1, %2 offen lds" :: "v"(vq[(q0 + 3) & 3][j & 3]), "s"(rx2), "s"((s + 2 - (q0 == 0) - (n < 23)) * 65536) : "memory");
        }
        if (k == 8 * SIGQ + 5 && i == 2) signal(s - 1);
        if (k >= 1 && k < 5 && (i == 0 || i == 3)) { const f32x4 x = acc[b ^ 1][k - 1]; o[2 * (~k & 1) + (i & 1)] = pk2v(x[2 * (i & 1)], x[2 * (i & 1) + 1]); }
        if ((k == 2 || k == 4) && i == 3) { __builtin_amdgcn_raw_buffer_store_b128(o, ru, (unsigned)(lane * 16 + (k >> 2) * 4096), so, ST_AUX); asm volatile("s_nop 0" :: "v"(o)); }
        SB0;
      }
  };
  A1 a1;
#pragma unroll
  for (int i = 0; i < 11; ++i) asm volatile("" : "=v"(a1.kk[i]), "=v"(a1.vv[i & 7]), "=v"(a1.sh[i & 3]));
  const int wi = 32 * (2 - kind) + 4 * (h & 7) + w;
  for (int s = 1, ph = 0;; ++ph) {
    const int n = ph == 1 ? wi : (ph == 2 && wi >= 64 ? 32 + wi : 256);
    for (const int e = s + (ph ? A1_D : A1_0); s < e || ph == 4; s += 2) {
      seg(s, 0);
      if (s == NSEG - 1) break;
      seg(s + 1, 1);
    }
    if (ph == 4) break;
    if (ph == 3 && lane == 0) ctr_add(a, CT_A1 + ct_near(~sel) + (h >> 3), 1u);
    if (n < 128) a1.finish(a, h >> 3, n, smem + 131072 + w * 8192, sel);
    a1.load(a, h >> 3, ph == 0 ? wi : (ph == 1 && wi >= 64 ? 32 + wi : 256));
    SB0;
  }
#pragma unroll
  for (int T = 0; T < 2; ++T) st16s(pk8v(acc[0][2 * T], acc[0][2 * T + 1]), ru, lane * 16 + T * 4096, x_slot(kind, NSEG - 1, h, sel) + w * 1024);
  bg_drain();
  signal(NSEG - 1);
}
constexpr int A2_CSTR = 4160;
constexpr int A2_UOFF = 4 * A2_CSTR + 16 * 80;
constexpr int A2_USTR = 80 * 80;
constexpr int A2_WLDS = 4 * A2_CSTR + 2 * A2_USTR;
__device__ __forceinline__ void a2_wave(const ChainArgs& a, int d0, int d1, char* smem, int w, Sel sel) {
  const int lane = opaque_tid(0);
  char* Wb = smem + 1024 + w * A2_WLDS;
  char* U = Wb + A2_UOFF;
  const __amdgpu_buffer_rsrc_t rf = mk_rsrc((void*)a.Assm, 4u << 20), rd_ = mk_rsrc((void*)a.AD, 2048);
  const __amdgpu_buffer_rsrc_t rkv = rsrc_u(a.sync);
  u32x4 hi[4], uv[2][4];
  u32x2 lo[4];
  unsigned dn;
  auto ld_h = [&](int d) {
#pragma unroll
    for (int i = 0; i < 4; ++i) {
      hi[i] = __builtin_amdgcn_raw_buffer_load_b128(rf, (unsigned)((255 - 64 * i - lane) * 16), (unsigned)(d * 4096), 2);
      lo[i] = __builtin_amdgcn_raw_buffer_load_b64(rf, i == 3 && lane == 63 ? 0x7fffffffu : (255 - 64 * i - lane) * 16 - 8, (unsigned)(d * 4096), 2);
    }
    dn = __builtin_amdgcn_raw_buffer_load_b16(rd_, 0, 2 * d, 0);
  };
  auto ld_u = [&](int d) {
#pragma unroll
    for (int i = 0; i < 8; ++i) uv[i & 1][i >> 1] = __builtin_amdgcn_raw_buffer_load_b128(rkv, (unsigned)max(((i >> 1) * 64 + lane) * 16 - 8 * (i & 1), 0), kv_row(d, sel), LD_AUX);
  };
  ld_h(d0);
  {
    bool dead = false;
    ctr_wait<BG_SLP>(ctr(a, CT_A1 + ct_near(~sel) + (d0 >> 8)), 96u, (GAS unsigned*)(a.sync + SY_ABORT), lane, dead);
  }
  ld_u(d0);
  if (lane < 12) *(f32x4*)(Wb + (lane / 3) * A2_CSTR + (256 + lane % 3) * 16) = vzero();
#pragma unroll
  for (int i = 0; i < 4; ++i) if (64 * (i & 1) + lane < 80) *(f32x4*)(U + (i >> 1) * A2_USTR - 16 * 80 + (64 * (i & 1) + lane) * 16) = vzero();
  const int r = lane & 15, g = lane >> 4;
#pragma unroll 1
  for (int d = d0; d < d1; d += 96) {
    u64 at[6]; (void)at;
    AST(0);
#pragma unroll
    for (int i = 0; i < 4; ++i) {
      const unsigned src[6] = {lo[i][0], lo[i][1], hi[i][0], hi[i][1], hi[i][2], hi[i][3]};
      unsigned f[10];
#pragma unroll
      for (int x = 0; x < 10; ++x) {
        if ((x & 1) == 0) f[x] = __builtin_amdgcn_alignbit(src[5 - x / 2], src[5 - x / 2], 16);
        else f[x] = __builtin_amdgcn_perm(src[5 - (x + 1) / 2], src[5 - (x - 1) / 2], 0x07060100u);
      }
#pragma unroll
      for (int m = 0; m < 4; ++m) *(u32x4*)(Wb + m * A2_CSTR + (64 * i + lane) * 16) = (u32x4){f[3 - m], f[5 - m], f[7 - m], f[9 - m]};
    }
    if (lane == 0) uv[1][0] = (u32x4){0u, 0u, uv[1][0][0], uv[1][0][1]};
#pragma unroll
    for (int i = 0; i < 8; ++i) {
      const int ch = (i >> 1) * 64 + lane;
      *(u32x4*)(U + (i & 1) * A2_USTR + (ch >> 2) * 80 + (ch & 3) * 16) = uv[i & 1][i >> 1];
    }
    const float Dd = blo(dn);
    asm volatile("" :: "v"(Dd) : "memory");
    AST(1);
    ld_h(d + 96 < d1 ? d + 96 : D_A); ld_u(d + 96 < d1 ? d + 96 : D_A);
    f32x4 acc[4][2];
#pragma unroll
    for (int i = 0; i < 8; ++i) acc[i >> 1][i & 1] = vzero();
    const char* pa = Wb + (r & 3) * A2_CSTR + 16 * (255 - (r >> 2) + g) - 3 * 1024 - 64;
    const char* pb = U + (r - 1) * 80 + 16 * g;
    bf16x8 fa[2][4], fb[2][8];
    auto rd = [&](int b, int n, int k) {
      if (n == 0 || n > 8) fa[b][n ? n - 8 : 0] = *(const bf16x8*)(pa + 1024 * (3 - (n ? n - 8 : 0)) + 64 * (1 - k));
      else fb[b][n - 1] = *(const bf16x8*)(pb + ((n - 1) & 1) * A2_USTR + 80 * (16 * ((n - 1) >> 1) + 1 - k));
    };
#pragma unroll
    for (int n = 0; n < 12; ++n) rd(0, n, 0);
    AST(2);
#pragma unroll 1
    for (int k = 0; k < 16; k += 2) {
#pragma unroll
      for (int b = 0; b < 2; ++b) {
        int q = 0;
#pragma unroll
        for (int j = 0; j < 4; ++j)
#pragma unroll
          for (int i = j; i < 4; ++i)
#pragma unroll
            for (int mt = 0; mt < 2; ++mt) {
              asm volatile("v_mfma_f32_16x16x32_bf16 %0, %1, %2, %0" : "+v"(acc[i][mt]) : "v"(fa[b][j]), "v"(fb[b][2 * (i - j) + mt]));
              SB0;
              if (q < 12) rd(b ^ 1, q, 1);
              if (q == 12) { pa -= 64; pb -= 80; }
              ++q;
              SB0;
            }
      }
    }
    AST(3);
    u32x4 kv[4];
#pragma unroll
    for (int i = 0; i < 4; ++i) kv[i] = *(const u32x4*)(U + (16 * i + r) * 80 + 16 * g);
    asm volatile("s_nop 7\n\ts_waitcnt vmcnt(0)" ::: "memory");
#pragma unroll
    for (int i = 0; i < 8; ++i) asm volatile("" : "+v"(acc[i >> 1][i & 1]));
    if (lane == 0 && d > d0) ctr_add(a, CT_A2 + ct_near(~sel) + ((d - 96) >> 6), 1u);
#pragma unroll
    for (int i = 0; i < 4; ++i) {
      const f32x4 x = acc[i][1], y = acc[i][0];
      st16s((u32x4){pk2(fmaf(blo(kv[i][0]), Dd, x[0]), fmaf(bhi(kv[i][0]), Dd, x[1])), pk2(fmaf(blo(kv[i][1]), Dd, x[2]), fmaf(bhi(kv[i][1]), Dd, x[3])),
                    pk2(fmaf(blo(kv[i][2]), Dd, y[0]), fmaf(bhi(kv[i][2]), Dd, y[1])), pk2(fmaf(blo(kv[i][3]), Dd, y[2]), fmaf(bhi(kv[i][3]), Dd, y[3]))},
            rkv, (unsigned)(i * 1024 + r * 64 + g * 16), kv_row(d, sel));
    }
    AST(4);
  }
  wait_vm<0>();
  if (lane == 0) ctr_add(a, CT_A2 + ct_near(~sel) + ((d0 + (d1 - 1 - d0) / 96 * 96) >> 6), 1u);
}
__device__ __forceinline__ void task_u(const ChainArgs& a, int h, char* smem, int wv, Gate& G) {
  const int lane = opaque_tid(0);
  const int w = wv;
  GAS unsigned* abortw = (GAS unsigned*)(a.sync + SY_ABORT);
  bool dead = false;
  char* sb = smem + w * 32768;
  const __amdgpu_buffer_rsrc_t rwo = mk_rsrc(a.Wo, 8u << 20);
  const unsigned lds0 = (unsigned)(uintptr_t)(LAS char*)sb;
  unsigned wo4[4];
#pragma unroll
  for (int i = 0; i < 4; ++i) { const int row = i * 4 + (lane >> 4); wo4[i] = row * DM * 2 + (((lane & 15) ^ swz_b(row)) * 16); }
  asm volatile("s_nop 4");
  auto stage = [&](int n) {
    const int p = (U_ORD >> (4 * n)) & 3;
#pragma unroll
    for (int i = 0; i < 16; ++i)
      dma16(rwo, wo4[i & 3], (unsigned)(((128 * h + 64 * (p & 1) + 16 * (i >> 2)) * DM + 256 * w + 128 * (p >> 1)) * 2), lds0 + (n & 1) * 16384 + i * 1024, W_NT & 2);
  };
  __builtin_amdgcn_s_waitcnt(0x0F70);
  u32x4 W[16][4];
  stage(0); stage(1);
#pragma unroll
  for (int n = 0; n < 4; ++n) {
    const int p = (U_ORD >> (4 * n)) & 3;
    if (n < 3) wait_vm<16>(); else wait_vm<0>();
#pragma unroll
    for (int t = 0; t < 8; ++t) {
      const u32x8 f = tr_frag(sb + (n & 1) * 16384, t, lane);
      u32x4 f0 = {f[0], f[1], f[2], f[3]}, f1 = {f[4], f[5], f[6], f[7]};
      asm volatile("" : "+a"(f0), "+a"(f1));
      W[8 * (p >> 1) + t][2 * (p & 1)] = f0; W[8 * (p >> 1) + t][2 * (p & 1) + 1] = f1;
    }
    asm volatile("s_waitcnt lgkmcnt(0)" ::: "memory");
    if (n + 2 < 4) stage(n + 2);
  }
  lds_barrier();
  if (!G.pass(a.sync)) return;
  SSTAMP(192 + h);
  const Sel sel = G.sel;
  const u32x4 ru = own_rsrc(a.sync, SY_BYTES);
  u32x4 V[2][8], pc[2], o[2];
  f32x4 c[2][2];
  unsigned cv, cw, vo[4], lk[2], sgo = (w | lane) ? 0x7fffffffu : SY_CT + (CT_U + ct_near(sel) + h) * 128, nd = (lane & 1) ? 3 - URING : 2;
#pragma unroll
  for (int i = 0; i < 4; ++i) { vo[i] = lane * 16 + ux_tile(16 * w + 4 * i); asm volatile("" : "+v"(vo[i])); }
#pragma unroll
  for (int i = 0; i < 2; ++i) lk[i] = SY_CT + (ct_near(~sel) + ((lane & 1) ? CT_DONE + 8 * i + (h & 7) : CT_V + h)) * 128;
  char* vl = lds_base(smem, lane * 16);
  const unsigned vl2 = lane * 16 + w * 2048;
  auto ld_v = [&](int s, bool pad = false) {
    if (pad) asm volatile("s_nop 4\n\tbuffer_load_dwordx4 %0, %2, %3, %4 offen sc1\n\tbuffer_load_dwordx4 %1, %2, %3, %4 offen offset:1024 sc1" : "=&v"(pc[0]), "=&v"(pc[1]) : "v"(vl2), "s"(ru), "s"(x_slot(2, s, h, sel)) : "memory");
    else asm volatile("buffer_load_dwordx4 %0, %2, %3, %4 offen sc1\n\tbuffer_load_dwordx4 %1, %2, %3, %4 offen offset:1024 sc1" : "=&v"(pc[0]), "=&v"(pc[1]) : "v"(vl2), "s"(ru), "s"(x_slot(2, s, h, sel)) : "memory");
  };
  auto signal = [&](int s) {
    asm volatile("buffer_store_dword %0, %1, %2, 0 offen sc1" :: "v"(s), "v"(sgo), "s"(ru) : "memory");
  };
  auto st_u = [&](int n, unsigned ub) { asm volatile("buffer_store_dwordx4 %0, %1, %2, %3 offen offset:%4 sc1" :: "v"(o[n & 1]), "v"(vo[n >> 2]), "s"(ru), "s"(ub), "n"((n & 3) * 1024) : "memory"); };
  auto fetch = [&](int s) {
    for (unsigned spins = 0;;) {
      asm volatile("buffer_load_dword %0, %1, %2, 0 offen sc1" : "=&v"(cw) : "v"(lk[0]), "s"(ru) : "memory");
      ld_v(s, true);
      asm volatile("s_waitcnt vmcnt(0)" : "+v"(pc[0]), "+v"(pc[1]), "+v"(cw) :: "memory");
      if (s && spins == 0) { __builtin_amdgcn_s_barrier(); signal(0); }
      if ((int)__builtin_amdgcn_readfirstlane(cw) >= s) break;
      __builtin_amdgcn_s_sleep(2);
      if (poll_fail(spins, abortw, lane)) { dead = true; break; }
    }
    WST(s == 0, 24);
#pragma unroll
    for (int i = 0; i < 2; ++i) *(u32x4*)(vl + 16384 + w * 2048 + i * 1024) = pc[i];
    lds_barrier();
#pragma unroll
    for (int i = 0; i < 8; ++i) V[s][i] = *(const u32x4*)(vl + 16384 + i * 1024);
  };
#pragma unroll
  for (int i = 0; i < 8; ++i) asm volatile("" : "=v"(V[0][i]));
  u64 bad = 0;
  unsigned up = 0x7f000000u;
  c[1][0] = c[1][1] = vzero();
  auto seg = [&](int s, int b) {
    const unsigned ub = s < 0 ? 0x7f000000u : ux_base(s, h, sel);
    WST(s >= 0 && s < 4, 25 + s);
#pragma unroll
    for (int n = 0; n < 16; ++n)
#pragma unroll
      for (int q = 0; q < 8; ++q) {
        const int kp = q >> 1, m = q & 1;
        if (kp == 0) asm volatile("v_mfma_f32_16x16x32_bf16 %0, %1, %2, 0" : "=&v"(c[n & 1][m]) : "v"(V[b][4 * m + kp]), "a"(W[n][kp]));
        else asm volatile("v_mfma_f32_16x16x32_bf16 %0, %1, %2, %0" : "+v"(c[n & 1][m]) : "v"(V[b][4 * m + kp]), "a"(W[n][kp]));
        SB0;
        if (q >= 2 && q < 6) { const f32x4 x = c[~n & 1][(q - 2) >> 1]; o[~n & 1][q - 2] = pk2v(x[2 * (q & 1)], x[2 * (q & 1) + 1]); }
        if (q == 6) st_u((n - 1) & 15, n ? ub : up);
        if (q == 5) asm volatile("" :: "v"(o[n & 1]));
        if (n == U_LD && q == 0) {
          if (__builtin_expect(bad != 0, 0)) {
            if (s <= U_NOW) {
              asm volatile("s_waitcnt vmcnt(%1)" : "+v"(cw) : "n"(U_LD - 2) : "memory");
              __builtin_amdgcn_s_barrier();
              signal(s - 1);
              bad = (int)__builtin_amdgcn_readfirstlane(cw) <= s;
            }
            if (bad & 1) ctr_wait<2>(ctr(a, CT_V + ct_near(~sel) + h), min(s + 2, NSEG), abortw, lane, dead);
            if (bad & 2) {
              ctr_wait<U_SLP>(ctr(a, CT_DONE + ct_near(~sel) + 8 * b + (h & 7)), max(s + 3 - URING, 0), abortw, lane, dead);
              for (int i = 0; i < U_STG * (h & 7) + U_ST0; ++i) __builtin_amdgcn_s_sleep(1);
            }
          }
          ld_v(min(s + 1u, NSEG - 1u));
        }
        if (n == 1 && q == 7) asm volatile("buffer_load_dword %0, %1, %2, 0 offen sc1" : "=&v"(cw) : "v"(s <= U_NOW ? lk[0] : 0x7fffffffu), "s"(ru) : "memory");
        if (n == U_LD && q == 7) asm volatile("buffer_load_dword %0, %1, %2, 0 offen sc1" : "=&v"(cv) : "v"(lk[b ^ 1]), "s"(ru) : "memory");
        if (n == U_SIG2 && q == 0) asm volatile("s_waitcnt vmcnt(%4)" : "+v"(pc[0]), "+v"(pc[1]), "+v"(cv), "+v"(cw) : "n"(U_SIG2 - U_LD - 1) : "memory");
        if (n == U_SIG2 && (q == 1 || q == 7)) *(u32x4*)(vl + (b ^ 1) * 8192 + w * 2048 + (q >> 2) * 1024) = pc[q >> 2];
        if (n == U_SIG2 + 1 && q == 0) { bad = __builtin_amdgcn_ballot_w64(s >= 0 && (int)cv < (s < U_NOW && lane > 1 ? NSEG : min(s + (int)nd, NSEG - 1))); asm volatile("" : "+s"(bad)); }
        if (n == U_SIG2 + 1 && q == 7) lds_barrier();
        if (n == U_SIG2 + 2 && q == 0) signal(s - 1);
        if ((n == U_SIG2 + 2 || n == U_SIG2 + 3) && (q & 1)) V[b ^ 1][4 * (n - U_SIG2 - 2) + (q >> 1)] = *(const u32x4*)(vl + (b ^ 1) * 8192 + (4 * (n - U_SIG2 - 2) + (q >> 1)) * 1024);
        SB0;
      }
    up = ub;
  };
  for (int s = -U_DRY; s < NSEG; s += 2) {
    if (__builtin_expect(s == 0, 0)) fetch(0);
    seg(s, 0);
    if (__builtin_expect(s == 0, 0)) {
      o[1] = pk8v(c[1][0], c[1][1]);
      st_u(15, up);
      fetch(1);
    }
    seg(s + 1, 1);
  }
  o[1] = pk8v(c[1][0], c[1][1]);
  st_u(15, up);
  bg_drain();
  signal(NSEG - 1);
}

__device__ __forceinline__ void task_a3(const ChainArgs& a, int dg, int t0, char* T, Sel sel) {
  const int lane = opaque_tid(0);
  const int d = dg * 64 + 8 * (lane & 7);
  const int tb = t0 + 8 * (lane >> 3);
  const __amdgpu_buffer_rsrc_t rz = rsrc_u(a.sync);
  u32x4 qq[16], zz[8][2];
#pragma unroll
  for (int i = 0; i < 16; ++i) qq[i] = LDNT((const u32x4*)(a.Aq + (long)(tb + 64 * (i >> 3) + (i & 7)) * D_A + d));
  {
    u32x4 zr[16];
    bool dead = false;
    ctr_wait<A3_SLP>(ctr(a, CT_A2 + ct_near(~sel) + dg), 64u, (GAS unsigned*)(a.sync + SY_ABORT), lane, dead);
  #pragma unroll
    for (int i = 0; i < 16; ++i) zr[i] = ld16(rz, kv_row(dg * 64, sel) + (2 * i + (lane >> 5)) * CHK + ((lane >> 4) & 1) * 4096 + (t0 + (lane & 15) * 8) * 2);
#pragma unroll
    for (int i = 0; i < 16; ++i) {
      const int row = 4 * i + (lane >> 4);
      *(u32x4*)(T + row * 256 + (((lane & 15) ^ (row >> 3)) * 16)) = zr[i];
    }
  }
#pragma unroll
  for (int c = 0; c < 8; ++c)
#pragma unroll
    for (int gq = 0; gq < 2; ++gq) zz[c][gq] = *(const u32x4*)(T + (8 * (lane & 7) + c) * 256 + (((gq * 8 + (lane >> 3)) ^ (lane & 7)) * 16));
#pragma unroll
  for (int i = 0; i < 16; ++i) {
    u32x4 o;
#pragma unroll
    for (int c = 0; c < 8; c += 2) {
      const unsigned z0 = zz[c][i >> 3][(i >> 1) & 3], z1 = zz[c + 1][i >> 3][(i >> 1) & 3];
      o[c >> 1] = pk2(blo(qq[i][c >> 1]) * ((i & 1) ? bhi(z0) : blo(z0)), bhi(qq[i][c >> 1]) * ((i & 1) ? bhi(z1) : blo(z1)));
    }
    st16g(a.out + (long)(tb + 64 * (i >> 3) + (i & 7)) * OUTW + d, o);
  }
}

__device__ __forceinline__ void part_a(const ChainArgs& a, char* smem, int x, int wi, int w, Sel sel) {
  char* T = smem + 1024 + w * A2_WLDS;
  a2_wave(a, 256 * x + wi, 256 * x + 256, smem, w, sel);
  while (true) {
    unsigned t = 0;
    if (opaque_tid(0) == 0) t = __hip_atomic_fetch_add(ctr(a, CT_TK + ct_near(~sel) + x), 1u, __ATOMIC_RELAXED, __HIP_MEMORY_SCOPE_AGENT) + 1u;
    t = __builtin_amdgcn_readfirstlane(t);
    if (t >= 64) break;
    task_a3(a, 4 * x + (t >> 4), (t & 15) * 128, T, sel);
  }
}

__global__ __launch_bounds__(256, 1) void chain_kernel(ChainArgs a) {
  extern __shared__ __attribute__((aligned(16))) char smem[];
  const int b = blockIdx.x;
  {
    u32x4 rc;
    asm volatile("s_getpc_b64 s[60:61]\n\t.Lchain_here:\n\ts_sub_u32 s60, s60, .Lchain_here-_Z12chain_kernel9ChainArgs\n\ts_subb_u32 s61, s61, 0\n\ts_and_b32 s61, s61, 0xffff\n\t"
                 "s_mov_b32 s62, .Lchain_end-_Z12chain_kernel9ChainArgs\n\ts_mov_b32 s63, 0x20000\n\t"
                 "buffer_load_dwordx4 a[252:255], %1, s[60:63], 0 offen\n\tbuffer_load_dwordx4 a[252:255], %1, s[60:63], s63 offen"
                 : "={s[60:63]}"(rc) : "v"((b >> 3) * 4096 + threadIdx.x * 16) : "scc", "a252", "a253", "a254", "a255");
  }
  int w = __builtin_amdgcn_readfirstlane(threadIdx.x >> 6);
  Gate G;
  G.entry = __builtin_amdgcn_s_memrealtime(); G.open = false; G.arr = w == 0 ? b & 7 : -1;
  G.rot = ((unsigned)__builtin_amdgcn_s_getreg(20 | (3 << 11)) - b + WRONG) & 7u;
  {
    volatile LAS unsigned* t = (volatile LAS unsigned*)smem;
    if (w == 0) { t[0] = (unsigned)G.entry; t[1] = (unsigned)(G.entry >> 32); }
    __syncthreads();
    G.entry = (u64)(unsigned)__builtin_amdgcn_readfirstlane(t[0]) | ((u64)(unsigned)__builtin_amdgcn_readfirstlane(t[1]) << 32);
    __syncthreads();
  }
  if (((b + G.rot) & 4) == 0 && b < 128) {
    const int c = (b >> 6) * 32 + 8 * ((b + G.rot) & 3) + ((b >> 3) & 7);
    if (w < 2) calibrate(a, w ? 32 + (0x3200 >> (4 * (c & 3)) & 3) : c, 2 * c + w, G.entry);
    __builtin_amdgcn_s_barrier();
  }
  if (b == 0) {
    const int lane = opaque_tid(0);
    const __amdgpu_buffer_rsrc_t rs = mk_rsrc(a.sync + SY_SET, 3 * CHK);
    st16(rs, CHK + w * 8192 + lane * 128, (u32x4){SENT, SENT, SENT, SENT});
    if (w == 0 && lane >= 2) st16(rs, lane * 128, (u32x4){SENT, SENT, SENT, SENT});
    if (w == 3) {
      u32x4 v;
      bool ok;
      do {
        v = ld16(mk_rsrc(a.sync + SY_CT + CT_CAL * 128, 8192), lane * 128);
        ok = ((u64)v[1] << 32 | v[0]) - G.entry + 2000u < (1ull << 31) && ((u64)v[3] << 32 | v[2]) - G.entry + 2000u < (1ull << 31);
      } while (!__all(ok) && __builtin_amdgcn_s_memrealtime() - G.entry < 2 * CALM);
      const u64 k = lane == 32 ? 0x3333333333333333ull : (lane == 34 || lane == 35 ? 0x1111111111111111ull << (lane & 3) : 0ull);
      const int n1 = __builtin_popcountll(k & __builtin_amdgcn_ballot_w64(ok && (v[2] & 3u) == 3u)), n0 = __builtin_popcountll(k & __builtin_amdgcn_ballot_w64(ok && (v[2] & 3u) == 2u));
      if (!ok) v[0] = 0;
      if (n0 != n1) v[0] = n1 > n0 ? 3u : 2u;
      const unsigned t = (v[0] ^ lane) & 1u;
      const bool most = __builtin_popcountll(__builtin_amdgcn_ballot_w64((v[0] & 2u) && t)) > __builtin_popcountll(__builtin_amdgcn_ballot_w64((v[0] & 2u) && !t));
      const u64 m = __builtin_amdgcn_ballot_w64((v[0] & 2u) ? v[0] & 1u : (lane ^ most) & 1u);
      if (lane == 0) __hip_atomic_store((GAS u64*)(a.sync + SY_SEL), m, __ATOMIC_RELAXED, __HIP_MEMORY_SCOPE_AGENT);
    }
    bg_drain();
    if (w == 0 && lane == 0) __hip_atomic_store((GAS u64*)(a.sync + SY_EPOCH), (__builtin_amdgcn_s_memrealtime() & ~7ull) | G.rot, __ATOMIC_RELAXED, __HIP_MEMORY_SCOPE_AGENT);
  }
  while (true) {
    const unsigned rot = G.rot;
    asm volatile("" : "+s"(w));
    const int x = (b + rot) & 7, j = b >> 3, h = 8 * (x & 3) + (j & 7), role = (x & 4) + (j >> 3);
    if ((role == 0 || role == 7) && w >= 2) {
      const int n = opaque_tid(w - 2);
      const unsigned x0 = __builtin_amdgcn_raw_buffer_load_b32(mk_rsrc(a.sync, 106u << 20), n * PAGE + 4096 + (j & 31) * 128, 0, 2);
      const unsigned x1 = __builtin_amdgcn_raw_buffer_load_b32(mk_rsrc(a.out, 8u << 20), n * PAGE + (j & 31) * 128, 0, 2);
      asm volatile("" :: "v"(x0), "v"(x1));
    }
    if (role < 2) role_cb(a, 32 * role + h, smem, w, G);
    else if (role == 2) role_qk(a, h, smem, w, G);
    else if (role == 3) role_vo(a, h, smem, w, G);
    else if (role == 4) task_u(a, h, smem, w, G);
    else task_w(a, 7 - role, h, smem, w, G);
    if (G.rot == rot && role > 4) part_a(a, smem, x & 3, 4 * (j - 8) + w, w, G.sel);
    if (G.rot == rot) break;
    __syncthreads();
  }
  asm volatile(".Lchain_end:");
}

extern "C" void kernel_launch(void* const* d_in, const int* in_sizes, int n_in,
                              void* d_out, int out_size, void* d_ws, size_t ws_size,
                              hipStream_t stream) {
  const u16* A_q = (const u16*)d_in[0];
  const u16* A_k = (const u16*)d_in[1];
  const u16* A_v = (const u16*)d_in[2];
  const u16* A_shift = (const u16*)d_in[3];
  const u16* A_ssmk = (const u16*)d_in[4];
  const u16* A_D = (const u16*)d_in[5];
  const u16* B_x = (const u16*)d_in[6];
  const u16* B_mem0 = (const u16*)d_in[7];
  const u16* B_Wq = (const u16*)d_in[8];
  const u16* B_Wk = (const u16*)d_in[9];
  const u16* B_Wv = (const u16*)d_in[10];
  const u16* B_Wo = (const u16*)d_in[11];
  u16* out = (u16*)d_out;
  char* ws = (char*)(((uintptr_t)d_ws + (2u << 20) - 1) & ~(uintptr_t)((2u << 20) - 1)) + WSOFF * MB;
  hipFuncSetAttribute((const void*)chain_kernel, hipFuncAttributeMaxDynamicSharedMemorySize, CHAIN_LDS);
  ChainArgs ca;
  ca.prof = (u64*)((char*)d_ws + PROF_AT);
  ca.mem0 = B_mem0; ca.Wq = B_Wq; ca.Wk = B_Wk; ca.Wv = B_Wv; ca.Wo = B_Wo; ca.X = B_x;
  ca.out = out; ca.sync = ws;
  ca.Aq = A_q; ca.Ak = A_k; ca.Av = A_v; ca.Ashift = A_shift; ca.Assm = A_ssmk; ca.AD = A_D;
  chain_kernel<<<256, 256, CHAIN_LDS, stream>>>(ca);
}
```

```cpp
#define KQ_D 350
#include <hip/hip_runtime.h>
#include <hip/hip_bf16.h>
#include <stdint.h>
#include <type_traits>

constexpr int S_LEN = 2048;
constexpr int D_A   = 1024;
constexpr int DM    = 1024;
constexpr int NH    = 32;
constexpr int DH    = 128;
constexpr int HD    = NH * DH;
constexpr int MEMR  = 8;
constexpr int SEG   = 32;
constexpr int NSEG  = S_LEN / SEG;
constexpr int OUTW  = 2048;

typedef unsigned short u16;
typedef unsigned long long u64;
typedef short bf16x8 __attribute__((ext_vector_type(8)));
typedef short bf16x4 __attribute__((ext_vector_type(4)));
typedef float f32x4 __attribute__((ext_vector_type(4)));
typedef float f32x16 __attribute__((ext_vector_type(16)));
typedef unsigned u32x4 __attribute__((ext_vector_type(4)));
typedef unsigned u32x2 __attribute__((ext_vector_type(2)));
#define LAS __attribute__((address_space(3)))
#define GAS __attribute__((address_space(1)))

constexpr size_t MB = 1u << 20;
constexpr size_t PROF_AT = 0;

__device__ __forceinline__ float bf2f(u16 b) { return __uint_as_float(((unsigned)b) << 16); }
__device__ __forceinline__ u16 f2bf(float f) { __bf16 b = (__bf16)f; return __builtin_bit_cast(u16, b); }
__device__ __forceinline__ float rbf(float f) { return bf2f(f2bf(f)); }
__device__ __forceinline__ unsigned pk2(float a, float b) {
  typedef float f2 __attribute__((ext_vector_type(2)));
  typedef __bf16 b2 __attribute__((ext_vector_type(2)));
  f2 v = {a, b}; b2 r = __builtin_convertvector(v, b2); return __builtin_bit_cast(unsigned, r);
}

__device__ __forceinline__ int opaque_tid(int wv) {
  int l;
  asm volatile("v_mbcnt_lo_u32_b32 %0, -1, 0\n\tv_mbcnt_hi_u32_b32 %0, -1, %0" : "=v"(l));
  return wv * 64 + l;
}
__device__ __forceinline__ int swz_b(int row) { return ((row & 3) << 2) | ((row >> 2) & 3); }

#define KNOB(name, v) constexpr int name = v
KNOB(K_DEF, 70);
KNOB(W_STEP, 33); KNOB(W_OFF, 56);
#define RHOV 3
KNOB(RHO, RHOV);
#define CDUR 1600
#define U_SLP 8
#define U_STG 0
#define U_ST0 0
#define WSOFF 122
#define START_SLP 3
#define QK_D 0
#define U_DRY 0
#define A1_0 20
#define A1_D 4
#define PSEG 1
#define KQ_NOW 2
#define CALN 3
#define CALM 500u
#define CALT 680u
#define W_NOW 4
#define U_SIG2 11
#define U_LD 5
#define U_NOW 6
#define WRONG 0
#define OUTP "sc1"
#define LDNT(p) __builtin_nontemporal_load(p)
constexpr int CHK      = 16384;
constexpr int PAGE     = 2 << 20;
constexpr int SY_UM    = 34 * PAGE;
constexpr int SY_HOT   = 32 * PAGE;
constexpr int H_MEM    = 0;
constexpr int H_P      = 16 * CHK;
constexpr int H_ANN    = 28 * CHK;
constexpr int H_DLT    = 32 * CHK;
constexpr int H_TR     = 33 * CHK;
constexpr int H_PX     = 64 * CHK;
constexpr int H_BYTES  = 128 * CHK;
constexpr int URING    = 16;
constexpr int SY_SET   = 52 * PAGE;
constexpr int SY_EPOCH = SY_SET + 128;
constexpr int SY_ABORT = SY_SET + 256;
constexpr int SY_SEL   = SY_EPOCH + 8;
constexpr int SY_CT    = SY_SET + CHK;
constexpr int CT_Q = 0, CT_K = 32, CT_U = 128;
constexpr int CT_V = 0, CT_DONE = 32, CT_A2 = 48, CT_A1 = 128, CT_TK = 132;
constexpr int CT_ARR = -124, CT_INIT = -116, CT_CAL = 256;
constexpr int SY_KV    = 48 * PAGE;
constexpr int SY_BYTES = SY_CT + (CT_CAL + 64) * 128;
constexpr unsigned SENT = 0xFFFFFFFFu;
constexpr unsigned SPIN_CAP = 1u << 19;
constexpr int NLF = 16;
constexpr int L_WTS = 0;
constexpr int L_MEM = L_WTS + 4 * NLF * 2048;
constexpr int L_XCH = L_MEM + 16384;
constexpr int L_XK  = L_XCH + 2048;
constexpr int L_KX  = L_XCH + 4096;
constexpr int L_PRF = L_KX + 8192;
constexpr int XRS = 40;
constexpr int CHAIN_LDS = 160 * 1024;
static_assert(L_XCH + 128 * XRS <= L_PRF && L_PRF + 4096 <= CHAIN_LDS, "lds");

typedef __bf16 bfv8 __attribute__((ext_vector_type(8)));
typedef __bf16 bfv16 __attribute__((ext_vector_type(16)));
typedef unsigned u32x8 __attribute__((ext_vector_type(8)));
typedef unsigned u32x12 __attribute__((ext_vector_type(12)));
typedef unsigned u32x16 __attribute__((ext_vector_type(16)));
typedef unsigned u32x32 __attribute__((ext_vector_type(32)));
struct Wts { u32x8 a[32]; u32x32 v[4]; };

#define STAMPL(i) do {} while (0)
#define PVAL(i, v) do {} while (0)
#define PDECL(base, str) do {} while (0)
#define PZERO do {} while (0)
#define PDUMP(mask) do {} while (0)
#define TA(x) do {} while (0)
#define TB(x) TS(x)
#define TS(x) do {} while (0)
#define SSTAMP(i) do {} while (0)
#define WST(c, i) do {} while (0)
#define AST(i) do {} while (0)
struct ChainArgs {
  u64* prof;
  const u16* mem0; const u16* Wq; const u16* Wk; const u16* Wv; const u16* Wo; const u16* X;
  u16* out; char* sync;
  const u16* Aq; const u16* Ak; const u16* Av; const u16* Ashift; const u16* Assm; const u16* AD;
};
__device__ __forceinline__ GAS unsigned* ctr(const ChainArgs& a, int i) { return (GAS unsigned*)(a.sync + SY_CT + i * 128); }
__device__ __forceinline__ void ctr_add(const ChainArgs& a, int i, unsigned v) {
  __hip_atomic_fetch_add(ctr(a, i), v, __ATOMIC_RELAXED, __HIP_MEMORY_SCOPE_AGENT);
}

__device__ __forceinline__ __amdgpu_buffer_rsrc_t mk_rsrc(const void* p, unsigned bytes) {
  const u64 v = (u64)p;
  const unsigned lo = (unsigned)__builtin_amdgcn_readfirstlane((unsigned)v), hi = (unsigned)__builtin_amdgcn_readfirstlane((unsigned)(v >> 32));
  const u64 u = (u64)lo | ((u64)hi << 32);
  return __builtin_amdgcn_make_buffer_rsrc((void*)u, 0, bytes, 0x00020000);
}
__device__ __forceinline__ u32x4 own_rsrc(const void* p, unsigned bytes) {
  u32x4 r = {(unsigned)__builtin_amdgcn_readfirstlane((unsigned)(u64)p), (unsigned)__builtin_amdgcn_readfirstlane((unsigned)((u64)p >> 32)) & 0xffffu, bytes, 0x00020000u};
  asm volatile("s_nop 4" : "+s"(r));
  return r;
}
constexpr int LD_AUX = 16, ST_AUX = 16;
__device__ __forceinline__ u32x4 ld16(__amdgpu_buffer_rsrc_t rs, unsigned off) {
  return __builtin_amdgcn_raw_buffer_load_b128(rs, off, 0, LD_AUX);
}
__device__ __forceinline__ void st16(__amdgpu_buffer_rsrc_t rs, unsigned off, u32x4 v) {
  __builtin_amdgcn_raw_buffer_store_b128(v, rs, off, 0, ST_AUX);
}
__device__ __forceinline__ void st16s(u32x4 v, __amdgpu_buffer_rsrc_t rs, unsigned voff, unsigned soff) {
  __builtin_amdgcn_raw_buffer_store_b128(v, rs, voff, soff, ST_AUX);
  asm volatile("s_nop 1" :: "v"(v) : "memory");
}
__device__ __forceinline__ void st8(__amdgpu_buffer_rsrc_t rs, unsigned off, u32x2 v) {
  __builtin_amdgcn_raw_buffer_store_b64(v, rs, off, 0, ST_AUX);
}
__device__ __forceinline__ void st4(__amdgpu_buffer_rsrc_t rs, unsigned off, unsigned v) {
  __builtin_amdgcn_raw_buffer_store_b32(v, rs, off, 0, ST_AUX);
}
__device__ __forceinline__ unsigned ld4(__amdgpu_buffer_rsrc_t rs, unsigned off) {
  return __builtin_amdgcn_raw_buffer_load_b32(rs, off, 0, LD_AUX);
}
constexpr int TU = 16;
__device__ __forceinline__ unsigned cyc32() { return (unsigned)__builtin_amdgcn_s_memtime(); }
__device__ __forceinline__ unsigned to_sgpr(unsigned x) {
  unsigned r;
  asm volatile("s_nop 0\n\tv_readfirstlane_b32 %0, %1\n\ts_nop 4" : "=s"(r) : "v"(x));
  return r;
}
struct Clk {
  unsigned ce, te;
  unsigned c2t, t2c;
  __device__ __forceinline__ void edge(unsigned& c, unsigned& t) {
    u64 c0, r0, r1, r2, r3, r4, r5;
    asm volatile("s_memtime %0\n\ts_memrealtime %1\n\ts_nop 4\n\ts_memrealtime %2\n\ts_nop 4\n\ts_memrealtime %3\n\ts_nop 4\n\t"
                 "s_memrealtime %4\n\ts_nop 4\n\ts_memrealtime %5\n\ts_nop 4\n\ts_memrealtime %6\n\ts_waitcnt lgkmcnt(0)"
                 : "=&s"(c0), "=&s"(r0), "=&s"(r1), "=&s"(r2), "=&s"(r3), "=&s"(r4), "=&s"(r5) :: "memory");
    const unsigned x = (unsigned)r0;
    const unsigned n = ((unsigned)r1 == x) + ((unsigned)r2 == x) + ((unsigned)r3 == x) + ((unsigned)r4 == x) + ((unsigned)r5 == x);
    c = to_sgpr((unsigned)c0 + 24u * n); t = (x + 4u) * TU;
  }
  __device__ __forceinline__ void init() { edge(ce, te); c2t = 44000u; t2c = 97600u; }
  __device__ __forceinline__ void sync() {
    unsigned c, t;
    edge(c, t);
    set(c, t);
  }
  __device__ __forceinline__ void set(unsigned c, unsigned t) {
    if (c == ce) return;
    const float q = (float)(t - te) * __builtin_amdgcn_rcpf((float)(c - ce));
    const float qc = fminf(fmaxf(q, 0.51f), 1.99f);
    c2t = to_sgpr((unsigned)(qc * 65536.f));
    t2c = to_sgpr((unsigned)(__builtin_amdgcn_rcpf(qc) * 65536.f));
    ce = c; te = t;
  }
  __device__ __forceinline__ unsigned time(unsigned c) const { return te + ((min(c - ce, 30000u) * c2t) >> 16); }
  __device__ __forceinline__ unsigned now() const { return time(cyc32()); }
  __device__ __forceinline__ unsigned target(unsigned t) const {
    return to_sgpr(ce + (((unsigned)min(max((int)(t - te), 0), 20000) * t2c) >> 16));
  }
  __device__ __forceinline__ void wait(unsigned t) const { wait_c(target(t)); }
  __device__ __forceinline__ void wait_c(unsigned c) const {
    unsigned y;
    asm volatile("1:\n\ts_memtime vcc\n\ts_waitcnt lgkmcnt(0)\n\ts_sub_i32 %0, %1, vcc_lo\n\ts_cmp_gt_i32 %0, 128\n\ts_cbranch_scc1 1b\n\t"
                 "2:\n\ts_sub_i32 %0, %0, %2\n\ts_cmp_gt_i32 %0, %3\n\ts_cbranch_scc1 2b"
                 : "=&s"(y) : "s"(c), "n"(W_STEP), "n"(W_OFF) : "memory", "scc", "vcc");
  }
};
__device__ __forceinline__ unsigned nosent(unsigned d) { return min(d, SENT - 1u); }
__device__ __forceinline__ int arow(int row, int half) { return 4 * (row >> 1) + 2 * half + (row & 1); }
__device__ __forceinline__ unsigned umax4(u32x4 v) { return max(max(v[0], v[1]), max(v[2], v[3])); }
__device__ __forceinline__ void stamp_again(GAS u64* p) {
  __hip_atomic_store(p, (__builtin_amdgcn_s_memrealtime() & ~7ull) | (__hip_atomic_load(p, __ATOMIC_RELAXED, __HIP_MEMORY_SCOPE_AGENT) & 7ull),
                     __ATOMIC_RELAXED, __HIP_MEMORY_SCOPE_AGENT);
}
__device__ __forceinline__ bool poll_fail(unsigned& spins, GAS unsigned* abortw, int lane) {
  ++spins;
  if (spins > SPIN_CAP) {
    if (lane == 0) __hip_atomic_store(abortw, 1u, __ATOMIC_RELAXED, __HIP_MEMORY_SCOPE_AGENT);
    return true;
  }
  if ((spins & 511u) == 0u && lane == 0) {
    unsigned n = 0;
    for (int i = 0; i < 8; ++i) n += __hip_atomic_load(abortw + (SY_CT - SY_ABORT + (CT_ARR + i) * 128) / 4, __ATOMIC_RELAXED, __HIP_MEMORY_SCOPE_AGENT) + 1u;
    if (n < 256u) stamp_again((GAS u64*)(abortw - 32));
  }
  if ((spins & 63u) == 0u)
    return __builtin_amdgcn_readfirstlane(__hip_atomic_load(abortw, __ATOMIC_RELAXED, __HIP_MEMORY_SCOPE_AGENT)) != SENT;
  return false;
}
#define TP(k) do {} while (0)
struct Gate {
  u64 tx; u64 entry, sel; int arr; unsigned rot; bool open;
  __device__ __forceinline__ bool look(char* sync, u32x4 v) {
    if (open) return true;
    for (unsigned spins = 1;; ++spins) {
#pragma unroll
      for (int i = 0; i < 4; ++i) v[i] = __builtin_amdgcn_readfirstlane(v[i]);
      if (((u64)v[1] << 32 | v[0]) - entry + 2000u < (1ull << 31) || spins > 4 * SPIN_CAP) break;
      if (spins > 1) __builtin_amdgcn_s_sleep(8);
      v = ld16(mk_rsrc(sync + SY_EPOCH, 16), 0);
    }
    asm volatile("" ::: "memory");
    sel = (u64)v[3] << 32 | v[2];
    const bool same = rot == (v[0] & 7u);
    rot = v[0] & 7u; open = true;
    return same;
  }
  __device__ __forceinline__ void ask(char* sync, u32x4& v) { asm volatile("s_nop 4\n\tbuffer_load_dwordx4 %0, off, %1, 0 sc1" : "=v"(v) : "s"(own_rsrc(sync + SY_EPOCH, 16)) : "memory"); }
  __device__ __forceinline__ void arrive(char* sync) {
    if (arr >= 0 && opaque_tid(0) == 0) __hip_atomic_fetch_add((GAS unsigned*)(sync + SY_CT + (CT_ARR + arr) * 128), 1u, __ATOMIC_RELAXED, __HIP_MEMORY_SCOPE_AGENT);
    arr = -1;
  }
  __device__ __forceinline__ bool pass(char* sync, u32x4 v = {0u, 0u, 0u, 0u}) { const bool r = look(sync, v); arrive(sync); return r; }
};
__device__ __forceinline__ unsigned ann_val(unsigned t, int s) { return (t << 8) | (unsigned)s; }
__device__ __forceinline__ unsigned tr_place(int s) { return (s & 2) * (CHK / 2) + (s & 1) * 4096; }
__device__ __forceinline__ unsigned die_half() { return ((unsigned)__builtin_amdgcn_s_getreg(20 | (3 << 11)) >> 2) & 1u; }
typedef u64 Sel;
template <class F>
__device__ __forceinline__ void start_wait(char* sync, GAS unsigned* abortw, int lane, bool& dead, int ci, unsigned n, F f) {
  unsigned spins = 0, v;
  const bool m = lane < 4 || ci < 0;
  while (true) {
    v = ld4(mk_rsrc(sync + SY_SET, SY_BYTES - SY_SET), lane < 6 ? CHK + (m ? CT_INIT + (lane & 3) : ci + 32 * (lane & 1)) * 128 : 0x7fffffffu) + 1u;
    asm volatile("" ::: "memory");
    f();
    if (__all(lane > 5 || v >= (m ? 16u : n))) break;
    __builtin_amdgcn_s_sleep(START_SLP);
    if (dead || poll_fail(spins, abortw, lane)) { dead = true; break; }
  }
}
__device__ __forceinline__ __amdgpu_buffer_rsrc_t rsrc_u(char* sync) { return mk_rsrc((void*)sync, SY_SET); }
__device__ __forceinline__ unsigned near_half(unsigned off, u64 sel) { return off + ((unsigned)(sel >> ((off >> 21) & 63)) & 1u) * 8192; }
__device__ __forceinline__ unsigned x_slot(int kind, int s, int h, Sel sel) {
  return kind == 2 ? near_half(32 * PAGE + (s * 32 + h) * CHK, ~sel) : near_half((s * 64 + 2 * h + 1 - kind) * CHK, sel);
}
__device__ __forceinline__ int ct_near(Sel sel) { return 64 * ((unsigned)(sel >> (SY_SET / PAGE)) & 1u); }
__device__ __forceinline__ unsigned kv_row(int d, Sel sel) { return near_half(SY_KV + (d >> 1) * CHK, ~sel) + (d & 1) * 4096; }
__device__ __forceinline__ int hpos(int h) { return (h & 3) * 8 + (h >> 2); }
constexpr unsigned UXH = 8 * CHK;
__device__ __forceinline__ unsigned ux_base(int s, int h, Sel sel) { return near_half(((s & (URING - 1)) * 32 + h) * UXH, ~sel); }
__device__ __forceinline__ constexpr unsigned ux_tile(int t) { return (t >> 3) * CHK + (t & 7) * 1024; }
__device__ __forceinline__ __amdgpu_buffer_rsrc_t rsrc_hot(char* sync, Sel sel) {
  return mk_rsrc((void*)(sync + near_half(SY_HOT, sel)), H_BYTES - 8192);
}
template <int SLP = 4>
__device__ __forceinline__ void ctr_wait(GAS unsigned* c, unsigned need, GAS unsigned* abortw, int lane, bool& dead) {
  unsigned spins = 0;
  while (true) {
    unsigned v = __builtin_amdgcn_readfirstlane(__hip_atomic_load(c, __ATOMIC_RELAXED, __HIP_MEMORY_SCOPE_AGENT)) + 1u;
    if (v >= need) break;
    __builtin_amdgcn_s_sleep(SLP);
    if (dead || poll_fail(spins, abortw, lane)) { dead = true; break; }
  }
}
template <int N> __device__ __forceinline__ void wait_vm() { asm volatile("s_waitcnt vmcnt(%0)" :: "n"(N) : "memory"); }
__device__ __forceinline__ void lds_barrier() {
  asm volatile("s_waitcnt lgkmcnt(0)" ::: "memory");
  __builtin_amdgcn_s_barrier();
  asm volatile("" ::: "memory");
}
__device__ __forceinline__ f32x4 vzero() { float z; asm volatile("v_mov_b32 %0, 0" : "=v"(z)); f32x4 r = {z, z, z, z}; asm volatile("" : "+v"(r)); return r; }
__device__ __forceinline__ f32x4 smf(u32x4 a, u32x8 b, f32x4 c, int idx) {
  return __builtin_amdgcn_smfmac_f32_16x16x64_bf16(__builtin_bit_cast(bfv8, a), __builtin_bit_cast(bfv16, b), c, idx, 0, 0);
}
__device__ __forceinline__ float red16max(float x) { auto r = __builtin_amdgcn_permlane16_swap(__float_as_uint(x), __float_as_uint(x), false, false); return fmaxf(__uint_as_float(r[0]), __uint_as_float(r[1])); }
__device__ __forceinline__ float red32max(float x) { auto r = __builtin_amdgcn_permlane32_swap(__float_as_uint(x), __float_as_uint(x), false, false); return fmaxf(__uint_as_float(r[0]), __uint_as_float(r[1])); }
__device__ __forceinline__ float red16add(float x) { auto r = __builtin_amdgcn_permlane16_swap(__float_as_uint(x), __float_as_uint(x), false, false); return __uint_as_float(r[0]) + __uint_as_float(r[1]); }
__device__ __forceinline__ float red32add(float x) { auto r = __builtin_amdgcn_permlane32_swap(__float_as_uint(x), __float_as_uint(x), false, false); return __uint_as_float(r[0]) + __uint_as_float(r[1]); }
constexpr bool blk_in_lds(int R, int b) {
  return R == 0 ? ((b & 7) == 1 || (b & 7) == 4) : (b < 16 ? (b & 3) == 0 : (b & 3) == 3);
}
constexpr int lds_index(int R, int b) { int n = 0; for (int i = 0; i < b; ++i) if (blk_in_lds(R, i)) ++n; return n; }
constexpr bool frag_in_lds(int R, int f) { return blk_in_lds(R, f >> 1); }
constexpr int frag_reg_slot(int R, int f) { int n = 0; for (int i = 0; i < f; ++i) if (!frag_in_lds(R, i)) ++n; return n; }

constexpr int m_block(int R, int m, int i) {
  return R == 0 ? 2 * (i < 12 ? (m ? i + i / 3 + 1 : i + (i + 1) / 3) : 4 * (i - 12) + 2 - 2 * m) + m : m ? 16 + 4 * (i & 3) + (i >> 2) : (i < 12 ? i + i / 3 + 1 : 4 * (i - 12));
}
#define QK_REV 0
#define W_BV 0xba9876543210fedcull
#define VO_ORD 3
constexpr int w_block(int R, int i) {
  if (R && VO_ORD == 2) return i < 12 ? m_block(1, 0, 11 - i) : m_block(1, 1, i - 12);
  if (R && VO_ORD == 3) return i & 1 ? m_block(1, 1, i >> 1) : m_block(1, 0, 11 - (i >> 1));
  if (!R && QK_REV) return m_block(0, i & 1, 11 - (i >> 1));
  return R && VO_ORD ? m_block(R, i < 12, i % 12) : m_block(R, i & 1, i >> 1);
}
#define W_BG 0xfedcba9876543210ull
#define W_NT 0
#define U_ORD 0x3210
constexpr u64 bg_order(int m) { u64 r = 0; for (int n = 0; n < 16; ++n) r |= (u64)(m_block(0, m > 0, ((m == 2 ? W_BV : W_BG) >> (4 * n)) & 15) >> 1) << (4 * n); return r; }
struct WSched { int ord[32]; int slot[32]; int issue[32]; int out[32]; int reg[32]; };
constexpr WSched make_wsched(int R) {
  WSched S{};
  int n = 0;
  for (int i = 0; i < 24; ++i) S.ord[n++] = w_block(R, i);
  for (int i = 0; i < n; ++i) S.reg[i] = frag_reg_slot(R, 2 * S.ord[i]);
  const int npure = n;
  for (int b = 0; b < 32; ++b) if (blk_in_lds(R, b)) S.ord[n++] = b;
  int seq[32] = {};
  int nissued = 0, next = 0;
  for (int i = 0; i < 10; ++i) { S.slot[i] = (i + 10 - npure % 10) % 10; S.issue[i] = -1; seq[i] = nissued++; }
  next = 10;
  S.out[0] = nissued - 1;
  for (int c = 0; c <= npure; ++c) {
    if (c >= 1) {
      const int k = S.slot[c - 1];
      if (next < npure) { S.slot[next] = k; S.issue[next] = c; seq[next] = nissued++; ++next; }
      else if (k < NLF / 2) { S.slot[npure + k] = k; S.issue[npure + k] = c; seq[npure + k] = nissued++; }
    }
    if (c + 1 < npure) S.out[c + 1] = nissued - 1 - seq[c + 1];
  }
  return S;
}
__device__ __forceinline__ int kcol(int k) { return 8 * ((k >> 1) & 1) + ((k & 1) ? 7 - (k >> 2) : k >> 2); }
template <bool PERM = false>
__device__ __forceinline__ u32x8 tr_frag(const char* sb, int tile, int lane) {
  u32x8 f;
#pragma unroll
  for (int q = 0; q < 4; ++q) {
    int row = (q >> 1) * 32 + 8 * (lane >> 4) + 4 * (q & 1) + ((lane & 15) >> 2);
    if (PERM) row = (row & ~15) + kcol(row & 15);
    int p = lane & 3;
    int lc = 2 * tile + (p >> 1);
    int addr = row * 256 + ((lc ^ swz_b(row)) * 16) + 8 * (p & 1);
    bf16x4 v = __builtin_amdgcn_ds_read_tr16_b64_v4i16((LAS bf16x4*)(sb + addr));
    u32x2 u = __builtin_bit_cast(u32x2, v);
    f[2 * q] = u[0]; f[2 * q + 1] = u[1];
  }
  return f;
}
__device__ __forceinline__ void dma16(__amdgpu_buffer_rsrc_t rs, unsigned voff, unsigned soff, unsigned lds, bool nt = false, bool pad = true) {
  if (nt) asm volatile("s_mov_b32 m0, %0\n\ts_nop 3\n\tbuffer_load_dwordx4 %1, %2, %3 offen nt lds" :: "s"(lds), "v"(voff), "s"(rs), "s"(soff) : "m0", "memory");
  else if (pad) asm volatile("s_mov_b32 m0, %0\n\ts_nop 3\n\tbuffer_load_dwordx4 %1, %2, %3 offen lds" :: "s"(lds), "v"(voff), "s"(rs), "s"(soff) : "m0", "memory");
  else asm volatile("s_mov_b32 m0, %0\n\ts_nop 0\n\tbuffer_load_dwordx4 %1, %2, %3 offen lds" :: "s"(lds), "v"(voff), "s"(rs), "s"(soff) : "m0", "memory");
}
__device__ __forceinline__ u32x8 tr_frag2(const char* sb, int tile, int f, int lane) {
  u32x8 r;
#pragma unroll
  for (int c = 0; c < 2; ++c)
#pragma unroll
    for (int t = 0; t < 2; ++t) {
      const int row = 32 * c + 16 * f + 4 * (lane >> 4) + ((lane & 15) >> 2);
      const int p = lane & 3, lc = 2 * (tile + t) + (p >> 1);
      const u32x2 u = __builtin_bit_cast(u32x2, __builtin_amdgcn_ds_read_tr16_b64_v4i16((LAS bf16x4*)(sb + row * 256 + ((lc ^ swz_b(row)) * 16) + 8 * (p & 1))));
      r[4 * c + t] = u[0]; r[4 * c + 2 + t] = u[1];
    }
  return r;
}
template <int ROLE>
__device__ __forceinline__ void load_weights(const ChainArgs& a, int h, int w, int lane, char* smem, Wts& W, Gate& G, u32x4& gv) {
#pragma unroll
  for (int i = 0; i < 4; ++i) asm volatile("" : "=v"(W.v[i]));
#pragma unroll
  for (int i = 0; i < 32; ++i) asm volatile("" : "=a"(W.a[i]));
  constexpr WSched S = make_wsched(ROLE);
  constexpr int NP = 32 - NLF / 2;
  const __amdgpu_buffer_rsrc_t r0 = mk_rsrc(ROLE == 0 ? a.Wq : a.Wv, 8u << 20), r1 = mk_rsrc(ROLE == 0 ? a.Wk : a.Wo, 8u << 20);
  const unsigned lds0 = (unsigned)(uintptr_t)(LAS char*)smem;
  unsigned vo[2][4];
#pragma unroll
  for (int j = 0; j < 4; ++j) {
    const int row = (w * 4 + j) * 4 + (lane >> 4), lc = (lane & 15) ^ swz_b(row);
    vo[0][j] = row * HD * 2 + lc * 16; vo[1][j] = row * DM * 2 + lc * 16;
  }
  asm volatile("s_nop 4");
  auto stage = [&](int i) {
    const int blk = S.ord[i];
    const bool second = ROLE == 0 ? (blk & 1) : blk >= 16, wo = ROLE == 1 && second;
    const unsigned so = wo ? (unsigned)(((128 * h + 64 * (blk & 1)) * DM + 128 * (((blk - 16) >> 1) ^ (2 * (h & 3)))) * 2)
                           : (unsigned)((64 * ((ROLE == 0 ? blk >> 1 : blk) ^ (h & RHO)) * HD + 128 * h) * 2);
#pragma unroll
    for (int j = 0; j < 4; ++j) dma16(second ? r1 : r0, vo[wo][j], so, lds0 + 16384 * S.slot[i] + (w * 4 + j) * 1024, W_NT & (ROLE ? 8 : 1));
  };
#pragma unroll
  for (int i = 0; i < 32; ++i) if (S.issue[i] == -1) stage(i);
#pragma unroll
  for (int c = 0; c < NP; ++c) {
    asm volatile("s_waitcnt vmcnt(%0)" :: "n"(4 * S.out[c]) : "memory");
    lds_barrier();
#pragma unroll
    for (int i = 0; i < 32; ++i) if (S.issue[i] == c) stage(i);
    const char* sb = smem + 16384 * S.slot[c];
#pragma unroll
    for (int t = 0; t < 2; ++t) {
      u32x8 f = (ROLE == 1 && S.ord[c] >= 16) ? tr_frag2(sb, 2 * w, t, lane) : tr_frag<true>(sb, 2 * w + t, lane);
      if (S.reg[c] < 32) {
#pragma unroll
        for (int i = 0; i < 8; ++i) asm volatile("v_accvgpr_write_b32 a[%1], %0" :: "v"(f[i]), "n"(8 * ((S.reg[c] + t) & 31) + i));
      }
      else {
#pragma unroll
        for (int i = 0; i < 8; ++i) W.v[(S.reg[c] >> 2) & 3][8 * ((S.reg[c] + t) & 3) + i] = f[i];
      }
    }
    if (S.reg[c] >= 32) asm volatile("" : "+v"(W.v[(S.reg[c] >> 2) & 3]));
  }
  static_assert(S.issue[31] < NP && S.issue[NP] < S.issue[31], "resident blocks are issued in slot order inside the loop");
  G.ask(a.sync, gv);
#pragma unroll
  for (int j = 0; j < NLF / 2; ++j) {
    asm volatile("s_waitcnt vmcnt(%0)" :: "n"(4 * (NLF / 2 - 1 - j) + 1) : "memory");
    lds_barrier();
    const bool two = ROLE == 1 && S.ord[NP + j] >= 16;
    u32x8 f0 = two ? tr_frag2(smem + 16384 * j, 2 * w, 0, lane) : tr_frag<true>(smem + 16384 * j, 2 * w, lane);
    u32x8 f1 = two ? tr_frag2(smem + 16384 * j, 2 * w, 1, lane) : tr_frag<true>(smem + 16384 * j, 2 * w + 1, lane);
    lds_barrier();
    char* d = smem + 16384 * j + w * 4096 + lane * 16;
    *(u32x4*)d = (u32x4){f0[0], f0[1], f0[2], f0[3]};
    *(u32x4*)(d + 1024) = (u32x4){f0[4], f0[5], f0[6], f0[7]};
    *(u32x4*)(d + 2048) = (u32x4){f1[0], f1[1], f1[2], f1[3]};
    *(u32x4*)(d + 3072) = (u32x4){f1[4], f1[5], f1[6], f1[7]};
  }
  asm volatile("s_waitcnt vmcnt(0)" : "+v"(gv) :: "memory");
  lds_barrier();
}
template <int... Is> struct iseq {};
template <int N, int... Is> struct mkseq : mkseq<N - 1, N - 1, Is...> {};
template <int... Is> struct mkseq<0, Is...> { typedef iseq<Is...> type; };

#define SB0 __builtin_amdgcn_sched_barrier(0)
#define CAT8(x, y) ((u32x8){x[0], x[1], x[2], x[3], y[0], y[1], y[2], y[3]})
template <class F, int... I>
__device__ __forceinline__ void for_slots(iseq<I...>, F f) { (f(std::integral_constant<int, I>{}), ...); }
#define QK_BODY \
  "s_cmp_eq_u32 %[wv], 1\n\t" \
  "s_cbranch_scc1 91f\n\t" \
  "s_cmp_eq_u32 %[wv], 2\n\t" \
  "s_cbranch_scc1 92f\n\t" \
  "s_cmp_eq_u32 %[wv], 3\n\t" \
  "s_cbranch_scc1 93f\n\t" \
  "90:\n\t" \
  "s_mov_b32 s58, s42\n\t" \
  "20:\n\t" \
  "s_and_b32 s76, s44, 7\n\t" \
  "s_lshl_b32 s76, s76, 15\n\t" \
  "s_add_i32 s40, s54, s76\n\t" \
  "s_and_b32 s76, s44, 3\n\t" \
  "s_lshl_b32 s45, s76, 14\n\t" \
  "s_mul_i32 s47, s76, 0xc000\n\t" \
  "s_add_i32 s47, s47, 0x40000\n\t" \
  "s_add_i32 s49, s44, -1\n\t" \
  "s_and_b32 s48, s49, 2\n\t" \
  "s_lshl_b32 s48, s48, 13\n\t" \
  "s_and_b32 s76, s49, 1\n\t" \
  "s_lshl_b32 s76, s76, 12\n\t" \
  "s_add_i32 s48, s48, s76\n\t" \
  "s_lshl_b32 s50, s53, 8\n\t" \
  "s_mov_b32 s59, 0\n\t" \
  "s_mov_b32 s92, s51\n\t" \
  "s_mov_b32 s93, s53\n\t" \
  "s_xor_b32 s76, s40, 0x400\n\t" \
  "s_xor_b32 s77, s40, 0x800\n\t" \
  "s_xor_b32 s78, s40, 0xc00\n\t" \
  "s_cmp_lg_u32 s58, 0\n\t" \
  "s_cbranch_scc1 11f\n\t" \
  "10:\n\t" \
  "ds_read_b128 v[48:51], v102 offset:0\n\t" \
  "ds_read_b128 v[52:55], v102 offset:1024\n\t" \
  "ds_read_b128 v[56:59], v102 offset:2048\n\t" \
  "ds_read_b128 v[60:63], v102 offset:3072\n\t" \
  "ds_read_b128 v[16:19], v102 offset:16384\n\t" \
  "ds_read_b128 v[20:23], v102 offset:17408\n\t" \
  "ds_read_b128 v[24:27], v102 offset:18432\n\t" \
  "ds_read_b128 v[28:31], v102 offset:19456\n\t" \
  "ds_read_b128 v[84:87], v102 offset:49152\n\t" \
  "ds_read_b128 v[88:91], v102 offset:50176\n\t" \
  "ds_read_b128 v[92:95], v102 offset:51200\n\t" \
  "ds_read_b128 v[96:99], v102 offset:52224\n\t" \
  "s_waitcnt lgkmcnt(4)\n\t" \
  "ds_read_b128 v[0:3], v103 offset:16384\n\t" \
  "ds_read_b128 v[4:7], v103 offset:17408\n\t" \
  "ds_read_b128 v[8:11], v103 offset:18432\n\t" \
  "ds_read_b128 v[12:15], v103 offset:19456\n\t" \
  "s_bitcmp1_b32 s58, 1\n\t" \
  "s_cselect_b64 s[80:81], -1, 0\n\t" \
  "s_cmp_lg_u32 s58, 0\n\t" \
  "s_cbranch_scc1 17f\n\t" \
  "s_mov_b32 exec_hi, 0\n\t" \
  "16:\n\t" \
  "s_memtime vcc\n\t" \
  "s_waitcnt lgkmcnt(0)\n\t" \
  "s_sub_i32 s79, s41, vcc_lo\n\t" \
  "s_cmp_gt_i32 s79, 64\n\t" \
  "s_cbranch_scc1 16b\n\t" \
  "13:\n\t" \
  "buffer_load_dword v116, v112, %[rh], s48 offen sc1\n\t" \
  "s_waitcnt vmcnt(0)\n\t" \
  "15:\n\t" \
  "v_mov_b32 v117, 20000\n\t" \
  "v_cmp_ne_u32_sdwa vcc, v116, s49 src0_sel:BYTE_0 src1_sel:DWORD\n\t" \
  "v_subrev_u32 v116, s50, v116\n\t" \
  "v_ashrrev_i32 v116, 8, v116\n\t" \
  "s_nop 1\n\t" \
  "v_max_i32_dpp v116, v116, v116 quad_perm:[1,0,3,2] row_mask:0xf bank_mask:0xf\n\t" \
  "s_cbranch_vccnz 14f\n\t" \
  "s_nop 0\n\t" \
  "v_max_i32_dpp v116, v116, v116 quad_perm:[2,3,0,1] row_mask:0xf bank_mask:0xf\n\t" \
  "s_nop 1\n\t" \
  "v_max_i32_dpp v116, v116, v116 row_half_mirror row_mask:0xf bank_mask:0xf\n\t" \
  "s_nop 1\n\t" \
  "v_max_i32_dpp v116, v116, v116 row_mirror row_mask:0xf bank_mask:0xf\n\t" \
  "v_med3_i32 v116, v116, 0, v117\n\t" \
  "v_mul_u32_u24 v116, s52, v116\n\t" \
  "v_lshrrev_b32 v116, 16, v116\n\t" \
  "s_mov_b64 exec, -1\n\t" \
  "v_readfirstlane_b32 s84, v116\n\t" \
  "v_readlane_b32 s85, v116, 16\n\t" \
  "s_max_u32 s84, s84, s85\n\t" \
  "s_add_i32 s84, s84, s51\n\t" \
  "1:\n\t" \
  "s_memtime vcc\n\t" \
  "s_waitcnt lgkmcnt(0)\n\t" \
  "s_sub_i32 s79, s84, vcc_lo\n\t" \
  "s_cmp_gt_i32 s79, 128\n\t" \
  "s_cbranch_scc1 1b\n\t" \
  ".p2align 4\n\t" \
  "2:\n\t" \
  "s_sub_i32 s79, s79, 33\n\t" \
  "s_cmp_gt_i32 s79, 56\n\t" \
  "s_cbranch_scc1 2b\n\t" \
  "buffer_load_dwordx4 v[32:35], v100, %[rh], s40 offen sc1\n\t" \
  "s_nop 12\n\t" \
  "buffer_load_dwordx4 v[36:39], v100, %[rh], s76 offen sc1\n\t" \
  "s_nop 12\n\t" \
  "buffer_load_dwordx4 v[40:43], v100, %[rh], s77 offen sc1\n\t" \
  "s_nop 12\n\t" \
  "buffer_load_dwordx4 v[44:47], v100, %[rh], s78 offen sc1\n\t" \
  "s_memtime vcc\n\t" \
  "s_memrealtime s[82:83]\n\t" \
  "s_nop 4\n\t" \
  "s_memrealtime s[84:85]\n\t" \
  "s_nop 4\n\t" \
  "s_memrealtime s[56:57]\n\t" \
  "s_nop 4\n\t" \
  "s_memrealtime s[86:87]\n\t" \
  "s_nop 4\n\t" \
  "s_memrealtime s[88:89]\n\t" \
  "s_nop 4\n\t" \
  "s_memrealtime s[90:91]\n\t" \
  "s_mov_b32 s92, 0\n\t" \
  "s_waitcnt lgkmcnt(0)\n\t" \
  "s_cmp_eq_u32 s82, s84\n\t" \
  "s_addc_u32 s92, s92, 0\n\t" \
  "s_cmp_eq_u32 s82, s56\n\t" \
  "s_addc_u32 s92, s92, 0\n\t" \
  "s_cmp_eq_u32 s82, s86\n\t" \
  "s_addc_u32 s92, s92, 0\n\t" \
  "s_cmp_eq_u32 s82, s88\n\t" \
  "s_addc_u32 s92, s92, 0\n\t" \
  "s_cmp_eq_u32 s82, s90\n\t" \
  "s_addc_u32 s92, s92, 0\n\t" \
  "s_mul_i32 s92, s92, 24\n\t" \
  "s_add_i32 s92, s92, vcc_lo\n\t" \
  "s_lshl4_add_u32 s93, s82, 64\n\t" \
  "3:\n\t" \
  "s_sub_i32 s84, s92, s43\n\t" \
  "s_lshl_b32 s85, s93, 8\n\t" \
  "s_or_b32 s85, s85, s44\n\t" \
  "v_mov_b32 v118, s85\n\t" \
  "v_mov_b64 v[68:69], 0\n\t" \
  "v_mov_b64 v[70:71], 0\n\t" \
  "v_mov_b64 v[72:73], 0\n\t" \
  "v_mov_b64 v[74:75], 0\n\t" \
  "v_mov_b64 v[76:77], 0\n\t" \
  "v_mov_b64 v[78:79], 0\n\t" \
  "v_mov_b64 v[80:81], 0\n\t" \
  "v_mov_b64 v[82:83], 0\n\t" \
  "s_waitcnt lgkmcnt(0)\n\t" \
  "s_waitcnt vmcnt(3)\n\t" \
  "v_smfmac_f32_16x16x64_bf16 v[68:71], v[32:35], a[0:7], v104\n\t" \
  "ds_write_b128 v101, v[32:35] offset:0\n\t" \
  "v_smfmac_f32_16x16x64_bf16 v[72:75], v[32:35], a[8:15], v104\n\t" \
  "v_smfmac_f32_16x16x64_bf16 v[76:79], v[32:35], v[48:55], v104\n\t" \
  "v_smfmac_f32_16x16x64_bf16 v[80:83], v[32:35], v[56:63], v104\n\t" \
  "s_waitcnt vmcnt(2)\n\t" \
  "v_smfmac_f32_16x16x64_bf16 v[68:71], v[36:39], a[16:23], v104\n\t" \
  "ds_write_b128 v101, v[36:39] offset:1024\n\t" \
  "v_smfmac_f32_16x16x64_bf16 v[72:75], v[36:39], a[24:31], v104\n\t" \
  "v_smfmac_f32_16x16x64_bf16 v[76:79], v[36:39], a[32:39], v104\n\t" \
  "v_cmp_u_f32 vcc, v68, v69\n\t" \
  "v_smfmac_f32_16x16x64_bf16 v[80:83], v[36:39], a[40:47], v104\n\t" \
  "v_cmp_u_f32 s[82:83], v70, v71\n\t" \
  "s_waitcnt vmcnt(1)\n\t" \
  "v_smfmac_f32_16x16x64_bf16 v[68:71], v[40:43], v[16:23], v104\n\t" \
  "ds_write_b128 v101, v[40:43] offset:2048\n\t" \
  "v_smfmac_f32_16x16x64_bf16 v[72:75], v[40:43], v[24:31], v104\n\t" \
  "s_or_b64 vcc, vcc, s[82:83]\n\t" \
  "s_or_b64 vcc, vcc, s[80:81]\n\t" \
  "s_waitcnt lgkmcnt(1)\n\t" \
  "v_smfmac_f32_16x16x64_bf16 v[76:79], v[40:43], a[48:55], v104\n\t" \
  "s_cbranch_scc1 8f\n\t" \
  "5:\n\t" \
  "s_barrier\n\t" \
  "4:\n\t" \
  "s_memtime s[56:57]\n\t" \
  "ds_read_b128 v[48:51], v101 offset:4096\n\t" \
  "v_smfmac_f32_16x16x64_bf16 v[80:83], v[40:43], a[56:63], v104\n\t" \
  "ds_read_b128 v[52:55], v101 offset:5120\n\t" \
  "ds_read_b128 v[56:59], v101 offset:8192\n\t" \
  "s_waitcnt vmcnt(0)\n\t" \
  "v_smfmac_f32_16x16x64_bf16 v[68:71], v[44:47], a[64:71], v104\n\t" \
  "ds_write_b128 v101, v[44:47] offset:3072\n\t" \
  "v_smfmac_f32_16x16x64_bf16 v[72:75], v[44:47], a[72:79], v104\n\t" \
  "v_smfmac_f32_16x16x64_bf16 v[76:79], v[44:47], a[80:87], v104\n\t" \
  "v_cmp_u_f32 vcc, v68, v69\n\t" \
  "v_smfmac_f32_16x16x64_bf16 v[80:83], v[44:47], a[88:95], v104\n\t" \
  "v_cmp_u_f32 s[82:83], v70, v71\n\t" \
  "s_waitcnt lgkmcnt(3)\n\t" \
  "v_smfmac_f32_16x16x64_bf16 v[68:71], v[48:51], a[96:103], v104\n\t" \
  "ds_read_b128 v[60:63], v101 offset:9216\n\t" \
  "ds_read_b128 v[64:67], v101 offset:12288\n\t" \
  "v_smfmac_f32_16x16x64_bf16 v[72:75], v[48:51], a[104:111], v104\n\t" \
  "s_or_b64 vcc, vcc, s[82:83]\n\t" \
  "s_waitcnt lgkmcnt(2)\n\t" \
  "v_smfmac_f32_16x16x64_bf16 v[68:71], v[52:55], a[112:119], v104\n\t" \
  "s_cbranch_scc1 9f\n\t" \
  "6:\n\t" \
  "s_barrier\n\t" \
  "v_smfmac_f32_16x16x64_bf16 v[72:75], v[52:55], a[120:127], v104\n\t" \
  "s_waitcnt lgkmcnt(0)\n\t" \
  "ds_read_b128 v[48:51], v101 offset:13312\n\t" \
  "v_smfmac_f32_16x16x64_bf16 v[68:71], v[56:59], a[192:199], v104\n\t" \
  "ds_read_b128 v[52:55], v101 offset:6144\n\t" \
  "s_sub_i32 s79, s56, s84\n\t" \
  "s_mul_hi_u32 s79, s79, s46\n\t" \
  "v_smfmac_f32_16x16x64_bf16 v[72:75], v[56:59], a[200:207], v104\n\t" \
  "v_lshl_add_u32 v116, s79, 9, v118\n\t" \
  "buffer_store_dword v116, v109, %[rh], s45 offen sc1\n\t" \
  "v_smfmac_f32_16x16x64_bf16 v[68:71], v[60:63], a[208:215], v104\n\t" \
  "ds_read_b128 v[56:59], v101 offset:7168\n\t" \
  "v_smfmac_f32_16x16x64_bf16 v[72:75], v[60:63], a[216:223], v104\n\t" \
  "v_smfmac_f32_16x16x64_bf16 v[68:71], v[64:67], v[152:159], v104\n\t" \
  "ds_read_b128 v[60:63], v101 offset:10240\n\t" \
  "v_smfmac_f32_16x16x64_bf16 v[72:75], v[64:67], v[160:167], v104\n\t" \
  "s_waitcnt lgkmcnt(3)\n\t" \
  "v_smfmac_f32_16x16x64_bf16 v[68:71], v[48:51], v[168:175], v104\n\t" \
  "ds_read_b128 v[64:67], v101 offset:11264\n\t" \
  "ds_read_b128 v[16:19], v103 offset:49152\n\t" \
  "v_smfmac_f32_16x16x64_bf16 v[72:75], v[48:51], v[176:183], v104\n\t" \
  "s_waitcnt lgkmcnt(4)\n\t" \
  "v_smfmac_f32_16x16x64_bf16 v[68:71], v[52:55], v[84:91], v104\n\t" \
  "ds_read_b128 v[48:51], v101 offset:15360\n\t" \
  "ds_read_b128 v[20:23], v103 offset:50176\n\t" \
  "v_smfmac_f32_16x16x64_bf16 v[72:75], v[52:55], v[92:99], v104\n\t" \
  "s_waitcnt lgkmcnt(5)\n\t" \
  "v_smfmac_f32_16x16x64_bf16 v[68:71], v[56:59], a[160:167], v104\n\t" \
  "ds_read_b128 v[52:55], v101 offset:14336\n\t" \
  "ds_read_b128 v[24:27], v103 offset:51200\n\t" \
  "v_smfmac_f32_16x16x64_bf16 v[72:75], v[56:59], a[168:175], v104\n\t" \
  "ds_read_b128 v[28:31], v103 offset:52224\n\t" \
  "s_waitcnt lgkmcnt(7)\n\t" \
  "v_smfmac_f32_16x16x64_bf16 v[68:71], v[60:63], v[0:7], v104\n\t" \
  "ds_read_b128 v[32:35], v101 offset:20480\n\t" \
  "v_smfmac_f32_16x16x64_bf16 v[72:75], v[60:63], v[8:15], v104\n\t" \
  "ds_read_b128 v[36:39], v101 offset:21504\n\t" \
  "v_mov_b64 v[92:93], 0\n\t" \
  "s_waitcnt lgkmcnt(8)\n\t" \
  "v_smfmac_f32_16x16x64_bf16 v[68:71], v[64:67], v[120:127], v104\n\t" \
  "ds_read_b128 v[0:3], v102 offset:32768\n\t" \
  "ds_read_b128 v[40:43], v101 offset:22528\n\t" \
  "v_smfmac_f32_16x16x64_bf16 v[72:75], v[64:67], v[128:135], v104\n\t" \
  "ds_read_b128 v[4:7], v102 offset:33792\n\t" \
  "s_waitcnt lgkmcnt(9)\n\t" \
  "v_smfmac_f32_16x16x64_bf16 v[68:71], v[48:51], v[216:223], v104\n\t" \
  "ds_read_b128 v[44:47], v101 offset:23552\n\t" \
  "v_mov_b64 v[94:95], 0\n\t" \
  "s_waitcnt lgkmcnt(8)\n\t" \
  "v_smfmac_f32_16x16x64_bf16 v[68:71], v[52:55], v[16:23], v104\n\t" \
  "ds_read_b128 v[8:11], v102 offset:34816\n\t" \
  "v_smfmac_f32_16x16x64_bf16 v[72:75], v[48:51], v[224:231], v104\n\t" \
  "ds_read_b128 v[12:15], v102 offset:35840\n\t" \
  "s_waitcnt lgkmcnt(8)\n\t" \
  "v_smfmac_f32_16x16x64_bf16 v[72:75], v[52:55], v[24:31], v104\n\t" \
  "v_add_f32 v96, v68, v70\n\t" \
  "v_smfmac_f32_16x16x64_bf16 v[76:79], v[56:59], a[176:183], v104\n\t" \
  "v_add_f32 v97, v69, v71\n\t" \
  "v_cvt_pk_bf16_f32 v96, v96, v97\n\t" \
  "v_smfmac_f32_16x16x64_bf16 v[80:83], v[56:59], a[184:191], v104\n\t" \
  "v_add_f32 v98, v72, v74\n\t" \
  "v_add_f32 v99, v73, v75\n\t" \
  "v_smfmac_f32_16x16x64_bf16 v[76:79], v[60:63], a[240:247], v104\n\t" \
  "v_cvt_pk_bf16_f32 v98, v98, v99\n\t" \
  "ds_write2st64_b32 v105, v96, v98 offset1:1\n\t" \
  "v_smfmac_f32_16x16x64_bf16 v[80:83], v[60:63], a[248:255], v104\n\t" \
  "ds_read_b128 v[56:59], v101 offset:4096\n\t" \
  "ds_read_b128 v[16:19], v101 offset:24576\n\t" \
  "v_mov_b64 v[68:69], 0\n\t" \
  "v_smfmac_f32_16x16x64_bf16 v[76:79], v[64:67], v[136:143], v104\n\t" \
  "ds_read_b128 v[20:23], v101 offset:25600\n\t" \
  "v_mov_b64 v[70:71], 0\n\t" \
  "v_smfmac_f32_16x16x64_bf16 v[80:83], v[64:67], v[144:151], v104\n\t" \
  "ds_read_b128 v[24:27], v101 offset:26624\n\t" \
  "v_smfmac_f32_16x16x64_bf16 v[76:79], v[48:51], v[232:239], v104\n\t" \
  "s_waitcnt lgkmcnt(4)\n\t" \
  "s_barrier\n\t" \
  "ds_read_b64_tr_b16 v[84:85], v107 offset:0\n\t" \
  "v_smfmac_f32_16x16x64_bf16 v[80:83], v[48:51], v[240:247], v104\n\t" \
  "ds_read_b64_tr_b16 v[86:87], v107 offset:128\n\t" \
  "ds_read_b64_tr_b16 v[88:89], v107 offset:1024\n\t" \
  "v_smfmac_f32_16x16x64_bf16 v[76:79], v[52:55], v[200:207], v104\n\t" \
  "ds_read_b128 v[60:63], v101 offset:13312\n\t" \
  "ds_read_b64_tr_b16 v[90:91], v107 offset:1152\n\t" \
  "v_smfmac_f32_16x16x64_bf16 v[80:83], v[52:55], v[208:215], v104\n\t" \
  "ds_read_b128 v[64:67], v101 offset:6144\n\t" \
  "ds_read_b128 v[28:31], v101 offset:27648\n\t" \
  "s_waitcnt lgkmcnt(10)\n\t" \
  "v_smfmac_f32_16x16x64_bf16 v[76:79], v[56:59], v[0:7], v104\n\t" \
  "ds_read_b128 v[48:51], v101 offset:8192\n\t" \
  "s_waitcnt lgkmcnt(6)\n\t" \
  "v_smfmac_f32_16x16x64_bf16 v[92:95], v[84:87], v[32:39], v104\n\t" \
  "ds_read_b128 v[0:3], v103 offset:0\n\t" \
  "s_waitcnt lgkmcnt(4)\n\t" \
  "v_smfmac_f32_16x16x64_bf16 v[92:95], v[88:91], v[40:47], v104\n\t" \
  "ds_read_b128 v[4:7], v103 offset:1024\n\t" \
  "v_smfmac_f32_16x16x64_bf16 v[68:71], v[84:87], v[16:23], v104\n\t" \
  "s_waitcnt lgkmcnt(3)\n\t" \
  "v_smfmac_f32_16x16x64_bf16 v[68:71], v[88:91], v[24:31], v104\n\t" \
  "v_add_f32 v92, v92, v94\n\t" \
  "v_add_f32 v94, v93, v95\n\t" \
  "v_smfmac_f32_16x16x64_bf16 v[80:83], v[56:59], v[8:15], v104\n\t" \
  "ds_read_b128 v[8:11], v103 offset:2048\n\t" \
  "v_mov_b32 v97, v94\n\t" \
  "v_mov_b32_dpp v94, v92 row_ror:8 row_mask:0xf bank_mask:0x3\n\t" \
  "s_nop 0\n\t" \
  "v_mov_b32_dpp v92, v97 row_ror:8 row_mask:0xf bank_mask:0xc\n\t" \
  "v_smfmac_f32_16x16x64_bf16 v[76:79], v[60:63], v[184:191], v104\n\t" \
  "ds_read_b128 v[52:55], v101 offset:5120\n\t" \
  "ds_read_b128 v[12:15], v103 offset:3072\n\t" \
  "v_add_f32 v93, v68, v70\n\t" \
  "v_add_f32 v95, v69, v71\n\t" \
  "v_max_f32 v98, v92, v94\n\t" \
  "v_mov_b32 v97, v95\n\t" \
  "v_mov_b32_dpp v95, v93 row_ror:8 row_mask:0xf bank_mask:0x3\n\t" \
  "v_smfmac_f32_16x16x64_bf16 v[80:83], v[60:63], v[192:199], v104\n\t" \
  "v_mov_b32_dpp v93, v97 row_ror:8 row_mask:0xf bank_mask:0xc\n\t" \
  "v_max3_f32 v98, v98, v93, v95\n\t" \
  "v_smfmac_f32_16x16x64_bf16 v[76:79], v[64:67], a[144:151], v104\n\t" \
  "ds_read_b128 v[56:59], v101 offset:9216\n\t" \
  "ds_read_b128 v[32:35], v103 offset:32768\n\t" \
  "v_max_f32_dpp v98, v98, v98 quad_perm:[1,0,3,2] row_mask:0xf bank_mask:0xf\n\t" \
  "v_smfmac_f32_16x16x64_bf16 v[80:83], v[64:67], a[152:159], v104\n\t" \
  "ds_read_b128 v[60:63], v101 offset:12288\n\t" \
  "ds_read_b128 v[36:39], v103 offset:33792\n\t" \
  "v_max_f32_dpp v98, v98, v98 quad_perm:[2,3,0,1] row_mask:0xf bank_mask:0xf\n\t" \
  "s_waitcnt lgkmcnt(7)\n\t" \
  "v_smfmac_f32_16x16x64_bf16 v[76:79], v[48:51], v[0:7], v104\n\t" \
  "ds_read_b128 v[40:43], v103 offset:34816\n\t" \
  "v_max_f32_dpp v98, v98, v98 row_half_mirror row_mask:0xf bank_mask:0xf\n\t" \
  "v_mul_f32 v98, %[cs], v98\n\t" \
  "v_fma_f32 v92, v92, %[cs], -v98\n\t" \
  "v_fma_f32 v93, v93, %[cs], -v98\n\t" \
  "v_fma_f32 v94, v94, %[cs], -v98\n\t" \
  "s_waitcnt lgkmcnt(5)\n\t" \
  "v_smfmac_f32_16x16x64_bf16 v[80:83], v[48:51], v[8:15], v104\n\t" \
  "ds_read_b128 v[44:47], v103 offset:35840\n\t" \
  "v_fma_f32 v95, v95, %[cs], -v98\n\t" \
  "v_exp_f32 v72, v92\n\t" \
  "v_exp_f32 v73, v93\n\t" \
  "v_exp_f32 v74, v94\n\t" \
  "v_exp_f32 v75, v95\n\t" \
  "v_cvt_pk_bf16_f32 v68, v72, v73\n\t" \
  "v_cvt_pk_bf16_f32 v69, v74, v75\n\t" \
  "buffer_store_dwordx2 v[68:69], v110, %[rh], s47 offen sc1\n\t" \
  "v_smfmac_f32_16x16x64_bf16 v[76:79], v[52:55], a[128:135], v104\n\t" \
  "v_smfmac_f32_16x16x64_bf16 v[80:83], v[52:55], a[136:143], v104\n\t" \
  "s_waitcnt lgkmcnt(5)\n\t" \
  "v_smfmac_f32_16x16x64_bf16 v[76:79], v[56:59], a[224:231], v104\n\t" \
  "s_waitcnt lgkmcnt(2)\n\t" \
  "v_smfmac_f32_16x16x64_bf16 v[76:79], v[60:63], v[32:39], v104\n\t" \
  "v_smfmac_f32_16x16x64_bf16 v[80:83], v[56:59], a[232:239], v104\n\t" \
  "s_waitcnt lgkmcnt(0)\n\t" \
  "v_smfmac_f32_16x16x64_bf16 v[80:83], v[60:63], v[40:47], v104\n\t" \
  "v_add_f32 v20, v76, v78\n\t" \
  "v_add_f32 v21, v77, v79\n\t" \
  "v_cvt_pk_bf16_f32 v20, v20, v21\n\t" \
  "ds_write_b32 v106, v20\n\t" \
  "s_nop 0\n\t" \
  "s_nop 0\n\t" \
  "s_nop 0\n\t" \
  "s_nop 0\n\t" \
  "v_add_f32 v23, v80, v82\n\t" \
  "v_add_f32 v21, v81, v83\n\t" \
  "v_cvt_pk_bf16_f32 v21, v23, v21\n\t" \
  "ds_write_b32 v106, v21 offset:256\n\t" \
  "s_waitcnt lgkmcnt(0)\n\t" \
  "s_barrier\n\t" \
  "ds_read_b64_tr_b16 v[4:5], v108\n\t" \
  "ds_read_b64_tr_b16 v[6:7], v108 offset:64\n\t" \
  "ds_read_b64_tr_b16 v[8:9], v108 offset:512\n\t" \
  "ds_read_b64_tr_b16 v[10:11], v108 offset:576\n\t" \
  "ds_read_b64_tr_b16 v[12:13], v108 offset:1024\n\t" \
  "ds_read_b64_tr_b16 v[14:15], v108 offset:1088\n\t" \
  "ds_read_b64_tr_b16 v[16:17], v108 offset:1536\n\t" \
  "ds_read_b64_tr_b16 v[18:19], v108 offset:1600\n\t" \
  "v_mov_b64 v[0:1], 0\n\t" \
  "v_mov_b64 v[2:3], 0\n\t" \
  "v_add_f32 v29, v72, v73\n\t" \
  "v_add_f32 v29, v29, v74\n\t" \
  "s_waitcnt lgkmcnt(4)\n\t" \
  "v_smfmac_f32_16x16x64_bf16 v[0:3], v[84:87], v[4:11], v104\n\t" \
  "s_waitcnt lgkmcnt(0)\n\t" \
  "v_smfmac_f32_16x16x64_bf16 v[0:3], v[88:91], v[12:19], v104\n\t" \
  "v_add_f32 v29, v29, v75\n\t" \
  "s_mov_b32 s76, 0xaaaaaaaa\n\t" \
  "s_mov_b32 s77, s76\n\t" \
  "s_nop 4\n\t" \
  "v_add_f32 v96, v0, v2\n\t" \
  "v_add_f32_dpp v96, v1, v3 quad_perm:[0,1,2,3] row_mask:0xf bank_mask:0xc\n\t" \
  "v_fma_f32 v96, v96, %[cs], -v98\n\t" \
  "v_exp_f32 v24, v96\n\t" \
  "s_nop 0\n\t" \
  "v_add_f32 v29, v29, v24\n\t" \
  "s_nop 0\n\t" \
  "v_mov_b32_dpp v25, v24 quad_perm:[1,0,3,2] row_mask:0xf bank_mask:0xf\n\t" \
  "v_add_f32_dpp v29, v29, v29 quad_perm:[1,0,3,2] row_mask:0xf bank_mask:0xf\n\t" \
  "v_cndmask_b32_e64 v24, v24, v114, s[76:77]\n\t" \
  "v_cndmask_b32_e64 v25, v25, v115, s[76:77]\n\t" \
  "v_add_f32_dpp v29, v29, v29 quad_perm:[2,3,0,1] row_mask:0xf bank_mask:0xf\n\t" \
  "s_nop 1\n\t" \
  "v_add_f32_dpp v29, v29, v29 row_half_mirror row_mask:0xf bank_mask:0xf\n\t" \
  "v_rcp_f32 v26, v29\n\t" \
  "v_cmpx_gt_f32 vcc, 0x5d5e0b6b, v29\n\t" \
  "v_mul_f32 v24, v24, v26\n\t" \
  "v_mul_f32 v25, v25, v26\n\t" \
  "v_cvt_pk_bf16_f32 v24, v24, v25\n\t" \
  "buffer_store_dword v24, v111, %[rh], s47 offen sc1\n\t" \
  "s_memtime s[76:77]\n\t" \
  "s_mov_b64 exec, -1\n\t" \
  "v_mov_b32 v97, v29\n\t" \
  "s_cmp_eq_u64 vcc, -1\n\t" \
  "s_cselect_b32 s42, 0, 8\n\t" \
  "s_waitcnt lgkmcnt(0)\n\t" \
  "s_mov_b32 s57, s76\n\t" \
  "s_barrier\n\t" \
  "s_add_i32 s41, s56, 1950\n\t" \
  "s_sub_i32 s76, s57, s56\n\t" \
  "s_add_i32 s77, s43, 8\n\t" \
  "s_min_u32 s76, s76, s77\n\t" \
  "s_sub_i32 s43, s43, 4\n\t" \
  "s_max_u32 s43, s43, s76\n\t" \
  "s_add_i32 s76, s45, 0x4000\n\t" \
  "s_and_b32 s76, s76, 0xc000\n\t" \
  "v_mov_b32 v116, -1\n\t" \
  "buffer_store_dword v116, v109, %[rh], s76 offen sc1\n\t" \
  "s_sub_i32 s76, s92, s51\n\t" \
  "s_sub_i32 s77, s93, s53\n\t" \
  "s_cmp_eq_u32 s76, 0\n\t" \
  "s_cbranch_scc1 21f\n\t" \
  "v_cvt_f32_u32 v117, s76\n\t" \
  "v_cvt_f32_u32 v116, s77\n\t" \
  "v_rcp_f32 v117, v117\n\t" \
  "s_mov_b32 s51, s92\n\t" \
  "v_mul_f32 v117, v117, v116\n\t" \
  "v_max_f32 v117, 0x3f028f5c, v117\n\t" \
  "v_min_f32 v117, 0x3ffeb852, v117\n\t" \
  "v_rcp_f32 v116, v117\n\t" \
  "v_mul_f32 v117, 0x4f000000, v117\n\t" \
  "v_mul_f32 v116, 0x47800000, v116\n\t" \
  "v_cvt_u32_f32 v117, v117\n\t" \
  "v_cvt_u32_f32 v116, v116\n\t" \
  "s_mov_b32 s53, s93\n\t" \
  "v_readfirstlane_b32 s46, v117\n\t" \
  "v_readfirstlane_b32 s52, v116\n\t" \
  "21:\n\t" \
  "s_add_i32 s44, s44, 1\n\t" \
  "s_and_b32 s58, s58, 4\n\t" \
  "s_and_b32 s76, s44, 64\n\t" \
  "s_or_b32 s42, s42, s76\n\t" \
  "s_cbranch_scc0 20b\n\t" \
  "s_branch 99f\n\t" \
  "14:\n\t" \
  "s_sleep 3\n\t" \
  "s_add_u32 s59, s59, 0x10000\n\t" \
  "s_cmp_lt_u32 s59, 0x100000\n\t" \
  "s_cbranch_scc1 13b\n\t" \
  "s_mov_b64 exec, -1\n\t" \
  "s_mov_b32 s42, 16\n\t" \
  "s_branch 99f\n\t" \
  "8:\n\t" \
  "s_bitcmp1_b32 s58, 1\n\t" \
  "s_cbranch_scc1 4b\n\t" \
  "s_bitcmp1_b32 s58, 2\n\t" \
  "s_cbranch_scc1 5b\n\t" \
  "v_max3_u32 v116, v32, v33, v34\n\t" \
  "v_max3_u32 v117, v35, v36, v37\n\t" \
  "v_max3_u32 v116, v116, v38, v39\n\t" \
  "v_max_u32 v116, v116, v117\n\t" \
  "v_cmp_eq_u32 vcc, -1, v116\n\t" \
  "s_cbranch_vccz 5b\n\t" \
  "s_branch 0f\n\t" \
  "9:\n\t" \
  "s_bitcmp1_b32 s58, 2\n\t" \
  "s_cbranch_scc1 6b\n\t" \
  "v_max3_u32 v116, v40, v41, v42\n\t" \
  "v_max3_u32 v117, v43, v44, v45\n\t" \
  "v_max3_u32 v116, v116, v46, v47\n\t" \
  "v_max_u32 v116, v116, v117\n\t" \
  "v_cmp_eq_u32 vcc, -1, v116\n\t" \
  "s_cbranch_vccz 6b\n\t" \
  "s_or_b32 s58, s58, 2\n\t" \
  "0:\n\t" \
  "s_or_b32 s58, s58, 1\n\t" \
  "11:\n\t" \
  "buffer_load_dwordx4 v[32:35], v100, %[rh], s40 offen sc1\n\t" \
  "buffer_load_dwordx4 v[36:39], v100, %[rh], s76 offen sc1\n\t" \
  "buffer_load_dwordx4 v[40:43], v100, %[rh], s77 offen sc1\n\t" \
  "buffer_load_dwordx4 v[44:47], v100, %[rh], s78 offen sc1\n\t" \
  "s_waitcnt lgkmcnt(0)\n\t" \
  "s_branch 10b\n\t" \
  "17:\n\t" \
  "s_add_u32 s59, s59, 1\n\t" \
  "s_bitcmp1_b32 s58, 2\n\t" \
  "s_cbranch_scc1 3b\n\t" \
  "s_waitcnt vmcnt(0)\n\t" \
  "v_max_u32 v116, v32, v33\n\t" \
  "v_max3_u32 v116, v116, v34, v35\n\t" \
  "v_max3_u32 v116, v116, v36, v37\n\t" \
  "v_max3_u32 v116, v116, v38, v39\n\t" \
  "v_max3_u32 v116, v116, v40, v41\n\t" \
  "v_max3_u32 v116, v116, v42, v43\n\t" \
  "v_max3_u32 v116, v116, v44, v45\n\t" \
  "v_max3_u32 v116, v116, v46, v47\n\t" \
  "v_cmp_eq_u32 vcc, -1, v116\n\t" \
  "s_cbranch_vccz 3b\n\t" \
  "s_bitcmp1_b32 s59, 6\n\t" \
  "s_cbranch_scc1 12f\n\t" \
  "buffer_load_dwordx4 v[32:35], v100, %[rh], s40 offen sc1\n\t" \
  "buffer_load_dwordx4 v[36:39], v100, %[rh], s76 offen sc1\n\t" \
  "buffer_load_dwordx4 v[40:43], v100, %[rh], s77 offen sc1\n\t" \
  "buffer_load_dwordx4 v[44:47], v100, %[rh], s78 offen sc1\n\t" \
  "s_branch 17b\n\t" \
  "12:\n\t" \
  "s_mov_b32 s42, s58\n\t" \
  "s_branch 99f\n\t" \
  "91:\n\t" \
  "s_mov_b32 s58, s42\n\t" \
  "s_mov_b32 s95, 0\n\t" \
  "20:\n\t" \
  "s_and_b32 s76, s44, 7\n\t" \
  "s_lshl_b32 s76, s76, 15\n\t" \
  "s_add_i32 s40, s54, s76\n\t" \
  "s_and_b32 s76, s44, 3\n\t" \
  "s_lshl_b32 s45, s76, 14\n\t" \
  "s_add_i32 s47, s44, 1\n\t" \
  "s_min_u32 s47, s47, 63\n\t" \
  "s_lshr_b32 s76, s47, 1\n\t" \
  "s_lshl_b32 s47, s47, 20\n\t" \
  "s_add_i32 s47, s47, s55\n\t" \
  "s_bitcmp1_b64 %[sel], s76\n\t" \
  "s_cselect_b32 s76, 0x2000, 0\n\t" \
  "s_add_i32 s47, s47, s76\n\t" \
  "s_lshr_b32 s76, s44, 1\n\t" \
  "s_lshl_b32 s94, s44, 20\n\t" \
  "s_bitcmp1_b64 %[sel], s76\n\t" \
  "s_cselect_b32 s76, 0x2000, 0\n\t" \
  "s_add_i32 s94, s94, s76\n\t" \
  "s_add_i32 s49, s44, -1\n\t" \
  "s_and_b32 s48, s49, 2\n\t" \
  "s_lshl_b32 s48, s48, 13\n\t" \
  "s_and_b32 s76, s49, 1\n\t" \
  "s_lshl_b32 s76, s76, 12\n\t" \
  "s_add_i32 s48, s48, s76\n\t" \
  "s_lshl_b32 s50, s53, 8\n\t" \
  "s_mov_b32 s59, 0\n\t" \
  "s_mov_b32 s92, s51\n\t" \
  "s_mov_b32 s93, s53\n\t" \
  "s_xor_b32 s76, s40, 0x400\n\t" \
  "s_xor_b32 s77, s40, 0x800\n\t" \
  "s_xor_b32 s78, s40, 0xc00\n\t" \
  "s_cmp_lg_u32 s58, 0\n\t" \
  "s_cbranch_scc1 11f\n\t" \
  "10:\n\t" \
  "ds_read_b128 v[48:51], v102 offset:32768\n\t" \
  "ds_read_b128 v[52:55], v102 offset:33792\n\t" \
  "ds_read_b128 v[56:59], v102 offset:34816\n\t" \
  "ds_read_b128 v[60:63], v102 offset:35840\n\t" \
  "ds_read_b128 v[16:19], v102 offset:49152\n\t" \
  "ds_read_b128 v[20:23], v102 offset:50176\n\t" \
  "ds_read_b128 v[24:27], v102 offset:51200\n\t" \
  "ds_read_b128 v[28:31], v102 offset:52224\n\t" \
  "ds_read_b128 v[84:87], v103 offset:16384\n\t" \
  "ds_read_b128 v[88:91], v103 offset:17408\n\t" \
  "ds_read_b128 v[92:95], v103 offset:18432\n\t" \
  "ds_read_b128 v[96:99], v103 offset:19456\n\t" \
  "s_waitcnt lgkmcnt(4)\n\t" \
  "ds_read_b128 v[0:3], v103 offset:49152\n\t" \
  "ds_read_b128 v[4:7], v103 offset:50176\n\t" \
  "ds_read_b128 v[8:11], v103 offset:51200\n\t" \
  "ds_read_b128 v[12:15], v103 offset:52224\n\t" \
  "s_bitcmp1_b32 s58, 1\n\t" \
  "s_cselect_b64 s[80:81], -1, 0\n\t" \
  "s_cmp_lg_u32 s58, 0\n\t" \
  "s_cbranch_scc1 17f\n\t" \
  "s_mov_b32 exec_hi, 0\n\t" \
  "s_cmp_eq_u32 s95, 0\n\t" \
  "s_cbranch_scc1 16f\n\t" \
  "s_waitcnt vmcnt(2)\n\t" \
  "v_mov_b32 v116, v118\n\t" \
  "s_branch 15f\n\t" \
  "16:\n\t" \
  "s_memtime vcc\n\t" \
  "s_waitcnt lgkmcnt(0)\n\t" \
  "s_sub_i32 s79, s41, vcc_lo\n\t" \
  "s_cmp_gt_i32 s79, 64\n\t" \
  "s_cbranch_scc1 16b\n\t" \
  "13:\n\t" \
  "buffer_load_dword v116, v112, %[rh], s48 offen sc1\n\t" \
  "s_waitcnt vmcnt(0)\n\t" \
  "15:\n\t" \
  "v_mov_b32 v117, 20000\n\t" \
  "v_cmp_ne_u32_sdwa vcc, v116, s49 src0_sel:BYTE_0 src1_sel:DWORD\n\t" \
  "v_subrev_u32 v116, s50, v116\n\t" \
  "v_ashrrev_i32 v116, 8, v116\n\t" \
  "s_nop 1\n\t" \
  "v_max_i32_dpp v116, v116, v116 quad_perm:[1,0,3,2] row_mask:0xf bank_mask:0xf\n\t" \
  "s_cbranch_vccnz 14f\n\t" \
  "s_nop 0\n\t" \
  "v_max_i32_dpp v116, v116, v116 quad_perm:[2,3,0,1] row_mask:0xf bank_mask:0xf\n\t" \
  "s_nop 1\n\t" \
  "v_max_i32_dpp v116, v116, v116 row_half_mirror row_mask:0xf bank_mask:0xf\n\t" \
  "s_nop 1\n\t" \
  "v_max_i32_dpp v116, v116, v116 row_mirror row_mask:0xf bank_mask:0xf\n\t" \
  "v_med3_i32 v116, v116, 0, v117\n\t" \
  "v_mul_u32_u24 v116, s52, v116\n\t" \
  "v_lshrrev_b32 v116, 16, v116\n\t" \
  "s_mov_b64 exec, -1\n\t" \
  "v_readfirstlane_b32 s84, v116\n\t" \
  "v_readlane_b32 s85, v116, 16\n\t" \
  "s_max_u32 s84, s84, s85\n\t" \
  "s_add_i32 s84, s84, s51\n\t" \
  "1:\n\t" \
  "s_memtime vcc\n\t" \
  "s_waitcnt lgkmcnt(0)\n\t" \
  "s_sub_i32 s79, s84, vcc_lo\n\t" \
  "s_cmp_gt_i32 s79, 128\n\t" \
  "s_cbranch_scc1 1b\n\t" \
  ".p2align 4\n\t" \
  "2:\n\t" \
  "s_sub_i32 s79, s79, 33\n\t" \
  "s_cmp_gt_i32 s79, 56\n\t" \
  "s_cbranch_scc1 2b\n\t" \
  "buffer_load_dwordx4 v[32:35], v100, %[rh], s40 offen sc1\n\t" \
  "s_nop 12\n\t" \
  "buffer_load_dwordx4 v[36:39], v100, %[rh], s76 offen sc1\n\t" \
  "s_nop 12\n\t" \
  "buffer_load_dwordx4 v[40:43], v100, %[rh], s77 offen sc1\n\t" \
  "s_nop 12\n\t" \
  "buffer_load_dwordx4 v[44:47], v100, %[rh], s78 offen sc1\n\t" \
  "s_memtime vcc\n\t" \
  "s_memrealtime s[82:83]\n\t" \
  "s_nop 4\n\t" \
  "s_memrealtime s[84:85]\n\t" \
  "s_nop 4\n\t" \
  "s_memrealtime s[56:57]\n\t" \
  "s_nop 4\n\t" \
  "s_memrealtime s[86:87]\n\t" \
  "s_nop 4\n\t" \
  "s_memrealtime s[88:89]\n\t" \
  "s_nop 4\n\t" \
  "s_memrealtime s[90:91]\n\t" \
  "s_mov_b32 s92, 0\n\t" \
  "s_waitcnt lgkmcnt(0)\n\t" \
  "s_cmp_eq_u32 s82, s84\n\t" \
  "s_addc_u32 s92, s92, 0\n\t" \
  "s_cmp_eq_u32 s82, s56\n\t" \
  "s_addc_u32 s92, s92, 0\n\t" \
  "s_cmp_eq_u32 s82, s86\n\t" \
  "s_addc_u32 s92, s92, 0\n\t" \
  "s_cmp_eq_u32 s82, s88\n\t" \
  "s_addc_u32 s92, s92, 0\n\t" \
  "s_cmp_eq_u32 s82, s90\n\t" \
  "s_addc_u32 s92, s92, 0\n\t" \
  "s_mul_i32 s92, s92, 24\n\t" \
  "s_add_i32 s92, s92, vcc_lo\n\t" \
  "s_lshl4_add_u32 s93, s82, 64\n\t" \
  "3:\n\t" \
  "v_mov_b64 v[68:69], 0\n\t" \
  "v_mov_b64 v[70:71], 0\n\t" \
  "v_mov_b64 v[72:73], 0\n\t" \
  "v_mov_b64 v[74:75], 0\n\t" \
  "v_mov_b64 v[76:77], 0\n\t" \
  "v_mov_b64 v[78:79], 0\n\t" \
  "v_mov_b64 v[80:81], 0\n\t" \
  "v_mov_b64 v[82:83], 0\n\t" \
  "s_waitcnt lgkmcnt(0)\n\t" \
  "s_waitcnt vmcnt(3)\n\t" \
  "v_smfmac_f32_16x16x64_bf16 v[68:71], v[32:35], a[96:103], v104\n\t" \
  "ds_write_b128 v101, v[32:35] offset:4096\n\t" \
  "v_smfmac_f32_16x16x64_bf16 v[72:75], v[32:35], a[104:111], v104\n\t" \
  "v_smfmac_f32_16x16x64_bf16 v[76:79], v[32:35], v[48:55], v104\n\t" \
  "v_smfmac_f32_16x16x64_bf16 v[80:83], v[32:35], v[56:63], v104\n\t" \
  "s_waitcnt vmcnt(2)\n\t" \
  "v_smfmac_f32_16x16x64_bf16 v[68:71], v[36:39], a[112:119], v104\n\t" \
  "ds_write_b128 v101, v[36:39] offset:5120\n\t" \
  "v_smfmac_f32_16x16x64_bf16 v[72:75], v[36:39], a[120:127], v104\n\t" \
  "v_smfmac_f32_16x16x64_bf16 v[76:79], v[36:39], a[128:135], v104\n\t" \
  "v_cmp_u_f32 vcc, v68, v69\n\t" \
  "v_smfmac_f32_16x16x64_bf16 v[80:83], v[36:39], a[136:143], v104\n\t" \
  "v_cmp_u_f32 s[82:83], v70, v71\n\t" \
  "s_waitcnt vmcnt(1)\n\t" \
  "v_smfmac_f32_16x16x64_bf16 v[68:71], v[40:43], v[16:23], v104\n\t" \
  "ds_write_b128 v101, v[40:43] offset:6144\n\t" \
  "v_smfmac_f32_16x16x64_bf16 v[72:75], v[40:43], v[24:31], v104\n\t" \
  "s_or_b64 vcc, vcc, s[82:83]\n\t" \
  "s_or_b64 vcc, vcc, s[80:81]\n\t" \
  "s_waitcnt lgkmcnt(1)\n\t" \
  "v_smfmac_f32_16x16x64_bf16 v[76:79], v[40:43], a[144:151], v104\n\t" \
  "s_cbranch_scc1 8f\n\t" \
  "5:\n\t" \
  "s_barrier\n\t" \
  "4:\n\t" \
  "s_memtime s[56:57]\n\t" \
  "ds_read_b128 v[48:51], v101 offset:8192\n\t" \
  "v_smfmac_f32_16x16x64_bf16 v[80:83], v[40:43], a[152:159], v104\n\t" \
  "ds_read_b128 v[52:55], v101 offset:9216\n\t" \
  "ds_read_b128 v[56:59], v101 offset:12288\n\t" \
  "s_waitcnt vmcnt(0)\n\t" \
  "v_smfmac_f32_16x16x64_bf16 v[68:71], v[44:47], a[160:167], v104\n\t" \
  "ds_write_b128 v101, v[44:47] offset:7168\n\t" \
  "v_smfmac_f32_16x16x64_bf16 v[72:75], v[44:47], a[168:175], v104\n\t" \
  "v_smfmac_f32_16x16x64_bf16 v[76:79], v[44:47], a[176:183], v104\n\t" \
  "v_cmp_u_f32 vcc, v68, v69\n\t" \
  "v_smfmac_f32_16x16x64_bf16 v[80:83], v[44:47], a[184:191], v104\n\t" \
  "v_cmp_u_f32 s[82:83], v70, v71\n\t" \
  "s_waitcnt lgkmcnt(3)\n\t" \
  "v_smfmac_f32_16x16x64_bf16 v[68:71], v[48:51], a[192:199], v104\n\t" \
  "ds_read_b128 v[60:63], v101 offset:13312\n\t" \
  "ds_read_b128 v[64:67], v101 offset:0\n\t" \
  "v_smfmac_f32_16x16x64_bf16 v[72:75], v[48:51], a[200:207], v104\n\t" \
  "s_or_b64 vcc, vcc, s[82:83]\n\t" \
  "s_waitcnt lgkmcnt(2)\n\t" \
  "v_smfmac_f32_16x16x64_bf16 v[68:71], v[52:55], a[208:215], v104\n\t" \
  "s_cbranch_scc1 9f\n\t" \
  "6:\n\t" \
  "s_barrier\n\t" \
  "v_smfmac_f32_16x16x64_bf16 v[72:75], v[52:55], a[216:223], v104\n\t" \
  "ds_read_b128 v[48:51], v101 offset:1024\n\t" \
  "v_smfmac_f32_16x16x64_bf16 v[68:71], v[56:59], v[152:159], v104\n\t" \
  "ds_read_b128 v[52:55], v101 offset:10240\n\t" \
  "v_smfmac_f32_16x16x64_bf16 v[72:75], v[56:59], v[160:167], v104\n\t" \
  "s_waitcnt lgkmcnt(3)\n\t" \
  "v_smfmac_f32_16x16x64_bf16 v[68:71], v[60:63], v[168:175], v104\n\t" \
  "ds_read_b128 v[56:59], v101 offset:11264\n\t" \
  "v_smfmac_f32_16x16x64_bf16 v[72:75], v[60:63], v[176:183], v104\n\t" \
  "s_waitcnt lgkmcnt(3)\n\t" \
  "v_smfmac_f32_16x16x64_bf16 v[68:71], v[64:67], a[0:7], v104\n\t" \
  "ds_read_b128 v[60:63], v101 offset:14336\n\t" \
  "v_smfmac_f32_16x16x64_bf16 v[72:75], v[64:67], a[8:15], v104\n\t" \
  "s_waitcnt lgkmcnt(3)\n\t" \
  "v_smfmac_f32_16x16x64_bf16 v[68:71], v[48:51], a[16:23], v104\n\t" \
  "ds_read_b128 v[64:67], v101 offset:15360\n\t" \
  "ds_read_b128 v[16:19], v102 offset:16384\n\t" \
  "v_smfmac_f32_16x16x64_bf16 v[72:75], v[48:51], a[24:31], v104\n\t" \
  "s_waitcnt lgkmcnt(4)\n\t" \
  "v_smfmac_f32_16x16x64_bf16 v[68:71], v[52:55], v[84:91], v104\n\t" \
  "ds_read_b128 v[48:51], v101 offset:3072\n\t" \
  "ds_read_b128 v[20:23], v102 offset:17408\n\t" \
  "v_smfmac_f32_16x16x64_bf16 v[72:75], v[52:55], v[92:99], v104\n\t" \
  "s_waitcnt lgkmcnt(5)\n\t" \
  "v_smfmac_f32_16x16x64_bf16 v[68:71], v[56:59], v[120:127], v104\n\t" \
  "ds_read_b128 v[52:55], v101 offset:2048\n\t" \
  "ds_read_b128 v[24:27], v102 offset:18432\n\t" \
  "v_smfmac_f32_16x16x64_bf16 v[72:75], v[56:59], v[128:135], v104\n\t" \
  "ds_read_b128 v[28:31], v102 offset:19456\n\t" \
  "buffer_load_dwordx4 v[84:87], v107, %[ru], s94 offen offset:0 sc1\n\t" \
  "s_waitcnt lgkmcnt(7)\n\t" \
  "v_smfmac_f32_16x16x64_bf16 v[68:71], v[60:63], v[0:7], v104\n\t" \
  "v_smfmac_f32_16x16x64_bf16 v[72:75], v[60:63], v[8:15], v104\n\t" \
  "buffer_load_dwordx4 v[88:91], v107, %[ru], s94 offen offset:1024 sc1\n\t" \
  "s_waitcnt lgkmcnt(6)\n\t" \
  "v_smfmac_f32_16x16x64_bf16 v[68:71], v[64:67], v[216:223], v104\n\t" \
  "ds_read_b128 v[0:3], v103 offset:0\n\t" \
  "v_smfmac_f32_16x16x64_bf16 v[72:75], v[64:67], v[224:231], v104\n\t" \
  "ds_read_b128 v[4:7], v103 offset:1024\n\t" \
  "buffer_load_dwordx4 v[92:95], v107, %[ru], s94 offen offset:2048 sc1\n\t" \
  "s_waitcnt lgkmcnt(6)\n\t" \
  "v_smfmac_f32_16x16x64_bf16 v[68:71], v[48:51], a[64:71], v104\n\t" \
  "s_waitcnt lgkmcnt(4)\n\t" \
  "v_smfmac_f32_16x16x64_bf16 v[68:71], v[52:55], v[16:23], v104\n\t" \
  "ds_read_b128 v[8:11], v103 offset:2048\n\t" \
  "buffer_load_dwordx4 v[96:99], v107, %[ru], s94 offen offset:3072 sc1\n\t" \
  "v_smfmac_f32_16x16x64_bf16 v[72:75], v[48:51], a[72:79], v104\n\t" \
  "ds_read_b128 v[12:15], v103 offset:3072\n\t" \
  "s_waitcnt lgkmcnt(4)\n\t" \
  "v_smfmac_f32_16x16x64_bf16 v[72:75], v[52:55], v[24:31], v104\n\t" \
  "v_add_f32 v44, v68, v70\n\t" \
  "v_smfmac_f32_16x16x64_bf16 v[76:79], v[56:59], v[136:143], v104\n\t" \
  "v_add_f32 v45, v69, v71\n\t" \
  "v_cvt_pk_bf16_f32 v44, v44, v45\n\t" \
  "v_smfmac_f32_16x16x64_bf16 v[80:83], v[56:59], v[144:151], v104\n\t" \
  "v_add_f32 v46, v72, v74\n\t" \
  "v_add_f32 v47, v73, v75\n\t" \
  "v_smfmac_f32_16x16x64_bf16 v[76:79], v[60:63], v[200:207], v104\n\t" \
  "v_cvt_pk_bf16_f32 v46, v46, v47\n\t" \
  "ds_write2st64_b32 v105, v44, v46 offset1:1\n\t" \
  "v_smfmac_f32_16x16x64_bf16 v[80:83], v[60:63], v[208:215], v104\n\t" \
  "ds_read_b128 v[56:59], v101 offset:8192\n\t" \
  "v_smfmac_f32_16x16x64_bf16 v[76:79], v[64:67], v[232:239], v104\n\t" \
  "ds_read_b128 v[16:19], v103 offset:32768\n\t" \
  "v_smfmac_f32_16x16x64_bf16 v[80:83], v[64:67], v[240:247], v104\n\t" \
  "ds_read_b128 v[20:23], v103 offset:33792\n\t" \
  "v_smfmac_f32_16x16x64_bf16 v[76:79], v[48:51], a[80:87], v104\n\t" \
  "s_waitcnt lgkmcnt(3)\n\t" \
  "s_barrier\n\t" \
  "v_smfmac_f32_16x16x64_bf16 v[80:83], v[48:51], a[88:95], v104\n\t" \
  "global_load_dword v119, v109, %[kc] sc1\n\t" \
  "v_smfmac_f32_16x16x64_bf16 v[76:79], v[52:55], a[48:55], v104\n\t" \
  "ds_read_b128 v[60:63], v101 offset:1024\n\t" \
  "ds_read_b128 v[24:27], v103 offset:34816\n\t" \
  "v_smfmac_f32_16x16x64_bf16 v[80:83], v[52:55], a[56:63], v104\n\t" \
  "ds_read_b128 v[64:67], v101 offset:10240\n\t" \
  "s_waitcnt lgkmcnt(5)\n\t" \
  "v_smfmac_f32_16x16x64_bf16 v[76:79], v[56:59], v[0:7], v104\n\t" \
  "ds_read_b128 v[48:51], v101 offset:12288\n\t" \
  "ds_read_b128 v[28:31], v103 offset:35840\n\t" \
  "v_smfmac_f32_16x16x64_bf16 v[80:83], v[56:59], v[8:15], v104\n\t" \
  "buffer_load_dwordx4 v[32:35], v100, %[ru], s47 offen offset:0 sc1\n\t" \
  "s_waitcnt lgkmcnt(4)\n\t" \
  "v_smfmac_f32_16x16x64_bf16 v[76:79], v[60:63], a[32:39], v104\n\t" \
  "ds_read_b128 v[52:55], v101 offset:9216\n\t" \
  "v_smfmac_f32_16x16x64_bf16 v[80:83], v[60:63], a[40:47], v104\n\t" \
  "ds_read_b128 v[0:3], v102 offset:0\n\t" \
  "s_waitcnt lgkmcnt(4)\n\t" \
  "v_smfmac_f32_16x16x64_bf16 v[76:79], v[64:67], a[240:247], v104\n\t" \
  "ds_read_b128 v[56:59], v101 offset:13312\n\t" \
  "buffer_load_dwordx4 v[36:39], v100, %[ru], s47 offen offset:1024 sc1\n\t" \
  "v_smfmac_f32_16x16x64_bf16 v[80:83], v[64:67], a[248:255], v104\n\t" \
  "ds_read_b128 v[60:63], v101 offset:0\n\t" \
  "ds_read_b128 v[4:7], v102 offset:1024\n\t" \
  "s_waitcnt lgkmcnt(6)\n\t" \
  "v_smfmac_f32_16x16x64_bf16 v[76:79], v[48:51], v[16:23], v104\n\t" \
  "ds_read_b128 v[8:11], v102 offset:2048\n\t" \
  "s_waitcnt lgkmcnt(6)\n\t" \
  "v_smfmac_f32_16x16x64_bf16 v[80:83], v[48:51], v[24:31], v104\n\t" \
  "ds_read_b128 v[12:15], v102 offset:3072\n\t" \
  "buffer_load_dwordx4 v[40:43], v100, %[ru], s47 offen offset:2048 sc1\n\t" \
  "s_waitcnt lgkmcnt(6)\n\t" \
  "v_smfmac_f32_16x16x64_bf16 v[76:79], v[52:55], a[224:231], v104\n\t" \
  "v_smfmac_f32_16x16x64_bf16 v[80:83], v[52:55], a[232:239], v104\n\t" \
  "s_waitcnt lgkmcnt(4)\n\t" \
  "v_smfmac_f32_16x16x64_bf16 v[76:79], v[56:59], v[184:191], v104\n\t" \
  "s_waitcnt lgkmcnt(2)\n\t" \
  "v_smfmac_f32_16x16x64_bf16 v[76:79], v[60:63], v[0:7], v104\n\t" \
  "v_smfmac_f32_16x16x64_bf16 v[80:83], v[56:59], v[192:199], v104\n\t" \
  "s_waitcnt lgkmcnt(0)\n\t" \
  "v_smfmac_f32_16x16x64_bf16 v[80:83], v[60:63], v[8:15], v104\n\t" \
  "v_add_f32 v20, v76, v78\n\t" \
  "v_add_f32 v21, v77, v79\n\t" \
  "v_cvt_pk_bf16_f32 v20, v20, v21\n\t" \
  "ds_write_b32 v106, v20\n\t" \
  "s_nop 3\n\t" \
  "v_add_f32 v23, v80, v82\n\t" \
  "v_add_f32 v21, v81, v83\n\t" \
  "v_cvt_pk_bf16_f32 v21, v23, v21\n\t" \
  "ds_write_b32 v106, v21 offset:256\n\t" \
  "s_waitcnt lgkmcnt(0)\n\t" \
  "s_barrier\n\t" \
  "v_bfe_i32 v64, v114, 0, 1\n\t" \
  "v_and_b32 v64, 0xff800000, v64\n\t" \
  "v_bfe_i32 v65, v114, 1, 1\n\t" \
  "v_and_b32 v65, 0xff800000, v65\n\t" \
  "v_bfe_i32 v66, v114, 2, 1\n\t" \
  "v_and_b32 v66, 0xff800000, v66\n\t" \
  "v_bfe_i32 v67, v114, 3, 1\n\t" \
  "v_and_b32 v67, 0xff800000, v67\n\t" \
  "v_bfe_i32 v68, v114, 4, 1\n\t" \
  "v_and_b32 v68, 0xff800000, v68\n\t" \
  "v_bfe_i32 v69, v114, 5, 1\n\t" \
  "v_and_b32 v69, 0xff800000, v69\n\t" \
  "v_bfe_i32 v70, v114, 6, 1\n\t" \
  "v_and_b32 v70, 0xff800000, v70\n\t" \
  "v_bfe_i32 v71, v114, 7, 1\n\t" \
  "v_and_b32 v71, 0xff800000, v71\n\t" \
  "s_lshl_b32 s94, s45, 4\n\t" \
  "s_and_b32 s96, s44, 2\n\t" \
  "s_lshl_b32 s96, s96, 13\n\t" \
  "s_and_b32 s76, s44, 1\n\t" \
  "s_lshl_b32 s76, s76, 12\n\t" \
  "s_add_i32 s96, s96, s76\n\t" \
  "s_mov_b32 s95, 1\n\t" \
  "s_sleep 1\n\t" \
  "ds_read_b128 v[0:3], v101 offset:20480\n\t" \
  "ds_read_b128 v[4:7], v101 offset:21504\n\t" \
  "ds_read_b128 v[8:11], v101 offset:22528\n\t" \
  "ds_read_b128 v[12:15], v101 offset:23552\n\t" \
  "ds_read_b128 v[16:19], v101 offset:24576\n\t" \
  "ds_read_b128 v[20:23], v101 offset:25600\n\t" \
  "ds_read_b128 v[24:27], v101 offset:26624\n\t" \
  "ds_read_b128 v[28:31], v101 offset:27648\n\t" \
  "ds_read_b64_tr_b16 v[48:49], v108 offset:0\n\t" \
  "ds_read_b64_tr_b16 v[50:51], v108 offset:64\n\t" \
  "ds_read_b64_tr_b16 v[52:53], v108 offset:512\n\t" \
  "ds_read_b64_tr_b16 v[54:55], v108 offset:576\n\t" \
  "ds_read_b64_tr_b16 v[56:57], v108 offset:1024\n\t" \
  "ds_read_b64_tr_b16 v[58:59], v108 offset:1088\n\t" \
  "ds_read_b64_tr_b16 v[60:61], v108 offset:1536\n\t" \
  "ds_read_b64_tr_b16 v[62:63], v108 offset:1600\n\t" \
  "s_waitcnt vmcnt(4) lgkmcnt(8)\n\t" \
  "v_mfma_f32_16x16x32_bf16 v[64:67], v[0:3], v[84:87], v[64:67]\n\t" \
  "v_mfma_f32_16x16x32_bf16 v[68:71], v[16:19], v[84:87], v[68:71]\n\t" \
  "v_mfma_f32_16x16x32_bf16 v[64:67], v[4:7], v[88:91], v[64:67]\n\t" \
  "v_mfma_f32_16x16x32_bf16 v[68:71], v[20:23], v[88:91], v[68:71]\n\t" \
  "v_mfma_f32_16x16x32_bf16 v[64:67], v[8:11], v[92:95], v[64:67]\n\t" \
  "v_mfma_f32_16x16x32_bf16 v[68:71], v[24:27], v[92:95], v[68:71]\n\t" \
  "v_mfma_f32_16x16x32_bf16 v[64:67], v[12:15], v[96:99], v[64:67]\n\t" \
  "v_mfma_f32_16x16x32_bf16 v[68:71], v[28:31], v[96:99], v[68:71]\n\t" \
  "s_waitcnt lgkmcnt(0)\n\t" \
  "v_mfma_f32_16x16x32_bf16 v[72:75], v[48:51], v[84:87], 0\n\t" \
  "v_mfma_f32_16x16x32_bf16 v[72:75], v[52:55], v[88:91], v[72:75]\n\t" \
  "v_mfma_f32_16x16x32_bf16 v[72:75], v[56:59], v[92:95], v[72:75]\n\t" \
  "v_mfma_f32_16x16x32_bf16 v[72:75], v[60:63], v[96:99], v[72:75]\n\t" \
  "s_barrier\n\t" \
  "v_max3_f32 v76, v64, v65, v66\n\t" \
  "v_max3_f32 v77, v67, v68, v69\n\t" \
  "v_max3_f32 v76, v76, v70, v71\n\t" \
  "s_nop 4\n\t" \
  "v_max3_f32 v77, v77, v72, v73\n\t" \
  "v_max3_f32 v76, v76, v74, v75\n\t" \
  "v_max_f32 v76, v76, v77\n\t" \
  "v_mov_b32 v77, v76\n\t" \
  "s_nop 1\n\t" \
  "v_permlane16_swap_b32 v76, v77\n\t" \
  "v_max_f32 v76, v76, v77\n\t" \
  "v_mov_b32 v77, v76\n\t" \
  "s_nop 1\n\t" \
  "v_permlane32_swap_b32 v76, v77\n\t" \
  "v_max_f32 v76, v76, v77\n\t" \
  "v_mul_f32 v76, %[cs], v76\n\t" \
  "v_fma_f32 v64, v64, %[cs], -v76\n\t" \
  "v_fma_f32 v65, v65, %[cs], -v76\n\t" \
  "v_fma_f32 v66, v66, %[cs], -v76\n\t" \
  "v_fma_f32 v67, v67, %[cs], -v76\n\t" \
  "v_fma_f32 v68, v68, %[cs], -v76\n\t" \
  "v_fma_f32 v69, v69, %[cs], -v76\n\t" \
  "v_fma_f32 v70, v70, %[cs], -v76\n\t" \
  "v_fma_f32 v71, v71, %[cs], -v76\n\t" \
  "v_fma_f32 v72, v72, %[cs], -v76\n\t" \
  "v_fma_f32 v73, v73, %[cs], -v76\n\t" \
  "v_fma_f32 v74, v74, %[cs], -v76\n\t" \
  "v_fma_f32 v75, v75, %[cs], -v76\n\t" \
  "v_exp_f32 v64, v64\n\t" \
  "v_exp_f32 v65, v65\n\t" \
  "v_exp_f32 v66, v66\n\t" \
  "v_exp_f32 v67, v67\n\t" \
  "v_exp_f32 v68, v68\n\t" \
  "v_exp_f32 v69, v69\n\t" \
  "v_exp_f32 v70, v70\n\t" \
  "v_exp_f32 v71, v71\n\t" \
  "v_exp_f32 v72, v72\n\t" \
  "v_exp_f32 v73, v73\n\t" \
  "v_exp_f32 v74, v74\n\t" \
  "v_exp_f32 v75, v75\n\t" \
  "v_add_f32 v76, v72, v73\n\t" \
  "v_add_f32 v77, v74, v75\n\t" \
  "v_add_f32 v78, v64, v65\n\t" \
  "v_add_f32 v79, v66, v67\n\t" \
  "v_add_f32 v76, v76, v77\n\t" \
  "v_add_f32 v78, v78, v68\n\t" \
  "v_add_f32 v79, v79, v69\n\t" \
  "v_add_f32 v78, v78, v70\n\t" \
  "v_add_f32 v79, v79, v71\n\t" \
  "v_fma_f32 v76, v76, 0.5, v78\n\t" \
  "v_add_f32 v76, v76, v79\n\t" \
  "v_mov_b32 v77, v76\n\t" \
  "s_nop 1\n\t" \
  "v_permlane16_swap_b32 v76, v77\n\t" \
  "v_add_f32 v76, v76, v77\n\t" \
  "v_mov_b32 v77, v76\n\t" \
  "s_nop 1\n\t" \
  "v_permlane32_swap_b32 v76, v77\n\t" \
  "v_add_f32 v76, v76, v77\n\t" \
  "v_rcp_f32 v76, v76\n\t" \
  "s_nop 0\n\t" \
  "v_mul_f32 v64, v64, v76\n\t" \
  "v_mul_f32 v65, v65, v76\n\t" \
  "v_mul_f32 v66, v66, v76\n\t" \
  "v_mul_f32 v67, v67, v76\n\t" \
  "v_mul_f32 v68, v68, v76\n\t" \
  "v_mul_f32 v69, v69, v76\n\t" \
  "v_mul_f32 v70, v70, v76\n\t" \
  "v_mul_f32 v71, v71, v76\n\t" \
  "v_mul_f32 v72, v72, v76\n\t" \
  "v_mul_f32 v73, v73, v76\n\t" \
  "v_mul_f32 v74, v74, v76\n\t" \
  "v_mul_f32 v75, v75, v76\n\t" \
  "v_cvt_pk_bf16_f32 v0, v64, v68\n\t" \
  "v_cvt_pk_bf16_f32 v1, v65, v69\n\t" \
  "v_cvt_pk_bf16_f32 v2, v66, v70\n\t" \
  "v_cvt_pk_bf16_f32 v3, v67, v71\n\t" \
  "v_cvt_pk_bf16_f32 v4, v72, v73\n\t" \
  "v_cvt_pk_bf16_f32 v5, v74, v75\n\t" \
  "v_permlane32_swap_b32 v0, v2\n\t" \
  "v_permlane32_swap_b32 v1, v3\n\t" \
  "v_mov_b64 v[6:7], v[4:5]\n\t" \
  "s_mov_b32 exec_hi, 0\n\t" \
  "buffer_load_dword v118, v112, %[rh], s96 offen sc1\n\t" \
  "s_mov_b64 exec, -1\n\t" \
  "buffer_store_dwordx4 v[0:3], v110, %[rh], s94 offen sc1\n\t" \
  "v_permlane16_swap_b32 v4, v6\n\t" \
  "v_permlane16_swap_b32 v5, v7\n\t" \
  "buffer_store_dwordx4 v[4:7], v111, %[rh], s94 offen sc1\n\t" \
  "s_waitcnt vmcnt(3)\n\t" \
  "ds_write_b128 v101, v[32:35] offset:20480\n\t" \
  "ds_write_b128 v101, v[36:39] offset:21504\n\t" \
  "ds_write_b128 v101, v[40:43] offset:22528\n\t" \
  "s_add_i32 s77, s44, 1\n\t" \
  "s_min_u32 s77, s77, 63\n\t" \
  "v_cmp_gt_i32 vcc, s77, v119\n\t" \
  "s_cmp_lg_u64 vcc, 0\n\t" \
  "s_cselect_b32 s42, 32, 0\n\t" \
  "s_add_i32 s41, s56, 1950\n\t" \
  "s_sub_i32 s76, s92, s51\n\t" \
  "s_sub_i32 s77, s93, s53\n\t" \
  "s_cmp_eq_u32 s76, 0\n\t" \
  "s_cbranch_scc1 21f\n\t" \
  "v_cvt_f32_u32 v117, s76\n\t" \
  "v_cvt_f32_u32 v116, s77\n\t" \
  "v_rcp_f32 v117, v117\n\t" \
  "s_mov_b32 s51, s92\n\t" \
  "v_mul_f32 v117, v117, v116\n\t" \
  "v_max_f32 v117, 0x3f028f5c, v117\n\t" \
  "v_min_f32 v117, 0x3ffeb852, v117\n\t" \
  "v_rcp_f32 v116, v117\n\t" \
  "v_mul_f32 v117, 0x4f000000, v117\n\t" \
  "v_mul_f32 v116, 0x47800000, v116\n\t" \
  "v_cvt_u32_f32 v117, v117\n\t" \
  "v_cvt_u32_f32 v116, v116\n\t" \
  "s_mov_b32 s53, s93\n\t" \
  "v_readfirstlane_b32 s46, v117\n\t" \
  "v_readfirstlane_b32 s52, v116\n\t" \
  "21:\n\t" \
  "s_add_i32 s44, s44, 1\n\t" \
  "s_and_b32 s58, s58, 4\n\t" \
  "s_and_b32 s76, s44, 64\n\t" \
  "s_or_b32 s42, s42, s76\n\t" \
  "s_cbranch_scc0 20b\n\t" \
  "s_branch 99f\n\t" \
  "14:\n\t" \
  "s_sleep 3\n\t" \
  "s_add_u32 s59, s59, 0x10000\n\t" \
  "s_cmp_lt_u32 s59, 0x100000\n\t" \
  "s_cbranch_scc1 13b\n\t" \
  "s_mov_b64 exec, -1\n\t" \
  "s_mov_b32 s42, 16\n\t" \
  "s_branch 99f\n\t" \
  "8:\n\t" \
  "s_bitcmp1_b32 s58, 1\n\t" \
  "s_cbranch_scc1 4b\n\t" \
  "s_bitcmp1_b32 s58, 2\n\t" \
  "s_cbranch_scc1 5b\n\t" \
  "v_max3_u32 v116, v32, v33, v34\n\t" \
  "v_max3_u32 v117, v35, v36, v37\n\t" \
  "v_max3_u32 v116, v116, v38, v39\n\t" \
  "v_max_u32 v116, v116, v117\n\t" \
  "v_cmp_eq_u32 vcc, -1, v116\n\t" \
  "s_cbranch_vccz 5b\n\t" \
  "s_branch 0f\n\t" \
  "9:\n\t" \
  "s_bitcmp1_b32 s58, 2\n\t" \
  "s_cbranch_scc1 6b\n\t" \
  "v_max3_u32 v116, v40, v41, v42\n\t" \
  "v_max3_u32 v117, v43, v44, v45\n\t" \
  "v_max3_u32 v116, v116, v46, v47\n\t" \
  "v_max_u32 v116, v116, v117\n\t" \
  "v_cmp_eq_u32 vcc, -1, v116\n\t" \
  "s_cbranch_vccz 6b\n\t" \
  "s_or_b32 s58, s58, 2\n\t" \
  "0:\n\t" \
  "s_or_b32 s58, s58, 1\n\t" \
  "11:\n\t" \
  "buffer_load_dwordx4 v[32:35], v100, %[rh], s40 offen sc1\n\t" \
  "buffer_load_dwordx4 v[36:39], v100, %[rh], s76 offen sc1\n\t" \
  "buffer_load_dwordx4 v[40:43], v100, %[rh], s77 offen sc1\n\t" \
  "buffer_load_dwordx4 v[44:47], v100, %[rh], s78 offen sc1\n\t" \
  "s_waitcnt lgkmcnt(0)\n\t" \
  "s_branch 10b\n\t" \
  "17:\n\t" \
  "s_add_u32 s59, s59, 1\n\t" \
  "s_bitcmp1_b32 s58, 2\n\t" \
  "s_cbranch_scc1 3b\n\t" \
  "s_waitcnt vmcnt(0)\n\t" \
  "v_max_u32 v116, v32, v33\n\t" \
  "v_max3_u32 v116, v116, v34, v35\n\t" \
  "v_max3_u32 v116, v116, v36, v37\n\t" \
  "v_max3_u32 v116, v116, v38, v39\n\t" \
  "v_max3_u32 v116, v116, v40, v41\n\t" \
  "v_max3_u32 v116, v116, v42, v43\n\t" \
  "v_max3_u32 v116, v116, v44, v45\n\t" \
  "v_max3_u32 v116, v116, v46, v47\n\t" \
  "v_cmp_eq_u32 vcc, -1, v116\n\t" \
  "s_cbranch_vccz 3b\n\t" \
  "s_bitcmp1_b32 s59, 6\n\t" \
  "s_cbranch_scc1 12f\n\t" \
  "buffer_load_dwordx4 v[32:35], v100, %[rh], s40 offen sc1\n\t" \
  "buffer_load_dwordx4 v[36:39], v100, %[rh], s76 offen sc1\n\t" \
  "buffer_load_dwordx4 v[40:43], v100, %[rh], s77 offen sc1\n\t" \
  "buffer_load_dwordx4 v[44:47], v100, %[rh], s78 offen sc1\n\t" \
  "s_branch 17b\n\t" \
  "12:\n\t" \
  "s_mov_b32 s42, s58\n\t" \
  "s_branch 99f\n\t" \
  "92:\n\t" \
  "s_mov_b32 s58, s42\n\t" \
  "s_mov_b32 s95, 0\n\t" \
  "20:\n\t" \
  "s_and_b32 s76, s44, 7\n\t" \
  "s_lshl_b32 s76, s76, 15\n\t" \
  "s_add_i32 s40, s54, s76\n\t" \
  "s_and_b32 s76, s44, 3\n\t" \
  "s_lshl_b32 s45, s76, 14\n\t" \
  "s_add_i32 s47, s44, 1\n\t" \
  "s_min_u32 s47, s47, 63\n\t" \
  "s_lshr_b32 s76, s47, 1\n\t" \
  "s_lshl_b32 s47, s47, 20\n\t" \
  "s_add_i32 s47, s47, s55\n\t" \
  "s_bitcmp1_b64 %[sel], s76\n\t" \
  "s_cselect_b32 s76, 0x2000, 0\n\t" \
  "s_add_i32 s47, s47, s76\n\t" \
  "s_lshr_b32 s76, s44, 1\n\t" \
  "s_lshl_b32 s94, s44, 20\n\t" \
  "s_bitcmp1_b64 %[sel], s76\n\t" \
  "s_cselect_b32 s76, 0x2000, 0\n\t" \
  "s_add_i32 s94, s94, s76\n\t" \
  "s_add_i32 s49, s44, -1\n\t" \
  "s_and_b32 s48, s49, 2\n\t" \
  "s_lshl_b32 s48, s48, 13\n\t" \
  "s_and_b32 s76, s49, 1\n\t" \
  "s_lshl_b32 s76, s76, 12\n\t" \
  "s_add_i32 s48, s48, s76\n\t" \
  "s_lshl_b32 s50, s53, 8\n\t" \
  "s_mov_b32 s59, 0\n\t" \
  "s_mov_b32 s92, s51\n\t" \
  "s_mov_b32 s93, s53\n\t" \
  "s_xor_b32 s76, s40, 0x400\n\t" \
  "s_xor_b32 s77, s40, 0x800\n\t" \
  "s_xor_b32 s78, s40, 0xc00\n\t" \
  "s_cmp_lg_u32 s58, 0\n\t" \
  "s_cbranch_scc1 11f\n\t" \
  "10:\n\t" \
  "ds_read_b128 v[48:51], v103 offset:0\n\t" \
  "ds_read_b128 v[52:55], v103 offset:1024\n\t" \
  "ds_read_b128 v[56:59], v103 offset:2048\n\t" \
  "ds_read_b128 v[60:63], v103 offset:3072\n\t" \
  "ds_read_b128 v[16:19], v103 offset:16384\n\t" \
  "ds_read_b128 v[20:23], v103 offset:17408\n\t" \
  "ds_read_b128 v[24:27], v103 offset:18432\n\t" \
  "ds_read_b128 v[28:31], v103 offset:19456\n\t" \
  "ds_read_b128 v[84:87], v103 offset:49152\n\t" \
  "ds_read_b128 v[88:91], v103 offset:50176\n\t" \
  "ds_read_b128 v[92:95], v103 offset:51200\n\t" \
  "ds_read_b128 v[96:99], v103 offset:52224\n\t" \
  "s_waitcnt lgkmcnt(4)\n\t" \
  "ds_read_b128 v[0:3], v102 offset:16384\n\t" \
  "ds_read_b128 v[4:7], v102 offset:17408\n\t" \
  "ds_read_b128 v[8:11], v102 offset:18432\n\t" \
  "ds_read_b128 v[12:15], v102 offset:19456\n\t" \
  "s_bitcmp1_b32 s58, 1\n\t" \
  "s_cselect_b64 s[80:81], -1, 0\n\t" \
  "s_cmp_lg_u32 s58, 0\n\t" \
  "s_cbranch_scc1 17f\n\t" \
  "s_mov_b32 exec_hi, 0\n\t" \
  "s_cmp_eq_u32 s95, 0\n\t" \
  "s_cbranch_scc1 16f\n\t" \
  "s_waitcnt vmcnt(2)\n\t" \
  "v_mov_b32 v116, v118\n\t" \
  "s_branch 15f\n\t" \
  "16:\n\t" \
  "s_memtime vcc\n\t" \
  "s_waitcnt lgkmcnt(0)\n\t" \
  "s_sub_i32 s79, s41, vcc_lo\n\t" \
  "s_cmp_gt_i32 s79, 64\n\t" \
  "s_cbranch_scc1 16b\n\t" \
  "13:\n\t" \
  "buffer_load_dword v116, v112, %[rh], s48 offen sc1\n\t" \
  "s_waitcnt vmcnt(0)\n\t" \
  "15:\n\t" \
  "v_mov_b32 v117, 20000\n\t" \
  "v_cmp_ne_u32_sdwa vcc, v116, s49 src0_sel:BYTE_0 src1_sel:DWORD\n\t" \
  "v_subrev_u32 v116, s50, v116\n\t" \
  "v_ashrrev_i32 v116, 8, v116\n\t" \
  "s_nop 1\n\t" \
  "v_max_i32_dpp v116, v116, v116 quad_perm:[1,0,3,2] row_mask:0xf bank_mask:0xf\n\t" \
  "s_cbranch_vccnz 14f\n\t" \
  "s_nop 0\n\t" \
  "v_max_i32_dpp v116, v116, v116 quad_perm:[2,3,0,1] row_mask:0xf bank_mask:0xf\n\t" \
  "s_nop 1\n\t" \
  "v_max_i32_dpp v116, v116, v116 row_half_mirror row_mask:0xf bank_mask:0xf\n\t" \
  "s_nop 1\n\t" \
  "v_max_i32_dpp v116, v116, v116 row_mirror row_mask:0xf bank_mask:0xf\n\t" \
  "v_med3_i32 v116, v116, 0, v117\n\t" \
  "v_mul_u32_u24 v116, s52, v116\n\t" \
  "v_lshrrev_b32 v116, 16, v116\n\t" \
  "s_mov_b64 exec, -1\n\t" \
  "v_readfirstlane_b32 s84, v116\n\t" \
  "v_readlane_b32 s85, v116, 16\n\t" \
  "s_max_u32 s84, s84, s85\n\t" \
  "s_add_i32 s84, s84, s51\n\t" \
  "1:\n\t" \
  "s_memtime vcc\n\t" \
  "s_waitcnt lgkmcnt(0)\n\t" \
  "s_sub_i32 s79, s84, vcc_lo\n\t" \
  "s_cmp_gt_i32 s79, 128\n\t" \
  "s_cbranch_scc1 1b\n\t" \
  ".p2align 4\n\t" \
  "2:\n\t" \
  "s_sub_i32 s79, s79, 33\n\t" \
  "s_cmp_gt_i32 s79, 56\n\t" \
  "s_cbranch_scc1 2b\n\t" \
  "buffer_load_dwordx4 v[32:35], v100, %[rh], s40 offen sc1\n\t" \
  "s_nop 12\n\t" \
  "buffer_load_dwordx4 v[36:39], v100, %[rh], s76 offen sc1\n\t" \
  "s_nop 12\n\t" \
  "buffer_load_dwordx4 v[40:43], v100, %[rh], s77 offen sc1\n\t" \
  "s_nop 12\n\t" \
  "buffer_load_dwordx4 v[44:47], v100, %[rh], s78 offen sc1\n\t" \
  "s_memtime vcc\n\t" \
  "s_memrealtime s[82:83]\n\t" \
  "s_nop 4\n\t" \
  "s_memrealtime s[84:85]\n\t" \
  "s_nop 4\n\t" \
  "s_memrealtime s[56:57]\n\t" \
  "s_nop 4\n\t" \
  "s_memrealtime s[86:87]\n\t" \
  "s_nop 4\n\t" \
  "s_memrealtime s[88:89]\n\t" \
  "s_nop 4\n\t" \
  "s_memrealtime s[90:91]\n\t" \
  "s_mov_b32 s92, 0\n\t" \
  "s_waitcnt lgkmcnt(0)\n\t" \
  "s_cmp_eq_u32 s82, s84\n\t" \
  "s_addc_u32 s92, s92, 0\n\t" \
  "s_cmp_eq_u32 s82, s56\n\t" \
  "s_addc_u32 s92, s92, 0\n\t" \
  "s_cmp_eq_u32 s82, s86\n\t" \
  "s_addc_u32 s92, s92, 0\n\t" \
  "s_cmp_eq_u32 s82, s88\n\t" \
  "s_addc_u32 s92, s92, 0\n\t" \
  "s_cmp_eq_u32 s82, s90\n\t" \
  "s_addc_u32 s92, s92, 0\n\t" \
  "s_mul_i32 s92, s92, 24\n\t" \
  "s_add_i32 s92, s92, vcc_lo\n\t" \
  "s_lshl4_add_u32 s93, s82, 64\n\t" \
  "3:\n\t" \
  "v_mov_b64 v[68:69], 0\n\t" \
  "v_mov_b64 v[70:71], 0\n\t" \
  "v_mov_b64 v[72:73], 0\n\t" \
  "v_mov_b64 v[74:75], 0\n\t" \
  "v_mov_b64 v[76:77], 0\n\t" \
  "v_mov_b64 v[78:79], 0\n\t" \
  "v_mov_b64 v[80:81], 0\n\t" \
  "v_mov_b64 v[82:83], 0\n\t" \
  "s_waitcnt lgkmcnt(0)\n\t" \
  "s_waitcnt vmcnt(3)\n\t" \
  "v_smfmac_f32_16x16x64_bf16 v[68:71], v[32:35], a[192:199], v104\n\t" \
  "ds_write_b128 v101, v[32:35] offset:8192\n\t" \
  "v_smfmac_f32_16x16x64_bf16 v[72:75], v[32:35], a[200:207], v104\n\t" \
  "v_smfmac_f32_16x16x64_bf16 v[76:79], v[32:35], v[48:55], v104\n\t" \
  "v_smfmac_f32_16x16x64_bf16 v[80:83], v[32:35], v[56:63], v104\n\t" \
  "s_waitcnt vmcnt(2)\n\t" \
  "v_smfmac_f32_16x16x64_bf16 v[68:71], v[36:39], a[208:215], v104\n\t" \
  "ds_write_b128 v101, v[36:39] offset:9216\n\t" \
  "v_smfmac_f32_16x16x64_bf16 v[72:75], v[36:39], a[216:223], v104\n\t" \
  "v_smfmac_f32_16x16x64_bf16 v[76:79], v[36:39], a[224:231], v104\n\t" \
  "v_cmp_u_f32 vcc, v68, v69\n\t" \
  "v_smfmac_f32_16x16x64_bf16 v[80:83], v[36:39], a[232:239], v104\n\t" \
  "v_cmp_u_f32 s[82:83], v70, v71\n\t" \
  "s_waitcnt vmcnt(1)\n\t" \
  "v_smfmac_f32_16x16x64_bf16 v[68:71], v[40:43], v[16:23], v104\n\t" \
  "ds_write_b128 v101, v[40:43] offset:10240\n\t" \
  "v_smfmac_f32_16x16x64_bf16 v[72:75], v[40:43], v[24:31], v104\n\t" \
  "s_or_b64 vcc, vcc, s[82:83]\n\t" \
  "s_or_b64 vcc, vcc, s[80:81]\n\t" \
  "s_waitcnt lgkmcnt(1)\n\t" \
  "v_smfmac_f32_16x16x64_bf16 v[76:79], v[40:43], a[240:247], v104\n\t" \
  "s_cbranch_scc1 8f\n\t" \
  "5:\n\t" \
  "s_barrier\n\t" \
  "4:\n\t" \
  "s_memtime s[56:57]\n\t" \
  "ds_read_b128 v[48:51], v101 offset:12288\n\t" \
  "v_smfmac_f32_16x16x64_bf16 v[80:83], v[40:43], a[248:255], v104\n\t" \
  "ds_read_b128 v[52:55], v101 offset:13312\n\t" \
  "ds_read_b128 v[56:59], v101 offset:0\n\t" \
  "s_waitcnt vmcnt(0)\n\t" \
  "v_smfmac_f32_16x16x64_bf16 v[68:71], v[44:47], v[120:127], v104\n\t" \
  "v_smfmac_f32_16x16x64_bf16 v[72:75], v[44:47], v[128:135], v104\n\t" \
  "ds_write_b128 v101, v[44:47] offset:11264\n\t" \
  "v_smfmac_f32_16x16x64_bf16 v[76:79], v[44:47], v[136:143], v104\n\t" \
  "v_cmp_u_f32 vcc, v68, v69\n\t" \
  "v_smfmac_f32_16x16x64_bf16 v[80:83], v[44:47], v[144:151], v104\n\t" \
  "v_cmp_u_f32 s[82:83], v70, v71\n\t" \
  "s_waitcnt lgkmcnt(3)\n\t" \
  "v_smfmac_f32_16x16x64_bf16 v[68:71], v[48:51], v[152:159], v104\n\t" \
  "ds_read_b128 v[60:63], v101 offset:1024\n\t" \
  "ds_read_b128 v[64:67], v101 offset:4096\n\t" \
  "v_smfmac_f32_16x16x64_bf16 v[72:75], v[48:51], v[160:167], v104\n\t" \
  "s_or_b64 vcc, vcc, s[82:83]\n\t" \
  "s_waitcnt lgkmcnt(2)\n\t" \
  "v_smfmac_f32_16x16x64_bf16 v[68:71], v[52:55], v[168:175], v104\n\t" \
  "s_cbranch_scc1 9f\n\t" \
  "6:\n\t" \
  "s_barrier\n\t" \
  "v_smfmac_f32_16x16x64_bf16 v[72:75], v[52:55], v[176:183], v104\n\t" \
  "ds_read_b128 v[48:51], v101 offset:5120\n\t" \
  "v_smfmac_f32_16x16x64_bf16 v[68:71], v[56:59], a[0:7], v104\n\t" \
  "ds_read_b128 v[52:55], v101 offset:14336\n\t" \
  "v_smfmac_f32_16x16x64_bf16 v[72:75], v[56:59], a[8:15], v104\n\t" \
  "s_waitcnt lgkmcnt(3)\n\t" \
  "v_smfmac_f32_16x16x64_bf16 v[68:71], v[60:63], a[16:23], v104\n\t" \
  "ds_read_b128 v[56:59], v101 offset:15360\n\t" \
  "v_smfmac_f32_16x16x64_bf16 v[72:75], v[60:63], a[24:31], v104\n\t" \
  "s_waitcnt lgkmcnt(3)\n\t" \
  "v_smfmac_f32_16x16x64_bf16 v[68:71], v[64:67], a[96:103], v104\n\t" \
  "ds_read_b128 v[60:63], v101 offset:2048\n\t" \
  "v_smfmac_f32_16x16x64_bf16 v[72:75], v[64:67], a[104:111], v104\n\t" \
  "s_waitcnt lgkmcnt(3)\n\t" \
  "v_smfmac_f32_16x16x64_bf16 v[68:71], v[48:51], a[112:119], v104\n\t" \
  "ds_read_b128 v[64:67], v101 offset:3072\n\t" \
  "ds_read_b128 v[16:19], v102 offset:49152\n\t" \
  "v_smfmac_f32_16x16x64_bf16 v[72:75], v[48:51], a[120:127], v104\n\t" \
  "s_waitcnt lgkmcnt(4)\n\t" \
  "v_smfmac_f32_16x16x64_bf16 v[68:71], v[52:55], v[84:91], v104\n\t" \
  "ds_read_b128 v[48:51], v101 offset:7168\n\t" \
  "ds_read_b128 v[20:23], v102 offset:50176\n\t" \
  "v_smfmac_f32_16x16x64_bf16 v[72:75], v[52:55], v[92:99], v104\n\t" \
  "s_waitcnt lgkmcnt(5)\n\t" \
  "v_smfmac_f32_16x16x64_bf16 v[68:71], v[56:59], v[216:223], v104\n\t" \
  "ds_read_b128 v[52:55], v101 offset:6144\n\t" \
  "ds_read_b128 v[24:27], v102 offset:51200\n\t" \
  "v_smfmac_f32_16x16x64_bf16 v[72:75], v[56:59], v[224:231], v104\n\t" \
  "ds_read_b128 v[28:31], v102 offset:52224\n\t" \
  "s_waitcnt lgkmcnt(7)\n\t" \
  "v_smfmac_f32_16x16x64_bf16 v[68:71], v[60:63], v[0:7], v104\n\t" \
  "buffer_load_dwordx4 v[84:87], v107, %[ru], s94 offen offset:0 sc1\n\t" \
  "v_smfmac_f32_16x16x64_bf16 v[72:75], v[60:63], v[8:15], v104\n\t" \
  "s_waitcnt lgkmcnt(6)\n\t" \
  "v_smfmac_f32_16x16x64_bf16 v[68:71], v[64:67], a[64:71], v104\n\t" \
  "ds_read_b128 v[0:3], v103 offset:32768\n\t" \
  "buffer_load_dwordx4 v[88:91], v107, %[ru], s94 offen offset:1024 sc1\n\t" \
  "v_smfmac_f32_16x16x64_bf16 v[72:75], v[64:67], a[72:79], v104\n\t" \
  "ds_read_b128 v[4:7], v103 offset:33792\n\t" \
  "s_waitcnt lgkmcnt(6)\n\t" \
  "v_smfmac_f32_16x16x64_bf16 v[68:71], v[48:51], a[160:167], v104\n\t" \
  "buffer_load_dwordx4 v[92:95], v107, %[ru], s94 offen offset:2048 sc1\n\t" \
  "s_waitcnt lgkmcnt(4)\n\t" \
  "v_smfmac_f32_16x16x64_bf16 v[68:71], v[52:55], v[16:23], v104\n\t" \
  "ds_read_b128 v[8:11], v103 offset:34816\n\t" \
  "v_smfmac_f32_16x16x64_bf16 v[72:75], v[48:51], a[168:175], v104\n\t" \
  "ds_read_b128 v[12:15], v103 offset:35840\n\t" \
  "buffer_load_dwordx4 v[96:99], v107, %[ru], s94 offen offset:3072 sc1\n\t" \
  "s_waitcnt lgkmcnt(4)\n\t" \
  "v_smfmac_f32_16x16x64_bf16 v[72:75], v[52:55], v[24:31], v104\n\t" \
  "v_add_f32 v44, v68, v70\n\t" \
  "v_smfmac_f32_16x16x64_bf16 v[76:79], v[56:59], v[232:239], v104\n\t" \
  "v_add_f32 v45, v69, v71\n\t" \
  "v_cvt_pk_bf16_f32 v44, v44, v45\n\t" \
  "v_smfmac_f32_16x16x64_bf16 v[80:83], v[56:59], v[240:247], v104\n\t" \
  "v_add_f32 v46, v72, v74\n\t" \
  "v_add_f32 v47, v73, v75\n\t" \
  "v_smfmac_f32_16x16x64_bf16 v[76:79], v[60:63], a[48:55], v104\n\t" \
  "v_cvt_pk_bf16_f32 v46, v46, v47\n\t" \
  "ds_write2st64_b32 v105, v44, v46 offset1:1\n\t" \
  "v_smfmac_f32_16x16x64_bf16 v[80:83], v[60:63], a[56:63], v104\n\t" \
  "ds_read_b128 v[56:59], v101 offset:12288\n\t" \
  "v_smfmac_f32_16x16x64_bf16 v[76:79], v[64:67], a[80:87], v104\n\t" \
  "ds_read_b128 v[16:19], v102 offset:0\n\t" \
  "v_smfmac_f32_16x16x64_bf16 v[80:83], v[64:67], a[88:95], v104\n\t" \
  "ds_read_b128 v[20:23], v102 offset:1024\n\t" \
  "v_smfmac_f32_16x16x64_bf16 v[76:79], v[48:51], a[176:183], v104\n\t" \
  "s_waitcnt lgkmcnt(3)\n\t" \
  "s_barrier\n\t" \
  "v_smfmac_f32_16x16x64_bf16 v[80:83], v[48:51], a[184:191], v104\n\t" \
  "v_smfmac_f32_16x16x64_bf16 v[76:79], v[52:55], a[144:151], v104\n\t" \
  "ds_read_b128 v[60:63], v101 offset:5120\n\t" \
  "ds_read_b128 v[24:27], v102 offset:2048\n\t" \
  "global_load_dword v119, v109, %[kc] sc1\n\t" \
  "v_smfmac_f32_16x16x64_bf16 v[80:83], v[52:55], a[152:159], v104\n\t" \
  "ds_read_b128 v[64:67], v101 offset:14336\n\t" \
  "s_waitcnt lgkmcnt(5)\n\t" \
  "v_smfmac_f32_16x16x64_bf16 v[76:79], v[56:59], v[0:7], v104\n\t" \
  "ds_read_b128 v[48:51], v101 offset:0\n\t" \
  "ds_read_b128 v[28:31], v102 offset:3072\n\t" \
  "v_smfmac_f32_16x16x64_bf16 v[80:83], v[56:59], v[8:15], v104\n\t" \
  "s_waitcnt lgkmcnt(4)\n\t" \
  "v_smfmac_f32_16x16x64_bf16 v[76:79], v[60:63], a[128:135], v104\n\t" \
  "ds_read_b128 v[52:55], v101 offset:13312\n\t" \
  "buffer_load_dwordx4 v[32:35], v100, %[ru], s47 offen offset:0 sc1\n\t" \
  "v_smfmac_f32_16x16x64_bf16 v[80:83], v[60:63], a[136:143], v104\n\t" \
  "ds_read_b128 v[0:3], v102 offset:32768\n\t" \
  "s_waitcnt lgkmcnt(4)\n\t" \
  "v_smfmac_f32_16x16x64_bf16 v[76:79], v[64:67], v[200:207], v104\n\t" \
  "ds_read_b128 v[56:59], v101 offset:1024\n\t" \
  "v_smfmac_f32_16x16x64_bf16 v[80:83], v[64:67], v[208:215], v104\n\t" \
  "ds_read_b128 v[60:63], v101 offset:4096\n\t" \
  "ds_read_b128 v[4:7], v102 offset:33792\n\t" \
  "buffer_load_dwordx4 v[36:39], v100, %[ru], s47 offen offset:1024 sc1\n\t" \
  "s_waitcnt lgkmcnt(6)\n\t" \
  "v_smfmac_f32_16x16x64_bf16 v[76:79], v[48:51], v[16:23], v104\n\t" \
  "ds_read_b128 v[8:11], v102 offset:34816\n\t" \
  "s_waitcnt lgkmcnt(6)\n\t" \
  "v_smfmac_f32_16x16x64_bf16 v[80:83], v[48:51], v[24:31], v104\n\t" \
  "ds_read_b128 v[12:15], v102 offset:35840\n\t" \
  "s_waitcnt lgkmcnt(6)\n\t" \
  "v_smfmac_f32_16x16x64_bf16 v[76:79], v[52:55], v[184:191], v104\n\t" \
  "buffer_load_dwordx4 v[40:43], v100, %[ru], s47 offen offset:2048 sc1\n\t" \
  "v_smfmac_f32_16x16x64_bf16 v[80:83], v[52:55], v[192:199], v104\n\t" \
  "s_waitcnt lgkmcnt(4)\n\t" \
  "v_smfmac_f32_16x16x64_bf16 v[76:79], v[56:59], a[32:39], v104\n\t" \
  "s_waitcnt lgkmcnt(2)\n\t" \
  "v_smfmac_f32_16x16x64_bf16 v[76:79], v[60:63], v[0:7], v104\n\t" \
  "v_smfmac_f32_16x16x64_bf16 v[80:83], v[56:59], a[40:47], v104\n\t" \
  "s_waitcnt lgkmcnt(0)\n\t" \
  "v_smfmac_f32_16x16x64_bf16 v[80:83], v[60:63], v[8:15], v104\n\t" \
  "v_add_f32 v20, v76, v78\n\t" \
  "v_add_f32 v21, v77, v79\n\t" \
  "v_cvt_pk_bf16_f32 v20, v20, v21\n\t" \
  "ds_write_b32 v106, v20\n\t" \
  "s_nop 3\n\t" \
  "v_add_f32 v23, v80, v82\n\t" \
  "v_add_f32 v21, v81, v83\n\t" \
  "v_cvt_pk_bf16_f32 v21, v23, v21\n\t" \
  "ds_write_b32 v106, v21 offset:256\n\t" \
  "s_waitcnt lgkmcnt(0)\n\t" \
  "s_barrier\n\t" \
  "v_bfe_i32 v64, v114, 0, 1\n\t" \
  "v_and_b32 v64, 0xff800000, v64\n\t" \
  "v_bfe_i32 v65, v114, 1, 1\n\t" \
  "v_and_b32 v65, 0xff800000, v65\n\t" \
  "v_bfe_i32 v66, v114, 2, 1\n\t" \
  "v_and_b32 v66, 0xff800000, v66\n\t" \
  "v_bfe_i32 v67, v114, 3, 1\n\t" \
  "v_and_b32 v67, 0xff800000, v67\n\t" \
  "v_bfe_i32 v68, v114, 4, 1\n\t" \
  "v_and_b32 v68, 0xff800000, v68\n\t" \
  "v_bfe_i32 v69, v114, 5, 1\n\t" \
  "v_and_b32 v69, 0xff800000, v69\n\t" \
  "v_bfe_i32 v70, v114, 6, 1\n\t" \
  "v_and_b32 v70, 0xff800000, v70\n\t" \
  "v_bfe_i32 v71, v114, 7, 1\n\t" \
  "v_and_b32 v71, 0xff800000, v71\n\t" \
  "s_lshl_b32 s94, s45, 4\n\t" \
  "s_and_b32 s96, s44, 2\n\t" \
  "s_lshl_b32 s96, s96, 13\n\t" \
  "s_and_b32 s76, s44, 1\n\t" \
  "s_lshl_b32 s76, s76, 12\n\t" \
  "s_add_i32 s96, s96, s76\n\t" \
  "s_mov_b32 s95, 1\n\t" \
  "s_sleep 1\n\t" \
  "ds_read_b128 v[0:3], v101 offset:20480\n\t" \
  "ds_read_b128 v[4:7], v101 offset:21504\n\t" \
  "ds_read_b128 v[8:11], v101 offset:22528\n\t" \
  "ds_read_b128 v[12:15], v101 offset:23552\n\t" \
  "ds_read_b128 v[16:19], v101 offset:24576\n\t" \
  "ds_read_b128 v[20:23], v101 offset:25600\n\t" \
  "ds_read_b128 v[24:27], v101 offset:26624\n\t" \
  "ds_read_b128 v[28:31], v101 offset:27648\n\t" \
  "ds_read_b64_tr_b16 v[48:49], v108 offset:0\n\t" \
  "ds_read_b64_tr_b16 v[50:51], v108 offset:64\n\t" \
  "ds_read_b64_tr_b16 v[52:53], v108 offset:512\n\t" \
  "ds_read_b64_tr_b16 v[54:55], v108 offset:576\n\t" \
  "ds_read_b64_tr_b16 v[56:57], v108 offset:1024\n\t" \
  "ds_read_b64_tr_b16 v[58:59], v108 offset:1088\n\t" \
  "ds_read_b64_tr_b16 v[60:61], v108 offset:1536\n\t" \
  "ds_read_b64_tr_b16 v[62:63], v108 offset:1600\n\t" \
  "s_waitcnt vmcnt(4) lgkmcnt(8)\n\t" \
  "v_mfma_f32_16x16x32_bf16 v[64:67], v[0:3], v[84:87], v[64:67]\n\t" \
  "v_mfma_f32_16x16x32_bf16 v[68:71], v[16:19], v[84:87], v[68:71]\n\t" \
  "v_mfma_f32_16x16x32_bf16 v[64:67], v[4:7], v[88:91], v[64:67]\n\t" \
  "v_mfma_f32_16x16x32_bf16 v[68:71], v[20:23], v[88:91], v[68:71]\n\t" \
  "v_mfma_f32_16x16x32_bf16 v[64:67], v[8:11], v[92:95], v[64:67]\n\t" \
  "v_mfma_f32_16x16x32_bf16 v[68:71], v[24:27], v[92:95], v[68:71]\n\t" \
  "v_mfma_f32_16x16x32_bf16 v[64:67], v[12:15], v[96:99], v[64:67]\n\t" \
  "v_mfma_f32_16x16x32_bf16 v[68:71], v[28:31], v[96:99], v[68:71]\n\t" \
  "s_waitcnt lgkmcnt(0)\n\t" \
  "v_mfma_f32_16x16x32_bf16 v[72:75], v[48:51], v[84:87], 0\n\t" \
  "v_mfma_f32_16x16x32_bf16 v[72:75], v[52:55], v[88:91], v[72:75]\n\t" \
  "v_mfma_f32_16x16x32_bf16 v[72:75], v[56:59], v[92:95], v[72:75]\n\t" \
  "v_mfma_f32_16x16x32_bf16 v[72:75], v[60:63], v[96:99], v[72:75]\n\t" \
  "s_barrier\n\t" \
  "v_max3_f32 v76, v64, v65, v66\n\t" \
  "v_max3_f32 v77, v67, v68, v69\n\t" \
  "v_max3_f32 v76, v76, v70, v71\n\t" \
  "s_nop 4\n\t" \
  "v_max3_f32 v77, v77, v72, v73\n\t" \
  "v_max3_f32 v76, v76, v74, v75\n\t" \
  "v_max_f32 v76, v76, v77\n\t" \
  "v_mov_b32 v77, v76\n\t" \
  "s_nop 1\n\t" \
  "v_permlane16_swap_b32 v76, v77\n\t" \
  "v_max_f32 v76, v76, v77\n\t" \
  "v_mov_b32 v77, v76\n\t" \
  "s_nop 1\n\t" \
  "v_permlane32_swap_b32 v76, v77\n\t" \
  "v_max_f32 v76, v76, v77\n\t" \
  "v_mul_f32 v76, %[cs], v76\n\t" \
  "v_fma_f32 v64, v64, %[cs], -v76\n\t" \
  "v_fma_f32 v65, v65, %[cs], -v76\n\t" \
  "v_fma_f32 v66, v66, %[cs], -v76\n\t" \
  "v_fma_f32 v67, v67, %[cs], -v76\n\t" \
  "v_fma_f32 v68, v68, %[cs], -v76\n\t" \
  "v_fma_f32 v69, v69, %[cs], -v76\n\t" \
  "v_fma_f32 v70, v70, %[cs], -v76\n\t" \
  "v_fma_f32 v71, v71, %[cs], -v76\n\t" \
  "v_fma_f32 v72, v72, %[cs], -v76\n\t" \
  "v_fma_f32 v73, v73, %[cs], -v76\n\t" \
  "v_fma_f32 v74, v74, %[cs], -v76\n\t" \
  "v_fma_f32 v75, v75, %[cs], -v76\n\t" \
  "v_exp_f32 v64, v64\n\t" \
  "v_exp_f32 v65, v65\n\t" \
  "v_exp_f32 v66, v66\n\t" \
  "v_exp_f32 v67, v67\n\t" \
  "v_exp_f32 v68, v68\n\t" \
  "v_exp_f32 v69, v69\n\t" \
  "v_exp_f32 v70, v70\n\t" \
  "v_exp_f32 v71, v71\n\t" \
  "v_exp_f32 v72, v72\n\t" \
  "v_exp_f32 v73, v73\n\t" \
  "v_exp_f32 v74, v74\n\t" \
  "v_exp_f32 v75, v75\n\t" \
  "v_add_f32 v76, v72, v73\n\t" \
  "v_add_f32 v77, v74, v75\n\t" \
  "v_add_f32 v78, v64, v65\n\t" \
  "v_add_f32 v79, v66, v67\n\t" \
  "v_add_f32 v76, v76, v77\n\t" \
  "v_add_f32 v78, v78, v68\n\t" \
  "v_add_f32 v79, v79, v69\n\t" \
  "v_add_f32 v78, v78, v70\n\t" \
  "v_add_f32 v79, v79, v71\n\t" \
  "v_fma_f32 v76, v76, 0.5, v78\n\t" \
  "v_add_f32 v76, v76, v79\n\t" \
  "v_mov_b32 v77, v76\n\t" \
  "s_nop 1\n\t" \
  "v_permlane16_swap_b32 v76, v77\n\t" \
  "v_add_f32 v76, v76, v77\n\t" \
  "v_mov_b32 v77, v76\n\t" \
  "s_nop 1\n\t" \
  "v_permlane32_swap_b32 v76, v77\n\t" \
  "v_add_f32 v76, v76, v77\n\t" \
  "v_rcp_f32 v76, v76\n\t" \
  "s_nop 0\n\t" \
  "v_mul_f32 v64, v64, v76\n\t" \
  "v_mul_f32 v65, v65, v76\n\t" \
  "v_mul_f32 v66, v66, v76\n\t" \
  "v_mul_f32 v67, v67, v76\n\t" \
  "v_mul_f32 v68, v68, v76\n\t" \
  "v_mul_f32 v69, v69, v76\n\t" \
  "v_mul_f32 v70, v70, v76\n\t" \
  "v_mul_f32 v71, v71, v76\n\t" \
  "v_mul_f32 v72, v72, v76\n\t" \
  "v_mul_f32 v73, v73, v76\n\t" \
  "v_mul_f32 v74, v74, v76\n\t" \
  "v_mul_f32 v75, v75, v76\n\t" \
  "v_cvt_pk_bf16_f32 v0, v64, v68\n\t" \
  "v_cvt_pk_bf16_f32 v1, v65, v69\n\t" \
  "v_cvt_pk_bf16_f32 v2, v66, v70\n\t" \
  "v_cvt_pk_bf16_f32 v3, v67, v71\n\t" \
  "v_cvt_pk_bf16_f32 v4, v72, v73\n\t" \
  "v_cvt_pk_bf16_f32 v5, v74, v75\n\t" \
  "v_permlane32_swap_b32 v0, v2\n\t" \
  "v_permlane32_swap_b32 v1, v3\n\t" \
  "v_mov_b64 v[6:7], v[4:5]\n\t" \
  "s_mov_b32 exec_hi, 0\n\t" \
  "buffer_load_dword v118, v112, %[rh], s96 offen sc1\n\t" \
  "s_mov_b64 exec, -1\n\t" \
  "buffer_store_dwordx4 v[0:3], v110, %[rh], s94 offen sc1\n\t" \
  "v_permlane16_swap_b32 v4, v6\n\t" \
  "v_permlane16_swap_b32 v5, v7\n\t" \
  "buffer_store_dwordx4 v[4:7], v111, %[rh], s94 offen sc1\n\t" \
  "s_waitcnt vmcnt(3)\n\t" \
  "ds_write_b128 v101, v[32:35] offset:23552\n\t" \
  "ds_write_b128 v101, v[36:39] offset:24576\n\t" \
  "ds_write_b128 v101, v[40:43] offset:25600\n\t" \
  "s_add_i32 s77, s44, 1\n\t" \
  "s_min_u32 s77, s77, 63\n\t" \
  "v_cmp_gt_i32 vcc, s77, v119\n\t" \
  "s_cmp_lg_u64 vcc, 0\n\t" \
  "s_cselect_b32 s42, 32, 0\n\t" \
  "s_add_i32 s41, s56, 1950\n\t" \
  "s_sub_i32 s76, s92, s51\n\t" \
  "s_sub_i32 s77, s93, s53\n\t" \
  "s_cmp_eq_u32 s76, 0\n\t" \
  "s_cbranch_scc1 21f\n\t" \
  "v_cvt_f32_u32 v117, s76\n\t" \
  "v_cvt_f32_u32 v116, s77\n\t" \
  "v_rcp_f32 v117, v117\n\t" \
  "s_mov_b32 s51, s92\n\t" \
  "v_mul_f32 v117, v117, v116\n\t" \
  "v_max_f32 v117, 0x3f028f5c, v117\n\t" \
  "v_min_f32 v117, 0x3ffeb852, v117\n\t" \
  "v_rcp_f32 v116, v117\n\t" \
  "v_mul_f32 v117, 0x4f000000, v117\n\t" \
  "v_mul_f32 v116, 0x47800000, v116\n\t" \
  "v_cvt_u32_f32 v117, v117\n\t" \
  "v_cvt_u32_f32 v116, v116\n\t" \
  "s_mov_b32 s53, s93\n\t" \
  "v_readfirstlane_b32 s46, v117\n\t" \
  "v_readfirstlane_b32 s52, v116\n\t" \
  "21:\n\t" \
  "s_add_i32 s44, s44, 1\n\t" \
  "s_and_b32 s58, s58, 4\n\t" \
  "s_and_b32 s76, s44, 64\n\t" \
  "s_or_b32 s42, s42, s76\n\t" \
  "s_cbranch_scc0 20b\n\t" \
  "s_branch 99f\n\t" \
  "14:\n\t" \
  "s_sleep 3\n\t" \
  "s_add_u32 s59, s59, 0x10000\n\t" \
  "s_cmp_lt_u32 s59, 0x100000\n\t" \
  "s_cbranch_scc1 13b\n\t" \
  "s_mov_b64 exec, -1\n\t" \
  "s_mov_b32 s42, 16\n\t" \
  "s_branch 99f\n\t" \
  "8:\n\t" \
  "s_bitcmp1_b32 s58, 1\n\t" \
  "s_cbranch_scc1 4b\n\t" \
  "s_bitcmp1_b32 s58, 2\n\t" \
  "s_cbranch_scc1 5b\n\t" \
  "v_max3_u32 v116, v32, v33, v34\n\t" \
  "v_max3_u32 v117, v35, v36, v37\n\t" \
  "v_max3_u32 v116, v116, v38, v39\n\t" \
  "v_max_u32 v116, v116, v117\n\t" \
  "v_cmp_eq_u32 vcc, -1, v116\n\t" \
  "s_cbranch_vccz 5b\n\t" \
  "s_branch 0f\n\t" \
  "9:\n\t" \
  "s_bitcmp1_b32 s58, 2\n\t" \
  "s_cbranch_scc1 6b\n\t" \
  "v_max3_u32 v116, v40, v41, v42\n\t" \
  "v_max3_u32 v117, v43, v44, v45\n\t" \
  "v_max3_u32 v116, v116, v46, v47\n\t" \
  "v_max_u32 v116, v116, v117\n\t" \
  "v_cmp_eq_u32 vcc, -1, v116\n\t" \
  "s_cbranch_vccz 6b\n\t" \
  "s_or_b32 s58, s58, 2\n\t" \
  "0:\n\t" \
  "s_or_b32 s58, s58, 1\n\t" \
  "11:\n\t" \
  "buffer_load_dwordx4 v[32:35], v100, %[rh], s40 offen sc1\n\t" \
  "buffer_load_dwordx4 v[36:39], v100, %[rh], s76 offen sc1\n\t" \
  "buffer_load_dwordx4 v[40:43], v100, %[rh], s77 offen sc1\n\t" \
  "buffer_load_dwordx4 v[44:47], v100, %[rh], s78 offen sc1\n\t" \
  "s_waitcnt lgkmcnt(0)\n\t" \
  "s_branch 10b\n\t" \
  "17:\n\t" \
  "s_add_u32 s59, s59, 1\n\t" \
  "s_bitcmp1_b32 s58, 2\n\t" \
  "s_cbranch_scc1 3b\n\t" \
  "s_waitcnt vmcnt(0)\n\t" \
  "v_max_u32 v116, v32, v33\n\t" \
  "v_max3_u32 v116, v116, v34, v35\n\t" \
  "v_max3_u32 v116, v116, v36, v37\n\t" \
  "v_max3_u32 v116, v116, v38, v39\n\t" \
  "v_max3_u32 v116, v116, v40, v41\n\t" \
  "v_max3_u32 v116, v116, v42, v43\n\t" \
  "v_max3_u32 v116, v116, v44, v45\n\t" \
  "v_max3_u32 v116, v116, v46, v47\n\t" \
  "v_cmp_eq_u32 vcc, -1, v116\n\t" \
  "s_cbranch_vccz 3b\n\t" \
  "s_bitcmp1_b32 s59, 6\n\t" \
  "s_cbranch_scc1 12f\n\t" \
  "buffer_load_dwordx4 v[32:35], v100, %[rh], s40 offen sc1\n\t" \
  "buffer_load_dwordx4 v[36:39], v100, %[rh], s76 offen sc1\n\t" \
  "buffer_load_dwordx4 v[40:43], v100, %[rh], s77 offen sc1\n\t" \
  "buffer_load_dwordx4 v[44:47], v100, %[rh], s78 offen sc1\n\t" \
  "s_branch 17b\n\t" \
  "12:\n\t" \
  "s_mov_b32 s42, s58\n\t" \
  "s_branch 99f\n\t" \
  "93:\n\t" \
  "s_mov_b32 s58, s42\n\t" \
  "20:\n\t" \
  "s_and_b32 s76, s44, 7\n\t" \
  "s_lshl_b32 s76, s76, 15\n\t" \
  "s_add_i32 s40, s54, s76\n\t" \
  "s_and_b32 s76, s44, 3\n\t" \
  "s_lshl_b32 s45, s76, 14\n\t" \
  "s_add_i32 s47, s44, 1\n\t" \
  "s_min_u32 s47, s47, 63\n\t" \
  "s_lshr_b32 s76, s47, 1\n\t" \
  "s_lshl_b32 s47, s47, 20\n\t" \
  "s_add_i32 s47, s47, s55\n\t" \
  "s_bitcmp1_b64 %[sel], s76\n\t" \
  "s_cselect_b32 s76, 0x2000, 0\n\t" \
  "s_add_i32 s47, s47, s76\n\t" \
  "s_add_i32 s49, s44, -1\n\t" \
  "s_and_b32 s48, s49, 2\n\t" \
  "s_lshl_b32 s48, s48, 13\n\t" \
  "s_and_b32 s76, s49, 1\n\t" \
  "s_lshl_b32 s76, s76, 12\n\t" \
  "s_add_i32 s48, s48, s76\n\t" \
  "s_lshl_b32 s50, s53, 8\n\t" \
  "s_mov_b32 s59, 0\n\t" \
  "s_mov_b32 s92, s51\n\t" \
  "s_mov_b32 s93, s53\n\t" \
  "s_xor_b32 s76, s40, 0x400\n\t" \
  "s_xor_b32 s77, s40, 0x800\n\t" \
  "s_xor_b32 s78, s40, 0xc00\n\t" \
  "s_cmp_lg_u32 s58, 0\n\t" \
  "s_cbranch_scc1 11f\n\t" \
  "10:\n\t" \
  "ds_read_b128 v[48:51], v103 offset:32768\n\t" \
  "ds_read_b128 v[52:55], v103 offset:33792\n\t" \
  "ds_read_b128 v[56:59], v103 offset:34816\n\t" \
  "ds_read_b128 v[60:63], v103 offset:35840\n\t" \
  "ds_read_b128 v[16:19], v103 offset:49152\n\t" \
  "ds_read_b128 v[20:23], v103 offset:50176\n\t" \
  "ds_read_b128 v[24:27], v103 offset:51200\n\t" \
  "ds_read_b128 v[28:31], v103 offset:52224\n\t" \
  "ds_read_b128 v[84:87], v102 offset:16384\n\t" \
  "ds_read_b128 v[88:91], v102 offset:17408\n\t" \
  "ds_read_b128 v[92:95], v102 offset:18432\n\t" \
  "ds_read_b128 v[96:99], v102 offset:19456\n\t" \
  "s_waitcnt lgkmcnt(4)\n\t" \
  "ds_read_b128 v[0:3], v102 offset:49152\n\t" \
  "ds_read_b128 v[4:7], v102 offset:50176\n\t" \
  "ds_read_b128 v[8:11], v102 offset:51200\n\t" \
  "ds_read_b128 v[12:15], v102 offset:52224\n\t" \
  "s_bitcmp1_b32 s58, 1\n\t" \
  "s_cselect_b64 s[80:81], -1, 0\n\t" \
  "s_cmp_lg_u32 s58, 0\n\t" \
  "s_cbranch_scc1 17f\n\t" \
  "s_mov_b32 exec_hi, 0\n\t" \
  "16:\n\t" \
  "s_memtime vcc\n\t" \
  "s_waitcnt lgkmcnt(0)\n\t" \
  "s_sub_i32 s79, s41, vcc_lo\n\t" \
  "s_cmp_gt_i32 s79, 64\n\t" \
  "s_cbranch_scc1 16b\n\t" \
  "13:\n\t" \
  "buffer_load_dword v116, v112, %[rh], s48 offen sc1\n\t" \
  "s_waitcnt vmcnt(0)\n\t" \
  "15:\n\t" \
  "v_mov_b32 v117, 20000\n\t" \
  "v_cmp_ne_u32_sdwa vcc, v116, s49 src0_sel:BYTE_0 src1_sel:DWORD\n\t" \
  "v_subrev_u32 v116, s50, v116\n\t" \
  "v_ashrrev_i32 v116, 8, v116\n\t" \
  "s_nop 1\n\t" \
  "v_max_i32_dpp v116, v116, v116 quad_perm:[1,0,3,2] row_mask:0xf bank_mask:0xf\n\t" \
  "s_cbranch_vccnz 14f\n\t" \
  "s_nop 0\n\t" \
  "v_max_i32_dpp v116, v116, v116 quad_perm:[2,3,0,1] row_mask:0xf bank_mask:0xf\n\t" \
  "s_nop 1\n\t" \
  "v_max_i32_dpp v116, v116, v116 row_half_mirror row_mask:0xf bank_mask:0xf\n\t" \
  "s_nop 1\n\t" \
  "v_max_i32_dpp v116, v116, v116 row_mirror row_mask:0xf bank_mask:0xf\n\t" \
  "v_med3_i32 v116, v116, 0, v117\n\t" \
  "v_mul_u32_u24 v116, s52, v116\n\t" \
  "v_lshrrev_b32 v116, 16, v116\n\t" \
  "s_mov_b64 exec, -1\n\t" \
  "v_readfirstlane_b32 s84, v116\n\t" \
  "v_readlane_b32 s85, v116, 16\n\t" \
  "s_max_u32 s84, s84, s85\n\t" \
  "s_add_i32 s84, s84, s51\n\t" \
  "1:\n\t" \
  "s_memtime vcc\n\t" \
  "s_waitcnt lgkmcnt(0)\n\t" \
  "s_sub_i32 s79, s84, vcc_lo\n\t" \
  "s_cmp_gt_i32 s79, 128\n\t" \
  "s_cbranch_scc1 1b\n\t" \
  ".p2align 4\n\t" \
  "2:\n\t" \
  "s_sub_i32 s79, s79, 33\n\t" \
  "s_cmp_gt_i32 s79, 56\n\t" \
  "s_cbranch_scc1 2b\n\t" \
  "buffer_load_dwordx4 v[32:35], v100, %[rh], s40 offen sc1\n\t" \
  "s_nop 12\n\t" \
  "buffer_load_dwordx4 v[36:39], v100, %[rh], s76 offen sc1\n\t" \
  "s_nop 12\n\t" \
  "buffer_load_dwordx4 v[40:43], v100, %[rh], s77 offen sc1\n\t" \
  "s_nop 12\n\t" \
  "buffer_load_dwordx4 v[44:47], v100, %[rh], s78 offen sc1\n\t" \
  "s_memtime vcc\n\t" \
  "s_memrealtime s[82:83]\n\t" \
  "s_nop 4\n\t" \
  "s_memrealtime s[84:85]\n\t" \
  "s_nop 4\n\t" \
  "s_memrealtime s[56:57]\n\t" \
  "s_nop 4\n\t" \
  "s_memrealtime s[86:87]\n\t" \
  "s_nop 4\n\t" \
  "s_memrealtime s[88:89]\n\t" \
  "s_nop 4\n\t" \
  "s_memrealtime s[90:91]\n\t" \
  "s_mov_b32 s92, 0\n\t" \
  "s_waitcnt lgkmcnt(0)\n\t" \
  "s_cmp_eq_u32 s82, s84\n\t" \
  "s_addc_u32 s92, s92, 0\n\t" \
  "s_cmp_eq_u32 s82, s56\n\t" \
  "s_addc_u32 s92, s92, 0\n\t" \
  "s_cmp_eq_u32 s82, s86\n\t" \
  "s_addc_u32 s92, s92, 0\n\t" \
  "s_cmp_eq_u32 s82, s88\n\t" \
  "s_addc_u32 s92, s92, 0\n\t" \
  "s_cmp_eq_u32 s82, s90\n\t" \
  "s_addc_u32 s92, s92, 0\n\t" \
  "s_mul_i32 s92, s92, 24\n\t" \
  "s_add_i32 s92, s92, vcc_lo\n\t" \
  "s_lshl4_add_u32 s93, s82, 64\n\t" \
  "3:\n\t" \
  "v_mov_b64 v[68:69], 0\n\t" \
  "v_mov_b64 v[70:71], 0\n\t" \
  "v_mov_b64 v[72:73], 0\n\t" \
  "v_mov_b64 v[74:75], 0\n\t" \
  "v_mov_b64 v[76:77], 0\n\t" \
  "v_mov_b64 v[78:79], 0\n\t" \
  "v_mov_b64 v[80:81], 0\n\t" \
  "v_mov_b64 v[82:83], 0\n\t" \
  "s_waitcnt lgkmcnt(0)\n\t" \
  "s_waitcnt vmcnt(3)\n\t" \
  "v_smfmac_f32_16x16x64_bf16 v[68:71], v[32:35], v[152:159], v104\n\t" \
  "ds_write_b128 v101, v[32:35] offset:12288\n\t" \
  "v_smfmac_f32_16x16x64_bf16 v[72:75], v[32:35], v[160:167], v104\n\t" \
  "v_smfmac_f32_16x16x64_bf16 v[76:79], v[32:35], v[48:55], v104\n\t" \
  "v_smfmac_f32_16x16x64_bf16 v[80:83], v[32:35], v[56:63], v104\n\t" \
  "s_waitcnt vmcnt(2)\n\t" \
  "v_smfmac_f32_16x16x64_bf16 v[68:71], v[36:39], v[168:175], v104\n\t" \
  "ds_write_b128 v101, v[36:39] offset:13312\n\t" \
  "v_smfmac_f32_16x16x64_bf16 v[72:75], v[36:39], v[176:183], v104\n\t" \
  "v_smfmac_f32_16x16x64_bf16 v[76:79], v[36:39], v[184:191], v104\n\t" \
  "v_cmp_u_f32 vcc, v68, v69\n\t" \
  "v_smfmac_f32_16x16x64_bf16 v[80:83], v[36:39], v[192:199], v104\n\t" \
  "v_cmp_u_f32 s[82:83], v70, v71\n\t" \
  "s_waitcnt vmcnt(1)\n\t" \
  "v_smfmac_f32_16x16x64_bf16 v[68:71], v[40:43], v[16:23], v104\n\t" \
  "ds_write_b128 v101, v[40:43] offset:14336\n\t" \
  "v_smfmac_f32_16x16x64_bf16 v[72:75], v[40:43], v[24:31], v104\n\t" \
  "s_or_b64 vcc, vcc, s[82:83]\n\t" \
  "s_or_b64 vcc, vcc, s[80:81]\n\t" \
  "s_waitcnt lgkmcnt(1)\n\t" \
  "v_smfmac_f32_16x16x64_bf16 v[76:79], v[40:43], v[200:207], v104\n\t" \
  "s_cbranch_scc1 8f\n\t" \
  "5:\n\t" \
  "s_barrier\n\t" \
  "4:\n\t" \
  "s_memtime s[56:57]\n\t" \
  "ds_read_b128 v[48:51], v101 offset:0\n\t" \
  "v_smfmac_f32_16x16x64_bf16 v[80:83], v[40:43], v[208:215], v104\n\t" \
  "ds_read_b128 v[52:55], v101 offset:1024\n\t" \
  "ds_read_b128 v[56:59], v101 offset:4096\n\t" \
  "s_waitcnt vmcnt(0)\n\t" \
  "v_smfmac_f32_16x16x64_bf16 v[68:71], v[44:47], v[216:223], v104\n\t" \
  "v_smfmac_f32_16x16x64_bf16 v[72:75], v[44:47], v[224:231], v104\n\t" \
  "ds_write_b128 v101, v[44:47] offset:15360\n\t" \
  "v_smfmac_f32_16x16x64_bf16 v[76:79], v[44:47], v[232:239], v104\n\t" \
  "v_cmp_u_f32 vcc, v68, v69\n\t" \
  "v_smfmac_f32_16x16x64_bf16 v[80:83], v[44:47], v[240:247], v104\n\t" \
  "v_cmp_u_f32 s[82:83], v70, v71\n\t" \
  "s_waitcnt lgkmcnt(3)\n\t" \
  "v_smfmac_f32_16x16x64_bf16 v[68:71], v[48:51], a[0:7], v104\n\t" \
  "ds_read_b128 v[60:63], v101 offset:5120\n\t" \
  "ds_read_b128 v[64:67], v101 offset:8192\n\t" \
  "v_smfmac_f32_16x16x64_bf16 v[72:75], v[48:51], a[8:15], v104\n\t" \
  "s_or_b64 vcc, vcc, s[82:83]\n\t" \
  "s_waitcnt lgkmcnt(2)\n\t" \
  "v_smfmac_f32_16x16x64_bf16 v[68:71], v[52:55], a[16:23], v104\n\t" \
  "s_cbranch_scc1 9f\n\t" \
  "6:\n\t" \
  "s_barrier\n\t" \
  "v_smfmac_f32_16x16x64_bf16 v[72:75], v[52:55], a[24:31], v104\n\t" \
  "ds_read_b128 v[48:51], v101 offset:9216\n\t" \
  "v_smfmac_f32_16x16x64_bf16 v[68:71], v[56:59], a[96:103], v104\n\t" \
  "ds_read_b128 v[52:55], v101 offset:2048\n\t" \
  "v_smfmac_f32_16x16x64_bf16 v[72:75], v[56:59], a[104:111], v104\n\t" \
  "s_waitcnt lgkmcnt(3)\n\t" \
  "v_smfmac_f32_16x16x64_bf16 v[68:71], v[60:63], a[112:119], v104\n\t" \
  "ds_read_b128 v[56:59], v101 offset:3072\n\t" \
  "v_smfmac_f32_16x16x64_bf16 v[72:75], v[60:63], a[120:127], v104\n\t" \
  "s_waitcnt lgkmcnt(3)\n\t" \
  "v_smfmac_f32_16x16x64_bf16 v[68:71], v[64:67], a[192:199], v104\n\t" \
  "ds_read_b128 v[60:63], v101 offset:6144\n\t" \
  "v_smfmac_f32_16x16x64_bf16 v[72:75], v[64:67], a[200:207], v104\n\t" \
  "s_waitcnt lgkmcnt(3)\n\t" \
  "v_smfmac_f32_16x16x64_bf16 v[68:71], v[48:51], a[208:215], v104\n\t" \
  "ds_read_b128 v[64:67], v101 offset:7168\n\t" \
  "ds_read_b128 v[16:19], v103 offset:16384\n\t" \
  "v_smfmac_f32_16x16x64_bf16 v[72:75], v[48:51], a[216:223], v104\n\t" \
  "s_waitcnt lgkmcnt(4)\n\t" \
  "v_smfmac_f32_16x16x64_bf16 v[68:71], v[52:55], v[84:91], v104\n\t" \
  "ds_read_b128 v[48:51], v101 offset:11264\n\t" \
  "ds_read_b128 v[20:23], v103 offset:17408\n\t" \
  "v_smfmac_f32_16x16x64_bf16 v[72:75], v[52:55], v[92:99], v104\n\t" \
  "s_waitcnt lgkmcnt(5)\n\t" \
  "v_smfmac_f32_16x16x64_bf16 v[68:71], v[56:59], a[64:71], v104\n\t" \
  "ds_read_b128 v[52:55], v101 offset:10240\n\t" \
  "ds_read_b128 v[24:27], v103 offset:18432\n\t" \
  "v_smfmac_f32_16x16x64_bf16 v[72:75], v[56:59], a[72:79], v104\n\t" \
  "ds_read_b128 v[28:31], v103 offset:19456\n\t" \
  "s_waitcnt lgkmcnt(7)\n\t" \
  "v_smfmac_f32_16x16x64_bf16 v[68:71], v[60:63], v[0:7], v104\n\t" \
  "v_smfmac_f32_16x16x64_bf16 v[72:75], v[60:63], v[8:15], v104\n\t" \
  "s_waitcnt lgkmcnt(6)\n\t" \
  "v_smfmac_f32_16x16x64_bf16 v[68:71], v[64:67], a[160:167], v104\n\t" \
  "ds_read_b128 v[0:3], v102 offset:0\n\t" \
  "v_smfmac_f32_16x16x64_bf16 v[72:75], v[64:67], a[168:175], v104\n\t" \
  "ds_read_b128 v[4:7], v102 offset:1024\n\t" \
  "s_waitcnt lgkmcnt(6)\n\t" \
  "v_smfmac_f32_16x16x64_bf16 v[68:71], v[48:51], v[120:127], v104\n\t" \
  "s_waitcnt lgkmcnt(4)\n\t" \
  "v_smfmac_f32_16x16x64_bf16 v[68:71], v[52:55], v[16:23], v104\n\t" \
  "ds_read_b128 v[8:11], v102 offset:2048\n\t" \
  "v_smfmac_f32_16x16x64_bf16 v[72:75], v[48:51], v[128:135], v104\n\t" \
  "ds_read_b128 v[12:15], v102 offset:3072\n\t" \
  "s_waitcnt lgkmcnt(4)\n\t" \
  "v_smfmac_f32_16x16x64_bf16 v[72:75], v[52:55], v[24:31], v104\n\t" \
  "v_add_f32 v44, v68, v70\n\t" \
  "v_smfmac_f32_16x16x64_bf16 v[76:79], v[56:59], a[80:87], v104\n\t" \
  "v_add_f32 v45, v69, v71\n\t" \
  "v_cvt_pk_bf16_f32 v44, v44, v45\n\t" \
  "v_smfmac_f32_16x16x64_bf16 v[80:83], v[56:59], a[88:95], v104\n\t" \
  "v_add_f32 v46, v72, v74\n\t" \
  "v_add_f32 v47, v73, v75\n\t" \
  "v_smfmac_f32_16x16x64_bf16 v[76:79], v[60:63], a[144:151], v104\n\t" \
  "v_cvt_pk_bf16_f32 v46, v46, v47\n\t" \
  "ds_write2st64_b32 v105, v44, v46 offset1:1\n\t" \
  "v_smfmac_f32_16x16x64_bf16 v[80:83], v[60:63], a[152:159], v104\n\t" \
  "ds_read_b128 v[56:59], v101 offset:0\n\t" \
  "v_smfmac_f32_16x16x64_bf16 v[76:79], v[64:67], a[176:183], v104\n\t" \
  "ds_read_b128 v[16:19], v102 offset:32768\n\t" \
  "v_smfmac_f32_16x16x64_bf16 v[80:83], v[64:67], a[184:191], v104\n\t" \
  "ds_read_b128 v[20:23], v102 offset:33792\n\t" \
  "v_smfmac_f32_16x16x64_bf16 v[76:79], v[48:51], v[136:143], v104\n\t" \
  "s_waitcnt lgkmcnt(3)\n\t" \
  "s_barrier\n\t" \
  "v_smfmac_f32_16x16x64_bf16 v[80:83], v[48:51], v[144:151], v104\n\t" \
  "v_smfmac_f32_16x16x64_bf16 v[76:79], v[52:55], a[240:247], v104\n\t" \
  "ds_read_b128 v[60:63], v101 offset:9216\n\t" \
  "ds_read_b128 v[24:27], v102 offset:34816\n\t" \
  "v_smfmac_f32_16x16x64_bf16 v[80:83], v[52:55], a[248:255], v104\n\t" \
  "ds_read_b128 v[64:67], v101 offset:2048\n\t" \
  "global_load_dword v119, v109, %[kc] sc1\n\t" \
  "s_waitcnt lgkmcnt(5)\n\t" \
  "v_smfmac_f32_16x16x64_bf16 v[76:79], v[56:59], v[0:7], v104\n\t" \
  "ds_read_b128 v[48:51], v101 offset:4096\n\t" \
  "ds_read_b128 v[28:31], v102 offset:35840\n\t" \
  "v_smfmac_f32_16x16x64_bf16 v[80:83], v[56:59], v[8:15], v104\n\t" \
  "s_waitcnt lgkmcnt(4)\n\t" \
  "v_smfmac_f32_16x16x64_bf16 v[76:79], v[60:63], a[224:231], v104\n\t" \
  "ds_read_b128 v[52:55], v101 offset:1024\n\t" \
  "v_smfmac_f32_16x16x64_bf16 v[80:83], v[60:63], a[232:239], v104\n\t" \
  "ds_read_b128 v[0:3], v103 offset:0\n\t" \
  "buffer_load_dwordx4 v[32:35], v100, %[ru], s47 offen offset:0 sc1\n\t" \
  "s_waitcnt lgkmcnt(4)\n\t" \
  "v_smfmac_f32_16x16x64_bf16 v[76:79], v[64:67], a[48:55], v104\n\t" \
  "ds_read_b128 v[56:59], v101 offset:5120\n\t" \
  "v_smfmac_f32_16x16x64_bf16 v[80:83], v[64:67], a[56:63], v104\n\t" \
  "ds_read_b128 v[60:63], v101 offset:8192\n\t" \
  "ds_read_b128 v[4:7], v103 offset:1024\n\t" \
  "s_waitcnt lgkmcnt(6)\n\t" \
  "v_smfmac_f32_16x16x64_bf16 v[76:79], v[48:51], v[16:23], v104\n\t" \
  "ds_read_b128 v[8:11], v103 offset:2048\n\t" \
  "buffer_load_dwordx4 v[36:39], v100, %[ru], s47 offen offset:1024 sc1\n\t" \
  "s_waitcnt lgkmcnt(6)\n\t" \
  "v_smfmac_f32_16x16x64_bf16 v[80:83], v[48:51], v[24:31], v104\n\t" \
  "ds_read_b128 v[12:15], v103 offset:3072\n\t" \
  "s_waitcnt lgkmcnt(6)\n\t" \
  "v_smfmac_f32_16x16x64_bf16 v[76:79], v[52:55], a[32:39], v104\n\t" \
  "v_smfmac_f32_16x16x64_bf16 v[80:83], v[52:55], a[40:47], v104\n\t" \
  "s_waitcnt lgkmcnt(4)\n\t" \
  "v_smfmac_f32_16x16x64_bf16 v[76:79], v[56:59], a[128:135], v104\n\t" \
  "s_waitcnt lgkmcnt(2)\n\t" \
  "v_smfmac_f32_16x16x64_bf16 v[76:79], v[60:63], v[0:7], v104\n\t" \
  "v_smfmac_f32_16x16x64_bf16 v[80:83], v[56:59], a[136:143], v104\n\t" \
  "s_waitcnt lgkmcnt(0)\n\t" \
  "v_smfmac_f32_16x16x64_bf16 v[80:83], v[60:63], v[8:15], v104\n\t" \
  "v_add_f32 v20, v76, v78\n\t" \
  "v_add_f32 v21, v77, v79\n\t" \
  "v_cvt_pk_bf16_f32 v20, v20, v21\n\t" \
  "ds_write_b32 v106, v20\n\t" \
  "s_nop 3\n\t" \
  "v_add_f32 v23, v80, v82\n\t" \
  "v_add_f32 v21, v81, v83\n\t" \
  "v_cvt_pk_bf16_f32 v21, v23, v21\n\t" \
  "ds_write_b32 v106, v21 offset:256\n\t" \
  "s_waitcnt lgkmcnt(0)\n\t" \
  "s_barrier\n\t" \
  "s_waitcnt vmcnt(0)\n\t" \
  "s_barrier\n\t" \
  "ds_write_b128 v101, v[32:35] offset:26624\n\t" \
  "ds_write_b128 v101, v[36:39] offset:27648\n\t" \
  "s_add_i32 s77, s44, 1\n\t" \
  "s_min_u32 s77, s77, 63\n\t" \
  "v_cmp_gt_i32 vcc, s77, v119\n\t" \
  "s_cmp_lg_u64 vcc, 0\n\t" \
  "s_cselect_b32 s42, 32, 0\n\t" \
  "s_add_i32 s41, s56, 1950\n\t" \
  "s_sub_i32 s76, s92, s51\n\t" \
  "s_sub_i32 s77, s93, s53\n\t" \
  "s_cmp_eq_u32 s76, 0\n\t" \
  "s_cbranch_scc1 21f\n\t" \
  "v_cvt_f32_u32 v117, s76\n\t" \
  "v_cvt_f32_u32 v116, s77\n\t" \
  "v_rcp_f32 v117, v117\n\t" \
  "s_mov_b32 s51, s92\n\t" \
  "v_mul_f32 v117, v117, v116\n\t" \
  "v_max_f32 v117, 0x3f028f5c, v117\n\t" \
  "v_min_f32 v117, 0x3ffeb852, v117\n\t" \
  "v_rcp_f32 v116, v117\n\t" \
  "v_mul_f32 v117, 0x4f000000, v117\n\t" \
  "v_mul_f32 v116, 0x47800000, v116\n\t" \
  "v_cvt_u32_f32 v117, v117\n\t" \
  "v_cvt_u32_f32 v116, v116\n\t" \
  "s_mov_b32 s53, s93\n\t" \
  "v_readfirstlane_b32 s46, v117\n\t" \
  "v_readfirstlane_b32 s52, v116\n\t" \
  "21:\n\t" \
  "v_mov_b32 v0, -1\n\t" \
  "v_mov_b32 v1, -1\n\t" \
  "v_mov_b32 v2, -1\n\t" \
  "v_mov_b32 v3, -1\n\t" \
  "s_and_b32 s76, s49, 3\n\t" \
  "s_mul_i32 s76, s76, 0xc000\n\t" \
  "s_add_i32 s76, s76, 0x40000\n\t" \
  "buffer_store_dwordx4 v[0:3], v110, %[rh], s76 offen sc1\n\t" \
  "s_add_i32 s76, s44, 1\n\t" \
  "s_and_b32 s76, s76, 3\n\t" \
  "s_lshl_b32 s76, s76, 18\n\t" \
  "s_add_i32 s77, s76, 0x10000\n\t" \
  "buffer_store_dwordx4 v[0:3], v107, %[rh], s76 offen sc1\n\t" \
  "buffer_store_dwordx4 v[0:3], v107, %[rh], s77 offen sc1\n\t" \
  "buffer_store_dwordx4 v[0:3], v111, %[rh], s76 offen sc1\n\t" \
  "s_add_i32 s44, s44, 1\n\t" \
  "s_and_b32 s58, s58, 4\n\t" \
  "s_and_b32 s76, s44, 64\n\t" \
  "s_or_b32 s42, s42, s76\n\t" \
  "s_cbranch_scc0 20b\n\t" \
  "s_branch 99f\n\t" \
  "14:\n\t" \
  "s_sleep 3\n\t" \
  "s_add_u32 s59, s59, 0x10000\n\t" \
  "s_cmp_lt_u32 s59, 0x100000\n\t" \
  "s_cbranch_scc1 13b\n\t" \
  "s_mov_b64 exec, -1\n\t" \
  "s_mov_b32 s42, 16\n\t" \
  "s_branch 99f\n\t" \
  "8:\n\t" \
  "s_bitcmp1_b32 s58, 1\n\t" \
  "s_cbranch_scc1 4b\n\t" \
  "s_bitcmp1_b32 s58, 2\n\t" \
  "s_cbranch_scc1 5b\n\t" \
  "v_max3_u32 v116, v32, v33, v34\n\t" \
  "v_max3_u32 v117, v35, v36, v37\n\t" \
  "v_max3_u32 v116, v116, v38, v39\n\t" \
  "v_max_u32 v116, v116, v117\n\t" \
  "v_cmp_eq_u32 vcc, -1, v116\n\t" \
  "s_cbranch_vccz 5b\n\t" \
  "s_branch 0f\n\t" \
  "9:\n\t" \
  "s_bitcmp1_b32 s58, 2\n\t" \
  "s_cbranch_scc1 6b\n\t" \
  "v_max3_u32 v116, v40, v41, v42\n\t" \
  "v_max3_u32 v117, v43, v44, v45\n\t" \
  "v_max3_u32 v116, v116, v46, v47\n\t" \
  "v_max_u32 v116, v116, v117\n\t" \
  "v_cmp_eq_u32 vcc, -1, v116\n\t" \
  "s_cbranch_vccz 6b\n\t" \
  "s_or_b32 s58, s58, 2\n\t" \
  "0:\n\t" \
  "s_or_b32 s58, s58, 1\n\t" \
  "11:\n\t" \
  "buffer_load_dwordx4 v[32:35], v100, %[rh], s40 offen sc1\n\t" \
  "buffer_load_dwordx4 v[36:39], v100, %[rh], s76 offen sc1\n\t" \
  "buffer_load_dwordx4 v[40:43], v100, %[rh], s77 offen sc1\n\t" \
  "buffer_load_dwordx4 v[44:47], v100, %[rh], s78 offen sc1\n\t" \
  "s_waitcnt lgkmcnt(0)\n\t" \
  "s_branch 10b\n\t" \
  "17:\n\t" \
  "s_add_u32 s59, s59, 1\n\t" \
  "s_bitcmp1_b32 s58, 2\n\t" \
  "s_cbranch_scc1 3b\n\t" \
  "s_waitcnt vmcnt(0)\n\t" \
  "v_max_u32 v116, v32, v33\n\t" \
  "v_max3_u32 v116, v116, v34, v35\n\t" \
  "v_max3_u32 v116, v116, v36, v37\n\t" \
  "v_max3_u32 v116, v116, v38, v39\n\t" \
  "v_max3_u32 v116, v116, v40, v41\n\t" \
  "v_max3_u32 v116, v116, v42, v43\n\t" \
  "v_max3_u32 v116, v116, v44, v45\n\t" \
  "v_max3_u32 v116, v116, v46, v47\n\t" \
  "v_cmp_eq_u32 vcc, -1, v116\n\t" \
  "s_cbranch_vccz 3b\n\t" \
  "s_bitcmp1_b32 s59, 6\n\t" \
  "s_cbranch_scc1 12f\n\t" \
  "buffer_load_dwordx4 v[32:35], v100, %[rh], s40 offen sc1\n\t" \
  "buffer_load_dwordx4 v[36:39], v100, %[rh], s76 offen sc1\n\t" \
  "buffer_load_dwordx4 v[40:43], v100, %[rh], s77 offen sc1\n\t" \
  "buffer_load_dwordx4 v[44:47], v100, %[rh], s78 offen sc1\n\t" \
  "s_branch 17b\n\t" \
  "12:\n\t" \
  "s_mov_b32 s42, s58\n\t" \
  "s_branch 99f\n\t" \
  "99:\n\t" \
  "s_waitcnt vmcnt(0) lgkmcnt(0)\n\t" \
  ""
#define VO_BODY \
  "s_cmp_eq_u32 %[wv], 1\n\t" \
  "s_cbranch_scc1 91f\n\t" \
  "s_cmp_eq_u32 %[wv], 2\n\t" \
  "s_cbranch_scc1 92f\n\t" \
  "s_cmp_eq_u32 %[wv], 3\n\t" \
  "s_cbranch_scc1 93f\n\t" \
  "90:\n\t" \
  "s_mov_b32 s58, s42\n\t" \
  "20:\n\t" \
  "s_and_b32 s76, s44, 7\n\t" \
  "s_lshl_b32 s76, s76, 15\n\t" \
  "s_add_i32 s40, s54, s76\n\t" \
  "s_and_b32 s76, s44, 3\n\t" \
  "s_lshl_b32 s45, s76, 14\n\t" \
  "s_lshl_b32 s47, s76, 20\n\t" \
  "s_add_i32 s47, s47, s55\n\t" \
  "s_lshr_b32 s76, s47, 21\n\t" \
  "s_bitcmp1_b64 %[sel], s76\n\t" \
  "s_cselect_b32 s76, 0x2000, 0\n\t" \
  "s_add_i32 s47, s47, s76\n\t" \
  "s_add_i32 s49, s44, -1\n\t" \
  "s_and_b32 s48, s49, 2\n\t" \
  "s_lshl_b32 s48, s48, 13\n\t" \
  "s_and_b32 s76, s49, 1\n\t" \
  "s_lshl_b32 s76, s76, 12\n\t" \
  "s_add_i32 s48, s48, s76\n\t" \
  "s_lshl_b32 s50, s53, 8\n\t" \
  "s_mov_b32 s59, 0\n\t" \
  "s_mov_b32 s92, s51\n\t" \
  "s_mov_b32 s93, s53\n\t" \
  "s_xor_b32 s76, s40, 0x400\n\t" \
  "s_xor_b32 s77, s40, 0x800\n\t" \
  "s_xor_b32 s78, s40, 0xc00\n\t" \
  "s_cmp_lg_u32 s58, 0\n\t" \
  "s_cbranch_scc1 11f\n\t" \
  "10:\n\t" \
  "ds_read_b128 v[48:51], v102 offset:0\n\t" \
  "ds_read_b128 v[52:55], v102 offset:1024\n\t" \
  "ds_read_b128 v[56:59], v102 offset:2048\n\t" \
  "ds_read_b128 v[60:63], v102 offset:3072\n\t" \
  "ds_read_b128 v[0:3], v102 offset:16384\n\t" \
  "ds_read_b128 v[4:7], v102 offset:17408\n\t" \
  "ds_read_b128 v[8:11], v102 offset:18432\n\t" \
  "ds_read_b128 v[12:15], v102 offset:19456\n\t" \
  "ds_read_b128 v[16:19], v102 offset:32768\n\t" \
  "ds_read_b128 v[20:23], v102 offset:33792\n\t" \
  "ds_read_b128 v[24:27], v102 offset:34816\n\t" \
  "ds_read_b128 v[28:31], v102 offset:35840\n\t" \
  "s_waitcnt lgkmcnt(4)\n\t" \
  "ds_read_b128 v[80:83], v102 offset:49152\n\t" \
  "ds_read_b128 v[84:87], v102 offset:50176\n\t" \
  "ds_read_b128 v[88:91], v102 offset:51200\n\t" \
  "ds_read_b128 v[92:95], v102 offset:52224\n\t" \
  "s_bitcmp1_b32 s58, 1\n\t" \
  "s_cselect_b64 s[80:81], -1, 0\n\t" \
  "s_cmp_lg_u32 s58, 0\n\t" \
  "s_cbranch_scc1 17f\n\t" \
  "s_mov_b32 exec_hi, 0\n\t" \
  "16:\n\t" \
  "s_memtime vcc\n\t" \
  "s_waitcnt lgkmcnt(0)\n\t" \
  "s_sub_i32 s79, s41, vcc_lo\n\t" \
  "s_cmp_gt_i32 s79, 64\n\t" \
  "s_cbranch_scc1 16b\n\t" \
  "13:\n\t" \
  "buffer_load_dword v116, v112, %[rh], s48 offen sc1\n\t" \
  "s_waitcnt vmcnt(0)\n\t" \
  "15:\n\t" \
  "v_mov_b32 v117, 20000\n\t" \
  "v_cmp_ne_u32_sdwa vcc, v116, s49 src0_sel:BYTE_0 src1_sel:DWORD\n\t" \
  "v_subrev_u32 v116, s50, v116\n\t" \
  "v_ashrrev_i32 v116, 8, v116\n\t" \
  "s_nop 1\n\t" \
  "v_max_i32_dpp v116, v116, v116 quad_perm:[1,0,3,2] row_mask:0xf bank_mask:0xf\n\t" \
  "s_cbranch_vccnz 14f\n\t" \
  "s_nop 0\n\t" \
  "v_max_i32_dpp v116, v116, v116 quad_perm:[2,3,0,1] row_mask:0xf bank_mask:0xf\n\t" \
  "s_nop 1\n\t" \
  "v_max_i32_dpp v116, v116, v116 row_half_mirror row_mask:0xf bank_mask:0xf\n\t" \
  "s_nop 1\n\t" \
  "v_max_i32_dpp v116, v116, v116 row_mirror row_mask:0xf bank_mask:0xf\n\t" \
  "v_med3_i32 v116, v116, 0, v117\n\t" \
  "v_mul_u32_u24 v116, s52, v116\n\t" \
  "v_lshrrev_b32 v116, 16, v116\n\t" \
  "s_mov_b64 exec, -1\n\t" \
  "v_readfirstlane_b32 s84, v116\n\t" \
  "v_readlane_b32 s85, v116, 16\n\t" \
  "s_max_u32 s84, s84, s85\n\t" \
  "s_add_i32 s84, s84, s51\n\t" \
  "1:\n\t" \
  "s_memtime vcc\n\t" \
  "s_waitcnt lgkmcnt(0)\n\t" \
  "s_sub_i32 s79, s84, vcc_lo\n\t" \
  "s_cmp_gt_i32 s79, 128\n\t" \
  "s_cbranch_scc1 1b\n\t" \
  ".p2align 4\n\t" \
  "2:\n\t" \
  "s_sub_i32 s79, s79, 33\n\t" \
  "s_cmp_gt_i32 s79, 56\n\t" \
  "s_cbranch_scc1 2b\n\t" \
  "buffer_load_dwordx4 v[32:35], v100, %[rh], s40 offen sc1\n\t" \
  "s_nop 12\n\t" \
  "buffer_load_dwordx4 v[36:39], v100, %[rh], s76 offen sc1\n\t" \
  "s_nop 12\n\t" \
  "buffer_load_dwordx4 v[40:43], v100, %[rh], s77 offen sc1\n\t" \
  "s_nop 12\n\t" \
  "buffer_load_dwordx4 v[44:47], v100, %[rh], s78 offen sc1\n\t" \
  "s_memtime vcc\n\t" \
  "s_memrealtime s[82:83]\n\t" \
  "s_nop 4\n\t" \
  "s_memrealtime s[84:85]\n\t" \
  "s_nop 4\n\t" \
  "s_memrealtime s[56:57]\n\t" \
  "s_nop 4\n\t" \
  "s_memrealtime s[86:87]\n\t" \
  "s_nop 4\n\t" \
  "s_memrealtime s[88:89]\n\t" \
  "s_nop 4\n\t" \
  "s_memrealtime s[90:91]\n\t" \
  "s_mov_b32 s92, 0\n\t" \
  "s_waitcnt lgkmcnt(0)\n\t" \
  "s_cmp_eq_u32 s82, s84\n\t" \
  "s_addc_u32 s92, s92, 0\n\t" \
  "s_cmp_eq_u32 s82, s56\n\t" \
  "s_addc_u32 s92, s92, 0\n\t" \
  "s_cmp_eq_u32 s82, s86\n\t" \
  "s_addc_u32 s92, s92, 0\n\t" \
  "s_cmp_eq_u32 s82, s88\n\t" \
  "s_addc_u32 s92, s92, 0\n\t" \
  "s_cmp_eq_u32 s82, s90\n\t" \
  "s_addc_u32 s92, s92, 0\n\t" \
  "s_mul_i32 s92, s92, 24\n\t" \
  "s_add_i32 s92, s92, vcc_lo\n\t" \
  "s_lshl4_add_u32 s93, s82, 64\n\t" \
  "3:\n\t" \
  "v_mov_b64 v[72:73], 0\n\t" \
  "v_mov_b64 v[74:75], 0\n\t" \
  "v_mov_b64 v[76:77], 0\n\t" \
  "v_mov_b64 v[78:79], 0\n\t" \
  "s_waitcnt lgkmcnt(0)\n\t" \
  "s_waitcnt vmcnt(3)\n\t" \
  "v_smfmac_f32_16x16x64_bf16 v[72:75], v[32:35], v[48:55], v104\n\t" \
  "ds_write_b128 v101, v[32:35] offset:0\n\t" \
  "v_smfmac_f32_16x16x64_bf16 v[76:79], v[32:35], v[56:63], v104\n\t" \
  "s_waitcnt vmcnt(2)\n\t" \
  "ds_write_b128 v101, v[36:39] offset:1024\n\t" \
  "v_smfmac_f32_16x16x64_bf16 v[72:75], v[36:39], a[0:7], v104\n\t" \
  "v_smfmac_f32_16x16x64_bf16 v[76:79], v[36:39], a[8:15], v104\n\t" \
  "s_nop 3\n\t" \
  "v_cmp_u_f32 vcc, v72, v73\n\t" \
  "v_cmp_u_f32 s[82:83], v74, v75\n\t" \
  "s_waitcnt vmcnt(1)\n\t" \
  "v_smfmac_f32_16x16x64_bf16 v[72:75], v[40:43], a[16:23], v104\n\t" \
  "s_or_b64 vcc, vcc, s[82:83]\n\t" \
  "s_or_b64 vcc, vcc, s[80:81]\n\t" \
  "v_smfmac_f32_16x16x64_bf16 v[76:79], v[40:43], a[24:31], v104\n\t" \
  "s_waitcnt lgkmcnt(0)\n\t" \
  "s_cbranch_scc1 8f\n\t" \
  "5:\n\t" \
  "s_barrier\n\t" \
  "4:\n\t" \
  "s_memtime s[56:57]\n\t" \
  "ds_read_b128 v[48:51], v101 offset:4096\n\t" \
  "ds_read_b128 v[52:55], v101 offset:5120\n\t" \
  "ds_read_b128 v[56:59], v101 offset:9216\n\t" \
  "s_waitcnt vmcnt(0)\n\t" \
  "v_smfmac_f32_16x16x64_bf16 v[72:75], v[44:47], a[32:39], v104\n\t" \
  "ds_read_b128 v[60:63], v101 offset:8192\n\t" \
  "ds_read_b128 v[64:67], v101 offset:13312\n\t" \
  "v_smfmac_f32_16x16x64_bf16 v[76:79], v[44:47], a[40:47], v104\n\t" \
  "ds_read_b128 v[68:71], v101 offset:12288\n\t" \
  "ds_write_b128 v101, v[40:43] offset:2048\n\t" \
  "s_nop 1\n\t" \
  "v_cmp_u_f32 vcc, v72, v73\n\t" \
  "v_cmp_u_f32 s[82:83], v74, v75\n\t" \
  "s_waitcnt lgkmcnt(6)\n\t" \
  "v_smfmac_f32_16x16x64_bf16 v[72:75], v[48:51], v[0:7], v104\n\t" \
  "s_or_b64 vcc, vcc, s[82:83]\n\t" \
  "v_smfmac_f32_16x16x64_bf16 v[76:79], v[48:51], v[8:15], v104\n\t" \
  "s_cbranch_scc1 9f\n\t" \
  "6:\n\t" \
  "s_waitcnt lgkmcnt(5)\n\t" \
  "v_smfmac_f32_16x16x64_bf16 v[72:75], v[52:55], a[48:55], v104\n\t" \
  "ds_write_b128 v101, v[44:47] offset:3072\n\t" \
  "v_smfmac_f32_16x16x64_bf16 v[76:79], v[52:55], a[56:63], v104\n\t" \
  "v_mov_b64 v[40:41], 0\n\t" \
  "s_waitcnt lgkmcnt(5)\n\t" \
  "v_smfmac_f32_16x16x64_bf16 v[72:75], v[56:59], a[96:103], v104\n\t" \
  "v_mov_b64 v[42:43], 0\n\t" \
  "v_smfmac_f32_16x16x64_bf16 v[76:79], v[56:59], a[104:111], v104\n\t" \
  "s_waitcnt lgkmcnt(4)\n\t" \
  "v_smfmac_f32_16x16x64_bf16 v[72:75], v[60:63], v[16:23], v104\n\t" \
  "s_waitcnt lgkmcnt(0)\n\t" \
  "s_barrier\n\t" \
  "v_smfmac_f32_16x16x64_bf16 v[76:79], v[60:63], v[24:31], v104\n\t" \
  "ds_read_b128 v[48:51], v101 offset:6144\n\t" \
  "ds_read_b128 v[52:55], v101 offset:10240\n\t" \
  "v_smfmac_f32_16x16x64_bf16 v[72:75], v[64:67], a[144:151], v104\n\t" \
  "ds_read_b128 v[56:59], v101 offset:14336\n\t" \
  "ds_read_b128 v[60:63], v101 offset:7168\n\t" \
  "v_smfmac_f32_16x16x64_bf16 v[76:79], v[64:67], a[152:159], v104\n\t" \
  "ds_read_b128 v[32:35], v101 offset:11264\n\t" \
  "ds_read_b128 v[36:39], v101 offset:15360\n\t" \
  "v_smfmac_f32_16x16x64_bf16 v[72:75], v[68:71], v[80:87], v104\n\t" \
  "ds_read_b128 v[0:3], v103 offset:0\n\t" \
  "v_mov_b64 v[44:45], 0\n\t" \
  "v_smfmac_f32_16x16x64_bf16 v[76:79], v[68:71], v[88:95], v104\n\t" \
  "v_mov_b64 v[46:47], 0\n\t" \
  "s_waitcnt lgkmcnt(5)\n\t" \
  "v_smfmac_f32_16x16x64_bf16 v[72:75], v[48:51], a[64:71], v104\n\t" \
  "ds_read_b128 v[4:7], v103 offset:1024\n\t" \
  "v_mov_b64 v[80:81], 0\n\t" \
  "v_smfmac_f32_16x16x64_bf16 v[72:75], v[52:55], a[112:119], v104\n\t" \
  "v_mov_b64 v[82:83], 0\n\t" \
  "s_waitcnt lgkmcnt(2)\n\t" \
  "v_smfmac_f32_16x16x64_bf16 v[72:75], v[56:59], a[160:167], v104\n\t" \
  "ds_read_b128 v[8:11], v103 offset:2048\n\t" \
  "v_mov_b64 v[84:85], 0\n\t" \
  "v_smfmac_f32_16x16x64_bf16 v[72:75], v[60:63], a[80:87], v104\n\t" \
  "ds_read_b128 v[12:15], v103 offset:3072\n\t" \
  "v_mov_b64 v[86:87], 0\n\t" \
  "v_smfmac_f32_16x16x64_bf16 v[72:75], v[32:35], a[128:135], v104\n\t" \
  "ds_read_b128 v[16:19], v103 offset:16384\n\t" \
  "v_mov_b64 v[88:89], 0\n\t" \
  "v_smfmac_f32_16x16x64_bf16 v[72:75], v[36:39], a[176:183], v104\n\t" \
  "ds_read_b128 v[20:23], v103 offset:17408\n\t" \
  "v_mov_b64 v[90:91], 0\n\t" \
  "v_smfmac_f32_16x16x64_bf16 v[76:79], v[48:51], a[72:79], v104\n\t" \
  "ds_read_b128 v[24:27], v103 offset:18432\n\t" \
  "v_mov_b64 v[92:93], 0\n\t" \
  "v_smfmac_f32_16x16x64_bf16 v[76:79], v[52:55], a[120:127], v104\n\t" \
  "v_add_f32 v96, v72, v74\n\t" \
  "v_add_f32 v97, v73, v75\n\t" \
  "v_cvt_pk_bf16_f32 v96, v96, v97\n\t" \
  "ds_write_b32 v105, v96\n\t" \
  "v_smfmac_f32_16x16x64_bf16 v[76:79], v[56:59], a[168:175], v104\n\t" \
  "ds_read_b128 v[28:31], v103 offset:19456\n\t" \
  "v_mov_b64 v[94:95], 0\n\t" \
  "v_smfmac_f32_16x16x64_bf16 v[76:79], v[60:63], a[88:95], v104\n\t" \
  "v_smfmac_f32_16x16x64_bf16 v[76:79], v[32:35], a[136:143], v104\n\t" \
  "v_smfmac_f32_16x16x64_bf16 v[76:79], v[36:39], a[184:191], v104\n\t" \
  "s_waitcnt lgkmcnt(1)\n\t" \
  "s_barrier\n\t" \
  "ds_read_b64_tr_b16 v[96:97], v107 offset:0\n\t" \
  "ds_read_b64_tr_b16 v[98:99], v107 offset:64\n\t" \
  "ds_read_b64_tr_b16 v[56:57], v107 offset:1024\n\t" \
  "ds_read_b64_tr_b16 v[58:59], v107 offset:1088\n\t" \
  "s_nop 1\n\t" \
  "v_add_f32 v116, v76, v78\n\t" \
  "v_add_f32 v117, v77, v79\n\t" \
  "v_cvt_pk_bf16_f32 v116, v116, v117\n\t" \
  "ds_write_b32 v105, v116 offset:256\n\t" \
  "s_waitcnt lgkmcnt(3)\n\t" \
  "v_smfmac_f32_16x16x64_bf16 v[40:43], v[96:99], a[192:199], v106\n\t" \
  "v_smfmac_f32_16x16x64_bf16 v[44:47], v[96:99], a[224:231], v106\n\t" \
  "s_waitcnt lgkmcnt(0)\n\t" \
  "s_barrier\n\t" \
  "ds_read_b64_tr_b16 v[52:53], v107 offset:256\n\t" \
  "ds_read_b64_tr_b16 v[54:55], v107 offset:320\n\t" \
  "v_smfmac_f32_16x16x64_bf16 v[40:43], v[56:59], a[208:215], v106\n\t" \
  "ds_read_b64_tr_b16 v[60:61], v107 offset:1280\n\t" \
  "ds_read_b64_tr_b16 v[62:63], v107 offset:1344\n\t" \
  "v_smfmac_f32_16x16x64_bf16 v[44:47], v[56:59], v[0:7], v106\n\t" \
  "v_smfmac_f32_16x16x64_bf16 v[80:83], v[96:99], a[240:247], v106\n\t" \
  "v_mov_b64 v[32:33], 0\n\t" \
  "v_smfmac_f32_16x16x64_bf16 v[84:87], v[96:99], v[136:143], v106\n\t" \
  "v_mov_b64 v[34:35], 0\n\t" \
  "v_smfmac_f32_16x16x64_bf16 v[80:83], v[56:59], v[120:127], v106\n\t" \
  "v_mov_b64 v[36:37], 0\n\t" \
  "v_smfmac_f32_16x16x64_bf16 v[84:87], v[56:59], v[16:23], v106\n\t" \
  "s_waitcnt lgkmcnt(2)\n\t" \
  "v_smfmac_f32_16x16x64_bf16 v[40:43], v[52:55], a[200:207], v106\n\t" \
  "v_smfmac_f32_16x16x64_bf16 v[44:47], v[52:55], a[232:239], v106\n\t" \
  "s_xor_b32 s76, s47, 0x40000\n\t" \
  "s_waitcnt lgkmcnt(0)\n\t" \
  "v_smfmac_f32_16x16x64_bf16 v[40:43], v[60:63], a[216:223], v106\n\t" \
  "v_mov_b64 v[38:39], 0\n\t" \
  "s_xor_b32 s77, s47, 0x80000\n\t" \
  "v_smfmac_f32_16x16x64_bf16 v[44:47], v[60:63], v[8:15], v106\n\t" \
  "s_xor_b32 s78, s47, 0xc0000\n\t" \
  "v_smfmac_f32_16x16x64_bf16 v[80:83], v[52:55], a[248:255], v106\n\t" \
  "ds_read_b128 v[0:3], v103 offset:32768\n\t" \
  "v_cvt_pk_bf16_f32 v64, v40, v41\n\t" \
  "v_smfmac_f32_16x16x64_bf16 v[84:87], v[52:55], v[144:151], v106\n\t" \
  "ds_read_b128 v[4:7], v103 offset:33792\n\t" \
  "v_cvt_pk_bf16_f32 v65, v42, v43\n\t" \
  "v_smfmac_f32_16x16x64_bf16 v[80:83], v[60:63], v[128:135], v106\n\t" \
  "ds_read_b128 v[8:11], v103 offset:34816\n\t" \
  "v_cvt_pk_bf16_f32 v66, v44, v45\n\t" \
  "v_smfmac_f32_16x16x64_bf16 v[84:87], v[60:63], v[24:31], v106\n\t" \
  "ds_read_b128 v[12:15], v103 offset:35840\n\t" \
  "v_cvt_pk_bf16_f32 v67, v46, v47\n\t" \
  "v_smfmac_f32_16x16x64_bf16 v[88:91], v[96:99], v[152:159], v106\n\t" \
  "v_permlane16_swap_b32 v64, v66\n\t" \
  "v_cvt_pk_bf16_f32 v68, v80, v81\n\t" \
  "v_smfmac_f32_16x16x64_bf16 v[92:95], v[96:99], v[184:191], v106\n\t" \
  "v_permlane16_swap_b32 v65, v67\n\t" \
  "buffer_store_dwordx4 v[64:67], v108, %[ru], s47 offen sc1\n\t" \
  "v_cvt_pk_bf16_f32 v69, v82, v83\n\t" \
  "v_smfmac_f32_16x16x64_bf16 v[88:91], v[52:55], v[160:167], v106\n\t" \
  "v_cvt_pk_bf16_f32 v70, v84, v85\n\t" \
  "v_smfmac_f32_16x16x64_bf16 v[92:95], v[52:55], v[192:199], v106\n\t" \
  "v_cvt_pk_bf16_f32 v71, v86, v87\n\t" \
  "v_smfmac_f32_16x16x64_bf16 v[88:91], v[56:59], v[168:175], v106\n\t" \
  "ds_read_b128 v[16:19], v103 offset:49152\n\t" \
  "v_permlane16_swap_b32 v68, v70\n\t" \
  "s_waitcnt lgkmcnt(3)\n\t" \
  "v_smfmac_f32_16x16x64_bf16 v[92:95], v[56:59], v[0:7], v106\n\t" \
  "ds_read_b128 v[20:23], v103 offset:50176\n\t" \
  "v_permlane16_swap_b32 v69, v71\n\t" \
  "buffer_store_dwordx4 v[68:71], v108, %[ru], s76 offen sc1\n\t" \
  "v_smfmac_f32_16x16x64_bf16 v[88:91], v[60:63], v[176:183], v106\n\t" \
  "ds_read_b128 v[24:27], v103 offset:51200\n\t" \
  "s_waitcnt lgkmcnt(3)\n\t" \
  "v_smfmac_f32_16x16x64_bf16 v[92:95], v[60:63], v[8:15], v106\n\t" \
  "ds_read_b128 v[28:31], v103 offset:52224\n\t" \
  "v_smfmac_f32_16x16x64_bf16 v[32:35], v[96:99], v[200:207], v106\n\t" \
  "v_cvt_pk_bf16_f32 v72, v88, v89\n\t" \
  "v_smfmac_f32_16x16x64_bf16 v[36:39], v[96:99], v[232:239], v106\n\t" \
  "v_cvt_pk_bf16_f32 v73, v90, v91\n\t" \
  "v_smfmac_f32_16x16x64_bf16 v[32:35], v[52:55], v[208:215], v106\n\t" \
  "v_cvt_pk_bf16_f32 v74, v92, v93\n\t" \
  "v_smfmac_f32_16x16x64_bf16 v[36:39], v[52:55], v[240:247], v106\n\t" \
  "v_cvt_pk_bf16_f32 v75, v94, v95\n\t" \
  "v_smfmac_f32_16x16x64_bf16 v[32:35], v[56:59], v[216:223], v106\n\t" \
  "v_permlane16_swap_b32 v72, v74\n\t" \
  "v_smfmac_f32_16x16x64_bf16 v[32:35], v[60:63], v[224:231], v106\n\t" \
  "v_permlane16_swap_b32 v73, v75\n\t" \
  "buffer_store_dwordx4 v[72:75], v108, %[ru], s77 offen sc1\n\t" \
  "s_waitcnt lgkmcnt(2)\n\t" \
  "v_smfmac_f32_16x16x64_bf16 v[36:39], v[56:59], v[16:23], v106\n\t" \
  "s_waitcnt lgkmcnt(0)\n\t" \
  "v_smfmac_f32_16x16x64_bf16 v[36:39], v[60:63], v[24:31], v106\n\t" \
  "v_cvt_pk_bf16_f32 v76, v32, v33\n\t" \
  "v_cvt_pk_bf16_f32 v77, v34, v35\n\t" \
  "s_nop 5\n\t" \
  "v_cvt_pk_bf16_f32 v78, v36, v37\n\t" \
  "v_cvt_pk_bf16_f32 v79, v38, v39\n\t" \
  "s_nop 0\n\t" \
  "v_permlane16_swap_b32 v76, v78\n\t" \
  "v_permlane16_swap_b32 v77, v79\n\t" \
  "buffer_store_dwordx4 v[76:79], v108, %[ru], s78 offen sc1\n\t" \
  "s_memtime s[76:77]\n\t" \
  "s_mov_b32 s42, 0\n\t" \
  "s_waitcnt lgkmcnt(0)\n\t" \
  "s_mov_b32 s57, s76\n\t" \
  "s_add_i32 s41, s56, 1950\n\t" \
  "s_sub_i32 s76, s92, s51\n\t" \
  "s_sub_i32 s77, s93, s53\n\t" \
  "s_cmp_eq_u32 s76, 0\n\t" \
  "s_cbranch_scc1 21f\n\t" \
  "v_cvt_f32_u32 v117, s76\n\t" \
  "v_cvt_f32_u32 v116, s77\n\t" \
  "v_rcp_f32 v117, v117\n\t" \
  "s_mov_b32 s51, s92\n\t" \
  "v_mul_f32 v117, v117, v116\n\t" \
  "v_max_f32 v117, 0x3f028f5c, v117\n\t" \
  "v_min_f32 v117, 0x3ffeb852, v117\n\t" \
  "v_rcp_f32 v116, v117\n\t" \
  "v_mul_f32 v117, 0x4f000000, v117\n\t" \
  "v_mul_f32 v116, 0x47800000, v116\n\t" \
  "v_cvt_u32_f32 v117, v117\n\t" \
  "v_cvt_u32_f32 v116, v116\n\t" \
  "s_mov_b32 s53, s93\n\t" \
  "v_readfirstlane_b32 s46, v117\n\t" \
  "v_readfirstlane_b32 s52, v116\n\t" \
  "21:\n\t" \
  "s_add_i32 s44, s44, 1\n\t" \
  "s_and_b32 s58, s58, 4\n\t" \
  "s_and_b32 s76, s44, 64\n\t" \
  "s_or_b32 s42, s42, s76\n\t" \
  "s_cbranch_scc0 20b\n\t" \
  "s_branch 99f\n\t" \
  "14:\n\t" \
  "s_sleep 3\n\t" \
  "s_add_u32 s59, s59, 0x10000\n\t" \
  "s_cmp_lt_u32 s59, 0x100000\n\t" \
  "s_cbranch_scc1 13b\n\t" \
  "s_mov_b64 exec, -1\n\t" \
  "s_mov_b32 s42, 16\n\t" \
  "s_branch 99f\n\t" \
  "8:\n\t" \
  "s_bitcmp1_b32 s58, 1\n\t" \
  "s_cbranch_scc1 4b\n\t" \
  "s_bitcmp1_b32 s58, 2\n\t" \
  "s_cbranch_scc1 5b\n\t" \
  "v_max3_u32 v116, v32, v33, v34\n\t" \
  "v_max3_u32 v117, v35, v36, v37\n\t" \
  "v_max3_u32 v116, v116, v38, v39\n\t" \
  "v_max_u32 v116, v116, v117\n\t" \
  "v_cmp_eq_u32 vcc, -1, v116\n\t" \
  "s_cbranch_vccz 5b\n\t" \
  "s_branch 0f\n\t" \
  "9:\n\t" \
  "s_bitcmp1_b32 s58, 2\n\t" \
  "s_cbranch_scc1 6b\n\t" \
  "v_max3_u32 v116, v40, v41, v42\n\t" \
  "v_max3_u32 v117, v43, v44, v45\n\t" \
  "v_max3_u32 v116, v116, v46, v47\n\t" \
  "v_max_u32 v116, v116, v117\n\t" \
  "v_cmp_eq_u32 vcc, -1, v116\n\t" \
  "s_cbranch_vccz 6b\n\t" \
  "s_or_b32 s58, s58, 2\n\t" \
  "0:\n\t" \
  "s_or_b32 s58, s58, 1\n\t" \
  "11:\n\t" \
  "buffer_load_dwordx4 v[32:35], v100, %[rh], s40 offen sc1\n\t" \
  "buffer_load_dwordx4 v[36:39], v100, %[rh], s76 offen sc1\n\t" \
  "buffer_load_dwordx4 v[40:43], v100, %[rh], s77 offen sc1\n\t" \
  "buffer_load_dwordx4 v[44:47], v100, %[rh], s78 offen sc1\n\t" \
  "s_waitcnt lgkmcnt(0)\n\t" \
  "s_branch 10b\n\t" \
  "17:\n\t" \
  "s_add_u32 s59, s59, 1\n\t" \
  "s_bitcmp1_b32 s58, 2\n\t" \
  "s_cbranch_scc1 3b\n\t" \
  "s_waitcnt vmcnt(0)\n\t" \
  "v_max_u32 v116, v32, v33\n\t" \
  "v_max3_u32 v116, v116, v34, v35\n\t" \
  "v_max3_u32 v116, v116, v36, v37\n\t" \
  "v_max3_u32 v116, v116, v38, v39\n\t" \
  "v_max3_u32 v116, v116, v40, v41\n\t" \
  "v_max3_u32 v116, v116, v42, v43\n\t" \
  "v_max3_u32 v116, v116, v44, v45\n\t" \
  "v_max3_u32 v116, v116, v46, v47\n\t" \
  "v_cmp_eq_u32 vcc, -1, v116\n\t" \
  "s_cbranch_vccz 3b\n\t" \
  "s_bitcmp1_b32 s59, 6\n\t" \
  "s_cbranch_scc1 12f\n\t" \
  "buffer_load_dwordx4 v[32:35], v100, %[rh], s40 offen sc1\n\t" \
  "buffer_load_dwordx4 v[36:39], v100, %[rh], s76 offen sc1\n\t" \
  "buffer_load_dwordx4 v[40:43], v100, %[rh], s77 offen sc1\n\t" \
  "buffer_load_dwordx4 v[44:47], v100, %[rh], s78 offen sc1\n\t" \
  "s_branch 17b\n\t" \
  "12:\n\t" \
  "s_mov_b32 s42, s58\n\t" \
  "s_branch 99f\n\t" \
  "91:\n\t" \
  "s_mov_b32 s58, s42\n\t" \
  "20:\n\t" \
  "s_and_b32 s76, s44, 7\n\t" \
  "s_lshl_b32 s76, s76, 15\n\t" \
  "s_add_i32 s40, s54, s76\n\t" \
  "s_and_b32 s76, s44, 3\n\t" \
  "s_lshl_b32 s45, s76, 14\n\t" \
  "s_lshl_b32 s47, s76, 20\n\t" \
  "s_add_i32 s47, s47, s55\n\t" \
  "s_lshr_b32 s76, s47, 21\n\t" \
  "s_bitcmp1_b64 %[sel], s76\n\t" \
  "s_cselect_b32 s76, 0x2000, 0\n\t" \
  "s_add_i32 s47, s47, s76\n\t" \
  "s_add_i32 s49, s44, -1\n\t" \
  "s_and_b32 s48, s49, 2\n\t" \
  "s_lshl_b32 s48, s48, 13\n\t" \
  "s_and_b32 s76, s49, 1\n\t" \
  "s_lshl_b32 s76, s76, 12\n\t" \
  "s_add_i32 s48, s48, s76\n\t" \
  "s_lshl_b32 s50, s53, 8\n\t" \
  "s_mov_b32 s59, 0\n\t" \
  "s_mov_b32 s92, s51\n\t" \
  "s_mov_b32 s93, s53\n\t" \
  "s_xor_b32 s76, s40, 0x400\n\t" \
  "s_xor_b32 s77, s40, 0x800\n\t" \
  "s_xor_b32 s78, s40, 0xc00\n\t" \
  "s_cmp_lg_u32 s58, 0\n\t" \
  "s_cbranch_scc1 11f\n\t" \
  "10:\n\t" \
  "ds_read_b128 v[48:51], v102 offset:16384\n\t" \
  "ds_read_b128 v[52:55], v102 offset:17408\n\t" \
  "ds_read_b128 v[56:59], v102 offset:18432\n\t" \
  "ds_read_b128 v[60:63], v102 offset:19456\n\t" \
  "ds_read_b128 v[0:3], v102 offset:32768\n\t" \
  "ds_read_b128 v[4:7], v102 offset:33792\n\t" \
  "ds_read_b128 v[8:11], v102 offset:34816\n\t" \
  "ds_read_b128 v[12:15], v102 offset:35840\n\t" \
  "ds_read_b128 v[16:19], v102 offset:49152\n\t" \
  "ds_read_b128 v[20:23], v102 offset:50176\n\t" \
  "ds_read_b128 v[24:27], v102 offset:51200\n\t" \
  "ds_read_b128 v[28:31], v102 offset:52224\n\t" \
  "s_waitcnt lgkmcnt(4)\n\t" \
  "ds_read_b128 v[80:83], v102 offset:0\n\t" \
  "ds_read_b128 v[84:87], v102 offset:1024\n\t" \
  "ds_read_b128 v[88:91], v102 offset:2048\n\t" \
  "ds_read_b128 v[92:95], v102 offset:3072\n\t" \
  "s_bitcmp1_b32 s58, 1\n\t" \
  "s_cselect_b64 s[80:81], -1, 0\n\t" \
  "s_cmp_lg_u32 s58, 0\n\t" \
  "s_cbranch_scc1 17f\n\t" \
  "s_mov_b32 exec_hi, 0\n\t" \
  "16:\n\t" \
  "s_memtime vcc\n\t" \
  "s_waitcnt lgkmcnt(0)\n\t" \
  "s_sub_i32 s79, s41, vcc_lo\n\t" \
  "s_cmp_gt_i32 s79, 64\n\t" \
  "s_cbranch_scc1 16b\n\t" \
  "13:\n\t" \
  "buffer_load_dword v116, v112, %[rh], s48 offen sc1\n\t" \
  "s_waitcnt vmcnt(0)\n\t" \
  "15:\n\t" \
  "v_mov_b32 v117, 20000\n\t" \
  "v_cmp_ne_u32_sdwa vcc, v116, s49 src0_sel:BYTE_0 src1_sel:DWORD\n\t" \
  "v_subrev_u32 v116, s50, v116\n\t" \
  "v_ashrrev_i32 v116, 8, v116\n\t" \
  "s_nop 1\n\t" \
  "v_max_i32_dpp v116, v116, v116 quad_perm:[1,0,3,2] row_mask:0xf bank_mask:0xf\n\t" \
  "s_cbranch_vccnz 14f\n\t" \
  "s_nop 0\n\t" \
  "v_max_i32_dpp v116, v116, v116 quad_perm:[2,3,0,1] row_mask:0xf bank_mask:0xf\n\t" \
  "s_nop 1\n\t" \
  "v_max_i32_dpp v116, v116, v116 row_half_mirror row_mask:0xf bank_mask:0xf\n\t" \
  "s_nop 1\n\t" \
  "v_max_i32_dpp v116, v116, v116 row_mirror row_mask:0xf bank_mask:0xf\n\t" \
  "v_med3_i32 v116, v116, 0, v117\n\t" \
  "v_mul_u32_u24 v116, s52, v116\n\t" \
  "v_lshrrev_b32 v116, 16, v116\n\t" \
  "s_mov_b64 exec, -1\n\t" \
  "v_readfirstlane_b32 s84, v116\n\t" \
  "v_readlane_b32 s85, v116, 16\n\t" \
  "s_max_u32 s84, s84, s85\n\t" \
  "s_add_i32 s84, s84, s51\n\t" \
  "1:\n\t" \
  "s_memtime vcc\n\t" \
  "s_waitcnt lgkmcnt(0)\n\t" \
  "s_sub_i32 s79, s84, vcc_lo\n\t" \
  "s_cmp_gt_i32 s79, 128\n\t" \
  "s_cbranch_scc1 1b\n\t" \
  ".p2align 4\n\t" \
  "2:\n\t" \
  "s_sub_i32 s79, s79, 33\n\t" \
  "s_cmp_gt_i32 s79, 56\n\t" \
  "s_cbranch_scc1 2b\n\t" \
  "buffer_load_dwordx4 v[32:35], v100, %[rh], s40 offen sc1\n\t" \
  "s_nop 12\n\t" \
  "buffer_load_dwordx4 v[36:39], v100, %[rh], s76 offen sc1\n\t" \
  "s_nop 12\n\t" \
  "buffer_load_dwordx4 v[40:43], v100, %[rh], s77 offen sc1\n\t" \
  "s_nop 12\n\t" \
  "buffer_load_dwordx4 v[44:47], v100, %[rh], s78 offen sc1\n\t" \
  "s_memtime vcc\n\t" \
  "s_memrealtime s[82:83]\n\t" \
  "s_nop 4\n\t" \
  "s_memrealtime s[84:85]\n\t" \
  "s_nop 4\n\t" \
  "s_memrealtime s[56:57]\n\t" \
  "s_nop 4\n\t" \
  "s_memrealtime s[86:87]\n\t" \
  "s_nop 4\n\t" \
  "s_memrealtime s[88:89]\n\t" \
  "s_nop 4\n\t" \
  "s_memrealtime s[90:91]\n\t" \
  "s_mov_b32 s92, 0\n\t" \
  "s_waitcnt lgkmcnt(0)\n\t" \
  "s_cmp_eq_u32 s82, s84\n\t" \
  "s_addc_u32 s92, s92, 0\n\t" \
  "s_cmp_eq_u32 s82, s56\n\t" \
  "s_addc_u32 s92, s92, 0\n\t" \
  "s_cmp_eq_u32 s82, s86\n\t" \
  "s_addc_u32 s92, s92, 0\n\t" \
  "s_cmp_eq_u32 s82, s88\n\t" \
  "s_addc_u32 s92, s92, 0\n\t" \
  "s_cmp_eq_u32 s82, s90\n\t" \
  "s_addc_u32 s92, s92, 0\n\t" \
  "s_mul_i32 s92, s92, 24\n\t" \
  "s_add_i32 s92, s92, vcc_lo\n\t" \
  "s_lshl4_add_u32 s93, s82, 64\n\t" \
  "3:\n\t" \
  "v_mov_b64 v[72:73], 0\n\t" \
  "v_mov_b64 v[74:75], 0\n\t" \
  "v_mov_b64 v[76:77], 0\n\t" \
  "v_mov_b64 v[78:79], 0\n\t" \
  "s_waitcnt lgkmcnt(0)\n\t" \
  "s_waitcnt vmcnt(3)\n\t" \
  "v_smfmac_f32_16x16x64_bf16 v[72:75], v[32:35], v[48:55], v104\n\t" \
  "ds_write_b128 v101, v[32:35] offset:4096\n\t" \
  "v_smfmac_f32_16x16x64_bf16 v[76:79], v[32:35], v[56:63], v104\n\t" \
  "s_waitcnt vmcnt(2)\n\t" \
  "ds_write_b128 v101, v[36:39] offset:5120\n\t" \
  "v_smfmac_f32_16x16x64_bf16 v[72:75], v[36:39], a[48:55], v104\n\t" \
  "v_smfmac_f32_16x16x64_bf16 v[76:79], v[36:39], a[56:63], v104\n\t" \
  "s_nop 3\n\t" \
  "v_cmp_u_f32 vcc, v72, v73\n\t" \
  "v_cmp_u_f32 s[82:83], v74, v75\n\t" \
  "s_waitcnt vmcnt(1)\n\t" \
  "v_smfmac_f32_16x16x64_bf16 v[72:75], v[40:43], a[64:71], v104\n\t" \
  "s_or_b64 vcc, vcc, s[82:83]\n\t" \
  "s_or_b64 vcc, vcc, s[80:81]\n\t" \
  "v_smfmac_f32_16x16x64_bf16 v[76:79], v[40:43], a[72:79], v104\n\t" \
  "s_waitcnt lgkmcnt(0)\n\t" \
  "s_cbranch_scc1 8f\n\t" \
  "5:\n\t" \
  "s_barrier\n\t" \
  "4:\n\t" \
  "s_memtime s[56:57]\n\t" \
  "ds_read_b128 v[48:51], v101 offset:8192\n\t" \
  "ds_read_b128 v[52:55], v101 offset:9216\n\t" \
  "ds_read_b128 v[56:59], v101 offset:13312\n\t" \
  "s_waitcnt vmcnt(0)\n\t" \
  "v_smfmac_f32_16x16x64_bf16 v[72:75], v[44:47], a[80:87], v104\n\t" \
  "ds_read_b128 v[60:63], v101 offset:12288\n\t" \
  "ds_read_b128 v[64:67], v101 offset:1024\n\t" \
  "v_smfmac_f32_16x16x64_bf16 v[76:79], v[44:47], a[88:95], v104\n\t" \
  "ds_read_b128 v[68:71], v101 offset:0\n\t" \
  "ds_write_b128 v101, v[40:43] offset:6144\n\t" \
  "s_nop 1\n\t" \
  "v_cmp_u_f32 vcc, v72, v73\n\t" \
  "v_cmp_u_f32 s[82:83], v74, v75\n\t" \
  "s_waitcnt lgkmcnt(6)\n\t" \
  "v_smfmac_f32_16x16x64_bf16 v[72:75], v[48:51], v[0:7], v104\n\t" \
  "s_or_b64 vcc, vcc, s[82:83]\n\t" \
  "v_smfmac_f32_16x16x64_bf16 v[76:79], v[48:51], v[8:15], v104\n\t" \
  "s_cbranch_scc1 9f\n\t" \
  "6:\n\t" \
  "s_waitcnt lgkmcnt(5)\n\t" \
  "v_smfmac_f32_16x16x64_bf16 v[72:75], v[52:55], a[96:103], v104\n\t" \
  "ds_write_b128 v101, v[44:47] offset:7168\n\t" \
  "v_smfmac_f32_16x16x64_bf16 v[76:79], v[52:55], a[104:111], v104\n\t" \
  "v_mov_b64 v[40:41], 0\n\t" \
  "s_waitcnt lgkmcnt(5)\n\t" \
  "v_smfmac_f32_16x16x64_bf16 v[72:75], v[56:59], a[144:151], v104\n\t" \
  "v_mov_b64 v[42:43], 0\n\t" \
  "v_smfmac_f32_16x16x64_bf16 v[76:79], v[56:59], a[152:159], v104\n\t" \
  "s_waitcnt lgkmcnt(4)\n\t" \
  "v_smfmac_f32_16x16x64_bf16 v[72:75], v[60:63], v[16:23], v104\n\t" \
  "s_waitcnt lgkmcnt(0)\n\t" \
  "s_barrier\n\t" \
  "v_smfmac_f32_16x16x64_bf16 v[76:79], v[60:63], v[24:31], v104\n\t" \
  "ds_read_b128 v[48:51], v101 offset:10240\n\t" \
  "ds_read_b128 v[52:55], v101 offset:14336\n\t" \
  "v_smfmac_f32_16x16x64_bf16 v[72:75], v[64:67], a[0:7], v104\n\t" \
  "ds_read_b128 v[56:59], v101 offset:2048\n\t" \
  "ds_read_b128 v[60:63], v101 offset:11264\n\t" \
  "v_smfmac_f32_16x16x64_bf16 v[76:79], v[64:67], a[8:15], v104\n\t" \
  "ds_read_b128 v[32:35], v101 offset:15360\n\t" \
  "ds_read_b128 v[36:39], v101 offset:3072\n\t" \
  "v_smfmac_f32_16x16x64_bf16 v[72:75], v[68:71], v[80:87], v104\n\t" \
  "ds_read_b128 v[0:3], v103 offset:0\n\t" \
  "v_mov_b64 v[44:45], 0\n\t" \
  "v_smfmac_f32_16x16x64_bf16 v[76:79], v[68:71], v[88:95], v104\n\t" \
  "v_mov_b64 v[46:47], 0\n\t" \
  "s_waitcnt lgkmcnt(5)\n\t" \
  "v_smfmac_f32_16x16x64_bf16 v[72:75], v[48:51], a[112:119], v104\n\t" \
  "ds_read_b128 v[4:7], v103 offset:1024\n\t" \
  "v_mov_b64 v[80:81], 0\n\t" \
  "v_smfmac_f32_16x16x64_bf16 v[72:75], v[52:55], a[160:167], v104\n\t" \
  "v_mov_b64 v[82:83], 0\n\t" \
  "s_waitcnt lgkmcnt(2)\n\t" \
  "v_smfmac_f32_16x16x64_bf16 v[72:75], v[56:59], a[16:23], v104\n\t" \
  "ds_read_b128 v[8:11], v103 offset:2048\n\t" \
  "v_mov_b64 v[84:85], 0\n\t" \
  "v_smfmac_f32_16x16x64_bf16 v[72:75], v[60:63], a[128:135], v104\n\t" \
  "ds_read_b128 v[12:15], v103 offset:3072\n\t" \
  "v_mov_b64 v[86:87], 0\n\t" \
  "v_smfmac_f32_16x16x64_bf16 v[72:75], v[32:35], a[176:183], v104\n\t" \
  "ds_read_b128 v[16:19], v103 offset:16384\n\t" \
  "v_mov_b64 v[88:89], 0\n\t" \
  "v_smfmac_f32_16x16x64_bf16 v[72:75], v[36:39], a[32:39], v104\n\t" \
  "ds_read_b128 v[20:23], v103 offset:17408\n\t" \
  "v_mov_b64 v[90:91], 0\n\t" \
  "v_smfmac_f32_16x16x64_bf16 v[76:79], v[48:51], a[120:127], v104\n\t" \
  "ds_read_b128 v[24:27], v103 offset:18432\n\t" \
  "v_mov_b64 v[92:93], 0\n\t" \
  "v_smfmac_f32_16x16x64_bf16 v[76:79], v[52:55], a[168:175], v104\n\t" \
  "v_add_f32 v96, v72, v74\n\t" \
  "v_add_f32 v97, v73, v75\n\t" \
  "v_cvt_pk_bf16_f32 v96, v96, v97\n\t" \
  "ds_write_b32 v105, v96\n\t" \
  "v_smfmac_f32_16x16x64_bf16 v[76:79], v[56:59], a[24:31], v104\n\t" \
  "ds_read_b128 v[28:31], v103 offset:19456\n\t" \
  "v_mov_b64 v[94:95], 0\n\t" \
  "v_smfmac_f32_16x16x64_bf16 v[76:79], v[60:63], a[136:143], v104\n\t" \
  "v_smfmac_f32_16x16x64_bf16 v[76:79], v[32:35], a[184:191], v104\n\t" \
  "v_smfmac_f32_16x16x64_bf16 v[76:79], v[36:39], a[40:47], v104\n\t" \
  "s_waitcnt lgkmcnt(1)\n\t" \
  "s_barrier\n\t" \
  "ds_read_b64_tr_b16 v[96:97], v107 offset:0\n\t" \
  "ds_read_b64_tr_b16 v[98:99], v107 offset:64\n\t" \
  "ds_read_b64_tr_b16 v[56:57], v107 offset:1024\n\t" \
  "ds_read_b64_tr_b16 v[58:59], v107 offset:1088\n\t" \
  "s_nop 1\n\t" \
  "v_add_f32 v116, v76, v78\n\t" \
  "v_add_f32 v117, v77, v79\n\t" \
  "v_cvt_pk_bf16_f32 v116, v116, v117\n\t" \
  "ds_write_b32 v105, v116 offset:256\n\t" \
  "s_waitcnt lgkmcnt(3)\n\t" \
  "v_smfmac_f32_16x16x64_bf16 v[40:43], v[96:99], a[192:199], v106\n\t" \
  "v_smfmac_f32_16x16x64_bf16 v[44:47], v[96:99], a[224:231], v106\n\t" \
  "s_waitcnt lgkmcnt(0)\n\t" \
  "s_barrier\n\t" \
  "ds_read_b64_tr_b16 v[52:53], v107 offset:256\n\t" \
  "ds_read_b64_tr_b16 v[54:55], v107 offset:320\n\t" \
  "v_smfmac_f32_16x16x64_bf16 v[40:43], v[56:59], a[208:215], v106\n\t" \
  "ds_read_b64_tr_b16 v[60:61], v107 offset:1280\n\t" \
  "ds_read_b64_tr_b16 v[62:63], v107 offset:1344\n\t" \
  "v_smfmac_f32_16x16x64_bf16 v[44:47], v[56:59], v[0:7], v106\n\t" \
  "v_smfmac_f32_16x16x64_bf16 v[80:83], v[96:99], a[240:247], v106\n\t" \
  "v_mov_b64 v[32:33], 0\n\t" \
  "v_smfmac_f32_16x16x64_bf16 v[84:87], v[96:99], v[136:143], v106\n\t" \
  "v_mov_b64 v[34:35], 0\n\t" \
  "v_smfmac_f32_16x16x64_bf16 v[80:83], v[56:59], v[120:127], v106\n\t" \
  "v_mov_b64 v[36:37], 0\n\t" \
  "v_smfmac_f32_16x16x64_bf16 v[84:87], v[56:59], v[16:23], v106\n\t" \
  "s_waitcnt lgkmcnt(2)\n\t" \
  "v_smfmac_f32_16x16x64_bf16 v[40:43], v[52:55], a[200:207], v106\n\t" \
  "v_smfmac_f32_16x16x64_bf16 v[44:47], v[52:55], a[232:239], v106\n\t" \
  "s_xor_b32 s76, s47, 0x40000\n\t" \
  "s_waitcnt lgkmcnt(0)\n\t" \
  "v_smfmac_f32_16x16x64_bf16 v[40:43], v[60:63], a[216:223], v106\n\t" \
  "v_mov_b64 v[38:39], 0\n\t" \
  "s_xor_b32 s77, s47, 0x80000\n\t" \
  "v_smfmac_f32_16x16x64_bf16 v[44:47], v[60:63], v[8:15], v106\n\t" \
  "s_xor_b32 s78, s47, 0xc0000\n\t" \
  "v_smfmac_f32_16x16x64_bf16 v[80:83], v[52:55], a[248:255], v106\n\t" \
  "ds_read_b128 v[0:3], v103 offset:32768\n\t" \
  "v_cvt_pk_bf16_f32 v64, v40, v41\n\t" \
  "v_smfmac_f32_16x16x64_bf16 v[84:87], v[52:55], v[144:151], v106\n\t" \
  "ds_read_b128 v[4:7], v103 offset:33792\n\t" \
  "v_cvt_pk_bf16_f32 v65, v42, v43\n\t" \
  "v_smfmac_f32_16x16x64_bf16 v[80:83], v[60:63], v[128:135], v106\n\t" \
  "ds_read_b128 v[8:11], v103 offset:34816\n\t" \
  "v_cvt_pk_bf16_f32 v66, v44, v45\n\t" \
  "v_smfmac_f32_16x16x64_bf16 v[84:87], v[60:63], v[24:31], v106\n\t" \
  "ds_read_b128 v[12:15], v103 offset:35840\n\t" \
  "v_cvt_pk_bf16_f32 v67, v46, v47\n\t" \
  "v_smfmac_f32_16x16x64_bf16 v[88:91], v[96:99], v[152:159], v106\n\t" \
  "v_permlane16_swap_b32 v64, v66\n\t" \
  "v_cvt_pk_bf16_f32 v68, v80, v81\n\t" \
  "v_smfmac_f32_16x16x64_bf16 v[92:95], v[96:99], v[184:191], v106\n\t" \
  "v_permlane16_swap_b32 v65, v67\n\t" \
  "v_cvt_pk_bf16_f32 v69, v82, v83\n\t" \
  "v_smfmac_f32_16x16x64_bf16 v[88:91], v[52:55], v[160:167], v106\n\t" \
  "buffer_store_dwordx4 v[64:67], v108, %[ru], s47 offen sc1\n\t" \
  "v_cvt_pk_bf16_f32 v70, v84, v85\n\t" \
  "v_smfmac_f32_16x16x64_bf16 v[92:95], v[52:55], v[192:199], v106\n\t" \
  "v_cvt_pk_bf16_f32 v71, v86, v87\n\t" \
  "v_smfmac_f32_16x16x64_bf16 v[88:91], v[56:59], v[168:175], v106\n\t" \
  "ds_read_b128 v[16:19], v103 offset:49152\n\t" \
  "v_permlane16_swap_b32 v68, v70\n\t" \
  "s_waitcnt lgkmcnt(3)\n\t" \
  "v_smfmac_f32_16x16x64_bf16 v[92:95], v[56:59], v[0:7], v106\n\t" \
  "ds_read_b128 v[20:23], v103 offset:50176\n\t" \
  "v_permlane16_swap_b32 v69, v71\n\t" \
  "v_smfmac_f32_16x16x64_bf16 v[88:91], v[60:63], v[176:183], v106\n\t" \
  "ds_read_b128 v[24:27], v103 offset:51200\n\t" \
  "buffer_store_dwordx4 v[68:71], v108, %[ru], s76 offen sc1\n\t" \
  "s_waitcnt lgkmcnt(3)\n\t" \
  "v_smfmac_f32_16x16x64_bf16 v[92:95], v[60:63], v[8:15], v106\n\t" \
  "ds_read_b128 v[28:31], v103 offset:52224\n\t" \
  "v_smfmac_f32_16x16x64_bf16 v[32:35], v[96:99], v[200:207], v106\n\t" \
  "v_cvt_pk_bf16_f32 v72, v88, v89\n\t" \
  "v_smfmac_f32_16x16x64_bf16 v[36:39], v[96:99], v[232:239], v106\n\t" \
  "v_cvt_pk_bf16_f32 v73, v90, v91\n\t" \
  "v_smfmac_f32_16x16x64_bf16 v[32:35], v[52:55], v[208:215], v106\n\t" \
  "v_cvt_pk_bf16_f32 v74, v92, v93\n\t" \
  "v_smfmac_f32_16x16x64_bf16 v[36:39], v[52:55], v[240:247], v106\n\t" \
  "v_cvt_pk_bf16_f32 v75, v94, v95\n\t" \
  "v_smfmac_f32_16x16x64_bf16 v[32:35], v[56:59], v[216:223], v106\n\t" \
  "v_permlane16_swap_b32 v72, v74\n\t" \
  "v_smfmac_f32_16x16x64_bf16 v[32:35], v[60:63], v[224:231], v106\n\t" \
  "v_permlane16_swap_b32 v73, v75\n\t" \
  "s_waitcnt lgkmcnt(2)\n\t" \
  "v_smfmac_f32_16x16x64_bf16 v[36:39], v[56:59], v[16:23], v106\n\t" \
  "buffer_store_dwordx4 v[72:75], v108, %[ru], s77 offen sc1\n\t" \
  "s_waitcnt lgkmcnt(0)\n\t" \
  "v_smfmac_f32_16x16x64_bf16 v[36:39], v[60:63], v[24:31], v106\n\t" \
  "v_cvt_pk_bf16_f32 v76, v32, v33\n\t" \
  "v_cvt_pk_bf16_f32 v77, v34, v35\n\t" \
  "s_nop 5\n\t" \
  "v_cvt_pk_bf16_f32 v78, v36, v37\n\t" \
  "v_cvt_pk_bf16_f32 v79, v38, v39\n\t" \
  "s_nop 0\n\t" \
  "v_permlane16_swap_b32 v76, v78\n\t" \
  "v_permlane16_swap_b32 v77, v79\n\t" \
  "buffer_store_dwordx4 v[76:79], v108, %[ru], s78 offen sc1\n\t" \
  "s_memtime s[76:77]\n\t" \
  "s_mov_b32 s42, 0\n\t" \
  "s_waitcnt lgkmcnt(0)\n\t" \
  "s_mov_b32 s57, s76\n\t" \
  "s_add_i32 s41, s56, 1950\n\t" \
  "s_sub_i32 s76, s92, s51\n\t" \
  "s_sub_i32 s77, s93, s53\n\t" \
  "s_cmp_eq_u32 s76, 0\n\t" \
  "s_cbranch_scc1 21f\n\t" \
  "v_cvt_f32_u32 v117, s76\n\t" \
  "v_cvt_f32_u32 v116, s77\n\t" \
  "v_rcp_f32 v117, v117\n\t" \
  "s_mov_b32 s51, s92\n\t" \
  "v_mul_f32 v117, v117, v116\n\t" \
  "v_max_f32 v117, 0x3f028f5c, v117\n\t" \
  "v_min_f32 v117, 0x3ffeb852, v117\n\t" \
  "v_rcp_f32 v116, v117\n\t" \
  "v_mul_f32 v117, 0x4f000000, v117\n\t" \
  "v_mul_f32 v116, 0x47800000, v116\n\t" \
  "v_cvt_u32_f32 v117, v117\n\t" \
  "v_cvt_u32_f32 v116, v116\n\t" \
  "s_mov_b32 s53, s93\n\t" \
  "v_readfirstlane_b32 s46, v117\n\t" \
  "v_readfirstlane_b32 s52, v116\n\t" \
  "21:\n\t" \
  "s_add_i32 s44, s44, 1\n\t" \
  "s_and_b32 s58, s58, 4\n\t" \
  "s_and_b32 s76, s44, 64\n\t" \
  "s_or_b32 s42, s42, s76\n\t" \
  "s_cbranch_scc0 20b\n\t" \
  "s_branch 99f\n\t" \
  "14:\n\t" \
  "s_sleep 3\n\t" \
  "s_add_u32 s59, s59, 0x10000\n\t" \
  "s_cmp_lt_u32 s59, 0x100000\n\t" \
  "s_cbranch_scc1 13b\n\t" \
  "s_mov_b64 exec, -1\n\t" \
  "s_mov_b32 s42, 16\n\t" \
  "s_branch 99f\n\t" \
  "8:\n\t" \
  "s_bitcmp1_b32 s58, 1\n\t" \
  "s_cbranch_scc1 4b\n\t" \
  "s_bitcmp1_b32 s58, 2\n\t" \
  "s_cbranch_scc1 5b\n\t" \
  "v_max3_u32 v116, v32, v33, v34\n\t" \
  "v_max3_u32 v117, v35, v36, v37\n\t" \
  "v_max3_u32 v116, v116, v38, v39\n\t" \
  "v_max_u32 v116, v116, v117\n\t" \
  "v_cmp_eq_u32 vcc, -1, v116\n\t" \
  "s_cbranch_vccz 5b\n\t" \
  "s_branch 0f\n\t" \
  "9:\n\t" \
  "s_bitcmp1_b32 s58, 2\n\t" \
  "s_cbranch_scc1 6b\n\t" \
  "v_max3_u32 v116, v40, v41, v42\n\t" \
  "v_max3_u32 v117, v43, v44, v45\n\t" \
  "v_max3_u32 v116, v116, v46, v47\n\t" \
  "v_max_u32 v116, v116, v117\n\t" \
  "v_cmp_eq_u32 vcc, -1, v116\n\t" \
  "s_cbranch_vccz 6b\n\t" \
  "s_or_b32 s58, s58, 2\n\t" \
  "0:\n\t" \
  "s_or_b32 s58, s58, 1\n\t" \
  "11:\n\t" \
  "buffer_load_dwordx4 v[32:35], v100, %[rh], s40 offen sc1\n\t" \
  "buffer_load_dwordx4 v[36:39], v100, %[rh], s76 offen sc1\n\t" \
  "buffer_load_dwordx4 v[40:43], v100, %[rh], s77 offen sc1\n\t" \
  "buffer_load_dwordx4 v[44:47], v100, %[rh], s78 offen sc1\n\t" \
  "s_waitcnt lgkmcnt(0)\n\t" \
  "s_branch 10b\n\t" \
  "17:\n\t" \
  "s_add_u32 s59, s59, 1\n\t" \
  "s_bitcmp1_b32 s58, 2\n\t" \
  "s_cbranch_scc1 3b\n\t" \
  "s_waitcnt vmcnt(0)\n\t" \
  "v_max_u32 v116, v32, v33\n\t" \
  "v_max3_u32 v116, v116, v34, v35\n\t" \
  "v_max3_u32 v116, v116, v36, v37\n\t" \
  "v_max3_u32 v116, v116, v38, v39\n\t" \
  "v_max3_u32 v116, v116, v40, v41\n\t" \
  "v_max3_u32 v116, v116, v42, v43\n\t" \
  "v_max3_u32 v116, v116, v44, v45\n\t" \
  "v_max3_u32 v116, v116, v46, v47\n\t" \
  "v_cmp_eq_u32 vcc, -1, v116\n\t" \
  "s_cbranch_vccz 3b\n\t" \
  "s_bitcmp1_b32 s59, 6\n\t" \
  "s_cbranch_scc1 12f\n\t" \
  "buffer_load_dwordx4 v[32:35], v100, %[rh], s40 offen sc1\n\t" \
  "buffer_load_dwordx4 v[36:39], v100, %[rh], s76 offen sc1\n\t" \
  "buffer_load_dwordx4 v[40:43], v100, %[rh], s77 offen sc1\n\t" \
  "buffer_load_dwordx4 v[44:47], v100, %[rh], s78 offen sc1\n\t" \
  "s_branch 17b\n\t" \
  "12:\n\t" \
  "s_mov_b32 s42, s58\n\t" \
  "s_branch 99f\n\t" \
  "92:\n\t" \
  "s_mov_b32 s58, s42\n\t" \
  "20:\n\t" \
  "s_and_b32 s76, s44, 7\n\t" \
  "s_lshl_b32 s76, s76, 15\n\t" \
  "s_add_i32 s40, s54, s76\n\t" \
  "s_and_b32 s76, s44, 3\n\t" \
  "s_lshl_b32 s45, s76, 14\n\t" \
  "s_lshl_b32 s47, s76, 20\n\t" \
  "s_add_i32 s47, s47, s55\n\t" \
  "s_lshr_b32 s76, s47, 21\n\t" \
  "s_bitcmp1_b64 %[sel], s76\n\t" \
  "s_cselect_b32 s76, 0x2000, 0\n\t" \
  "s_add_i32 s47, s47, s76\n\t" \
  "s_add_i32 s49, s44, -1\n\t" \
  "s_and_b32 s48, s49, 2\n\t" \
  "s_lshl_b32 s48, s48, 13\n\t" \
  "s_and_b32 s76, s49, 1\n\t" \
  "s_lshl_b32 s76, s76, 12\n\t" \
  "s_add_i32 s48, s48, s76\n\t" \
  "s_lshl_b32 s50, s53, 8\n\t" \
  "s_mov_b32 s59, 0\n\t" \
  "s_mov_b32 s92, s51\n\t" \
  "s_mov_b32 s93, s53\n\t" \
  "s_xor_b32 s76, s40, 0x400\n\t" \
  "s_xor_b32 s77, s40, 0x800\n\t" \
  "s_xor_b32 s78, s40, 0xc00\n\t" \
  "s_cmp_lg_u32 s58, 0\n\t" \
  "s_cbranch_scc1 11f\n\t" \
  "10:\n\t" \
  "ds_read_b128 v[48:51], v102 offset:32768\n\t" \
  "ds_read_b128 v[52:55], v102 offset:33792\n\t" \
  "ds_read_b128 v[56:59], v102 offset:34816\n\t" \
  "ds_read_b128 v[60:63], v102 offset:35840\n\t" \
  "ds_read_b128 v[0:3], v102 offset:49152\n\t" \
  "ds_read_b128 v[4:7], v102 offset:50176\n\t" \
  "ds_read_b128 v[8:11], v102 offset:51200\n\t" \
  "ds_read_b128 v[12:15], v102 offset:52224\n\t" \
  "ds_read_b128 v[16:19], v102 offset:0\n\t" \
  "ds_read_b128 v[20:23], v102 offset:1024\n\t" \
  "ds_read_b128 v[24:27], v102 offset:2048\n\t" \
  "ds_read_b128 v[28:31], v102 offset:3072\n\t" \
  "s_waitcnt lgkmcnt(4)\n\t" \
  "ds_read_b128 v[80:83], v102 offset:16384\n\t" \
  "ds_read_b128 v[84:87], v102 offset:17408\n\t" \
  "ds_read_b128 v[88:91], v102 offset:18432\n\t" \
  "ds_read_b128 v[92:95], v102 offset:19456\n\t" \
  "s_bitcmp1_b32 s58, 1\n\t" \
  "s_cselect_b64 s[80:81], -1, 0\n\t" \
  "s_cmp_lg_u32 s58, 0\n\t" \
  "s_cbranch_scc1 17f\n\t" \
  "s_mov_b32 exec_hi, 0\n\t" \
  "16:\n\t" \
  "s_memtime vcc\n\t" \
  "s_waitcnt lgkmcnt(0)\n\t" \
  "s_sub_i32 s79, s41, vcc_lo\n\t" \
  "s_cmp_gt_i32 s79, 64\n\t" \
  "s_cbranch_scc1 16b\n\t" \
  "13:\n\t" \
  "buffer_load_dword v116, v112, %[rh], s48 offen sc1\n\t" \
  "s_waitcnt vmcnt(0)\n\t" \
  "15:\n\t" \
  "v_mov_b32 v117, 20000\n\t" \
  "v_cmp_ne_u32_sdwa vcc, v116, s49 src0_sel:BYTE_0 src1_sel:DWORD\n\t" \
  "v_subrev_u32 v116, s50, v116\n\t" \
  "v_ashrrev_i32 v116, 8, v116\n\t" \
  "s_nop 1\n\t" \
  "v_max_i32_dpp v116, v116, v116 quad_perm:[1,0,3,2] row_mask:0xf bank_mask:0xf\n\t" \
  "s_cbranch_vccnz 14f\n\t" \
  "s_nop 0\n\t" \
  "v_max_i32_dpp v116, v116, v116 quad_perm:[2,3,0,1] row_mask:0xf bank_mask:0xf\n\t" \
  "s_nop 1\n\t" \
  "v_max_i32_dpp v116, v116, v116 row_half_mirror row_mask:0xf bank_mask:0xf\n\t" \
  "s_nop 1\n\t" \
  "v_max_i32_dpp v116, v116, v116 row_mirror row_mask:0xf bank_mask:0xf\n\t" \
  "v_med3_i32 v116, v116, 0, v117\n\t" \
  "v_mul_u32_u24 v116, s52, v116\n\t" \
  "v_lshrrev_b32 v116, 16, v116\n\t" \
  "s_mov_b64 exec, -1\n\t" \
  "v_readfirstlane_b32 s84, v116\n\t" \
  "v_readlane_b32 s85, v116, 16\n\t" \
  "s_max_u32 s84, s84, s85\n\t" \
  "s_add_i32 s84, s84, s51\n\t" \
  "1:\n\t" \
  "s_memtime vcc\n\t" \
  "s_waitcnt lgkmcnt(0)\n\t" \
  "s_sub_i32 s79, s84, vcc_lo\n\t" \
  "s_cmp_gt_i32 s79, 128\n\t" \
  "s_cbranch_scc1 1b\n\t" \
  ".p2align 4\n\t" \
  "2:\n\t" \
  "s_sub_i32 s79, s79, 33\n\t" \
  "s_cmp_gt_i32 s79, 56\n\t" \
  "s_cbranch_scc1 2b\n\t" \
  "buffer_load_dwordx4 v[32:35], v100, %[rh], s40 offen sc1\n\t" \
  "s_nop 12\n\t" \
  "buffer_load_dwordx4 v[36:39], v100, %[rh], s76 offen sc1\n\t" \
  "s_nop 12\n\t" \
  "buffer_load_dwordx4 v[40:43], v100, %[rh], s77 offen sc1\n\t" \
  "s_nop 12\n\t" \
  "buffer_load_dwordx4 v[44:47], v100, %[rh], s78 offen sc1\n\t" \
  "s_memtime vcc\n\t" \
  "s_memrealtime s[82:83]\n\t" \
  "s_nop 4\n\t" \
  "s_memrealtime s[84:85]\n\t" \
  "s_nop 4\n\t" \
  "s_memrealtime s[56:57]\n\t" \
  "s_nop 4\n\t" \
  "s_memrealtime s[86:87]\n\t" \
  "s_nop 4\n\t" \
  "s_memrealtime s[88:89]\n\t" \
  "s_nop 4\n\t" \
  "s_memrealtime s[90:91]\n\t" \
  "s_mov_b32 s92, 0\n\t" \
  "s_waitcnt lgkmcnt(0)\n\t" \
  "s_cmp_eq_u32 s82, s84\n\t" \
  "s_addc_u32 s92, s92, 0\n\t" \
  "s_cmp_eq_u32 s82, s56\n\t" \
  "s_addc_u32 s92, s92, 0\n\t" \
  "s_cmp_eq_u32 s82, s86\n\t" \
  "s_addc_u32 s92, s92, 0\n\t" \
  "s_cmp_eq_u32 s82, s88\n\t" \
  "s_addc_u32 s92, s92, 0\n\t" \
  "s_cmp_eq_u32 s82, s90\n\t" \
  "s_addc_u32 s92, s92, 0\n\t" \
  "s_mul_i32 s92, s92, 24\n\t" \
  "s_add_i32 s92, s92, vcc_lo\n\t" \
  "s_lshl4_add_u32 s93, s82, 64\n\t" \
  "3:\n\t" \
  "v_mov_b64 v[72:73], 0\n\t" \
  "v_mov_b64 v[74:75], 0\n\t" \
  "v_mov_b64 v[76:77], 0\n\t" \
  "v_mov_b64 v[78:79], 0\n\t" \
  "s_waitcnt lgkmcnt(0)\n\t" \
  "s_waitcnt vmcnt(3)\n\t" \
  "v_smfmac_f32_16x16x64_bf16 v[72:75], v[32:35], v[48:55], v104\n\t" \
  "ds_write_b128 v101, v[32:35] offset:8192\n\t" \
  "v_smfmac_f32_16x16x64_bf16 v[76:79], v[32:35], v[56:63], v104\n\t" \
  "s_waitcnt vmcnt(2)\n\t" \
  "ds_write_b128 v101, v[36:39] offset:9216\n\t" \
  "v_smfmac_f32_16x16x64_bf16 v[72:75], v[36:39], a[96:103], v104\n\t" \
  "v_smfmac_f32_16x16x64_bf16 v[76:79], v[36:39], a[104:111], v104\n\t" \
  "s_nop 3\n\t" \
  "v_cmp_u_f32 vcc, v72, v73\n\t" \
  "v_cmp_u_f32 s[82:83], v74, v75\n\t" \
  "s_waitcnt vmcnt(1)\n\t" \
  "v_smfmac_f32_16x16x64_bf16 v[72:75], v[40:43], a[112:119], v104\n\t" \
  "s_or_b64 vcc, vcc, s[82:83]\n\t" \
  "s_or_b64 vcc, vcc, s[80:81]\n\t" \
  "v_smfmac_f32_16x16x64_bf16 v[76:79], v[40:43], a[120:127], v104\n\t" \
  "s_waitcnt lgkmcnt(0)\n\t" \
  "s_cbranch_scc1 8f\n\t" \
  "5:\n\t" \
  "s_barrier\n\t" \
  "4:\n\t" \
  "s_memtime s[56:57]\n\t" \
  "ds_read_b128 v[48:51], v101 offset:12288\n\t" \
  "ds_read_b128 v[52:55], v101 offset:13312\n\t" \
  "ds_read_b128 v[56:59], v101 offset:1024\n\t" \
  "s_waitcnt vmcnt(0)\n\t" \
  "v_smfmac_f32_16x16x64_bf16 v[72:75], v[44:47], a[128:135], v104\n\t" \
  "ds_read_b128 v[60:63], v101 offset:0\n\t" \
  "ds_read_b128 v[64:67], v101 offset:5120\n\t" \
  "v_smfmac_f32_16x16x64_bf16 v[76:79], v[44:47], a[136:143], v104\n\t" \
  "ds_read_b128 v[68:71], v101 offset:4096\n\t" \
  "ds_write_b128 v101, v[40:43] offset:10240\n\t" \
  "s_nop 1\n\t" \
  "v_cmp_u_f32 vcc, v72, v73\n\t" \
  "v_cmp_u_f32 s[82:83], v74, v75\n\t" \
  "s_waitcnt lgkmcnt(6)\n\t" \
  "v_smfmac_f32_16x16x64_bf16 v[72:75], v[48:51], v[0:7], v104\n\t" \
  "s_or_b64 vcc, vcc, s[82:83]\n\t" \
  "v_smfmac_f32_16x16x64_bf16 v[76:79], v[48:51], v[8:15], v104\n\t" \
  "s_cbranch_scc1 9f\n\t" \
  "6:\n\t" \
  "s_waitcnt lgkmcnt(5)\n\t" \
  "v_smfmac_f32_16x16x64_bf16 v[72:75], v[52:55], a[144:151], v104\n\t" \
  "ds_write_b128 v101, v[44:47] offset:11264\n\t" \
  "v_smfmac_f32_16x16x64_bf16 v[76:79], v[52:55], a[152:159], v104\n\t" \
  "v_mov_b64 v[40:41], 0\n\t" \
  "s_waitcnt lgkmcnt(5)\n\t" \
  "v_smfmac_f32_16x16x64_bf16 v[72:75], v[56:59], a[0:7], v104\n\t" \
  "v_mov_b64 v[42:43], 0\n\t" \
  "v_smfmac_f32_16x16x64_bf16 v[76:79], v[56:59], a[8:15], v104\n\t" \
  "s_waitcnt lgkmcnt(4)\n\t" \
  "v_smfmac_f32_16x16x64_bf16 v[72:75], v[60:63], v[16:23], v104\n\t" \
  "s_waitcnt lgkmcnt(0)\n\t" \
  "s_barrier\n\t" \
  "v_smfmac_f32_16x16x64_bf16 v[76:79], v[60:63], v[24:31], v104\n\t" \
  "ds_read_b128 v[48:51], v101 offset:14336\n\t" \
  "ds_read_b128 v[52:55], v101 offset:2048\n\t" \
  "v_smfmac_f32_16x16x64_bf16 v[72:75], v[64:67], a[48:55], v104\n\t" \
  "ds_read_b128 v[56:59], v101 offset:6144\n\t" \
  "ds_read_b128 v[60:63], v101 offset:15360\n\t" \
  "v_smfmac_f32_16x16x64_bf16 v[76:79], v[64:67], a[56:63], v104\n\t" \
  "ds_read_b128 v[32:35], v101 offset:3072\n\t" \
  "ds_read_b128 v[36:39], v101 offset:7168\n\t" \
  "v_smfmac_f32_16x16x64_bf16 v[72:75], v[68:71], v[80:87], v104\n\t" \
  "ds_read_b128 v[0:3], v103 offset:0\n\t" \
  "v_mov_b64 v[44:45], 0\n\t" \
  "v_smfmac_f32_16x16x64_bf16 v[76:79], v[68:71], v[88:95], v104\n\t" \
  "v_mov_b64 v[46:47], 0\n\t" \
  "s_waitcnt lgkmcnt(5)\n\t" \
  "v_smfmac_f32_16x16x64_bf16 v[72:75], v[48:51], a[160:167], v104\n\t" \
  "ds_read_b128 v[4:7], v103 offset:1024\n\t" \
  "v_mov_b64 v[80:81], 0\n\t" \
  "v_smfmac_f32_16x16x64_bf16 v[72:75], v[52:55], a[16:23], v104\n\t" \
  "v_mov_b64 v[82:83], 0\n\t" \
  "s_waitcnt lgkmcnt(2)\n\t" \
  "v_smfmac_f32_16x16x64_bf16 v[72:75], v[56:59], a[64:71], v104\n\t" \
  "ds_read_b128 v[8:11], v103 offset:2048\n\t" \
  "v_mov_b64 v[84:85], 0\n\t" \
  "v_smfmac_f32_16x16x64_bf16 v[72:75], v[60:63], a[176:183], v104\n\t" \
  "ds_read_b128 v[12:15], v103 offset:3072\n\t" \
  "v_mov_b64 v[86:87], 0\n\t" \
  "v_smfmac_f32_16x16x64_bf16 v[72:75], v[32:35], a[32:39], v104\n\t" \
  "ds_read_b128 v[16:19], v103 offset:16384\n\t" \
  "v_mov_b64 v[88:89], 0\n\t" \
  "v_smfmac_f32_16x16x64_bf16 v[72:75], v[36:39], a[80:87], v104\n\t" \
  "ds_read_b128 v[20:23], v103 offset:17408\n\t" \
  "v_mov_b64 v[90:91], 0\n\t" \
  "v_smfmac_f32_16x16x64_bf16 v[76:79], v[48:51], a[168:175], v104\n\t" \
  "ds_read_b128 v[24:27], v103 offset:18432\n\t" \
  "v_mov_b64 v[92:93], 0\n\t" \
  "v_smfmac_f32_16x16x64_bf16 v[76:79], v[52:55], a[24:31], v104\n\t" \
  "v_add_f32 v96, v72, v74\n\t" \
  "v_add_f32 v97, v73, v75\n\t" \
  "v_cvt_pk_bf16_f32 v96, v96, v97\n\t" \
  "ds_write_b32 v105, v96\n\t" \
  "v_smfmac_f32_16x16x64_bf16 v[76:79], v[56:59], a[72:79], v104\n\t" \
  "ds_read_b128 v[28:31], v103 offset:19456\n\t" \
  "v_mov_b64 v[94:95], 0\n\t" \
  "v_smfmac_f32_16x16x64_bf16 v[76:79], v[60:63], a[184:191], v104\n\t" \
  "v_smfmac_f32_16x16x64_bf16 v[76:79], v[32:35], a[40:47], v104\n\t" \
  "v_smfmac_f32_16x16x64_bf16 v[76:79], v[36:39], a[88:95], v104\n\t" \
  "s_waitcnt lgkmcnt(1)\n\t" \
  "s_barrier\n\t" \
  "ds_read_b64_tr_b16 v[96:97], v107 offset:0\n\t" \
  "ds_read_b64_tr_b16 v[98:99], v107 offset:64\n\t" \
  "ds_read_b64_tr_b16 v[56:57], v107 offset:1024\n\t" \
  "ds_read_b64_tr_b16 v[58:59], v107 offset:1088\n\t" \
  "s_nop 1\n\t" \
  "v_add_f32 v116, v76, v78\n\t" \
  "v_add_f32 v117, v77, v79\n\t" \
  "v_cvt_pk_bf16_f32 v116, v116, v117\n\t" \
  "ds_write_b32 v105, v116 offset:256\n\t" \
  "s_waitcnt lgkmcnt(3)\n\t" \
  "v_smfmac_f32_16x16x64_bf16 v[40:43], v[96:99], a[192:199], v106\n\t" \
  "v_smfmac_f32_16x16x64_bf16 v[44:47], v[96:99], a[224:231], v106\n\t" \
  "s_waitcnt lgkmcnt(0)\n\t" \
  "s_barrier\n\t" \
  "ds_read_b64_tr_b16 v[52:53], v107 offset:256\n\t" \
  "ds_read_b64_tr_b16 v[54:55], v107 offset:320\n\t" \
  "v_smfmac_f32_16x16x64_bf16 v[40:43], v[56:59], a[208:215], v106\n\t" \
  "ds_read_b64_tr_b16 v[60:61], v107 offset:1280\n\t" \
  "ds_read_b64_tr_b16 v[62:63], v107 offset:1344\n\t" \
  "v_smfmac_f32_16x16x64_bf16 v[44:47], v[56:59], v[0:7], v106\n\t" \
  "v_smfmac_f32_16x16x64_bf16 v[80:83], v[96:99], a[240:247], v106\n\t" \
  "v_mov_b64 v[32:33], 0\n\t" \
  "v_smfmac_f32_16x16x64_bf16 v[84:87], v[96:99], v[136:143], v106\n\t" \
  "v_mov_b64 v[34:35], 0\n\t" \
  "v_smfmac_f32_16x16x64_bf16 v[80:83], v[56:59], v[120:127], v106\n\t" \
  "v_mov_b64 v[36:37], 0\n\t" \
  "v_smfmac_f32_16x16x64_bf16 v[84:87], v[56:59], v[16:23], v106\n\t" \
  "s_waitcnt lgkmcnt(2)\n\t" \
  "v_smfmac_f32_16x16x64_bf16 v[40:43], v[52:55], a[200:207], v106\n\t" \
  "v_smfmac_f32_16x16x64_bf16 v[44:47], v[52:55], a[232:239], v106\n\t" \
  "s_xor_b32 s76, s47, 0x40000\n\t" \
  "s_waitcnt lgkmcnt(0)\n\t" \
  "v_smfmac_f32_16x16x64_bf16 v[40:43], v[60:63], a[216:223], v106\n\t" \
  "v_mov_b64 v[38:39], 0\n\t" \
  "s_xor_b32 s77, s47, 0x80000\n\t" \
  "v_smfmac_f32_16x16x64_bf16 v[44:47], v[60:63], v[8:15], v106\n\t" \
  "s_xor_b32 s78, s47, 0xc0000\n\t" \
  "v_smfmac_f32_16x16x64_bf16 v[80:83], v[52:55], a[248:255], v106\n\t" \
  "ds_read_b128 v[0:3], v103 offset:32768\n\t" \
  "v_cvt_pk_bf16_f32 v64, v40, v41\n\t" \
  "v_smfmac_f32_16x16x64_bf16 v[84:87], v[52:55], v[144:151], v106\n\t" \
  "ds_read_b128 v[4:7], v103 offset:33792\n\t" \
  "v_cvt_pk_bf16_f32 v65, v42, v43\n\t" \
  "v_smfmac_f32_16x16x64_bf16 v[80:83], v[60:63], v[128:135], v106\n\t" \
  "ds_read_b128 v[8:11], v103 offset:34816\n\t" \
  "v_cvt_pk_bf16_f32 v66, v44, v45\n\t" \
  "v_smfmac_f32_16x16x64_bf16 v[84:87], v[60:63], v[24:31], v106\n\t" \
  "ds_read_b128 v[12:15], v103 offset:35840\n\t" \
  "v_cvt_pk_bf16_f32 v67, v46, v47\n\t" \
  "v_smfmac_f32_16x16x64_bf16 v[88:91], v[96:99], v[152:159], v106\n\t" \
  "v_permlane16_swap_b32 v64, v66\n\t" \
  "v_cvt_pk_bf16_f32 v68, v80, v81\n\t" \
  "v_smfmac_f32_16x16x64_bf16 v[92:95], v[96:99], v[184:191], v106\n\t" \
  "v_permlane16_swap_b32 v65, v67\n\t" \
  "v_cvt_pk_bf16_f32 v69, v82, v83\n\t" \
  "v_smfmac_f32_16x16x64_bf16 v[88:91], v[52:55], v[160:167], v106\n\t" \
  "v_cvt_pk_bf16_f32 v70, v84, v85\n\t" \
  "v_smfmac_f32_16x16x64_bf16 v[92:95], v[52:55], v[192:199], v106\n\t" \
  "buffer_store_dwordx4 v[64:67], v108, %[ru], s47 offen sc1\n\t" \
  "v_cvt_pk_bf16_f32 v71, v86, v87\n\t" \
  "v_smfmac_f32_16x16x64_bf16 v[88:91], v[56:59], v[168:175], v106\n\t" \
  "ds_read_b128 v[16:19], v103 offset:49152\n\t" \
  "v_permlane16_swap_b32 v68, v70\n\t" \
  "s_waitcnt lgkmcnt(3)\n\t" \
  "v_smfmac_f32_16x16x64_bf16 v[92:95], v[56:59], v[0:7], v106\n\t" \
  "ds_read_b128 v[20:23], v103 offset:50176\n\t" \
  "v_permlane16_swap_b32 v69, v71\n\t" \
  "v_smfmac_f32_16x16x64_bf16 v[88:91], v[60:63], v[176:183], v106\n\t" \
  "ds_read_b128 v[24:27], v103 offset:51200\n\t" \
  "s_waitcnt lgkmcnt(3)\n\t" \
  "v_smfmac_f32_16x16x64_bf16 v[92:95], v[60:63], v[8:15], v106\n\t" \
  "ds_read_b128 v[28:31], v103 offset:52224\n\t" \
  "buffer_store_dwordx4 v[68:71], v108, %[ru], s76 offen sc1\n\t" \
  "v_smfmac_f32_16x16x64_bf16 v[32:35], v[96:99], v[200:207], v106\n\t" \
  "v_cvt_pk_bf16_f32 v72, v88, v89\n\t" \
  "v_smfmac_f32_16x16x64_bf16 v[36:39], v[96:99], v[232:239], v106\n\t" \
  "v_cvt_pk_bf16_f32 v73, v90, v91\n\t" \
  "v_smfmac_f32_16x16x64_bf16 v[32:35], v[52:55], v[208:215], v106\n\t" \
  "v_cvt_pk_bf16_f32 v74, v92, v93\n\t" \
  "v_smfmac_f32_16x16x64_bf16 v[36:39], v[52:55], v[240:247], v106\n\t" \
  "v_cvt_pk_bf16_f32 v75, v94, v95\n\t" \
  "v_smfmac_f32_16x16x64_bf16 v[32:35], v[56:59], v[216:223], v106\n\t" \
  "v_permlane16_swap_b32 v72, v74\n\t" \
  "v_smfmac_f32_16x16x64_bf16 v[32:35], v[60:63], v[224:231], v106\n\t" \
  "v_permlane16_swap_b32 v73, v75\n\t" \
  "s_waitcnt lgkmcnt(2)\n\t" \
  "v_smfmac_f32_16x16x64_bf16 v[36:39], v[56:59], v[16:23], v106\n\t" \
  "s_waitcnt lgkmcnt(0)\n\t" \
  "v_smfmac_f32_16x16x64_bf16 v[36:39], v[60:63], v[24:31], v106\n\t" \
  "buffer_store_dwordx4 v[72:75], v108, %[ru], s77 offen sc1\n\t" \
  "v_cvt_pk_bf16_f32 v76, v32, v33\n\t" \
  "v_cvt_pk_bf16_f32 v77, v34, v35\n\t" \
  "s_nop 5\n\t" \
  "v_cvt_pk_bf16_f32 v78, v36, v37\n\t" \
  "v_cvt_pk_bf16_f32 v79, v38, v39\n\t" \
  "s_nop 0\n\t" \
  "v_permlane16_swap_b32 v76, v78\n\t" \
  "v_permlane16_swap_b32 v77, v79\n\t" \
  "buffer_store_dwordx4 v[76:79], v108, %[ru], s78 offen sc1\n\t" \
  "s_memtime s[76:77]\n\t" \
  "s_mov_b32 s42, 0\n\t" \
  "s_waitcnt lgkmcnt(0)\n\t" \
  "s_mov_b32 s57, s76\n\t" \
  "s_add_i32 s41, s56, 1950\n\t" \
  "s_sub_i32 s76, s92, s51\n\t" \
  "s_sub_i32 s77, s93, s53\n\t" \
  "s_cmp_eq_u32 s76, 0\n\t" \
  "s_cbranch_scc1 21f\n\t" \
  "v_cvt_f32_u32 v117, s76\n\t" \
  "v_cvt_f32_u32 v116, s77\n\t" \
  "v_rcp_f32 v117, v117\n\t" \
  "s_mov_b32 s51, s92\n\t" \
  "v_mul_f32 v117, v117, v116\n\t" \
  "v_max_f32 v117, 0x3f028f5c, v117\n\t" \
  "v_min_f32 v117, 0x3ffeb852, v117\n\t" \
  "v_rcp_f32 v116, v117\n\t" \
  "v_mul_f32 v117, 0x4f000000, v117\n\t" \
  "v_mul_f32 v116, 0x47800000, v116\n\t" \
  "v_cvt_u32_f32 v117, v117\n\t" \
  "v_cvt_u32_f32 v116, v116\n\t" \
  "s_mov_b32 s53, s93\n\t" \
  "v_readfirstlane_b32 s46, v117\n\t" \
  "v_readfirstlane_b32 s52, v116\n\t" \
  "21:\n\t" \
  "s_add_i32 s44, s44, 1\n\t" \
  "s_and_b32 s58, s58, 4\n\t" \
  "s_and_b32 s76, s44, 64\n\t" \
  "s_or_b32 s42, s42, s76\n\t" \
  "s_cbranch_scc0 20b\n\t" \
  "s_branch 99f\n\t" \
  "14:\n\t" \
  "s_sleep 3\n\t" \
  "s_add_u32 s59, s59, 0x10000\n\t" \
  "s_cmp_lt_u32 s59, 0x100000\n\t" \
  "s_cbranch_scc1 13b\n\t" \
  "s_mov_b64 exec, -1\n\t" \
  "s_mov_b32 s42, 16\n\t" \
  "s_branch 99f\n\t" \
  "8:\n\t" \
  "s_bitcmp1_b32 s58, 1\n\t" \
  "s_cbranch_scc1 4b\n\t" \
  "s_bitcmp1_b32 s58, 2\n\t" \
  "s_cbranch_scc1 5b\n\t" \
  "v_max3_u32 v116, v32, v33, v34\n\t" \
  "v_max3_u32 v117, v35, v36, v37\n\t" \
  "v_max3_u32 v116, v116, v38, v39\n\t" \
  "v_max_u32 v116, v116, v117\n\t" \
  "v_cmp_eq_u32 vcc, -1, v116\n\t" \
  "s_cbranch_vccz 5b\n\t" \
  "s_branch 0f\n\t" \
  "9:\n\t" \
  "s_bitcmp1_b32 s58, 2\n\t" \
  "s_cbranch_scc1 6b\n\t" \
  "v_max3_u32 v116, v40, v41, v42\n\t" \
  "v_max3_u32 v117, v43, v44, v45\n\t" \
  "v_max3_u32 v116, v116, v46, v47\n\t" \
  "v_max_u32 v116, v116, v117\n\t" \
  "v_cmp_eq_u32 vcc, -1, v116\n\t" \
  "s_cbranch_vccz 6b\n\t" \
  "s_or_b32 s58, s58, 2\n\t" \
  "0:\n\t" \
  "s_or_b32 s58, s58, 1\n\t" \
  "11:\n\t" \
  "buffer_load_dwordx4 v[32:35], v100, %[rh], s40 offen sc1\n\t" \
  "buffer_load_dwordx4 v[36:39], v100, %[rh], s76 offen sc1\n\t" \
  "buffer_load_dwordx4 v[40:43], v100, %[rh], s77 offen sc1\n\t" \
  "buffer_load_dwordx4 v[44:47], v100, %[rh], s78 offen sc1\n\t" \
  "s_waitcnt lgkmcnt(0)\n\t" \
  "s_branch 10b\n\t" \
  "17:\n\t" \
  "s_add_u32 s59, s59, 1\n\t" \
  "s_bitcmp1_b32 s58, 2\n\t" \
  "s_cbranch_scc1 3b\n\t" \
  "s_waitcnt vmcnt(0)\n\t" \
  "v_max_u32 v116, v32, v33\n\t" \
  "v_max3_u32 v116, v116, v34, v35\n\t" \
  "v_max3_u32 v116, v116, v36, v37\n\t" \
  "v_max3_u32 v116, v116, v38, v39\n\t" \
  "v_max3_u32 v116, v116, v40, v41\n\t" \
  "v_max3_u32 v116, v116, v42, v43\n\t" \
  "v_max3_u32 v116, v116, v44, v45\n\t" \
  "v_max3_u32 v116, v116, v46, v47\n\t" \
  "v_cmp_eq_u32 vcc, -1, v116\n\t" \
  "s_cbranch_vccz 3b\n\t" \
  "s_bitcmp1_b32 s59, 6\n\t" \
  "s_cbranch_scc1 12f\n\t" \
  "buffer_load_dwordx4 v[32:35], v100, %[rh], s40 offen sc1\n\t" \
  "buffer_load_dwordx4 v[36:39], v100, %[rh], s76 offen sc1\n\t" \
  "buffer_load_dwordx4 v[40:43], v100, %[rh], s77 offen sc1\n\t" \
  "buffer_load_dwordx4 v[44:47], v100, %[rh], s78 offen sc1\n\t" \
  "s_branch 17b\n\t" \
  "12:\n\t" \
  "s_mov_b32 s42, s58\n\t" \
  "s_branch 99f\n\t" \
  "93:\n\t" \
  "s_mov_b32 s58, s42\n\t" \
  "20:\n\t" \
  "s_and_b32 s76, s44, 7\n\t" \
  "s_lshl_b32 s76, s76, 15\n\t" \
  "s_add_i32 s40, s54, s76\n\t" \
  "s_and_b32 s76, s44, 3\n\t" \
  "s_lshl_b32 s45, s76, 14\n\t" \
  "s_lshl_b32 s47, s76, 20\n\t" \
  "s_add_i32 s47, s47, s55\n\t" \
  "s_lshr_b32 s76, s47, 21\n\t" \
  "s_bitcmp1_b64 %[sel], s76\n\t" \
  "s_cselect_b32 s76, 0x2000, 0\n\t" \
  "s_add_i32 s47, s47, s76\n\t" \
  "s_add_i32 s49, s44, -1\n\t" \
  "s_and_b32 s48, s49, 2\n\t" \
  "s_lshl_b32 s48, s48, 13\n\t" \
  "s_and_b32 s76, s49, 1\n\t" \
  "s_lshl_b32 s76, s76, 12\n\t" \
  "s_add_i32 s48, s48, s76\n\t" \
  "s_lshl_b32 s50, s53, 8\n\t" \
  "s_mov_b32 s59, 0\n\t" \
  "s_mov_b32 s92, s51\n\t" \
  "s_mov_b32 s93, s53\n\t" \
  "s_xor_b32 s76, s40, 0x400\n\t" \
  "s_xor_b32 s77, s40, 0x800\n\t" \
  "s_xor_b32 s78, s40, 0xc00\n\t" \
  "s_cmp_lg_u32 s58, 0\n\t" \
  "s_cbranch_scc1 11f\n\t" \
  "10:\n\t" \
  "ds_read_b128 v[48:51], v102 offset:49152\n\t" \
  "ds_read_b128 v[52:55], v102 offset:50176\n\t" \
  "ds_read_b128 v[56:59], v102 offset:51200\n\t" \
  "ds_read_b128 v[60:63], v102 offset:52224\n\t" \
  "ds_read_b128 v[0:3], v102 offset:0\n\t" \
  "ds_read_b128 v[4:7], v102 offset:1024\n\t" \
  "ds_read_b128 v[8:11], v102 offset:2048\n\t" \
  "ds_read_b128 v[12:15], v102 offset:3072\n\t" \
  "ds_read_b128 v[16:19], v102 offset:16384\n\t" \
  "ds_read_b128 v[20:23], v102 offset:17408\n\t" \
  "ds_read_b128 v[24:27], v102 offset:18432\n\t" \
  "ds_read_b128 v[28:31], v102 offset:19456\n\t" \
  "s_waitcnt lgkmcnt(4)\n\t" \
  "ds_read_b128 v[80:83], v102 offset:32768\n\t" \
  "ds_read_b128 v[84:87], v102 offset:33792\n\t" \
  "ds_read_b128 v[88:91], v102 offset:34816\n\t" \
  "ds_read_b128 v[92:95], v102 offset:35840\n\t" \
  "s_bitcmp1_b32 s58, 1\n\t" \
  "s_cselect_b64 s[80:81], -1, 0\n\t" \
  "s_cmp_lg_u32 s58, 0\n\t" \
  "s_cbranch_scc1 17f\n\t" \
  "s_mov_b32 exec_hi, 0\n\t" \
  "16:\n\t" \
  "s_memtime vcc\n\t" \
  "s_waitcnt lgkmcnt(0)\n\t" \
  "s_sub_i32 s79, s41, vcc_lo\n\t" \
  "s_cmp_gt_i32 s79, 64\n\t" \
  "s_cbranch_scc1 16b\n\t" \
  "13:\n\t" \
  "buffer_load_dword v116, v112, %[rh], s48 offen sc1\n\t" \
  "s_waitcnt vmcnt(0)\n\t" \
  "15:\n\t" \
  "v_mov_b32 v117, 20000\n\t" \
  "v_cmp_ne_u32_sdwa vcc, v116, s49 src0_sel:BYTE_0 src1_sel:DWORD\n\t" \
  "v_subrev_u32 v116, s50, v116\n\t" \
  "v_ashrrev_i32 v116, 8, v116\n\t" \
  "s_nop 1\n\t" \
  "v_max_i32_dpp v116, v116, v116 quad_perm:[1,0,3,2] row_mask:0xf bank_mask:0xf\n\t" \
  "s_cbranch_vccnz 14f\n\t" \
  "s_nop 0\n\t" \
  "v_max_i32_dpp v116, v116, v116 quad_perm:[2,3,0,1] row_mask:0xf bank_mask:0xf\n\t" \
  "s_nop 1\n\t" \
  "v_max_i32_dpp v116, v116, v116 row_half_mirror row_mask:0xf bank_mask:0xf\n\t" \
  "s_nop 1\n\t" \
  "v_max_i32_dpp v116, v116, v116 row_mirror row_mask:0xf bank_mask:0xf\n\t" \
  "v_med3_i32 v116, v116, 0, v117\n\t" \
  "v_mul_u32_u24 v116, s52, v116\n\t" \
  "v_lshrrev_b32 v116, 16, v116\n\t" \
  "s_mov_b64 exec, -1\n\t" \
  "v_readfirstlane_b32 s84, v116\n\t" \
  "v_readlane_b32 s85, v116, 16\n\t" \
  "s_max_u32 s84, s84, s85\n\t" \
  "s_add_i32 s84, s84, s51\n\t" \
  "1:\n\t" \
  "s_memtime vcc\n\t" \
  "s_waitcnt lgkmcnt(0)\n\t" \
  "s_sub_i32 s79, s84, vcc_lo\n\t" \
  "s_cmp_gt_i32 s79, 128\n\t" \
  "s_cbranch_scc1 1b\n\t" \
  ".p2align 4\n\t" \
  "2:\n\t" \
  "s_sub_i32 s79, s79, 33\n\t" \
  "s_cmp_gt_i32 s79, 56\n\t" \
  "s_cbranch_scc1 2b\n\t" \
  "buffer_load_dwordx4 v[32:35], v100, %[rh], s40 offen sc1\n\t" \
  "s_nop 12\n\t" \
  "buffer_load_dwordx4 v[36:39], v100, %[rh], s76 offen sc1\n\t" \
  "s_nop 12\n\t" \
  "buffer_load_dwordx4 v[40:43], v100, %[rh], s77 offen sc1\n\t" \
  "s_nop 12\n\t" \
  "buffer_load_dwordx4 v[44:47], v100, %[rh], s78 offen sc1\n\t" \
  "s_memtime vcc\n\t" \
  "s_memrealtime s[82:83]\n\t" \
  "s_nop 4\n\t" \
  "s_memrealtime s[84:85]\n\t" \
  "s_nop 4\n\t" \
  "s_memrealtime s[56:57]\n\t" \
  "s_nop 4\n\t" \
  "s_memrealtime s[86:87]\n\t" \
  "s_nop 4\n\t" \
  "s_memrealtime s[88:89]\n\t" \
  "s_nop 4\n\t" \
  "s_memrealtime s[90:91]\n\t" \
  "s_mov_b32 s92, 0\n\t" \
  "s_waitcnt lgkmcnt(0)\n\t" \
  "s_cmp_eq_u32 s82, s84\n\t" \
  "s_addc_u32 s92, s92, 0\n\t" \
  "s_cmp_eq_u32 s82, s56\n\t" \
  "s_addc_u32 s92, s92, 0\n\t" \
  "s_cmp_eq_u32 s82, s86\n\t" \
  "s_addc_u32 s92, s92, 0\n\t" \
  "s_cmp_eq_u32 s82, s88\n\t" \
  "s_addc_u32 s92, s92, 0\n\t" \
  "s_cmp_eq_u32 s82, s90\n\t" \
  "s_addc_u32 s92, s92, 0\n\t" \
  "s_mul_i32 s92, s92, 24\n\t" \
  "s_add_i32 s92, s92, vcc_lo\n\t" \
  "s_lshl4_add_u32 s93, s82, 64\n\t" \
  "3:\n\t" \
  "v_mov_b64 v[72:73], 0\n\t" \
  "v_mov_b64 v[74:75], 0\n\t" \
  "v_mov_b64 v[76:77], 0\n\t" \
  "v_mov_b64 v[78:79], 0\n\t" \
  "s_waitcnt lgkmcnt(0)\n\t" \
  "s_waitcnt vmcnt(3)\n\t" \
  "v_smfmac_f32_16x16x64_bf16 v[72:75], v[32:35], v[48:55], v104\n\t" \
  "ds_write_b128 v101, v[32:35] offset:12288\n\t" \
  "v_smfmac_f32_16x16x64_bf16 v[76:79], v[32:35], v[56:63], v104\n\t" \
  "s_waitcnt vmcnt(2)\n\t" \
  "ds_write_b128 v101, v[36:39] offset:13312\n\t" \
  "v_smfmac_f32_16x16x64_bf16 v[72:75], v[36:39], a[144:151], v104\n\t" \
  "v_smfmac_f32_16x16x64_bf16 v[76:79], v[36:39], a[152:159], v104\n\t" \
  "s_nop 3\n\t" \
  "v_cmp_u_f32 vcc, v72, v73\n\t" \
  "v_cmp_u_f32 s[82:83], v74, v75\n\t" \
  "s_waitcnt vmcnt(1)\n\t" \
  "v_smfmac_f32_16x16x64_bf16 v[72:75], v[40:43], a[160:167], v104\n\t" \
  "s_or_b64 vcc, vcc, s[82:83]\n\t" \
  "s_or_b64 vcc, vcc, s[80:81]\n\t" \
  "v_smfmac_f32_16x16x64_bf16 v[76:79], v[40:43], a[168:175], v104\n\t" \
  "s_waitcnt lgkmcnt(0)\n\t" \
  "s_cbranch_scc1 8f\n\t" \
  "5:\n\t" \
  "s_barrier\n\t" \
  "4:\n\t" \
  "s_memtime s[56:57]\n\t" \
  "ds_read_b128 v[48:51], v101 offset:0\n\t" \
  "ds_read_b128 v[52:55], v101 offset:1024\n\t" \
  "ds_read_b128 v[56:59], v101 offset:5120\n\t" \
  "s_waitcnt vmcnt(0)\n\t" \
  "v_smfmac_f32_16x16x64_bf16 v[72:75], v[44:47], a[176:183], v104\n\t" \
  "ds_read_b128 v[60:63], v101 offset:4096\n\t" \
  "ds_read_b128 v[64:67], v101 offset:9216\n\t" \
  "v_smfmac_f32_16x16x64_bf16 v[76:79], v[44:47], a[184:191], v104\n\t" \
  "ds_read_b128 v[68:71], v101 offset:8192\n\t" \
  "ds_write_b128 v101, v[40:43] offset:14336\n\t" \
  "s_nop 1\n\t" \
  "v_cmp_u_f32 vcc, v72, v73\n\t" \
  "v_cmp_u_f32 s[82:83], v74, v75\n\t" \
  "s_waitcnt lgkmcnt(6)\n\t" \
  "v_smfmac_f32_16x16x64_bf16 v[72:75], v[48:51], v[0:7], v104\n\t" \
  "s_or_b64 vcc, vcc, s[82:83]\n\t" \
  "v_smfmac_f32_16x16x64_bf16 v[76:79], v[48:51], v[8:15], v104\n\t" \
  "s_cbranch_scc1 9f\n\t" \
  "6:\n\t" \
  "s_waitcnt lgkmcnt(5)\n\t" \
  "v_smfmac_f32_16x16x64_bf16 v[72:75], v[52:55], a[0:7], v104\n\t" \
  "ds_write_b128 v101, v[44:47] offset:15360\n\t" \
  "v_smfmac_f32_16x16x64_bf16 v[76:79], v[52:55], a[8:15], v104\n\t" \
  "v_mov_b64 v[40:41], 0\n\t" \
  "s_waitcnt lgkmcnt(5)\n\t" \
  "v_smfmac_f32_16x16x64_bf16 v[72:75], v[56:59], a[48:55], v104\n\t" \
  "v_mov_b64 v[42:43], 0\n\t" \
  "v_smfmac_f32_16x16x64_bf16 v[76:79], v[56:59], a[56:63], v104\n\t" \
  "s_waitcnt lgkmcnt(4)\n\t" \
  "v_smfmac_f32_16x16x64_bf16 v[72:75], v[60:63], v[16:23], v104\n\t" \
  "s_waitcnt lgkmcnt(0)\n\t" \
  "s_barrier\n\t" \
  "v_smfmac_f32_16x16x64_bf16 v[76:79], v[60:63], v[24:31], v104\n\t" \
  "ds_read_b128 v[48:51], v101 offset:2048\n\t" \
  "ds_read_b128 v[52:55], v101 offset:6144\n\t" \
  "v_smfmac_f32_16x16x64_bf16 v[72:75], v[64:67], a[96:103], v104\n\t" \
  "ds_read_b128 v[56:59], v101 offset:10240\n\t" \
  "ds_read_b128 v[60:63], v101 offset:3072\n\t" \
  "v_smfmac_f32_16x16x64_bf16 v[76:79], v[64:67], a[104:111], v104\n\t" \
  "ds_read_b128 v[32:35], v101 offset:7168\n\t" \
  "ds_read_b128 v[36:39], v101 offset:11264\n\t" \
  "v_smfmac_f32_16x16x64_bf16 v[72:75], v[68:71], v[80:87], v104\n\t" \
  "ds_read_b128 v[0:3], v103 offset:0\n\t" \
  "v_mov_b64 v[44:45], 0\n\t" \
  "v_smfmac_f32_16x16x64_bf16 v[76:79], v[68:71], v[88:95], v104\n\t" \
  "v_mov_b64 v[46:47], 0\n\t" \
  "s_waitcnt lgkmcnt(5)\n\t" \
  "v_smfmac_f32_16x16x64_bf16 v[72:75], v[48:51], a[16:23], v104\n\t" \
  "ds_read_b128 v[4:7], v103 offset:1024\n\t" \
  "v_mov_b64 v[80:81], 0\n\t" \
  "v_smfmac_f32_16x16x64_bf16 v[72:75], v[52:55], a[64:71], v104\n\t" \
  "v_mov_b64 v[82:83], 0\n\t" \
  "s_waitcnt lgkmcnt(2)\n\t" \
  "v_smfmac_f32_16x16x64_bf16 v[72:75], v[56:59], a[112:119], v104\n\t" \
  "ds_read_b128 v[8:11], v103 offset:2048\n\t" \
  "v_mov_b64 v[84:85], 0\n\t" \
  "v_smfmac_f32_16x16x64_bf16 v[72:75], v[60:63], a[32:39], v104\n\t" \
  "ds_read_b128 v[12:15], v103 offset:3072\n\t" \
  "v_mov_b64 v[86:87], 0\n\t" \
  "v_smfmac_f32_16x16x64_bf16 v[72:75], v[32:35], a[80:87], v104\n\t" \
  "ds_read_b128 v[16:19], v103 offset:16384\n\t" \
  "v_mov_b64 v[88:89], 0\n\t" \
  "v_smfmac_f32_16x16x64_bf16 v[72:75], v[36:39], a[128:135], v104\n\t" \
  "ds_read_b128 v[20:23], v103 offset:17408\n\t" \
  "v_mov_b64 v[90:91], 0\n\t" \
  "v_smfmac_f32_16x16x64_bf16 v[76:79], v[48:51], a[24:31], v104\n\t" \
  "ds_read_b128 v[24:27], v103 offset:18432\n\t" \
  "v_mov_b64 v[92:93], 0\n\t" \
  "v_smfmac_f32_16x16x64_bf16 v[76:79], v[52:55], a[72:79], v104\n\t" \
  "v_add_f32 v96, v72, v74\n\t" \
  "v_add_f32 v97, v73, v75\n\t" \
  "v_cvt_pk_bf16_f32 v96, v96, v97\n\t" \
  "ds_write_b32 v105, v96\n\t" \
  "v_smfmac_f32_16x16x64_bf16 v[76:79], v[56:59], a[120:127], v104\n\t" \
  "ds_read_b128 v[28:31], v103 offset:19456\n\t" \
  "v_mov_b64 v[94:95], 0\n\t" \
  "v_smfmac_f32_16x16x64_bf16 v[76:79], v[60:63], a[40:47], v104\n\t" \
  "v_smfmac_f32_16x16x64_bf16 v[76:79], v[32:35], a[88:95], v104\n\t" \
  "v_smfmac_f32_16x16x64_bf16 v[76:79], v[36:39], a[136:143], v104\n\t" \
  "s_waitcnt lgkmcnt(1)\n\t" \
  "s_barrier\n\t" \
  "ds_read_b64_tr_b16 v[96:97], v107 offset:0\n\t" \
  "ds_read_b64_tr_b16 v[98:99], v107 offset:64\n\t" \
  "ds_read_b64_tr_b16 v[56:57], v107 offset:1024\n\t" \
  "ds_read_b64_tr_b16 v[58:59], v107 offset:1088\n\t" \
  "s_nop 1\n\t" \
  "v_add_f32 v116, v76, v78\n\t" \
  "v_add_f32 v117, v77, v79\n\t" \
  "v_cvt_pk_bf16_f32 v116, v116, v117\n\t" \
  "ds_write_b32 v105, v116 offset:256\n\t" \
  "s_waitcnt lgkmcnt(3)\n\t" \
  "v_smfmac_f32_16x16x64_bf16 v[40:43], v[96:99], a[192:199], v106\n\t" \
  "v_smfmac_f32_16x16x64_bf16 v[44:47], v[96:99], a[224:231], v106\n\t" \
  "s_waitcnt lgkmcnt(0)\n\t" \
  "s_barrier\n\t" \
  "ds_read_b64_tr_b16 v[52:53], v107 offset:256\n\t" \
  "ds_read_b64_tr_b16 v[54:55], v107 offset:320\n\t" \
  "v_smfmac_f32_16x16x64_bf16 v[40:43], v[56:59], a[208:215], v106\n\t" \
  "ds_read_b64_tr_b16 v[60:61], v107 offset:1280\n\t" \
  "ds_read_b64_tr_b16 v[62:63], v107 offset:1344\n\t" \
  "v_smfmac_f32_16x16x64_bf16 v[44:47], v[56:59], v[0:7], v106\n\t" \
  "v_smfmac_f32_16x16x64_bf16 v[80:83], v[96:99], a[240:247], v106\n\t" \
  "v_mov_b64 v[32:33], 0\n\t" \
  "v_smfmac_f32_16x16x64_bf16 v[84:87], v[96:99], v[136:143], v106\n\t" \
  "v_mov_b64 v[34:35], 0\n\t" \
  "v_smfmac_f32_16x16x64_bf16 v[80:83], v[56:59], v[120:127], v106\n\t" \
  "v_mov_b64 v[36:37], 0\n\t" \
  "v_smfmac_f32_16x16x64_bf16 v[84:87], v[56:59], v[16:23], v106\n\t" \
  "s_waitcnt lgkmcnt(2)\n\t" \
  "v_smfmac_f32_16x16x64_bf16 v[40:43], v[52:55], a[200:207], v106\n\t" \
  "v_smfmac_f32_16x16x64_bf16 v[44:47], v[52:55], a[232:239], v106\n\t" \
  "s_xor_b32 s76, s47, 0x40000\n\t" \
  "s_waitcnt lgkmcnt(0)\n\t" \
  "v_smfmac_f32_16x16x64_bf16 v[40:43], v[60:63], a[216:223], v106\n\t" \
  "v_mov_b64 v[38:39], 0\n\t" \
  "s_xor_b32 s77, s47, 0x80000\n\t" \
  "v_smfmac_f32_16x16x64_bf16 v[44:47], v[60:63], v[8:15], v106\n\t" \
  "s_xor_b32 s78, s47, 0xc0000\n\t" \
  "v_smfmac_f32_16x16x64_bf16 v[80:83], v[52:55], a[248:255], v106\n\t" \
  "ds_read_b128 v[0:3], v103 offset:32768\n\t" \
  "v_cvt_pk_bf16_f32 v64, v40, v41\n\t" \
  "v_smfmac_f32_16x16x64_bf16 v[84:87], v[52:55], v[144:151], v106\n\t" \
  "ds_read_b128 v[4:7], v103 offset:33792\n\t" \
  "v_cvt_pk_bf16_f32 v65, v42, v43\n\t" \
  "v_smfmac_f32_16x16x64_bf16 v[80:83], v[60:63], v[128:135], v106\n\t" \
  "ds_read_b128 v[8:11], v103 offset:34816\n\t" \
  "v_cvt_pk_bf16_f32 v66, v44, v45\n\t" \
  "v_smfmac_f32_16x16x64_bf16 v[84:87], v[60:63], v[24:31], v106\n\t" \
  "ds_read_b128 v[12:15], v103 offset:35840\n\t" \
  "v_cvt_pk_bf16_f32 v67, v46, v47\n\t" \
  "v_smfmac_f32_16x16x64_bf16 v[88:91], v[96:99], v[152:159], v106\n\t" \
  "v_permlane16_swap_b32 v64, v66\n\t" \
  "v_cvt_pk_bf16_f32 v68, v80, v81\n\t" \
  "v_smfmac_f32_16x16x64_bf16 v[92:95], v[96:99], v[184:191], v106\n\t" \
  "v_permlane16_swap_b32 v65, v67\n\t" \
  "v_cvt_pk_bf16_f32 v69, v82, v83\n\t" \
  "v_smfmac_f32_16x16x64_bf16 v[88:91], v[52:55], v[160:167], v106\n\t" \
  "v_cvt_pk_bf16_f32 v70, v84, v85\n\t" \
  "v_smfmac_f32_16x16x64_bf16 v[92:95], v[52:55], v[192:199], v106\n\t" \
  "v_cvt_pk_bf16_f32 v71, v86, v87\n\t" \
  "v_smfmac_f32_16x16x64_bf16 v[88:91], v[56:59], v[168:175], v106\n\t" \
  "ds_read_b128 v[16:19], v103 offset:49152\n\t" \
  "buffer_store_dwordx4 v[64:67], v108, %[ru], s47 offen sc1\n\t" \
  "v_permlane16_swap_b32 v68, v70\n\t" \
  "s_waitcnt lgkmcnt(3)\n\t" \
  "v_smfmac_f32_16x16x64_bf16 v[92:95], v[56:59], v[0:7], v106\n\t" \
  "ds_read_b128 v[20:23], v103 offset:50176\n\t" \
  "v_permlane16_swap_b32 v69, v71\n\t" \
  "v_smfmac_f32_16x16x64_bf16 v[88:91], v[60:63], v[176:183], v106\n\t" \
  "ds_read_b128 v[24:27], v103 offset:51200\n\t" \
  "s_waitcnt lgkmcnt(3)\n\t" \
  "v_smfmac_f32_16x16x64_bf16 v[92:95], v[60:63], v[8:15], v106\n\t" \
  "ds_read_b128 v[28:31], v103 offset:52224\n\t" \
  "v_smfmac_f32_16x16x64_bf16 v[32:35], v[96:99], v[200:207], v106\n\t" \
  "buffer_store_dwordx4 v[68:71], v108, %[ru], s76 offen sc1\n\t" \
  "v_cvt_pk_bf16_f32 v72, v88, v89\n\t" \
  "v_smfmac_f32_16x16x64_bf16 v[36:39], v[96:99], v[232:239], v106\n\t" \
  "v_cvt_pk_bf16_f32 v73, v90, v91\n\t" \
  "v_smfmac_f32_16x16x64_bf16 v[32:35], v[52:55], v[208:215], v106\n\t" \
  "v_cvt_pk_bf16_f32 v74, v92, v93\n\t" \
  "v_smfmac_f32_16x16x64_bf16 v[36:39], v[52:55], v[240:247], v106\n\t" \
  "v_cvt_pk_bf16_f32 v75, v94, v95\n\t" \
  "v_smfmac_f32_16x16x64_bf16 v[32:35], v[56:59], v[216:223], v106\n\t" \
  "v_permlane16_swap_b32 v72, v74\n\t" \
  "v_smfmac_f32_16x16x64_bf16 v[32:35], v[60:63], v[224:231], v106\n\t" \
  "v_permlane16_swap_b32 v73, v75\n\t" \
  "s_waitcnt lgkmcnt(2)\n\t" \
  "v_smfmac_f32_16x16x64_bf16 v[36:39], v[56:59], v[16:23], v106\n\t" \
  "s_waitcnt lgkmcnt(0)\n\t" \
  "v_smfmac_f32_16x16x64_bf16 v[36:39], v[60:63], v[24:31], v106\n\t" \
  "buffer_store_dwordx4 v[72:75], v108, %[ru], s77 offen sc1\n\t" \
  "v_cvt_pk_bf16_f32 v76, v32, v33\n\t" \
  "v_cvt_pk_bf16_f32 v77, v34, v35\n\t" \
  "s_nop 5\n\t" \
  "v_cvt_pk_bf16_f32 v78, v36, v37\n\t" \
  "v_cvt_pk_bf16_f32 v79, v38, v39\n\t" \
  "s_nop 0\n\t" \
  "v_permlane16_swap_b32 v76, v78\n\t" \
  "v_permlane16_swap_b32 v77, v79\n\t" \
  "buffer_store_dwordx4 v[76:79], v108, %[ru], s78 offen sc1\n\t" \
  "s_memtime s[76:77]\n\t" \
  "s_mov_b32 s42, 0\n\t" \
  "s_waitcnt lgkmcnt(0)\n\t" \
  "s_mov_b32 s57, s76\n\t" \
  "s_add_i32 s41, s56, 1950\n\t" \
  "s_sub_i32 s76, s92, s51\n\t" \
  "s_sub_i32 s77, s93, s53\n\t" \
  "s_cmp_eq_u32 s76, 0\n\t" \
  "s_cbranch_scc1 21f\n\t" \
  "v_cvt_f32_u32 v117, s76\n\t" \
  "v_cvt_f32_u32 v116, s77\n\t" \
  "v_rcp_f32 v117, v117\n\t" \
  "s_mov_b32 s51, s92\n\t" \
  "v_mul_f32 v117, v117, v116\n\t" \
  "v_max_f32 v117, 0x3f028f5c, v117\n\t" \
  "v_min_f32 v117, 0x3ffeb852, v117\n\t" \
  "v_rcp_f32 v116, v117\n\t" \
  "v_mul_f32 v117, 0x4f000000, v117\n\t" \
  "v_mul_f32 v116, 0x47800000, v116\n\t" \
  "v_cvt_u32_f32 v117, v117\n\t" \
  "v_cvt_u32_f32 v116, v116\n\t" \
  "s_mov_b32 s53, s93\n\t" \
  "v_readfirstlane_b32 s46, v117\n\t" \
  "v_readfirstlane_b32 s52, v116\n\t" \
  "21:\n\t" \
  "s_add_i32 s44, s44, 1\n\t" \
  "s_and_b32 s58, s58, 4\n\t" \
  "s_and_b32 s76, s44, 64\n\t" \
  "s_or_b32 s42, s42, s76\n\t" \
  "s_cbranch_scc0 20b\n\t" \
  "s_branch 99f\n\t" \
  "14:\n\t" \
  "s_sleep 3\n\t" \
  "s_add_u32 s59, s59, 0x10000\n\t" \
  "s_cmp_lt_u32 s59, 0x100000\n\t" \
  "s_cbranch_scc1 13b\n\t" \
  "s_mov_b64 exec, -1\n\t" \
  "s_mov_b32 s42, 16\n\t" \
  "s_branch 99f\n\t" \
  "8:\n\t" \
  "s_bitcmp1_b32 s58, 1\n\t" \
  "s_cbranch_scc1 4b\n\t" \
  "s_bitcmp1_b32 s58, 2\n\t" \
  "s_cbranch_scc1 5b\n\t" \
  "v_max3_u32 v116, v32, v33, v34\n\t" \
  "v_max3_u32 v117, v35, v36, v37\n\t" \
  "v_max3_u32 v116, v116, v38, v39\n\t" \
  "v_max_u32 v116, v116, v117\n\t" \
  "v_cmp_eq_u32 vcc, -1, v116\n\t" \
  "s_cbranch_vccz 5b\n\t" \
  "s_branch 0f\n\t" \
  "9:\n\t" \
  "s_bitcmp1_b32 s58, 2\n\t" \
  "s_cbranch_scc1 6b\n\t" \
  "v_max3_u32 v116, v40, v41, v42\n\t" \
  "v_max3_u32 v117, v43, v44, v45\n\t" \
  "v_max3_u32 v116, v116, v46, v47\n\t" \
  "v_max_u32 v116, v116, v117\n\t" \
  "v_cmp_eq_u32 vcc, -1, v116\n\t" \
  "s_cbranch_vccz 6b\n\t" \
  "s_or_b32 s58, s58, 2\n\t" \
  "0:\n\t" \
  "s_or_b32 s58, s58, 1\n\t" \
  "11:\n\t" \
  "buffer_load_dwordx4 v[32:35], v100, %[rh], s40 offen sc1\n\t" \
  "buffer_load_dwordx4 v[36:39], v100, %[rh], s76 offen sc1\n\t" \
  "buffer_load_dwordx4 v[40:43], v100, %[rh], s77 offen sc1\n\t" \
  "buffer_load_dwordx4 v[44:47], v100, %[rh], s78 offen sc1\n\t" \
  "s_waitcnt lgkmcnt(0)\n\t" \
  "s_branch 10b\n\t" \
  "17:\n\t" \
  "s_add_u32 s59, s59, 1\n\t" \
  "s_bitcmp1_b32 s58, 2\n\t" \
  "s_cbranch_scc1 3b\n\t" \
  "s_waitcnt vmcnt(0)\n\t" \
  "v_max_u32 v116, v32, v33\n\t" \
  "v_max3_u32 v116, v116, v34, v35\n\t" \
  "v_max3_u32 v116, v116, v36, v37\n\t" \
  "v_max3_u32 v116, v116, v38, v39\n\t" \
  "v_max3_u32 v116, v116, v40, v41\n\t" \
  "v_max3_u32 v116, v116, v42, v43\n\t" \
  "v_max3_u32 v116, v116, v44, v45\n\t" \
  "v_max3_u32 v116, v116, v46, v47\n\t" \
  "v_cmp_eq_u32 vcc, -1, v116\n\t" \
  "s_cbranch_vccz 3b\n\t" \
  "s_bitcmp1_b32 s59, 6\n\t" \
  "s_cbranch_scc1 12f\n\t" \
  "buffer_load_dwordx4 v[32:35], v100, %[rh], s40 offen sc1\n\t" \
  "buffer_load_dwordx4 v[36:39], v100, %[rh], s76 offen sc1\n\t" \
  "buffer_load_dwordx4 v[40:43], v100, %[rh], s77 offen sc1\n\t" \
  "buffer_load_dwordx4 v[44:47], v100, %[rh], s78 offen sc1\n\t" \
  "s_branch 17b\n\t" \
  "12:\n\t" \
  "s_mov_b32 s42, s58\n\t" \
  "s_branch 99f\n\t" \
  "99:\n\t" \
  "s_waitcnt vmcnt(0) lgkmcnt(0)\n\t" \
  ".Lchain_last:\n\t" \
  ""
#define CB_BODY \
  "s_cmp_gt_u32 %[wv], 1\n\t" \
  "s_cbranch_scc1 91f\n\t" \
  "s_mov_b32 s58, s42\n\t" \
  "s_mov_b32 s59, 0\n\t" \
  "s_bitcmp1_b32 s58, 4\n\t" \
  "s_cbranch_scc1 26f\n\t" \
  "s_bitcmp1_b32 s58, 3\n\t" \
  "s_cbranch_scc1 34f\n\t" \
  "s_bitcmp1_b32 s58, 1\n\t" \
  "s_cbranch_scc1 18f\n\t" \
  "17:\n\t" \
  "19:\n\t" \
  "s_add_i32 s85, s40, -1\n\t" \
  "s_and_b32 s82, s85, 2\n\t" \
  "s_lshl_b32 s82, s82, 13\n\t" \
  "s_and_b32 s80, s85, 1\n\t" \
  "s_lshl_b32 s80, s80, 12\n\t" \
  "s_add_i32 s82, s82, s80\n\t" \
  "s_add_i32 s82, s82, 0x84000\n\t" \
  "s_mov_b32 exec_hi, 0\n\t" \
  "buffer_load_dword v184, v200, %[rh], s82 offen sc1\n\t" \
  "buffer_load_dword v186, v203, %[ru], 0 offen sc1\n\t" \
  "s_mov_b64 exec, -1\n\t" \
  "20:\n\t" \
  "s_and_b32 s80, s40, 3\n\t" \
  "s_mul_i32 s77, s80, 0xc000\n\t" \
  "s_add_i32 s77, s77, 0x40000\n\t" \
  "s_lshl_b32 s78, s80, 14\n\t" \
  "s_add_i32 s78, s78, 0x70000\n\t" \
  "s_mov_b32 s57, 0\n\t" \
  "s_add_i32 s96, s77, 0x1000\n\t" \
  "s_lshl_b32 s88, s80, 20\n\t" \
  "s_add_i32 s88, s88, s48\n\t" \
  "s_lshr_b32 s80, s88, 21\n\t" \
  "s_bitcmp1_b64 %[sel], s80\n\t" \
  "s_cselect_b32 s80, 0x2000, 0\n\t" \
  "s_add_i32 s88, s88, s80\n\t" \
  "s_xor_b32 s89, s88, 0x800\n\t" \
  "s_xor_b32 s90, s88, 0x1000\n\t" \
  "s_xor_b32 s91, s88, 0x1800\n\t" \
  "s_add_i32 s92, s77, s50\n\t" \
  "s_xor_b32 s93, s92, 0x100\n\t" \
  "s_xor_b32 s94, s92, 0x200\n\t" \
  "s_xor_b32 s95, s92, 0x300\n\t" \
  "s_add_i32 s97, s40, 1\n\t" \
  "s_and_b32 s97, s97, 7\n\t" \
  "s_lshl_b32 s97, s97, 15\n\t" \
  "s_add_i32 s97, s97, s49\n\t" \
  "s_memtime vcc\n\t" \
  "s_memrealtime s[86:87]\n\t" \
  "s_nop 4\n\t" \
  "s_memrealtime s[60:61]\n\t" \
  "s_nop 4\n\t" \
  "s_memrealtime s[62:63]\n\t" \
  "s_nop 4\n\t" \
  "s_memrealtime s[64:65]\n\t" \
  "s_nop 4\n\t" \
  "s_memrealtime s[66:67]\n\t" \
  "s_nop 4\n\t" \
  "s_memrealtime s[68:69]\n\t" \
  "s_mov_b32 s79, 0\n\t" \
  "s_waitcnt lgkmcnt(0)\n\t" \
  "s_cmp_eq_u32 s86, s60\n\t" \
  "s_addc_u32 s79, s79, 0\n\t" \
  "s_cmp_eq_u32 s86, s62\n\t" \
  "s_addc_u32 s79, s79, 0\n\t" \
  "s_cmp_eq_u32 s86, s64\n\t" \
  "s_addc_u32 s79, s79, 0\n\t" \
  "s_cmp_eq_u32 s86, s66\n\t" \
  "s_addc_u32 s79, s79, 0\n\t" \
  "s_cmp_eq_u32 s86, s68\n\t" \
  "s_addc_u32 s79, s79, 0\n\t" \
  "s_mul_i32 s79, s79, 24\n\t" \
  "s_add_i32 s79, s79, vcc_lo\n\t" \
  "s_lshl4_add_u32 s80, s86, 64\n\t" \
  "s_sub_i32 s86, s79, s41\n\t" \
  "s_sub_i32 s87, s80, s43\n\t" \
  "s_cmp_lt_u32 s86, 4000\n\t" \
  "s_cbranch_scc1 21f\n\t" \
  "v_cvt_f32_u32 v165, s86\n\t" \
  "v_cvt_f32_u32 v164, s87\n\t" \
  "v_rcp_f32 v165, v165\n\t" \
  "s_nop 0\n\t" \
  "v_mul_f32 v165, v165, v164\n\t" \
  "v_max_f32 v165, 0x3f028f5c, v165\n\t" \
  "v_min_f32 v165, 0x3ffeb852, v165\n\t" \
  "v_rcp_f32 v164, v165\n\t" \
  "v_mul_f32 v165, 0x4f000000, v165\n\t" \
  "v_mul_f32 v164, 0x47800000, v164\n\t" \
  "v_cvt_u32_f32 v165, v165\n\t" \
  "v_cvt_u32_f32 v164, v164\n\t" \
  "s_nop 0\n\t" \
  "v_readfirstlane_b32 s45, v165\n\t" \
  "v_readfirstlane_b32 s44, v164\n\t" \
  "21:\n\t" \
  "s_mov_b32 s41, s79\n\t" \
  "s_mov_b32 s43, s80\n\t" \
  "s_lshl_b32 s83, s43, 8\n\t" \
  "s_mov_b32 s84, 650\n\t" \
  "s_mov_b32 exec_hi, 0\n\t" \
  "s_waitcnt vmcnt(0)\n\t" \
  "v_cmp_ne_u32_sdwa vcc, v184, s85 src0_sel:BYTE_0 src1_sel:DWORD\n\t" \
  "v_subrev_u32 v184, s83, v184\n\t" \
  "v_ashrrev_i32 v184, 8, v184\n\t" \
  "s_cbranch_vccnz 22f\n\t" \
  "s_nop 0\n\t" \
  "v_max_i32_dpp v184, v184, v184 quad_perm:[1,0,3,2] row_mask:0xf bank_mask:0xf\n\t" \
  "s_nop 1\n\t" \
  "v_max_i32_dpp v184, v184, v184 quad_perm:[2,3,0,1] row_mask:0xf bank_mask:0xf\n\t" \
  "s_nop 1\n\t" \
  "v_max_i32_dpp v184, v184, v184 row_half_mirror row_mask:0xf bank_mask:0xf\n\t" \
  "s_nop 1\n\t" \
  "v_max_i32_dpp v184, v184, v184 row_mirror row_mask:0xf bank_mask:0xf\n\t" \
  "s_nop 0\n\t" \
  "v_readlane_b32 s81, v184, 0\n\t" \
  "v_readlane_b32 s82, v184, 16\n\t" \
  "s_max_i32 s81, s81, s82\n\t" \
  "s_add_i32 s81, s81, 1056\n\t" \
  "s_max_i32 s81, s81, 0\n\t" \
  "s_min_i32 s81, s81, 20000\n\t" \
  "s_mul_i32 s81, s81, s44\n\t" \
  "s_lshr_b32 s81, s81, 16\n\t" \
  "24:\n\t" \
  "s_add_i32 s81, s81, s41\n\t" \
  "1:\n\t" \
  "s_memtime vcc\n\t" \
  "s_waitcnt lgkmcnt(0)\n\t" \
  "s_sub_i32 s79, s81, vcc_lo\n\t" \
  "s_cmp_gt_i32 s79, 128\n\t" \
  "s_cbranch_scc1 1b\n\t" \
  ".p2align 4\n\t" \
  "2:\n\t" \
  "s_sub_i32 s79, s79, 33\n\t" \
  "s_cmp_gt_i32 s79, 56\n\t" \
  "s_cbranch_scc1 2b\n\t" \
  "buffer_load_dword v187, v200, %[rh], s78 offen sc1\n\t" \
  "s_memtime vcc\n\t" \
  "s_memrealtime s[86:87]\n\t" \
  "s_nop 4\n\t" \
  "s_memrealtime s[60:61]\n\t" \
  "s_nop 4\n\t" \
  "s_memrealtime s[62:63]\n\t" \
  "s_nop 4\n\t" \
  "s_memrealtime s[64:65]\n\t" \
  "s_nop 4\n\t" \
  "s_memrealtime s[66:67]\n\t" \
  "s_nop 4\n\t" \
  "s_memrealtime s[68:69]\n\t" \
  "s_mov_b32 s79, 0\n\t" \
  "s_waitcnt lgkmcnt(0)\n\t" \
  "s_cmp_eq_u32 s86, s60\n\t" \
  "s_addc_u32 s79, s79, 0\n\t" \
  "s_cmp_eq_u32 s86, s62\n\t" \
  "s_addc_u32 s79, s79, 0\n\t" \
  "s_cmp_eq_u32 s86, s64\n\t" \
  "s_addc_u32 s79, s79, 0\n\t" \
  "s_cmp_eq_u32 s86, s66\n\t" \
  "s_addc_u32 s79, s79, 0\n\t" \
  "s_cmp_eq_u32 s86, s68\n\t" \
  "s_addc_u32 s79, s79, 0\n\t" \
  "s_mul_i32 s79, s79, 24\n\t" \
  "s_add_i32 s79, s79, vcc_lo\n\t" \
  "s_lshl4_add_u32 s80, s86, 64\n\t" \
  "s_mov_b32 s41, s79\n\t" \
  "s_mov_b32 s43, s80\n\t" \
  "s_lshl_b32 s83, s43, 8\n\t" \
  "13:\n\t" \
  "s_waitcnt vmcnt(0)\n\t" \
  "s_memtime s[86:87]\n\t" \
  "v_cmp_ne_u32_sdwa vcc, v187, s40 src0_sel:BYTE_0 src1_sel:DWORD\n\t" \
  "v_subrev_u32 v184, s83, v187\n\t" \
  "v_ashrrev_i32 v184, 8, v184\n\t" \
  "s_nop 1\n\t" \
  "v_max_i32_dpp v184, v184, v184 quad_perm:[1,0,3,2] row_mask:0xf bank_mask:0xf\n\t" \
  "s_nop 1\n\t" \
  "v_max_i32_dpp v184, v184, v184 quad_perm:[2,3,0,1] row_mask:0xf bank_mask:0xf\n\t" \
  "s_cbranch_vccnz 14f\n\t" \
  "15:\n\t" \
  "s_nop 0\n\t" \
  "v_max_i32_dpp v184, v184, v184 row_half_mirror row_mask:0xf bank_mask:0xf\n\t" \
  "s_nop 1\n\t" \
  "v_max_i32_dpp v184, v184, v184 row_mirror row_mask:0xf bank_mask:0xf\n\t" \
  "s_mov_b64 exec, -1\n\t" \
  "v_readlane_b32 s57, v184, 0\n\t" \
  "v_readlane_b32 s82, v184, 16\n\t" \
  "s_waitcnt lgkmcnt(0)\n\t" \
  "s_sub_i32 s79, s41, s86\n\t" \
  "s_max_i32 s57, s57, s82\n\t" \
  "s_add_i32 s81, s57, 200\n\t" \
  "s_max_i32 s81, s81, 0\n\t" \
  "s_min_i32 s81, s81, 20000\n\t" \
  "s_mul_i32 s81, s81, s44\n\t" \
  "s_lshr_b32 s81, s81, 16\n\t" \
  "s_add_i32 s79, s79, s81\n\t" \
  "s_add_i32 s79, s79, -370\n\t" \
  ".p2align 4\n\t" \
  "3:\n\t" \
  "s_sub_i32 s79, s79, 33\n\t" \
  "s_cmp_gt_i32 s79, 56\n\t" \
  "s_cbranch_scc1 3b\n\t" \
  "buffer_load_dwordx4 v[72:75], v192, %[ru], s88 offen offset:0 sc1\n\t" \
  "s_nop 4\n\t" \
  "buffer_load_dwordx4 v[76:79], v192, %[ru], s88 offen offset:1024 sc1\n\t" \
  "s_nop 4\n\t" \
  "s_add_i32 s79, s79, 22\n\t" \
  ".p2align 4\n\t" \
  "4:\n\t" \
  "s_sub_i32 s79, s79, 33\n\t" \
  "s_cmp_gt_i32 s79, 56\n\t" \
  "s_cbranch_scc1 4b\n\t" \
  "buffer_load_dwordx4 v[80:83], v192, %[ru], s89 offen offset:0 sc1\n\t" \
  "s_nop 4\n\t" \
  "buffer_load_dwordx4 v[84:87], v192, %[ru], s89 offen offset:1024 sc1\n\t" \
  "s_nop 4\n\t" \
  "s_add_i32 s79, s79, 42\n\t" \
  ".p2align 4\n\t" \
  "5:\n\t" \
  "s_sub_i32 s79, s79, 33\n\t" \
  "s_cmp_gt_i32 s79, 56\n\t" \
  "s_cbranch_scc1 5b\n\t" \
  "buffer_load_dwordx4 v[88:91], v192, %[ru], s90 offen offset:0 sc1\n\t" \
  "buffer_load_dwordx4 v[92:95], v192, %[ru], s90 offen offset:1024 sc1\n\t" \
  "s_add_i32 s79, s79, 122\n\t" \
  ".p2align 4\n\t" \
  "9:\n\t" \
  "s_sub_i32 s79, s79, 33\n\t" \
  "s_cmp_gt_i32 s79, 56\n\t" \
  "s_cbranch_scc1 9b\n\t" \
  "buffer_load_dwordx4 v[104:107], v194, %[rh], s92 offen sc1\n\t" \
  "s_nop 4\n\t" \
  "buffer_load_dwordx4 v[108:111], v194, %[rh], s93 offen sc1\n\t" \
  "s_nop 4\n\t" \
  "buffer_load_dwordx4 v[112:115], v194, %[rh], s94 offen sc1\n\t" \
  "s_nop 4\n\t" \
  "buffer_load_dwordx4 v[116:119], v194, %[rh], s95 offen sc1\n\t" \
  "s_nop 4\n\t" \
  "buffer_load_dwordx4 v[96:99], v192, %[ru], s91 offen offset:0 sc1\n\t" \
  "s_nop 4\n\t" \
  "buffer_load_dwordx4 v[100:103], v192, %[ru], s91 offen offset:1024 sc1\n\t" \
  "s_nop 4\n\t" \
  "10:\n\t" \
  "s_waitcnt vmcnt(5)\n\t" \
  "v_mfma_f32_16x16x32_bf16 v[136:139], v[104:107], v[72:75], 0\n\t" \
  "v_mfma_f32_16x16x32_bf16 v[140:143], v[104:107], v[76:79], 0\n\t" \
  "s_waitcnt vmcnt(4)\n\t" \
  "v_mfma_f32_16x16x32_bf16 v[136:139], v[108:111], v[80:83], v[136:139]\n\t" \
  "v_mfma_f32_16x16x32_bf16 v[140:143], v[108:111], v[84:87], v[140:143]\n\t" \
  "s_waitcnt vmcnt(3)\n\t" \
  "v_mfma_f32_16x16x32_bf16 v[136:139], v[112:115], v[88:91], v[136:139]\n\t" \
  "v_mfma_f32_16x16x32_bf16 v[140:143], v[112:115], v[92:95], v[140:143]\n\t" \
  "s_waitcnt vmcnt(1)\n\t" \
  "v_mfma_f32_16x16x32_bf16 v[136:139], v[116:119], v[96:99], v[136:139]\n\t" \
  "s_waitcnt vmcnt(0)\n\t" \
  "v_mfma_f32_16x16x32_bf16 v[140:143], v[116:119], v[100:103], v[140:143]\n\t" \
  "s_barrier\n\t" \
  "ds_read2st64_b64 v[144:147], v195 offset1:1\n\t" \
  "s_nop 1\n\t" \
  "v_permlane32_swap_b32 v136, v138\n\t" \
  "v_permlane32_swap_b32 v137, v139\n\t" \
  "s_nop 1\n\t" \
  "v_permlane32_swap_b32 v140, v142\n\t" \
  "v_permlane32_swap_b32 v141, v143\n\t" \
  "v_add_f32 v136, v136, v142\n\t" \
  "v_add_f32 v137, v137, v143\n\t" \
  "s_waitcnt lgkmcnt(0)\n\t" \
  "v_add_f32 v136, v136, v144\n\t" \
  "v_add_f32 v137, v137, v145\n\t" \
  "v_add_f32 v136, v136, v146\n\t" \
  "v_add_f32 v137, v137, v147\n\t" \
  "v_cvt_pk_bf16_f32 v148, v136, v137\n\t" \
  "s_nop 1\n\t" \
  "v_mov_b32_dpp v149, v148 row_half_mirror row_mask:0xf bank_mask:0xf\n\t" \
  "v_perm_b32 v150, v148, v149, v205\n\t" \
  "v_cmpx_o_f16_sdwa vcc, v150, v150 src0_sel:WORD_0 src1_sel:WORD_1\n\t" \
  "6:\n\t" \
  "buffer_store_dword v150, v201, %[rh], s97 offen sc1\n\t" \
  "s_memtime s[82:83]\n\t" \
  "s_mov_b64 exec, -1\n\t" \
  "s_cmp_eq_u64 vcc, -1\n\t" \
  "s_cbranch_scc0 8f\n\t" \
  "v_mov_b32 v151, 0\n\t" \
  "5:\n\t" \
  "ds_write_b32 v206, v151\n\t" \
  "s_waitcnt lgkmcnt(0)\n\t" \
  "s_mov_b32 s56, s82\n\t" \
  "s_barrier\n\t" \
  "ds_read2_b32 v[148:149], v207 offset1:8\n\t" \
  "s_waitcnt lgkmcnt(0)\n\t" \
  "v_or_b32 v148, v148, v149\n\t" \
  "v_cmp_ne_u32 vcc, 0, v148\n\t" \
  "s_cbranch_vccnz 12f\n\t" \
  "s_and_b32 s58, s58, 4\n\t" \
  "25:\n\t" \
  "s_add_i32 s82, s40, 2\n\t" \
  "s_min_i32 s83, s82, 63\n\t" \
  "s_mov_b32 exec_hi, 0\n\t" \
  "v_cmp_gt_i32 vcc, s83, v186\n\t" \
  "s_mov_b64 exec, -1\n\t" \
  "s_and_b32 s81, s82, 15\n\t" \
  "s_lshl_b32 s81, s81, 22\n\t" \
  "s_add_i32 s81, s81, s51\n\t" \
  "s_lshr_b32 s80, s81, 21\n\t" \
  "s_bitcmp0_b64 %[sel], s80\n\t" \
  "s_cselect_b32 s80, 0x2000, 0\n\t" \
  "s_add_i32 s81, s81, s80\n\t" \
  "s_cbranch_vccnz 27f\n\t" \
  "28:\n\t" \
  "s_cmp_ge_i32 s40, 62\n\t" \
  "s_cbranch_scc1 37f\n\t" \
  "s_and_b32 s76, s40, 3\n\t" \
  "s_lshl_b32 s76, s76, 18\n\t" \
  "buffer_load_dwordx4 v[0:3], v210, %[ru], s81 offen offset:0 sc1\n\t" \
  "buffer_load_dwordx4 v[4:7], v210, %[ru], s81 offen offset:1024 sc1\n\t" \
  "s_add_i32 s81, s81, 0x20000\n\t" \
  "buffer_load_dwordx4 v[8:11], v210, %[ru], s81 offen offset:0 sc1\n\t" \
  "buffer_load_dwordx4 v[12:15], v210, %[ru], s81 offen offset:1024 sc1\n\t" \
  "s_add_i32 s81, s81, 0x20000\n\t" \
  "buffer_load_dwordx4 v[16:19], v210, %[ru], s81 offen offset:0 sc1\n\t" \
  "buffer_load_dwordx4 v[20:23], v210, %[ru], s81 offen offset:1024 sc1\n\t" \
  "s_add_i32 s81, s81, 0x20000\n\t" \
  "buffer_load_dwordx4 v[24:27], v210, %[ru], s81 offen offset:0 sc1\n\t" \
  "buffer_load_dwordx4 v[28:31], v210, %[ru], s81 offen offset:1024 sc1\n\t" \
  "s_add_i32 s81, s81, 0x20000\n\t" \
  "s_bitcmp1_b32 s58, 4\n\t" \
  "s_cbranch_scc1 29f\n\t" \
  "s_sub_i32 s80, s56, s41\n\t" \
  "s_mul_hi_u32 s80, s80, s45\n\t" \
  "s_lshl_b32 s80, s80, 1\n\t" \
  "s_sub_i32 s80, s80, s57\n\t" \
  "s_min_u32 s47, s47, s46\n\t" \
  "s_min_u32 s47, s47, s80\n\t" \
  "v_mov_b32 v188, s47\n\t" \
  "s_mov_b32 s47, s46\n\t" \
  "s_mov_b32 s46, s80\n\t" \
  "buffer_store_dword v188, v202, %[rh], s52 offen sc1\n\t" \
  "v_mov_b64 v[188:189], -1\n\t" \
  "v_mov_b64 v[190:191], -1\n\t" \
  "v_mov_b32 v187, s40\n\t" \
  "s_add_i32 s80, s40, -1\n\t" \
  "s_and_b32 s80, s80, 7\n\t" \
  "s_lshl_b32 s80, s80, 15\n\t" \
  "s_add_i32 s80, s80, s49\n\t" \
  "buffer_store_dwordx4 v[188:191], v208, %[rh], s80 offen sc1\n\t" \
  "buffer_store_dword v187, v209, %[ru], 0 offen sc1\n\t" \
  "ds_write_b128 v197, v[72:75] offset:16384\n\t" \
  "ds_write_b128 v197, v[76:79] offset:17408\n\t" \
  "ds_write_b128 v197, v[80:83] offset:18432\n\t" \
  "ds_write_b128 v197, v[84:87] offset:19456\n\t" \
  "ds_write_b128 v197, v[88:91] offset:20480\n\t" \
  "ds_write_b128 v197, v[92:95] offset:21504\n\t" \
  "ds_write_b128 v197, v[96:99] offset:22528\n\t" \
  "ds_write_b128 v197, v[100:103] offset:23552\n\t" \
  "29:\n\t" \
  "s_and_b32 s58, s58, 36\n\t" \
  "buffer_load_dwordx4 v[32:35], v210, %[ru], s81 offen offset:0 sc1\n\t" \
  "buffer_load_dwordx4 v[36:39], v210, %[ru], s81 offen offset:1024 sc1\n\t" \
  "s_add_i32 s81, s81, 0x20000\n\t" \
  "buffer_load_dwordx4 v[40:43], v210, %[ru], s81 offen offset:0 sc1\n\t" \
  "buffer_load_dwordx4 v[44:47], v210, %[ru], s81 offen offset:1024 sc1\n\t" \
  "s_add_i32 s81, s81, 0x20000\n\t" \
  "buffer_load_dwordx4 a[0:3], v211, %[rh], s76 offen offset:0 sc1\n\t" \
  "buffer_load_dwordx4 a[4:7], v211, %[rh], s76 offen offset:1024 sc1\n\t" \
  "s_waitcnt lgkmcnt(0)\n\t" \
  "s_barrier\n\t" \
  "buffer_load_dwordx4 v[48:51], v210, %[ru], s81 offen offset:0 sc1\n\t" \
  "buffer_load_dwordx4 v[52:55], v210, %[ru], s81 offen offset:1024 sc1\n\t" \
  "s_add_i32 s81, s81, 0x20000\n\t" \
  "buffer_load_dwordx4 v[56:59], v210, %[ru], s81 offen offset:0 sc1\n\t" \
  "buffer_load_dwordx4 v[60:63], v210, %[ru], s81 offen offset:1024 sc1\n\t" \
  "s_add_i32 s81, s81, 0x20000\n\t" \
  "buffer_load_dwordx4 a[8:11], v212, %[rh], s76 offen offset:0 sc1\n\t" \
  "buffer_load_dwordx4 a[12:15], v212, %[rh], s76 offen offset:1024 sc1\n\t" \
  "buffer_load_dwordx4 v[64:67], v210, %[ru], s81 offen offset:0 sc1\n\t" \
  "buffer_load_dwordx4 v[68:71], v210, %[ru], s81 offen offset:1024 sc1\n\t" \
  "s_add_i32 s81, s81, 0x20000\n\t" \
  "buffer_load_dwordx4 v[104:107], v210, %[ru], s81 offen offset:0 sc1\n\t" \
  "buffer_load_dwordx4 v[108:111], v210, %[ru], s81 offen offset:1024 sc1\n\t" \
  "s_add_i32 s81, s81, 0x20000\n\t" \
  "buffer_load_dwordx4 a[16:19], v213, %[rh], s76 offen offset:0 sc1\n\t" \
  "buffer_load_dwordx4 a[20:23], v213, %[rh], s76 offen offset:1024 sc1\n\t" \
  "ds_read_b128 v[160:163], v193 offset:0\n\t" \
  "ds_read_b128 v[164:167], v193 offset:256\n\t" \
  "ds_read_b128 v[168:171], v193 offset:2048\n\t" \
  "ds_read_b128 v[172:175], v193 offset:2304\n\t" \
  "ds_read_b128 v[176:179], v193 offset:4096\n\t" \
  "ds_read_b128 v[180:183], v193 offset:4352\n\t" \
  "s_waitcnt lgkmcnt(2)\n\t" \
  "v_pk_add_f32 v[160:161], v[160:161], v[168:169]\n\t" \
  "v_pk_add_f32 v[162:163], v[162:163], v[170:171]\n\t" \
  "v_pk_add_f32 v[164:165], v[164:165], v[172:173]\n\t" \
  "v_pk_add_f32 v[166:167], v[166:167], v[174:175]\n\t" \
  "s_waitcnt lgkmcnt(0)\n\t" \
  "v_pk_add_f32 v[160:161], v[160:161], v[176:177]\n\t" \
  "v_pk_add_f32 v[162:163], v[162:163], v[178:179]\n\t" \
  "v_pk_add_f32 v[164:165], v[164:165], v[180:181]\n\t" \
  "v_pk_add_f32 v[166:167], v[166:167], v[182:183]\n\t" \
  "v_cvt_pk_bf16_f32 v160, v160, v161\n\t" \
  "v_cvt_pk_bf16_f32 v161, v162, v163\n\t" \
  "v_cvt_pk_bf16_f32 v162, v164, v165\n\t" \
  "v_cvt_pk_bf16_f32 v163, v166, v167\n\t" \
  "buffer_load_dwordx4 v[112:115], v210, %[ru], s81 offen offset:0 sc1\n\t" \
  "buffer_load_dwordx4 v[116:119], v210, %[ru], s81 offen offset:1024 sc1\n\t" \
  "s_add_i32 s81, s81, 0x20000\n\t" \
  "buffer_load_dwordx4 v[120:123], v210, %[ru], s81 offen offset:0 sc1\n\t" \
  "buffer_load_dwordx4 v[124:127], v210, %[ru], s81 offen offset:1024 sc1\n\t" \
  "s_add_i32 s81, s81, 0x20000\n\t" \
  "buffer_load_dwordx4 a[24:27], v214, %[rh], s76 offen offset:0 sc1\n\t" \
  "buffer_load_dwordx4 a[28:31], v214, %[rh], s76 offen offset:1024 sc1\n\t" \
  "s_waitcnt vmcnt(28)\n\t" \
  "v_swap_b32 v1, v8\n\t" \
  "v_swap_b32 v3, v10\n\t" \
  "v_swap_b32 v5, v12\n\t" \
  "v_swap_b32 v7, v14\n\t" \
  "ds_write_b128 v204, v[0:3] offset:0\n\t" \
  "ds_write_b128 v204, v[8:11] offset:1024\n\t" \
  "ds_write_b128 v204, v[4:7] offset:2048\n\t" \
  "ds_write_b128 v204, v[12:15] offset:3072\n\t" \
  "buffer_load_dwordx4 v[128:131], v210, %[ru], s81 offen offset:0 sc1\n\t" \
  "buffer_load_dwordx4 v[132:135], v210, %[ru], s81 offen offset:1024 sc1\n\t" \
  "s_add_i32 s81, s81, 0x20000\n\t" \
  "buffer_load_dwordx4 v[136:139], v210, %[ru], s81 offen offset:0 sc1\n\t" \
  "buffer_load_dwordx4 v[140:143], v210, %[ru], s81 offen offset:1024 sc1\n\t" \
  "s_add_i32 s81, s81, 0x20000\n\t" \
  "s_waitcnt vmcnt(28)\n\t" \
  "v_swap_b32 v17, v24\n\t" \
  "v_swap_b32 v19, v26\n\t" \
  "v_swap_b32 v21, v28\n\t" \
  "v_swap_b32 v23, v30\n\t" \
  "ds_write_b128 v204, v[16:19] offset:4096\n\t" \
  "ds_write_b128 v204, v[24:27] offset:5120\n\t" \
  "ds_write_b128 v204, v[20:23] offset:6144\n\t" \
  "ds_write_b128 v204, v[28:31] offset:7168\n\t" \
  "buffer_load_dwordx4 v[144:147], v210, %[ru], s81 offen offset:0 sc1\n\t" \
  "buffer_load_dwordx4 v[148:151], v210, %[ru], s81 offen offset:1024 sc1\n\t" \
  "s_add_i32 s81, s81, 0x20000\n\t" \
  "buffer_load_dwordx4 v[152:155], v210, %[ru], s81 offen offset:0 sc1\n\t" \
  "buffer_load_dwordx4 v[156:159], v210, %[ru], s81 offen offset:1024 sc1\n\t" \
  "s_add_i32 s81, s81, 0x20000\n\t" \
  "s_waitcnt vmcnt(28)\n\t" \
  "v_swap_b32 v33, v40\n\t" \
  "v_swap_b32 v35, v42\n\t" \
  "v_swap_b32 v37, v44\n\t" \
  "v_swap_b32 v39, v46\n\t" \
  "ds_write_b128 v204, v[32:35] offset:8192\n\t" \
  "ds_write_b128 v204, v[40:43] offset:9216\n\t" \
  "ds_write_b128 v204, v[36:39] offset:10240\n\t" \
  "ds_write_b128 v204, v[44:47] offset:11264\n\t" \
  "s_waitcnt vmcnt(22)\n\t" \
  "v_swap_b32 v49, v56\n\t" \
  "v_swap_b32 v51, v58\n\t" \
  "v_swap_b32 v53, v60\n\t" \
  "v_swap_b32 v55, v62\n\t" \
  "ds_write_b128 v204, v[48:51] offset:12288\n\t" \
  "ds_write_b128 v204, v[56:59] offset:13312\n\t" \
  "ds_write_b128 v204, v[52:55] offset:14336\n\t" \
  "ds_write_b128 v204, v[60:63] offset:15360\n\t" \
  "s_waitcnt vmcnt(16)\n\t" \
  "v_swap_b32 v65, v104\n\t" \
  "v_swap_b32 v67, v106\n\t" \
  "v_swap_b32 v69, v108\n\t" \
  "v_swap_b32 v71, v110\n\t" \
  "ds_write_b128 v204, v[64:67] offset:16384\n\t" \
  "ds_write_b128 v204, v[104:107] offset:17408\n\t" \
  "ds_write_b128 v204, v[68:71] offset:18432\n\t" \
  "ds_write_b128 v204, v[108:111] offset:19456\n\t" \
  "s_waitcnt vmcnt(10)\n\t" \
  "v_swap_b32 v113, v120\n\t" \
  "v_swap_b32 v115, v122\n\t" \
  "v_swap_b32 v117, v124\n\t" \
  "v_swap_b32 v119, v126\n\t" \
  "ds_write_b128 v204, v[112:115] offset:20480\n\t" \
  "ds_write_b128 v204, v[120:123] offset:21504\n\t" \
  "ds_write_b128 v204, v[116:119] offset:22528\n\t" \
  "ds_write_b128 v204, v[124:127] offset:23552\n\t" \
  "s_waitcnt vmcnt(4)\n\t" \
  "v_swap_b32 v129, v136\n\t" \
  "v_swap_b32 v131, v138\n\t" \
  "v_swap_b32 v133, v140\n\t" \
  "v_swap_b32 v135, v142\n\t" \
  "ds_write_b128 v204, v[128:131] offset:24576\n\t" \
  "ds_write_b128 v204, v[136:139] offset:25600\n\t" \
  "ds_write_b128 v204, v[132:135] offset:26624\n\t" \
  "ds_write_b128 v204, v[140:143] offset:27648\n\t" \
  "s_waitcnt vmcnt(0)\n\t" \
  "v_swap_b32 v145, v152\n\t" \
  "v_swap_b32 v147, v154\n\t" \
  "v_swap_b32 v149, v156\n\t" \
  "v_swap_b32 v151, v158\n\t" \
  "ds_write_b128 v204, v[144:147] offset:28672\n\t" \
  "ds_write_b128 v204, v[152:155] offset:29696\n\t" \
  "ds_write_b128 v204, v[148:151] offset:30720\n\t" \
  "ds_write_b128 v204, v[156:159] offset:31744\n\t" \
  "s_waitcnt lgkmcnt(0)\n\t" \
  "s_cmp_lt_i32 s40, 0\n\t" \
  "s_cbranch_scc1 39f\n\t" \
  "38:\n\t" \
  "s_add_i32 s80, s40, -2\n\t" \
  "s_lshl_b32 s80, s80, 17\n\t" \
  "s_min_u32 s80, s80, 0x70000000\n\t" \
  "buffer_store_dwordx4 v[160:163], v196, %[ro], s80 offen sc1\n\t" \
  "ds_read_b128 a[32:35], v198 offset:0\n\t" \
  "ds_read_b128 a[36:39], v198 offset:1024\n\t" \
  "ds_read_b128 a[40:43], v198 offset:2048\n\t" \
  "ds_read_b128 a[44:47], v198 offset:3072\n\t" \
  "ds_read_b128 a[48:51], v198 offset:4096\n\t" \
  "ds_read_b128 a[52:55], v198 offset:5120\n\t" \
  "ds_read_b128 a[56:59], v198 offset:6144\n\t" \
  "ds_read_b128 a[60:63], v198 offset:7168\n\t" \
  "ds_read_b128 a[64:67], v198 offset:8192\n\t" \
  "ds_read_b128 a[68:71], v198 offset:9216\n\t" \
  "ds_read_b128 a[72:75], v198 offset:10240\n\t" \
  "ds_read_b128 a[76:79], v198 offset:11264\n\t" \
  "ds_read_b128 a[80:83], v198 offset:12288\n\t" \
  "ds_read_b128 a[84:87], v198 offset:13312\n\t" \
  "ds_read_b128 a[88:91], v198 offset:14336\n\t" \
  "ds_read_b128 a[92:95], v198 offset:15360\n\t" \
  "s_waitcnt vmcnt(1) lgkmcnt(0)\n\t" \
  "31:\n\t" \
  "v_mfma_f32_16x16x32_bf16 v[168:171], a[32:35], a[0:3], 0\n\t" \
  "v_mfma_f32_16x16x32_bf16 v[172:175], a[64:67], a[0:3], 0\n\t" \
  "v_mfma_f32_16x16x32_bf16 v[168:171], a[36:39], a[4:7], v[168:171]\n\t" \
  "v_mfma_f32_16x16x32_bf16 v[172:175], a[68:71], a[4:7], v[172:175]\n\t" \
  "v_mfma_f32_16x16x32_bf16 v[168:171], a[40:43], a[8:11], v[168:171]\n\t" \
  "v_mfma_f32_16x16x32_bf16 v[172:175], a[72:75], a[8:11], v[172:175]\n\t" \
  "v_mfma_f32_16x16x32_bf16 v[168:171], a[44:47], a[12:15], v[168:171]\n\t" \
  "v_mfma_f32_16x16x32_bf16 v[172:175], a[76:79], a[12:15], v[172:175]\n\t" \
  "v_mfma_f32_16x16x32_bf16 v[168:171], a[48:51], a[16:19], v[168:171]\n\t" \
  "v_mfma_f32_16x16x32_bf16 v[172:175], a[80:83], a[16:19], v[172:175]\n\t" \
  "v_mfma_f32_16x16x32_bf16 v[168:171], a[52:55], a[20:23], v[168:171]\n\t" \
  "v_mfma_f32_16x16x32_bf16 v[172:175], a[84:87], a[20:23], v[172:175]\n\t" \
  "v_mfma_f32_16x16x32_bf16 v[168:171], a[56:59], a[24:27], v[168:171]\n\t" \
  "v_mfma_f32_16x16x32_bf16 v[172:175], a[88:91], a[24:27], v[172:175]\n\t" \
  "v_mfma_f32_16x16x32_bf16 v[168:171], a[60:63], a[28:31], v[168:171]\n\t" \
  "v_mfma_f32_16x16x32_bf16 v[172:175], a[92:95], a[28:31], v[172:175]\n\t" \
  "s_nop 7\n\t" \
  "39:\n\t" \
  "s_add_i32 s40, s40, 2\n\t" \
  "v_cmp_u_f32 vcc, v168, v168\n\t" \
  "s_cbranch_vccnz 32f\n\t" \
  "33:\n\t" \
  "s_barrier\n\t" \
  "s_add_i32 s85, s40, -1\n\t" \
  "s_and_b32 s82, s85, 2\n\t" \
  "s_lshl_b32 s82, s82, 13\n\t" \
  "s_and_b32 s80, s85, 1\n\t" \
  "s_lshl_b32 s80, s80, 12\n\t" \
  "s_add_i32 s82, s82, s80\n\t" \
  "s_add_i32 s82, s82, 0x84000\n\t" \
  "s_mov_b32 exec_hi, 0\n\t" \
  "buffer_load_dword v184, v200, %[rh], s82 offen sc1\n\t" \
  "buffer_load_dword v186, v203, %[ru], 0 offen sc1\n\t" \
  "s_mov_b64 exec, -1\n\t" \
  "ds_write_b128 v199, v[168:171] offset:0\n\t" \
  "ds_write_b128 v199, v[172:175] offset:1024\n\t" \
  "s_mov_b32 s59, 0\n\t" \
  "s_and_b32 s58, s58, 4\n\t" \
  "s_cmp_lt_i32 s40, 64\n\t" \
  "s_cbranch_scc1 20b\n\t" \
  "s_waitcnt lgkmcnt(0)\n\t" \
  "s_barrier\n\t" \
  "ds_read_b128 v[160:163], v193 offset:0\n\t" \
  "ds_read_b128 v[164:167], v193 offset:256\n\t" \
  "ds_read_b128 v[168:171], v193 offset:2048\n\t" \
  "ds_read_b128 v[172:175], v193 offset:2304\n\t" \
  "ds_read_b128 v[176:179], v193 offset:4096\n\t" \
  "ds_read_b128 v[180:183], v193 offset:4352\n\t" \
  "s_waitcnt lgkmcnt(2)\n\t" \
  "v_pk_add_f32 v[160:161], v[160:161], v[168:169]\n\t" \
  "v_pk_add_f32 v[162:163], v[162:163], v[170:171]\n\t" \
  "v_pk_add_f32 v[164:165], v[164:165], v[172:173]\n\t" \
  "v_pk_add_f32 v[166:167], v[166:167], v[174:175]\n\t" \
  "s_waitcnt lgkmcnt(0)\n\t" \
  "v_pk_add_f32 v[160:161], v[160:161], v[176:177]\n\t" \
  "v_pk_add_f32 v[162:163], v[162:163], v[178:179]\n\t" \
  "v_pk_add_f32 v[164:165], v[164:165], v[180:181]\n\t" \
  "v_pk_add_f32 v[166:167], v[166:167], v[182:183]\n\t" \
  "v_cvt_pk_bf16_f32 v160, v160, v161\n\t" \
  "v_cvt_pk_bf16_f32 v161, v162, v163\n\t" \
  "v_cvt_pk_bf16_f32 v162, v164, v165\n\t" \
  "v_cvt_pk_bf16_f32 v163, v166, v167\n\t" \
  "s_add_i32 s80, s40, -2\n\t" \
  "s_lshl_b32 s80, s80, 17\n\t" \
  "s_min_u32 s80, s80, 0x70000000\n\t" \
  "buffer_store_dwordx4 v[160:163], v196, %[ro], s80 offen sc1\n\t" \
  "s_mov_b32 s58, 64\n\t" \
  "s_branch 7f\n\t" \
  "22:\n\t" \
  "s_mov_b32 s81, 0\n\t" \
  "s_cmp_eq_u32 s40, 0\n\t" \
  "s_cbranch_scc1 24b\n\t" \
  "s_bitcmp1_b32 s58, 2\n\t" \
  "s_cbranch_scc1 24b\n\t" \
  "s_mov_b64 exec, -1\n\t" \
  "s_sleep 10\n\t" \
  "s_add_u32 s59, s59, 0x10000\n\t" \
  "s_cmp_lt_u32 s59, 0x100000\n\t" \
  "s_cbranch_scc1 19b\n\t" \
  "s_or_b32 s58, s58, 1\n\t" \
  "s_branch 7f\n\t" \
  "14:\n\t" \
  "v_mov_b32 v184, 0\n\t" \
  "s_bitcmp1_b32 s58, 2\n\t" \
  "s_nop 0\n\t" \
  "s_cbranch_scc1 15b\n\t" \
  "s_mov_b32 s84, 950\n\t" \
  "s_add_u32 s59, s59, 0x10000\n\t" \
  "s_cmp_lt_u32 s59, 0x100000\n\t" \
  "s_mov_b64 exec, vcc\n\t" \
  "buffer_load_dword v187, v200, %[rh], s78 offen sc1\n\t" \
  "s_mov_b32 exec_lo, -1\n\t" \
  "s_cbranch_scc1 13b\n\t" \
  "s_mov_b64 exec, -1\n\t" \
  "s_or_b32 s58, s58, 1\n\t" \
  "s_branch 7f\n\t" \
  "27:\n\t" \
  "s_bitcmp1_b32 s58, 2\n\t" \
  "s_cbranch_scc1 28b\n\t" \
  "s_bitcmp1_b32 s58, 4\n\t" \
  "s_cbranch_scc1 23f\n\t" \
  "s_sub_i32 s80, s56, s41\n\t" \
  "s_mul_hi_u32 s80, s80, s45\n\t" \
  "s_lshl_b32 s80, s80, 1\n\t" \
  "s_sub_i32 s80, s80, s57\n\t" \
  "s_min_u32 s47, s47, s46\n\t" \
  "s_min_u32 s47, s47, s80\n\t" \
  "v_mov_b32 v188, s47\n\t" \
  "s_mov_b32 s47, s46\n\t" \
  "s_mov_b32 s46, s80\n\t" \
  "buffer_store_dword v188, v202, %[rh], s52 offen sc1\n\t" \
  "v_mov_b64 v[188:189], -1\n\t" \
  "v_mov_b64 v[190:191], -1\n\t" \
  "v_mov_b32 v187, s40\n\t" \
  "s_add_i32 s80, s40, -1\n\t" \
  "s_and_b32 s80, s80, 7\n\t" \
  "s_lshl_b32 s80, s80, 15\n\t" \
  "s_add_i32 s80, s80, s49\n\t" \
  "buffer_store_dwordx4 v[188:191], v208, %[rh], s80 offen sc1\n\t" \
  "buffer_store_dword v187, v209, %[ru], 0 offen sc1\n\t" \
  "ds_write_b128 v197, v[72:75] offset:16384\n\t" \
  "ds_write_b128 v197, v[76:79] offset:17408\n\t" \
  "ds_write_b128 v197, v[80:83] offset:18432\n\t" \
  "ds_write_b128 v197, v[84:87] offset:19456\n\t" \
  "ds_write_b128 v197, v[88:91] offset:20480\n\t" \
  "ds_write_b128 v197, v[92:95] offset:21504\n\t" \
  "ds_write_b128 v197, v[96:99] offset:22528\n\t" \
  "ds_write_b128 v197, v[100:103] offset:23552\n\t" \
  "s_or_b32 s58, s58, 16\n\t" \
  "23:\n\t" \
  "s_add_u32 s59, s59, 0x1000000\n\t" \
  "s_cmp_lt_u32 s59, 0x40000000\n\t" \
  "s_cbranch_scc1 26f\n\t" \
  "s_or_b32 s58, s58, 1\n\t" \
  "s_branch 7f\n\t" \
  "26:\n\t" \
  "buffer_load_dword v186, v203, %[ru], 0 offen sc1\n\t" \
  "s_waitcnt vmcnt(0)\n\t" \
  "s_branch 25b\n\t" \
  "37:\n\t" \
  "s_and_b32 s76, s40, 3\n\t" \
  "s_lshl_b32 s76, s76, 18\n\t" \
  "buffer_load_dwordx4 a[0:3], v211, %[rh], s76 offen offset:0 sc1\n\t" \
  "buffer_load_dwordx4 a[4:7], v211, %[rh], s76 offen offset:1024 sc1\n\t" \
  "buffer_load_dwordx4 a[8:11], v212, %[rh], s76 offen offset:0 sc1\n\t" \
  "buffer_load_dwordx4 a[12:15], v212, %[rh], s76 offen offset:1024 sc1\n\t" \
  "buffer_load_dwordx4 a[16:19], v213, %[rh], s76 offen offset:0 sc1\n\t" \
  "buffer_load_dwordx4 a[20:23], v213, %[rh], s76 offen offset:1024 sc1\n\t" \
  "buffer_load_dwordx4 a[24:27], v214, %[rh], s76 offen offset:0 sc1\n\t" \
  "buffer_load_dwordx4 a[28:31], v214, %[rh], s76 offen offset:1024 sc1\n\t" \
  "s_bitcmp1_b32 s58, 4\n\t" \
  "s_cbranch_scc1 29f\n\t" \
  "s_sub_i32 s80, s56, s41\n\t" \
  "s_mul_hi_u32 s80, s80, s45\n\t" \
  "s_lshl_b32 s80, s80, 1\n\t" \
  "s_sub_i32 s80, s80, s57\n\t" \
  "s_min_u32 s47, s47, s46\n\t" \
  "s_min_u32 s47, s47, s80\n\t" \
  "v_mov_b32 v188, s47\n\t" \
  "s_mov_b32 s47, s46\n\t" \
  "s_mov_b32 s46, s80\n\t" \
  "buffer_store_dword v188, v202, %[rh], s52 offen sc1\n\t" \
  "v_mov_b64 v[188:189], -1\n\t" \
  "v_mov_b64 v[190:191], -1\n\t" \
  "v_mov_b32 v187, s40\n\t" \
  "s_add_i32 s80, s40, -1\n\t" \
  "s_and_b32 s80, s80, 7\n\t" \
  "s_lshl_b32 s80, s80, 15\n\t" \
  "s_add_i32 s80, s80, s49\n\t" \
  "buffer_store_dwordx4 v[188:191], v208, %[rh], s80 offen sc1\n\t" \
  "buffer_store_dword v187, v209, %[ru], 0 offen sc1\n\t" \
  "ds_write_b128 v197, v[72:75] offset:16384\n\t" \
  "ds_write_b128 v197, v[76:79] offset:17408\n\t" \
  "ds_write_b128 v197, v[80:83] offset:18432\n\t" \
  "ds_write_b128 v197, v[84:87] offset:19456\n\t" \
  "ds_write_b128 v197, v[88:91] offset:20480\n\t" \
  "ds_write_b128 v197, v[92:95] offset:21504\n\t" \
  "ds_write_b128 v197, v[96:99] offset:22528\n\t" \
  "ds_write_b128 v197, v[100:103] offset:23552\n\t" \
  "29:\n\t" \
  "s_and_b32 s58, s58, 36\n\t" \
  "s_waitcnt lgkmcnt(0)\n\t" \
  "s_barrier\n\t" \
  "ds_read_b128 v[160:163], v193 offset:0\n\t" \
  "ds_read_b128 v[164:167], v193 offset:256\n\t" \
  "ds_read_b128 v[168:171], v193 offset:2048\n\t" \
  "ds_read_b128 v[172:175], v193 offset:2304\n\t" \
  "ds_read_b128 v[176:179], v193 offset:4096\n\t" \
  "ds_read_b128 v[180:183], v193 offset:4352\n\t" \
  "s_waitcnt lgkmcnt(2)\n\t" \
  "v_pk_add_f32 v[160:161], v[160:161], v[168:169]\n\t" \
  "v_pk_add_f32 v[162:163], v[162:163], v[170:171]\n\t" \
  "v_pk_add_f32 v[164:165], v[164:165], v[172:173]\n\t" \
  "v_pk_add_f32 v[166:167], v[166:167], v[174:175]\n\t" \
  "s_waitcnt lgkmcnt(0)\n\t" \
  "v_pk_add_f32 v[160:161], v[160:161], v[176:177]\n\t" \
  "v_pk_add_f32 v[162:163], v[162:163], v[178:179]\n\t" \
  "v_pk_add_f32 v[164:165], v[164:165], v[180:181]\n\t" \
  "v_pk_add_f32 v[166:167], v[166:167], v[182:183]\n\t" \
  "v_cvt_pk_bf16_f32 v160, v160, v161\n\t" \
  "v_cvt_pk_bf16_f32 v161, v162, v163\n\t" \
  "v_cvt_pk_bf16_f32 v162, v164, v165\n\t" \
  "v_cvt_pk_bf16_f32 v163, v166, v167\n\t" \
  "s_branch 38b\n\t" \
  "8:\n\t" \
  "s_mov_b64 vcc, -1\n\t" \
  "s_and_b32 s80, s58, 6\n\t" \
  "s_cbranch_scc1 6b\n\t" \
  "v_mov_b32 v151, 1\n\t" \
  "s_branch 5b\n\t" \
  "32:\n\t" \
  "s_and_b32 s80, s58, 36\n\t" \
  "s_cbranch_scc1 33b\n\t" \
  "s_add_i32 s40, s40, -2\n\t" \
  "34:\n\t" \
  "s_or_b32 s59, s59, 128\n\t" \
  "s_and_b32 s76, s40, 3\n\t" \
  "s_lshl_b32 s76, s76, 18\n\t" \
  "35:\n\t" \
  "buffer_load_dwordx4 a[0:3], v211, %[rh], s76 offen offset:0 sc1\n\t" \
  "buffer_load_dwordx4 a[4:7], v211, %[rh], s76 offen offset:1024 sc1\n\t" \
  "buffer_load_dwordx4 a[8:11], v212, %[rh], s76 offen offset:0 sc1\n\t" \
  "buffer_load_dwordx4 a[12:15], v212, %[rh], s76 offen offset:1024 sc1\n\t" \
  "buffer_load_dwordx4 a[16:19], v213, %[rh], s76 offen offset:0 sc1\n\t" \
  "buffer_load_dwordx4 a[20:23], v213, %[rh], s76 offen offset:1024 sc1\n\t" \
  "buffer_load_dwordx4 a[24:27], v214, %[rh], s76 offen offset:0 sc1\n\t" \
  "buffer_load_dwordx4 a[28:31], v214, %[rh], s76 offen offset:1024 sc1\n\t" \
  "s_waitcnt vmcnt(0)\n\t" \
  "s_bitcmp1_b32 s58, 2\n\t" \
  "s_cbranch_scc1 36f\n\t" \
  "v_accvgpr_read_b32 v0, a0\n\t" \
  "v_accvgpr_read_b32 v1, a1\n\t" \
  "v_accvgpr_read_b32 v2, a2\n\t" \
  "v_accvgpr_read_b32 v3, a3\n\t" \
  "v_accvgpr_read_b32 v4, a4\n\t" \
  "v_accvgpr_read_b32 v5, a5\n\t" \
  "v_accvgpr_read_b32 v6, a6\n\t" \
  "v_accvgpr_read_b32 v7, a7\n\t" \
  "v_accvgpr_read_b32 v8, a8\n\t" \
  "v_accvgpr_read_b32 v9, a9\n\t" \
  "v_accvgpr_read_b32 v10, a10\n\t" \
  "v_accvgpr_read_b32 v11, a11\n\t" \
  "v_accvgpr_read_b32 v12, a12\n\t" \
  "v_accvgpr_read_b32 v13, a13\n\t" \
  "v_accvgpr_read_b32 v14, a14\n\t" \
  "v_accvgpr_read_b32 v15, a15\n\t" \
  "v_accvgpr_read_b32 v16, a16\n\t" \
  "v_accvgpr_read_b32 v17, a17\n\t" \
  "v_accvgpr_read_b32 v18, a18\n\t" \
  "v_accvgpr_read_b32 v19, a19\n\t" \
  "v_accvgpr_read_b32 v20, a20\n\t" \
  "v_accvgpr_read_b32 v21, a21\n\t" \
  "v_accvgpr_read_b32 v22, a22\n\t" \
  "v_accvgpr_read_b32 v23, a23\n\t" \
  "v_accvgpr_read_b32 v24, a24\n\t" \
  "v_accvgpr_read_b32 v25, a25\n\t" \
  "v_accvgpr_read_b32 v26, a26\n\t" \
  "v_accvgpr_read_b32 v27, a27\n\t" \
  "v_accvgpr_read_b32 v28, a28\n\t" \
  "v_accvgpr_read_b32 v29, a29\n\t" \
  "v_accvgpr_read_b32 v30, a30\n\t" \
  "v_accvgpr_read_b32 v31, a31\n\t" \
  "v_max_u32 v148, v0, v1\n\t" \
  "v_max3_u32 v148, v148, v2, v3\n\t" \
  "v_max3_u32 v148, v148, v4, v5\n\t" \
  "v_max3_u32 v148, v148, v6, v7\n\t" \
  "v_max3_u32 v148, v148, v8, v9\n\t" \
  "v_max3_u32 v148, v148, v10, v11\n\t" \
  "v_max3_u32 v148, v148, v12, v13\n\t" \
  "v_max3_u32 v148, v148, v14, v15\n\t" \
  "v_max3_u32 v148, v148, v16, v17\n\t" \
  "v_max3_u32 v148, v148, v18, v19\n\t" \
  "v_max3_u32 v148, v148, v20, v21\n\t" \
  "v_max3_u32 v148, v148, v22, v23\n\t" \
  "v_max3_u32 v148, v148, v24, v25\n\t" \
  "v_max3_u32 v148, v148, v26, v27\n\t" \
  "v_max3_u32 v148, v148, v28, v29\n\t" \
  "v_max3_u32 v148, v148, v30, v31\n\t" \
  "v_cmp_eq_u32 vcc, -1, v148\n\t" \
  "s_cbranch_vccz 36f\n\t" \
  "s_add_u32 s59, s59, 0x1000000\n\t" \
  "s_cmp_lt_u32 s59, 0x10000000\n\t" \
  "s_cbranch_scc1 35b\n\t" \
  "s_or_b32 s58, s58, 9\n\t" \
  "s_branch 7f\n\t" \
  "36:\n\t" \
  "s_and_b32 s58, s58, 4\n\t" \
  "s_or_b32 s58, s58, 32\n\t" \
  "s_branch 31b\n\t" \
  "18:\n\t" \
  "v_mov_b32 v186, -1\n\t" \
  "s_and_b32 s80, s40, 3\n\t" \
  "s_mul_i32 s77, s80, 0xc000\n\t" \
  "s_add_i32 s77, s77, 0x40000\n\t" \
  "s_lshl_b32 s78, s80, 14\n\t" \
  "s_add_i32 s78, s78, 0x70000\n\t" \
  "s_mov_b32 s57, 0\n\t" \
  "s_add_i32 s96, s77, 0x1000\n\t" \
  "s_lshl_b32 s88, s80, 20\n\t" \
  "s_add_i32 s88, s88, s48\n\t" \
  "s_lshr_b32 s80, s88, 21\n\t" \
  "s_bitcmp1_b64 %[sel], s80\n\t" \
  "s_cselect_b32 s80, 0x2000, 0\n\t" \
  "s_add_i32 s88, s88, s80\n\t" \
  "s_xor_b32 s89, s88, 0x800\n\t" \
  "s_xor_b32 s90, s88, 0x1000\n\t" \
  "s_xor_b32 s91, s88, 0x1800\n\t" \
  "s_add_i32 s92, s77, s50\n\t" \
  "s_xor_b32 s93, s92, 0x100\n\t" \
  "s_xor_b32 s94, s92, 0x200\n\t" \
  "s_xor_b32 s95, s92, 0x300\n\t" \
  "s_add_i32 s97, s40, 1\n\t" \
  "s_and_b32 s97, s97, 7\n\t" \
  "s_lshl_b32 s97, s97, 15\n\t" \
  "s_add_i32 s97, s97, s49\n\t" \
  "12:\n\t" \
  "s_or_b32 s58, s58, 3\n\t" \
  "11:\n\t" \
  "buffer_load_dwordx4 v[72:75], v192, %[ru], s88 offen offset:0 sc1\n\t" \
  "buffer_load_dwordx4 v[76:79], v192, %[ru], s88 offen offset:1024 sc1\n\t" \
  "buffer_load_dwordx4 v[80:83], v192, %[ru], s89 offen offset:0 sc1\n\t" \
  "buffer_load_dwordx4 v[84:87], v192, %[ru], s89 offen offset:1024 sc1\n\t" \
  "buffer_load_dwordx4 v[88:91], v192, %[ru], s90 offen offset:0 sc1\n\t" \
  "buffer_load_dwordx4 v[92:95], v192, %[ru], s90 offen offset:1024 sc1\n\t" \
  "buffer_load_dwordx4 v[96:99], v192, %[ru], s91 offen offset:0 sc1\n\t" \
  "buffer_load_dwordx4 v[100:103], v192, %[ru], s91 offen offset:1024 sc1\n\t" \
  "buffer_load_dwordx4 v[104:107], v194, %[rh], s92 offen sc1\n\t" \
  "buffer_load_dwordx4 v[108:111], v194, %[rh], s93 offen sc1\n\t" \
  "buffer_load_dwordx4 v[112:115], v194, %[rh], s94 offen sc1\n\t" \
  "buffer_load_dwordx4 v[116:119], v194, %[rh], s95 offen sc1\n\t" \
  "s_waitcnt vmcnt(0)\n\t" \
  "s_add_u32 s59, s59, 1\n\t" \
  "s_bitcmp1_b32 s58, 2\n\t" \
  "s_cbranch_scc1 10b\n\t" \
  "v_max_u32 v148, v72, v73\n\t" \
  "v_max3_u32 v148, v148, v74, v75\n\t" \
  "v_max3_u32 v148, v148, v76, v77\n\t" \
  "v_max3_u32 v148, v148, v78, v79\n\t" \
  "v_max3_u32 v148, v148, v80, v81\n\t" \
  "v_max3_u32 v148, v148, v82, v83\n\t" \
  "v_max3_u32 v148, v148, v84, v85\n\t" \
  "v_max3_u32 v148, v148, v86, v87\n\t" \
  "v_max3_u32 v148, v148, v88, v89\n\t" \
  "v_max3_u32 v148, v148, v90, v91\n\t" \
  "v_max3_u32 v148, v148, v92, v93\n\t" \
  "v_max3_u32 v148, v148, v94, v95\n\t" \
  "v_max3_u32 v148, v148, v96, v97\n\t" \
  "v_max3_u32 v148, v148, v98, v99\n\t" \
  "v_max3_u32 v148, v148, v100, v101\n\t" \
  "v_max3_u32 v148, v148, v102, v103\n\t" \
  "v_max3_u32 v148, v148, v104, v105\n\t" \
  "v_max3_u32 v148, v148, v106, v107\n\t" \
  "v_max3_u32 v148, v148, v108, v109\n\t" \
  "v_max3_u32 v148, v148, v110, v111\n\t" \
  "v_max3_u32 v148, v148, v112, v113\n\t" \
  "v_max3_u32 v148, v148, v114, v115\n\t" \
  "v_max3_u32 v148, v148, v116, v117\n\t" \
  "v_max3_u32 v148, v148, v118, v119\n\t" \
  "v_cmp_eq_u32 vcc, -1, v148\n\t" \
  "s_cbranch_vccz 10b\n\t" \
  "s_bitcmp1_b32 s59, 6\n\t" \
  "s_cbranch_scc0 11b\n\t" \
  "7:\n\t" \
  "s_branch 92f\n\t" \
  "91:\n\t" \
  "s_mov_b32 s58, s42\n\t" \
  "s_mov_b32 s59, 0\n\t" \
  "s_bitcmp1_b32 s58, 4\n\t" \
  "s_cbranch_scc1 37f\n\t" \
  "s_bitcmp1_b32 s58, 3\n\t" \
  "s_cbranch_scc1 34f\n\t" \
  "s_bitcmp1_b32 s58, 1\n\t" \
  "s_cbranch_scc1 18f\n\t" \
  "17:\n\t" \
  "v_mov_b64 v[40:41], 0\n\t" \
  "v_mov_b64 v[42:43], 0\n\t" \
  "v_mov_b64 v[44:45], 0\n\t" \
  "v_mov_b64 v[46:47], 0\n\t" \
  "v_mov_b64 v[48:49], 0\n\t" \
  "v_mov_b64 v[50:51], 0\n\t" \
  "v_mov_b64 v[52:53], 0\n\t" \
  "v_mov_b64 v[54:55], 0\n\t" \
  "v_mov_b64 v[56:57], 0\n\t" \
  "v_mov_b64 v[58:59], 0\n\t" \
  "v_mov_b64 v[60:61], 0\n\t" \
  "v_mov_b64 v[62:63], 0\n\t" \
  "v_mov_b64 v[64:65], 0\n\t" \
  "v_mov_b64 v[66:67], 0\n\t" \
  "v_mov_b64 v[68:69], 0\n\t" \
  "v_mov_b64 v[70:71], 0\n\t" \
  "v_mov_b64 v[72:73], 0\n\t" \
  "v_mov_b64 v[74:75], 0\n\t" \
  "v_mov_b64 v[76:77], 0\n\t" \
  "v_mov_b64 v[78:79], 0\n\t" \
  "v_mov_b64 v[80:81], 0\n\t" \
  "v_mov_b64 v[82:83], 0\n\t" \
  "v_mov_b64 v[84:85], 0\n\t" \
  "v_mov_b64 v[86:87], 0\n\t" \
  "v_mov_b64 v[88:89], 0\n\t" \
  "v_mov_b64 v[90:91], 0\n\t" \
  "v_mov_b64 v[92:93], 0\n\t" \
  "v_mov_b64 v[94:95], 0\n\t" \
  "v_mov_b64 v[96:97], 0\n\t" \
  "v_mov_b64 v[98:99], 0\n\t" \
  "v_mov_b64 v[100:101], 0\n\t" \
  "v_mov_b64 v[102:103], 0\n\t" \
  "v_mov_b64 v[136:137], 0\n\t" \
  "v_mov_b64 v[138:139], 0\n\t" \
  "v_mov_b64 v[140:141], 0\n\t" \
  "v_mov_b64 v[142:143], 0\n\t" \
  "19:\n\t" \
  "s_add_i32 s85, s40, -1\n\t" \
  "s_and_b32 s82, s85, 2\n\t" \
  "s_lshl_b32 s82, s82, 13\n\t" \
  "s_and_b32 s80, s85, 1\n\t" \
  "s_lshl_b32 s80, s80, 12\n\t" \
  "s_add_i32 s82, s82, s80\n\t" \
  "s_add_i32 s82, s82, 0x84000\n\t" \
  "s_mov_b32 exec_hi, 0\n\t" \
  "buffer_load_dword v184, v200, %[rh], s82 offen sc1\n\t" \
  "buffer_load_dword v185, v200, %[rh], s52 offen sc1\n\t" \
  "s_mov_b64 exec, -1\n\t" \
  "20:\n\t" \
  "s_and_b32 s80, s40, 3\n\t" \
  "s_mul_i32 s77, s80, 0xc000\n\t" \
  "s_add_i32 s77, s77, 0x40000\n\t" \
  "s_lshl_b32 s78, s80, 14\n\t" \
  "s_add_i32 s78, s78, 0x70000\n\t" \
  "s_mov_b32 s57, 0\n\t" \
  "s_add_i32 s96, s77, 0x1000\n\t" \
  "s_and_b32 s98, s40, 2\n\t" \
  "s_lshl_b32 s98, s98, 13\n\t" \
  "s_and_b32 s80, s40, 1\n\t" \
  "s_lshl_b32 s80, s80, 12\n\t" \
  "s_add_i32 s98, s98, s80\n\t" \
  "s_add_i32 s98, s98, 0x84000\n\t" \
  "s_memtime vcc\n\t" \
  "s_memrealtime s[86:87]\n\t" \
  "s_nop 4\n\t" \
  "s_memrealtime s[60:61]\n\t" \
  "s_nop 4\n\t" \
  "s_memrealtime s[62:63]\n\t" \
  "s_nop 4\n\t" \
  "s_memrealtime s[64:65]\n\t" \
  "s_nop 4\n\t" \
  "s_memrealtime s[66:67]\n\t" \
  "s_nop 4\n\t" \
  "s_memrealtime s[68:69]\n\t" \
  "s_mov_b32 s79, 0\n\t" \
  "s_waitcnt lgkmcnt(0)\n\t" \
  "s_cmp_eq_u32 s86, s60\n\t" \
  "s_addc_u32 s79, s79, 0\n\t" \
  "s_cmp_eq_u32 s86, s62\n\t" \
  "s_addc_u32 s79, s79, 0\n\t" \
  "s_cmp_eq_u32 s86, s64\n\t" \
  "s_addc_u32 s79, s79, 0\n\t" \
  "s_cmp_eq_u32 s86, s66\n\t" \
  "s_addc_u32 s79, s79, 0\n\t" \
  "s_cmp_eq_u32 s86, s68\n\t" \
  "s_addc_u32 s79, s79, 0\n\t" \
  "s_mul_i32 s79, s79, 24\n\t" \
  "s_add_i32 s79, s79, vcc_lo\n\t" \
  "s_lshl4_add_u32 s80, s86, 64\n\t" \
  "s_sub_i32 s86, s79, s41\n\t" \
  "s_sub_i32 s87, s80, s43\n\t" \
  "s_cmp_lt_u32 s86, 4000\n\t" \
  "s_cbranch_scc1 21f\n\t" \
  "v_cvt_f32_u32 v165, s86\n\t" \
  "v_cvt_f32_u32 v164, s87\n\t" \
  "v_rcp_f32 v165, v165\n\t" \
  "s_nop 0\n\t" \
  "v_mul_f32 v165, v165, v164\n\t" \
  "v_max_f32 v165, 0x3f028f5c, v165\n\t" \
  "v_min_f32 v165, 0x3ffeb852, v165\n\t" \
  "v_rcp_f32 v164, v165\n\t" \
  "v_mul_f32 v165, 0x4f000000, v165\n\t" \
  "v_mul_f32 v164, 0x47800000, v164\n\t" \
  "v_cvt_u32_f32 v165, v165\n\t" \
  "v_cvt_u32_f32 v164, v164\n\t" \
  "s_nop 0\n\t" \
  "v_readfirstlane_b32 s45, v165\n\t" \
  "v_readfirstlane_b32 s44, v164\n\t" \
  "21:\n\t" \
  "s_mov_b32 s41, s79\n\t" \
  "s_mov_b32 s43, s80\n\t" \
  "s_lshl_b32 s83, s43, 8\n\t" \
  "s_mov_b32 s84, 650\n\t" \
  "s_mov_b32 exec_hi, 0\n\t" \
  "s_waitcnt vmcnt(0)\n\t" \
  "v_cmp_ne_u32_sdwa vcc, v184, s85 src0_sel:BYTE_0 src1_sel:DWORD\n\t" \
  "v_subrev_u32 v184, s83, v184\n\t" \
  "v_ashrrev_i32 v184, 8, v184\n\t" \
  "s_cbranch_vccnz 22f\n\t" \
  "s_nop 0\n\t" \
  "v_max_i32_dpp v184, v184, v184 quad_perm:[1,0,3,2] row_mask:0xf bank_mask:0xf\n\t" \
  "s_nop 1\n\t" \
  "v_max_i32_dpp v184, v184, v184 quad_perm:[2,3,0,1] row_mask:0xf bank_mask:0xf\n\t" \
  "s_nop 1\n\t" \
  "v_max_i32_dpp v184, v184, v184 row_half_mirror row_mask:0xf bank_mask:0xf\n\t" \
  "s_nop 1\n\t" \
  "v_max_i32_dpp v184, v184, v184 row_mirror row_mask:0xf bank_mask:0xf\n\t" \
  "s_nop 0\n\t" \
  "v_readlane_b32 s81, v184, 0\n\t" \
  "v_readlane_b32 s82, v184, 16\n\t" \
  "s_max_i32 s81, s81, s82\n\t" \
  "s_add_i32 s81, s81, 1056\n\t" \
  "s_max_i32 s81, s81, 0\n\t" \
  "s_min_i32 s81, s81, 20000\n\t" \
  "s_mul_i32 s81, s81, s44\n\t" \
  "s_lshr_b32 s81, s81, 16\n\t" \
  "24:\n\t" \
  "s_add_i32 s81, s81, s41\n\t" \
  "v_min_u32 v185, 1760, v185\n\t" \
  "s_nop 1\n\t" \
  "v_max_i32_dpp v185, v185, v185 quad_perm:[1,0,3,2] row_mask:0xf bank_mask:0xf\n\t" \
  "s_nop 1\n\t" \
  "v_max_i32_dpp v185, v185, v185 quad_perm:[2,3,0,1] row_mask:0xf bank_mask:0xf\n\t" \
  "s_nop 1\n\t" \
  "v_max_i32_dpp v185, v185, v185 row_half_mirror row_mask:0xf bank_mask:0xf\n\t" \
  "s_nop 1\n\t" \
  "v_max_i32_dpp v185, v185, v185 row_mirror row_mask:0xf bank_mask:0xf\n\t" \
  "s_nop 0\n\t" \
  "v_readlane_b32 s54, v185, 0\n\t" \
  "v_readlane_b32 s82, v185, 16\n\t" \
  "s_max_i32 s54, s54, s82\n\t" \
  "s_add_i32 s54, s54, 245\n\t" \
  "1:\n\t" \
  "s_memtime vcc\n\t" \
  "s_waitcnt lgkmcnt(0)\n\t" \
  "s_sub_i32 s79, s81, vcc_lo\n\t" \
  "s_cmp_gt_i32 s79, 128\n\t" \
  "s_cbranch_scc1 1b\n\t" \
  ".p2align 4\n\t" \
  "2:\n\t" \
  "s_sub_i32 s79, s79, 33\n\t" \
  "s_cmp_gt_i32 s79, 56\n\t" \
  "s_cbranch_scc1 2b\n\t" \
  "buffer_load_dword v187, v200, %[rh], s78 offen sc1\n\t" \
  "s_memtime vcc\n\t" \
  "s_memrealtime s[86:87]\n\t" \
  "s_nop 4\n\t" \
  "s_memrealtime s[60:61]\n\t" \
  "s_nop 4\n\t" \
  "s_memrealtime s[62:63]\n\t" \
  "s_nop 4\n\t" \
  "s_memrealtime s[64:65]\n\t" \
  "s_nop 4\n\t" \
  "s_memrealtime s[66:67]\n\t" \
  "s_nop 4\n\t" \
  "s_memrealtime s[68:69]\n\t" \
  "s_mov_b32 s79, 0\n\t" \
  "s_waitcnt lgkmcnt(0)\n\t" \
  "s_cmp_eq_u32 s86, s60\n\t" \
  "s_addc_u32 s79, s79, 0\n\t" \
  "s_cmp_eq_u32 s86, s62\n\t" \
  "s_addc_u32 s79, s79, 0\n\t" \
  "s_cmp_eq_u32 s86, s64\n\t" \
  "s_addc_u32 s79, s79, 0\n\t" \
  "s_cmp_eq_u32 s86, s66\n\t" \
  "s_addc_u32 s79, s79, 0\n\t" \
  "s_cmp_eq_u32 s86, s68\n\t" \
  "s_addc_u32 s79, s79, 0\n\t" \
  "s_mul_i32 s79, s79, 24\n\t" \
  "s_add_i32 s79, s79, vcc_lo\n\t" \
  "s_lshl4_add_u32 s80, s86, 64\n\t" \
  "s_mov_b32 s41, s79\n\t" \
  "s_mov_b32 s43, s80\n\t" \
  "s_lshl_b32 s83, s43, 8\n\t" \
  "s_add_i32 s54, s54, s43\n\t" \
  "13:\n\t" \
  "s_waitcnt vmcnt(0)\n\t" \
  "s_memtime s[86:87]\n\t" \
  "v_cmp_ne_u32_sdwa vcc, v187, s40 src0_sel:BYTE_0 src1_sel:DWORD\n\t" \
  "v_subrev_u32 v184, s83, v187\n\t" \
  "v_ashrrev_i32 v184, 8, v184\n\t" \
  "s_nop 1\n\t" \
  "v_max_i32_dpp v184, v184, v184 quad_perm:[1,0,3,2] row_mask:0xf bank_mask:0xf\n\t" \
  "s_nop 1\n\t" \
  "v_max_i32_dpp v184, v184, v184 quad_perm:[2,3,0,1] row_mask:0xf bank_mask:0xf\n\t" \
  "s_cbranch_vccnz 14f\n\t" \
  "15:\n\t" \
  "s_nop 0\n\t" \
  "v_max_i32_dpp v184, v184, v184 row_half_mirror row_mask:0xf bank_mask:0xf\n\t" \
  "s_nop 1\n\t" \
  "v_max_i32_dpp v184, v184, v184 row_mirror row_mask:0xf bank_mask:0xf\n\t" \
  "s_mov_b64 exec, -1\n\t" \
  "v_readlane_b32 s57, v184, 0\n\t" \
  "v_readlane_b32 s82, v184, 16\n\t" \
  "s_waitcnt lgkmcnt(0)\n\t" \
  "s_sub_i32 s79, s41, s86\n\t" \
  "s_max_i32 s57, s57, s82\n\t" \
  "s_add_i32 s81, s57, 200\n\t" \
  "s_max_i32 s81, s81, 0\n\t" \
  "s_min_i32 s81, s81, 20000\n\t" \
  "s_mul_i32 s81, s81, s44\n\t" \
  "s_lshr_b32 s81, s81, 16\n\t" \
  "s_add_i32 s79, s79, s81\n\t" \
  "s_sub_i32 s85, s84, s79\n\t" \
  "s_max_i32 s85, s85, 0\n\t" \
  "s_mul_hi_u32 s85, s85, s45\n\t" \
  "s_lshl1_add_u32 s85, s85, s57\n\t" \
  "s_add_i32 s85, s85, s54\n\t" \
  "s_lshl_b32 s85, s85, 8\n\t" \
  "s_or_b32 s85, s85, s40\n\t" \
  "v_mov_b32 v184, s85\n\t" \
  "buffer_store_dword v184, v202, %[rh], s98 offen sc1\n\t" \
  "s_add_i32 s79, s79, -640\n\t" \
  ".p2align 4\n\t" \
  "3:\n\t" \
  "s_sub_i32 s79, s79, 33\n\t" \
  "s_cmp_gt_i32 s79, 56\n\t" \
  "s_cbranch_scc1 3b\n\t" \
  "buffer_load_dwordx4 v[0:3], v193, %[rh], s77 offen offset:0 sc1\n\t" \
  "s_nop 4\n\t" \
  "buffer_load_dwordx4 v[4:7], v193, %[rh], s77 offen offset:1024 sc1\n\t" \
  "s_nop 4\n\t" \
  "buffer_load_dwordx4 v[8:11], v193, %[rh], s77 offen offset:2048 sc1\n\t" \
  "s_nop 4\n\t" \
  "buffer_load_dwordx4 v[12:15], v193, %[rh], s77 offen offset:3072 sc1\n\t" \
  "s_nop 4\n\t" \
  "buffer_load_dwordx4 v[16:19], v193, %[rh], s96 offen offset:0 sc1\n\t" \
  "s_nop 4\n\t" \
  "buffer_load_dwordx4 v[20:23], v193, %[rh], s96 offen offset:1024 sc1\n\t" \
  "s_nop 4\n\t" \
  "buffer_load_dwordx4 v[24:27], v193, %[rh], s96 offen offset:2048 sc1\n\t" \
  "s_nop 4\n\t" \
  "buffer_load_dwordx4 v[28:31], v193, %[rh], s96 offen offset:3072 sc1\n\t" \
  "s_add_i32 s79, s79, 272\n\t" \
  ".p2align 4\n\t" \
  "4:\n\t" \
  "s_sub_i32 s79, s79, 33\n\t" \
  "s_cmp_gt_i32 s79, 56\n\t" \
  "s_cbranch_scc1 4b\n\t" \
  "buffer_load_dwordx4 v[32:35], v195, %[rh], s77 offen offset:0 sc1\n\t" \
  "buffer_load_dwordx4 v[36:39], v195, %[rh], s77 offen offset:2048 sc1\n\t" \
  "10:\n\t" \
  "s_waitcnt vmcnt(9)\n\t" \
  "v_smfmac_f32_16x16x64_bf16 v[40:43], v[0:3], a[0:7], v203\n\t" \
  "v_smfmac_f32_16x16x64_bf16 v[56:59], v[0:3], a[8:15], v203\n\t" \
  "s_waitcnt vmcnt(8)\n\t" \
  "v_smfmac_f32_16x16x64_bf16 v[44:47], v[4:7], a[16:23], v203\n\t" \
  "v_cvt_pk_bf16_f32 v40, v40, v41\n\t" \
  "v_cvt_pk_bf16_f32 v41, v42, v43\n\t" \
  "v_smfmac_f32_16x16x64_bf16 v[60:63], v[4:7], a[24:31], v203\n\t" \
  "v_cvt_pk_bf16_f32 v56, v56, v57\n\t" \
  "v_cvt_pk_bf16_f32 v57, v58, v59\n\t" \
  "s_waitcnt vmcnt(7)\n\t" \
  "v_smfmac_f32_16x16x64_bf16 v[48:51], v[8:11], a[32:39], v203\n\t" \
  "v_cvt_pk_bf16_f32 v42, v44, v45\n\t" \
  "v_cvt_pk_bf16_f32 v43, v46, v47\n\t" \
  "v_smfmac_f32_16x16x64_bf16 v[64:67], v[8:11], a[40:47], v203\n\t" \
  "v_cvt_pk_bf16_f32 v58, v60, v61\n\t" \
  "v_cvt_pk_bf16_f32 v59, v62, v63\n\t" \
  "s_waitcnt vmcnt(6)\n\t" \
  "v_smfmac_f32_16x16x64_bf16 v[52:55], v[12:15], a[48:55], v203\n\t" \
  "v_cvt_pk_bf16_f32 v44, v48, v49\n\t" \
  "v_cvt_pk_bf16_f32 v45, v50, v51\n\t" \
  "v_smfmac_f32_16x16x64_bf16 v[68:71], v[12:15], a[56:63], v203\n\t" \
  "v_cvt_pk_bf16_f32 v60, v64, v65\n\t" \
  "v_cvt_pk_bf16_f32 v61, v66, v67\n\t" \
  "s_waitcnt vmcnt(5)\n\t" \
  "v_smfmac_f32_16x16x64_bf16 v[72:75], v[16:19], a[64:71], v203\n\t" \
  "v_cvt_pk_bf16_f32 v46, v52, v53\n\t" \
  "v_cvt_pk_bf16_f32 v47, v54, v55\n\t" \
  "v_smfmac_f32_16x16x64_bf16 v[88:91], v[16:19], a[72:79], v203\n\t" \
  "v_cvt_pk_bf16_f32 v62, v68, v69\n\t" \
  "v_cvt_pk_bf16_f32 v63, v70, v71\n\t" \
  "s_waitcnt vmcnt(4)\n\t" \
  "v_smfmac_f32_16x16x64_bf16 v[76:79], v[20:23], a[80:87], v203\n\t" \
  "v_cvt_pk_bf16_f32 v72, v72, v73\n\t" \
  "v_cvt_pk_bf16_f32 v73, v74, v75\n\t" \
  "v_smfmac_f32_16x16x64_bf16 v[92:95], v[20:23], a[88:95], v203\n\t" \
  "v_cvt_pk_bf16_f32 v88, v88, v89\n\t" \
  "v_cvt_pk_bf16_f32 v89, v90, v91\n\t" \
  "s_waitcnt vmcnt(3)\n\t" \
  "v_smfmac_f32_16x16x64_bf16 v[80:83], v[24:27], a[96:103], v203\n\t" \
  "v_cvt_pk_bf16_f32 v74, v76, v77\n\t" \
  "v_cvt_pk_bf16_f32 v75, v78, v79\n\t" \
  "v_smfmac_f32_16x16x64_bf16 v[96:99], v[24:27], a[104:111], v203\n\t" \
  "v_cvt_pk_bf16_f32 v90, v92, v93\n\t" \
  "v_cvt_pk_bf16_f32 v91, v94, v95\n\t" \
  "s_waitcnt vmcnt(2)\n\t" \
  "v_smfmac_f32_16x16x64_bf16 v[84:87], v[28:31], a[112:119], v203\n\t" \
  "v_cvt_pk_bf16_f32 v76, v80, v81\n\t" \
  "v_cvt_pk_bf16_f32 v77, v82, v83\n\t" \
  "v_smfmac_f32_16x16x64_bf16 v[100:103], v[28:31], a[120:127], v203\n\t" \
  "v_cvt_pk_bf16_f32 v92, v96, v97\n\t" \
  "v_cvt_pk_bf16_f32 v93, v98, v99\n\t" \
  "s_waitcnt vmcnt(1)\n\t" \
  "v_smfmac_f32_16x16x64_bf16 v[136:139], v[32:35], v[40:47], v204\n\t" \
  "v_cvt_pk_bf16_f32 v78, v84, v85\n\t" \
  "v_cvt_pk_bf16_f32 v79, v86, v87\n\t" \
  "v_smfmac_f32_16x16x64_bf16 v[140:143], v[32:35], v[56:63], v204\n\t" \
  "v_cvt_pk_bf16_f32 v94, v100, v101\n\t" \
  "v_cvt_pk_bf16_f32 v95, v102, v103\n\t" \
  "s_waitcnt vmcnt(0)\n\t" \
  "v_smfmac_f32_16x16x64_bf16 v[136:139], v[36:39], v[72:79], v204\n\t" \
  "v_smfmac_f32_16x16x64_bf16 v[140:143], v[36:39], v[88:95], v204\n\t" \
  "s_nop 3\n\t" \
  "v_permlane32_swap_b32 v136, v138\n\t" \
  "v_permlane32_swap_b32 v137, v139\n\t" \
  "v_add_f32 v136, v136, v138\n\t" \
  "v_add_f32 v137, v137, v139\n\t" \
  "v_permlane32_swap_b32 v140, v142\n\t" \
  "v_permlane32_swap_b32 v141, v143\n\t" \
  "v_add_f32 v138, v140, v142\n\t" \
  "v_add_f32 v139, v141, v143\n\t" \
  "ds_write2st64_b64 v197, v[136:137], v[138:139] offset1:2\n\t" \
  "s_waitcnt lgkmcnt(0)\n\t" \
  "s_barrier\n\t" \
  "s_barrier\n\t" \
  "ds_read2_b32 v[148:149], v207 offset1:8\n\t" \
  "s_waitcnt lgkmcnt(0)\n\t" \
  "v_or_b32 v148, v148, v149\n\t" \
  "v_cmp_ne_u32 vcc, 0, v148\n\t" \
  "s_cbranch_vccnz 12f\n\t" \
  "s_and_b32 s58, s58, 4\n\t" \
  "s_cmp_ge_i32 s40, 62\n\t" \
  "s_cbranch_scc1 26f\n\t" \
  "s_and_b32 s82, s40, 3\n\t" \
  "s_lshl_b32 s82, s82, 20\n\t" \
  "s_add_i32 s82, s82, s48\n\t" \
  "s_andn2_b32 s82, s82, 0x1800\n\t" \
  "s_lshr_b32 s80, s82, 21\n\t" \
  "s_bitcmp1_b64 %[sel], s80\n\t" \
  "s_cselect_b32 s80, 0x2000, 0\n\t" \
  "s_add_i32 s82, s82, s80\n\t" \
  "s_add_i32 s80, s82, 0x1000\n\t" \
  "v_mov_b64 v[188:189], -1\n\t" \
  "v_mov_b64 v[190:191], -1\n\t" \
  "s_sleep 8\n\t" \
  "buffer_store_dwordx4 v[188:191], v192, %[ru], s82 offen offset:0 sc1\n\t" \
  "buffer_store_dwordx4 v[188:191], v192, %[ru], s82 offen offset:1024 sc1\n\t" \
  "buffer_store_dwordx4 v[188:191], v192, %[ru], s82 offen offset:2048 sc1\n\t" \
  "buffer_store_dwordx4 v[188:191], v192, %[ru], s82 offen offset:3072 sc1\n\t" \
  "buffer_store_dwordx4 v[188:191], v192, %[ru], s80 offen offset:0 sc1\n\t" \
  "buffer_store_dwordx4 v[188:191], v192, %[ru], s80 offen offset:1024 sc1\n\t" \
  "buffer_store_dwordx4 v[188:191], v192, %[ru], s80 offen offset:2048 sc1\n\t" \
  "buffer_store_dwordx4 v[188:191], v192, %[ru], s80 offen offset:3072 sc1\n\t" \
  "26:\n\t" \
  "s_barrier\n\t" \
  "s_and_b32 s76, s40, 3\n\t" \
  "s_lshl_b32 s76, s76, 18\n\t" \
  "s_add_i32 s77, s76, 0x10000\n\t" \
  "s_add_i32 s78, s76, 0x1000\n\t" \
  "s_add_i32 s79, s76, 0x11000\n\t" \
  "31:\n\t" \
  "buffer_load_dwordx4 v[0:3], v210, %[rh], s76 offen offset:0 sc1\n\t" \
  "s_nop 10\n\t" \
  "buffer_load_dwordx4 v[4:7], v210, %[rh], s76 offen offset:1024 sc1\n\t" \
  "s_nop 10\n\t" \
  "buffer_load_dwordx4 v[8:11], v210, %[rh], s77 offen offset:0 sc1\n\t" \
  "s_nop 10\n\t" \
  "buffer_load_dwordx4 v[12:15], v210, %[rh], s77 offen offset:1024 sc1\n\t" \
  "s_nop 10\n\t" \
  "buffer_load_dwordx4 v[16:19], v210, %[rh], s76 offen offset:2048 sc1\n\t" \
  "s_nop 10\n\t" \
  "buffer_load_dwordx4 v[20:23], v210, %[rh], s76 offen offset:3072 sc1\n\t" \
  "s_nop 10\n\t" \
  "buffer_load_dwordx4 v[24:27], v210, %[rh], s77 offen offset:2048 sc1\n\t" \
  "s_nop 10\n\t" \
  "buffer_load_dwordx4 v[28:31], v210, %[rh], s77 offen offset:3072 sc1\n\t" \
  "s_nop 10\n\t" \
  "buffer_load_dwordx4 v[32:35], v210, %[rh], s78 offen offset:0 sc1\n\t" \
  "s_nop 10\n\t" \
  "buffer_load_dwordx4 v[36:39], v210, %[rh], s78 offen offset:1024 sc1\n\t" \
  "s_nop 10\n\t" \
  "buffer_load_dwordx4 v[40:43], v210, %[rh], s79 offen offset:0 sc1\n\t" \
  "s_nop 10\n\t" \
  "buffer_load_dwordx4 v[44:47], v210, %[rh], s79 offen offset:1024 sc1\n\t" \
  "s_nop 10\n\t" \
  "buffer_load_dwordx4 v[48:51], v210, %[rh], s78 offen offset:2048 sc1\n\t" \
  "s_nop 10\n\t" \
  "buffer_load_dwordx4 v[52:55], v210, %[rh], s78 offen offset:3072 sc1\n\t" \
  "s_nop 10\n\t" \
  "buffer_load_dwordx4 v[56:59], v210, %[rh], s79 offen offset:2048 sc1\n\t" \
  "s_nop 10\n\t" \
  "buffer_load_dwordx4 v[60:63], v210, %[rh], s79 offen offset:3072 sc1\n\t" \
  "s_nop 10\n\t" \
  "buffer_load_dwordx4 v[64:67], v211, %[rh], s76 offen offset:0 sc1\n\t" \
  "s_nop 10\n\t" \
  "buffer_load_dwordx4 v[68:71], v211, %[rh], s76 offen offset:1024 sc1\n\t" \
  "s_nop 10\n\t" \
  "buffer_load_dwordx4 v[72:75], v211, %[rh], s77 offen offset:0 sc1\n\t" \
  "s_nop 10\n\t" \
  "buffer_load_dwordx4 v[76:79], v211, %[rh], s77 offen offset:1024 sc1\n\t" \
  "s_nop 10\n\t" \
  "buffer_load_dwordx4 v[80:83], v211, %[rh], s76 offen offset:2048 sc1\n\t" \
  "s_nop 10\n\t" \
  "buffer_load_dwordx4 v[84:87], v211, %[rh], s76 offen offset:3072 sc1\n\t" \
  "s_nop 10\n\t" \
  "v_mov_b64 v[136:137], 0\n\t" \
  "v_mov_b64 v[138:139], 0\n\t" \
  "v_mov_b64 v[140:141], 0\n\t" \
  "v_mov_b64 v[142:143], 0\n\t" \
  "s_waitcnt vmcnt(20)\n\t" \
  "v_swap_b32 v1, v4\n\t" \
  "v_swap_b32 v3, v6\n\t" \
  "s_nop 1\n\t" \
  "v_mfma_f32_16x16x32_bf16 v[168:171], a[0:3], v[0:3], 0\n\t" \
  "buffer_load_dwordx4 v[88:91], v211, %[rh], s77 offen offset:2048 sc1\n\t" \
  "v_mfma_f32_16x16x32_bf16 v[172:175], a[8:11], v[0:3], 0\n\t" \
  "s_waitcnt vmcnt(19)\n\t" \
  "v_swap_b32 v9, v12\n\t" \
  "v_mfma_f32_16x16x32_bf16 v[168:171], a[4:7], v[4:7], v[168:171]\n\t" \
  "v_swap_b32 v11, v14\n\t" \
  "v_mfma_f32_16x16x32_bf16 v[172:175], a[12:15], v[4:7], v[172:175]\n\t" \
  "buffer_load_dwordx4 v[92:95], v211, %[rh], s77 offen offset:3072 sc1\n\t" \
  "v_mfma_f32_16x16x32_bf16 v[176:179], a[0:3], v[8:11], 0\n\t" \
  "buffer_load_dwordx4 v[96:99], v211, %[rh], s78 offen offset:0 sc1\n\t" \
  "v_mfma_f32_16x16x32_bf16 v[180:183], a[8:11], v[8:11], 0\n\t" \
  "s_waitcnt vmcnt(19)\n\t" \
  "v_swap_b32 v17, v20\n\t" \
  "v_mfma_f32_16x16x32_bf16 v[176:179], a[4:7], v[12:15], v[176:179]\n\t" \
  "v_swap_b32 v19, v22\n\t" \
  "v_mfma_f32_16x16x32_bf16 v[180:183], a[12:15], v[12:15], v[180:183]\n\t" \
  "buffer_load_dwordx4 v[100:103], v211, %[rh], s78 offen offset:1024 sc1\n\t" \
  "v_mfma_f32_16x16x32_bf16 v[168:171], a[16:19], v[16:19], v[168:171]\n\t" \
  "buffer_load_dwordx4 v[104:107], v211, %[rh], s79 offen offset:0 sc1\n\t" \
  "v_mfma_f32_16x16x32_bf16 v[172:175], a[24:27], v[16:19], v[172:175]\n\t" \
  "s_waitcnt vmcnt(19)\n\t" \
  "v_swap_b32 v25, v28\n\t" \
  "v_mfma_f32_16x16x32_bf16 v[168:171], a[20:23], v[20:23], v[168:171]\n\t" \
  "v_swap_b32 v27, v30\n\t" \
  "v_mfma_f32_16x16x32_bf16 v[172:175], a[28:31], v[20:23], v[172:175]\n\t" \
  "buffer_load_dwordx4 v[108:111], v211, %[rh], s79 offen offset:1024 sc1\n\t" \
  "v_mfma_f32_16x16x32_bf16 v[176:179], a[16:19], v[24:27], v[176:179]\n\t" \
  "buffer_load_dwordx4 v[112:115], v211, %[rh], s78 offen offset:2048 sc1\n\t" \
  "v_mfma_f32_16x16x32_bf16 v[180:183], a[24:27], v[24:27], v[180:183]\n\t" \
  "s_waitcnt vmcnt(19)\n\t" \
  "v_swap_b32 v33, v36\n\t" \
  "v_mfma_f32_16x16x32_bf16 v[176:179], a[20:23], v[28:31], v[176:179]\n\t" \
  "v_swap_b32 v35, v38\n\t" \
  "v_mfma_f32_16x16x32_bf16 v[180:183], a[28:31], v[28:31], v[180:183]\n\t" \
  "buffer_load_dwordx4 v[116:119], v211, %[rh], s78 offen offset:3072 sc1\n\t" \
  "v_mfma_f32_16x16x32_bf16 v[168:171], a[32:35], v[32:35], v[168:171]\n\t" \
  "buffer_load_dwordx4 v[120:123], v211, %[rh], s79 offen offset:2048 sc1\n\t" \
  "v_mfma_f32_16x16x32_bf16 v[172:175], a[40:43], v[32:35], v[172:175]\n\t" \
  "s_waitcnt vmcnt(19)\n\t" \
  "v_swap_b32 v41, v44\n\t" \
  "v_mfma_f32_16x16x32_bf16 v[168:171], a[36:39], v[36:39], v[168:171]\n\t" \
  "v_swap_b32 v43, v46\n\t" \
  "v_mfma_f32_16x16x32_bf16 v[172:175], a[44:47], v[36:39], v[172:175]\n\t" \
  "buffer_load_dwordx4 v[124:127], v211, %[rh], s79 offen offset:3072 sc1\n\t" \
  "v_mfma_f32_16x16x32_bf16 v[176:179], a[32:35], v[40:43], v[176:179]\n\t" \
  "v_mfma_f32_16x16x32_bf16 v[180:183], a[40:43], v[40:43], v[180:183]\n\t" \
  "s_waitcnt vmcnt(18)\n\t" \
  "v_swap_b32 v49, v52\n\t" \
  "v_mfma_f32_16x16x32_bf16 v[176:179], a[36:39], v[44:47], v[176:179]\n\t" \
  "v_swap_b32 v51, v54\n\t" \
  "v_mfma_f32_16x16x32_bf16 v[180:183], a[44:47], v[44:47], v[180:183]\n\t" \
  "v_mov_b64 v[40:41], 0\n\t" \
  "v_mov_b64 v[42:43], 0\n\t" \
  "v_mfma_f32_16x16x32_bf16 v[168:171], a[48:51], v[48:51], v[168:171]\n\t" \
  "v_mov_b64 v[44:45], 0\n\t" \
  "v_mov_b64 v[46:47], 0\n\t" \
  "v_mfma_f32_16x16x32_bf16 v[172:175], a[56:59], v[48:51], v[172:175]\n\t" \
  "s_waitcnt vmcnt(16)\n\t" \
  "v_swap_b32 v57, v60\n\t" \
  "v_mfma_f32_16x16x32_bf16 v[168:171], a[52:55], v[52:55], v[168:171]\n\t" \
  "v_swap_b32 v59, v62\n\t" \
  "v_mfma_f32_16x16x32_bf16 v[172:175], a[60:63], v[52:55], v[172:175]\n\t" \
  "v_mov_b64 v[48:49], 0\n\t" \
  "v_mov_b64 v[50:51], 0\n\t" \
  "v_mfma_f32_16x16x32_bf16 v[176:179], a[48:51], v[56:59], v[176:179]\n\t" \
  "v_mov_b64 v[52:53], 0\n\t" \
  "v_mov_b64 v[54:55], 0\n\t" \
  "v_mfma_f32_16x16x32_bf16 v[180:183], a[56:59], v[56:59], v[180:183]\n\t" \
  "s_waitcnt vmcnt(14)\n\t" \
  "v_swap_b32 v65, v68\n\t" \
  "v_mfma_f32_16x16x32_bf16 v[176:179], a[52:55], v[60:63], v[176:179]\n\t" \
  "v_swap_b32 v67, v70\n\t" \
  "v_mfma_f32_16x16x32_bf16 v[180:183], a[60:63], v[60:63], v[180:183]\n\t" \
  "v_mov_b64 v[56:57], 0\n\t" \
  "v_mov_b64 v[58:59], 0\n\t" \
  "v_mfma_f32_16x16x32_bf16 v[168:171], a[64:67], v[64:67], v[168:171]\n\t" \
  "v_mov_b64 v[60:61], 0\n\t" \
  "v_mov_b64 v[62:63], 0\n\t" \
  "v_mfma_f32_16x16x32_bf16 v[172:175], a[72:75], v[64:67], v[172:175]\n\t" \
  "s_waitcnt vmcnt(12)\n\t" \
  "v_swap_b32 v73, v76\n\t" \
  "v_mfma_f32_16x16x32_bf16 v[168:171], a[68:71], v[68:71], v[168:171]\n\t" \
  "v_swap_b32 v75, v78\n\t" \
  "v_mfma_f32_16x16x32_bf16 v[172:175], a[76:79], v[68:71], v[172:175]\n\t" \
  "v_mov_b64 v[64:65], 0\n\t" \
  "v_mov_b64 v[66:67], 0\n\t" \
  "v_mfma_f32_16x16x32_bf16 v[176:179], a[64:67], v[72:75], v[176:179]\n\t" \
  "v_mov_b64 v[68:69], 0\n\t" \
  "v_mov_b64 v[70:71], 0\n\t" \
  "v_mfma_f32_16x16x32_bf16 v[180:183], a[72:75], v[72:75], v[180:183]\n\t" \
  "s_waitcnt vmcnt(10)\n\t" \
  "v_swap_b32 v81, v84\n\t" \
  "v_mfma_f32_16x16x32_bf16 v[176:179], a[68:71], v[76:79], v[176:179]\n\t" \
  "v_swap_b32 v83, v86\n\t" \
  "v_mfma_f32_16x16x32_bf16 v[180:183], a[76:79], v[76:79], v[180:183]\n\t" \
  "v_mov_b64 v[72:73], 0\n\t" \
  "v_mov_b64 v[74:75], 0\n\t" \
  "v_mfma_f32_16x16x32_bf16 v[168:171], a[80:83], v[80:83], v[168:171]\n\t" \
  "v_mov_b64 v[76:77], 0\n\t" \
  "v_mov_b64 v[78:79], 0\n\t" \
  "v_mfma_f32_16x16x32_bf16 v[172:175], a[88:91], v[80:83], v[172:175]\n\t" \
  "s_waitcnt vmcnt(8)\n\t" \
  "v_swap_b32 v89, v92\n\t" \
  "v_mfma_f32_16x16x32_bf16 v[168:171], a[84:87], v[84:87], v[168:171]\n\t" \
  "v_swap_b32 v91, v94\n\t" \
  "v_mfma_f32_16x16x32_bf16 v[172:175], a[92:95], v[84:87], v[172:175]\n\t" \
  "v_mov_b64 v[80:81], 0\n\t" \
  "v_mov_b64 v[82:83], 0\n\t" \
  "v_mfma_f32_16x16x32_bf16 v[176:179], a[80:83], v[88:91], v[176:179]\n\t" \
  "v_mov_b64 v[84:85], 0\n\t" \
  "v_mov_b64 v[86:87], 0\n\t" \
  "v_mfma_f32_16x16x32_bf16 v[180:183], a[88:91], v[88:91], v[180:183]\n\t" \
  "s_waitcnt vmcnt(6)\n\t" \
  "v_swap_b32 v97, v100\n\t" \
  "v_mfma_f32_16x16x32_bf16 v[176:179], a[84:87], v[92:95], v[176:179]\n\t" \
  "v_swap_b32 v99, v102\n\t" \
  "v_mfma_f32_16x16x32_bf16 v[180:183], a[92:95], v[92:95], v[180:183]\n\t" \
  "v_mov_b64 v[88:89], 0\n\t" \
  "v_mov_b64 v[90:91], 0\n\t" \
  "v_mfma_f32_16x16x32_bf16 v[168:171], a[96:99], v[96:99], v[168:171]\n\t" \
  "v_mov_b64 v[92:93], 0\n\t" \
  "v_mov_b64 v[94:95], 0\n\t" \
  "v_mfma_f32_16x16x32_bf16 v[172:175], a[104:107], v[96:99], v[172:175]\n\t" \
  "s_waitcnt vmcnt(4)\n\t" \
  "v_swap_b32 v105, v108\n\t" \
  "v_mfma_f32_16x16x32_bf16 v[168:171], a[100:103], v[100:103], v[168:171]\n\t" \
  "v_swap_b32 v107, v110\n\t" \
  "v_mfma_f32_16x16x32_bf16 v[172:175], a[108:111], v[100:103], v[172:175]\n\t" \
  "v_mov_b64 v[96:97], 0\n\t" \
  "v_mov_b64 v[98:99], 0\n\t" \
  "v_mfma_f32_16x16x32_bf16 v[176:179], a[96:99], v[104:107], v[176:179]\n\t" \
  "v_mov_b64 v[100:101], 0\n\t" \
  "v_mov_b64 v[102:103], 0\n\t" \
  "v_mfma_f32_16x16x32_bf16 v[180:183], a[104:107], v[104:107], v[180:183]\n\t" \
  "s_waitcnt vmcnt(2)\n\t" \
  "v_swap_b32 v113, v116\n\t" \
  "v_mfma_f32_16x16x32_bf16 v[176:179], a[100:103], v[108:111], v[176:179]\n\t" \
  "v_swap_b32 v115, v118\n\t" \
  "v_mfma_f32_16x16x32_bf16 v[180:183], a[108:111], v[108:111], v[180:183]\n\t" \
  "s_nop 0\n\t" \
  "v_mfma_f32_16x16x32_bf16 v[168:171], a[112:115], v[112:115], v[168:171]\n\t" \
  "v_mfma_f32_16x16x32_bf16 v[172:175], a[120:123], v[112:115], v[172:175]\n\t" \
  "s_waitcnt vmcnt(0)\n\t" \
  "v_swap_b32 v121, v124\n\t" \
  "v_mfma_f32_16x16x32_bf16 v[168:171], a[116:119], v[116:119], v[168:171]\n\t" \
  "v_swap_b32 v123, v126\n\t" \
  "v_mfma_f32_16x16x32_bf16 v[172:175], a[124:127], v[116:119], v[172:175]\n\t" \
  "s_nop 0\n\t" \
  "v_mfma_f32_16x16x32_bf16 v[176:179], a[112:115], v[120:123], v[176:179]\n\t" \
  "v_mfma_f32_16x16x32_bf16 v[180:183], a[120:123], v[120:123], v[180:183]\n\t" \
  "v_mfma_f32_16x16x32_bf16 v[176:179], a[116:119], v[124:127], v[176:179]\n\t" \
  "v_mfma_f32_16x16x32_bf16 v[180:183], a[124:127], v[124:127], v[180:183]\n\t" \
  "s_nop 6\n\t" \
  "39:\n\t" \
  "s_add_i32 s40, s40, 2\n\t" \
  "v_cmp_u_f32 vcc, v168, v176\n\t" \
  "s_cbranch_vccnz 32f\n\t" \
  "33:\n\t" \
  "s_barrier\n\t" \
  "s_add_i32 s85, s40, -1\n\t" \
  "s_and_b32 s82, s85, 2\n\t" \
  "s_lshl_b32 s82, s82, 13\n\t" \
  "s_and_b32 s80, s85, 1\n\t" \
  "s_lshl_b32 s80, s80, 12\n\t" \
  "s_add_i32 s82, s82, s80\n\t" \
  "s_add_i32 s82, s82, 0x84000\n\t" \
  "s_mov_b32 exec_hi, 0\n\t" \
  "buffer_load_dword v184, v200, %[rh], s82 offen sc1\n\t" \
  "buffer_load_dword v185, v200, %[rh], s52 offen sc1\n\t" \
  "s_mov_b64 exec, -1\n\t" \
  "ds_read_b128 a[0:3], v194 offset:0\n\t" \
  "ds_read_b128 a[4:7], v194 offset:1024\n\t" \
  "ds_read_b128 a[8:11], v194 offset:2048\n\t" \
  "ds_read_b128 a[12:15], v194 offset:3072\n\t" \
  "ds_read_b128 a[16:19], v194 offset:4096\n\t" \
  "ds_read_b128 a[20:23], v194 offset:5120\n\t" \
  "ds_read_b128 a[24:27], v194 offset:6144\n\t" \
  "ds_read_b128 a[28:31], v194 offset:7168\n\t" \
  "ds_read_b128 a[32:35], v194 offset:8192\n\t" \
  "ds_read_b128 a[36:39], v194 offset:9216\n\t" \
  "ds_read_b128 a[40:43], v194 offset:10240\n\t" \
  "ds_read_b128 a[44:47], v194 offset:11264\n\t" \
  "ds_read_b128 a[48:51], v194 offset:12288\n\t" \
  "ds_read_b128 a[52:55], v194 offset:13312\n\t" \
  "ds_read_b128 a[56:59], v194 offset:14336\n\t" \
  "ds_read_b128 a[60:63], v194 offset:15360\n\t" \
  "ds_read_b128 a[64:67], v194 offset:16384\n\t" \
  "ds_read_b128 a[68:71], v194 offset:17408\n\t" \
  "ds_read_b128 a[72:75], v194 offset:18432\n\t" \
  "ds_read_b128 a[76:79], v194 offset:19456\n\t" \
  "ds_read_b128 a[80:83], v194 offset:20480\n\t" \
  "ds_read_b128 a[84:87], v194 offset:21504\n\t" \
  "ds_read_b128 a[88:91], v194 offset:22528\n\t" \
  "ds_read_b128 a[92:95], v194 offset:23552\n\t" \
  "ds_read_b128 a[96:99], v194 offset:24576\n\t" \
  "ds_read_b128 a[100:103], v194 offset:25600\n\t" \
  "ds_read_b128 a[104:107], v194 offset:26624\n\t" \
  "ds_read_b128 a[108:111], v194 offset:27648\n\t" \
  "ds_read_b128 a[112:115], v194 offset:28672\n\t" \
  "ds_read_b128 a[116:119], v194 offset:29696\n\t" \
  "ds_read_b128 a[120:123], v194 offset:30720\n\t" \
  "ds_read_b128 a[124:127], v194 offset:31744\n\t" \
  "ds_write_b128 v199, v[168:171] offset:0\n\t" \
  "ds_write_b128 v199, v[172:175] offset:1024\n\t" \
  "ds_write_b128 v199, v[176:179] offset:8192\n\t" \
  "ds_write_b128 v199, v[180:183] offset:9216\n\t" \
  "s_mov_b32 s59, 0\n\t" \
  "s_and_b32 s58, s58, 4\n\t" \
  "s_cmp_lt_i32 s40, 64\n\t" \
  "s_cbranch_scc1 20b\n\t" \
  "s_waitcnt lgkmcnt(0)\n\t" \
  "s_barrier\n\t" \
  "s_mov_b32 s58, 64\n\t" \
  "s_branch 7f\n\t" \
  "22:\n\t" \
  "s_mov_b32 s81, 0\n\t" \
  "s_cmp_eq_u32 s40, 0\n\t" \
  "s_cbranch_scc1 24b\n\t" \
  "s_bitcmp1_b32 s58, 2\n\t" \
  "s_cbranch_scc1 24b\n\t" \
  "s_mov_b64 exec, -1\n\t" \
  "s_sleep 10\n\t" \
  "s_add_u32 s59, s59, 0x10000\n\t" \
  "s_cmp_lt_u32 s59, 0x100000\n\t" \
  "s_cbranch_scc1 19b\n\t" \
  "s_or_b32 s58, s58, 1\n\t" \
  "s_branch 7f\n\t" \
  "14:\n\t" \
  "v_mov_b32 v184, 0\n\t" \
  "s_bitcmp1_b32 s58, 2\n\t" \
  "s_nop 0\n\t" \
  "s_cbranch_scc1 15b\n\t" \
  "s_mov_b32 s84, 950\n\t" \
  "s_add_u32 s59, s59, 0x10000\n\t" \
  "s_cmp_lt_u32 s59, 0x100000\n\t" \
  "s_mov_b64 exec, vcc\n\t" \
  "buffer_load_dword v187, v200, %[rh], s78 offen sc1\n\t" \
  "s_mov_b32 exec_lo, -1\n\t" \
  "s_cbranch_scc1 13b\n\t" \
  "s_mov_b64 exec, -1\n\t" \
  "s_or_b32 s58, s58, 1\n\t" \
  "s_branch 7f\n\t" \
  "37:\n\t" \
  "s_barrier\n\t" \
  "v_mov_b64 v[40:41], 0\n\t" \
  "v_mov_b64 v[42:43], 0\n\t" \
  "v_mov_b64 v[44:45], 0\n\t" \
  "v_mov_b64 v[46:47], 0\n\t" \
  "v_mov_b64 v[48:49], 0\n\t" \
  "v_mov_b64 v[50:51], 0\n\t" \
  "v_mov_b64 v[52:53], 0\n\t" \
  "v_mov_b64 v[54:55], 0\n\t" \
  "v_mov_b64 v[56:57], 0\n\t" \
  "v_mov_b64 v[58:59], 0\n\t" \
  "v_mov_b64 v[60:61], 0\n\t" \
  "v_mov_b64 v[62:63], 0\n\t" \
  "v_mov_b64 v[64:65], 0\n\t" \
  "v_mov_b64 v[66:67], 0\n\t" \
  "v_mov_b64 v[68:69], 0\n\t" \
  "v_mov_b64 v[70:71], 0\n\t" \
  "v_mov_b64 v[72:73], 0\n\t" \
  "v_mov_b64 v[74:75], 0\n\t" \
  "v_mov_b64 v[76:77], 0\n\t" \
  "v_mov_b64 v[78:79], 0\n\t" \
  "v_mov_b64 v[80:81], 0\n\t" \
  "v_mov_b64 v[82:83], 0\n\t" \
  "v_mov_b64 v[84:85], 0\n\t" \
  "v_mov_b64 v[86:87], 0\n\t" \
  "v_mov_b64 v[88:89], 0\n\t" \
  "v_mov_b64 v[90:91], 0\n\t" \
  "v_mov_b64 v[92:93], 0\n\t" \
  "v_mov_b64 v[94:95], 0\n\t" \
  "v_mov_b64 v[96:97], 0\n\t" \
  "v_mov_b64 v[98:99], 0\n\t" \
  "v_mov_b64 v[100:101], 0\n\t" \
  "v_mov_b64 v[102:103], 0\n\t" \
  "v_mov_b64 v[136:137], 0\n\t" \
  "v_mov_b64 v[138:139], 0\n\t" \
  "v_mov_b64 v[140:141], 0\n\t" \
  "v_mov_b64 v[142:143], 0\n\t" \
  "s_add_i32 s40, s40, 2\n\t" \
  "s_branch 33b\n\t" \
  "32:\n\t" \
  "s_and_b32 s80, s58, 36\n\t" \
  "s_cbranch_scc1 33b\n\t" \
  "s_add_i32 s40, s40, -2\n\t" \
  "34:\n\t" \
  "s_or_b32 s59, s59, 128\n\t" \
  "s_and_b32 s76, s40, 3\n\t" \
  "s_lshl_b32 s76, s76, 18\n\t" \
  "s_add_i32 s77, s76, 0x10000\n\t" \
  "s_add_i32 s78, s76, 0x1000\n\t" \
  "s_add_i32 s79, s76, 0x11000\n\t" \
  "35:\n\t" \
  "buffer_load_dwordx4 v[0:3], v210, %[rh], s76 offen offset:0 sc1\n\t" \
  "buffer_load_dwordx4 v[4:7], v210, %[rh], s76 offen offset:1024 sc1\n\t" \
  "buffer_load_dwordx4 v[8:11], v210, %[rh], s77 offen offset:0 sc1\n\t" \
  "buffer_load_dwordx4 v[12:15], v210, %[rh], s77 offen offset:1024 sc1\n\t" \
  "buffer_load_dwordx4 v[16:19], v210, %[rh], s76 offen offset:2048 sc1\n\t" \
  "buffer_load_dwordx4 v[20:23], v210, %[rh], s76 offen offset:3072 sc1\n\t" \
  "buffer_load_dwordx4 v[24:27], v210, %[rh], s77 offen offset:2048 sc1\n\t" \
  "buffer_load_dwordx4 v[28:31], v210, %[rh], s77 offen offset:3072 sc1\n\t" \
  "buffer_load_dwordx4 v[32:35], v210, %[rh], s78 offen offset:0 sc1\n\t" \
  "buffer_load_dwordx4 v[36:39], v210, %[rh], s78 offen offset:1024 sc1\n\t" \
  "buffer_load_dwordx4 v[40:43], v210, %[rh], s79 offen offset:0 sc1\n\t" \
  "buffer_load_dwordx4 v[44:47], v210, %[rh], s79 offen offset:1024 sc1\n\t" \
  "buffer_load_dwordx4 v[48:51], v210, %[rh], s78 offen offset:2048 sc1\n\t" \
  "buffer_load_dwordx4 v[52:55], v210, %[rh], s78 offen offset:3072 sc1\n\t" \
  "buffer_load_dwordx4 v[56:59], v210, %[rh], s79 offen offset:2048 sc1\n\t" \
  "buffer_load_dwordx4 v[60:63], v210, %[rh], s79 offen offset:3072 sc1\n\t" \
  "buffer_load_dwordx4 v[64:67], v211, %[rh], s76 offen offset:0 sc1\n\t" \
  "buffer_load_dwordx4 v[68:71], v211, %[rh], s76 offen offset:1024 sc1\n\t" \
  "buffer_load_dwordx4 v[72:75], v211, %[rh], s77 offen offset:0 sc1\n\t" \
  "buffer_load_dwordx4 v[76:79], v211, %[rh], s77 offen offset:1024 sc1\n\t" \
  "buffer_load_dwordx4 v[80:83], v211, %[rh], s76 offen offset:2048 sc1\n\t" \
  "buffer_load_dwordx4 v[84:87], v211, %[rh], s76 offen offset:3072 sc1\n\t" \
  "buffer_load_dwordx4 v[88:91], v211, %[rh], s77 offen offset:2048 sc1\n\t" \
  "buffer_load_dwordx4 v[92:95], v211, %[rh], s77 offen offset:3072 sc1\n\t" \
  "buffer_load_dwordx4 v[96:99], v211, %[rh], s78 offen offset:0 sc1\n\t" \
  "buffer_load_dwordx4 v[100:103], v211, %[rh], s78 offen offset:1024 sc1\n\t" \
  "buffer_load_dwordx4 v[104:107], v211, %[rh], s79 offen offset:0 sc1\n\t" \
  "buffer_load_dwordx4 v[108:111], v211, %[rh], s79 offen offset:1024 sc1\n\t" \
  "buffer_load_dwordx4 v[112:115], v211, %[rh], s78 offen offset:2048 sc1\n\t" \
  "buffer_load_dwordx4 v[116:119], v211, %[rh], s78 offen offset:3072 sc1\n\t" \
  "buffer_load_dwordx4 v[120:123], v211, %[rh], s79 offen offset:2048 sc1\n\t" \
  "buffer_load_dwordx4 v[124:127], v211, %[rh], s79 offen offset:3072 sc1\n\t" \
  "s_waitcnt vmcnt(0)\n\t" \
  "s_bitcmp1_b32 s58, 2\n\t" \
  "s_cbranch_scc1 36f\n\t" \
  "v_max_u32 v148, v0, v1\n\t" \
  "v_max3_u32 v148, v148, v2, v3\n\t" \
  "v_max3_u32 v148, v148, v4, v5\n\t" \
  "v_max3_u32 v148, v148, v6, v7\n\t" \
  "v_max3_u32 v148, v148, v8, v9\n\t" \
  "v_max3_u32 v148, v148, v10, v11\n\t" \
  "v_max3_u32 v148, v148, v12, v13\n\t" \
  "v_max3_u32 v148, v148, v14, v15\n\t" \
  "v_max3_u32 v148, v148, v16, v17\n\t" \
  "v_max3_u32 v148, v148, v18, v19\n\t" \
  "v_max3_u32 v148, v148, v20, v21\n\t" \
  "v_max3_u32 v148, v148, v22, v23\n\t" \
  "v_max3_u32 v148, v148, v24, v25\n\t" \
  "v_max3_u32 v148, v148, v26, v27\n\t" \
  "v_max3_u32 v148, v148, v28, v29\n\t" \
  "v_max3_u32 v148, v148, v30, v31\n\t" \
  "v_max3_u32 v148, v148, v32, v33\n\t" \
  "v_max3_u32 v148, v148, v34, v35\n\t" \
  "v_max3_u32 v148, v148, v36, v37\n\t" \
  "v_max3_u32 v148, v148, v38, v39\n\t" \
  "v_max3_u32 v148, v148, v40, v41\n\t" \
  "v_max3_u32 v148, v148, v42, v43\n\t" \
  "v_max3_u32 v148, v148, v44, v45\n\t" \
  "v_max3_u32 v148, v148, v46, v47\n\t" \
  "v_max3_u32 v148, v148, v48, v49\n\t" \
  "v_max3_u32 v148, v148, v50, v51\n\t" \
  "v_max3_u32 v148, v148, v52, v53\n\t" \
  "v_max3_u32 v148, v148, v54, v55\n\t" \
  "v_max3_u32 v148, v148, v56, v57\n\t" \
  "v_max3_u32 v148, v148, v58, v59\n\t" \
  "v_max3_u32 v148, v148, v60, v61\n\t" \
  "v_max3_u32 v148, v148, v62, v63\n\t" \
  "v_max3_u32 v148, v148, v64, v65\n\t" \
  "v_max3_u32 v148, v148, v66, v67\n\t" \
  "v_max3_u32 v148, v148, v68, v69\n\t" \
  "v_max3_u32 v148, v148, v70, v71\n\t" \
  "v_max3_u32 v148, v148, v72, v73\n\t" \
  "v_max3_u32 v148, v148, v74, v75\n\t" \
  "v_max3_u32 v148, v148, v76, v77\n\t" \
  "v_max3_u32 v148, v148, v78, v79\n\t" \
  "v_max3_u32 v148, v148, v80, v81\n\t" \
  "v_max3_u32 v148, v148, v82, v83\n\t" \
  "v_max3_u32 v148, v148, v84, v85\n\t" \
  "v_max3_u32 v148, v148, v86, v87\n\t" \
  "v_max3_u32 v148, v148, v88, v89\n\t" \
  "v_max3_u32 v148, v148, v90, v91\n\t" \
  "v_max3_u32 v148, v148, v92, v93\n\t" \
  "v_max3_u32 v148, v148, v94, v95\n\t" \
  "v_max3_u32 v148, v148, v96, v97\n\t" \
  "v_max3_u32 v148, v148, v98, v99\n\t" \
  "v_max3_u32 v148, v148, v100, v101\n\t" \
  "v_max3_u32 v148, v148, v102, v103\n\t" \
  "v_max3_u32 v148, v148, v104, v105\n\t" \
  "v_max3_u32 v148, v148, v106, v107\n\t" \
  "v_max3_u32 v148, v148, v108, v109\n\t" \
  "v_max3_u32 v148, v148, v110, v111\n\t" \
  "v_max3_u32 v148, v148, v112, v113\n\t" \
  "v_max3_u32 v148, v148, v114, v115\n\t" \
  "v_max3_u32 v148, v148, v116, v117\n\t" \
  "v_max3_u32 v148, v148, v118, v119\n\t" \
  "v_max3_u32 v148, v148, v120, v121\n\t" \
  "v_max3_u32 v148, v148, v122, v123\n\t" \
  "v_max3_u32 v148, v148, v124, v125\n\t" \
  "v_max3_u32 v148, v148, v126, v127\n\t" \
  "v_cmp_eq_u32 vcc, -1, v148\n\t" \
  "s_cbranch_vccz 36f\n\t" \
  "s_add_u32 s59, s59, 0x1000000\n\t" \
  "s_cmp_lt_u32 s59, 0x10000000\n\t" \
  "s_cbranch_scc1 35b\n\t" \
  "s_or_b32 s58, s58, 9\n\t" \
  "s_branch 7f\n\t" \
  "36:\n\t" \
  "s_and_b32 s58, s58, 4\n\t" \
  "s_or_b32 s58, s58, 32\n\t" \
  "s_branch 31b\n\t" \
  "18:\n\t" \
  "v_mov_b32 v186, -1\n\t" \
  "s_and_b32 s80, s40, 3\n\t" \
  "s_mul_i32 s77, s80, 0xc000\n\t" \
  "s_add_i32 s77, s77, 0x40000\n\t" \
  "s_lshl_b32 s78, s80, 14\n\t" \
  "s_add_i32 s78, s78, 0x70000\n\t" \
  "s_mov_b32 s57, 0\n\t" \
  "s_add_i32 s96, s77, 0x1000\n\t" \
  "s_and_b32 s98, s40, 2\n\t" \
  "s_lshl_b32 s98, s98, 13\n\t" \
  "s_and_b32 s80, s40, 1\n\t" \
  "s_lshl_b32 s80, s80, 12\n\t" \
  "s_add_i32 s98, s98, s80\n\t" \
  "s_add_i32 s98, s98, 0x84000\n\t" \
  "12:\n\t" \
  "s_or_b32 s58, s58, 3\n\t" \
  "11:\n\t" \
  "buffer_load_dwordx4 v[0:3], v193, %[rh], s77 offen offset:0 sc1\n\t" \
  "buffer_load_dwordx4 v[4:7], v193, %[rh], s77 offen offset:1024 sc1\n\t" \
  "buffer_load_dwordx4 v[8:11], v193, %[rh], s77 offen offset:2048 sc1\n\t" \
  "buffer_load_dwordx4 v[12:15], v193, %[rh], s77 offen offset:3072 sc1\n\t" \
  "buffer_load_dwordx4 v[16:19], v193, %[rh], s96 offen offset:0 sc1\n\t" \
  "buffer_load_dwordx4 v[20:23], v193, %[rh], s96 offen offset:1024 sc1\n\t" \
  "buffer_load_dwordx4 v[24:27], v193, %[rh], s96 offen offset:2048 sc1\n\t" \
  "buffer_load_dwordx4 v[28:31], v193, %[rh], s96 offen offset:3072 sc1\n\t" \
  "buffer_load_dwordx4 v[32:35], v195, %[rh], s77 offen offset:0 sc1\n\t" \
  "buffer_load_dwordx4 v[36:39], v195, %[rh], s77 offen offset:2048 sc1\n\t" \
  "v_mov_b64 v[40:41], 0\n\t" \
  "v_mov_b64 v[42:43], 0\n\t" \
  "v_mov_b64 v[44:45], 0\n\t" \
  "v_mov_b64 v[46:47], 0\n\t" \
  "v_mov_b64 v[48:49], 0\n\t" \
  "v_mov_b64 v[50:51], 0\n\t" \
  "v_mov_b64 v[52:53], 0\n\t" \
  "v_mov_b64 v[54:55], 0\n\t" \
  "v_mov_b64 v[56:57], 0\n\t" \
  "v_mov_b64 v[58:59], 0\n\t" \
  "v_mov_b64 v[60:61], 0\n\t" \
  "v_mov_b64 v[62:63], 0\n\t" \
  "v_mov_b64 v[64:65], 0\n\t" \
  "v_mov_b64 v[66:67], 0\n\t" \
  "v_mov_b64 v[68:69], 0\n\t" \
  "v_mov_b64 v[70:71], 0\n\t" \
  "v_mov_b64 v[72:73], 0\n\t" \
  "v_mov_b64 v[74:75], 0\n\t" \
  "v_mov_b64 v[76:77], 0\n\t" \
  "v_mov_b64 v[78:79], 0\n\t" \
  "v_mov_b64 v[80:81], 0\n\t" \
  "v_mov_b64 v[82:83], 0\n\t" \
  "v_mov_b64 v[84:85], 0\n\t" \
  "v_mov_b64 v[86:87], 0\n\t" \
  "v_mov_b64 v[88:89], 0\n\t" \
  "v_mov_b64 v[90:91], 0\n\t" \
  "v_mov_b64 v[92:93], 0\n\t" \
  "v_mov_b64 v[94:95], 0\n\t" \
  "v_mov_b64 v[96:97], 0\n\t" \
  "v_mov_b64 v[98:99], 0\n\t" \
  "v_mov_b64 v[100:101], 0\n\t" \
  "v_mov_b64 v[102:103], 0\n\t" \
  "v_mov_b64 v[136:137], 0\n\t" \
  "v_mov_b64 v[138:139], 0\n\t" \
  "v_mov_b64 v[140:141], 0\n\t" \
  "v_mov_b64 v[142:143], 0\n\t" \
  "s_waitcnt vmcnt(0)\n\t" \
  "s_add_u32 s59, s59, 1\n\t" \
  "s_bitcmp1_b32 s58, 2\n\t" \
  "s_cbranch_scc1 10b\n\t" \
  "v_max_u32 v148, v0, v1\n\t" \
  "v_max3_u32 v148, v148, v2, v3\n\t" \
  "v_max3_u32 v148, v148, v4, v5\n\t" \
  "v_max3_u32 v148, v148, v6, v7\n\t" \
  "v_max3_u32 v148, v148, v8, v9\n\t" \
  "v_max3_u32 v148, v148, v10, v11\n\t" \
  "v_max3_u32 v148, v148, v12, v13\n\t" \
  "v_max3_u32 v148, v148, v14, v15\n\t" \
  "v_max3_u32 v148, v148, v16, v17\n\t" \
  "v_max3_u32 v148, v148, v18, v19\n\t" \
  "v_max3_u32 v148, v148, v20, v21\n\t" \
  "v_max3_u32 v148, v148, v22, v23\n\t" \
  "v_max3_u32 v148, v148, v24, v25\n\t" \
  "v_max3_u32 v148, v148, v26, v27\n\t" \
  "v_max3_u32 v148, v148, v28, v29\n\t" \
  "v_max3_u32 v148, v148, v30, v31\n\t" \
  "v_max3_u32 v148, v148, v32, v33\n\t" \
  "v_max3_u32 v148, v148, v34, v35\n\t" \
  "v_max3_u32 v148, v148, v36, v37\n\t" \
  "v_max3_u32 v148, v148, v38, v39\n\t" \
  "v_cmp_eq_u32 vcc, -1, v148\n\t" \
  "s_cbranch_vccz 10b\n\t" \
  "s_bitcmp1_b32 s59, 6\n\t" \
  "s_cbranch_scc0 11b\n\t" \
  "7:\n\t" \
  "92:\n\t" \
  "s_mov_b32 s42, s58\n\t" \
  "s_waitcnt vmcnt(0) lgkmcnt(0)\n\t" \
  ""

#define V53 "v0", "v1", "v2", "v3", "v4", "v5", "v6", "v7", "v8", "v9", "v10", "v11", "v12", "v13", "v14", "v15", "v16", "v17", \
  "v18", "v19", "v20", "v21", "v22", "v23", "v24", "v25", "v26", "v27", "v28", "v29", "v30", "v31", "v32", "v33", "v34", "v35", \
  "v36", "v37", "v38", "v39", "v40", "v41", "v42", "v43", "v44", "v45", "v46", "v47", "v48", "v49", "v50", "v51", "v52"
#define V100 V53, "v53", "v54", "v55", "v56", "v57", "v58", "v59", "v60", "v61", "v62", "v63", "v64", "v65", "v66", "v67", "v68", "v69", "v70", \
  "v71", "v72", "v73", "v74", "v75", "v76", "v77", "v78", "v79", "v80", "v81", "v82", "v83", "v84", "v85", "v86", "v87", "v88", \
  "v89", "v90", "v91", "v116", "v117", "v118", "v119"
#define STEP_PV
#define CB_CL "s60", "s61", "s62", "s63", "s64", "s65", "s66", "s67", "s68", "s69",
#define STEPS(BODY, ...) asm volatile(BODY : "=&{v[92:99]}"(X), "+{s[40:55]}"(P) STEP_PV \
  : "{v[120:151]}"(W.v[0]), "{v[152:183]}"(W.v[1]), "{v[184:215]}"(W.v[2]), "{v[216:247]}"(W.v[3]), "{v[100:115]}"(C), [rh] "s"(rh), [wv] "s"(w), [ru] "s"(rum), [sel] "s"(sel) __VA_ARGS__ \
  : "memory", "vcc", "scc", "s56", "s57", "s58", "s59", "s76", "s77", "s78", "s79", "s80", "s81", "s82", "s83", "s84", "s85", "s86", "s87", "s88", "s89", \
    "s90", "s91", "s92", "s93", "s94", "s95", "s96", "s97", V100)
__device__ __forceinline__ void keep_a(const Wts& W) {
#pragma unroll
  for (int i = 0; i < 32; ++i) asm volatile("" :: "a"(W.a[i]));
}
__device__ __forceinline__ char* lds_base(char* smem, int off) { asm volatile("" : "+v"(off)); return smem + off; }
__device__ __forceinline__ u32x2 tr4(unsigned p) { return __builtin_bit_cast(u32x2, __builtin_amdgcn_ds_read_tr16_b64_v4i16((LAS bf16x4*)(uintptr_t)p)); }
__device__ __forceinline__ float rb1(float x) { return __uint_as_float(pk2(0.f, x)); }

__device__ __forceinline__ void steps_init(u32x16& P, int h, int w, unsigned place) {
#pragma unroll
  for (int i = 0; i < 16; ++i) P[i] = 0;
  P[2] = 1; P[3] = CDUR; P[14] = H_MEM + (w >> 1) * CHK + (w & 1) * 4096 + (h & RHO) * 1024; P[15] = place;
}
__device__ __forceinline__ void steps_enter(u32x16& P, Clk& ck, bool dead) {
  ck.sync();
  P[2] = (P[2] & 3u) | (dead ? 4u : 0u); P[6] = ck.c2t << 15; P[11] = ck.ce; P[12] = ck.t2c; P[13] = ck.te;
#pragma unroll
  for (int i = 0; i < 16; ++i) P[i] = __builtin_amdgcn_readfirstlane(P[i]);
}

__device__ __forceinline__ void role_qk(const ChainArgs& a, int h, char* smem, const int w, Gate& G) {
  int lane = opaque_tid(0);
  const int b = h; (void)b;
  u64* prof = a.prof; (void)prof;
  PDECL(L_PRF, 4);
  const float CS = 0.08838834764831845f * 1.4426950408889634f;
  GAS unsigned* abortw = (GAS unsigned*)(a.sync + SY_ABORT);
  bool dead = false;
  Wts W;
  Clk ck; ck.init();
  u32x4 gv;
  if (QK_D) while (__builtin_amdgcn_s_memrealtime() - G.entry < QK_D) __builtin_amdgcn_s_sleep(8);
  load_weights<0>(a, h, w, lane, smem, W, G, gv);
  TP(1);
  if (!G.look(a.sync, gv)) return;
  PZERO;
  const Sel sel = G.sel;
  const __amdgpu_buffer_rsrc_t rh = rsrc_hot(a.sync, sel), rum = rsrc_u(a.sync);
  auto pm_o = [&](int l) { return 2 * CHK + h * 256 + (l & 1) * 128 + (l >> 3) * 16 + (l & 6) * 2; };
  auto fac = [&](int l, int hf) { return ((l & 7) >> 1) == 2 * (l >> 5) + ((h >> 1) & 1) && ((l >> 3) & 1) == hf ? 1.f : 0.f; };
  u32x16 C, P;
  {
    const int r = lane & 15, g = lane >> 4;
    const unsigned l0 = (unsigned)(uintptr_t)(LAS char*)smem;
    C[0] = lane * 16; C[1] = l0 + L_MEM + lane * 16; C[2] = l0 + L_WTS + w * 4096 + lane * 16; C[3] = C[2] + 65536;
    C[4] = (lane & 2) ? 0xEEEE : 0x4444;
    C[5] = l0 + L_XCH + (16 * w + 2 * (r >> 2) + (r & 1)) * 32 + g * 8 + ((r >> 1) & 1) * 4;
    C[6] = l0 + L_XK + (32 * w + r) * 16 + g * 4;
    C[7] = w == 3 ? H_PX + (h >> 3) * CHK + (h & 7) * 1024 + lane * 16 : w ? (2 * h + 1) * CHK + (w - 1) * 4096 + lane * 16 : l0 + L_XCH + (8 * g + (r >> 2)) * 32 + (r & 3) * 8;
    C[8] = l0 + L_XK + (8 * g + (r >> 2)) * 16 + (r & 1) * 8;
    C[9] = w ? (lane & 1) * 4096 : (lane == 0 ? H_ANN + h * 128 : 0x7fffffffu);
    C[10] = w == 0 ? (h >> 4) * CHK + (h & 15) * 512 + ((lane >> 1) & 1) * 256 + (lane >> 5) * 128 + (lane & 1) * 64 + ((lane >> 3) & 3) * 16 + ((lane >> 2) & 1) * 8 :
            w < 3 ? H_PX + ((w - 1) * 4 + (h >> 3)) * CHK + (h & 7) * 1024 + lane * 16 :
            lane < 32 ? (h >> 4) * CHK + (h & 15) * 512 + lane * 16 : (lane < 48 ? 2 * CHK + h * 256 + (lane - 32) * 16 : 0x7fffffffu);
    C[11] = w == 0 ? pm_o(lane) : (lane < (w == 3 ? 32 : 16) ? H_PX + (w == 3 ? 8 + g : 7 + w) * CHK + hpos(h) * 256 + r * 16 : 0x7fffffffu);
    C[12] = H_TR + (lane & 31) * 128; C[13] = l0 + L_PRF + w * 1024 + (lane >> 6);
    unsigned mk = 0;
#pragma unroll
    for (int i = 0; i < 8; ++i) mk |= (16 * ((i >> 1) & 1) + 8 * (g & 1) + 4 * (g >> 1) + 2 * (i & 1) + (i >> 2) > 16 * (w - 1) + r ? 1u : 0u) << i;
    C[14] = w ? mk : __float_as_uint(fac(lane, 0)); C[15] = __float_as_uint(fac(lane, 1));
    asm volatile("" : "+v"(C));
  }
  steps_init(P, h, w, 2 * h * CHK + (3 * w - 3) * 1024);
  u32x4 k0[2];
  start_wait(a.sync, abortw, lane, dead, CT_Q + ct_near(sel) + h, 1u, [&]() { for (int i = 0; i < 2; ++i) k0[i] = ld16(rum, x_slot(1, 0, h, sel) + (2 * w + i) * 1024 + lane * 16); });
  G.arrive(a.sync);
#pragma unroll
  for (int i = 0; i < 2; ++i) *(u32x4*)(smem + L_KX + (2 * w + i) * 1024 + lane * 16) = k0[i];
  SSTAMP(96 + h);
  TP(2);
  GAS unsigned* kcp = ctr(a, CT_Q + ct_near(sel) + h);
  unsigned spins = 0;
  while (true) {
    u32x8 X; u32x16 pv; (void)pv;
    steps_enter(P, ck, dead);
    STEPS(QK_BODY, , [cs] "s"(CS), [kc] "s"(kcp));
    keep_a(W);
    const unsigned res = __builtin_amdgcn_readfirstlane(P[2]), s = __builtin_amdgcn_readfirstlane(P[4]);
    lane = opaque_tid(0);
    if (res & 8u) {
      auto sw = [](float v, int k) { return __int_as_float(k == 0 ? __builtin_amdgcn_mov_dpp(__float_as_int(v), 0xB1, 0xf, 0xf, false) :
                                                           k == 1 ? __builtin_amdgcn_mov_dpp(__float_as_int(v), 0x4E, 0xf, 0xf, false) :
                                                                    __builtin_amdgcn_mov_dpp(__float_as_int(v), 0x141, 0xf, 0xf, false)); };
      float x[5], d = fmaxf(__uint_as_float(X[4]), 0.f);
      d = fmaxf(d, sw(d, 0)); d = fmaxf(d, sw(d, 1)); d = fmaxf(d, sw(d, 2));
#pragma unroll
      for (int i = 0; i < 5; ++i) x[i] = __builtin_amdgcn_exp2f(__uint_as_float(X[i]) - d);
      float z = ((x[0] + x[1]) + (x[2] + x[3])) + x[4];
      z += sw(z, 0); z += sw(z, 1); z += sw(z, 2);
      z = __builtin_amdgcn_rcpf(z);
      const float y = sw(x[4], 0), f = __builtin_amdgcn_exp2f(-d) * z;
      const unsigned pm = (lane & 1) ? pk2(fac(lane, 0) * f, fac(lane, 1) * f) : pk2(x[4] * z, y * z);
      if (!(__uint_as_float(X[5]) < 1.0e18f))
        __builtin_amdgcn_raw_buffer_store_b32(nosent(pm), rh, pm_o(lane), H_P + ((s - 1) & 3) * 3 * CHK, ST_AUX);
    }
    if (res & 64u) break;
    if (res & 32u) {
      u32x4 k[3];
      start_wait(a.sync, abortw, lane, dead, CT_Q + ct_near(sel) + h, s + 1, [&]() { for (int i = 0; i < 3; ++i) k[i] = ld16(rum, x_slot(1, s, h, sel) + min(3 * w - 3 + i, 7) * 1024 + lane * 16); });
#pragma unroll
      for (int i = 0; i < 3; ++i) *(u32x4*)(smem + L_KX + min(3 * w - 3 + i, 7) * 1024 + lane * 16) = k[i];
    }
    if (res & 17u) {
      spins |= 63u;
      if (dead || poll_fail(spins, abortw, lane)) dead = true;
    }
  }
  PDUMP(0);
}

__device__ __forceinline__ void role_vo(const ChainArgs& a, int h, char* smem, const int w, Gate& G) {
  int lane = opaque_tid(0);
  const int b = 32 + h; (void)b;
  u64* prof = a.prof; (void)prof;
  PDECL(L_PRF, 4);
  GAS unsigned* abortw = (GAS unsigned*)(a.sync + SY_ABORT);
  bool dead = false;
  Wts W;
  Clk ck; ck.init();
  u32x4 gv;
  load_weights<1>(a, h, w, lane, smem, W, G, gv);
  if (!G.look(a.sync, gv)) return;
  PZERO;
  const Sel sel = G.sel;
  const __amdgpu_buffer_rsrc_t rh = rsrc_hot(a.sync, sel), rum = rsrc_u(a.sync);
  u32x16 C, P;
  {
    const int r = lane & 15, g = lane >> 4;
    const unsigned l0 = (unsigned)(uintptr_t)(LAS char*)smem;
#pragma unroll
    for (int i = 0; i < 16; ++i) C[i] = H_TR + (lane & 31) * 128;
    C[0] = lane * 16; C[1] = l0 + L_MEM + lane * 16; C[2] = l0 + L_WTS + w * 4096 + lane * 16; C[3] = C[2] + 65536;
    C[4] = (lane & 2) ? 0xEEEE : 0x4444;
    C[5] = l0 + L_XCH + (32 * w + r) * 16 + g * 4;
    C[6] = (lane & 8) ? 0xEEEE : 0x4444;
    C[7] = l0 + L_XCH + (32 * (g >> 1) + 8 * (g & 1) + (r >> 2)) * 16 + (r & 1) * 8;
    C[8] = ((g & 1) * 8 + (g >> 1)) * CHK + r * 16;
    C[13] = l0 + L_PRF + w * 1024 + (lane >> 6);
    asm volatile("" : "+v"(C));
  }
  start_wait(a.sync, abortw, lane, dead, -1, 0u, [] {});
  G.arrive(a.sync);
  SSTAMP(128 + h);
  steps_init(P, h, w, SY_UM + (16 * (h & 3) + 2 * w) * CHK + hpos(h) * 256);
  unsigned spins = 0;
  while (true) {
    u32x8 X; u32x16 pv; (void)pv;
    steps_enter(P, ck, dead);
    STEPS(VO_BODY);
    keep_a(W);
    lane = opaque_tid(0);
    if (__builtin_amdgcn_readfirstlane(P[2]) & 64u) break;
    spins |= 63u;
    if (dead || poll_fail(spins, abortw, lane)) dead = true;
  }
  PDUMP(0);
}

__device__ __forceinline__ void calibrate(const ChainArgs& a, int cal, int slot, u64 entry) {
  const u32x4 rs = own_rsrc(a.sync, 64 * PAGE);
  const unsigned so = cal * PAGE + (slot >> 1) * CHK + (slot & 1) * 4096;
  unsigned best[2] = {1u << 30, 1u << 30};
  bool sure;
  {
    unsigned z = 0;
    asm volatile("s_mov_b64 exec, 0xffff\n\tbuffer_atomic_or %0, %1, %2, %3 offen sc0 sc1\n\ts_mov_b64 exec, -1\n\ts_waitcnt vmcnt(0)"
                 : "+v"(z) : "v"((opaque_tid(0) & 8) * 1024 + (opaque_tid(0) & 7) * 256), "s"(rs), "s"(so) : "memory");
  }
  for (int i = 0;; ++i) {
    sure = min(best[0], best[1]) < CALT && max(best[0], best[1]) - min(best[0], best[1]) > 110u;
    if (i >= CALN && (sure || __builtin_amdgcn_s_memrealtime() - entry > CALM)) break;
#pragma unroll
    for (int hf = 0; hf < 2; ++hf) {
      u64 c0, c1;
      unsigned z = 0;
      asm volatile("s_mov_b64 exec, 1\n\ts_memtime %0\n\ts_waitcnt lgkmcnt(0)\n\tbuffer_atomic_or %2, off, %3, %4 sc0 sc1\n\ts_waitcnt vmcnt(0)\n\ts_memtime %1\n\ts_mov_b64 exec, -1\n\ts_waitcnt lgkmcnt(0)"
                   : "=&s"(c0), "=&s"(c1), "+v"(z) : "s"(rs), "s"(so + hf * 8192 + (i & 7) * 256) : "memory");
      best[hf] = min(best[hf], (unsigned)(c1 - c0));
    }
  }
  const u64 hs = (__builtin_amdgcn_s_memrealtime() & ~3ull) | (sure ? 2u : 0u) | ((best[1] < best[0] ? 1u : 0u) ^ die_half());
  if (opaque_tid(0) == 0) __hip_atomic_store((GAS u64*)ctr(a, CT_CAL + (slot >> 1)) + (slot & 1), hs, __ATOMIC_RELAXED, __HIP_MEMORY_SCOPE_AGENT);
}
__device__ __forceinline__ u32x4 pk8(f32x4 a, f32x4 b) { return (u32x4){pk2(a[0], a[1]), pk2(a[2], a[3]), pk2(b[0], b[1]), pk2(b[2], b[3])}; }
__device__ __forceinline__ unsigned pk2v(float a, float b) { unsigned r; asm volatile("v_cvt_pk_bf16_f32 %0, %1, %2" : "=v"(r) : "v"(a), "v"(b)); return r; }
__device__ __forceinline__ u32x4 pk8v(f32x4 a, f32x4 b) { asm volatile("s_nop 7"); return (u32x4){pk2v(a[0], a[1]), pk2v(a[2], a[3]), pk2v(b[0], b[1]), pk2v(b[2], b[3])}; }
__device__ __forceinline__ void st16g(u16* p, u32x4 v) { asm volatile("global_store_dwordx4 %0, %1, off " OUTP "\n\ts_nop 1" :: "v"(p), "v"(v) : "memory"); }
__device__ __forceinline__ void role_cb(const ChainArgs& a, int cbi, char* smem, const int w, Gate& G) {
  int lane = opaque_tid(0);
  const int b = 64 + cbi; (void)b;
  const int par = cbi >> 5, ci = cbi & 31, J = ci >> 3;
  u64* prof = a.prof; (void)prof;
  PDECL(65536, 8); PZERO;
  GAS unsigned* abortw = (GAS unsigned*)(a.sync + SY_ABORT);
  bool dead = false;
  const int tw = 2 * ci + (w & 1);
  const unsigned mo = (tw >> 5) * CHK + (tw & 31) * 256;
  Sel sel = 0;
  auto start = [&]() {
    lane = opaque_tid(0);
    SSTAMP(0 + ci);
    unsigned m0 = 0;
    if (par == 0 && w < 2) {
      const u16* m = a.mem0 + (2 * (lane >> 4) + ((lane >> 2) & 1)) * DM + 16 * tw + (lane & 8);
      m0 = m[lane & 3] | (unsigned)m[7 - (lane & 3)] << 16;
    }
    if (!G.look(a.sync, (u32x4){0u, 0u, 0u, 0u})) return false;
    sel = G.sel;
    SSTAMP(64 + ci);
    const __amdgpu_buffer_rsrc_t rh = rsrc_hot(a.sync, sel), rum = rsrc_u(a.sync);
    const unsigned s1v = ~(unsigned)(lane >> 6);
    const u32x4 sv = {s1v, s1v, s1v, s1v};
    auto um = [&](int k) {
#pragma unroll
      for (int i = 0; i < 4; ++i) st16(rum, near_half(SY_UM + (64 * k + tw) * CHK, sel) + (w >> 1) * 4096 + i * 1024 + lane * 16, sv);
    };
    um(par);
    const unsigned dv = cbi == H_DLT / CHK ? K_DEF * TU : s1v;
#pragma unroll
    for (int i = 0; i < 2; ++i) st16(rh, cbi >= 2 && cbi < 45 ? (cbi < 35 ? cbi * CHK : H_PX + (cbi - 35) * CHK) + w * 2048 + i * 1024 + lane * 16 : 0x7fffffffu, (u32x4){dv, s1v, s1v, s1v});
    if (par == 0 && w < 2) st4(rh, H_MEM + mo + lane * 4, nosent(m0));
    asm volatile("s_waitcnt vmcnt(0)" ::: "memory");
    lds_barrier();
    if (w == 0 && lane == 0) ctr_add(a, CT_INIT + J, 1u);
    SSTAMP(256 + ci);
    TP(6);
    G.arrive(a.sync);
    um(par + 2);
    {
      const __amdgpu_buffer_rsrc_t rx = mk_rsrc((void*)a.X, 4u << 20);
      u32x4 v[16];
#pragma unroll
      for (int i = 0; i < 16; ++i) v[i] = __builtin_amdgcn_raw_buffer_load_b128(rx, (unsigned)(cbi * 4096 + w * 1024 + lane * 16), (unsigned)(i * 262144), 0);
#pragma unroll
      for (int i = 0; i < 16; ++i) asm volatile("" :: "v"(v[i]));
    }
    SSTAMP(224 + ci);
    start_wait(a.sync, abortw, lane, dead, -1, 0u, [] {});
    return true;
  };
  if (!start()) return;
  Wts W;
#pragma unroll
  for (int i = 0; i < 32; ++i) asm volatile("" : "=a"(W.a[i]));
  u32x16 C, P; u32x8 C2;
  {
    const unsigned l0 = (unsigned)(uintptr_t)(LAS char*)smem, oor = 0x7fffffffu;
    const int r = lane & 7, t = (lane >> 3) & 1, g = lane >> 4, e = w & 1;
    const bool L = w < 2;
    C[0] = lane * 16;
    C[1] = L ? l0 + 32768 + e * 8192 + (lane & 2) * 512 + ((lane & 1) * 32 + (lane >> 2)) * 16 : e * CHK + t * 512 + (g & 1) * 256 + (r >> 2) * 128 + (g >> 1) * 64 + (r & 3) * 16;
    C[2] = L ? (4 * t + g) * 1024 + r * 16 : l0 + 81920 + e * 32768 + lane * 16;
    C[3] = L ? l0 + e * 1024 + lane * 8 : 2 * CHK + e * 4096 + (4 * (g >> 1) + 2 * t + (g & 1)) * 256 + 128 + r * 16;
    C[4] = ((16 * e + (lane >> 2)) * OUTW + DM + 32 * ci + 8 * (lane & 3)) * 2;
    C[5] = l0 + (L ? e * 8192 + lane * 16 : e * 512 + lane * 8); C[6] = l0 + 16384 + lane * 16; C[7] = l0 + 32768 + (L ? e * 8192 + 4096 : e * 2048) + lane * 16;
    C[8] = (lane & 31) * 128;
    C[9] = (8 * (g & 1) + 4 * (g >> 1) + 2 * t + (r >> 2)) * 16 + 4 * (r < 4 ? r : 7 - r);
    C[10] = (lane | e) ? oor : ci * 128;
    C[11] = L ? SY_CT + (CT_U + ct_near(sel) + 16 * e + (lane & 15)) * 128 : (t ? 0xEEEE : 0x4444); C[12] = L ? l0 + 81920 + e * 32768 + lane * 16 : ((lane & 2) ? 0xEEEE : 0x4444); C[13] = r < 4 ? 0x01000504u : 0x07060302u;
    C[14] = l0 + 49152 + 32 * e + (lane >> 6); C[15] = l0 + 49152 + (lane >> 6);
    C2[0] = L ? (lane < 16 ? lane * 16 : oor) : ux_tile(2 * ci) + lane * 16;
    C2[1] = lane == 0 && e && ci < 8 ? SY_CT + (CT_DONE + ct_near(~sel) + 8 * par + ci) * 128 : oor;
    C2[2] = (L ? ux_tile(2 * ci) : H_PX + 2 * e * CHK) + lane * 16;
#pragma unroll
    for (int n = 0; n < 4; ++n) C2[3 + n] = L ? H_PX + (8 + e) * CHK + (J ^ n) * 2048 + lane * 16 : C2[2] + CHK;
    C2[7] = l0 + 65536 + w * 2048 - 64 + (lane >> 6);
    asm volatile("" : "+v"(C), "+v"(C2));
#pragma unroll
    for (int i = 0; i < 16; ++i) P[i] = par - 2;
    P[2] = 48; P[6] = P[7] = K_DEF * TU; P[8] = SY_UM + tw * CHK + J * 2048; P[9] = H_MEM + mo; P[10] = 2 * CHK + J * 256; P[11] = e * 16 * UXH; P[12] = H_DLT + par * 4096;
  }
  Clk ck; ck.init();
  unsigned spins = 0;
  while (true) {
    u32x16 pv; (void)pv;
    unsigned res = __builtin_amdgcn_readfirstlane(P[2]);
    if (res & 64u) break;
    const u32x4 rh2 = own_rsrc(a.sync + near_half(SY_HOT, sel), H_BYTES - 8192), ra = own_rsrc(a.sync, SY_BYTES), ro = own_rsrc(a.out, S_LEN * OUTW * 2);
    if (res & 1u) {
      spins |= 63u;
      if (dead || poll_fail(spins, abortw, opaque_tid(0))) dead = true;
    }
    ck.sync();
    P[2] = (res & 58u) | (dead ? 4u : 0u); P[1] = ck.ce; P[3] = ck.te; P[4] = ck.t2c; P[5] = ck.c2t << 15;
#pragma unroll
    for (int i = 0; i < 16; ++i) P[i] = __builtin_amdgcn_readfirstlane(P[i]);
    asm volatile(CB_BODY : "+{s[40:55]}"(P) STEP_PV
                 : "{v[192:207]}"(C), "{v[208:215]}"(C2), [rh] "s"(rh2), [ru] "s"(ra), [ro] "s"(ro), [wv] "s"(w), [sel] "s"(sel)
                 : "memory", "vcc", "scc", CB_CL "s56", "s57", "s58", "s59", "s76", "s77", "s78", "s79", "s80", "s81", "s82", "s83", "s84", "s85", "s86", "s87", "s88", "s89", "s90", "s91", "s92", "s93", "s94", "s95", "s96", "s97", "s98", "s99", "s100", "s101", V53, "v53", "v54", "v55", "v56", "v57", "v58", "v59", "v60", "v61", "v62", "v63", "v64", "v65", "v66", "v67", "v68", "v69", "v70", "v71", "v72", "v73", "v74", "v75", "v76", "v77", "v78", "v79", "v80", "v81", "v82", "v83", "v84", "v85", "v86", "v87", "v88", "v89", "v90", "v91", "v92", "v93", "v94", "v95", "v96", "v97", "v98", "v99", "v100", "v101", "v102", "v103", "v104", "v105", "v106", "v107", "v108", "v109", "v110", "v111", "v112", "v113", "v114", "v115", "v116", "v117", "v118", "v119", "v120", "v121", "v122", "v123", "v124", "v125", "v126", "v127", "v128", "v129", "v130", "v131", "v132", "v133", "v134", "v135", "v136", "v137", "v138", "v139", "v140", "v141", "v142", "v143", "v144", "v145", "v146", "v147", "v148", "v149", "v150", "v151", "v152", "v153", "v154", "v155", "v156", "v157", "v158", "v159", "v160", "v161", "v162", "v163", "v164", "v165", "v166", "v167", "v168", "v169", "v170", "v171", "v172", "v173", "v174", "v175", "v176", "v177", "v178", "v179", "v180", "v181", "v182", "v183", "v184", "v185", "v186", "v187", "v188", "v189", "v190", "v191");
    keep_a(W);
  }
  PDUMP(2);
}

__device__ __forceinline__ void bg_drain() {
  asm volatile("s_waitcnt vmcnt(0)" ::: "memory");
  __builtin_amdgcn_s_barrier();
  asm volatile("" ::: "memory");
}
#define BG_SLP 16
#define A3_SLP 48
__device__ __forceinline__ float blo(unsigned d) { return __uint_as_float(d << 16); }
__device__ __forceinline__ float bhi(unsigned d) { return __uint_as_float(d & 0xffff0000u); }
struct A1 {
  u32x4 kk[11], vv[8], sh[4];
  __device__ __forceinline__ void load(const ChainArgs& a, int x, int n) {
    const int lane = opaque_tid(0);
    const int d = (4 * x + (n & 3)) * 64 + 8 * (lane & 7), tb = (n >> 2) * 64 + 8 * (lane >> 3);
    const __amdgpu_buffer_rsrc_t rk = mk_rsrc(a.Ak, 4u << 20), rv = mk_rsrc(a.Av, 4u << 20), rs = mk_rsrc(a.Ashift, 8192);
#pragma unroll
    for (int i = 0; i < 4; ++i) sh[i] = __builtin_amdgcn_raw_buffer_load_b128(rs, d * 8 + 16 * i, 0, 0);
#pragma unroll
    for (int i = 0; i < 11; ++i) kk[i] = __builtin_amdgcn_raw_buffer_load_b128(rk, tb - 3 + i < 0 ? 0x7fffffffu : ((tb - 3 + i) * D_A + d) * 2, 0, 2);
#pragma unroll
    for (int i = 0; i < 8; ++i) vv[i] = __builtin_amdgcn_raw_buffer_load_b128(rv, ((tb + i) * D_A + d) * 2, 0, 2);
  }
  __device__ __forceinline__ void finish(const ChainArgs& a, int x, int n, char* T, Sel sel) {
    const int lane = opaque_tid(0);
#pragma unroll
    for (int c = 0; c < 8; ++c) {
      const unsigned t01 = sh[c >> 1][2 * (c & 1)], t23 = sh[c >> 1][2 * (c & 1) + 1];
      const float s0 = blo(t01), s1 = bhi(t01), s2 = blo(t23), s3 = bhi(t23);
      float kf[11];
#pragma unroll
      for (int i = 0; i < 11; ++i) kf[i] = (c & 1) ? bhi(kk[i][c >> 1]) : blo(kk[i][c >> 1]);
      u32x4 o;
#pragma unroll
      for (int i = 0; i < 8; i += 2) {
        float r[2];
#pragma unroll
        for (int j = 0; j < 2; ++j)
          r[j] = fmaf(kf[i + j + 3], s0, fmaf(kf[i + j + 2], s1, fmaf(kf[i + j + 1], s2, kf[i + j] * s3))) * ((c & 1) ? bhi(vv[i + j][c >> 1]) : blo(vv[i + j][c >> 1]));
        o[i >> 1] = pk2(r[0], r[1]);
      }
      *(u32x4*)(T + (8 * (lane & 7) + c) * 128 + (((lane >> 3) ^ (lane & 7)) * 16)) = o;
    }
    const __amdgpu_buffer_rsrc_t rkv = rsrc_u(a.sync);
#pragma unroll
    for (int i = 0; i < 8; ++i)
      st16(rkv, kv_row((4 * x + (n & 3)) * 64, sel) + (4 * i + (lane >> 4)) * CHK + ((lane >> 3) & 1) * 4096 + (n >> 2) * 128 + (lane & 7) * 16,
           *(const u32x4*)(T + (8 * i + (lane >> 3)) * 128 + (((lane & 7) ^ i) * 16)));
  }
};
#define WAH 4
#define DSP 4
#define SIGQ 2
__device__ __forceinline__ void task_w(const ChainArgs& a, int kind, int h, char* smem, int wv, Gate& G) {
  const int lane = opaque_tid(0), w = wv, r = lane & 15, g = lane >> 4;
  const __amdgpu_buffer_rsrc_t rw = mk_rsrc(kind == 0 ? a.Wq : (kind == 1 ? a.Wk : a.Wv), 8u << 20), rx = mk_rsrc(a.X, 4u << 20);
  const unsigned lds0 = (unsigned)(uintptr_t)(LAS char*)smem + w * 4096;
  unsigned vx[2], vw[4];
#pragma unroll
  for (int j = 0; j < 2; ++j) {
    const int rr = 8 * j + (lane >> 3);
    const int tb = kind == 0 ? rr : (kind == 1 ? 16 * ((rr >> 1) & 1) + 8 * ((rr >> 2) & 1) + 4 * (rr >> 3) + 2 * (rr & 1) : 8 * (rr >> 2) + (rr & 3));
    vx[j] = tb * DM * 2 + ((lane & 7) ^ (rr >> 1)) * 16;
  }
#pragma unroll
  for (int j = 0; j < 4; ++j) { const int row = (w * 4 + j) * 4 + g; vw[j] = row * HD * 2 + (r ^ swz_b(row)) * 16; }
  const u64 wo = kind == 2 ? bg_order(2) : (kind ? bg_order(1) : bg_order(0));
  auto xo = [&](int q) { return ((int)((wo >> (16 * q + 4 * w)) & 15) ^ (h & RHO)) * 128; };
  unsigned dT = (kind == 0 ? 16 : (kind == 1 ? 1 : 4)) * DM * 2;
  asm volatile("s_nop 4" : "+s"(dT));
  const unsigned vx0 = ((w & 1) ? vx[1] : vx[0]) + (w >> 1) * dT;
  auto st_w = [&](int n) {
#pragma unroll
    for (int j = 0; j < 4; ++j) dma16(rw, vw[j], (64 * ((int)((wo >> (4 * n)) & 15) ^ (h & RHO)) * HD + 128 * h) * 2, lds0 + (n & 7) * 16384 + j * 1024, W_NT & 16);
    dma16(rx, vx0, ((int)((wo >> (4 * n)) & 15) ^ (h & RHO)) * 128, lds0 - 3072 * w + 131072 + (n & 7) * 4096);
  };
  auto st_x = [&](int s, int b, int q, int j, bool pad) { dma16(rx, vx[j & 1], s * 65536 + (j >> 1) * dT + xo(q), lds0 + (b * 4 + q) * 16384 + j * 1024, false, pad); };
  if (KQ_D && kind < 2) while (__builtin_amdgcn_s_memrealtime() - G.entry < KQ_D) __builtin_amdgcn_s_sleep(8);
  __builtin_amdgcn_s_waitcnt(0x0F70);
  u32x4 W[32][2], gv;
  char* xb[3][2];
#pragma unroll
  for (int i = 0; i < 6; ++i) xb[i >> 1][i & 1] = lds_base(smem, (i >> 1) * 65536 + (r >> 3) * 1024 + (r & 7) * 128 + ((4 * (i & 1) + g) ^ (r >> 1)) * 16);
  f32x4 acc[2][4];
#pragma unroll
  for (int i = 0; i < 4; ++i) acc[1][i] = vzero();
#pragma unroll
  for (int n = 0; n < WAH; ++n) st_w(n);
#pragma unroll
  for (int n = 0; n < 16; ++n) {
    asm volatile("s_waitcnt vmcnt(%0)" :: "n"(4 * WAH - 4 + (n < 16 - WAH ? WAH - 1 : 15 - n)) : "memory");
    lds_barrier();
    u32x4 f[4], y[4];
#pragma unroll
    for (int i = 0; i < 4; ++i) {
#pragma unroll
      for (int q = 0; q < 2; ++q) {
        const int row = (i & 1) * 32 + 8 * g + 4 * q + (r >> 2);
        const u32x2 u = __builtin_bit_cast(u32x2, __builtin_amdgcn_ds_read_tr16_b64_v4i16((LAS bf16x4*)(smem + (n & 7) * 16384 + row * 256 + (((4 * w + (r & 3)) ^ swz_b(row)) * 16) + 8 * (i >> 1))));
        f[i][2 * q] = u[0]; f[i][2 * q + 1] = u[1];
      }
      y[i] = *(const u32x4*)(xb[2][i & 1] + (n & 7) * 4096 + (i >> 1) * 2048);
    }
    SB0;
    if (n == 16 - WAH) G.ask(a.sync, gv);
    if (n == 15) asm volatile("" : "+v"(gv));
    if (n + WAH < 16) st_w(n + WAH);
    else {
#pragma unroll
      for (int j = 0; j < 4; ++j) st_x(1 + ((n + WAH - 16) >> 2), (n + WAH - 16) >> 2, (n + WAH) & 3, j, true);
    }
#pragma unroll
    for (int i = 0; i < 8; ++i)
      asm volatile("s_nop 1\n\tv_mfma_f32_16x16x32_bf16 %0, %1, %2, %0" : "+v"(acc[1][i & 3]) : "v"(f[2 * (i & 1) + (i >> 2)]), "v"(y[(i & 2) + (i >> 2)]));
#pragma unroll
    for (int i = 0; i < 4; ++i) { asm volatile("" : "+a"(f[i])); W[2 * n + (i & 1)][i >> 1] = f[i]; }
  }
  if (!G.look(a.sync, gv)) { bg_drain(); return; }
  TP(4);
  SSTAMP(160 + h);
  const Sel sel = G.sel;
  const __amdgpu_buffer_rsrc_t ru = mk_rsrc(a.sync, SY_BYTES);
  const unsigned sgo = (w | lane) ? 0x7fffffffu : SY_CT + (kind == 2 ? CT_V + ct_near(~sel) : CT_Q + 32 * kind + ct_near(sel)) * 128 + h * 128;
#pragma unroll
  for (int T = 0; T < 2; ++T) st16s(pk8v(acc[1][2 * T], acc[1][2 * T + 1]), ru, lane * 16 + T * 4096, x_slot(kind, 0, h, sel) + w * 1024);
  SB0;
  const u32x4 rx2 = own_rsrc(a.X, 4u << 20);
  unsigned vq[4][4];
#pragma unroll
  for (int i = 0; i < 16; ++i) { vq[i >> 2][i & 3] = vx[i & 1] + ((i >> 1) & 1) * dT + xo(i >> 2); asm volatile("" : "+v"(vq[i >> 2][i & 3])); }
  u32x4 xf[4][2], o;
  auto rd = [&](int b, int k, int T) { xf[k & 3][T] = *(const u32x4*)(xb[b][k & 1] + (k >> 1) * 4096 + T * 2048); };
  auto signal = [&](int s) {
    st4(ru, sgo, s);
    if (w == 0 && lane == 0 && s >= 0 && (s < PSEG || (s & 7) == 0 || ((s & 15) == 2 && PSEG > 2) || s > 61)) {
    }
  };
  if (kind < KQ_NOW) { bg_drain(); signal(0); }
  G.arrive(a.sync);
#pragma unroll
  for (int i = 4 * WAH; i < 28; ++i) st_x(1 + (i >> 4), i >> 4, (i >> 2) & 3, i & 3, true);
  wait_vm<26>();
  lds_barrier();
#pragma unroll
  for (int i = 0; i < 6; ++i) rd(0, i >> 1, i & 1);
  WST(kind == 2, 18);
  auto seg = [&](int s, int b) {
    const unsigned so = x_slot(kind, s - 1, h, sel) + w * 1024;
    WST(kind == 2 && s > 0 && s < 4, 18 + s);
#pragma unroll
    for (int k = 0; k < 32; ++k)
#pragma unroll
      for (int i = 0; i < 4; ++i) {
        if (k == 0) asm volatile("v_mfma_f32_16x16x32_bf16 %0, %1, %2, 0" : "=&v"(acc[b][i]) : "a"(W[k][i & 1]), "v"(xf[k & 3][i >> 1]));
        else asm volatile("v_mfma_f32_16x16x32_bf16 %0, %1, %2, %0" : "+v"(acc[b][i]) : "a"(W[k][i & 1]), "v"(xf[k & 3][i >> 1]));
        SB0;
        const int q = k >> 3, n = 4 * k + i;
        if ((k & 7) == 5 && i == 0) {
          if (q == SIGQ) wait_vm<4 * SIGQ>(); else wait_vm<20>();
          if (q == 1 && __builtin_expect(s - 2u < W_NOW, 0)) { wait_vm<4>(); __builtin_amdgcn_s_barrier(); signal(s - 1); }
          else if (q == 0 && b == 0 && __builtin_expect(s == 1, 0)) { wait_vm<32 - 4 * WAH>(); __builtin_amdgcn_s_barrier(); signal(0); }
          else __builtin_amdgcn_s_barrier();
          asm volatile("" ::: "memory");
        }
        if (i == 1 || i == 2) rd(k + 3 < 32 ? b : b ^ 1, (k + 3) & 31, i - 1);
        {
          const int m = (n + 128 - 23) & 127, j = (m & 31) / DSP, q0 = m >> 5;
          if (j < 4 && (m & 31) % DSP == 0) asm volatile("s_add_i32 m0, %0, %1" :: "s"(lds0), "n"(((b ^ (q0 == 0) ^ (n < 23)) * 4 + ((q0 + 3) & 3)) * 16384 + j * 1024) : "m0", "scc");
          if (j < 4 && (m & 31) % DSP == 1)
            asm volatile("buffer_load_dwordx4 %0, %1, %2 offen lds" :: "v"(vq[(q0 + 3) & 3][j & 3]), "s"(rx2), "s"((s + 2 - (q0 == 0) - (n < 23)) * 65536) : "memory");
        }
        if (k == 8 * SIGQ + 5 && i == 2) signal(s - 1);
        if (k >= 1 && k < 5 && (i == 0 || i == 3)) { const f32x4 x = acc[b ^ 1][k - 1]; o[2 * (~k & 1) + (i & 1)] = pk2v(x[2 * (i & 1)], x[2 * (i & 1) + 1]); }
        if ((k == 2 || k == 4) && i == 3) { __builtin_amdgcn_raw_buffer_store_b128(o, ru, (unsigned)(lane * 16 + (k >> 2) * 4096), so, ST_AUX); asm volatile("s_nop 0" :: "v"(o)); }
        SB0;
      }
  };
  A1 a1;
#pragma unroll
  for (int i = 0; i < 11; ++i) asm volatile("" : "=v"(a1.kk[i]), "=v"(a1.vv[i & 7]), "=v"(a1.sh[i & 3]));
  const int wi = 32 * (2 - kind) + 4 * (h & 7) + w;
  for (int s = 1, ph = 0;; ++ph) {
    const int n = ph == 1 ? wi : (ph == 2 && wi >= 64 ? 32 + wi : 256);
    for (const int e = s + (ph ? A1_D : A1_0); s < e || ph == 4; s += 2) {
      seg(s, 0);
      if (s == NSEG - 1) break;
      seg(s + 1, 1);
    }
    if (ph == 4) break;
    if (ph == 3 && lane == 0) ctr_add(a, CT_A1 + ct_near(~sel) + (h >> 3), 1u);
    if (n < 128) a1.finish(a, h >> 3, n, smem + 131072 + w * 8192, sel);
    a1.load(a, h >> 3, ph == 0 ? wi : (ph == 1 && wi >= 64 ? 32 + wi : 256));
    SB0;
  }
#pragma unroll
  for (int T = 0; T < 2; ++T) st16s(pk8v(acc[0][2 * T], acc[0][2 * T + 1]), ru, lane * 16 + T * 4096, x_slot(kind, NSEG - 1, h, sel) + w * 1024);
  bg_drain();
  signal(NSEG - 1);
}
constexpr int A2_CSTR = 4160;
constexpr int A2_UOFF = 4 * A2_CSTR + 16 * 80;
constexpr int A2_USTR = 80 * 80;
constexpr int A2_WLDS = 4 * A2_CSTR + 2 * A2_USTR;
__device__ __forceinline__ void a2_wave(const ChainArgs& a, int d0, int d1, char* smem, int w, Sel sel) {
  const int lane = opaque_tid(0);
  char* Wb = smem + 1024 + w * A2_WLDS;
  char* U = Wb + A2_UOFF;
  const __amdgpu_buffer_rsrc_t rf = mk_rsrc((void*)a.Assm, 4u << 20), rd_ = mk_rsrc((void*)a.AD, 2048);
  const __amdgpu_buffer_rsrc_t rkv = rsrc_u(a.sync);
  u32x4 hi[4], uv[2][4];
  u32x2 lo[4];
  unsigned dn;
  auto ld_h = [&](int d) {
#pragma unroll
    for (int i = 0; i < 4; ++i) {
      hi[i] = __builtin_amdgcn_raw_buffer_load_b128(rf, (unsigned)((255 - 64 * i - lane) * 16), (unsigned)(d * 4096), 2);
      lo[i] = __builtin_amdgcn_raw_buffer_load_b64(rf, i == 3 && lane == 63 ? 0x7fffffffu : (255 - 64 * i - lane) * 16 - 8, (unsigned)(d * 4096), 2);
    }
    dn = __builtin_amdgcn_raw_buffer_load_b16(rd_, 0, 2 * d, 0);
  };
  auto ld_u = [&](int d) {
#pragma unroll
    for (int i = 0; i < 8; ++i) uv[i & 1][i >> 1] = __builtin_amdgcn_raw_buffer_load_b128(rkv, (unsigned)max(((i >> 1) * 64 + lane) * 16 - 8 * (i & 1), 0), kv_row(d, sel), LD_AUX);
  };
  ld_h(d0);
  {
    bool dead = false;
    ctr_wait<BG_SLP>(ctr(a, CT_A1 + ct_near(~sel) + (d0 >> 8)), 96u, (GAS unsigned*)(a.sync + SY_ABORT), lane, dead);
  }
  ld_u(d0);
  if (lane < 12) *(f32x4*)(Wb + (lane / 3) * A2_CSTR + (256 + lane % 3) * 16) = vzero();
#pragma unroll
  for (int i = 0; i < 4; ++i) if (64 * (i & 1) + lane < 80) *(f32x4*)(U + (i >> 1) * A2_USTR - 16 * 80 + (64 * (i & 1) + lane) * 16) = vzero();
  const int r = lane & 15, g = lane >> 4;
#pragma unroll 1
  for (int d = d0; d < d1; d += 96) {
    u64 at[6]; (void)at;
    AST(0);
#pragma unroll
    for (int i = 0; i < 4; ++i) {
      const unsigned src[6] = {lo[i][0], lo[i][1], hi[i][0], hi[i][1], hi[i][2], hi[i][3]};
      unsigned f[10];
#pragma unroll
      for (int x = 0; x < 10; ++x) {
        if ((x & 1) == 0) f[x] = __builtin_amdgcn_alignbit(src[5 - x / 2], src[5 - x / 2], 16);
        else f[x] = __builtin_amdgcn_perm(src[5 - (x + 1) / 2], src[5 - (x - 1) / 2], 0x07060100u);
      }
#pragma unroll
      for (int m = 0; m < 4; ++m) *(u32x4*)(Wb + m * A2_CSTR + (64 * i + lane) * 16) = (u32x4){f[3 - m], f[5 - m], f[7 - m], f[9 - m]};
    }
    if (lane == 0) uv[1][0] = (u32x4){0u, 0u, uv[1][0][0], uv[1][0][1]};
#pragma unroll
    for (int i = 0; i < 8; ++i) {
      const int ch = (i >> 1) * 64 + lane;
      *(u32x4*)(U + (i & 1) * A2_USTR + (ch >> 2) * 80 + (ch & 3) * 16) = uv[i & 1][i >> 1];
    }
    const float Dd = blo(dn);
    asm volatile("" :: "v"(Dd) : "memory");
    AST(1);
    ld_h(d + 96 < d1 ? d + 96 : D_A); ld_u(d + 96 < d1 ? d + 96 : D_A);
    f32x4 acc[4][2];
#pragma unroll
    for (int i = 0; i < 8; ++i) acc[i >> 1][i & 1] = vzero();
    const char* pa = Wb + (r & 3) * A2_CSTR + 16 * (255 - (r >> 2) + g) - 3 * 1024 - 64;
    const char* pb = U + (r - 1) * 80 + 16 * g;
    bf16x8 fa[2][4], fb[2][8];
    auto rd = [&](int b, int n, int k) {
      if (n == 0 || n > 8) fa[b][n ? n - 8 : 0] = *(const bf16x8*)(pa + 1024 * (3 - (n ? n - 8 : 0)) + 64 * (1 - k));
      else fb[b][n - 1] = *(const bf16x8*)(pb + ((n - 1) & 1) * A2_USTR + 80 * (16 * ((n - 1) >> 1) + 1 - k));
    };
#pragma unroll
    for (int n = 0; n < 12; ++n) rd(0, n, 0);
    AST(2);
#pragma unroll 1
    for (int k = 0; k < 16; k += 2) {
#pragma unroll
      for (int b = 0; b < 2; ++b) {
        int q = 0;
#pragma unroll
        for (int j = 0; j < 4; ++j)
#pragma unroll
          for (int i = j; i < 4; ++i)
#pragma unroll
            for (int mt = 0; mt < 2; ++mt) {
              asm volatile("v_mfma_f32_16x16x32_bf16 %0, %1, %2, %0" : "+v"(acc[i][mt]) : "v"(fa[b][j]), "v"(fb[b][2 * (i - j) + mt]));
              SB0;
              if (q < 12) rd(b ^ 1, q, 1);
              if (q == 12) { pa -= 64; pb -= 80; }
              ++q;
              SB0;
            }
      }
    }
    AST(3);
    u32x4 kv[4];
#pragma unroll
    for (int i = 0; i < 4; ++i) kv[i] = *(const u32x4*)(U + (16 * i + r) * 80 + 16 * g);
    asm volatile("s_nop 7\n\ts_waitcnt vmcnt(0)" ::: "memory");
#pragma unroll
    for (int i = 0; i < 8; ++i) asm volatile("" : "+v"(acc[i >> 1][i & 1]));
    if (lane == 0 && d > d0) ctr_add(a, CT_A2 + ct_near(~sel) + ((d - 96) >> 6), 1u);
#pragma unroll
    for (int i = 0; i < 4; ++i) {
      const f32x4 x = acc[i][1], y = acc[i][0];
      st16s((u32x4){pk2(fmaf(blo(kv[i][0]), Dd, x[0]), fmaf(bhi(kv[i][0]), Dd, x[1])), pk2(fmaf(blo(kv[i][1]), Dd, x[2]), fmaf(bhi(kv[i][1]), Dd, x[3])),
                    pk2(fmaf(blo(kv[i][2]), Dd, y[0]), fmaf(bhi(kv[i][2]), Dd, y[1])), pk2(fmaf(blo(kv[i][3]), Dd, y[2]), fmaf(bhi(kv[i][3]), Dd, y[3]))},
            rkv, (unsigned)(i * 1024 + r * 64 + g * 16), kv_row(d, sel));
    }
    AST(4);
  }
  wait_vm<0>();
  if (lane == 0) ctr_add(a, CT_A2 + ct_near(~sel) + ((d0 + (d1 - 1 - d0) / 96 * 96) >> 6), 1u);
}
__device__ __forceinline__ void task_u(const ChainArgs& a, int h, char* smem, int wv, Gate& G) {
  const int lane = opaque_tid(0);
  const int w = wv;
  GAS unsigned* abortw = (GAS unsigned*)(a.sync + SY_ABORT);
  bool dead = false;
  char* sb = smem + w * 32768;
  const __amdgpu_buffer_rsrc_t rwo = mk_rsrc(a.Wo, 8u << 20);
  const unsigned lds0 = (unsigned)(uintptr_t)(LAS char*)sb;
  unsigned wo4[4];
#pragma unroll
  for (int i = 0; i < 4; ++i) { const int row = i * 4 + (lane >> 4); wo4[i] = row * DM * 2 + (((lane & 15) ^ swz_b(row)) * 16); }
  asm volatile("s_nop 4");
  auto stage = [&](int n) {
    const int p = (U_ORD >> (4 * n)) & 3;
#pragma unroll
    for (int i = 0; i < 16; ++i)
      dma16(rwo, wo4[i & 3], (unsigned)(((128 * h + 64 * (p & 1) + 16 * (i >> 2)) * DM + 256 * w + 128 * (p >> 1)) * 2), lds0 + (n & 1) * 16384 + i * 1024, W_NT & 2);
  };
  __builtin_amdgcn_s_waitcnt(0x0F70);
  u32x4 W[16][4];
  stage(0); stage(1);
#pragma unroll
  for (int n = 0; n < 4; ++n) {
    const int p = (U_ORD >> (4 * n)) & 3;
    if (n < 3) wait_vm<16>(); else wait_vm<0>();
#pragma unroll
    for (int t = 0; t < 8; ++t) {
      const u32x8 f = tr_frag(sb + (n & 1) * 16384, t, lane);
      u32x4 f0 = {f[0], f[1], f[2], f[3]}, f1 = {f[4], f[5], f[6], f[7]};
      asm volatile("" : "+a"(f0), "+a"(f1));
      W[8 * (p >> 1) + t][2 * (p & 1)] = f0; W[8 * (p >> 1) + t][2 * (p & 1) + 1] = f1;
    }
    asm volatile("s_waitcnt lgkmcnt(0)" ::: "memory");
    if (n + 2 < 4) stage(n + 2);
  }
  lds_barrier();
  if (!G.pass(a.sync)) return;
  SSTAMP(192 + h);
  const Sel sel = G.sel;
  const u32x4 ru = own_rsrc(a.sync, SY_BYTES);
  u32x4 V[2][8], pc[2], o[2];
  f32x4 c[2][2];
  unsigned cv, cw, vo[4], lk[2], sgo = (w | lane) ? 0x7fffffffu : SY_CT + (CT_U + ct_near(sel) + h) * 128, nd = (lane & 1) ? 3 - URING : 2;
#pragma unroll
  for (int i = 0; i < 4; ++i) { vo[i] = lane * 16 + ux_tile(16 * w + 4 * i); asm volatile("" : "+v"(vo[i])); }
#pragma unroll
  for (int i = 0; i < 2; ++i) lk[i] = SY_CT + (ct_near(~sel) + ((lane & 1) ? CT_DONE + 8 * i + (h & 7) : CT_V + h)) * 128;
  char* vl = lds_base(smem, lane * 16);
  const unsigned vl2 = lane * 16 + w * 2048;
  auto ld_v = [&](int s, bool pad = false) {
    if (pad) asm volatile("s_nop 4\n\tbuffer_load_dwordx4 %0, %2, %3, %4 offen sc1\n\tbuffer_load_dwordx4 %1, %2, %3, %4 offen offset:1024 sc1" : "=&v"(pc[0]), "=&v"(pc[1]) : "v"(vl2), "s"(ru), "s"(x_slot(2, s, h, sel)) : "memory");
    else asm volatile("buffer_load_dwordx4 %0, %2, %3, %4 offen sc1\n\tbuffer_load_dwordx4 %1, %2, %3, %4 offen offset:1024 sc1" : "=&v"(pc[0]), "=&v"(pc[1]) : "v"(vl2), "s"(ru), "s"(x_slot(2, s, h, sel)) : "memory");
  };
  auto signal = [&](int s) {
    asm volatile("buffer_store_dword %0, %1, %2, 0 offen sc1" :: "v"(s), "v"(sgo), "s"(ru) : "memory");
  };
  auto st_u = [&](int n, unsigned ub) { asm volatile("buffer_store_dwordx4 %0, %1, %2, %3 offen offset:%4 sc1" :: "v"(o[n & 1]), "v"(vo[n >> 2]), "s"(ru), "s"(ub), "n"((n & 3) * 1024) : "memory"); };
  auto fetch = [&](int s) {
    for (unsigned spins = 0;;) {
      asm volatile("buffer_load_dword %0, %1, %2, 0 offen sc1" : "=&v"(cw) : "v"(lk[0]), "s"(ru) : "memory");
      ld_v(s, true);
      asm volatile("s_waitcnt vmcnt(0)" : "+v"(pc[0]), "+v"(pc[1]), "+v"(cw) :: "memory");
      if (s && spins == 0) { __builtin_amdgcn_s_barrier(); signal(0); }
      if ((int)__builtin_amdgcn_readfirstlane(cw) >= s) break;
      __builtin_amdgcn_s_sleep(2);
      if (poll_fail(spins, abortw, lane)) { dead = true; break; }
    }
    WST(s == 0, 24);
    if (s == 0) TP(3);
#pragma unroll
    for (int i = 0; i < 2; ++i) *(u32x4*)(vl + 16384 + w * 2048 + i * 1024) = pc[i];
    lds_barrier();
#pragma unroll
    for (int i = 0; i < 8; ++i) V[s][i] = *(const u32x4*)(vl + 16384 + i * 1024);
  };
#pragma unroll
  for (int i = 0; i < 8; ++i) asm volatile("" : "=v"(V[0][i]));
  u64 bad = 0;
  unsigned up = 0x7f000000u;
  c[1][0] = c[1][1] = vzero();
  auto seg = [&](int s, int b) {
    const unsigned ub = s < 0 ? 0x7f000000u : ux_base(s, h, sel);
    WST(s >= 0 && s < 4, 25 + s);
#pragma unroll
    for (int n = 0; n < 16; ++n)
#pragma unroll
      for (int q = 0; q < 8; ++q) {
        const int kp = q >> 1, m = q & 1;
        if (kp == 0) asm volatile("v_mfma_f32_16x16x32_bf16 %0, %1, %2, 0" : "=&v"(c[n & 1][m]) : "v"(V[b][4 * m + kp]), "a"(W[n][kp]));
        else asm volatile("v_mfma_f32_16x16x32_bf16 %0, %1, %2, %0" : "+v"(c[n & 1][m]) : "v"(V[b][4 * m + kp]), "a"(W[n][kp]));
        SB0;
        if (q >= 2 && q < 6) { const f32x4 x = c[~n & 1][(q - 2) >> 1]; o[~n & 1][q - 2] = pk2v(x[2 * (q & 1)], x[2 * (q & 1) + 1]); }
        if (q == 6) st_u((n - 1) & 15, n ? ub : up);
        if (q == 5) asm volatile("" :: "v"(o[n & 1]));
        if (n == U_LD && q == 0) {
          if (__builtin_expect(bad != 0, 0)) {
            if (s <= U_NOW) {
              asm volatile("s_waitcnt vmcnt(%1)" : "+v"(cw) : "n"(U_LD - 2) : "memory");
              __builtin_amdgcn_s_barrier();
              signal(s - 1);
              bad = (int)__builtin_amdgcn_readfirstlane(cw) <= s;
            }
            if (bad & 1) ctr_wait<2>(ctr(a, CT_V + ct_near(~sel) + h), min(s + 2, NSEG), abortw, lane, dead);
            if (bad & 2) {
              ctr_wait<U_SLP>(ctr(a, CT_DONE + ct_near(~sel) + 8 * b + (h & 7)), max(s + 3 - URING, 0), abortw, lane, dead);
              for (int i = 0; i < U_STG * (h & 7) + U_ST0; ++i) __builtin_amdgcn_s_sleep(1);
            }
          }
          ld_v(min(s + 1u, NSEG - 1u));
        }
        if (n == 1 && q == 7) asm volatile("buffer_load_dword %0, %1, %2, 0 offen sc1" : "=&v"(cw) : "v"(s <= U_NOW ? lk[0] : 0x7fffffffu), "s"(ru) : "memory");
        if (n == U_LD && q == 7) asm volatile("buffer_load_dword %0, %1, %2, 0 offen sc1" : "=&v"(cv) : "v"(lk[b ^ 1]), "s"(ru) : "memory");
        if (n == U_SIG2 && q == 0) asm volatile("s_waitcnt vmcnt(%4)" : "+v"(pc[0]), "+v"(pc[1]), "+v"(cv), "+v"(cw) : "n"(U_SIG2 - U_LD - 1) : "memory");
        if (n == U_SIG2 && (q == 1 || q == 7)) *(u32x4*)(vl + (b ^ 1) * 8192 + w * 2048 + (q >> 2) * 1024) = pc[q >> 2];
        if (n == U_SIG2 + 1 && q == 0) { bad = __builtin_amdgcn_ballot_w64(s >= 0 && (int)cv < (s < U_NOW && lane > 1 ? NSEG : min(s + (int)nd, NSEG - 1))); asm volatile("" : "+s"(bad)); }
        if (n == U_SIG2 + 1 && q == 7) lds_barrier();
        if (n == U_SIG2 + 2 && q == 0) signal(s - 1);
        if ((n == U_SIG2 + 2 || n == U_SIG2 + 3) && (q & 1)) V[b ^ 1][4 * (n - U_SIG2 - 2) + (q >> 1)] = *(const u32x4*)(vl + (b ^ 1) * 8192 + (4 * (n - U_SIG2 - 2) + (q >> 1)) * 1024);
        SB0;
      }
    up = ub;
  };
  for (int s = -U_DRY; s < NSEG; s += 2) {
    if (__builtin_expect(s == 0, 0)) fetch(0);
    seg(s, 0);
    if (__builtin_expect(s == 0, 0)) {
      o[1] = pk8v(c[1][0], c[1][1]);
      st_u(15, up);
      fetch(1);
    }
    seg(s + 1, 1);
  }
  o[1] = pk8v(c[1][0], c[1][1]);
  st_u(15, up);
  bg_drain();
  signal(NSEG - 1);
}

__device__ __forceinline__ void task_a3(const ChainArgs& a, int dg, int t0, char* T, Sel sel) {
  const int lane = opaque_tid(0);
  const int d = dg * 64 + 8 * (lane & 7);
  const int tb = t0 + 8 * (lane >> 3);
  const __amdgpu_buffer_rsrc_t rz = rsrc_u(a.sync);
  u32x4 qq[16], zz[8][2];
#pragma unroll
  for (int i = 0; i < 16; ++i) qq[i] = LDNT((const u32x4*)(a.Aq + (long)(tb + 64 * (i >> 3) + (i & 7)) * D_A + d));
  {
    u32x4 zr[16];
    bool dead = false;
    ctr_wait<A3_SLP>(ctr(a, CT_A2 + ct_near(~sel) + dg), 64u, (GAS unsigned*)(a.sync + SY_ABORT), lane, dead);
  #pragma unroll
    for (int i = 0; i < 16; ++i) zr[i] = ld16(rz, kv_row(dg * 64, sel) + (2 * i + (lane >> 5)) * CHK + ((lane >> 4) & 1) * 4096 + (t0 + (lane & 15) * 8) * 2);
#pragma unroll
    for (int i = 0; i < 16; ++i) {
      const int row = 4 * i + (lane >> 4);
      *(u32x4*)(T + row * 256 + (((lane & 15) ^ (row >> 3)) * 16)) = zr[i];
    }
  }
#pragma unroll
  for (int c = 0; c < 8; ++c)
#pragma unroll
    for (int gq = 0; gq < 2; ++gq) zz[c][gq] = *(const u32x4*)(T + (8 * (lane & 7) + c) * 256 + (((gq * 8 + (lane >> 3)) ^ (lane & 7)) * 16));
#pragma unroll
  for (int i = 0; i < 16; ++i) {
    u32x4 o;
#pragma unroll
    for (int c = 0; c < 8; c += 2) {
      const unsigned z0 = zz[c][i >> 3][(i >> 1) & 3], z1 = zz[c + 1][i >> 3][(i >> 1) & 3];
      o[c >> 1] = pk2(blo(qq[i][c >> 1]) * ((i & 1) ? bhi(z0) : blo(z0)), bhi(qq[i][c >> 1]) * ((i & 1) ? bhi(z1) : blo(z1)));
    }
    st16g(a.out + (long)(tb + 64 * (i >> 3) + (i & 7)) * OUTW + d, o);
  }
}

__device__ __forceinline__ void part_a(const ChainArgs& a, char* smem, int x, int wi, int w, Sel sel) {
  char* T = smem + 1024 + w * A2_WLDS;
  a2_wave(a, 256 * x + wi, 256 * x + 256, smem, w, sel);
  while (true) {
    unsigned t = 0;
    if (opaque_tid(0) == 0) t = __hip_atomic_fetch_add(ctr(a, CT_TK + ct_near(~sel) + x), 1u, __ATOMIC_RELAXED, __HIP_MEMORY_SCOPE_AGENT) + 1u;
    t = __builtin_amdgcn_readfirstlane(t);
    if (t >= 64) break;
    task_a3(a, 4 * x + (t >> 4), (t & 15) * 128, T, sel);
  }
}

__global__ __launch_bounds__(256, 1) void chain_kernel(ChainArgs a) {
  extern __shared__ __attribute__((aligned(16))) char smem[];
  const int b = blockIdx.x;
  {
    u32x4 rc;
    asm volatile("s_getpc_b64 s[60:61]\n\t.Lchain_here:\n\ts_sub_u32 s60, s60, .Lchain_here-_Z12chain_kernel9ChainArgs\n\ts_subb_u32 s61, s61, 0\n\ts_and_b32 s61, s61, 0xffff\n\t"
                 "s_mov_b32 s62, .Lchain_end-_Z12chain_kernel9ChainArgs\n\ts_mov_b32 s63, 0x20000\n\t"
                 "buffer_load_dwordx4 a[252:255], %1, s[60:63], 0 offen\n\tbuffer_load_dwordx4 a[252:255], %1, s[60:63], s63 offen"
                 : "={s[60:63]}"(rc) : "v"((b >> 3) * 4096 + threadIdx.x * 16) : "scc", "a252", "a253", "a254", "a255");
  }
  int w = __builtin_amdgcn_readfirstlane(threadIdx.x >> 6);
  Gate G;
  G.entry = __builtin_amdgcn_s_memrealtime(); G.open = false; G.arr = w == 0 ? b & 7 : -1;
  G.rot = ((unsigned)__builtin_amdgcn_s_getreg(20 | (3 << 11)) - b + WRONG) & 7u;
  {
    volatile LAS unsigned* t = (volatile LAS unsigned*)smem;
    if (w == 0) { t[0] = (unsigned)G.entry; t[1] = (unsigned)(G.entry >> 32); }
    __syncthreads();
    G.entry = (u64)(unsigned)__builtin_amdgcn_readfirstlane(t[0]) | ((u64)(unsigned)__builtin_amdgcn_readfirstlane(t[1]) << 32);
    __syncthreads();
  }
  G.tx = G.entry;
  if (((b + G.rot) & 4) == 0 && b < 128) {
    const int c = (b >> 6) * 32 + 8 * ((b + G.rot) & 3) + ((b >> 3) & 7);
    if (w < 2) calibrate(a, w ? 32 + (0x3200 >> (4 * (c & 3)) & 3) : c, 2 * c + w, G.entry);
    __builtin_amdgcn_s_barrier();
  }
  if (b == 0) {
    const int lane = opaque_tid(0);
    const __amdgpu_buffer_rsrc_t rs = mk_rsrc(a.sync + SY_SET, 3 * CHK);
    st16(rs, CHK + w * 8192 + lane * 128, (u32x4){SENT, SENT, SENT, SENT});
    if (w == 0 && lane >= 2) st16(rs, lane * 128, (u32x4){SENT, SENT, SENT, SENT});
    if (w == 3) {
      u32x4 v;
      bool ok;
      do {
        v = ld16(mk_rsrc(a.sync + SY_CT + CT_CAL * 128, 8192), lane * 128);
        ok = ((u64)v[1] << 32 | v[0]) - G.entry + 2000u < (1ull << 31) && ((u64)v[3] << 32 | v[2]) - G.entry + 2000u < (1ull << 31);
      } while (!__all(ok) && __builtin_amdgcn_s_memrealtime() - G.entry < 2 * CALM);
      const u64 k = lane == 32 ? 0x3333333333333333ull : (lane == 34 || lane == 35 ? 0x1111111111111111ull << (lane & 3) : 0ull);
      const int n1 = __builtin_popcountll(k & __builtin_amdgcn_ballot_w64(ok && (v[2] & 3u) == 3u)), n0 = __builtin_popcountll(k & __builtin_amdgcn_ballot_w64(ok && (v[2] & 3u) == 2u));
      if (!ok) v[0] = 0;
      if (n0 != n1) v[0] = n1 > n0 ? 3u : 2u;
      const unsigned t = (v[0] ^ lane) & 1u;
      const bool most = __builtin_popcountll(__builtin_amdgcn_ballot_w64((v[0] & 2u) && t)) > __builtin_popcountll(__builtin_amdgcn_ballot_w64((v[0] & 2u) && !t));
      const u64 m = __builtin_amdgcn_ballot_w64((v[0] & 2u) ? v[0] & 1u : (lane ^ most) & 1u);
      if (lane == 0) __hip_atomic_store((GAS u64*)(a.sync + SY_SEL), m, __ATOMIC_RELAXED, __HIP_MEMORY_SCOPE_AGENT);
    }
    bg_drain();
    if (w == 0 && lane == 0) __hip_atomic_store((GAS u64*)(a.sync + SY_EPOCH), (__builtin_amdgcn_s_memrealtime() & ~7ull) | G.rot, __ATOMIC_RELAXED, __HIP_MEMORY_SCOPE_AGENT);
  }
  while (true) {
    const unsigned rot = G.rot;
    asm volatile("" : "+s"(w));
    const int x = (b + rot) & 7, j = b >> 3, h = 8 * (x & 3) + (j & 7), role = (x & 4) + (j >> 3);
    if ((role == 0 || role == 7) && w >= 2) {
      const int n = opaque_tid(w - 2);
      const unsigned x0 = __builtin_amdgcn_raw_buffer_load_b32(mk_rsrc(a.sync, 106u << 20), n * PAGE + 4096 + (j & 31) * 128, 0, 2);
      const unsigned x1 = __builtin_amdgcn_raw_buffer_load_b32(mk_rsrc(a.out, 8u << 20), n * PAGE + (j & 31) * 128, 0, 2);
      asm volatile("" :: "v"(x0), "v"(x1));
    }
    if (role < 2) role_cb(a, 32 * role + h, smem, w, G);
    else if (role == 2) role_qk(a, h, smem, w, G);
    else if (role == 3) role_vo(a, h, smem, w, G);
    else if (role == 4) task_u(a, h, smem, w, G);
    else task_w(a, 7 - role, h, smem, w, G);
    if (G.rot == rot && role > 4) part_a(a, smem, x & 3, 4 * (j - 8) + w, w, G.sel);
    if (G.rot == rot) break;
    __syncthreads();
  }
  asm volatile(".Lchain_end:");
}

extern "C" void kernel_launch(void* const* d_in, const int* in_sizes, int n_in,
                              void* d_out, int out_size, void* d_ws, size_t ws_size,
                              hipStream_t stream) {
  const u16* A_q = (const u16*)d_in[0];
  const u16* A_k = (const u16*)d_in[1];
  const u16* A_v = (const u16*)d_in[2];
  const u16* A_shift = (const u16*)d_in[3];
  const u16* A_ssmk = (const u16*)d_in[4];
  const u16* A_D = (const u16*)d_in[5];
  const u16* B_x = (const u16*)d_in[6];
  const u16* B_mem0 = (const u16*)d_in[7];
  const u16* B_Wq = (const u16*)d_in[8];
  const u16* B_Wk = (const u16*)d_in[9];
  const u16* B_Wv = (const u16*)d_in[10];
  const u16* B_Wo = (const u16*)d_in[11];
  u16* out = (u16*)d_out;
  char* ws = (char*)(((uintptr_t)d_ws + (2u << 20) - 1) & ~(uintptr_t)((2u << 20) - 1)) + WSOFF * MB;
  hipFuncSetAttribute((const void*)chain_kernel, hipFuncAttributeMaxDynamicSharedMemorySize, CHAIN_LDS);
  ChainArgs ca;
  ca.prof = (u64*)((char*)d_ws + PROF_AT);
  ca.mem0 = B_mem0; ca.Wq = B_Wq; ca.Wk = B_Wk; ca.Wv = B_Wv; ca.Wo = B_Wo; ca.X = B_x;
  ca.out = out; ca.sync = ws;
  ca.Aq = A_q; ca.Ak = A_k; ca.Av = A_v; ca.Ashift = A_shift; ca.Assm = A_ssmk; ca.AD = A_D;
  chain_kernel<<<256, 256, CHAIN_LDS, stream>>>(ca);
}
```

```cpp
#include <hip/hip_runtime.h>
#include <hip/hip_bf16.h>
#include <stdint.h>
#include <type_traits>

constexpr int S_LEN = 2048;
constexpr int D_A   = 1024;
constexpr int DM    = 1024;
constexpr int NH    = 32;
constexpr int DH    = 128;
constexpr int HD    = NH * DH;
constexpr int MEMR  = 8;
constexpr int SEG   = 32;
constexpr int NSEG  = S_LEN / SEG;
constexpr int OUTW  = 2048;

typedef unsigned short u16;
typedef unsigned long long u64;
typedef short bf16x8 __attribute__((ext_vector_type(8)));
typedef short bf16x4 __attribute__((ext_vector_type(4)));
typedef float f32x4 __attribute__((ext_vector_type(4)));
typedef float f32x16 __attribute__((ext_vector_type(16)));
typedef unsigned u32x4 __attribute__((ext_vector_type(4)));
typedef unsigned u32x2 __attribute__((ext_vector_type(2)));
#define LAS __attribute__((address_space(3)))
#define GAS __attribute__((address_space(1)))

constexpr size_t MB = 1u << 20;
constexpr size_t PROF_AT = 0;

__device__ __forceinline__ float bf2f(u16 b) { return __uint_as_float(((unsigned)b) << 16); }
__device__ __forceinline__ u16 f2bf(float f) { __bf16 b = (__bf16)f; return __builtin_bit_cast(u16, b); }
__device__ __forceinline__ float rbf(float f) { return bf2f(f2bf(f)); }
__device__ __forceinline__ unsigned pk2(float a, float b) {
  typedef float f2 __attribute__((ext_vector_type(2)));
  typedef __bf16 b2 __attribute__((ext_vector_type(2)));
  f2 v = {a, b}; b2 r = __builtin_convertvector(v, b2); return __builtin_bit_cast(unsigned, r);
}

__device__ __forceinline__ int opaque_tid(int wv) {
  int l;
  asm volatile("v_mbcnt_lo_u32_b32 %0, -1, 0\n\tv_mbcnt_hi_u32_b32 %0, -1, %0" : "=v"(l));
  return wv * 64 + l;
}
__device__ __forceinline__ int swz_b(int row) { return ((row & 3) << 2) | ((row >> 2) & 3); }

#define KNOB(name, v) constexpr int name = v
KNOB(K_DEF, 70);
KNOB(W_STEP, 33); KNOB(W_OFF, 56);
#define RHOV 3
KNOB(RHO, RHOV);
#define CDUR 1490
#define U_SLP 8
#define U_STG 0
#define U_ST0 0
#define WSOFF 122
#define START_SLP 3
#define QK_D 0
#define U_DRY 0
#define A1_0 20
#define A1_D 4
#define PSEG 1
#define KQ_NOW 2
#define CALN 3
#define CALM 500u
#define CALT 680u
#define KQ_D 350
#define W_NOW 4
#define U_SIG2 11
#define U_LD 5
#define U_NOW 6
#define WRONG 0
#define OUTP "sc1"
#define LDNT(p) __builtin_nontemporal_load(p)
constexpr int CHK      = 16384;
constexpr int PAGE     = 2 << 20;
constexpr int SY_UM    = 34 * PAGE;
constexpr int SY_HOT   = 32 * PAGE;
constexpr int H_MEM    = 0;
constexpr int H_P      = 16 * CHK;
constexpr int H_ANN    = 28 * CHK;
constexpr int H_DLT    = 32 * CHK;
constexpr int H_TR     = 33 * CHK;
constexpr int H_PX     = 64 * CHK;
constexpr int H_BYTES  = 128 * CHK;
constexpr int URING    = 16;
constexpr int SY_SET   = 52 * PAGE;
constexpr int SY_EPOCH = SY_SET + 128;
constexpr int SY_ABORT = SY_SET + 256;
constexpr int SY_SEL   = SY_EPOCH + 8;
constexpr int SY_CT    = SY_SET + CHK;
constexpr int CT_Q = 0, CT_K = 32, CT_U = 128;
constexpr int CT_V = 0, CT_DONE = 32, CT_A2 = 48, CT_A1 = 128, CT_TK = 132;
constexpr int CT_ARR = -124, CT_INIT = -116, CT_CAL = 256;
constexpr int SY_KV    = 48 * PAGE;
constexpr int SY_BYTES = SY_CT + (CT_CAL + 64) * 128;
constexpr unsigned SENT = 0xFFFFFFFFu;
constexpr unsigned SPIN_CAP = 1u << 19;
constexpr int NLF = 16;
constexpr int L_WTS = 0;
constexpr int L_MEM = L_WTS + 4 * NLF * 2048;
constexpr int L_XCH = L_MEM + 16384;
constexpr int L_XK  = L_XCH + 2048;
constexpr int L_KX  = L_XCH + 4096;
constexpr int L_PRF = L_KX + 8192;
constexpr int XRS = 40;
constexpr int CHAIN_LDS = 160 * 1024;
static_assert(L_XCH + 128 * XRS <= L_PRF && L_PRF + 4096 <= CHAIN_LDS, "lds");

typedef __bf16 bfv8 __attribute__((ext_vector_type(8)));
typedef __bf16 bfv16 __attribute__((ext_vector_type(16)));
typedef unsigned u32x8 __attribute__((ext_vector_type(8)));
typedef unsigned u32x12 __attribute__((ext_vector_type(12)));
typedef unsigned u32x16 __attribute__((ext_vector_type(16)));
typedef unsigned u32x32 __attribute__((ext_vector_type(32)));
struct Wts { u32x8 a[32]; u32x32 v[4]; };

#define STAMPL(i) do {} while (0)
#define PVAL(i, v) do {} while (0)
#define PDECL(base, str) do {} while (0)
#define PZERO do {} while (0)
#define PDUMP(mask) do {} while (0)
#define TA(x) do {} while (0)
#define TB(x) TS(x)
#define TS(x) do {} while (0)
#define SSTAMP(i) do {} while (0)
#define WST(c, i) do {} while (0)
#define AST(i) do {} while (0)
struct ChainArgs {
  u64* prof;
  const u16* mem0; const u16* Wq; const u16* Wk; const u16* Wv; const u16* Wo; const u16* X;
  u16* out; char* sync;
  const u16* Aq; const u16* Ak; const u16* Av; const u16* Ashift; const u16* Assm; const u16* AD;
};
__device__ __forceinline__ GAS unsigned* ctr(const ChainArgs& a, int i) { return (GAS unsigned*)(a.sync + SY_CT + i * 128); }
__device__ __forceinline__ void ctr_add(const ChainArgs& a, int i, unsigned v) {
  __hip_atomic_fetch_add(ctr(a, i), v, __ATOMIC_RELAXED, __HIP_MEMORY_SCOPE_AGENT);
}

__device__ __forceinline__ __amdgpu_buffer_rsrc_t mk_rsrc(const void* p, unsigned bytes) {
  const u64 v = (u64)p;
  const unsigned lo = (unsigned)__builtin_amdgcn_readfirstlane((unsigned)v), hi = (unsigned)__builtin_amdgcn_readfirstlane((unsigned)(v >> 32));
  const u64 u = (u64)lo | ((u64)hi << 32);
  return __builtin_amdgcn_make_buffer_rsrc((void*)u, 0, bytes, 0x00020000);
}
__device__ __forceinline__ u32x4 own_rsrc(const void* p, unsigned bytes) {
  u32x4 r = {(unsigned)__builtin_amdgcn_readfirstlane((unsigned)(u64)p), (unsigned)__builtin_amdgcn_readfirstlane((unsigned)((u64)p >> 32)) & 0xffffu, bytes, 0x00020000u};
  asm volatile("s_nop 4" : "+s"(r));
  return r;
}
constexpr int LD_AUX = 16, ST_AUX = 16;
__device__ __forceinline__ u32x4 ld16(__amdgpu_buffer_rsrc_t rs, unsigned off) {
  return __builtin_amdgcn_raw_buffer_load_b128(rs, off, 0, LD_AUX);
}
__device__ __forceinline__ void st16(__amdgpu_buffer_rsrc_t rs, unsigned off, u32x4 v) {
  __builtin_amdgcn_raw_buffer_store_b128(v, rs, off, 0, ST_AUX);
}
__device__ __forceinline__ void st16s(u32x4 v, __amdgpu_buffer_rsrc_t rs, unsigned voff, unsigned soff) {
  __builtin_amdgcn_raw_buffer_store_b128(v, rs, voff, soff, ST_AUX);
  asm volatile("s_nop 1" :: "v"(v) : "memory");
}
__device__ __forceinline__ void st8(__amdgpu_buffer_rsrc_t rs, unsigned off, u32x2 v) {
  __builtin_amdgcn_raw_buffer_store_b64(v, rs, off, 0, ST_AUX);
}
__device__ __forceinline__ void st4(__amdgpu_buffer_rsrc_t rs, unsigned off, unsigned v) {
  __builtin_amdgcn_raw_buffer_store_b32(v, rs, off, 0, ST_AUX);
}
__device__ __forceinline__ unsigned ld4(__amdgpu_buffer_rsrc_t rs, unsigned off) {
  return __builtin_amdgcn_raw_buffer_load_b32(rs, off, 0, LD_AUX);
}
constexpr int TU = 16;
__device__ __forceinline__ unsigned cyc32() { return (unsigned)__builtin_amdgcn_s_memtime(); }
__device__ __forceinline__ unsigned to_sgpr(unsigned x) {
  unsigned r;
  asm volatile("s_nop 0\n\tv_readfirstlane_b32 %0, %1\n\ts_nop 4" : "=s"(r) : "v"(x));
  return r;
}
struct Clk {
  unsigned ce, te;
  unsigned c2t, t2c;
  __device__ __forceinline__ void edge(unsigned& c, unsigned& t) {
    u64 c0, r0, r1, r2, r3, r4, r5;
    asm volatile("s_memtime %0\n\ts_memrealtime %1\n\ts_nop 4\n\ts_memrealtime %2\n\ts_nop 4\n\ts_memrealtime %3\n\ts_nop 4\n\t"
                 "s_memrealtime %4\n\ts_nop 4\n\ts_memrealtime %5\n\ts_nop 4\n\ts_memrealtime %6\n\ts_waitcnt lgkmcnt(0)"
                 : "=&s"(c0), "=&s"(r0), "=&s"(r1), "=&s"(r2), "=&s"(r3), "=&s"(r4), "=&s"(r5) :: "memory");
    const unsigned x = (unsigned)r0;
    const unsigned n = ((unsigned)r1 == x) + ((unsigned)r2 == x) + ((unsigned)r3 == x) + ((unsigned)r4 == x) + ((unsigned)r5 == x);
    c = to_sgpr((unsigned)c0 + 24u * n); t = (x + 4u) * TU;
  }
  __device__ __forceinline__ void init() { edge(ce, te); c2t = 44000u; t2c = 97600u; }
  __device__ __forceinline__ void sync() {
    unsigned c, t;
    edge(c, t);
    set(c, t);
  }
  __device__ __forceinline__ void set(unsigned c, unsigned t) {
    if (c == ce) return;
    const float q = (float)(t - te) * __builtin_amdgcn_rcpf((float)(c - ce));
    const float qc = fminf(fmaxf(q, 0.51f), 1.99f);
    c2t = to_sgpr((unsigned)(qc * 65536.f));
    t2c = to_sgpr((unsigned)(__builtin_amdgcn_rcpf(qc) * 65536.f));
    ce = c; te = t;
  }
  __device__ __forceinline__ unsigned time(unsigned c) const { return te + ((min(c - ce, 30000u) * c2t) >> 16); }
  __device__ __forceinline__ unsigned now() const { return time(cyc32()); }
  __device__ __forceinline__ unsigned target(unsigned t) const {
    return to_sgpr(ce + (((unsigned)min(max((int)(t - te), 0), 20000) * t2c) >> 16));
  }
  __device__ __forceinline__ void wait(unsigned t) const { wait_c(target(t)); }
  __device__ __forceinline__ void wait_c(unsigned c) const {
    unsigned y;
    asm volatile("1:\n\ts_memtime vcc\n\ts_waitcnt lgkmcnt(0)\n\ts_sub_i32 %0, %1, vcc_lo\n\ts_cmp_gt_i32 %0, 128\n\ts_cbranch_scc1 1b\n\t"
                 "2:\n\ts_sub_i32 %0, %0, %2\n\ts_cmp_gt_i32 %0, %3\n\ts_cbranch_scc1 2b"
                 : "=&s"(y) : "s"(c), "n"(W_STEP), "n"(W_OFF) : "memory", "scc", "vcc");
  }
};
__device__ __forceinline__ unsigned nosent(unsigned d) { return min(d, SENT - 1u); }
__device__ __forceinline__ int arow(int row, int half) { return 4 * (row >> 1) + 2 * half + (row & 1); }
__device__ __forceinline__ unsigned umax4(u32x4 v) { return max(max(v[0], v[1]), max(v[2], v[3])); }
__device__ __forceinline__ void stamp_again(GAS u64* p) {
  __hip_atomic_store(p, (__builtin_amdgcn_s_memrealtime() & ~7ull) | (__hip_atomic_load(p, __ATOMIC_RELAXED, __HIP_MEMORY_SCOPE_AGENT) & 7ull),
                     __ATOMIC_RELAXED, __HIP_MEMORY_SCOPE_AGENT);
}
__device__ __forceinline__ bool poll_fail(unsigned& spins, GAS unsigned* abortw, int lane) {
  ++spins;
  if (spins > SPIN_CAP) {
    if (lane == 0) __hip_atomic_store(abortw, 1u, __ATOMIC_RELAXED, __HIP_MEMORY_SCOPE_AGENT);
    return true;
  }
  if ((spins & 511u) == 0u && lane == 0) {
    unsigned n = 0;
    for (int i = 0; i < 8; ++i) n += __hip_atomic_load(abortw + (SY_CT - SY_ABORT + (CT_ARR + i) * 128) / 4, __ATOMIC_RELAXED, __HIP_MEMORY_SCOPE_AGENT) + 1u;
    if (n < 256u) stamp_again((GAS u64*)(abortw - 32));
  }
  if ((spins & 63u) == 0u)
    return __builtin_amdgcn_readfirstlane(__hip_atomic_load(abortw, __ATOMIC_RELAXED, __HIP_MEMORY_SCOPE_AGENT)) != SENT;
  return false;
}
#define TP(k) do {} while (0)
struct Gate {
  u64 tx; u64 entry, sel; int arr; unsigned rot; bool open;
  __device__ __forceinline__ bool look(char* sync, u32x4 v) {
    if (open) return true;
    for (unsigned spins = 1;; ++spins) {
#pragma unroll
      for (int i = 0; i < 4; ++i) v[i] = __builtin_amdgcn_readfirstlane(v[i]);
      if (((u64)v[1] << 32 | v[0]) - entry + 2000u < (1ull << 31) || spins > 4 * SPIN_CAP) break;
      if (spins > 1) __builtin_amdgcn_s_sleep(8);
      v = ld16(mk_rsrc(sync + SY_EPOCH, 16), 0);
    }
    asm volatile("" ::: "memory");
    sel = (u64)v[3] << 32 | v[2];
    const bool same = rot == (v[0] & 7u);
    rot = v[0] & 7u; open = true;
    return same;
  }
  __device__ __forceinline__ void ask(char* sync, u32x4& v) { asm volatile("s_nop 4\n\tbuffer_load_dwordx4 %0, off, %1, 0 sc1" : "=v"(v) : "s"(own_rsrc(sync + SY_EPOCH, 16)) : "memory"); }
  __device__ __forceinline__ void arrive(char* sync) {
    if (arr >= 0 && opaque_tid(0) == 0) __hip_atomic_fetch_add((GAS unsigned*)(sync + SY_CT + (CT_ARR + arr) * 128), 1u, __ATOMIC_RELAXED, __HIP_MEMORY_SCOPE_AGENT);
    arr = -1;
  }
  __device__ __forceinline__ bool pass(char* sync, u32x4 v = {0u, 0u, 0u, 0u}) { const bool r = look(sync, v); arrive(sync); return r; }
};
__device__ __forceinline__ unsigned ann_val(unsigned t, int s) { return (t << 8) | (unsigned)s; }
__device__ __forceinline__ unsigned tr_place(int s) { return (s & 2) * (CHK / 2) + (s & 1) * 4096; }
__device__ __forceinline__ unsigned die_half() { return ((unsigned)__builtin_amdgcn_s_getreg(20 | (3 << 11)) >> 2) & 1u; }
typedef u64 Sel;
template <class F>
__device__ __forceinline__ void start_wait(char* sync, GAS unsigned* abortw, int lane, bool& dead, int ci, unsigned n, F f) {
  unsigned spins = 0, v;
  const bool m = lane < 4 || ci < 0;
  while (true) {
    v = ld4(mk_rsrc(sync + SY_SET, SY_BYTES - SY_SET), lane < 6 ? CHK + (m ? CT_INIT + (lane & 3) : ci + 32 * (lane & 1)) * 128 : 0x7fffffffu) + 1u;
    asm volatile("" ::: "memory");
    f();
    if (__all(lane > 5 || v >= (m ? 16u : n))) break;
    __builtin_amdgcn_s_sleep(START_SLP);
    if (dead || poll_fail(spins, abortw, lane)) { dead = true; break; }
  }
}
__device__ __forceinline__ __amdgpu_buffer_rsrc_t rsrc_u(char* sync) { return mk_rsrc((void*)sync, SY_SET); }
__device__ __forceinline__ unsigned near_half(unsigned off, u64 sel) { return off + ((unsigned)(sel >> ((off >> 21) & 63)) & 1u) * 8192; }
__device__ __forceinline__ unsigned x_slot(int kind, int s, int h, Sel sel) {
  return kind == 2 ? near_half(32 * PAGE + (s * 32 + h) * CHK, ~sel) : near_half((s * 64 + 2 * h + 1 - kind) * CHK, sel);
}
__device__ __forceinline__ int ct_near(Sel sel) { return 64 * ((unsigned)(sel >> (SY_SET / PAGE)) & 1u); }
__device__ __forceinline__ unsigned kv_row(int d, Sel sel) { return near_half(SY_KV + (d >> 1) * CHK, ~sel) + (d & 1) * 4096; }
__device__ __forceinline__ int hpos(int h) { return (h & 3) * 8 + (h >> 2); }
constexpr unsigned UXH = 8 * CHK;
__device__ __forceinline__ unsigned ux_base(int s, int h, Sel sel) { return near_half(((s & (URING - 1)) * 32 + h) * UXH, ~sel); }
__device__ __forceinline__ constexpr unsigned ux_tile(int t) { return (t >> 3) * CHK + (t & 7) * 1024; }
__device__ __forceinline__ __amdgpu_buffer_rsrc_t rsrc_hot(char* sync, Sel sel) {
  return mk_rsrc((void*)(sync + near_half(SY_HOT, sel)), H_BYTES - 8192);
}
template <int SLP = 4>
__device__ __forceinline__ void ctr_wait(GAS unsigned* c, unsigned need, GAS unsigned* abortw, int lane, bool& dead) {
  unsigned spins = 0;
  while (true) {
    unsigned v = __builtin_amdgcn_readfirstlane(__hip_atomic_load(c, __ATOMIC_RELAXED, __HIP_MEMORY_SCOPE_AGENT)) + 1u;
    if (v >= need) break;
    __builtin_amdgcn_s_sleep(SLP);
    if (dead || poll_fail(spins, abortw, lane)) { dead = true; break; }
  }
}
template <int N> __device__ __forceinline__ void wait_vm() { asm volatile("s_waitcnt vmcnt(%0)" :: "n"(N) : "memory"); }
__device__ __forceinline__ void lds_barrier() {
  asm volatile("s_waitcnt lgkmcnt(0)" ::: "memory");
  __builtin_amdgcn_s_barrier();
  asm volatile("" ::: "memory");
}
__device__ __forceinline__ f32x4 vzero() { float z; asm volatile("v_mov_b32 %0, 0" : "=v"(z)); f32x4 r = {z, z, z, z}; asm volatile("" : "+v"(r)); return r; }
__device__ __forceinline__ f32x4 smf(u32x4 a, u32x8 b, f32x4 c, int idx) {
  return __builtin_amdgcn_smfmac_f32_16x16x64_bf16(__builtin_bit_cast(bfv8, a), __builtin_bit_cast(bfv16, b), c, idx, 0, 0);
}
__device__ __forceinline__ float red16max(float x) { auto r = __builtin_amdgcn_permlane16_swap(__float_as_uint(x), __float_as_uint(x), false, false); return fmaxf(__uint_as_float(r[0]), __uint_as_float(r[1])); }
__device__ __forceinline__ float red32max(float x) { auto r = __builtin_amdgcn_permlane32_swap(__float_as_uint(x), __float_as_uint(x), false, false); return fmaxf(__uint_as_float(r[0]), __uint_as_float(r[1])); }
__device__ __forceinline__ float red16add(float x) { auto r = __builtin_amdgcn_permlane16_swap(__float_as_uint(x), __float_as_uint(x), false, false); return __uint_as_float(r[0]) + __uint_as_float(r[1]); }
__device__ __forceinline__ float red32add(float x) { auto r = __builtin_amdgcn_permlane32_swap(__float_as_uint(x), __float_as_uint(x), false, false); return __uint_as_float(r[0]) + __uint_as_float(r[1]); }
constexpr bool blk_in_lds(int R, int b) {
  return R == 0 ? ((b & 7) == 1 || (b & 7) == 4) : (b < 16 ? (b & 3) == 0 : (b & 3) == 3);
}
constexpr int lds_index(int R, int b) { int n = 0; for (int i = 0; i < b; ++i) if (blk_in_lds(R, i)) ++n; return n; }
constexpr bool frag_in_lds(int R, int f) { return blk_in_lds(R, f >> 1); }
constexpr int frag_reg_slot(int R, int f) { int n = 0; for (int i = 0; i < f; ++i) if (!frag_in_lds(R, i)) ++n; return n; }

constexpr int m_block(int R, int m, int i) {
  return R == 0 ? 2 * (i < 12 ? (m ? i + i / 3 + 1 : i + (i + 1) / 3) : 4 * (i - 12) + 2 - 2 * m) + m : m ? 16 + 4 * (i & 3) + (i >> 2) : (i < 12 ? i + i / 3 + 1 : 4 * (i - 12));
}
#define QK_REV 0
#define W_BV 0xba9876543210fedcull
#define VO_ORD 3
constexpr int w_block(int R, int i) {
  if (R && VO_ORD == 2) return i < 12 ? m_block(1, 0, 11 - i) : m_block(1, 1, i - 12);
  if (R && VO_ORD == 3) return i & 1 ? m_block(1, 1, i >> 1) : m_block(1, 0, 11 - (i >> 1));
  if (!R && QK_REV) return m_block(0, i & 1, 11 - (i >> 1));
  return R && VO_ORD ? m_block(R, i < 12, i % 12) : m_block(R, i & 1, i >> 1);
}
#define W_BG 0xfedcba9876543210ull
#define W_NT 0
#define U_ORD 0x3210
constexpr u64 bg_order(int m) { u64 r = 0; for (int n = 0; n < 16; ++n) r |= (u64)(m_block(0, m > 0, ((m == 2 ? W_BV : W_BG) >> (4 * n)) & 15) >> 1) << (4 * n); return r; }
struct WSched { int ord[32]; int slot[32]; int issue[32]; int out[32]; int reg[32]; };
constexpr WSched make_wsched(int R) {
  WSched S{};
  int n = 0;
  for (int i = 0; i < 24; ++i) S.ord[n++] = w_block(R, i);
  for (int i = 0; i < n; ++i) S.reg[i] = frag_reg_slot(R, 2 * S.ord[i]);
  const int npure = n;
  for (int b = 0; b < 32; ++b) if (blk_in_lds(R, b)) S.ord[n++] = b;
  int seq[32] = {};
  int nissued = 0, next = 0;
  for (int i = 0; i < 10; ++i) { S.slot[i] = (i + 10 - npure % 10) % 10; S.issue[i] = -1; seq[i] = nissued++; }
  next = 10;
  S.out[0] = nissued - 1;
  for (int c = 0; c <= npure; ++c) {
    if (c >= 1) {
      const int k = S.slot[c - 1];
      if (next < npure) { S.slot[next] = k; S.issue[next] = c; seq[next] = nissued++; ++next; }
      else if (k < NLF / 2) { S.slot[npure + k] = k; S.issue[npure + k] = c; seq[npure + k] = nissued++; }
    }
    if (c + 1 < npure) S.out[c + 1] = nissued - 1 - seq[c + 1];
  }
  return S;
}
__device__ __forceinline__ int kcol(int k) { return 8 * ((k >> 1) & 1) + ((k & 1) ? 7 - (k >> 2) : k >> 2); }
template <bool PERM = false>
__device__ __forceinline__ u32x8 tr_frag(const char* sb, int tile, int lane) {
  u32x8 f;
#pragma unroll
  for (int q = 0; q < 4; ++q) {
    int row = (q >> 1) * 32 + 8 * (lane >> 4) + 4 * (q & 1) + ((lane & 15) >> 2);
    if (PERM) row = (row & ~15) + kcol(row & 15);
    int p = lane & 3;
    int lc = 2 * tile + (p >> 1);
    int addr = row * 256 + ((lc ^ swz_b(row)) * 16) + 8 * (p & 1);
    bf16x4 v = __builtin_amdgcn_ds_read_tr16_b64_v4i16((LAS bf16x4*)(sb + addr));
    u32x2 u = __builtin_bit_cast(u32x2, v);
    f[2 * q] = u[0]; f[2 * q + 1] = u[1];
  }
  return f;
}
__device__ __forceinline__ void dma16(__amdgpu_buffer_rsrc_t rs, unsigned voff, unsigned soff, unsigned lds, bool nt = false, bool pad = true) {
  if (nt) asm volatile("s_mov_b32 m0, %0\n\ts_nop 3\n\tbuffer_load_dwordx4 %1, %2, %3 offen nt lds" :: "s"(lds), "v"(voff), "s"(rs), "s"(soff) : "m0", "memory");
  else if (pad) asm volatile("s_mov_b32 m0, %0\n\ts_nop 3\n\tbuffer_load_dwordx4 %1, %2, %3 offen lds" :: "s"(lds), "v"(voff), "s"(rs), "s"(soff) : "m0", "memory");
  else asm volatile("s_mov_b32 m0, %0\n\ts_nop 0\n\tbuffer_load_dwordx4 %1, %2, %3 offen lds" :: "s"(lds), "v"(voff), "s"(rs), "s"(soff) : "m0", "memory");
}
__device__ __forceinline__ u32x8 tr_frag2(const char* sb, int tile, int f, int lane) {
  u32x8 r;
#pragma unroll
  for (int c = 0; c < 2; ++c)
#pragma unroll
    for (int t = 0; t < 2; ++t) {
      const int row = 32 * c + 16 * f + 4 * (lane >> 4) + ((lane & 15) >> 2);
      const int p = lane & 3, lc = 2 * (tile + t) + (p >> 1);
      const u32x2 u = __builtin_bit_cast(u32x2, __builtin_amdgcn_ds_read_tr16_b64_v4i16((LAS bf16x4*)(sb + row * 256 + ((lc ^ swz_b(row)) * 16) + 8 * (p & 1))));
      r[4 * c + t] = u[0]; r[4 * c + 2 + t] = u[1];
    }
  return r;
}
template <int ROLE>
__device__ __forceinline__ void load_weights(const ChainArgs& a, int h, int w, int lane, char* smem, Wts& W, Gate& G, u32x4& gv) {
#pragma unroll
  for (int i = 0; i < 4; ++i) asm volatile("" : "=v"(W.v[i]));
#pragma unroll
  for (int i = 0; i < 32; ++i) asm volatile("" : "=a"(W.a[i]));
  constexpr WSched S = make_wsched(ROLE);
  constexpr int NP = 32 - NLF / 2;
  const __amdgpu_buffer_rsrc_t r0 = mk_rsrc(ROLE == 0 ? a.Wq : a.Wv, 8u << 20), r1 = mk_rsrc(ROLE == 0 ? a.Wk : a.Wo, 8u << 20);
  const unsigned lds0 = (unsigned)(uintptr_t)(LAS char*)smem;
  unsigned vo[2][4];
#pragma unroll
  for (int j = 0; j < 4; ++j) {
    const int row = (w * 4 + j) * 4 + (lane >> 4), lc = (lane & 15) ^ swz_b(row);
    vo[0][j] = row * HD * 2 + lc * 16; vo[1][j] = row * DM * 2 + lc * 16;
  }
  asm volatile("s_nop 4");
  auto stage = [&](int i) {
    const int blk = S.ord[i];
    const bool second = ROLE == 0 ? (blk & 1) : blk >= 16, wo = ROLE == 1 && second;
    const unsigned so = wo ? (unsigned)(((128 * h + 64 * (blk & 1)) * DM + 128 * (((blk - 16) >> 1) ^ (2 * (h & 3)))) * 2)
                           : (unsigned)((64 * ((ROLE == 0 ? blk >> 1 : blk) ^ (h & RHO)) * HD + 128 * h) * 2);
#pragma unroll
    for (int j = 0; j < 4; ++j) dma16(second ? r1 : r0, vo[wo][j], so, lds0 + 16384 * S.slot[i] + (w * 4 + j) * 1024, W_NT & (ROLE ? 8 : 1));
  };
#pragma unroll
  for (int i = 0; i < 32; ++i) if (S.issue[i] == -1) stage(i);
#pragma unroll
  for (int c = 0; c < NP; ++c) {
    asm volatile("s_waitcnt vmcnt(%0)" :: "n"(4 * S.out[c]) : "memory");
    lds_barrier();
#pragma unroll
    for (int i = 0; i < 32; ++i) if (S.issue[i] == c) stage(i);
    const char* sb = smem + 16384 * S.slot[c];
#pragma unroll
    for (int t = 0; t < 2; ++t) {
      u32x8 f = (ROLE == 1 && S.ord[c] >= 16) ? tr_frag2(sb, 2 * w, t, lane) : tr_frag<true>(sb, 2 * w + t, lane);
      if (S.reg[c] < 32) {
#pragma unroll
        for (int i = 0; i < 8; ++i) asm volatile("v_accvgpr_write_b32 a[%1], %0" :: "v"(f[i]), "n"(8 * ((S.reg[c] + t) & 31) + i));
      }
      else {
#pragma unroll
        for (int i = 0; i < 8; ++i) W.v[(S.reg[c] >> 2) & 3][8 * ((S.reg[c] + t) & 3) + i] = f[i];
      }
    }
    if (S.reg[c] >= 32) asm volatile("" : "+v"(W.v[(S.reg[c] >> 2) & 3]));
  }
  static_assert(S.issue[31] < NP && S.issue[NP] < S.issue[31], "resident blocks are issued in slot order inside the loop");
  G.ask(a.sync, gv);
#pragma unroll
  for (int j = 0; j < NLF / 2; ++j) {
    asm volatile("s_waitcnt vmcnt(%0)" :: "n"(4 * (NLF / 2 - 1 - j) + 1) : "memory");
    lds_barrier();
    const bool two = ROLE == 1 && S.ord[NP + j] >= 16;
    u32x8 f0 = two ? tr_frag2(smem + 16384 * j, 2 * w, 0, lane) : tr_frag<true>(smem + 16384 * j, 2 * w, lane);
    u32x8 f1 = two ? tr_frag2(smem + 16384 * j, 2 * w, 1, lane) : tr_frag<true>(smem + 16384 * j, 2 * w + 1, lane);
    lds_barrier();
    char* d = smem + 16384 * j + w * 4096 + lane * 16;
    *(u32x4*)d = (u32x4){f0[0], f0[1], f0[2], f0[3]};
    *(u32x4*)(d + 1024) = (u32x4){f0[4], f0[5], f0[6], f0[7]};
    *(u32x4*)(d + 2048) = (u32x4){f1[0], f1[1], f1[2], f1[3]};
    *(u32x4*)(d + 3072) = (u32x4){f1[4], f1[5], f1[6], f1[7]};
  }
  asm volatile("s_waitcnt vmcnt(0)" : "+v"(gv) :: "memory");
  lds_barrier();
}
template <int... Is> struct iseq {};
template <int N, int... Is> struct mkseq : mkseq<N - 1, N - 1, Is...> {};
template <int... Is> struct mkseq<0, Is...> { typedef iseq<Is...> type; };

#define SB0 __builtin_amdgcn_sched_barrier(0)
#define CAT8(x, y) ((u32x8){x[0], x[1], x[2], x[3], y[0], y[1], y[2], y[3]})
template <class F, int... I>
__device__ __forceinline__ void for_slots(iseq<I...>, F f) { (f(std::integral_constant<int, I>{}), ...); }
#define QK_BODY \
  "s_cmp_eq_u32 %[wv], 1\n\t" \
  "s_cbranch_scc1 91f\n\t" \
  "s_cmp_eq_u32 %[wv], 2\n\t" \
  "s_cbranch_scc1 92f\n\t" \
  "s_cmp_eq_u32 %[wv], 3\n\t" \
  "s_cbranch_scc1 93f\n\t" \
  "90:\n\t" \
  "s_mov_b32 s58, s42\n\t" \
  "20:\n\t" \
  "s_and_b32 s76, s44, 7\n\t" \
  "s_lshl_b32 s76, s76, 15\n\t" \
  "s_add_i32 s40, s54, s76\n\t" \
  "s_and_b32 s76, s44, 3\n\t" \
  "s_lshl_b32 s45, s76, 14\n\t" \
  "s_mul_i32 s47, s76, 0xc000\n\t" \
  "s_add_i32 s47, s47, 0x40000\n\t" \
  "s_add_i32 s49, s44, -1\n\t" \
  "s_and_b32 s48, s49, 2\n\t" \
  "s_lshl_b32 s48, s48, 13\n\t" \
  "s_and_b32 s76, s49, 1\n\t" \
  "s_lshl_b32 s76, s76, 12\n\t" \
  "s_add_i32 s48, s48, s76\n\t" \
  "s_lshl_b32 s50, s53, 8\n\t" \
  "s_mov_b32 s59, 0\n\t" \
  "s_mov_b32 s92, s51\n\t" \
  "s_mov_b32 s93, s53\n\t" \
  "s_xor_b32 s76, s40, 0x400\n\t" \
  "s_xor_b32 s77, s40, 0x800\n\t" \
  "s_xor_b32 s78, s40, 0xc00\n\t" \
  "s_cmp_lg_u32 s58, 0\n\t" \
  "s_cbranch_scc1 11f\n\t" \
  "10:\n\t" \
  "ds_read_b128 v[48:51], v102 offset:0\n\t" \
  "ds_read_b128 v[52:55], v102 offset:1024\n\t" \
  "ds_read_b128 v[56:59], v102 offset:2048\n\t" \
  "ds_read_b128 v[60:63], v102 offset:3072\n\t" \
  "ds_read_b128 v[16:19], v102 offset:16384\n\t" \
  "ds_read_b128 v[20:23], v102 offset:17408\n\t" \
  "ds_read_b128 v[24:27], v102 offset:18432\n\t" \
  "ds_read_b128 v[28:31], v102 offset:19456\n\t" \
  "ds_read_b128 v[84:87], v102 offset:49152\n\t" \
  "ds_read_b128 v[88:91], v102 offset:50176\n\t" \
  "ds_read_b128 v[92:95], v102 offset:51200\n\t" \
  "ds_read_b128 v[96:99], v102 offset:52224\n\t" \
  "s_waitcnt lgkmcnt(4)\n\t" \
  "ds_read_b128 v[0:3], v103 offset:16384\n\t" \
  "ds_read_b128 v[4:7], v103 offset:17408\n\t" \
  "ds_read_b128 v[8:11], v103 offset:18432\n\t" \
  "ds_read_b128 v[12:15], v103 offset:19456\n\t" \
  "46:\n\t" \
  "s_bitcmp1_b32 s58, 1\n\t" \
  "s_cselect_b64 s[80:81], -1, 0\n\t" \
  "s_cmp_lg_u32 s58, 0\n\t" \
  "s_cbranch_scc1 17f\n\t" \
  "s_mov_b32 exec_hi, 0\n\t" \
  "16:\n\t" \
  "s_memtime vcc\n\t" \
  "s_waitcnt lgkmcnt(0)\n\t" \
  "s_sub_i32 s79, s41, vcc_lo\n\t" \
  "s_cmp_gt_i32 s79, 64\n\t" \
  "s_cbranch_scc1 16b\n\t" \
  "13:\n\t" \
  "buffer_load_dword v116, v112, %[rh], s48 offen sc1\n\t" \
  "s_waitcnt vmcnt(0)\n\t" \
  "15:\n\t" \
  "v_mov_b32 v117, 20000\n\t" \
  "v_cmp_ne_u32_sdwa vcc, v116, s49 src0_sel:BYTE_0 src1_sel:DWORD\n\t" \
  "v_subrev_u32 v116, s50, v116\n\t" \
  "v_ashrrev_i32 v116, 8, v116\n\t" \
  "s_nop 1\n\t" \
  "v_max_i32_dpp v116, v116, v116 quad_perm:[1,0,3,2] row_mask:0xf bank_mask:0xf\n\t" \
  "s_cbranch_vccnz 14f\n\t" \
  "s_nop 0\n\t" \
  "v_max_i32_dpp v116, v116, v116 quad_perm:[2,3,0,1] row_mask:0xf bank_mask:0xf\n\t" \
  "s_nop 1\n\t" \
  "v_max_i32_dpp v116, v116, v116 row_half_mirror row_mask:0xf bank_mask:0xf\n\t" \
  "s_nop 1\n\t" \
  "v_max_i32_dpp v116, v116, v116 row_mirror row_mask:0xf bank_mask:0xf\n\t" \
  "v_med3_i32 v116, v116, 0, v117\n\t" \
  "v_mul_u32_u24 v116, s52, v116\n\t" \
  "v_lshrrev_b32 v116, 16, v116\n\t" \
  "s_mov_b64 exec, -1\n\t" \
  "v_readfirstlane_b32 s84, v116\n\t" \
  "v_readlane_b32 s85, v116, 16\n\t" \
  "s_max_u32 s84, s84, s85\n\t" \
  "s_add_i32 s84, s84, s51\n\t" \
  "1:\n\t" \
  "s_memtime vcc\n\t" \
  "s_waitcnt lgkmcnt(0)\n\t" \
  "s_sub_i32 s79, s84, vcc_lo\n\t" \
  "s_cmp_gt_i32 s79, 128\n\t" \
  "s_cbranch_scc1 1b\n\t" \
  ".p2align 4\n\t" \
  "2:\n\t" \
  "s_sub_i32 s79, s79, 33\n\t" \
  "s_cmp_gt_i32 s79, 56\n\t" \
  "s_cbranch_scc1 2b\n\t" \
  "buffer_load_dwordx4 v[32:35], v100, %[rh], s40 offen sc1\n\t" \
  "s_nop 12\n\t" \
  "buffer_load_dwordx4 v[36:39], v100, %[rh], s76 offen sc1\n\t" \
  "s_nop 12\n\t" \
  "buffer_load_dwordx4 v[40:43], v100, %[rh], s77 offen sc1\n\t" \
  "s_nop 12\n\t" \
  "buffer_load_dwordx4 v[44:47], v100, %[rh], s78 offen sc1\n\t" \
  "s_memtime vcc\n\t" \
  "s_memrealtime s[82:83]\n\t" \
  "s_nop 4\n\t" \
  "s_memrealtime s[84:85]\n\t" \
  "s_nop 4\n\t" \
  "s_memrealtime s[56:57]\n\t" \
  "s_nop 4\n\t" \
  "s_memrealtime s[86:87]\n\t" \
  "s_nop 4\n\t" \
  "s_memrealtime s[88:89]\n\t" \
  "s_nop 4\n\t" \
  "s_memrealtime s[90:91]\n\t" \
  "s_mov_b32 s92, 0\n\t" \
  "s_waitcnt lgkmcnt(0)\n\t" \
  "s_cmp_eq_u32 s82, s84\n\t" \
  "s_addc_u32 s92, s92, 0\n\t" \
  "s_cmp_eq_u32 s82, s56\n\t" \
  "s_addc_u32 s92, s92, 0\n\t" \
  "s_cmp_eq_u32 s82, s86\n\t" \
  "s_addc_u32 s92, s92, 0\n\t" \
  "s_cmp_eq_u32 s82, s88\n\t" \
  "s_addc_u32 s92, s92, 0\n\t" \
  "s_cmp_eq_u32 s82, s90\n\t" \
  "s_addc_u32 s92, s92, 0\n\t" \
  "s_mul_i32 s92, s92, 24\n\t" \
  "s_add_i32 s92, s92, vcc_lo\n\t" \
  "s_lshl4_add_u32 s93, s82, 64\n\t" \
  "3:\n\t" \
  "s_sub_i32 s84, s92, s43\n\t" \
  "s_lshl_b32 s85, s93, 8\n\t" \
  "s_or_b32 s85, s85, s44\n\t" \
  "v_mov_b32 v118, s85\n\t" \
  "v_mov_b64 v[68:69], 0\n\t" \
  "v_mov_b64 v[70:71], 0\n\t" \
  "v_mov_b64 v[72:73], 0\n\t" \
  "v_mov_b64 v[74:75], 0\n\t" \
  "v_mov_b64 v[76:77], 0\n\t" \
  "v_mov_b64 v[78:79], 0\n\t" \
  "v_mov_b64 v[80:81], 0\n\t" \
  "v_mov_b64 v[82:83], 0\n\t" \
  "s_waitcnt lgkmcnt(0)\n\t" \
  "s_waitcnt vmcnt(3)\n\t" \
  "v_smfmac_f32_16x16x64_bf16 v[68:71], v[32:35], a[0:7], v104\n\t" \
  "ds_write_b128 v101, v[32:35] offset:0\n\t" \
  "v_smfmac_f32_16x16x64_bf16 v[72:75], v[32:35], a[8:15], v104\n\t" \
  "v_smfmac_f32_16x16x64_bf16 v[76:79], v[32:35], v[48:55], v104\n\t" \
  "v_smfmac_f32_16x16x64_bf16 v[80:83], v[32:35], v[56:63], v104\n\t" \
  "s_waitcnt vmcnt(2)\n\t" \
  "v_smfmac_f32_16x16x64_bf16 v[68:71], v[36:39], a[16:23], v104\n\t" \
  "ds_write_b128 v101, v[36:39] offset:1024\n\t" \
  "v_smfmac_f32_16x16x64_bf16 v[72:75], v[36:39], a[24:31], v104\n\t" \
  "v_smfmac_f32_16x16x64_bf16 v[76:79], v[36:39], a[32:39], v104\n\t" \
  "v_cmp_u_f32 vcc, v68, v69\n\t" \
  "v_smfmac_f32_16x16x64_bf16 v[80:83], v[36:39], a[40:47], v104\n\t" \
  "v_cmp_u_f32 s[82:83], v70, v71\n\t" \
  "s_waitcnt vmcnt(1)\n\t" \
  "v_smfmac_f32_16x16x64_bf16 v[68:71], v[40:43], v[16:23], v104\n\t" \
  "ds_write_b128 v101, v[40:43] offset:2048\n\t" \
  "v_smfmac_f32_16x16x64_bf16 v[72:75], v[40:43], v[24:31], v104\n\t" \
  "s_or_b64 vcc, vcc, s[82:83]\n\t" \
  "s_or_b64 vcc, vcc, s[80:81]\n\t" \
  "s_waitcnt lgkmcnt(1)\n\t" \
  "v_smfmac_f32_16x16x64_bf16 v[76:79], v[40:43], a[48:55], v104\n\t" \
  "s_cbranch_scc1 8f\n\t" \
  "5:\n\t" \
  "s_barrier\n\t" \
  "4:\n\t" \
  "s_memtime s[56:57]\n\t" \
  "ds_read_b128 v[48:51], v101 offset:4096\n\t" \
  "v_smfmac_f32_16x16x64_bf16 v[80:83], v[40:43], a[56:63], v104\n\t" \
  "ds_read_b128 v[52:55], v101 offset:5120\n\t" \
  "ds_read_b128 v[56:59], v101 offset:8192\n\t" \
  "s_waitcnt vmcnt(0)\n\t" \
  "v_smfmac_f32_16x16x64_bf16 v[68:71], v[44:47], a[64:71], v104\n\t" \
  "ds_write_b128 v101, v[44:47] offset:3072\n\t" \
  "v_smfmac_f32_16x16x64_bf16 v[72:75], v[44:47], a[72:79], v104\n\t" \
  "v_smfmac_f32_16x16x64_bf16 v[76:79], v[44:47], a[80:87], v104\n\t" \
  "v_cmp_u_f32 vcc, v68, v69\n\t" \
  "v_smfmac_f32_16x16x64_bf16 v[80:83], v[44:47], a[88:95], v104\n\t" \
  "v_cmp_u_f32 s[82:83], v70, v71\n\t" \
  "s_waitcnt lgkmcnt(3)\n\t" \
  "v_smfmac_f32_16x16x64_bf16 v[68:71], v[48:51], a[96:103], v104\n\t" \
  "ds_read_b128 v[60:63], v101 offset:9216\n\t" \
  "ds_read_b128 v[64:67], v101 offset:12288\n\t" \
  "v_smfmac_f32_16x16x64_bf16 v[72:75], v[48:51], a[104:111], v104\n\t" \
  "s_or_b64 vcc, vcc, s[82:83]\n\t" \
  "s_waitcnt lgkmcnt(2)\n\t" \
  "v_smfmac_f32_16x16x64_bf16 v[68:71], v[52:55], a[112:119], v104\n\t" \
  "s_cbranch_scc1 9f\n\t" \
  "6:\n\t" \
  "s_barrier\n\t" \
  "v_smfmac_f32_16x16x64_bf16 v[72:75], v[52:55], a[120:127], v104\n\t" \
  "s_waitcnt lgkmcnt(0)\n\t" \
  "ds_read_b128 v[48:51], v101 offset:13312\n\t" \
  "v_smfmac_f32_16x16x64_bf16 v[68:71], v[56:59], a[192:199], v104\n\t" \
  "ds_read_b128 v[52:55], v101 offset:6144\n\t" \
  "s_sub_i32 s79, s56, s84\n\t" \
  "s_mul_hi_u32 s79, s79, s46\n\t" \
  "v_smfmac_f32_16x16x64_bf16 v[72:75], v[56:59], a[200:207], v104\n\t" \
  "v_lshl_add_u32 v116, s79, 9, v118\n\t" \
  "buffer_store_dword v116, v109, %[rh], s45 offen sc1\n\t" \
  "v_smfmac_f32_16x16x64_bf16 v[68:71], v[60:63], a[208:215], v104\n\t" \
  "ds_read_b128 v[56:59], v101 offset:7168\n\t" \
  "v_smfmac_f32_16x16x64_bf16 v[72:75], v[60:63], a[216:223], v104\n\t" \
  "v_smfmac_f32_16x16x64_bf16 v[68:71], v[64:67], v[152:159], v104\n\t" \
  "ds_read_b128 v[60:63], v101 offset:10240\n\t" \
  "v_smfmac_f32_16x16x64_bf16 v[72:75], v[64:67], v[160:167], v104\n\t" \
  "ds_read_b128 v[32:35], v101 offset:20480\n\t" \
  "s_waitcnt lgkmcnt(4)\n\t" \
  "v_smfmac_f32_16x16x64_bf16 v[68:71], v[48:51], v[168:175], v104\n\t" \
  "ds_read_b128 v[64:67], v101 offset:11264\n\t" \
  "ds_read_b128 v[16:19], v103 offset:49152\n\t" \
  "v_smfmac_f32_16x16x64_bf16 v[72:75], v[48:51], v[176:183], v104\n\t" \
  "ds_read_b128 v[36:39], v101 offset:21504\n\t" \
  "s_waitcnt lgkmcnt(6)\n\t" \
  "v_smfmac_f32_16x16x64_bf16 v[68:71], v[52:55], v[84:91], v104\n\t" \
  "ds_read_b128 v[48:51], v101 offset:15360\n\t" \
  "ds_read_b128 v[20:23], v103 offset:50176\n\t" \
  "v_smfmac_f32_16x16x64_bf16 v[72:75], v[52:55], v[92:99], v104\n\t" \
  "ds_read_b128 v[40:43], v101 offset:22528\n\t" \
  "s_waitcnt lgkmcnt(8)\n\t" \
  "v_smfmac_f32_16x16x64_bf16 v[68:71], v[56:59], a[160:167], v104\n\t" \
  "ds_read_b128 v[52:55], v101 offset:14336\n\t" \
  "ds_read_b128 v[24:27], v103 offset:51200\n\t" \
  "v_smfmac_f32_16x16x64_bf16 v[72:75], v[56:59], a[168:175], v104\n\t" \
  "ds_read_b128 v[28:31], v103 offset:52224\n\t" \
  "s_waitcnt lgkmcnt(10)\n\t" \
  "v_smfmac_f32_16x16x64_bf16 v[68:71], v[60:63], v[0:7], v104\n\t" \
  "ds_read_b128 v[44:47], v101 offset:23552\n\t" \
  "v_smfmac_f32_16x16x64_bf16 v[72:75], v[60:63], v[8:15], v104\n\t" \
  "v_mov_b64 v[92:93], 0\n\t" \
  "s_waitcnt lgkmcnt(9)\n\t" \
  "v_smfmac_f32_16x16x64_bf16 v[68:71], v[64:67], v[120:127], v104\n\t" \
  "ds_read_b128 v[0:3], v101 offset:24576\n\t" \
  "v_smfmac_f32_16x16x64_bf16 v[72:75], v[64:67], v[128:135], v104\n\t" \
  "ds_read_b128 v[4:7], v101 offset:25600\n\t" \
  "s_waitcnt lgkmcnt(8)\n\t" \
  "v_smfmac_f32_16x16x64_bf16 v[68:71], v[48:51], v[216:223], v104\n\t" \
  "v_mov_b64 v[94:95], 0\n\t" \
  "s_waitcnt lgkmcnt(5)\n\t" \
  "v_smfmac_f32_16x16x64_bf16 v[68:71], v[52:55], v[16:23], v104\n\t" \
  "ds_read_b128 v[8:11], v101 offset:26624\n\t" \
  "v_smfmac_f32_16x16x64_bf16 v[72:75], v[48:51], v[224:231], v104\n\t" \
  "ds_read_b128 v[12:15], v101 offset:27648\n\t" \
  "s_waitcnt lgkmcnt(5)\n\t" \
  "v_smfmac_f32_16x16x64_bf16 v[72:75], v[52:55], v[24:31], v104\n\t" \
  "v_add_f32 v96, v68, v70\n\t" \
  "v_smfmac_f32_16x16x64_bf16 v[76:79], v[56:59], a[176:183], v104\n\t" \
  "v_add_f32 v97, v69, v71\n\t" \
  "v_cvt_pk_bf16_f32 v96, v96, v97\n\t" \
  "v_smfmac_f32_16x16x64_bf16 v[80:83], v[56:59], a[184:191], v104\n\t" \
  "v_add_f32 v98, v72, v74\n\t" \
  "v_add_f32 v99, v73, v75\n\t" \
  "v_smfmac_f32_16x16x64_bf16 v[76:79], v[60:63], a[240:247], v104\n\t" \
  "v_cvt_pk_bf16_f32 v98, v98, v99\n\t" \
  "ds_write2st64_b32 v105, v96, v98 offset1:1\n\t" \
  "v_smfmac_f32_16x16x64_bf16 v[80:83], v[60:63], a[248:255], v104\n\t" \
  "ds_read_b128 v[56:59], v101 offset:9216\n\t" \
  "v_mov_b64 v[68:69], 0\n\t" \
  "v_smfmac_f32_16x16x64_bf16 v[76:79], v[64:67], v[136:143], v104\n\t" \
  "ds_read_b128 v[60:63], v101 offset:13312\n\t" \
  "v_mov_b64 v[70:71], 0\n\t" \
  "v_smfmac_f32_16x16x64_bf16 v[80:83], v[64:67], v[144:151], v104\n\t" \
  "v_smfmac_f32_16x16x64_bf16 v[76:79], v[48:51], v[232:239], v104\n\t" \
  "s_waitcnt lgkmcnt(2)\n\t" \
  "s_barrier\n\t" \
  "ds_read_b64_tr_b16 v[84:85], v107 offset:0\n\t" \
  "v_smfmac_f32_16x16x64_bf16 v[80:83], v[48:51], v[240:247], v104\n\t" \
  "ds_read_b64_tr_b16 v[86:87], v107 offset:128\n\t" \
  "ds_read_b64_tr_b16 v[88:89], v107 offset:1024\n\t" \
  "v_smfmac_f32_16x16x64_bf16 v[76:79], v[52:55], v[200:207], v104\n\t" \
  "ds_read_b64_tr_b16 v[90:91], v107 offset:1152\n\t" \
  "v_smfmac_f32_16x16x64_bf16 v[80:83], v[52:55], v[208:215], v104\n\t" \
  "ds_read_b128 v[64:67], v101 offset:6144\n\t" \
  "s_waitcnt lgkmcnt(5)\n\t" \
  "v_smfmac_f32_16x16x64_bf16 v[76:79], v[60:63], v[184:191], v104\n\t" \
  "ds_read_b128 v[52:55], v101 offset:5120\n\t" \
  "s_waitcnt lgkmcnt(4)\n\t" \
  "v_smfmac_f32_16x16x64_bf16 v[92:95], v[84:87], v[32:39], v104\n\t" \
  "s_waitcnt lgkmcnt(2)\n\t" \
  "v_smfmac_f32_16x16x64_bf16 v[92:95], v[88:91], v[40:47], v104\n\t" \
  "v_smfmac_f32_16x16x64_bf16 v[68:71], v[84:87], v[0:7], v104\n\t" \
  "v_smfmac_f32_16x16x64_bf16 v[68:71], v[88:91], v[8:15], v104\n\t" \
  "v_add_f32 v92, v92, v94\n\t" \
  "v_add_f32 v94, v93, v95\n\t" \
  "v_smfmac_f32_16x16x64_bf16 v[80:83], v[60:63], v[192:199], v104\n\t" \
  "v_mov_b32 v97, v94\n\t" \
  "v_mov_b32_dpp v94, v92 row_ror:8 row_mask:0xf bank_mask:0x3\n\t" \
  "s_nop 0\n\t" \
  "v_mov_b32_dpp v92, v97 row_ror:8 row_mask:0xf bank_mask:0xc\n\t" \
  "s_waitcnt lgkmcnt(1)\n\t" \
  "v_smfmac_f32_16x16x64_bf16 v[76:79], v[64:67], a[144:151], v104\n\t" \
  "v_add_f32 v93, v68, v70\n\t" \
  "v_add_f32 v95, v69, v71\n\t" \
  "v_max_f32 v98, v92, v94\n\t" \
  "v_mov_b32 v97, v95\n\t" \
  "v_mov_b32_dpp v95, v93 row_ror:8 row_mask:0xf bank_mask:0x3\n\t" \
  "v_smfmac_f32_16x16x64_bf16 v[80:83], v[64:67], a[152:159], v104\n\t" \
  "v_mov_b32_dpp v93, v97 row_ror:8 row_mask:0xf bank_mask:0xc\n\t" \
  "v_max3_f32 v98, v98, v93, v95\n\t" \
  "s_waitcnt lgkmcnt(0)\n\t" \
  "v_smfmac_f32_16x16x64_bf16 v[76:79], v[52:55], a[128:135], v104\n\t" \
  "v_max_f32_dpp v98, v98, v98 quad_perm:[1,0,3,2] row_mask:0xf bank_mask:0xf\n\t" \
  "v_smfmac_f32_16x16x64_bf16 v[76:79], v[56:59], a[224:231], v104\n\t" \
  "s_nop 0\n\t" \
  "v_max_f32_dpp v98, v98, v98 quad_perm:[2,3,0,1] row_mask:0xf bank_mask:0xf\n\t" \
  "v_smfmac_f32_16x16x64_bf16 v[80:83], v[52:55], a[136:143], v104\n\t" \
  "s_nop 0\n\t" \
  "v_max_f32_dpp v98, v98, v98 row_half_mirror row_mask:0xf bank_mask:0xf\n\t" \
  "v_mul_f32 v98, %[cs], v98\n\t" \
  "v_fma_f32 v92, v92, %[cs], -v98\n\t" \
  "v_fma_f32 v93, v93, %[cs], -v98\n\t" \
  "v_fma_f32 v94, v94, %[cs], -v98\n\t" \
  "v_fma_f32 v95, v95, %[cs], -v98\n\t" \
  "v_exp_f32 v72, v92\n\t" \
  "v_exp_f32 v73, v93\n\t" \
  "v_exp_f32 v74, v94\n\t" \
  "v_smfmac_f32_16x16x64_bf16 v[80:83], v[56:59], a[232:239], v104\n\t" \
  "v_exp_f32 v75, v95\n\t" \
  "v_cvt_pk_bf16_f32 v68, v72, v73\n\t" \
  "v_cvt_pk_bf16_f32 v69, v74, v75\n\t" \
  "buffer_store_dwordx2 v[68:69], v110, %[rh], s47 offen sc1\n\t" \
  "v_add_f32 v20, v76, v78\n\t" \
  "v_add_f32 v21, v77, v79\n\t" \
  "v_cvt_pk_bf16_f32 v20, v20, v21\n\t" \
  "ds_write_b32 v106, v20\n\t" \
  "v_add_f32 v23, v80, v82\n\t" \
  "v_add_f32 v21, v81, v83\n\t" \
  "v_cvt_pk_bf16_f32 v21, v23, v21\n\t" \
  "ds_write_b32 v106, v21 offset:256\n\t" \
  "v_mov_b64 v[0:1], 0\n\t" \
  "v_mov_b64 v[2:3], 0\n\t" \
  "v_mov_b64 v[36:37], 0\n\t" \
  "v_mov_b64 v[38:39], 0\n\t" \
  "s_waitcnt lgkmcnt(0)\n\t" \
  "s_barrier\n\t" \
  "ds_read_b64_tr_b16 v[32:33], v108 offset:12288\n\t" \
  "ds_read_b64_tr_b16 v[34:35], v108 offset:12352\n\t" \
  "ds_read_b64_tr_b16 v[4:5], v108\n\t" \
  "ds_read_b64_tr_b16 v[6:7], v108 offset:64\n\t" \
  "ds_read_b64_tr_b16 v[8:9], v108 offset:512\n\t" \
  "ds_read_b64_tr_b16 v[10:11], v108 offset:576\n\t" \
  "ds_read_b64_tr_b16 v[12:13], v108 offset:1024\n\t" \
  "ds_read_b64_tr_b16 v[14:15], v108 offset:1088\n\t" \
  "ds_read_b64_tr_b16 v[16:17], v108 offset:1536\n\t" \
  "ds_read_b64_tr_b16 v[18:19], v108 offset:1600\n\t" \
  "v_add_f32 v29, v72, v73\n\t" \
  "v_add_f32 v29, v29, v74\n\t" \
  "s_waitcnt lgkmcnt(8)\n\t" \
  "v_smfmac_f32_16x16x64_bf16 v[0:3], v[84:87], v[32:39], v104\n\t" \
  "s_waitcnt lgkmcnt(4)\n\t" \
  "v_smfmac_f32_16x16x64_bf16 v[0:3], v[84:87], v[4:11], v104\n\t" \
  "s_waitcnt lgkmcnt(0)\n\t" \
  "v_smfmac_f32_16x16x64_bf16 v[0:3], v[88:91], v[12:19], v104\n\t" \
  "v_add_f32 v29, v29, v75\n\t" \
  "s_mov_b32 s76, 0xaaaaaaaa\n\t" \
  "s_mov_b32 s77, s76\n\t" \
  "s_nop 4\n\t" \
  "v_add_f32 v96, v0, v2\n\t" \
  "v_add_f32_dpp v96, v1, v3 quad_perm:[0,1,2,3] row_mask:0xf bank_mask:0xc\n\t" \
  "v_fma_f32 v96, v96, %[cs], -v98\n\t" \
  "v_exp_f32 v24, v96\n\t" \
  "s_nop 0\n\t" \
  "v_add_f32 v29, v29, v24\n\t" \
  "s_nop 0\n\t" \
  "v_mov_b32_dpp v25, v24 quad_perm:[1,0,3,2] row_mask:0xf bank_mask:0xf\n\t" \
  "v_add_f32_dpp v29, v29, v29 quad_perm:[1,0,3,2] row_mask:0xf bank_mask:0xf\n\t" \
  "v_cndmask_b32_e64 v24, v24, v114, s[76:77]\n\t" \
  "v_cndmask_b32_e64 v25, v25, v115, s[76:77]\n\t" \
  "v_add_f32_dpp v29, v29, v29 quad_perm:[2,3,0,1] row_mask:0xf bank_mask:0xf\n\t" \
  "s_nop 1\n\t" \
  "v_add_f32_dpp v29, v29, v29 row_half_mirror row_mask:0xf bank_mask:0xf\n\t" \
  "v_rcp_f32 v26, v29\n\t" \
  "v_cmpx_gt_f32 vcc, 0x5d5e0b6b, v29\n\t" \
  "v_mul_f32 v24, v24, v26\n\t" \
  "v_mul_f32 v25, v25, v26\n\t" \
  "v_cvt_pk_bf16_f32 v24, v24, v25\n\t" \
  "buffer_store_dword v24, v111, %[rh], s47 offen sc1\n\t" \
  "s_memtime s[76:77]\n\t" \
  "s_mov_b64 exec, -1\n\t" \
  "v_mov_b32 v97, v29\n\t" \
  "s_cmp_eq_u64 vcc, -1\n\t" \
  "s_cselect_b32 s42, 0, 8\n\t" \
  "s_waitcnt lgkmcnt(0)\n\t" \
  "s_mov_b32 s57, s76\n\t" \
  "s_barrier\n\t" \
  "s_add_i32 s41, s56, 1950\n\t" \
  "s_sub_i32 s76, s57, s56\n\t" \
  "s_add_i32 s77, s43, 8\n\t" \
  "s_min_u32 s76, s76, s77\n\t" \
  "s_sub_i32 s43, s43, 4\n\t" \
  "s_max_u32 s43, s43, s76\n\t" \
  "s_add_i32 s76, s45, 0x4000\n\t" \
  "s_and_b32 s76, s76, 0xc000\n\t" \
  "v_mov_b32 v116, -1\n\t" \
  "buffer_store_dword v116, v109, %[rh], s76 offen sc1\n\t" \
  "s_sub_i32 s76, s92, s51\n\t" \
  "s_sub_i32 s77, s93, s53\n\t" \
  "s_cmp_eq_u32 s76, 0\n\t" \
  "s_cbranch_scc1 21f\n\t" \
  "v_cvt_f32_u32 v117, s76\n\t" \
  "v_cvt_f32_u32 v116, s77\n\t" \
  "v_rcp_f32 v117, v117\n\t" \
  "s_mov_b32 s51, s92\n\t" \
  "v_mul_f32 v117, v117, v116\n\t" \
  "v_max_f32 v117, 0x3f028f5c, v117\n\t" \
  "v_min_f32 v117, 0x3ffeb852, v117\n\t" \
  "v_rcp_f32 v116, v117\n\t" \
  "v_mul_f32 v117, 0x4f000000, v117\n\t" \
  "v_mul_f32 v116, 0x47800000, v116\n\t" \
  "v_cvt_u32_f32 v117, v117\n\t" \
  "v_cvt_u32_f32 v116, v116\n\t" \
  "s_mov_b32 s53, s93\n\t" \
  "v_readfirstlane_b32 s46, v117\n\t" \
  "v_readfirstlane_b32 s52, v116\n\t" \
  "21:\n\t" \
  "s_add_i32 s44, s44, 1\n\t" \
  "s_and_b32 s58, s58, 4\n\t" \
  "s_and_b32 s76, s44, 64\n\t" \
  "s_or_b32 s42, s42, s76\n\t" \
  "s_cbranch_scc0 20b\n\t" \
  "s_branch 99f\n\t" \
  "14:\n\t" \
  "s_sleep 3\n\t" \
  "s_add_u32 s59, s59, 0x10000\n\t" \
  "s_cmp_lt_u32 s59, 0x100000\n\t" \
  "s_cbranch_scc1 13b\n\t" \
  "s_mov_b64 exec, -1\n\t" \
  "s_mov_b32 s42, 16\n\t" \
  "s_branch 99f\n\t" \
  "8:\n\t" \
  "s_bitcmp1_b32 s58, 1\n\t" \
  "s_cbranch_scc1 4b\n\t" \
  "s_bitcmp1_b32 s58, 2\n\t" \
  "s_cbranch_scc1 5b\n\t" \
  "v_max3_u32 v116, v32, v33, v34\n\t" \
  "v_max3_u32 v117, v35, v36, v37\n\t" \
  "v_max3_u32 v116, v116, v38, v39\n\t" \
  "v_max_u32 v116, v116, v117\n\t" \
  "v_cmp_eq_u32 vcc, -1, v116\n\t" \
  "s_cbranch_vccz 5b\n\t" \
  "s_branch 0f\n\t" \
  "9:\n\t" \
  "s_bitcmp1_b32 s58, 2\n\t" \
  "s_cbranch_scc1 6b\n\t" \
  "v_max3_u32 v116, v40, v41, v42\n\t" \
  "v_max3_u32 v117, v43, v44, v45\n\t" \
  "v_max3_u32 v116, v116, v46, v47\n\t" \
  "v_max_u32 v116, v116, v117\n\t" \
  "v_cmp_eq_u32 vcc, -1, v116\n\t" \
  "s_cbranch_vccz 6b\n\t" \
  "s_or_b32 s58, s58, 2\n\t" \
  "0:\n\t" \
  "s_or_b32 s58, s58, 1\n\t" \
  "11:\n\t" \
  "buffer_load_dwordx4 v[32:35], v100, %[rh], s40 offen sc1\n\t" \
  "buffer_load_dwordx4 v[36:39], v100, %[rh], s76 offen sc1\n\t" \
  "buffer_load_dwordx4 v[40:43], v100, %[rh], s77 offen sc1\n\t" \
  "buffer_load_dwordx4 v[44:47], v100, %[rh], s78 offen sc1\n\t" \
  "s_waitcnt lgkmcnt(0)\n\t" \
  "s_branch 10b\n\t" \
  "17:\n\t" \
  "s_add_u32 s59, s59, 1\n\t" \
  "s_bitcmp1_b32 s58, 2\n\t" \
  "s_cbranch_scc1 3b\n\t" \
  "s_waitcnt vmcnt(0)\n\t" \
  "v_max_u32 v116, v32, v33\n\t" \
  "v_max3_u32 v116, v116, v34, v35\n\t" \
  "v_max3_u32 v116, v116, v36, v37\n\t" \
  "v_max3_u32 v116, v116, v38, v39\n\t" \
  "v_max3_u32 v116, v116, v40, v41\n\t" \
  "v_max3_u32 v116, v116, v42, v43\n\t" \
  "v_max3_u32 v116, v116, v44, v45\n\t" \
  "v_max3_u32 v116, v116, v46, v47\n\t" \
  "v_cmp_eq_u32 vcc, -1, v116\n\t" \
  "s_cbranch_vccz 3b\n\t" \
  "s_bitcmp1_b32 s59, 6\n\t" \
  "s_cbranch_scc1 12f\n\t" \
  "buffer_load_dwordx4 v[32:35], v100, %[rh], s40 offen sc1\n\t" \
  "buffer_load_dwordx4 v[36:39], v100, %[rh], s76 offen sc1\n\t" \
  "buffer_load_dwordx4 v[40:43], v100, %[rh], s77 offen sc1\n\t" \
  "buffer_load_dwordx4 v[44:47], v100, %[rh], s78 offen sc1\n\t" \
  "s_branch 17b\n\t" \
  "12:\n\t" \
  "s_mov_b32 s42, s58\n\t" \
  "s_branch 99f\n\t" \
  "91:\n\t" \
  "s_mov_b32 s58, s42\n\t" \
  "s_mov_b32 s95, 0\n\t" \
  "20:\n\t" \
  "s_and_b32 s76, s44, 7\n\t" \
  "s_lshl_b32 s76, s76, 15\n\t" \
  "s_add_i32 s40, s54, s76\n\t" \
  "s_and_b32 s76, s44, 3\n\t" \
  "s_lshl_b32 s45, s76, 14\n\t" \
  "s_add_i32 s47, s44, 1\n\t" \
  "s_min_u32 s47, s47, 63\n\t" \
  "s_lshr_b32 s76, s47, 1\n\t" \
  "s_lshl_b32 s47, s47, 20\n\t" \
  "s_add_i32 s47, s47, s55\n\t" \
  "s_bitcmp1_b64 %[sel], s76\n\t" \
  "s_cselect_b32 s76, 0x2000, 0\n\t" \
  "s_add_i32 s47, s47, s76\n\t" \
  "s_lshr_b32 s76, s44, 1\n\t" \
  "s_lshl_b32 s94, s44, 20\n\t" \
  "s_bitcmp1_b64 %[sel], s76\n\t" \
  "s_cselect_b32 s76, 0x2000, 0\n\t" \
  "s_add_i32 s94, s94, s76\n\t" \
  "s_add_i32 s49, s44, -1\n\t" \
  "s_and_b32 s48, s49, 2\n\t" \
  "s_lshl_b32 s48, s48, 13\n\t" \
  "s_and_b32 s76, s49, 1\n\t" \
  "s_lshl_b32 s76, s76, 12\n\t" \
  "s_add_i32 s48, s48, s76\n\t" \
  "s_lshl_b32 s50, s53, 8\n\t" \
  "s_mov_b32 s59, 0\n\t" \
  "s_mov_b32 s92, s51\n\t" \
  "s_mov_b32 s93, s53\n\t" \
  "s_xor_b32 s76, s40, 0x400\n\t" \
  "s_xor_b32 s77, s40, 0x800\n\t" \
  "s_xor_b32 s78, s40, 0xc00\n\t" \
  "s_cmp_lg_u32 s58, 0\n\t" \
  "s_cbranch_scc1 11f\n\t" \
  "s_cmp_lg_u32 s95, 0\n\t" \
  "s_cbranch_scc1 46f\n\t" \
  "10:\n\t" \
  "ds_read_b128 v[48:51], v102 offset:32768\n\t" \
  "ds_read_b128 v[52:55], v102 offset:33792\n\t" \
  "ds_read_b128 v[56:59], v102 offset:34816\n\t" \
  "ds_read_b128 v[60:63], v102 offset:35840\n\t" \
  "ds_read_b128 v[16:19], v102 offset:49152\n\t" \
  "ds_read_b128 v[20:23], v102 offset:50176\n\t" \
  "ds_read_b128 v[24:27], v102 offset:51200\n\t" \
  "ds_read_b128 v[28:31], v102 offset:52224\n\t" \
  "ds_read_b128 v[84:87], v103 offset:16384\n\t" \
  "ds_read_b128 v[88:91], v103 offset:17408\n\t" \
  "ds_read_b128 v[92:95], v103 offset:18432\n\t" \
  "ds_read_b128 v[96:99], v103 offset:19456\n\t" \
  "s_waitcnt lgkmcnt(4)\n\t" \
  "ds_read_b128 v[0:3], v103 offset:49152\n\t" \
  "ds_read_b128 v[4:7], v103 offset:50176\n\t" \
  "ds_read_b128 v[8:11], v103 offset:51200\n\t" \
  "ds_read_b128 v[12:15], v103 offset:52224\n\t" \
  "46:\n\t" \
  "s_bitcmp1_b32 s58, 1\n\t" \
  "s_cselect_b64 s[80:81], -1, 0\n\t" \
  "s_cmp_lg_u32 s58, 0\n\t" \
  "s_cbranch_scc1 17f\n\t" \
  "s_mov_b32 exec_hi, 0\n\t" \
  "s_cmp_eq_u32 s95, 0\n\t" \
  "s_cbranch_scc1 16f\n\t" \
  "s_waitcnt vmcnt(2)\n\t" \
  "v_mov_b32 v116, v118\n\t" \
  "s_branch 15f\n\t" \
  "16:\n\t" \
  "s_memtime vcc\n\t" \
  "s_waitcnt lgkmcnt(0)\n\t" \
  "s_sub_i32 s79, s41, vcc_lo\n\t" \
  "s_cmp_gt_i32 s79, 64\n\t" \
  "s_cbranch_scc1 16b\n\t" \
  "13:\n\t" \
  "buffer_load_dword v116, v112, %[rh], s48 offen sc1\n\t" \
  "s_waitcnt vmcnt(0)\n\t" \
  "15:\n\t" \
  "v_mov_b32 v117, 20000\n\t" \
  "v_cmp_ne_u32_sdwa vcc, v116, s49 src0_sel:BYTE_0 src1_sel:DWORD\n\t" \
  "v_subrev_u32 v116, s50, v116\n\t" \
  "v_ashrrev_i32 v116, 8, v116\n\t" \
  "s_nop 1\n\t" \
  "v_max_i32_dpp v116, v116, v116 quad_perm:[1,0,3,2] row_mask:0xf bank_mask:0xf\n\t" \
  "s_cbranch_vccnz 14f\n\t" \
  "s_nop 0\n\t" \
  "v_max_i32_dpp v116, v116, v116 quad_perm:[2,3,0,1] row_mask:0xf bank_mask:0xf\n\t" \
  "s_nop 1\n\t" \
  "v_max_i32_dpp v116, v116, v116 row_half_mirror row_mask:0xf bank_mask:0xf\n\t" \
  "s_nop 1\n\t" \
  "v_max_i32_dpp v116, v116, v116 row_mirror row_mask:0xf bank_mask:0xf\n\t" \
  "v_med3_i32 v116, v116, 0, v117\n\t" \
  "v_mul_u32_u24 v116, s52, v116\n\t" \
  "v_lshrrev_b32 v116, 16, v116\n\t" \
  "s_mov_b64 exec, -1\n\t" \
  "v_readfirstlane_b32 s84, v116\n\t" \
  "v_readlane_b32 s85, v116, 16\n\t" \
  "s_max_u32 s84, s84, s85\n\t" \
  "s_add_i32 s84, s84, s51\n\t" \
  "1:\n\t" \
  "s_memtime vcc\n\t" \
  "s_waitcnt lgkmcnt(0)\n\t" \
  "s_sub_i32 s79, s84, vcc_lo\n\t" \
  "s_cmp_gt_i32 s79, 128\n\t" \
  "s_cbranch_scc1 1b\n\t" \
  ".p2align 4\n\t" \
  "2:\n\t" \
  "s_sub_i32 s79, s79, 33\n\t" \
  "s_cmp_gt_i32 s79, 56\n\t" \
  "s_cbranch_scc1 2b\n\t" \
  "buffer_load_dwordx4 v[32:35], v100, %[rh], s40 offen sc1\n\t" \
  "s_nop 12\n\t" \
  "buffer_load_dwordx4 v[36:39], v100, %[rh], s76 offen sc1\n\t" \
  "s_nop 12\n\t" \
  "buffer_load_dwordx4 v[40:43], v100, %[rh], s77 offen sc1\n\t" \
  "s_nop 12\n\t" \
  "buffer_load_dwordx4 v[44:47], v100, %[rh], s78 offen sc1\n\t" \
  "s_memtime vcc\n\t" \
  "s_memrealtime s[82:83]\n\t" \
  "s_nop 4\n\t" \
  "s_memrealtime s[84:85]\n\t" \
  "s_nop 4\n\t" \
  "s_memrealtime s[56:57]\n\t" \
  "s_nop 4\n\t" \
  "s_memrealtime s[86:87]\n\t" \
  "s_nop 4\n\t" \
  "s_memrealtime s[88:89]\n\t" \
  "s_nop 4\n\t" \
  "s_memrealtime s[90:91]\n\t" \
  "s_mov_b32 s92, 0\n\t" \
  "s_waitcnt lgkmcnt(0)\n\t" \
  "s_cmp_eq_u32 s82, s84\n\t" \
  "s_addc_u32 s92, s92, 0\n\t" \
  "s_cmp_eq_u32 s82, s56\n\t" \
  "s_addc_u32 s92, s92, 0\n\t" \
  "s_cmp_eq_u32 s82, s86\n\t" \
  "s_addc_u32 s92, s92, 0\n\t" \
  "s_cmp_eq_u32 s82, s88\n\t" \
  "s_addc_u32 s92, s92, 0\n\t" \
  "s_cmp_eq_u32 s82, s90\n\t" \
  "s_addc_u32 s92, s92, 0\n\t" \
  "s_mul_i32 s92, s92, 24\n\t" \
  "s_add_i32 s92, s92, vcc_lo\n\t" \
  "s_lshl4_add_u32 s93, s82, 64\n\t" \
  "3:\n\t" \
  "v_mov_b64 v[68:69], 0\n\t" \
  "v_mov_b64 v[70:71], 0\n\t" \
  "v_mov_b64 v[72:73], 0\n\t" \
  "v_mov_b64 v[74:75], 0\n\t" \
  "v_mov_b64 v[76:77], 0\n\t" \
  "v_mov_b64 v[78:79], 0\n\t" \
  "v_mov_b64 v[80:81], 0\n\t" \
  "v_mov_b64 v[82:83], 0\n\t" \
  "s_waitcnt lgkmcnt(0)\n\t" \
  "s_waitcnt vmcnt(3)\n\t" \
  "v_smfmac_f32_16x16x64_bf16 v[68:71], v[32:35], a[96:103], v104\n\t" \
  "ds_write_b128 v101, v[32:35] offset:4096\n\t" \
  "v_smfmac_f32_16x16x64_bf16 v[72:75], v[32:35], a[104:111], v104\n\t" \
  "v_smfmac_f32_16x16x64_bf16 v[76:79], v[32:35], v[48:55], v104\n\t" \
  "v_smfmac_f32_16x16x64_bf16 v[80:83], v[32:35], v[56:63], v104\n\t" \
  "s_waitcnt vmcnt(2)\n\t" \
  "v_smfmac_f32_16x16x64_bf16 v[68:71], v[36:39], a[112:119], v104\n\t" \
  "ds_write_b128 v101, v[36:39] offset:5120\n\t" \
  "v_smfmac_f32_16x16x64_bf16 v[72:75], v[36:39], a[120:127], v104\n\t" \
  "v_smfmac_f32_16x16x64_bf16 v[76:79], v[36:39], a[128:135], v104\n\t" \
  "v_cmp_u_f32 vcc, v68, v69\n\t" \
  "v_smfmac_f32_16x16x64_bf16 v[80:83], v[36:39], a[136:143], v104\n\t" \
  "v_cmp_u_f32 s[82:83], v70, v71\n\t" \
  "s_waitcnt vmcnt(1)\n\t" \
  "v_smfmac_f32_16x16x64_bf16 v[68:71], v[40:43], v[16:23], v104\n\t" \
  "ds_write_b128 v101, v[40:43] offset:6144\n\t" \
  "v_smfmac_f32_16x16x64_bf16 v[72:75], v[40:43], v[24:31], v104\n\t" \
  "s_or_b64 vcc, vcc, s[82:83]\n\t" \
  "s_or_b64 vcc, vcc, s[80:81]\n\t" \
  "s_waitcnt lgkmcnt(1)\n\t" \
  "v_smfmac_f32_16x16x64_bf16 v[76:79], v[40:43], a[144:151], v104\n\t" \
  "s_cbranch_scc1 8f\n\t" \
  "5:\n\t" \
  "s_barrier\n\t" \
  "4:\n\t" \
  "s_memtime s[56:57]\n\t" \
  "ds_read_b128 v[48:51], v101 offset:8192\n\t" \
  "v_smfmac_f32_16x16x64_bf16 v[80:83], v[40:43], a[152:159], v104\n\t" \
  "ds_read_b128 v[52:55], v101 offset:9216\n\t" \
  "ds_read_b128 v[56:59], v101 offset:12288\n\t" \
  "s_waitcnt vmcnt(0)\n\t" \
  "v_smfmac_f32_16x16x64_bf16 v[68:71], v[44:47], a[160:167], v104\n\t" \
  "ds_write_b128 v101, v[44:47] offset:7168\n\t" \
  "v_smfmac_f32_16x16x64_bf16 v[72:75], v[44:47], a[168:175], v104\n\t" \
  "v_smfmac_f32_16x16x64_bf16 v[76:79], v[44:47], a[176:183], v104\n\t" \
  "v_cmp_u_f32 vcc, v68, v69\n\t" \
  "v_smfmac_f32_16x16x64_bf16 v[80:83], v[44:47], a[184:191], v104\n\t" \
  "v_cmp_u_f32 s[82:83], v70, v71\n\t" \
  "s_waitcnt lgkmcnt(3)\n\t" \
  "v_smfmac_f32_16x16x64_bf16 v[68:71], v[48:51], a[192:199], v104\n\t" \
  "ds_read_b128 v[60:63], v101 offset:13312\n\t" \
  "ds_read_b128 v[64:67], v101 offset:0\n\t" \
  "v_smfmac_f32_16x16x64_bf16 v[72:75], v[48:51], a[200:207], v104\n\t" \
  "s_or_b64 vcc, vcc, s[82:83]\n\t" \
  "s_waitcnt lgkmcnt(2)\n\t" \
  "v_smfmac_f32_16x16x64_bf16 v[68:71], v[52:55], a[208:215], v104\n\t" \
  "s_cbranch_scc1 9f\n\t" \
  "6:\n\t" \
  "s_barrier\n\t" \
  "v_smfmac_f32_16x16x64_bf16 v[72:75], v[52:55], a[216:223], v104\n\t" \
  "ds_read_b128 v[48:51], v101 offset:1024\n\t" \
  "v_smfmac_f32_16x16x64_bf16 v[68:71], v[56:59], v[152:159], v104\n\t" \
  "ds_read_b128 v[52:55], v101 offset:10240\n\t" \
  "v_smfmac_f32_16x16x64_bf16 v[72:75], v[56:59], v[160:167], v104\n\t" \
  "s_waitcnt lgkmcnt(3)\n\t" \
  "v_smfmac_f32_16x16x64_bf16 v[68:71], v[60:63], v[168:175], v104\n\t" \
  "ds_read_b128 v[56:59], v101 offset:11264\n\t" \
  "v_smfmac_f32_16x16x64_bf16 v[72:75], v[60:63], v[176:183], v104\n\t" \
  "s_waitcnt lgkmcnt(3)\n\t" \
  "v_smfmac_f32_16x16x64_bf16 v[68:71], v[64:67], a[0:7], v104\n\t" \
  "ds_read_b128 v[60:63], v101 offset:14336\n\t" \
  "v_smfmac_f32_16x16x64_bf16 v[72:75], v[64:67], a[8:15], v104\n\t" \
  "s_waitcnt lgkmcnt(3)\n\t" \
  "v_smfmac_f32_16x16x64_bf16 v[68:71], v[48:51], a[16:23], v104\n\t" \
  "ds_read_b128 v[64:67], v101 offset:15360\n\t" \
  "ds_read_b128 v[16:19], v102 offset:16384\n\t" \
  "v_smfmac_f32_16x16x64_bf16 v[72:75], v[48:51], a[24:31], v104\n\t" \
  "s_waitcnt lgkmcnt(4)\n\t" \
  "v_smfmac_f32_16x16x64_bf16 v[68:71], v[52:55], v[84:91], v104\n\t" \
  "ds_read_b128 v[48:51], v101 offset:3072\n\t" \
  "ds_read_b128 v[20:23], v102 offset:17408\n\t" \
  "v_smfmac_f32_16x16x64_bf16 v[72:75], v[52:55], v[92:99], v104\n\t" \
  "s_waitcnt lgkmcnt(5)\n\t" \
  "v_smfmac_f32_16x16x64_bf16 v[68:71], v[56:59], v[120:127], v104\n\t" \
  "ds_read_b128 v[52:55], v101 offset:2048\n\t" \
  "ds_read_b128 v[24:27], v102 offset:18432\n\t" \
  "v_smfmac_f32_16x16x64_bf16 v[72:75], v[56:59], v[128:135], v104\n\t" \
  "ds_read_b128 v[28:31], v102 offset:19456\n\t" \
  "s_waitcnt lgkmcnt(7)\n\t" \
  "v_smfmac_f32_16x16x64_bf16 v[68:71], v[60:63], v[0:7], v104\n\t" \
  "ds_read_b128 v[84:87], v102 offset:28672\n\t" \
  "v_smfmac_f32_16x16x64_bf16 v[72:75], v[60:63], v[8:15], v104\n\t" \
  "ds_read_b128 v[88:91], v102 offset:29696\n\t" \
  "s_waitcnt lgkmcnt(8)\n\t" \
  "v_smfmac_f32_16x16x64_bf16 v[68:71], v[64:67], v[216:223], v104\n\t" \
  "ds_read_b128 v[0:3], v103 offset:0\n\t" \
  "v_smfmac_f32_16x16x64_bf16 v[72:75], v[64:67], v[224:231], v104\n\t" \
  "ds_read_b128 v[4:7], v103 offset:1024\n\t" \
  "s_waitcnt lgkmcnt(8)\n\t" \
  "v_smfmac_f32_16x16x64_bf16 v[68:71], v[48:51], a[64:71], v104\n\t" \
  "ds_read_b128 v[92:95], v102 offset:61440\n\t" \
  "s_waitcnt lgkmcnt(7)\n\t" \
  "v_smfmac_f32_16x16x64_bf16 v[68:71], v[52:55], v[16:23], v104\n\t" \
  "ds_read_b128 v[8:11], v103 offset:2048\n\t" \
  "v_smfmac_f32_16x16x64_bf16 v[72:75], v[48:51], a[72:79], v104\n\t" \
  "ds_read_b128 v[12:15], v103 offset:3072\n\t" \
  "s_waitcnt lgkmcnt(7)\n\t" \
  "v_smfmac_f32_16x16x64_bf16 v[72:75], v[52:55], v[24:31], v104\n\t" \
  "v_add_f32 v44, v68, v70\n\t" \
  "ds_read_b128 v[96:99], v102 offset:62464\n\t" \
  "v_smfmac_f32_16x16x64_bf16 v[76:79], v[56:59], v[136:143], v104\n\t" \
  "v_add_f32 v45, v69, v71\n\t" \
  "v_cvt_pk_bf16_f32 v44, v44, v45\n\t" \
  "v_smfmac_f32_16x16x64_bf16 v[80:83], v[56:59], v[144:151], v104\n\t" \
  "v_add_f32 v46, v72, v74\n\t" \
  "v_add_f32 v47, v73, v75\n\t" \
  "v_smfmac_f32_16x16x64_bf16 v[76:79], v[60:63], v[200:207], v104\n\t" \
  "v_cvt_pk_bf16_f32 v46, v46, v47\n\t" \
  "ds_write2st64_b32 v105, v44, v46 offset1:1\n\t" \
  "v_smfmac_f32_16x16x64_bf16 v[80:83], v[60:63], v[208:215], v104\n\t" \
  "ds_read_b128 v[56:59], v101 offset:8192\n\t" \
  "v_mov_b64 v[68:69], 0\n\t" \
  "v_smfmac_f32_16x16x64_bf16 v[76:79], v[64:67], v[232:239], v104\n\t" \
  "ds_read_b128 v[16:19], v103 offset:32768\n\t" \
  "v_mov_b64 v[70:71], 0\n\t" \
  "v_smfmac_f32_16x16x64_bf16 v[80:83], v[64:67], v[240:247], v104\n\t" \
  "ds_read_b128 v[20:23], v103 offset:33792\n\t" \
  "v_smfmac_f32_16x16x64_bf16 v[76:79], v[48:51], a[80:87], v104\n\t" \
  "s_waitcnt lgkmcnt(3)\n\t" \
  "s_barrier\n\t" \
  "v_smfmac_f32_16x16x64_bf16 v[80:83], v[48:51], a[88:95], v104\n\t" \
  "v_smfmac_f32_16x16x64_bf16 v[68:71], v[32:35], v[84:91], v104\n\t" \
  "global_load_dword v119, v109, %[kc] sc1\n\t" \
  "v_smfmac_f32_16x16x64_bf16 v[76:79], v[52:55], a[48:55], v104\n\t" \
  "ds_read_b128 v[60:63], v101 offset:1024\n\t" \
  "ds_read_b128 v[24:27], v103 offset:34816\n\t" \
  "v_smfmac_f32_16x16x64_bf16 v[80:83], v[52:55], a[56:63], v104\n\t" \
  "ds_read_b128 v[64:67], v101 offset:10240\n\t" \
  "s_waitcnt lgkmcnt(5)\n\t" \
  "v_smfmac_f32_16x16x64_bf16 v[76:79], v[56:59], v[0:7], v104\n\t" \
  "ds_read_b128 v[48:51], v101 offset:12288\n\t" \
  "ds_read_b128 v[28:31], v103 offset:35840\n\t" \
  "v_smfmac_f32_16x16x64_bf16 v[80:83], v[56:59], v[8:15], v104\n\t" \
  "buffer_load_dwordx4 v[32:35], v100, %[ru], s47 offen offset:0 sc1\n\t" \
  "v_smfmac_f32_16x16x64_bf16 v[68:71], v[56:59], v[92:99], v104\n\t" \
  "ds_read_b128 v[84:87], v103 offset:28672\n\t" \
  "s_waitcnt lgkmcnt(5)\n\t" \
  "v_smfmac_f32_16x16x64_bf16 v[76:79], v[60:63], a[32:39], v104\n\t" \
  "ds_read_b128 v[52:55], v101 offset:9216\n\t" \
  "ds_read_b128 v[88:91], v103 offset:29696\n\t" \
  "v_smfmac_f32_16x16x64_bf16 v[80:83], v[60:63], a[40:47], v104\n\t" \
  "ds_read_b128 v[0:3], v102 offset:0\n\t" \
  "s_waitcnt lgkmcnt(6)\n\t" \
  "v_smfmac_f32_16x16x64_bf16 v[76:79], v[64:67], a[240:247], v104\n\t" \
  "ds_read_b128 v[56:59], v101 offset:13312\n\t" \
  "buffer_load_dwordx4 v[36:39], v100, %[ru], s47 offen offset:1024 sc1\n\t" \
  "v_smfmac_f32_16x16x64_bf16 v[80:83], v[64:67], a[248:255], v104\n\t" \
  "ds_read_b128 v[60:63], v101 offset:0\n\t" \
  "ds_read_b128 v[4:7], v102 offset:1024\n\t" \
  "s_waitcnt lgkmcnt(8)\n\t" \
  "v_smfmac_f32_16x16x64_bf16 v[76:79], v[48:51], v[16:23], v104\n\t" \
  "ds_read_b128 v[8:11], v102 offset:2048\n\t" \
  "s_waitcnt lgkmcnt(8)\n\t" \
  "v_smfmac_f32_16x16x64_bf16 v[80:83], v[48:51], v[24:31], v104\n\t" \
  "ds_read_b128 v[12:15], v102 offset:3072\n\t" \
  "buffer_load_dwordx4 v[40:43], v100, %[ru], s47 offen offset:2048 sc1\n\t" \
  "s_waitcnt lgkmcnt(6)\n\t" \
  "v_smfmac_f32_16x16x64_bf16 v[68:71], v[48:51], v[84:91], v104\n\t" \
  "v_smfmac_f32_16x16x64_bf16 v[76:79], v[52:55], a[224:231], v104\n\t" \
  "v_smfmac_f32_16x16x64_bf16 v[80:83], v[52:55], a[232:239], v104\n\t" \
  "v_add_f32 v72, v68, v70\n\t" \
  "s_waitcnt lgkmcnt(4)\n\t" \
  "v_smfmac_f32_16x16x64_bf16 v[76:79], v[56:59], v[184:191], v104\n\t" \
  "v_add_f32 v73, v69, v71\n\t" \
  "s_waitcnt lgkmcnt(2)\n\t" \
  "v_smfmac_f32_16x16x64_bf16 v[76:79], v[60:63], v[0:7], v104\n\t" \
  "v_cvt_pk_bf16_f32 v72, v72, v73\n\t" \
  "v_smfmac_f32_16x16x64_bf16 v[80:83], v[56:59], v[192:199], v104\n\t" \
  "ds_write_b32 v106, v72 offset:11776\n\t" \
  "s_waitcnt lgkmcnt(1)\n\t" \
  "v_smfmac_f32_16x16x64_bf16 v[80:83], v[60:63], v[8:15], v104\n\t" \
  "v_add_f32 v20, v76, v78\n\t" \
  "v_add_f32 v21, v77, v79\n\t" \
  "v_cvt_pk_bf16_f32 v20, v20, v21\n\t" \
  "ds_write_b32 v106, v20\n\t" \
  "s_nop 3\n\t" \
  "v_add_f32 v23, v80, v82\n\t" \
  "v_add_f32 v21, v81, v83\n\t" \
  "v_cvt_pk_bf16_f32 v21, v23, v21\n\t" \
  "ds_write_b32 v106, v21 offset:256\n\t" \
  "s_waitcnt lgkmcnt(0)\n\t" \
  "s_barrier\n\t" \
  "buffer_load_dwordx4 v[84:87], v107, %[ru], s94 offen offset:0 sc1\n\t" \
  "buffer_load_dwordx4 v[88:91], v107, %[ru], s94 offen offset:1024 sc1\n\t" \
  "buffer_load_dwordx4 v[92:95], v107, %[ru], s94 offen offset:2048 sc1\n\t" \
  "buffer_load_dwordx4 v[96:99], v107, %[ru], s94 offen offset:3072 sc1\n\t" \
  "v_bfe_i32 v64, v114, 0, 1\n\t" \
  "v_and_b32 v64, 0xff800000, v64\n\t" \
  "v_bfe_i32 v65, v114, 1, 1\n\t" \
  "v_and_b32 v65, 0xff800000, v65\n\t" \
  "v_bfe_i32 v66, v114, 2, 1\n\t" \
  "v_and_b32 v66, 0xff800000, v66\n\t" \
  "v_bfe_i32 v67, v114, 3, 1\n\t" \
  "v_and_b32 v67, 0xff800000, v67\n\t" \
  "v_bfe_i32 v68, v114, 4, 1\n\t" \
  "v_and_b32 v68, 0xff800000, v68\n\t" \
  "v_bfe_i32 v69, v114, 5, 1\n\t" \
  "v_and_b32 v69, 0xff800000, v69\n\t" \
  "v_bfe_i32 v70, v114, 6, 1\n\t" \
  "v_and_b32 v70, 0xff800000, v70\n\t" \
  "v_bfe_i32 v71, v114, 7, 1\n\t" \
  "v_and_b32 v71, 0xff800000, v71\n\t" \
  "s_lshl_b32 s94, s45, 4\n\t" \
  "s_and_b32 s96, s44, 2\n\t" \
  "s_lshl_b32 s96, s96, 13\n\t" \
  "s_and_b32 s76, s44, 1\n\t" \
  "s_lshl_b32 s76, s76, 12\n\t" \
  "s_add_i32 s96, s96, s76\n\t" \
  "s_mov_b32 s95, 1\n\t" \
  "s_add_i32 s41, s56, 1950\n\t" \
  "s_sub_i32 s76, s92, s51\n\t" \
  "s_sub_i32 s77, s93, s53\n\t" \
  "s_cmp_eq_u32 s76, 0\n\t" \
  "s_cbranch_scc1 21f\n\t" \
  "v_cvt_f32_u32 v117, s76\n\t" \
  "v_cvt_f32_u32 v116, s77\n\t" \
  "v_rcp_f32 v117, v117\n\t" \
  "s_mov_b32 s51, s92\n\t" \
  "v_mul_f32 v117, v117, v116\n\t" \
  "v_max_f32 v117, 0x3f028f5c, v117\n\t" \
  "v_min_f32 v117, 0x3ffeb852, v117\n\t" \
  "v_rcp_f32 v116, v117\n\t" \
  "v_mul_f32 v117, 0x4f000000, v117\n\t" \
  "v_mul_f32 v116, 0x47800000, v116\n\t" \
  "v_cvt_u32_f32 v117, v117\n\t" \
  "v_cvt_u32_f32 v116, v116\n\t" \
  "s_mov_b32 s53, s93\n\t" \
  "v_readfirstlane_b32 s46, v117\n\t" \
  "v_readfirstlane_b32 s52, v116\n\t" \
  "21:\n\t" \
  "ds_read_b128 v[0:3], v101 offset:20480\n\t" \
  "ds_read_b128 v[4:7], v101 offset:21504\n\t" \
  "ds_read_b128 v[8:11], v101 offset:22528\n\t" \
  "ds_read_b128 v[12:15], v101 offset:23552\n\t" \
  "ds_read_b128 v[16:19], v101 offset:24576\n\t" \
  "ds_read_b128 v[20:23], v101 offset:25600\n\t" \
  "ds_read_b128 v[24:27], v101 offset:26624\n\t" \
  "ds_read_b128 v[28:31], v101 offset:27648\n\t" \
  "ds_read_b64_tr_b16 v[48:49], v108 offset:0\n\t" \
  "ds_read_b64_tr_b16 v[50:51], v108 offset:64\n\t" \
  "ds_read_b64_tr_b16 v[52:53], v108 offset:512\n\t" \
  "ds_read_b64_tr_b16 v[54:55], v108 offset:576\n\t" \
  "ds_read_b64_tr_b16 v[56:57], v108 offset:1024\n\t" \
  "ds_read_b64_tr_b16 v[58:59], v108 offset:1088\n\t" \
  "ds_read_b64_tr_b16 v[60:61], v108 offset:1536\n\t" \
  "ds_read_b64_tr_b16 v[62:63], v108 offset:1600\n\t" \
  "ds_read_b64_tr_b16 v[76:77], v108 offset:12288\n\t" \
  "ds_read_b64_tr_b16 v[78:79], v108 offset:12352\n\t" \
  "s_waitcnt vmcnt(0) lgkmcnt(10)\n\t" \
  "v_mfma_f32_16x16x32_bf16 v[64:67], v[0:3], v[84:87], v[64:67]\n\t" \
  "v_mfma_f32_16x16x32_bf16 v[68:71], v[16:19], v[84:87], v[68:71]\n\t" \
  "v_mfma_f32_16x16x32_bf16 v[64:67], v[4:7], v[88:91], v[64:67]\n\t" \
  "v_mfma_f32_16x16x32_bf16 v[68:71], v[20:23], v[88:91], v[68:71]\n\t" \
  "v_mfma_f32_16x16x32_bf16 v[64:67], v[8:11], v[92:95], v[64:67]\n\t" \
  "v_mfma_f32_16x16x32_bf16 v[68:71], v[24:27], v[92:95], v[68:71]\n\t" \
  "v_mfma_f32_16x16x32_bf16 v[64:67], v[12:15], v[96:99], v[64:67]\n\t" \
  "v_mfma_f32_16x16x32_bf16 v[68:71], v[28:31], v[96:99], v[68:71]\n\t" \
  "s_waitcnt lgkmcnt(0)\n\t" \
  "v_mfma_f32_16x16x32_bf16 v[72:75], v[48:51], v[84:87], 0\n\t" \
  "v_mfma_f32_16x16x32_bf16 v[72:75], v[52:55], v[88:91], v[72:75]\n\t" \
  "v_mfma_f32_16x16x32_bf16 v[72:75], v[56:59], v[92:95], v[72:75]\n\t" \
  "v_mfma_f32_16x16x32_bf16 v[72:75], v[60:63], v[96:99], v[72:75]\n\t" \
  "v_mfma_f32_16x16x32_bf16 v[72:75], v[76:79], v[84:87], v[72:75]\n\t" \
  "s_barrier\n\t" \
  "ds_write_b128 v101, v[32:35] offset:20480\n\t" \
  "ds_write_b128 v101, v[36:39] offset:21504\n\t" \
  "ds_write_b128 v101, v[40:43] offset:22528\n\t" \
  "ds_read_b128 v[48:51], v102 offset:32768\n\t" \
  "ds_read_b128 v[52:55], v102 offset:33792\n\t" \
  "ds_read_b128 v[56:59], v102 offset:34816\n\t" \
  "ds_read_b128 v[60:63], v102 offset:35840\n\t" \
  "ds_read_b128 v[16:19], v102 offset:49152\n\t" \
  "ds_read_b128 v[20:23], v102 offset:50176\n\t" \
  "ds_read_b128 v[24:27], v102 offset:51200\n\t" \
  "ds_read_b128 v[28:31], v102 offset:52224\n\t" \
  "ds_read_b128 v[84:87], v103 offset:16384\n\t" \
  "ds_read_b128 v[88:91], v103 offset:17408\n\t" \
  "ds_read_b128 v[92:95], v103 offset:18432\n\t" \
  "ds_read_b128 v[96:99], v103 offset:19456\n\t" \
  "ds_read_b128 v[0:3], v103 offset:49152\n\t" \
  "ds_read_b128 v[4:7], v103 offset:50176\n\t" \
  "ds_read_b128 v[8:11], v103 offset:51200\n\t" \
  "ds_read_b128 v[12:15], v103 offset:52224\n\t" \
  "v_max3_f32 v76, v64, v65, v66\n\t" \
  "v_max3_f32 v77, v67, v68, v69\n\t" \
  "v_max3_f32 v76, v76, v70, v71\n\t" \
  "v_max3_f32 v77, v77, v72, v73\n\t" \
  "v_max3_f32 v76, v76, v74, v75\n\t" \
  "v_max_f32 v76, v76, v77\n\t" \
  "v_mov_b32 v77, v76\n\t" \
  "s_nop 1\n\t" \
  "v_permlane16_swap_b32 v76, v77\n\t" \
  "v_max_f32 v76, v76, v77\n\t" \
  "v_mov_b32 v77, v76\n\t" \
  "s_nop 1\n\t" \
  "v_permlane32_swap_b32 v76, v77\n\t" \
  "v_max_f32 v76, v76, v77\n\t" \
  "v_mul_f32 v76, %[cs], v76\n\t" \
  "v_fma_f32 v64, v64, %[cs], -v76\n\t" \
  "v_fma_f32 v65, v65, %[cs], -v76\n\t" \
  "v_fma_f32 v66, v66, %[cs], -v76\n\t" \
  "v_fma_f32 v67, v67, %[cs], -v76\n\t" \
  "v_fma_f32 v68, v68, %[cs], -v76\n\t" \
  "v_fma_f32 v69, v69, %[cs], -v76\n\t" \
  "v_fma_f32 v70, v70, %[cs], -v76\n\t" \
  "v_fma_f32 v71, v71, %[cs], -v76\n\t" \
  "v_fma_f32 v72, v72, %[cs], -v76\n\t" \
  "v_fma_f32 v73, v73, %[cs], -v76\n\t" \
  "v_fma_f32 v74, v74, %[cs], -v76\n\t" \
  "v_fma_f32 v75, v75, %[cs], -v76\n\t" \
  "v_exp_f32 v64, v64\n\t" \
  "v_exp_f32 v65, v65\n\t" \
  "v_exp_f32 v66, v66\n\t" \
  "v_exp_f32 v67, v67\n\t" \
  "v_exp_f32 v68, v68\n\t" \
  "v_exp_f32 v69, v69\n\t" \
  "v_exp_f32 v70, v70\n\t" \
  "v_exp_f32 v71, v71\n\t" \
  "v_exp_f32 v72, v72\n\t" \
  "v_exp_f32 v73, v73\n\t" \
  "v_exp_f32 v74, v74\n\t" \
  "v_exp_f32 v75, v75\n\t" \
  "v_add_f32 v76, v72, v73\n\t" \
  "v_add_f32 v77, v74, v75\n\t" \
  "v_add_f32 v78, v64, v65\n\t" \
  "v_add_f32 v79, v66, v67\n\t" \
  "v_add_f32 v76, v76, v77\n\t" \
  "v_add_f32 v78, v78, v68\n\t" \
  "v_add_f32 v79, v79, v69\n\t" \
  "v_add_f32 v78, v78, v70\n\t" \
  "v_add_f32 v79, v79, v71\n\t" \
  "v_fma_f32 v76, v76, 0.5, v78\n\t" \
  "v_add_f32 v76, v76, v79\n\t" \
  "v_mov_b32 v77, v76\n\t" \
  "s_nop 1\n\t" \
  "v_permlane16_swap_b32 v76, v77\n\t" \
  "v_add_f32 v76, v76, v77\n\t" \
  "v_mov_b32 v77, v76\n\t" \
  "s_nop 1\n\t" \
  "v_permlane32_swap_b32 v76, v77\n\t" \
  "v_add_f32 v76, v76, v77\n\t" \
  "v_rcp_f32 v76, v76\n\t" \
  "s_mov_b32 exec_hi, 0\n\t" \
  "buffer_load_dword v118, v112, %[rh], s96 offen sc1\n\t" \
  "s_mov_b64 exec, -1\n\t" \
  "v_mul_f32 v64, v64, v76\n\t" \
  "v_mul_f32 v65, v65, v76\n\t" \
  "v_mul_f32 v66, v66, v76\n\t" \
  "v_mul_f32 v67, v67, v76\n\t" \
  "v_mul_f32 v68, v68, v76\n\t" \
  "v_mul_f32 v69, v69, v76\n\t" \
  "v_mul_f32 v70, v70, v76\n\t" \
  "v_mul_f32 v71, v71, v76\n\t" \
  "v_mul_f32 v72, v72, v76\n\t" \
  "v_mul_f32 v73, v73, v76\n\t" \
  "v_mul_f32 v74, v74, v76\n\t" \
  "v_mul_f32 v75, v75, v76\n\t" \
  "v_cvt_pk_bf16_f32 v76, v64, v68\n\t" \
  "v_cvt_pk_bf16_f32 v77, v65, v69\n\t" \
  "v_cvt_pk_bf16_f32 v78, v66, v70\n\t" \
  "v_cvt_pk_bf16_f32 v79, v67, v71\n\t" \
  "v_cvt_pk_bf16_f32 v80, v72, v73\n\t" \
  "v_cvt_pk_bf16_f32 v81, v74, v75\n\t" \
  "v_permlane32_swap_b32 v76, v78\n\t" \
  "v_permlane32_swap_b32 v77, v79\n\t" \
  "v_mov_b64 v[82:83], v[80:81]\n\t" \
  "buffer_store_dwordx4 v[76:79], v110, %[rh], s94 offen sc1\n\t" \
  "s_nop 0\n\t" \
  "v_permlane16_swap_b32 v80, v82\n\t" \
  "v_permlane16_swap_b32 v81, v83\n\t" \
  "buffer_store_dwordx4 v[80:83], v111, %[rh], s94 offen sc1\n\t" \
  "s_add_i32 s77, s44, 1\n\t" \
  "s_min_u32 s77, s77, 63\n\t" \
  "v_cmp_gt_i32 vcc, s77, v119\n\t" \
  "s_cmp_lg_u64 vcc, 0\n\t" \
  "s_cselect_b32 s42, 32, 0\n\t" \
  "s_add_i32 s44, s44, 1\n\t" \
  "s_and_b32 s58, s58, 4\n\t" \
  "s_and_b32 s76, s44, 64\n\t" \
  "s_or_b32 s42, s42, s76\n\t" \
  "s_cbranch_scc0 20b\n\t" \
  "s_branch 99f\n\t" \
  "14:\n\t" \
  "s_sleep 3\n\t" \
  "s_add_u32 s59, s59, 0x10000\n\t" \
  "s_cmp_lt_u32 s59, 0x100000\n\t" \
  "s_cbranch_scc1 13b\n\t" \
  "s_mov_b64 exec, -1\n\t" \
  "s_mov_b32 s42, 16\n\t" \
  "s_branch 99f\n\t" \
  "8:\n\t" \
  "s_bitcmp1_b32 s58, 1\n\t" \
  "s_cbranch_scc1 4b\n\t" \
  "s_bitcmp1_b32 s58, 2\n\t" \
  "s_cbranch_scc1 5b\n\t" \
  "v_max3_u32 v116, v32, v33, v34\n\t" \
  "v_max3_u32 v117, v35, v36, v37\n\t" \
  "v_max3_u32 v116, v116, v38, v39\n\t" \
  "v_max_u32 v116, v116, v117\n\t" \
  "v_cmp_eq_u32 vcc, -1, v116\n\t" \
  "s_cbranch_vccz 5b\n\t" \
  "s_branch 0f\n\t" \
  "9:\n\t" \
  "s_bitcmp1_b32 s58, 2\n\t" \
  "s_cbranch_scc1 6b\n\t" \
  "v_max3_u32 v116, v40, v41, v42\n\t" \
  "v_max3_u32 v117, v43, v44, v45\n\t" \
  "v_max3_u32 v116, v116, v46, v47\n\t" \
  "v_max_u32 v116, v116, v117\n\t" \
  "v_cmp_eq_u32 vcc, -1, v116\n\t" \
  "s_cbranch_vccz 6b\n\t" \
  "s_or_b32 s58, s58, 2\n\t" \
  "0:\n\t" \
  "s_or_b32 s58, s58, 1\n\t" \
  "11:\n\t" \
  "buffer_load_dwordx4 v[32:35], v100, %[rh], s40 offen sc1\n\t" \
  "buffer_load_dwordx4 v[36:39], v100, %[rh], s76 offen sc1\n\t" \
  "buffer_load_dwordx4 v[40:43], v100, %[rh], s77 offen sc1\n\t" \
  "buffer_load_dwordx4 v[44:47], v100, %[rh], s78 offen sc1\n\t" \
  "s_waitcnt lgkmcnt(0)\n\t" \
  "s_branch 10b\n\t" \
  "17:\n\t" \
  "s_add_u32 s59, s59, 1\n\t" \
  "s_bitcmp1_b32 s58, 2\n\t" \
  "s_cbranch_scc1 3b\n\t" \
  "s_waitcnt vmcnt(0)\n\t" \
  "v_max_u32 v116, v32, v33\n\t" \
  "v_max3_u32 v116, v116, v34, v35\n\t" \
  "v_max3_u32 v116, v116, v36, v37\n\t" \
  "v_max3_u32 v116, v116, v38, v39\n\t" \
  "v_max3_u32 v116, v116, v40, v41\n\t" \
  "v_max3_u32 v116, v116, v42, v43\n\t" \
  "v_max3_u32 v116, v116, v44, v45\n\t" \
  "v_max3_u32 v116, v116, v46, v47\n\t" \
  "v_cmp_eq_u32 vcc, -1, v116\n\t" \
  "s_cbranch_vccz 3b\n\t" \
  "s_bitcmp1_b32 s59, 6\n\t" \
  "s_cbranch_scc1 12f\n\t" \
  "buffer_load_dwordx4 v[32:35], v100, %[rh], s40 offen sc1\n\t" \
  "buffer_load_dwordx4 v[36:39], v100, %[rh], s76 offen sc1\n\t" \
  "buffer_load_dwordx4 v[40:43], v100, %[rh], s77 offen sc1\n\t" \
  "buffer_load_dwordx4 v[44:47], v100, %[rh], s78 offen sc1\n\t" \
  "s_branch 17b\n\t" \
  "12:\n\t" \
  "s_mov_b32 s42, s58\n\t" \
  "s_branch 99f\n\t" \
  "92:\n\t" \
  "s_mov_b32 s58, s42\n\t" \
  "s_mov_b32 s95, 0\n\t" \
  "20:\n\t" \
  "s_and_b32 s76, s44, 7\n\t" \
  "s_lshl_b32 s76, s76, 15\n\t" \
  "s_add_i32 s40, s54, s76\n\t" \
  "s_and_b32 s76, s44, 3\n\t" \
  "s_lshl_b32 s45, s76, 14\n\t" \
  "s_add_i32 s47, s44, 1\n\t" \
  "s_min_u32 s47, s47, 63\n\t" \
  "s_lshr_b32 s76, s47, 1\n\t" \
  "s_lshl_b32 s47, s47, 20\n\t" \
  "s_add_i32 s47, s47, s55\n\t" \
  "s_bitcmp1_b64 %[sel], s76\n\t" \
  "s_cselect_b32 s76, 0x2000, 0\n\t" \
  "s_add_i32 s47, s47, s76\n\t" \
  "s_lshr_b32 s76, s44, 1\n\t" \
  "s_lshl_b32 s94, s44, 20\n\t" \
  "s_bitcmp1_b64 %[sel], s76\n\t" \
  "s_cselect_b32 s76, 0x2000, 0\n\t" \
  "s_add_i32 s94, s94, s76\n\t" \
  "s_add_i32 s49, s44, -1\n\t" \
  "s_and_b32 s48, s49, 2\n\t" \
  "s_lshl_b32 s48, s48, 13\n\t" \
  "s_and_b32 s76, s49, 1\n\t" \
  "s_lshl_b32 s76, s76, 12\n\t" \
  "s_add_i32 s48, s48, s76\n\t" \
  "s_lshl_b32 s50, s53, 8\n\t" \
  "s_mov_b32 s59, 0\n\t" \
  "s_mov_b32 s92, s51\n\t" \
  "s_mov_b32 s93, s53\n\t" \
  "s_xor_b32 s76, s40, 0x400\n\t" \
  "s_xor_b32 s77, s40, 0x800\n\t" \
  "s_xor_b32 s78, s40, 0xc00\n\t" \
  "s_cmp_lg_u32 s58, 0\n\t" \
  "s_cbranch_scc1 11f\n\t" \
  "s_cmp_lg_u32 s95, 0\n\t" \
  "s_cbranch_scc1 46f\n\t" \
  "10:\n\t" \
  "ds_read_b128 v[48:51], v103 offset:0\n\t" \
  "ds_read_b128 v[52:55], v103 offset:1024\n\t" \
  "ds_read_b128 v[56:59], v103 offset:2048\n\t" \
  "ds_read_b128 v[60:63], v103 offset:3072\n\t" \
  "ds_read_b128 v[16:19], v103 offset:16384\n\t" \
  "ds_read_b128 v[20:23], v103 offset:17408\n\t" \
  "ds_read_b128 v[24:27], v103 offset:18432\n\t" \
  "ds_read_b128 v[28:31], v103 offset:19456\n\t" \
  "ds_read_b128 v[84:87], v103 offset:49152\n\t" \
  "ds_read_b128 v[88:91], v103 offset:50176\n\t" \
  "ds_read_b128 v[92:95], v103 offset:51200\n\t" \
  "ds_read_b128 v[96:99], v103 offset:52224\n\t" \
  "s_waitcnt lgkmcnt(4)\n\t" \
  "ds_read_b128 v[0:3], v102 offset:16384\n\t" \
  "ds_read_b128 v[4:7], v102 offset:17408\n\t" \
  "ds_read_b128 v[8:11], v102 offset:18432\n\t" \
  "ds_read_b128 v[12:15], v102 offset:19456\n\t" \
  "46:\n\t" \
  "s_bitcmp1_b32 s58, 1\n\t" \
  "s_cselect_b64 s[80:81], -1, 0\n\t" \
  "s_cmp_lg_u32 s58, 0\n\t" \
  "s_cbranch_scc1 17f\n\t" \
  "s_mov_b32 exec_hi, 0\n\t" \
  "s_cmp_eq_u32 s95, 0\n\t" \
  "s_cbranch_scc1 16f\n\t" \
  "s_waitcnt vmcnt(2)\n\t" \
  "v_mov_b32 v116, v118\n\t" \
  "s_branch 15f\n\t" \
  "16:\n\t" \
  "s_memtime vcc\n\t" \
  "s_waitcnt lgkmcnt(0)\n\t" \
  "s_sub_i32 s79, s41, vcc_lo\n\t" \
  "s_cmp_gt_i32 s79, 64\n\t" \
  "s_cbranch_scc1 16b\n\t" \
  "13:\n\t" \
  "buffer_load_dword v116, v112, %[rh], s48 offen sc1\n\t" \
  "s_waitcnt vmcnt(0)\n\t" \
  "15:\n\t" \
  "v_mov_b32 v117, 20000\n\t" \
  "v_cmp_ne_u32_sdwa vcc, v116, s49 src0_sel:BYTE_0 src1_sel:DWORD\n\t" \
  "v_subrev_u32 v116, s50, v116\n\t" \
  "v_ashrrev_i32 v116, 8, v116\n\t" \
  "s_nop 1\n\t" \
  "v_max_i32_dpp v116, v116, v116 quad_perm:[1,0,3,2] row_mask:0xf bank_mask:0xf\n\t" \
  "s_cbranch_vccnz 14f\n\t" \
  "s_nop 0\n\t" \
  "v_max_i32_dpp v116, v116, v116 quad_perm:[2,3,0,1] row_mask:0xf bank_mask:0xf\n\t" \
  "s_nop 1\n\t" \
  "v_max_i32_dpp v116, v116, v116 row_half_mirror row_mask:0xf bank_mask:0xf\n\t" \
  "s_nop 1\n\t" \
  "v_max_i32_dpp v116, v116, v116 row_mirror row_mask:0xf bank_mask:0xf\n\t" \
  "v_med3_i32 v116, v116, 0, v117\n\t" \
  "v_mul_u32_u24 v116, s52, v116\n\t" \
  "v_lshrrev_b32 v116, 16, v116\n\t" \
  "s_mov_b64 exec, -1\n\t" \
  "v_readfirstlane_b32 s84, v116\n\t" \
  "v_readlane_b32 s85, v116, 16\n\t" \
  "s_max_u32 s84, s84, s85\n\t" \
  "s_add_i32 s84, s84, s51\n\t" \
  "1:\n\t" \
  "s_memtime vcc\n\t" \
  "s_waitcnt lgkmcnt(0)\n\t" \
  "s_sub_i32 s79, s84, vcc_lo\n\t" \
  "s_cmp_gt_i32 s79, 128\n\t" \
  "s_cbranch_scc1 1b\n\t" \
  ".p2align 4\n\t" \
  "2:\n\t" \
  "s_sub_i32 s79, s79, 33\n\t" \
  "s_cmp_gt_i32 s79, 56\n\t" \
  "s_cbranch_scc1 2b\n\t" \
  "buffer_load_dwordx4 v[32:35], v100, %[rh], s40 offen sc1\n\t" \
  "s_nop 12\n\t" \
  "buffer_load_dwordx4 v[36:39], v100, %[rh], s76 offen sc1\n\t" \
  "s_nop 12\n\t" \
  "buffer_load_dwordx4 v[40:43], v100, %[rh], s77 offen sc1\n\t" \
  "s_nop 12\n\t" \
  "buffer_load_dwordx4 v[44:47], v100, %[rh], s78 offen sc1\n\t" \
  "s_memtime vcc\n\t" \
  "s_memrealtime s[82:83]\n\t" \
  "s_nop 4\n\t" \
  "s_memrealtime s[84:85]\n\t" \
  "s_nop 4\n\t" \
  "s_memrealtime s[56:57]\n\t" \
  "s_nop 4\n\t" \
  "s_memrealtime s[86:87]\n\t" \
  "s_nop 4\n\t" \
  "s_memrealtime s[88:89]\n\t" \
  "s_nop 4\n\t" \
  "s_memrealtime s[90:91]\n\t" \
  "s_mov_b32 s92, 0\n\t" \
  "s_waitcnt lgkmcnt(0)\n\t" \
  "s_cmp_eq_u32 s82, s84\n\t" \
  "s_addc_u32 s92, s92, 0\n\t" \
  "s_cmp_eq_u32 s82, s56\n\t" \
  "s_addc_u32 s92, s92, 0\n\t" \
  "s_cmp_eq_u32 s82, s86\n\t" \
  "s_addc_u32 s92, s92, 0\n\t" \
  "s_cmp_eq_u32 s82, s88\n\t" \
  "s_addc_u32 s92, s92, 0\n\t" \
  "s_cmp_eq_u32 s82, s90\n\t" \
  "s_addc_u32 s92, s92, 0\n\t" \
  "s_mul_i32 s92, s92, 24\n\t" \
  "s_add_i32 s92, s92, vcc_lo\n\t" \
  "s_lshl4_add_u32 s93, s82, 64\n\t" \
  "3:\n\t" \
  "v_mov_b64 v[68:69], 0\n\t" \
  "v_mov_b64 v[70:71], 0\n\t" \
  "v_mov_b64 v[72:73], 0\n\t" \
  "v_mov_b64 v[74:75], 0\n\t" \
  "v_mov_b64 v[76:77], 0\n\t" \
  "v_mov_b64 v[78:79], 0\n\t" \
  "v_mov_b64 v[80:81], 0\n\t" \
  "v_mov_b64 v[82:83], 0\n\t" \
  "s_waitcnt lgkmcnt(0)\n\t" \
  "s_waitcnt vmcnt(3)\n\t" \
  "v_smfmac_f32_16x16x64_bf16 v[68:71], v[32:35], a[192:199], v104\n\t" \
  "ds_write_b128 v101, v[32:35] offset:8192\n\t" \
  "v_smfmac_f32_16x16x64_bf16 v[72:75], v[32:35], a[200:207], v104\n\t" \
  "v_smfmac_f32_16x16x64_bf16 v[76:79], v[32:35], v[48:55], v104\n\t" \
  "v_smfmac_f32_16x16x64_bf16 v[80:83], v[32:35], v[56:63], v104\n\t" \
  "s_waitcnt vmcnt(2)\n\t" \
  "v_smfmac_f32_16x16x64_bf16 v[68:71], v[36:39], a[208:215], v104\n\t" \
  "ds_write_b128 v101, v[36:39] offset:9216\n\t" \
  "v_smfmac_f32_16x16x64_bf16 v[72:75], v[36:39], a[216:223], v104\n\t" \
  "v_smfmac_f32_16x16x64_bf16 v[76:79], v[36:39], a[224:231], v104\n\t" \
  "v_cmp_u_f32 vcc, v68, v69\n\t" \
  "v_smfmac_f32_16x16x64_bf16 v[80:83], v[36:39], a[232:239], v104\n\t" \
  "v_cmp_u_f32 s[82:83], v70, v71\n\t" \
  "s_waitcnt vmcnt(1)\n\t" \
  "v_smfmac_f32_16x16x64_bf16 v[68:71], v[40:43], v[16:23], v104\n\t" \
  "ds_write_b128 v101, v[40:43] offset:10240\n\t" \
  "v_smfmac_f32_16x16x64_bf16 v[72:75], v[40:43], v[24:31], v104\n\t" \
  "s_or_b64 vcc, vcc, s[82:83]\n\t" \
  "s_or_b64 vcc, vcc, s[80:81]\n\t" \
  "s_waitcnt lgkmcnt(1)\n\t" \
  "v_smfmac_f32_16x16x64_bf16 v[76:79], v[40:43], a[240:247], v104\n\t" \
  "s_cbranch_scc1 8f\n\t" \
  "5:\n\t" \
  "s_barrier\n\t" \
  "4:\n\t" \
  "s_memtime s[56:57]\n\t" \
  "ds_read_b128 v[48:51], v101 offset:12288\n\t" \
  "v_smfmac_f32_16x16x64_bf16 v[80:83], v[40:43], a[248:255], v104\n\t" \
  "ds_read_b128 v[52:55], v101 offset:13312\n\t" \
  "ds_read_b128 v[56:59], v101 offset:0\n\t" \
  "s_waitcnt vmcnt(0)\n\t" \
  "v_smfmac_f32_16x16x64_bf16 v[68:71], v[44:47], v[120:127], v104\n\t" \
  "v_smfmac_f32_16x16x64_bf16 v[72:75], v[44:47], v[128:135], v104\n\t" \
  "ds_write_b128 v101, v[44:47] offset:11264\n\t" \
  "v_smfmac_f32_16x16x64_bf16 v[76:79], v[44:47], v[136:143], v104\n\t" \
  "v_cmp_u_f32 vcc, v68, v69\n\t" \
  "v_smfmac_f32_16x16x64_bf16 v[80:83], v[44:47], v[144:151], v104\n\t" \
  "v_cmp_u_f32 s[82:83], v70, v71\n\t" \
  "s_waitcnt lgkmcnt(3)\n\t" \
  "v_smfmac_f32_16x16x64_bf16 v[68:71], v[48:51], v[152:159], v104\n\t" \
  "ds_read_b128 v[60:63], v101 offset:1024\n\t" \
  "ds_read_b128 v[64:67], v101 offset:4096\n\t" \
  "v_smfmac_f32_16x16x64_bf16 v[72:75], v[48:51], v[160:167], v104\n\t" \
  "s_or_b64 vcc, vcc, s[82:83]\n\t" \
  "s_waitcnt lgkmcnt(2)\n\t" \
  "v_smfmac_f32_16x16x64_bf16 v[68:71], v[52:55], v[168:175], v104\n\t" \
  "s_cbranch_scc1 9f\n\t" \
  "6:\n\t" \
  "s_barrier\n\t" \
  "v_smfmac_f32_16x16x64_bf16 v[72:75], v[52:55], v[176:183], v104\n\t" \
  "ds_read_b128 v[48:51], v101 offset:5120\n\t" \
  "v_smfmac_f32_16x16x64_bf16 v[68:71], v[56:59], a[0:7], v104\n\t" \
  "ds_read_b128 v[52:55], v101 offset:14336\n\t" \
  "v_smfmac_f32_16x16x64_bf16 v[72:75], v[56:59], a[8:15], v104\n\t" \
  "s_waitcnt lgkmcnt(3)\n\t" \
  "v_smfmac_f32_16x16x64_bf16 v[68:71], v[60:63], a[16:23], v104\n\t" \
  "ds_read_b128 v[56:59], v101 offset:15360\n\t" \
  "v_smfmac_f32_16x16x64_bf16 v[72:75], v[60:63], a[24:31], v104\n\t" \
  "s_waitcnt lgkmcnt(3)\n\t" \
  "v_smfmac_f32_16x16x64_bf16 v[68:71], v[64:67], a[96:103], v104\n\t" \
  "ds_read_b128 v[60:63], v101 offset:2048\n\t" \
  "v_smfmac_f32_16x16x64_bf16 v[72:75], v[64:67], a[104:111], v104\n\t" \
  "s_waitcnt lgkmcnt(3)\n\t" \
  "v_smfmac_f32_16x16x64_bf16 v[68:71], v[48:51], a[112:119], v104\n\t" \
  "ds_read_b128 v[64:67], v101 offset:3072\n\t" \
  "ds_read_b128 v[16:19], v102 offset:49152\n\t" \
  "v_smfmac_f32_16x16x64_bf16 v[72:75], v[48:51], a[120:127], v104\n\t" \
  "s_waitcnt lgkmcnt(4)\n\t" \
  "v_smfmac_f32_16x16x64_bf16 v[68:71], v[52:55], v[84:91], v104\n\t" \
  "ds_read_b128 v[48:51], v101 offset:7168\n\t" \
  "ds_read_b128 v[20:23], v102 offset:50176\n\t" \
  "v_smfmac_f32_16x16x64_bf16 v[72:75], v[52:55], v[92:99], v104\n\t" \
  "s_waitcnt lgkmcnt(5)\n\t" \
  "v_smfmac_f32_16x16x64_bf16 v[68:71], v[56:59], v[216:223], v104\n\t" \
  "ds_read_b128 v[52:55], v101 offset:6144\n\t" \
  "ds_read_b128 v[24:27], v102 offset:51200\n\t" \
  "v_smfmac_f32_16x16x64_bf16 v[72:75], v[56:59], v[224:231], v104\n\t" \
  "ds_read_b128 v[28:31], v102 offset:52224\n\t" \
  "s_waitcnt lgkmcnt(7)\n\t" \
  "v_smfmac_f32_16x16x64_bf16 v[68:71], v[60:63], v[0:7], v104\n\t" \
  "v_smfmac_f32_16x16x64_bf16 v[72:75], v[60:63], v[8:15], v104\n\t" \
  "s_waitcnt lgkmcnt(6)\n\t" \
  "v_smfmac_f32_16x16x64_bf16 v[68:71], v[64:67], a[64:71], v104\n\t" \
  "ds_read_b128 v[0:3], v103 offset:32768\n\t" \
  "v_smfmac_f32_16x16x64_bf16 v[72:75], v[64:67], a[72:79], v104\n\t" \
  "ds_read_b128 v[4:7], v103 offset:33792\n\t" \
  "s_waitcnt lgkmcnt(6)\n\t" \
  "v_smfmac_f32_16x16x64_bf16 v[68:71], v[48:51], a[160:167], v104\n\t" \
  "s_waitcnt lgkmcnt(4)\n\t" \
  "v_smfmac_f32_16x16x64_bf16 v[68:71], v[52:55], v[16:23], v104\n\t" \
  "ds_read_b128 v[8:11], v103 offset:34816\n\t" \
  "v_smfmac_f32_16x16x64_bf16 v[72:75], v[48:51], a[168:175], v104\n\t" \
  "ds_read_b128 v[12:15], v103 offset:35840\n\t" \
  "s_waitcnt lgkmcnt(4)\n\t" \
  "v_smfmac_f32_16x16x64_bf16 v[72:75], v[52:55], v[24:31], v104\n\t" \
  "v_add_f32 v44, v68, v70\n\t" \
  "v_smfmac_f32_16x16x64_bf16 v[76:79], v[56:59], v[232:239], v104\n\t" \
  "v_add_f32 v45, v69, v71\n\t" \
  "v_cvt_pk_bf16_f32 v44, v44, v45\n\t" \
  "v_smfmac_f32_16x16x64_bf16 v[80:83], v[56:59], v[240:247], v104\n\t" \
  "v_add_f32 v46, v72, v74\n\t" \
  "v_add_f32 v47, v73, v75\n\t" \
  "v_smfmac_f32_16x16x64_bf16 v[76:79], v[60:63], a[48:55], v104\n\t" \
  "v_cvt_pk_bf16_f32 v46, v46, v47\n\t" \
  "ds_write2st64_b32 v105, v44, v46 offset1:1\n\t" \
  "v_smfmac_f32_16x16x64_bf16 v[80:83], v[60:63], a[56:63], v104\n\t" \
  "ds_read_b128 v[56:59], v101 offset:12288\n\t" \
  "v_smfmac_f32_16x16x64_bf16 v[76:79], v[64:67], a[80:87], v104\n\t" \
  "ds_read_b128 v[16:19], v102 offset:0\n\t" \
  "v_smfmac_f32_16x16x64_bf16 v[80:83], v[64:67], a[88:95], v104\n\t" \
  "ds_read_b128 v[20:23], v102 offset:1024\n\t" \
  "v_smfmac_f32_16x16x64_bf16 v[76:79], v[48:51], a[176:183], v104\n\t" \
  "s_waitcnt lgkmcnt(3)\n\t" \
  "s_barrier\n\t" \
  "v_smfmac_f32_16x16x64_bf16 v[80:83], v[48:51], a[184:191], v104\n\t" \
  "buffer_load_dwordx4 v[84:87], v107, %[ru], s94 offen offset:0 sc1\n\t" \
  "v_smfmac_f32_16x16x64_bf16 v[76:79], v[52:55], a[144:151], v104\n\t" \
  "ds_read_b128 v[60:63], v101 offset:5120\n\t" \
  "ds_read_b128 v[24:27], v102 offset:2048\n\t" \
  "buffer_load_dwordx4 v[88:91], v107, %[ru], s94 offen offset:1024 sc1\n\t" \
  "global_load_dword v119, v109, %[kc] sc1\n\t" \
  "v_smfmac_f32_16x16x64_bf16 v[80:83], v[52:55], a[152:159], v104\n\t" \
  "ds_read_b128 v[64:67], v101 offset:14336\n\t" \
  "buffer_load_dwordx4 v[92:95], v107, %[ru], s94 offen offset:2048 sc1\n\t" \
  "s_waitcnt lgkmcnt(5)\n\t" \
  "v_smfmac_f32_16x16x64_bf16 v[76:79], v[56:59], v[0:7], v104\n\t" \
  "ds_read_b128 v[48:51], v101 offset:0\n\t" \
  "ds_read_b128 v[28:31], v102 offset:3072\n\t" \
  "buffer_load_dwordx4 v[96:99], v107, %[ru], s94 offen offset:3072 sc1\n\t" \
  "v_smfmac_f32_16x16x64_bf16 v[80:83], v[56:59], v[8:15], v104\n\t" \
  "s_waitcnt lgkmcnt(4)\n\t" \
  "v_smfmac_f32_16x16x64_bf16 v[76:79], v[60:63], a[128:135], v104\n\t" \
  "ds_read_b128 v[52:55], v101 offset:13312\n\t" \
  "buffer_load_dwordx4 v[32:35], v100, %[ru], s47 offen offset:0 sc1\n\t" \
  "v_smfmac_f32_16x16x64_bf16 v[80:83], v[60:63], a[136:143], v104\n\t" \
  "ds_read_b128 v[0:3], v102 offset:32768\n\t" \
  "s_waitcnt lgkmcnt(4)\n\t" \
  "v_smfmac_f32_16x16x64_bf16 v[76:79], v[64:67], v[200:207], v104\n\t" \
  "ds_read_b128 v[56:59], v101 offset:1024\n\t" \
  "v_smfmac_f32_16x16x64_bf16 v[80:83], v[64:67], v[208:215], v104\n\t" \
  "ds_read_b128 v[60:63], v101 offset:4096\n\t" \
  "ds_read_b128 v[4:7], v102 offset:33792\n\t" \
  "buffer_load_dwordx4 v[36:39], v100, %[ru], s47 offen offset:1024 sc1\n\t" \
  "s_waitcnt lgkmcnt(6)\n\t" \
  "v_smfmac_f32_16x16x64_bf16 v[76:79], v[48:51], v[16:23], v104\n\t" \
  "ds_read_b128 v[8:11], v102 offset:34816\n\t" \
  "s_waitcnt lgkmcnt(6)\n\t" \
  "v_smfmac_f32_16x16x64_bf16 v[80:83], v[48:51], v[24:31], v104\n\t" \
  "ds_read_b128 v[12:15], v102 offset:35840\n\t" \
  "s_waitcnt lgkmcnt(6)\n\t" \
  "v_smfmac_f32_16x16x64_bf16 v[76:79], v[52:55], v[184:191], v104\n\t" \
  "buffer_load_dwordx4 v[40:43], v100, %[ru], s47 offen offset:2048 sc1\n\t" \
  "v_smfmac_f32_16x16x64_bf16 v[80:83], v[52:55], v[192:199], v104\n\t" \
  "s_waitcnt lgkmcnt(4)\n\t" \
  "v_smfmac_f32_16x16x64_bf16 v[76:79], v[56:59], a[32:39], v104\n\t" \
  "s_waitcnt lgkmcnt(2)\n\t" \
  "v_smfmac_f32_16x16x64_bf16 v[76:79], v[60:63], v[0:7], v104\n\t" \
  "v_smfmac_f32_16x16x64_bf16 v[80:83], v[56:59], a[40:47], v104\n\t" \
  "s_waitcnt lgkmcnt(0)\n\t" \
  "v_smfmac_f32_16x16x64_bf16 v[80:83], v[60:63], v[8:15], v104\n\t" \
  "v_add_f32 v20, v76, v78\n\t" \
  "v_add_f32 v21, v77, v79\n\t" \
  "v_cvt_pk_bf16_f32 v20, v20, v21\n\t" \
  "ds_write_b32 v106, v20\n\t" \
  "s_nop 3\n\t" \
  "v_add_f32 v23, v80, v82\n\t" \
  "v_add_f32 v21, v81, v83\n\t" \
  "v_cvt_pk_bf16_f32 v21, v23, v21\n\t" \
  "ds_write_b32 v106, v21 offset:256\n\t" \
  "s_waitcnt lgkmcnt(0)\n\t" \
  "s_barrier\n\t" \
  "v_bfe_i32 v64, v114, 0, 1\n\t" \
  "v_and_b32 v64, 0xff800000, v64\n\t" \
  "v_bfe_i32 v65, v114, 1, 1\n\t" \
  "v_and_b32 v65, 0xff800000, v65\n\t" \
  "v_bfe_i32 v66, v114, 2, 1\n\t" \
  "v_and_b32 v66, 0xff800000, v66\n\t" \
  "v_bfe_i32 v67, v114, 3, 1\n\t" \
  "v_and_b32 v67, 0xff800000, v67\n\t" \
  "v_bfe_i32 v68, v114, 4, 1\n\t" \
  "v_and_b32 v68, 0xff800000, v68\n\t" \
  "v_bfe_i32 v69, v114, 5, 1\n\t" \
  "v_and_b32 v69, 0xff800000, v69\n\t" \
  "v_bfe_i32 v70, v114, 6, 1\n\t" \
  "v_and_b32 v70, 0xff800000, v70\n\t" \
  "v_bfe_i32 v71, v114, 7, 1\n\t" \
  "v_and_b32 v71, 0xff800000, v71\n\t" \
  "s_lshl_b32 s94, s45, 4\n\t" \
  "s_and_b32 s96, s44, 2\n\t" \
  "s_lshl_b32 s96, s96, 13\n\t" \
  "s_and_b32 s76, s44, 1\n\t" \
  "s_lshl_b32 s76, s76, 12\n\t" \
  "s_add_i32 s96, s96, s76\n\t" \
  "s_mov_b32 s95, 1\n\t" \
  "s_add_i32 s41, s56, 1950\n\t" \
  "s_sub_i32 s76, s92, s51\n\t" \
  "s_sub_i32 s77, s93, s53\n\t" \
  "s_cmp_eq_u32 s76, 0\n\t" \
  "s_cbranch_scc1 21f\n\t" \
  "v_cvt_f32_u32 v117, s76\n\t" \
  "v_cvt_f32_u32 v116, s77\n\t" \
  "v_rcp_f32 v117, v117\n\t" \
  "s_mov_b32 s51, s92\n\t" \
  "v_mul_f32 v117, v117, v116\n\t" \
  "v_max_f32 v117, 0x3f028f5c, v117\n\t" \
  "v_min_f32 v117, 0x3ffeb852, v117\n\t" \
  "v_rcp_f32 v116, v117\n\t" \
  "v_mul_f32 v117, 0x4f000000, v117\n\t" \
  "v_mul_f32 v116, 0x47800000, v116\n\t" \
  "v_cvt_u32_f32 v117, v117\n\t" \
  "v_cvt_u32_f32 v116, v116\n\t" \
  "s_mov_b32 s53, s93\n\t" \
  "v_readfirstlane_b32 s46, v117\n\t" \
  "v_readfirstlane_b32 s52, v116\n\t" \
  "21:\n\t" \
  "ds_read_b128 v[0:3], v101 offset:20480\n\t" \
  "ds_read_b128 v[4:7], v101 offset:21504\n\t" \
  "ds_read_b128 v[8:11], v101 offset:22528\n\t" \
  "ds_read_b128 v[12:15], v101 offset:23552\n\t" \
  "ds_read_b128 v[16:19], v101 offset:24576\n\t" \
  "ds_read_b128 v[20:23], v101 offset:25600\n\t" \
  "ds_read_b128 v[24:27], v101 offset:26624\n\t" \
  "ds_read_b128 v[28:31], v101 offset:27648\n\t" \
  "ds_read_b64_tr_b16 v[48:49], v108 offset:0\n\t" \
  "ds_read_b64_tr_b16 v[50:51], v108 offset:64\n\t" \
  "ds_read_b64_tr_b16 v[52:53], v108 offset:512\n\t" \
  "ds_read_b64_tr_b16 v[54:55], v108 offset:576\n\t" \
  "ds_read_b64_tr_b16 v[56:57], v108 offset:1024\n\t" \
  "ds_read_b64_tr_b16 v[58:59], v108 offset:1088\n\t" \
  "ds_read_b64_tr_b16 v[60:61], v108 offset:1536\n\t" \
  "ds_read_b64_tr_b16 v[62:63], v108 offset:1600\n\t" \
  "ds_read_b64_tr_b16 v[76:77], v108 offset:12288\n\t" \
  "ds_read_b64_tr_b16 v[78:79], v108 offset:12352\n\t" \
  "s_waitcnt vmcnt(0) lgkmcnt(10)\n\t" \
  "v_mfma_f32_16x16x32_bf16 v[64:67], v[0:3], v[84:87], v[64:67]\n\t" \
  "v_mfma_f32_16x16x32_bf16 v[68:71], v[16:19], v[84:87], v[68:71]\n\t" \
  "v_mfma_f32_16x16x32_bf16 v[64:67], v[4:7], v[88:91], v[64:67]\n\t" \
  "v_mfma_f32_16x16x32_bf16 v[68:71], v[20:23], v[88:91], v[68:71]\n\t" \
  "v_mfma_f32_16x16x32_bf16 v[64:67], v[8:11], v[92:95], v[64:67]\n\t" \
  "v_mfma_f32_16x16x32_bf16 v[68:71], v[24:27], v[92:95], v[68:71]\n\t" \
  "v_mfma_f32_16x16x32_bf16 v[64:67], v[12:15], v[96:99], v[64:67]\n\t" \
  "v_mfma_f32_16x16x32_bf16 v[68:71], v[28:31], v[96:99], v[68:71]\n\t" \
  "s_waitcnt lgkmcnt(0)\n\t" \
  "v_mfma_f32_16x16x32_bf16 v[72:75], v[48:51], v[84:87], 0\n\t" \
  "v_mfma_f32_16x16x32_bf16 v[72:75], v[52:55], v[88:91], v[72:75]\n\t" \
  "v_mfma_f32_16x16x32_bf16 v[72:75], v[56:59], v[92:95], v[72:75]\n\t" \
  "v_mfma_f32_16x16x32_bf16 v[72:75], v[60:63], v[96:99], v[72:75]\n\t" \
  "v_mfma_f32_16x16x32_bf16 v[72:75], v[76:79], v[84:87], v[72:75]\n\t" \
  "s_barrier\n\t" \
  "ds_write_b128 v101, v[32:35] offset:23552\n\t" \
  "ds_write_b128 v101, v[36:39] offset:24576\n\t" \
  "ds_write_b128 v101, v[40:43] offset:25600\n\t" \
  "ds_read_b128 v[48:51], v103 offset:0\n\t" \
  "ds_read_b128 v[52:55], v103 offset:1024\n\t" \
  "ds_read_b128 v[56:59], v103 offset:2048\n\t" \
  "ds_read_b128 v[60:63], v103 offset:3072\n\t" \
  "ds_read_b128 v[16:19], v103 offset:16384\n\t" \
  "ds_read_b128 v[20:23], v103 offset:17408\n\t" \
  "ds_read_b128 v[24:27], v103 offset:18432\n\t" \
  "ds_read_b128 v[28:31], v103 offset:19456\n\t" \
  "ds_read_b128 v[84:87], v103 offset:49152\n\t" \
  "ds_read_b128 v[88:91], v103 offset:50176\n\t" \
  "ds_read_b128 v[92:95], v103 offset:51200\n\t" \
  "ds_read_b128 v[96:99], v103 offset:52224\n\t" \
  "ds_read_b128 v[0:3], v102 offset:16384\n\t" \
  "ds_read_b128 v[4:7], v102 offset:17408\n\t" \
  "ds_read_b128 v[8:11], v102 offset:18432\n\t" \
  "ds_read_b128 v[12:15], v102 offset:19456\n\t" \
  "v_max3_f32 v76, v64, v65, v66\n\t" \
  "v_max3_f32 v77, v67, v68, v69\n\t" \
  "v_max3_f32 v76, v76, v70, v71\n\t" \
  "v_max3_f32 v77, v77, v72, v73\n\t" \
  "v_max3_f32 v76, v76, v74, v75\n\t" \
  "v_max_f32 v76, v76, v77\n\t" \
  "v_mov_b32 v77, v76\n\t" \
  "s_nop 1\n\t" \
  "v_permlane16_swap_b32 v76, v77\n\t" \
  "v_max_f32 v76, v76, v77\n\t" \
  "v_mov_b32 v77, v76\n\t" \
  "s_nop 1\n\t" \
  "v_permlane32_swap_b32 v76, v77\n\t" \
  "v_max_f32 v76, v76, v77\n\t" \
  "v_mul_f32 v76, %[cs], v76\n\t" \
  "v_fma_f32 v64, v64, %[cs], -v76\n\t" \
  "v_fma_f32 v65, v65, %[cs], -v76\n\t" \
  "v_fma_f32 v66, v66, %[cs], -v76\n\t" \
  "v_fma_f32 v67, v67, %[cs], -v76\n\t" \
  "v_fma_f32 v68, v68, %[cs], -v76\n\t" \
  "v_fma_f32 v69, v69, %[cs], -v76\n\t" \
  "v_fma_f32 v70, v70, %[cs], -v76\n\t" \
  "v_fma_f32 v71, v71, %[cs], -v76\n\t" \
  "v_fma_f32 v72, v72, %[cs], -v76\n\t" \
  "v_fma_f32 v73, v73, %[cs], -v76\n\t" \
  "v_fma_f32 v74, v74, %[cs], -v76\n\t" \
  "v_fma_f32 v75, v75, %[cs], -v76\n\t" \
  "v_exp_f32 v64, v64\n\t" \
  "v_exp_f32 v65, v65\n\t" \
  "v_exp_f32 v66, v66\n\t" \
  "v_exp_f32 v67, v67\n\t" \
  "v_exp_f32 v68, v68\n\t" \
  "v_exp_f32 v69, v69\n\t" \
  "v_exp_f32 v70, v70\n\t" \
  "v_exp_f32 v71, v71\n\t" \
  "v_exp_f32 v72, v72\n\t" \
  "v_exp_f32 v73, v73\n\t" \
  "v_exp_f32 v74, v74\n\t" \
  "v_exp_f32 v75, v75\n\t" \
  "v_add_f32 v76, v72, v73\n\t" \
  "v_add_f32 v77, v74, v75\n\t" \
  "v_add_f32 v78, v64, v65\n\t" \
  "v_add_f32 v79, v66, v67\n\t" \
  "v_add_f32 v76, v76, v77\n\t" \
  "v_add_f32 v78, v78, v68\n\t" \
  "v_add_f32 v79, v79, v69\n\t" \
  "v_add_f32 v78, v78, v70\n\t" \
  "v_add_f32 v79, v79, v71\n\t" \
  "v_fma_f32 v76, v76, 0.5, v78\n\t" \
  "v_add_f32 v76, v76, v79\n\t" \
  "v_mov_b32 v77, v76\n\t" \
  "s_nop 1\n\t" \
  "v_permlane16_swap_b32 v76, v77\n\t" \
  "v_add_f32 v76, v76, v77\n\t" \
  "v_mov_b32 v77, v76\n\t" \
  "s_nop 1\n\t" \
  "v_permlane32_swap_b32 v76, v77\n\t" \
  "v_add_f32 v76, v76, v77\n\t" \
  "v_rcp_f32 v76, v76\n\t" \
  "s_mov_b32 exec_hi, 0\n\t" \
  "buffer_load_dword v118, v112, %[rh], s96 offen sc1\n\t" \
  "s_mov_b64 exec, -1\n\t" \
  "v_mul_f32 v64, v64, v76\n\t" \
  "v_mul_f32 v65, v65, v76\n\t" \
  "v_mul_f32 v66, v66, v76\n\t" \
  "v_mul_f32 v67, v67, v76\n\t" \
  "v_mul_f32 v68, v68, v76\n\t" \
  "v_mul_f32 v69, v69, v76\n\t" \
  "v_mul_f32 v70, v70, v76\n\t" \
  "v_mul_f32 v71, v71, v76\n\t" \
  "v_mul_f32 v72, v72, v76\n\t" \
  "v_mul_f32 v73, v73, v76\n\t" \
  "v_mul_f32 v74, v74, v76\n\t" \
  "v_mul_f32 v75, v75, v76\n\t" \
  "v_cvt_pk_bf16_f32 v76, v64, v68\n\t" \
  "v_cvt_pk_bf16_f32 v77, v65, v69\n\t" \
  "v_cvt_pk_bf16_f32 v78, v66, v70\n\t" \
  "v_cvt_pk_bf16_f32 v79, v67, v71\n\t" \
  "v_cvt_pk_bf16_f32 v80, v72, v73\n\t" \
  "v_cvt_pk_bf16_f32 v81, v74, v75\n\t" \
  "v_permlane32_swap_b32 v76, v78\n\t" \
  "v_permlane32_swap_b32 v77, v79\n\t" \
  "v_mov_b64 v[82:83], v[80:81]\n\t" \
  "buffer_store_dwordx4 v[76:79], v110, %[rh], s94 offen sc1\n\t" \
  "s_nop 0\n\t" \
  "v_permlane16_swap_b32 v80, v82\n\t" \
  "v_permlane16_swap_b32 v81, v83\n\t" \
  "buffer_store_dwordx4 v[80:83], v111, %[rh], s94 offen sc1\n\t" \
  "s_add_i32 s77, s44, 1\n\t" \
  "s_min_u32 s77, s77, 63\n\t" \
  "v_cmp_gt_i32 vcc, s77, v119\n\t" \
  "s_cmp_lg_u64 vcc, 0\n\t" \
  "s_cselect_b32 s42, 32, 0\n\t" \
  "s_add_i32 s44, s44, 1\n\t" \
  "s_and_b32 s58, s58, 4\n\t" \
  "s_and_b32 s76, s44, 64\n\t" \
  "s_or_b32 s42, s42, s76\n\t" \
  "s_cbranch_scc0 20b\n\t" \
  "s_branch 99f\n\t" \
  "14:\n\t" \
  "s_sleep 3\n\t" \
  "s_add_u32 s59, s59, 0x10000\n\t" \
  "s_cmp_lt_u32 s59, 0x100000\n\t" \
  "s_cbranch_scc1 13b\n\t" \
  "s_mov_b64 exec, -1\n\t" \
  "s_mov_b32 s42, 16\n\t" \
  "s_branch 99f\n\t" \
  "8:\n\t" \
  "s_bitcmp1_b32 s58, 1\n\t" \
  "s_cbranch_scc1 4b\n\t" \
  "s_bitcmp1_b32 s58, 2\n\t" \
  "s_cbranch_scc1 5b\n\t" \
  "v_max3_u32 v116, v32, v33, v34\n\t" \
  "v_max3_u32 v117, v35, v36, v37\n\t" \
  "v_max3_u32 v116, v116, v38, v39\n\t" \
  "v_max_u32 v116, v116, v117\n\t" \
  "v_cmp_eq_u32 vcc, -1, v116\n\t" \
  "s_cbranch_vccz 5b\n\t" \
  "s_branch 0f\n\t" \
  "9:\n\t" \
  "s_bitcmp1_b32 s58, 2\n\t" \
  "s_cbranch_scc1 6b\n\t" \
  "v_max3_u32 v116, v40, v41, v42\n\t" \
  "v_max3_u32 v117, v43, v44, v45\n\t" \
  "v_max3_u32 v116, v116, v46, v47\n\t" \
  "v_max_u32 v116, v116, v117\n\t" \
  "v_cmp_eq_u32 vcc, -1, v116\n\t" \
  "s_cbranch_vccz 6b\n\t" \
  "s_or_b32 s58, s58, 2\n\t" \
  "0:\n\t" \
  "s_or_b32 s58, s58, 1\n\t" \
  "11:\n\t" \
  "buffer_load_dwordx4 v[32:35], v100, %[rh], s40 offen sc1\n\t" \
  "buffer_load_dwordx4 v[36:39], v100, %[rh], s76 offen sc1\n\t" \
  "buffer_load_dwordx4 v[40:43], v100, %[rh], s77 offen sc1\n\t" \
  "buffer_load_dwordx4 v[44:47], v100, %[rh], s78 offen sc1\n\t" \
  "s_waitcnt lgkmcnt(0)\n\t" \
  "s_branch 10b\n\t" \
  "17:\n\t" \
  "s_add_u32 s59, s59, 1\n\t" \
  "s_bitcmp1_b32 s58, 2\n\t" \
  "s_cbranch_scc1 3b\n\t" \
  "s_waitcnt vmcnt(0)\n\t" \
  "v_max_u32 v116, v32, v33\n\t" \
  "v_max3_u32 v116, v116, v34, v35\n\t" \
  "v_max3_u32 v116, v116, v36, v37\n\t" \
  "v_max3_u32 v116, v116, v38, v39\n\t" \
  "v_max3_u32 v116, v116, v40, v41\n\t" \
  "v_max3_u32 v116, v116, v42, v43\n\t" \
  "v_max3_u32 v116, v116, v44, v45\n\t" \
  "v_max3_u32 v116, v116, v46, v47\n\t" \
  "v_cmp_eq_u32 vcc, -1, v116\n\t" \
  "s_cbranch_vccz 3b\n\t" \
  "s_bitcmp1_b32 s59, 6\n\t" \
  "s_cbranch_scc1 12f\n\t" \
  "buffer_load_dwordx4 v[32:35], v100, %[rh], s40 offen sc1\n\t" \
  "buffer_load_dwordx4 v[36:39], v100, %[rh], s76 offen sc1\n\t" \
  "buffer_load_dwordx4 v[40:43], v100, %[rh], s77 offen sc1\n\t" \
  "buffer_load_dwordx4 v[44:47], v100, %[rh], s78 offen sc1\n\t" \
  "s_branch 17b\n\t" \
  "12:\n\t" \
  "s_mov_b32 s42, s58\n\t" \
  "s_branch 99f\n\t" \
  "93:\n\t" \
  "s_mov_b32 s58, s42\n\t" \
  "20:\n\t" \
  "s_and_b32 s76, s44, 7\n\t" \
  "s_lshl_b32 s76, s76, 15\n\t" \
  "s_add_i32 s40, s54, s76\n\t" \
  "s_and_b32 s76, s44, 3\n\t" \
  "s_lshl_b32 s45, s76, 14\n\t" \
  "s_add_i32 s47, s44, 1\n\t" \
  "s_min_u32 s47, s47, 63\n\t" \
  "s_lshr_b32 s76, s47, 1\n\t" \
  "s_lshl_b32 s47, s47, 20\n\t" \
  "s_add_i32 s47, s47, s55\n\t" \
  "s_bitcmp1_b64 %[sel], s76\n\t" \
  "s_cselect_b32 s76, 0x2000, 0\n\t" \
  "s_add_i32 s47, s47, s76\n\t" \
  "s_add_i32 s49, s44, -1\n\t" \
  "s_and_b32 s48, s49, 2\n\t" \
  "s_lshl_b32 s48, s48, 13\n\t" \
  "s_and_b32 s76, s49, 1\n\t" \
  "s_lshl_b32 s76, s76, 12\n\t" \
  "s_add_i32 s48, s48, s76\n\t" \
  "s_lshl_b32 s50, s53, 8\n\t" \
  "s_mov_b32 s59, 0\n\t" \
  "s_mov_b32 s92, s51\n\t" \
  "s_mov_b32 s93, s53\n\t" \
  "s_xor_b32 s76, s40, 0x400\n\t" \
  "s_xor_b32 s77, s40, 0x800\n\t" \
  "s_xor_b32 s78, s40, 0xc00\n\t" \
  "s_cmp_lg_u32 s58, 0\n\t" \
  "s_cbranch_scc1 11f\n\t" \
  "10:\n\t" \
  "ds_read_b128 v[48:51], v103 offset:32768\n\t" \
  "ds_read_b128 v[52:55], v103 offset:33792\n\t" \
  "ds_read_b128 v[56:59], v103 offset:34816\n\t" \
  "ds_read_b128 v[60:63], v103 offset:35840\n\t" \
  "ds_read_b128 v[16:19], v103 offset:49152\n\t" \
  "ds_read_b128 v[20:23], v103 offset:50176\n\t" \
  "ds_read_b128 v[24:27], v103 offset:51200\n\t" \
  "ds_read_b128 v[28:31], v103 offset:52224\n\t" \
  "ds_read_b128 v[84:87], v102 offset:16384\n\t" \
  "ds_read_b128 v[88:91], v102 offset:17408\n\t" \
  "ds_read_b128 v[92:95], v102 offset:18432\n\t" \
  "ds_read_b128 v[96:99], v102 offset:19456\n\t" \
  "s_waitcnt lgkmcnt(4)\n\t" \
  "ds_read_b128 v[0:3], v102 offset:49152\n\t" \
  "ds_read_b128 v[4:7], v102 offset:50176\n\t" \
  "ds_read_b128 v[8:11], v102 offset:51200\n\t" \
  "ds_read_b128 v[12:15], v102 offset:52224\n\t" \
  "46:\n\t" \
  "s_bitcmp1_b32 s58, 1\n\t" \
  "s_cselect_b64 s[80:81], -1, 0\n\t" \
  "s_cmp_lg_u32 s58, 0\n\t" \
  "s_cbranch_scc1 17f\n\t" \
  "s_mov_b32 exec_hi, 0\n\t" \
  "16:\n\t" \
  "s_memtime vcc\n\t" \
  "s_waitcnt lgkmcnt(0)\n\t" \
  "s_sub_i32 s79, s41, vcc_lo\n\t" \
  "s_cmp_gt_i32 s79, 64\n\t" \
  "s_cbranch_scc1 16b\n\t" \
  "13:\n\t" \
  "buffer_load_dword v116, v112, %[rh], s48 offen sc1\n\t" \
  "s_waitcnt vmcnt(0)\n\t" \
  "15:\n\t" \
  "v_mov_b32 v117, 20000\n\t" \
  "v_cmp_ne_u32_sdwa vcc, v116, s49 src0_sel:BYTE_0 src1_sel:DWORD\n\t" \
  "v_subrev_u32 v116, s50, v116\n\t" \
  "v_ashrrev_i32 v116, 8, v116\n\t" \
  "s_nop 1\n\t" \
  "v_max_i32_dpp v116, v116, v116 quad_perm:[1,0,3,2] row_mask:0xf bank_mask:0xf\n\t" \
  "s_cbranch_vccnz 14f\n\t" \
  "s_nop 0\n\t" \
  "v_max_i32_dpp v116, v116, v116 quad_perm:[2,3,0,1] row_mask:0xf bank_mask:0xf\n\t" \
  "s_nop 1\n\t" \
  "v_max_i32_dpp v116, v116, v116 row_half_mirror row_mask:0xf bank_mask:0xf\n\t" \
  "s_nop 1\n\t" \
  "v_max_i32_dpp v116, v116, v116 row_mirror row_mask:0xf bank_mask:0xf\n\t" \
  "v_med3_i32 v116, v116, 0, v117\n\t" \
  "v_mul_u32_u24 v116, s52, v116\n\t" \
  "v_lshrrev_b32 v116, 16, v116\n\t" \
  "s_mov_b64 exec, -1\n\t" \
  "v_readfirstlane_b32 s84, v116\n\t" \
  "v_readlane_b32 s85, v116, 16\n\t" \
  "s_max_u32 s84, s84, s85\n\t" \
  "s_add_i32 s84, s84, s51\n\t" \
  "1:\n\t" \
  "s_memtime vcc\n\t" \
  "s_waitcnt lgkmcnt(0)\n\t" \
  "s_sub_i32 s79, s84, vcc_lo\n\t" \
  "s_cmp_gt_i32 s79, 128\n\t" \
  "s_cbranch_scc1 1b\n\t" \
  ".p2align 4\n\t" \
  "2:\n\t" \
  "s_sub_i32 s79, s79, 33\n\t" \
  "s_cmp_gt_i32 s79, 56\n\t" \
  "s_cbranch_scc1 2b\n\t" \
  "buffer_load_dwordx4 v[32:35], v100, %[rh], s40 offen sc1\n\t" \
  "s_nop 12\n\t" \
  "buffer_load_dwordx4 v[36:39], v100, %[rh], s76 offen sc1\n\t" \
  "s_nop 12\n\t" \
  "buffer_load_dwordx4 v[40:43], v100, %[rh], s77 offen sc1\n\t" \
  "s_nop 12\n\t" \
  "buffer_load_dwordx4 v[44:47], v100, %[rh], s78 offen sc1\n\t" \
  "s_memtime vcc\n\t" \
  "s_memrealtime s[82:83]\n\t" \
  "s_nop 4\n\t" \
  "s_memrealtime s[84:85]\n\t" \
  "s_nop 4\n\t" \
  "s_memrealtime s[56:57]\n\t" \
  "s_nop 4\n\t" \
  "s_memrealtime s[86:87]\n\t" \
  "s_nop 4\n\t" \
  "s_memrealtime s[88:89]\n\t" \
  "s_nop 4\n\t" \
  "s_memrealtime s[90:91]\n\t" \
  "s_mov_b32 s92, 0\n\t" \
  "s_waitcnt lgkmcnt(0)\n\t" \
  "s_cmp_eq_u32 s82, s84\n\t" \
  "s_addc_u32 s92, s92, 0\n\t" \
  "s_cmp_eq_u32 s82, s56\n\t" \
  "s_addc_u32 s92, s92, 0\n\t" \
  "s_cmp_eq_u32 s82, s86\n\t" \
  "s_addc_u32 s92, s92, 0\n\t" \
  "s_cmp_eq_u32 s82, s88\n\t" \
  "s_addc_u32 s92, s92, 0\n\t" \
  "s_cmp_eq_u32 s82, s90\n\t" \
  "s_addc_u32 s92, s92, 0\n\t" \
  "s_mul_i32 s92, s92, 24\n\t" \
  "s_add_i32 s92, s92, vcc_lo\n\t" \
  "s_lshl4_add_u32 s93, s82, 64\n\t" \
  "3:\n\t" \
  "v_mov_b64 v[68:69], 0\n\t" \
  "v_mov_b64 v[70:71], 0\n\t" \
  "v_mov_b64 v[72:73], 0\n\t" \
  "v_mov_b64 v[74:75], 0\n\t" \
  "v_mov_b64 v[76:77], 0\n\t" \
  "v_mov_b64 v[78:79], 0\n\t" \
  "v_mov_b64 v[80:81], 0\n\t" \
  "v_mov_b64 v[82:83], 0\n\t" \
  "s_waitcnt lgkmcnt(0)\n\t" \
  "s_waitcnt vmcnt(3)\n\t" \
  "v_smfmac_f32_16x16x64_bf16 v[68:71], v[32:35], v[152:159], v104\n\t" \
  "ds_write_b128 v101, v[32:35] offset:12288\n\t" \
  "v_smfmac_f32_16x16x64_bf16 v[72:75], v[32:35], v[160:167], v104\n\t" \
  "v_smfmac_f32_16x16x64_bf16 v[76:79], v[32:35], v[48:55], v104\n\t" \
  "v_smfmac_f32_16x16x64_bf16 v[80:83], v[32:35], v[56:63], v104\n\t" \
  "s_waitcnt vmcnt(2)\n\t" \
  "v_smfmac_f32_16x16x64_bf16 v[68:71], v[36:39], v[168:175], v104\n\t" \
  "ds_write_b128 v101, v[36:39] offset:13312\n\t" \
  "v_smfmac_f32_16x16x64_bf16 v[72:75], v[36:39], v[176:183], v104\n\t" \
  "v_smfmac_f32_16x16x64_bf16 v[76:79], v[36:39], v[184:191], v104\n\t" \
  "v_cmp_u_f32 vcc, v68, v69\n\t" \
  "v_smfmac_f32_16x16x64_bf16 v[80:83], v[36:39], v[192:199], v104\n\t" \
  "v_cmp_u_f32 s[82:83], v70, v71\n\t" \
  "s_waitcnt vmcnt(1)\n\t" \
  "v_smfmac_f32_16x16x64_bf16 v[68:71], v[40:43], v[16:23], v104\n\t" \
  "ds_write_b128 v101, v[40:43] offset:14336\n\t" \
  "v_smfmac_f32_16x16x64_bf16 v[72:75], v[40:43], v[24:31], v104\n\t" \
  "s_or_b64 vcc, vcc, s[82:83]\n\t" \
  "s_or_b64 vcc, vcc, s[80:81]\n\t" \
  "s_waitcnt lgkmcnt(1)\n\t" \
  "v_smfmac_f32_16x16x64_bf16 v[76:79], v[40:43], v[200:207], v104\n\t" \
  "s_cbranch_scc1 8f\n\t" \
  "5:\n\t" \
  "s_barrier\n\t" \
  "4:\n\t" \
  "s_memtime s[56:57]\n\t" \
  "ds_read_b128 v[48:51], v101 offset:0\n\t" \
  "v_smfmac_f32_16x16x64_bf16 v[80:83], v[40:43], v[208:215], v104\n\t" \
  "ds_read_b128 v[52:55], v101 offset:1024\n\t" \
  "ds_read_b128 v[56:59], v101 offset:4096\n\t" \
  "s_waitcnt vmcnt(0)\n\t" \
  "v_smfmac_f32_16x16x64_bf16 v[68:71], v[44:47], v[216:223], v104\n\t" \
  "v_smfmac_f32_16x16x64_bf16 v[72:75], v[44:47], v[224:231], v104\n\t" \
  "ds_write_b128 v101, v[44:47] offset:15360\n\t" \
  "v_smfmac_f32_16x16x64_bf16 v[76:79], v[44:47], v[232:239], v104\n\t" \
  "v_cmp_u_f32 vcc, v68, v69\n\t" \
  "v_smfmac_f32_16x16x64_bf16 v[80:83], v[44:47], v[240:247], v104\n\t" \
  "v_cmp_u_f32 s[82:83], v70, v71\n\t" \
  "s_waitcnt lgkmcnt(3)\n\t" \
  "v_smfmac_f32_16x16x64_bf16 v[68:71], v[48:51], a[0:7], v104\n\t" \
  "ds_read_b128 v[60:63], v101 offset:5120\n\t" \
  "ds_read_b128 v[64:67], v101 offset:8192\n\t" \
  "v_smfmac_f32_16x16x64_bf16 v[72:75], v[48:51], a[8:15], v104\n\t" \
  "s_or_b64 vcc, vcc, s[82:83]\n\t" \
  "s_waitcnt lgkmcnt(2)\n\t" \
  "v_smfmac_f32_16x16x64_bf16 v[68:71], v[52:55], a[16:23], v104\n\t" \
  "s_cbranch_scc1 9f\n\t" \
  "6:\n\t" \
  "s_barrier\n\t" \
  "v_smfmac_f32_16x16x64_bf16 v[72:75], v[52:55], a[24:31], v104\n\t" \
  "ds_read_b128 v[48:51], v101 offset:9216\n\t" \
  "v_smfmac_f32_16x16x64_bf16 v[68:71], v[56:59], a[96:103], v104\n\t" \
  "ds_read_b128 v[52:55], v101 offset:2048\n\t" \
  "v_smfmac_f32_16x16x64_bf16 v[72:75], v[56:59], a[104:111], v104\n\t" \
  "s_waitcnt lgkmcnt(3)\n\t" \
  "v_smfmac_f32_16x16x64_bf16 v[68:71], v[60:63], a[112:119], v104\n\t" \
  "ds_read_b128 v[56:59], v101 offset:3072\n\t" \
  "v_smfmac_f32_16x16x64_bf16 v[72:75], v[60:63], a[120:127], v104\n\t" \
  "s_waitcnt lgkmcnt(3)\n\t" \
  "v_smfmac_f32_16x16x64_bf16 v[68:71], v[64:67], a[192:199], v104\n\t" \
  "ds_read_b128 v[60:63], v101 offset:6144\n\t" \
  "v_smfmac_f32_16x16x64_bf16 v[72:75], v[64:67], a[200:207], v104\n\t" \
  "s_waitcnt lgkmcnt(3)\n\t" \
  "v_smfmac_f32_16x16x64_bf16 v[68:71], v[48:51], a[208:215], v104\n\t" \
  "ds_read_b128 v[64:67], v101 offset:7168\n\t" \
  "ds_read_b128 v[16:19], v103 offset:16384\n\t" \
  "v_smfmac_f32_16x16x64_bf16 v[72:75], v[48:51], a[216:223], v104\n\t" \
  "s_waitcnt lgkmcnt(4)\n\t" \
  "v_smfmac_f32_16x16x64_bf16 v[68:71], v[52:55], v[84:91], v104\n\t" \
  "ds_read_b128 v[48:51], v101 offset:11264\n\t" \
  "ds_read_b128 v[20:23], v103 offset:17408\n\t" \
  "v_smfmac_f32_16x16x64_bf16 v[72:75], v[52:55], v[92:99], v104\n\t" \
  "s_waitcnt lgkmcnt(5)\n\t" \
  "v_smfmac_f32_16x16x64_bf16 v[68:71], v[56:59], a[64:71], v104\n\t" \
  "ds_read_b128 v[52:55], v101 offset:10240\n\t" \
  "ds_read_b128 v[24:27], v103 offset:18432\n\t" \
  "v_smfmac_f32_16x16x64_bf16 v[72:75], v[56:59], a[72:79], v104\n\t" \
  "ds_read_b128 v[28:31], v103 offset:19456\n\t" \
  "s_waitcnt lgkmcnt(7)\n\t" \
  "v_smfmac_f32_16x16x64_bf16 v[68:71], v[60:63], v[0:7], v104\n\t" \
  "ds_read_b128 v[84:87], v103 offset:22528\n\t" \
  "v_smfmac_f32_16x16x64_bf16 v[72:75], v[60:63], v[8:15], v104\n\t" \
  "ds_read_b128 v[88:91], v103 offset:23552\n\t" \
  "s_waitcnt lgkmcnt(8)\n\t" \
  "v_smfmac_f32_16x16x64_bf16 v[68:71], v[64:67], a[160:167], v104\n\t" \
  "ds_read_b128 v[0:3], v102 offset:0\n\t" \
  "v_smfmac_f32_16x16x64_bf16 v[72:75], v[64:67], a[168:175], v104\n\t" \
  "ds_read_b128 v[4:7], v102 offset:1024\n\t" \
  "s_waitcnt lgkmcnt(8)\n\t" \
  "v_smfmac_f32_16x16x64_bf16 v[68:71], v[48:51], v[120:127], v104\n\t" \
  "ds_read_b128 v[92:95], v102 offset:22528\n\t" \
  "s_waitcnt lgkmcnt(7)\n\t" \
  "v_smfmac_f32_16x16x64_bf16 v[68:71], v[52:55], v[16:23], v104\n\t" \
  "ds_read_b128 v[8:11], v102 offset:2048\n\t" \
  "v_smfmac_f32_16x16x64_bf16 v[72:75], v[48:51], v[128:135], v104\n\t" \
  "ds_read_b128 v[12:15], v102 offset:3072\n\t" \
  "s_waitcnt lgkmcnt(7)\n\t" \
  "v_smfmac_f32_16x16x64_bf16 v[72:75], v[52:55], v[24:31], v104\n\t" \
  "v_add_f32 v44, v68, v70\n\t" \
  "ds_read_b128 v[96:99], v102 offset:23552\n\t" \
  "v_smfmac_f32_16x16x64_bf16 v[76:79], v[56:59], a[80:87], v104\n\t" \
  "v_add_f32 v45, v69, v71\n\t" \
  "v_cvt_pk_bf16_f32 v44, v44, v45\n\t" \
  "v_smfmac_f32_16x16x64_bf16 v[80:83], v[56:59], a[88:95], v104\n\t" \
  "v_add_f32 v46, v72, v74\n\t" \
  "v_add_f32 v47, v73, v75\n\t" \
  "v_smfmac_f32_16x16x64_bf16 v[76:79], v[60:63], a[144:151], v104\n\t" \
  "v_cvt_pk_bf16_f32 v46, v46, v47\n\t" \
  "ds_write2st64_b32 v105, v44, v46 offset1:1\n\t" \
  "v_smfmac_f32_16x16x64_bf16 v[80:83], v[60:63], a[152:159], v104\n\t" \
  "ds_read_b128 v[56:59], v101 offset:0\n\t" \
  "v_mov_b64 v[68:69], 0\n\t" \
  "v_smfmac_f32_16x16x64_bf16 v[76:79], v[64:67], a[176:183], v104\n\t" \
  "ds_read_b128 v[16:19], v102 offset:32768\n\t" \
  "v_mov_b64 v[70:71], 0\n\t" \
  "v_smfmac_f32_16x16x64_bf16 v[80:83], v[64:67], a[184:191], v104\n\t" \
  "ds_read_b128 v[20:23], v102 offset:33792\n\t" \
  "v_smfmac_f32_16x16x64_bf16 v[76:79], v[48:51], v[136:143], v104\n\t" \
  "s_waitcnt lgkmcnt(3)\n\t" \
  "s_barrier\n\t" \
  "v_smfmac_f32_16x16x64_bf16 v[80:83], v[48:51], v[144:151], v104\n\t" \
  "v_smfmac_f32_16x16x64_bf16 v[68:71], v[32:35], v[84:91], v104\n\t" \
  "v_smfmac_f32_16x16x64_bf16 v[76:79], v[52:55], a[240:247], v104\n\t" \
  "ds_read_b128 v[60:63], v101 offset:9216\n\t" \
  "ds_read_b128 v[24:27], v102 offset:34816\n\t" \
  "v_smfmac_f32_16x16x64_bf16 v[80:83], v[52:55], a[248:255], v104\n\t" \
  "ds_read_b128 v[64:67], v101 offset:2048\n\t" \
  "global_load_dword v119, v109, %[kc] sc1\n\t" \
  "s_waitcnt lgkmcnt(5)\n\t" \
  "v_smfmac_f32_16x16x64_bf16 v[76:79], v[56:59], v[0:7], v104\n\t" \
  "ds_read_b128 v[48:51], v101 offset:4096\n\t" \
  "ds_read_b128 v[28:31], v102 offset:35840\n\t" \
  "v_smfmac_f32_16x16x64_bf16 v[80:83], v[56:59], v[8:15], v104\n\t" \
  "ds_read_b128 v[84:87], v102 offset:55296\n\t" \
  "s_waitcnt lgkmcnt(5)\n\t" \
  "v_smfmac_f32_16x16x64_bf16 v[76:79], v[60:63], a[224:231], v104\n\t" \
  "ds_read_b128 v[52:55], v101 offset:1024\n\t" \
  "ds_read_b128 v[88:91], v102 offset:56320\n\t" \
  "v_smfmac_f32_16x16x64_bf16 v[80:83], v[60:63], a[232:239], v104\n\t" \
  "ds_read_b128 v[0:3], v103 offset:0\n\t" \
  "buffer_load_dwordx4 v[32:35], v100, %[ru], s47 offen offset:0 sc1\n\t" \
  "s_waitcnt lgkmcnt(6)\n\t" \
  "v_smfmac_f32_16x16x64_bf16 v[76:79], v[64:67], a[48:55], v104\n\t" \
  "ds_read_b128 v[56:59], v101 offset:5120\n\t" \
  "v_smfmac_f32_16x16x64_bf16 v[80:83], v[64:67], a[56:63], v104\n\t" \
  "ds_read_b128 v[60:63], v101 offset:8192\n\t" \
  "ds_read_b128 v[4:7], v103 offset:1024\n\t" \
  "s_waitcnt lgkmcnt(8)\n\t" \
  "v_smfmac_f32_16x16x64_bf16 v[76:79], v[48:51], v[16:23], v104\n\t" \
  "ds_read_b128 v[8:11], v103 offset:2048\n\t" \
  "buffer_load_dwordx4 v[36:39], v100, %[ru], s47 offen offset:1024 sc1\n\t" \
  "s_waitcnt lgkmcnt(8)\n\t" \
  "v_smfmac_f32_16x16x64_bf16 v[80:83], v[48:51], v[24:31], v104\n\t" \
  "ds_read_b128 v[12:15], v103 offset:3072\n\t" \
  "v_smfmac_f32_16x16x64_bf16 v[68:71], v[48:51], v[92:99], v104\n\t" \
  "s_waitcnt lgkmcnt(7)\n\t" \
  "v_smfmac_f32_16x16x64_bf16 v[76:79], v[52:55], a[32:39], v104\n\t" \
  "v_smfmac_f32_16x16x64_bf16 v[80:83], v[52:55], a[40:47], v104\n\t" \
  "s_waitcnt lgkmcnt(4)\n\t" \
  "v_smfmac_f32_16x16x64_bf16 v[76:79], v[56:59], a[128:135], v104\n\t" \
  "s_waitcnt lgkmcnt(2)\n\t" \
  "v_smfmac_f32_16x16x64_bf16 v[76:79], v[60:63], v[0:7], v104\n\t" \
  "v_smfmac_f32_16x16x64_bf16 v[68:71], v[60:63], v[84:91], v104\n\t" \
  "v_smfmac_f32_16x16x64_bf16 v[80:83], v[56:59], a[136:143], v104\n\t" \
  "s_waitcnt lgkmcnt(0)\n\t" \
  "v_smfmac_f32_16x16x64_bf16 v[80:83], v[60:63], v[8:15], v104\n\t" \
  "v_add_f32 v20, v76, v78\n\t" \
  "v_add_f32 v21, v77, v79\n\t" \
  "v_cvt_pk_bf16_f32 v20, v20, v21\n\t" \
  "ds_write_b32 v106, v20\n\t" \
  "v_add_f32 v72, v68, v70\n\t" \
  "v_add_f32 v73, v69, v71\n\t" \
  "v_cvt_pk_bf16_f32 v72, v72, v73\n\t" \
  "ds_write_b32 v106, v72 offset:11008\n\t" \
  "v_add_f32 v23, v80, v82\n\t" \
  "v_add_f32 v21, v81, v83\n\t" \
  "v_cvt_pk_bf16_f32 v21, v23, v21\n\t" \
  "ds_write_b32 v106, v21 offset:256\n\t" \
  "s_waitcnt lgkmcnt(0)\n\t" \
  "s_barrier\n\t" \
  "s_waitcnt vmcnt(0)\n\t" \
  "s_barrier\n\t" \
  "ds_write_b128 v101, v[32:35] offset:26624\n\t" \
  "ds_write_b128 v101, v[36:39] offset:27648\n\t" \
  "s_add_i32 s77, s44, 1\n\t" \
  "s_min_u32 s77, s77, 63\n\t" \
  "v_cmp_gt_i32 vcc, s77, v119\n\t" \
  "s_cmp_lg_u64 vcc, 0\n\t" \
  "s_cselect_b32 s42, 32, 0\n\t" \
  "s_add_i32 s41, s56, 1950\n\t" \
  "s_sub_i32 s76, s92, s51\n\t" \
  "s_sub_i32 s77, s93, s53\n\t" \
  "s_cmp_eq_u32 s76, 0\n\t" \
  "s_cbranch_scc1 21f\n\t" \
  "v_cvt_f32_u32 v117, s76\n\t" \
  "v_cvt_f32_u32 v116, s77\n\t" \
  "v_rcp_f32 v117, v117\n\t" \
  "s_mov_b32 s51, s92\n\t" \
  "v_mul_f32 v117, v117, v116\n\t" \
  "v_max_f32 v117, 0x3f028f5c, v117\n\t" \
  "v_min_f32 v117, 0x3ffeb852, v117\n\t" \
  "v_rcp_f32 v116, v117\n\t" \
  "v_mul_f32 v117, 0x4f000000, v117\n\t" \
  "v_mul_f32 v116, 0x47800000, v116\n\t" \
  "v_cvt_u32_f32 v117, v117\n\t" \
  "v_cvt_u32_f32 v116, v116\n\t" \
  "s_mov_b32 s53, s93\n\t" \
  "v_readfirstlane_b32 s46, v117\n\t" \
  "v_readfirstlane_b32 s52, v116\n\t" \
  "21:\n\t" \
  "v_mov_b32 v0, -1\n\t" \
  "v_mov_b32 v1, -1\n\t" \
  "v_mov_b32 v2, -1\n\t" \
  "v_mov_b32 v3, -1\n\t" \
  "s_and_b32 s76, s49, 3\n\t" \
  "s_mul_i32 s76, s76, 0xc000\n\t" \
  "s_add_i32 s76, s76, 0x40000\n\t" \
  "buffer_store_dwordx4 v[0:3], v110, %[rh], s76 offen sc1\n\t" \
  "s_add_i32 s76, s44, 1\n\t" \
  "s_and_b32 s76, s76, 3\n\t" \
  "s_lshl_b32 s76, s76, 18\n\t" \
  "s_add_i32 s77, s76, 0x10000\n\t" \
  "buffer_store_dwordx4 v[0:3], v107, %[rh], s76 offen sc1\n\t" \
  "buffer_store_dwordx4 v[0:3], v107, %[rh], s77 offen sc1\n\t" \
  "buffer_store_dwordx4 v[0:3], v111, %[rh], s76 offen sc1\n\t" \
  "s_add_i32 s44, s44, 1\n\t" \
  "s_and_b32 s58, s58, 4\n\t" \
  "s_and_b32 s76, s44, 64\n\t" \
  "s_or_b32 s42, s42, s76\n\t" \
  "s_cbranch_scc0 20b\n\t" \
  "s_branch 99f\n\t" \
  "14:\n\t" \
  "s_sleep 3\n\t" \
  "s_add_u32 s59, s59, 0x10000\n\t" \
  "s_cmp_lt_u32 s59, 0x100000\n\t" \
  "s_cbranch_scc1 13b\n\t" \
  "s_mov_b64 exec, -1\n\t" \
  "s_mov_b32 s42, 16\n\t" \
  "s_branch 99f\n\t" \
  "8:\n\t" \
  "s_bitcmp1_b32 s58, 1\n\t" \
  "s_cbranch_scc1 4b\n\t" \
  "s_bitcmp1_b32 s58, 2\n\t" \
  "s_cbranch_scc1 5b\n\t" \
  "v_max3_u32 v116, v32, v33, v34\n\t" \
  "v_max3_u32 v117, v35, v36, v37\n\t" \
  "v_max3_u32 v116, v116, v38, v39\n\t" \
  "v_max_u32 v116, v116, v117\n\t" \
  "v_cmp_eq_u32 vcc, -1, v116\n\t" \
  "s_cbranch_vccz 5b\n\t" \
  "s_branch 0f\n\t" \
  "9:\n\t" \
  "s_bitcmp1_b32 s58, 2\n\t" \
  "s_cbranch_scc1 6b\n\t" \
  "v_max3_u32 v116, v40, v41, v42\n\t" \
  "v_max3_u32 v117, v43, v44, v45\n\t" \
  "v_max3_u32 v116, v116, v46, v47\n\t" \
  "v_max_u32 v116, v116, v117\n\t" \
  "v_cmp_eq_u32 vcc, -1, v116\n\t" \
  "s_cbranch_vccz 6b\n\t" \
  "s_or_b32 s58, s58, 2\n\t" \
  "0:\n\t" \
  "s_or_b32 s58, s58, 1\n\t" \
  "11:\n\t" \
  "buffer_load_dwordx4 v[32:35], v100, %[rh], s40 offen sc1\n\t" \
  "buffer_load_dwordx4 v[36:39], v100, %[rh], s76 offen sc1\n\t" \
  "buffer_load_dwordx4 v[40:43], v100, %[rh], s77 offen sc1\n\t" \
  "buffer_load_dwordx4 v[44:47], v100, %[rh], s78 offen sc1\n\t" \
  "s_waitcnt lgkmcnt(0)\n\t" \
  "s_branch 10b\n\t" \
  "17:\n\t" \
  "s_add_u32 s59, s59, 1\n\t" \
  "s_bitcmp1_b32 s58, 2\n\t" \
  "s_cbranch_scc1 3b\n\t" \
  "s_waitcnt vmcnt(0)\n\t" \
  "v_max_u32 v116, v32, v33\n\t" \
  "v_max3_u32 v116, v116, v34, v35\n\t" \
  "v_max3_u32 v116, v116, v36, v37\n\t" \
  "v_max3_u32 v116, v116, v38, v39\n\t" \
  "v_max3_u32 v116, v116, v40, v41\n\t" \
  "v_max3_u32 v116, v116, v42, v43\n\t" \
  "v_max3_u32 v116, v116, v44, v45\n\t" \
  "v_max3_u32 v116, v116, v46, v47\n\t" \
  "v_cmp_eq_u32 vcc, -1, v116\n\t" \
  "s_cbranch_vccz 3b\n\t" \
  "s_bitcmp1_b32 s59, 6\n\t" \
  "s_cbranch_scc1 12f\n\t" \
  "buffer_load_dwordx4 v[32:35], v100, %[rh], s40 offen sc1\n\t" \
  "buffer_load_dwordx4 v[36:39], v100, %[rh], s76 offen sc1\n\t" \
  "buffer_load_dwordx4 v[40:43], v100, %[rh], s77 offen sc1\n\t" \
  "buffer_load_dwordx4 v[44:47], v100, %[rh], s78 offen sc1\n\t" \
  "s_branch 17b\n\t" \
  "12:\n\t" \
  "s_mov_b32 s42, s58\n\t" \
  "s_branch 99f\n\t" \
  "99:\n\t" \
  "s_waitcnt vmcnt(0) lgkmcnt(0)\n\t" \
  ""
#define VO_BODY \
  "s_cmp_eq_u32 %[wv], 1\n\t" \
  "s_cbranch_scc1 91f\n\t" \
  "s_cmp_eq_u32 %[wv], 2\n\t" \
  "s_cbranch_scc1 92f\n\t" \
  "s_cmp_eq_u32 %[wv], 3\n\t" \
  "s_cbranch_scc1 93f\n\t" \
  "90:\n\t" \
  "s_mov_b32 s58, s42\n\t" \
  "20:\n\t" \
  "s_and_b32 s76, s44, 7\n\t" \
  "s_lshl_b32 s76, s76, 15\n\t" \
  "s_add_i32 s40, s54, s76\n\t" \
  "s_and_b32 s76, s44, 3\n\t" \
  "s_lshl_b32 s45, s76, 14\n\t" \
  "s_lshl_b32 s47, s76, 20\n\t" \
  "s_add_i32 s47, s47, s55\n\t" \
  "s_lshr_b32 s76, s47, 21\n\t" \
  "s_bitcmp1_b64 %[sel], s76\n\t" \
  "s_cselect_b32 s76, 0x2000, 0\n\t" \
  "s_add_i32 s47, s47, s76\n\t" \
  "s_add_i32 s49, s44, -1\n\t" \
  "s_and_b32 s48, s49, 2\n\t" \
  "s_lshl_b32 s48, s48, 13\n\t" \
  "s_and_b32 s76, s49, 1\n\t" \
  "s_lshl_b32 s76, s76, 12\n\t" \
  "s_add_i32 s48, s48, s76\n\t" \
  "s_lshl_b32 s50, s53, 8\n\t" \
  "s_mov_b32 s59, 0\n\t" \
  "s_mov_b32 s92, s51\n\t" \
  "s_mov_b32 s93, s53\n\t" \
  "s_xor_b32 s76, s40, 0x400\n\t" \
  "s_xor_b32 s77, s40, 0x800\n\t" \
  "s_xor_b32 s78, s40, 0xc00\n\t" \
  "s_cmp_lg_u32 s58, 0\n\t" \
  "s_cbranch_scc1 11f\n\t" \
  "10:\n\t" \
  "ds_read_b128 v[48:51], v102 offset:0\n\t" \
  "ds_read_b128 v[52:55], v102 offset:1024\n\t" \
  "ds_read_b128 v[56:59], v102 offset:2048\n\t" \
  "ds_read_b128 v[60:63], v102 offset:3072\n\t" \
  "ds_read_b128 v[0:3], v102 offset:16384\n\t" \
  "ds_read_b128 v[4:7], v102 offset:17408\n\t" \
  "ds_read_b128 v[8:11], v102 offset:18432\n\t" \
  "ds_read_b128 v[12:15], v102 offset:19456\n\t" \
  "ds_read_b128 v[16:19], v102 offset:32768\n\t" \
  "ds_read_b128 v[20:23], v102 offset:33792\n\t" \
  "ds_read_b128 v[24:27], v102 offset:34816\n\t" \
  "ds_read_b128 v[28:31], v102 offset:35840\n\t" \
  "s_waitcnt lgkmcnt(4)\n\t" \
  "ds_read_b128 v[80:83], v102 offset:49152\n\t" \
  "ds_read_b128 v[84:87], v102 offset:50176\n\t" \
  "ds_read_b128 v[88:91], v102 offset:51200\n\t" \
  "ds_read_b128 v[92:95], v102 offset:52224\n\t" \
  "46:\n\t" \
  "s_bitcmp1_b32 s58, 1\n\t" \
  "s_cselect_b64 s[80:81], -1, 0\n\t" \
  "s_cmp_lg_u32 s58, 0\n\t" \
  "s_cbranch_scc1 17f\n\t" \
  "s_mov_b32 exec_hi, 0\n\t" \
  "16:\n\t" \
  "s_memtime vcc\n\t" \
  "s_waitcnt lgkmcnt(0)\n\t" \
  "s_sub_i32 s79, s41, vcc_lo\n\t" \
  "s_cmp_gt_i32 s79, 64\n\t" \
  "s_cbranch_scc1 16b\n\t" \
  "13:\n\t" \
  "buffer_load_dword v116, v112, %[rh], s48 offen sc1\n\t" \
  "s_waitcnt vmcnt(0)\n\t" \
  "15:\n\t" \
  "v_mov_b32 v117, 20000\n\t" \
  "v_cmp_ne_u32_sdwa vcc, v116, s49 src0_sel:BYTE_0 src1_sel:DWORD\n\t" \
  "v_subrev_u32 v116, s50, v116\n\t" \
  "v_ashrrev_i32 v116, 8, v116\n\t" \
  "s_nop 1\n\t" \
  "v_max_i32_dpp v116, v116, v116 quad_perm:[1,0,3,2] row_mask:0xf bank_mask:0xf\n\t" \
  "s_cbranch_vccnz 14f\n\t" \
  "s_nop 0\n\t" \
  "v_max_i32_dpp v116, v116, v116 quad_perm:[2,3,0,1] row_mask:0xf bank_mask:0xf\n\t" \
  "s_nop 1\n\t" \
  "v_max_i32_dpp v116, v116, v116 row_half_mirror row_mask:0xf bank_mask:0xf\n\t" \
  "s_nop 1\n\t" \
  "v_max_i32_dpp v116, v116, v116 row_mirror row_mask:0xf bank_mask:0xf\n\t" \
  "v_med3_i32 v116, v116, 0, v117\n\t" \
  "v_mul_u32_u24 v116, s52, v116\n\t" \
  "v_lshrrev_b32 v116, 16, v116\n\t" \
  "s_mov_b64 exec, -1\n\t" \
  "v_readfirstlane_b32 s84, v116\n\t" \
  "v_readlane_b32 s85, v116, 16\n\t" \
  "s_max_u32 s84, s84, s85\n\t" \
  "s_add_i32 s84, s84, s51\n\t" \
  "1:\n\t" \
  "s_memtime vcc\n\t" \
  "s_waitcnt lgkmcnt(0)\n\t" \
  "s_sub_i32 s79, s84, vcc_lo\n\t" \
  "s_cmp_gt_i32 s79, 128\n\t" \
  "s_cbranch_scc1 1b\n\t" \
  ".p2align 4\n\t" \
  "2:\n\t" \
  "s_sub_i32 s79, s79, 33\n\t" \
  "s_cmp_gt_i32 s79, 56\n\t" \
  "s_cbranch_scc1 2b\n\t" \
  "buffer_load_dwordx4 v[32:35], v100, %[rh], s40 offen sc1\n\t" \
  "s_nop 12\n\t" \
  "buffer_load_dwordx4 v[36:39], v100, %[rh], s76 offen sc1\n\t" \
  "s_nop 12\n\t" \
  "buffer_load_dwordx4 v[40:43], v100, %[rh], s77 offen sc1\n\t" \
  "s_nop 12\n\t" \
  "buffer_load_dwordx4 v[44:47], v100, %[rh], s78 offen sc1\n\t" \
  "s_memtime vcc\n\t" \
  "s_memrealtime s[82:83]\n\t" \
  "s_nop 4\n\t" \
  "s_memrealtime s[84:85]\n\t" \
  "s_nop 4\n\t" \
  "s_memrealtime s[56:57]\n\t" \
  "s_nop 4\n\t" \
  "s_memrealtime s[86:87]\n\t" \
  "s_nop 4\n\t" \
  "s_memrealtime s[88:89]\n\t" \
  "s_nop 4\n\t" \
  "s_memrealtime s[90:91]\n\t" \
  "s_mov_b32 s92, 0\n\t" \
  "s_waitcnt lgkmcnt(0)\n\t" \
  "s_cmp_eq_u32 s82, s84\n\t" \
  "s_addc_u32 s92, s92, 0\n\t" \
  "s_cmp_eq_u32 s82, s56\n\t" \
  "s_addc_u32 s92, s92, 0\n\t" \
  "s_cmp_eq_u32 s82, s86\n\t" \
  "s_addc_u32 s92, s92, 0\n\t" \
  "s_cmp_eq_u32 s82, s88\n\t" \
  "s_addc_u32 s92, s92, 0\n\t" \
  "s_cmp_eq_u32 s82, s90\n\t" \
  "s_addc_u32 s92, s92, 0\n\t" \
  "s_mul_i32 s92, s92, 24\n\t" \
  "s_add_i32 s92, s92, vcc_lo\n\t" \
  "s_lshl4_add_u32 s93, s82, 64\n\t" \
  "3:\n\t" \
  "v_mov_b64 v[72:73], 0\n\t" \
  "v_mov_b64 v[74:75], 0\n\t" \
  "v_mov_b64 v[76:77], 0\n\t" \
  "v_mov_b64 v[78:79], 0\n\t" \
  "s_waitcnt lgkmcnt(0)\n\t" \
  "s_waitcnt vmcnt(3)\n\t" \
  "v_smfmac_f32_16x16x64_bf16 v[72:75], v[32:35], v[48:55], v104\n\t" \
  "ds_write_b128 v101, v[32:35] offset:0\n\t" \
  "v_smfmac_f32_16x16x64_bf16 v[76:79], v[32:35], v[56:63], v104\n\t" \
  "s_waitcnt vmcnt(2)\n\t" \
  "ds_write_b128 v101, v[36:39] offset:1024\n\t" \
  "v_smfmac_f32_16x16x64_bf16 v[72:75], v[36:39], a[0:7], v104\n\t" \
  "v_smfmac_f32_16x16x64_bf16 v[76:79], v[36:39], a[8:15], v104\n\t" \
  "s_nop 3\n\t" \
  "v_cmp_u_f32 vcc, v72, v73\n\t" \
  "v_cmp_u_f32 s[82:83], v74, v75\n\t" \
  "s_waitcnt vmcnt(1)\n\t" \
  "v_smfmac_f32_16x16x64_bf16 v[72:75], v[40:43], a[16:23], v104\n\t" \
  "s_or_b64 vcc, vcc, s[82:83]\n\t" \
  "s_or_b64 vcc, vcc, s[80:81]\n\t" \
  "v_smfmac_f32_16x16x64_bf16 v[76:79], v[40:43], a[24:31], v104\n\t" \
  "s_waitcnt lgkmcnt(0)\n\t" \
  "s_cbranch_scc1 8f\n\t" \
  "5:\n\t" \
  "s_barrier\n\t" \
  "4:\n\t" \
  "s_memtime s[56:57]\n\t" \
  "ds_read_b128 v[48:51], v101 offset:4096\n\t" \
  "ds_read_b128 v[52:55], v101 offset:5120\n\t" \
  "ds_read_b128 v[56:59], v101 offset:9216\n\t" \
  "s_waitcnt vmcnt(0)\n\t" \
  "v_smfmac_f32_16x16x64_bf16 v[72:75], v[44:47], a[32:39], v104\n\t" \
  "ds_read_b128 v[60:63], v101 offset:8192\n\t" \
  "ds_read_b128 v[64:67], v101 offset:13312\n\t" \
  "v_smfmac_f32_16x16x64_bf16 v[76:79], v[44:47], a[40:47], v104\n\t" \
  "ds_read_b128 v[68:71], v101 offset:12288\n\t" \
  "ds_write_b128 v101, v[40:43] offset:2048\n\t" \
  "s_nop 1\n\t" \
  "v_cmp_u_f32 vcc, v72, v73\n\t" \
  "v_cmp_u_f32 s[82:83], v74, v75\n\t" \
  "s_waitcnt lgkmcnt(6)\n\t" \
  "v_smfmac_f32_16x16x64_bf16 v[72:75], v[48:51], v[0:7], v104\n\t" \
  "s_or_b64 vcc, vcc, s[82:83]\n\t" \
  "v_smfmac_f32_16x16x64_bf16 v[76:79], v[48:51], v[8:15], v104\n\t" \
  "s_cbranch_scc1 9f\n\t" \
  "6:\n\t" \
  "s_waitcnt lgkmcnt(5)\n\t" \
  "v_smfmac_f32_16x16x64_bf16 v[72:75], v[52:55], a[48:55], v104\n\t" \
  "ds_write_b128 v101, v[44:47] offset:3072\n\t" \
  "v_smfmac_f32_16x16x64_bf16 v[76:79], v[52:55], a[56:63], v104\n\t" \
  "v_mov_b64 v[40:41], 0\n\t" \
  "s_waitcnt lgkmcnt(5)\n\t" \
  "v_smfmac_f32_16x16x64_bf16 v[72:75], v[56:59], a[96:103], v104\n\t" \
  "v_mov_b64 v[42:43], 0\n\t" \
  "v_smfmac_f32_16x16x64_bf16 v[76:79], v[56:59], a[104:111], v104\n\t" \
  "s_waitcnt lgkmcnt(4)\n\t" \
  "v_smfmac_f32_16x16x64_bf16 v[72:75], v[60:63], v[16:23], v104\n\t" \
  "s_waitcnt lgkmcnt(0)\n\t" \
  "s_barrier\n\t" \
  "v_smfmac_f32_16x16x64_bf16 v[76:79], v[60:63], v[24:31], v104\n\t" \
  "ds_read_b128 v[48:51], v101 offset:6144\n\t" \
  "ds_read_b128 v[52:55], v101 offset:10240\n\t" \
  "v_smfmac_f32_16x16x64_bf16 v[72:75], v[64:67], a[144:151], v104\n\t" \
  "ds_read_b128 v[56:59], v101 offset:14336\n\t" \
  "ds_read_b128 v[60:63], v101 offset:7168\n\t" \
  "v_smfmac_f32_16x16x64_bf16 v[76:79], v[64:67], a[152:159], v104\n\t" \
  "ds_read_b128 v[32:35], v101 offset:11264\n\t" \
  "ds_read_b128 v[36:39], v101 offset:15360\n\t" \
  "v_smfmac_f32_16x16x64_bf16 v[72:75], v[68:71], v[80:87], v104\n\t" \
  "ds_read_b128 v[0:3], v103 offset:0\n\t" \
  "v_mov_b64 v[44:45], 0\n\t" \
  "v_smfmac_f32_16x16x64_bf16 v[76:79], v[68:71], v[88:95], v104\n\t" \
  "v_mov_b64 v[46:47], 0\n\t" \
  "s_waitcnt lgkmcnt(5)\n\t" \
  "v_smfmac_f32_16x16x64_bf16 v[72:75], v[48:51], a[64:71], v104\n\t" \
  "ds_read_b128 v[4:7], v103 offset:1024\n\t" \
  "v_mov_b64 v[80:81], 0\n\t" \
  "v_smfmac_f32_16x16x64_bf16 v[72:75], v[52:55], a[112:119], v104\n\t" \
  "v_mov_b64 v[82:83], 0\n\t" \
  "s_waitcnt lgkmcnt(2)\n\t" \
  "v_smfmac_f32_16x16x64_bf16 v[72:75], v[56:59], a[160:167], v104\n\t" \
  "ds_read_b128 v[8:11], v103 offset:2048\n\t" \
  "v_mov_b64 v[84:85], 0\n\t" \
  "v_smfmac_f32_16x16x64_bf16 v[72:75], v[60:63], a[80:87], v104\n\t" \
  "ds_read_b128 v[12:15], v103 offset:3072\n\t" \
  "v_mov_b64 v[86:87], 0\n\t" \
  "v_smfmac_f32_16x16x64_bf16 v[72:75], v[32:35], a[128:135], v104\n\t" \
  "ds_read_b128 v[16:19], v103 offset:16384\n\t" \
  "v_mov_b64 v[88:89], 0\n\t" \
  "v_smfmac_f32_16x16x64_bf16 v[72:75], v[36:39], a[176:183], v104\n\t" \
  "ds_read_b128 v[20:23], v103 offset:17408\n\t" \
  "v_mov_b64 v[90:91], 0\n\t" \
  "v_smfmac_f32_16x16x64_bf16 v[76:79], v[48:51], a[72:79], v104\n\t" \
  "ds_read_b128 v[24:27], v103 offset:18432\n\t" \
  "v_mov_b64 v[92:93], 0\n\t" \
  "v_smfmac_f32_16x16x64_bf16 v[76:79], v[52:55], a[120:127], v104\n\t" \
  "v_add_f32 v96, v72, v74\n\t" \
  "v_add_f32 v97, v73, v75\n\t" \
  "v_cvt_pk_bf16_f32 v96, v96, v97\n\t" \
  "ds_write_b32 v105, v96\n\t" \
  "v_smfmac_f32_16x16x64_bf16 v[76:79], v[56:59], a[168:175], v104\n\t" \
  "ds_read_b128 v[28:31], v103 offset:19456\n\t" \
  "v_mov_b64 v[94:95], 0\n\t" \
  "v_smfmac_f32_16x16x64_bf16 v[76:79], v[60:63], a[88:95], v104\n\t" \
  "v_smfmac_f32_16x16x64_bf16 v[76:79], v[32:35], a[136:143], v104\n\t" \
  "v_smfmac_f32_16x16x64_bf16 v[76:79], v[36:39], a[184:191], v104\n\t" \
  "s_waitcnt lgkmcnt(1)\n\t" \
  "s_barrier\n\t" \
  "ds_read_b64_tr_b16 v[96:97], v107 offset:0\n\t" \
  "ds_read_b64_tr_b16 v[98:99], v107 offset:64\n\t" \
  "ds_read_b64_tr_b16 v[56:57], v107 offset:1024\n\t" \
  "ds_read_b64_tr_b16 v[58:59], v107 offset:1088\n\t" \
  "s_nop 1\n\t" \
  "v_add_f32 v116, v76, v78\n\t" \
  "v_add_f32 v117, v77, v79\n\t" \
  "v_cvt_pk_bf16_f32 v116, v116, v117\n\t" \
  "ds_write_b32 v105, v116 offset:256\n\t" \
  "s_waitcnt lgkmcnt(3)\n\t" \
  "v_smfmac_f32_16x16x64_bf16 v[40:43], v[96:99], a[192:199], v106\n\t" \
  "v_smfmac_f32_16x16x64_bf16 v[44:47], v[96:99], a[224:231], v106\n\t" \
  "s_waitcnt lgkmcnt(0)\n\t" \
  "s_barrier\n\t" \
  "ds_read_b64_tr_b16 v[52:53], v107 offset:256\n\t" \
  "ds_read_b64_tr_b16 v[54:55], v107 offset:320\n\t" \
  "v_smfmac_f32_16x16x64_bf16 v[40:43], v[56:59], a[208:215], v106\n\t" \
  "ds_read_b64_tr_b16 v[60:61], v107 offset:1280\n\t" \
  "ds_read_b64_tr_b16 v[62:63], v107 offset:1344\n\t" \
  "v_smfmac_f32_16x16x64_bf16 v[44:47], v[56:59], v[0:7], v106\n\t" \
  "v_smfmac_f32_16x16x64_bf16 v[80:83], v[96:99], a[240:247], v106\n\t" \
  "v_mov_b64 v[32:33], 0\n\t" \
  "v_smfmac_f32_16x16x64_bf16 v[84:87], v[96:99], v[136:143], v106\n\t" \
  "v_mov_b64 v[34:35], 0\n\t" \
  "v_smfmac_f32_16x16x64_bf16 v[80:83], v[56:59], v[120:127], v106\n\t" \
  "v_mov_b64 v[36:37], 0\n\t" \
  "v_smfmac_f32_16x16x64_bf16 v[84:87], v[56:59], v[16:23], v106\n\t" \
  "s_waitcnt lgkmcnt(2)\n\t" \
  "v_smfmac_f32_16x16x64_bf16 v[40:43], v[52:55], a[200:207], v106\n\t" \
  "v_smfmac_f32_16x16x64_bf16 v[44:47], v[52:55], a[232:239], v106\n\t" \
  "s_xor_b32 s76, s47, 0x40000\n\t" \
  "s_waitcnt lgkmcnt(0)\n\t" \
  "v_smfmac_f32_16x16x64_bf16 v[40:43], v[60:63], a[216:223], v106\n\t" \
  "v_mov_b64 v[38:39], 0\n\t" \
  "s_xor_b32 s77, s47, 0x80000\n\t" \
  "v_smfmac_f32_16x16x64_bf16 v[44:47], v[60:63], v[8:15], v106\n\t" \
  "s_xor_b32 s78, s47, 0xc0000\n\t" \
  "v_smfmac_f32_16x16x64_bf16 v[80:83], v[52:55], a[248:255], v106\n\t" \
  "ds_read_b128 v[0:3], v103 offset:32768\n\t" \
  "v_cvt_pk_bf16_f32 v64, v40, v41\n\t" \
  "v_smfmac_f32_16x16x64_bf16 v[84:87], v[52:55], v[144:151], v106\n\t" \
  "ds_read_b128 v[4:7], v103 offset:33792\n\t" \
  "v_cvt_pk_bf16_f32 v65, v42, v43\n\t" \
  "v_smfmac_f32_16x16x64_bf16 v[80:83], v[60:63], v[128:135], v106\n\t" \
  "ds_read_b128 v[8:11], v103 offset:34816\n\t" \
  "v_cvt_pk_bf16_f32 v66, v44, v45\n\t" \
  "v_smfmac_f32_16x16x64_bf16 v[84:87], v[60:63], v[24:31], v106\n\t" \
  "ds_read_b128 v[12:15], v103 offset:35840\n\t" \
  "v_cvt_pk_bf16_f32 v67, v46, v47\n\t" \
  "v_smfmac_f32_16x16x64_bf16 v[88:91], v[96:99], v[152:159], v106\n\t" \
  "v_permlane16_swap_b32 v64, v66\n\t" \
  "v_cvt_pk_bf16_f32 v68, v80, v81\n\t" \
  "v_smfmac_f32_16x16x64_bf16 v[92:95], v[96:99], v[184:191], v106\n\t" \
  "v_permlane16_swap_b32 v65, v67\n\t" \
  "buffer_store_dwordx4 v[64:67], v108, %[ru], s47 offen sc1\n\t" \
  "v_cvt_pk_bf16_f32 v69, v82, v83\n\t" \
  "v_smfmac_f32_16x16x64_bf16 v[88:91], v[52:55], v[160:167], v106\n\t" \
  "v_cvt_pk_bf16_f32 v70, v84, v85\n\t" \
  "v_smfmac_f32_16x16x64_bf16 v[92:95], v[52:55], v[192:199], v106\n\t" \
  "v_cvt_pk_bf16_f32 v71, v86, v87\n\t" \
  "v_smfmac_f32_16x16x64_bf16 v[88:91], v[56:59], v[168:175], v106\n\t" \
  "ds_read_b128 v[16:19], v103 offset:49152\n\t" \
  "v_permlane16_swap_b32 v68, v70\n\t" \
  "s_waitcnt lgkmcnt(3)\n\t" \
  "v_smfmac_f32_16x16x64_bf16 v[92:95], v[56:59], v[0:7], v106\n\t" \
  "ds_read_b128 v[20:23], v103 offset:50176\n\t" \
  "v_permlane16_swap_b32 v69, v71\n\t" \
  "buffer_store_dwordx4 v[68:71], v108, %[ru], s76 offen sc1\n\t" \
  "v_smfmac_f32_16x16x64_bf16 v[88:91], v[60:63], v[176:183], v106\n\t" \
  "ds_read_b128 v[24:27], v103 offset:51200\n\t" \
  "s_waitcnt lgkmcnt(3)\n\t" \
  "v_smfmac_f32_16x16x64_bf16 v[92:95], v[60:63], v[8:15], v106\n\t" \
  "ds_read_b128 v[28:31], v103 offset:52224\n\t" \
  "v_smfmac_f32_16x16x64_bf16 v[32:35], v[96:99], v[200:207], v106\n\t" \
  "v_cvt_pk_bf16_f32 v72, v88, v89\n\t" \
  "v_smfmac_f32_16x16x64_bf16 v[36:39], v[96:99], v[232:239], v106\n\t" \
  "v_cvt_pk_bf16_f32 v73, v90, v91\n\t" \
  "v_smfmac_f32_16x16x64_bf16 v[32:35], v[52:55], v[208:215], v106\n\t" \
  "v_cvt_pk_bf16_f32 v74, v92, v93\n\t" \
  "v_smfmac_f32_16x16x64_bf16 v[36:39], v[52:55], v[240:247], v106\n\t" \
  "v_cvt_pk_bf16_f32 v75, v94, v95\n\t" \
  "v_smfmac_f32_16x16x64_bf16 v[32:35], v[56:59], v[216:223], v106\n\t" \
  "v_permlane16_swap_b32 v72, v74\n\t" \
  "v_smfmac_f32_16x16x64_bf16 v[32:35], v[60:63], v[224:231], v106\n\t" \
  "v_permlane16_swap_b32 v73, v75\n\t" \
  "buffer_store_dwordx4 v[72:75], v108, %[ru], s77 offen sc1\n\t" \
  "s_waitcnt lgkmcnt(2)\n\t" \
  "v_smfmac_f32_16x16x64_bf16 v[36:39], v[56:59], v[16:23], v106\n\t" \
  "s_waitcnt lgkmcnt(0)\n\t" \
  "v_smfmac_f32_16x16x64_bf16 v[36:39], v[60:63], v[24:31], v106\n\t" \
  "v_cvt_pk_bf16_f32 v76, v32, v33\n\t" \
  "v_cvt_pk_bf16_f32 v77, v34, v35\n\t" \
  "s_nop 5\n\t" \
  "v_cvt_pk_bf16_f32 v78, v36, v37\n\t" \
  "v_cvt_pk_bf16_f32 v79, v38, v39\n\t" \
  "s_nop 0\n\t" \
  "v_permlane16_swap_b32 v76, v78\n\t" \
  "v_permlane16_swap_b32 v77, v79\n\t" \
  "buffer_store_dwordx4 v[76:79], v108, %[ru], s78 offen sc1\n\t" \
  "s_memtime s[76:77]\n\t" \
  "s_mov_b32 s42, 0\n\t" \
  "s_waitcnt lgkmcnt(0)\n\t" \
  "s_mov_b32 s57, s76\n\t" \
  "s_add_i32 s41, s56, 1950\n\t" \
  "s_sub_i32 s76, s92, s51\n\t" \
  "s_sub_i32 s77, s93, s53\n\t" \
  "s_cmp_eq_u32 s76, 0\n\t" \
  "s_cbranch_scc1 21f\n\t" \
  "v_cvt_f32_u32 v117, s76\n\t" \
  "v_cvt_f32_u32 v116, s77\n\t" \
  "v_rcp_f32 v117, v117\n\t" \
  "s_mov_b32 s51, s92\n\t" \
  "v_mul_f32 v117, v117, v116\n\t" \
  "v_max_f32 v117, 0x3f028f5c, v117\n\t" \
  "v_min_f32 v117, 0x3ffeb852, v117\n\t" \
  "v_rcp_f32 v116, v117\n\t" \
  "v_mul_f32 v117, 0x4f000000, v117\n\t" \
  "v_mul_f32 v116, 0x47800000, v116\n\t" \
  "v_cvt_u32_f32 v117, v117\n\t" \
  "v_cvt_u32_f32 v116, v116\n\t" \
  "s_mov_b32 s53, s93\n\t" \
  "v_readfirstlane_b32 s46, v117\n\t" \
  "v_readfirstlane_b32 s52, v116\n\t" \
  "21:\n\t" \
  "s_add_i32 s44, s44, 1\n\t" \
  "s_and_b32 s58, s58, 4\n\t" \
  "s_and_b32 s76, s44, 64\n\t" \
  "s_or_b32 s42, s42, s76\n\t" \
  "s_cbranch_scc0 20b\n\t" \
  "s_branch 99f\n\t" \
  "14:\n\t" \
  "s_sleep 3\n\t" \
  "s_add_u32 s59, s59, 0x10000\n\t" \
  "s_cmp_lt_u32 s59, 0x100000\n\t" \
  "s_cbranch_scc1 13b\n\t" \
  "s_mov_b64 exec, -1\n\t" \
  "s_mov_b32 s42, 16\n\t" \
  "s_branch 99f\n\t" \
  "8:\n\t" \
  "s_bitcmp1_b32 s58, 1\n\t" \
  "s_cbranch_scc1 4b\n\t" \
  "s_bitcmp1_b32 s58, 2\n\t" \
  "s_cbranch_scc1 5b\n\t" \
  "v_max3_u32 v116, v32, v33, v34\n\t" \
  "v_max3_u32 v117, v35, v36, v37\n\t" \
  "v_max3_u32 v116, v116, v38, v39\n\t" \
  "v_max_u32 v116, v116, v117\n\t" \
  "v_cmp_eq_u32 vcc, -1, v116\n\t" \
  "s_cbranch_vccz 5b\n\t" \
  "s_branch 0f\n\t" \
  "9:\n\t" \
  "s_bitcmp1_b32 s58, 2\n\t" \
  "s_cbranch_scc1 6b\n\t" \
  "v_max3_u32 v116, v40, v41, v42\n\t" \
  "v_max3_u32 v117, v43, v44, v45\n\t" \
  "v_max3_u32 v116, v116, v46, v47\n\t" \
  "v_max_u32 v116, v116, v117\n\t" \
  "v_cmp_eq_u32 vcc, -1, v116\n\t" \
  "s_cbranch_vccz 6b\n\t" \
  "s_or_b32 s58, s58, 2\n\t" \
  "0:\n\t" \
  "s_or_b32 s58, s58, 1\n\t" \
  "11:\n\t" \
  "buffer_load_dwordx4 v[32:35], v100, %[rh], s40 offen sc1\n\t" \
  "buffer_load_dwordx4 v[36:39], v100, %[rh], s76 offen sc1\n\t" \
  "buffer_load_dwordx4 v[40:43], v100, %[rh], s77 offen sc1\n\t" \
  "buffer_load_dwordx4 v[44:47], v100, %[rh], s78 offen sc1\n\t" \
  "s_waitcnt lgkmcnt(0)\n\t" \
  "s_branch 10b\n\t" \
  "17:\n\t" \
  "s_add_u32 s59, s59, 1\n\t" \
  "s_bitcmp1_b32 s58, 2\n\t" \
  "s_cbranch_scc1 3b\n\t" \
  "s_waitcnt vmcnt(0)\n\t" \
  "v_max_u32 v116, v32, v33\n\t" \
  "v_max3_u32 v116, v116, v34, v35\n\t" \
  "v_max3_u32 v116, v116, v36, v37\n\t" \
  "v_max3_u32 v116, v116, v38, v39\n\t" \
  "v_max3_u32 v116, v116, v40, v41\n\t" \
  "v_max3_u32 v116, v116, v42, v43\n\t" \
  "v_max3_u32 v116, v116, v44, v45\n\t" \
  "v_max3_u32 v116, v116, v46, v47\n\t" \
  "v_cmp_eq_u32 vcc, -1, v116\n\t" \
  "s_cbranch_vccz 3b\n\t" \
  "s_bitcmp1_b32 s59, 6\n\t" \
  "s_cbranch_scc1 12f\n\t" \
  "buffer_load_dwordx4 v[32:35], v100, %[rh], s40 offen sc1\n\t" \
  "buffer_load_dwordx4 v[36:39], v100, %[rh], s76 offen sc1\n\t" \
  "buffer_load_dwordx4 v[40:43], v100, %[rh], s77 offen sc1\n\t" \
  "buffer_load_dwordx4 v[44:47], v100, %[rh], s78 offen sc1\n\t" \
  "s_branch 17b\n\t" \
  "12:\n\t" \
  "s_mov_b32 s42, s58\n\t" \
  "s_branch 99f\n\t" \
  "91:\n\t" \
  "s_mov_b32 s58, s42\n\t" \
  "20:\n\t" \
  "s_and_b32 s76, s44, 7\n\t" \
  "s_lshl_b32 s76, s76, 15\n\t" \
  "s_add_i32 s40, s54, s76\n\t" \
  "s_and_b32 s76, s44, 3\n\t" \
  "s_lshl_b32 s45, s76, 14\n\t" \
  "s_lshl_b32 s47, s76, 20\n\t" \
  "s_add_i32 s47, s47, s55\n\t" \
  "s_lshr_b32 s76, s47, 21\n\t" \
  "s_bitcmp1_b64 %[sel], s76\n\t" \
  "s_cselect_b32 s76, 0x2000, 0\n\t" \
  "s_add_i32 s47, s47, s76\n\t" \
  "s_add_i32 s49, s44, -1\n\t" \
  "s_and_b32 s48, s49, 2\n\t" \
  "s_lshl_b32 s48, s48, 13\n\t" \
  "s_and_b32 s76, s49, 1\n\t" \
  "s_lshl_b32 s76, s76, 12\n\t" \
  "s_add_i32 s48, s48, s76\n\t" \
  "s_lshl_b32 s50, s53, 8\n\t" \
  "s_mov_b32 s59, 0\n\t" \
  "s_mov_b32 s92, s51\n\t" \
  "s_mov_b32 s93, s53\n\t" \
  "s_xor_b32 s76, s40, 0x400\n\t" \
  "s_xor_b32 s77, s40, 0x800\n\t" \
  "s_xor_b32 s78, s40, 0xc00\n\t" \
  "s_cmp_lg_u32 s58, 0\n\t" \
  "s_cbranch_scc1 11f\n\t" \
  "10:\n\t" \
  "ds_read_b128 v[48:51], v102 offset:16384\n\t" \
  "ds_read_b128 v[52:55], v102 offset:17408\n\t" \
  "ds_read_b128 v[56:59], v102 offset:18432\n\t" \
  "ds_read_b128 v[60:63], v102 offset:19456\n\t" \
  "ds_read_b128 v[0:3], v102 offset:32768\n\t" \
  "ds_read_b128 v[4:7], v102 offset:33792\n\t" \
  "ds_read_b128 v[8:11], v102 offset:34816\n\t" \
  "ds_read_b128 v[12:15], v102 offset:35840\n\t" \
  "ds_read_b128 v[16:19], v102 offset:49152\n\t" \
  "ds_read_b128 v[20:23], v102 offset:50176\n\t" \
  "ds_read_b128 v[24:27], v102 offset:51200\n\t" \
  "ds_read_b128 v[28:31], v102 offset:52224\n\t" \
  "s_waitcnt lgkmcnt(4)\n\t" \
  "ds_read_b128 v[80:83], v102 offset:0\n\t" \
  "ds_read_b128 v[84:87], v102 offset:1024\n\t" \
  "ds_read_b128 v[88:91], v102 offset:2048\n\t" \
  "ds_read_b128 v[92:95], v102 offset:3072\n\t" \
  "46:\n\t" \
  "s_bitcmp1_b32 s58, 1\n\t" \
  "s_cselect_b64 s[80:81], -1, 0\n\t" \
  "s_cmp_lg_u32 s58, 0\n\t" \
  "s_cbranch_scc1 17f\n\t" \
  "s_mov_b32 exec_hi, 0\n\t" \
  "16:\n\t" \
  "s_memtime vcc\n\t" \
  "s_waitcnt lgkmcnt(0)\n\t" \
  "s_sub_i32 s79, s41, vcc_lo\n\t" \
  "s_cmp_gt_i32 s79, 64\n\t" \
  "s_cbranch_scc1 16b\n\t" \
  "13:\n\t" \
  "buffer_load_dword v116, v112, %[rh], s48 offen sc1\n\t" \
  "s_waitcnt vmcnt(0)\n\t" \
  "15:\n\t" \
  "v_mov_b32 v117, 20000\n\t" \
  "v_cmp_ne_u32_sdwa vcc, v116, s49 src0_sel:BYTE_0 src1_sel:DWORD\n\t" \
  "v_subrev_u32 v116, s50, v116\n\t" \
  "v_ashrrev_i32 v116, 8, v116\n\t" \
  "s_nop 1\n\t" \
  "v_max_i32_dpp v116, v116, v116 quad_perm:[1,0,3,2] row_mask:0xf bank_mask:0xf\n\t" \
  "s_cbranch_vccnz 14f\n\t" \
  "s_nop 0\n\t" \
  "v_max_i32_dpp v116, v116, v116 quad_perm:[2,3,0,1] row_mask:0xf bank_mask:0xf\n\t" \
  "s_nop 1\n\t" \
  "v_max_i32_dpp v116, v116, v116 row_half_mirror row_mask:0xf bank_mask:0xf\n\t" \
  "s_nop 1\n\t" \
  "v_max_i32_dpp v116, v116, v116 row_mirror row_mask:0xf bank_mask:0xf\n\t" \
  "v_med3_i32 v116, v116, 0, v117\n\t" \
  "v_mul_u32_u24 v116, s52, v116\n\t" \
  "v_lshrrev_b32 v116, 16, v116\n\t" \
  "s_mov_b64 exec, -1\n\t" \
  "v_readfirstlane_b32 s84, v116\n\t" \
  "v_readlane_b32 s85, v116, 16\n\t" \
  "s_max_u32 s84, s84, s85\n\t" \
  "s_add_i32 s84, s84, s51\n\t" \
  "1:\n\t" \
  "s_memtime vcc\n\t" \
  "s_waitcnt lgkmcnt(0)\n\t" \
  "s_sub_i32 s79, s84, vcc_lo\n\t" \
  "s_cmp_gt_i32 s79, 128\n\t" \
  "s_cbranch_scc1 1b\n\t" \
  ".p2align 4\n\t" \
  "2:\n\t" \
  "s_sub_i32 s79, s79, 33\n\t" \
  "s_cmp_gt_i32 s79, 56\n\t" \
  "s_cbranch_scc1 2b\n\t" \
  "buffer_load_dwordx4 v[32:35], v100, %[rh], s40 offen sc1\n\t" \
  "s_nop 12\n\t" \
  "buffer_load_dwordx4 v[36:39], v100, %[rh], s76 offen sc1\n\t" \
  "s_nop 12\n\t" \
  "buffer_load_dwordx4 v[40:43], v100, %[rh], s77 offen sc1\n\t" \
  "s_nop 12\n\t" \
  "buffer_load_dwordx4 v[44:47], v100, %[rh], s78 offen sc1\n\t" \
  "s_memtime vcc\n\t" \
  "s_memrealtime s[82:83]\n\t" \
  "s_nop 4\n\t" \
  "s_memrealtime s[84:85]\n\t" \
  "s_nop 4\n\t" \
  "s_memrealtime s[56:57]\n\t" \
  "s_nop 4\n\t" \
  "s_memrealtime s[86:87]\n\t" \
  "s_nop 4\n\t" \
  "s_memrealtime s[88:89]\n\t" \
  "s_nop 4\n\t" \
  "s_memrealtime s[90:91]\n\t" \
  "s_mov_b32 s92, 0\n\t" \
  "s_waitcnt lgkmcnt(0)\n\t" \
  "s_cmp_eq_u32 s82, s84\n\t" \
  "s_addc_u32 s92, s92, 0\n\t" \
  "s_cmp_eq_u32 s82, s56\n\t" \
  "s_addc_u32 s92, s92, 0\n\t" \
  "s_cmp_eq_u32 s82, s86\n\t" \
  "s_addc_u32 s92, s92, 0\n\t" \
  "s_cmp_eq_u32 s82, s88\n\t" \
  "s_addc_u32 s92, s92, 0\n\t" \
  "s_cmp_eq_u32 s82, s90\n\t" \
  "s_addc_u32 s92, s92, 0\n\t" \
  "s_mul_i32 s92, s92, 24\n\t" \
  "s_add_i32 s92, s92, vcc_lo\n\t" \
  "s_lshl4_add_u32 s93, s82, 64\n\t" \
  "3:\n\t" \
  "v_mov_b64 v[72:73], 0\n\t" \
  "v_mov_b64 v[74:75], 0\n\t" \
  "v_mov_b64 v[76:77], 0\n\t" \
  "v_mov_b64 v[78:79], 0\n\t" \
  "s_waitcnt lgkmcnt(0)\n\t" \
  "s_waitcnt vmcnt(3)\n\t" \
  "v_smfmac_f32_16x16x64_bf16 v[72:75], v[32:35], v[48:55], v104\n\t" \
  "ds_write_b128 v101, v[32:35] offset:4096\n\t" \
  "v_smfmac_f32_16x16x64_bf16 v[76:79], v[32:35], v[56:63], v104\n\t" \
  "s_waitcnt vmcnt(2)\n\t" \
  "ds_write_b128 v101, v[36:39] offset:5120\n\t" \
  "v_smfmac_f32_16x16x64_bf16 v[72:75], v[36:39], a[48:55], v104\n\t" \
  "v_smfmac_f32_16x16x64_bf16 v[76:79], v[36:39], a[56:63], v104\n\t" \
  "s_nop 3\n\t" \
  "v_cmp_u_f32 vcc, v72, v73\n\t" \
  "v_cmp_u_f32 s[82:83], v74, v75\n\t" \
  "s_waitcnt vmcnt(1)\n\t" \
  "v_smfmac_f32_16x16x64_bf16 v[72:75], v[40:43], a[64:71], v104\n\t" \
  "s_or_b64 vcc, vcc, s[82:83]\n\t" \
  "s_or_b64 vcc, vcc, s[80:81]\n\t" \
  "v_smfmac_f32_16x16x64_bf16 v[76:79], v[40:43], a[72:79], v104\n\t" \
  "s_waitcnt lgkmcnt(0)\n\t" \
  "s_cbranch_scc1 8f\n\t" \
  "5:\n\t" \
  "s_barrier\n\t" \
  "4:\n\t" \
  "s_memtime s[56:57]\n\t" \
  "ds_read_b128 v[48:51], v101 offset:8192\n\t" \
  "ds_read_b128 v[52:55], v101 offset:9216\n\t" \
  "ds_read_b128 v[56:59], v101 offset:13312\n\t" \
  "s_waitcnt vmcnt(0)\n\t" \
  "v_smfmac_f32_16x16x64_bf16 v[72:75], v[44:47], a[80:87], v104\n\t" \
  "ds_read_b128 v[60:63], v101 offset:12288\n\t" \
  "ds_read_b128 v[64:67], v101 offset:1024\n\t" \
  "v_smfmac_f32_16x16x64_bf16 v[76:79], v[44:47], a[88:95], v104\n\t" \
  "ds_read_b128 v[68:71], v101 offset:0\n\t" \
  "ds_write_b128 v101, v[40:43] offset:6144\n\t" \
  "s_nop 1\n\t" \
  "v_cmp_u_f32 vcc, v72, v73\n\t" \
  "v_cmp_u_f32 s[82:83], v74, v75\n\t" \
  "s_waitcnt lgkmcnt(6)\n\t" \
  "v_smfmac_f32_16x16x64_bf16 v[72:75], v[48:51], v[0:7], v104\n\t" \
  "s_or_b64 vcc, vcc, s[82:83]\n\t" \
  "v_smfmac_f32_16x16x64_bf16 v[76:79], v[48:51], v[8:15], v104\n\t" \
  "s_cbranch_scc1 9f\n\t" \
  "6:\n\t" \
  "s_waitcnt lgkmcnt(5)\n\t" \
  "v_smfmac_f32_16x16x64_bf16 v[72:75], v[52:55], a[96:103], v104\n\t" \
  "ds_write_b128 v101, v[44:47] offset:7168\n\t" \
  "v_smfmac_f32_16x16x64_bf16 v[76:79], v[52:55], a[104:111], v104\n\t" \
  "v_mov_b64 v[40:41], 0\n\t" \
  "s_waitcnt lgkmcnt(5)\n\t" \
  "v_smfmac_f32_16x16x64_bf16 v[72:75], v[56:59], a[144:151], v104\n\t" \
  "v_mov_b64 v[42:43], 0\n\t" \
  "v_smfmac_f32_16x16x64_bf16 v[76:79], v[56:59], a[152:159], v104\n\t" \
  "s_waitcnt lgkmcnt(4)\n\t" \
  "v_smfmac_f32_16x16x64_bf16 v[72:75], v[60:63], v[16:23], v104\n\t" \
  "s_waitcnt lgkmcnt(0)\n\t" \
  "s_barrier\n\t" \
  "v_smfmac_f32_16x16x64_bf16 v[76:79], v[60:63], v[24:31], v104\n\t" \
  "ds_read_b128 v[48:51], v101 offset:10240\n\t" \
  "ds_read_b128 v[52:55], v101 offset:14336\n\t" \
  "v_smfmac_f32_16x16x64_bf16 v[72:75], v[64:67], a[0:7], v104\n\t" \
  "ds_read_b128 v[56:59], v101 offset:2048\n\t" \
  "ds_read_b128 v[60:63], v101 offset:11264\n\t" \
  "v_smfmac_f32_16x16x64_bf16 v[76:79], v[64:67], a[8:15], v104\n\t" \
  "ds_read_b128 v[32:35], v101 offset:15360\n\t" \
  "ds_read_b128 v[36:39], v101 offset:3072\n\t" \
  "v_smfmac_f32_16x16x64_bf16 v[72:75], v[68:71], v[80:87], v104\n\t" \
  "ds_read_b128 v[0:3], v103 offset:0\n\t" \
  "v_mov_b64 v[44:45], 0\n\t" \
  "v_smfmac_f32_16x16x64_bf16 v[76:79], v[68:71], v[88:95], v104\n\t" \
  "v_mov_b64 v[46:47], 0\n\t" \
  "s_waitcnt lgkmcnt(5)\n\t" \
  "v_smfmac_f32_16x16x64_bf16 v[72:75], v[48:51], a[112:119], v104\n\t" \
  "ds_read_b128 v[4:7], v103 offset:1024\n\t" \
  "v_mov_b64 v[80:81], 0\n\t" \
  "v_smfmac_f32_16x16x64_bf16 v[72:75], v[52:55], a[160:167], v104\n\t" \
  "v_mov_b64 v[82:83], 0\n\t" \
  "s_waitcnt lgkmcnt(2)\n\t" \
  "v_smfmac_f32_16x16x64_bf16 v[72:75], v[56:59], a[16:23], v104\n\t" \
  "ds_read_b128 v[8:11], v103 offset:2048\n\t" \
  "v_mov_b64 v[84:85], 0\n\t" \
  "v_smfmac_f32_16x16x64_bf16 v[72:75], v[60:63], a[128:135], v104\n\t" \
  "ds_read_b128 v[12:15], v103 offset:3072\n\t" \
  "v_mov_b64 v[86:87], 0\n\t" \
  "v_smfmac_f32_16x16x64_bf16 v[72:75], v[32:35], a[176:183], v104\n\t" \
  "ds_read_b128 v[16:19], v103 offset:16384\n\t" \
  "v_mov_b64 v[88:89], 0\n\t" \
  "v_smfmac_f32_16x16x64_bf16 v[72:75], v[36:39], a[32:39], v104\n\t" \
  "ds_read_b128 v[20:23], v103 offset:17408\n\t" \
  "v_mov_b64 v[90:91], 0\n\t" \
  "v_smfmac_f32_16x16x64_bf16 v[76:79], v[48:51], a[120:127], v104\n\t" \
  "ds_read_b128 v[24:27], v103 offset:18432\n\t" \
  "v_mov_b64 v[92:93], 0\n\t" \
  "v_smfmac_f32_16x16x64_bf16 v[76:79], v[52:55], a[168:175], v104\n\t" \
  "v_add_f32 v96, v72, v74\n\t" \
  "v_add_f32 v97, v73, v75\n\t" \
  "v_cvt_pk_bf16_f32 v96, v96, v97\n\t" \
  "ds_write_b32 v105, v96\n\t" \
  "v_smfmac_f32_16x16x64_bf16 v[76:79], v[56:59], a[24:31], v104\n\t" \
  "ds_read_b128 v[28:31], v103 offset:19456\n\t" \
  "v_mov_b64 v[94:95], 0\n\t" \
  "v_smfmac_f32_16x16x64_bf16 v[76:79], v[60:63], a[136:143], v104\n\t" \
  "v_smfmac_f32_16x16x64_bf16 v[76:79], v[32:35], a[184:191], v104\n\t" \
  "v_smfmac_f32_16x16x64_bf16 v[76:79], v[36:39], a[40:47], v104\n\t" \
  "s_waitcnt lgkmcnt(1)\n\t" \
  "s_barrier\n\t" \
  "ds_read_b64_tr_b16 v[96:97], v107 offset:0\n\t" \
  "ds_read_b64_tr_b16 v[98:99], v107 offset:64\n\t" \
  "ds_read_b64_tr_b16 v[56:57], v107 offset:1024\n\t" \
  "ds_read_b64_tr_b16 v[58:59], v107 offset:1088\n\t" \
  "s_nop 1\n\t" \
  "v_add_f32 v116, v76, v78\n\t" \
  "v_add_f32 v117, v77, v79\n\t" \
  "v_cvt_pk_bf16_f32 v116, v116, v117\n\t" \
  "ds_write_b32 v105, v116 offset:256\n\t" \
  "s_waitcnt lgkmcnt(3)\n\t" \
  "v_smfmac_f32_16x16x64_bf16 v[40:43], v[96:99], a[192:199], v106\n\t" \
  "v_smfmac_f32_16x16x64_bf16 v[44:47], v[96:99], a[224:231], v106\n\t" \
  "s_waitcnt lgkmcnt(0)\n\t" \
  "s_barrier\n\t" \
  "ds_read_b64_tr_b16 v[52:53], v107 offset:256\n\t" \
  "ds_read_b64_tr_b16 v[54:55], v107 offset:320\n\t" \
  "v_smfmac_f32_16x16x64_bf16 v[40:43], v[56:59], a[208:215], v106\n\t" \
  "ds_read_b64_tr_b16 v[60:61], v107 offset:1280\n\t" \
  "ds_read_b64_tr_b16 v[62:63], v107 offset:1344\n\t" \
  "v_smfmac_f32_16x16x64_bf16 v[44:47], v[56:59], v[0:7], v106\n\t" \
  "v_smfmac_f32_16x16x64_bf16 v[80:83], v[96:99], a[240:247], v106\n\t" \
  "v_mov_b64 v[32:33], 0\n\t" \
  "v_smfmac_f32_16x16x64_bf16 v[84:87], v[96:99], v[136:143], v106\n\t" \
  "v_mov_b64 v[34:35], 0\n\t" \
  "v_smfmac_f32_16x16x64_bf16 v[80:83], v[56:59], v[120:127], v106\n\t" \
  "v_mov_b64 v[36:37], 0\n\t" \
  "v_smfmac_f32_16x16x64_bf16 v[84:87], v[56:59], v[16:23], v106\n\t" \
  "s_waitcnt lgkmcnt(2)\n\t" \
  "v_smfmac_f32_16x16x64_bf16 v[40:43], v[52:55], a[200:207], v106\n\t" \
  "v_smfmac_f32_16x16x64_bf16 v[44:47], v[52:55], a[232:239], v106\n\t" \
  "s_xor_b32 s76, s47, 0x40000\n\t" \
  "s_waitcnt lgkmcnt(0)\n\t" \
  "v_smfmac_f32_16x16x64_bf16 v[40:43], v[60:63], a[216:223], v106\n\t" \
  "v_mov_b64 v[38:39], 0\n\t" \
  "s_xor_b32 s77, s47, 0x80000\n\t" \
  "v_smfmac_f32_16x16x64_bf16 v[44:47], v[60:63], v[8:15], v106\n\t" \
  "s_xor_b32 s78, s47, 0xc0000\n\t" \
  "v_smfmac_f32_16x16x64_bf16 v[80:83], v[52:55], a[248:255], v106\n\t" \
  "ds_read_b128 v[0:3], v103 offset:32768\n\t" \
  "v_cvt_pk_bf16_f32 v64, v40, v41\n\t" \
  "v_smfmac_f32_16x16x64_bf16 v[84:87], v[52:55], v[144:151], v106\n\t" \
  "ds_read_b128 v[4:7], v103 offset:33792\n\t" \
  "v_cvt_pk_bf16_f32 v65, v42, v43\n\t" \
  "v_smfmac_f32_16x16x64_bf16 v[80:83], v[60:63], v[128:135], v106\n\t" \
  "ds_read_b128 v[8:11], v103 offset:34816\n\t" \
  "v_cvt_pk_bf16_f32 v66, v44, v45\n\t" \
  "v_smfmac_f32_16x16x64_bf16 v[84:87], v[60:63], v[24:31], v106\n\t" \
  "ds_read_b128 v[12:15], v103 offset:35840\n\t" \
  "v_cvt_pk_bf16_f32 v67, v46, v47\n\t" \
  "v_smfmac_f32_16x16x64_bf16 v[88:91], v[96:99], v[152:159], v106\n\t" \
  "v_permlane16_swap_b32 v64, v66\n\t" \
  "v_cvt_pk_bf16_f32 v68, v80, v81\n\t" \
  "v_smfmac_f32_16x16x64_bf16 v[92:95], v[96:99], v[184:191], v106\n\t" \
  "v_permlane16_swap_b32 v65, v67\n\t" \
  "v_cvt_pk_bf16_f32 v69, v82, v83\n\t" \
  "v_smfmac_f32_16x16x64_bf16 v[88:91], v[52:55], v[160:167], v106\n\t" \
  "buffer_store_dwordx4 v[64:67], v108, %[ru], s47 offen sc1\n\t" \
  "v_cvt_pk_bf16_f32 v70, v84, v85\n\t" \
  "v_smfmac_f32_16x16x64_bf16 v[92:95], v[52:55], v[192:199], v106\n\t" \
  "v_cvt_pk_bf16_f32 v71, v86, v87\n\t" \
  "v_smfmac_f32_16x16x64_bf16 v[88:91], v[56:59], v[168:175], v106\n\t" \
  "ds_read_b128 v[16:19], v103 offset:49152\n\t" \
  "v_permlane16_swap_b32 v68, v70\n\t" \
  "s_waitcnt lgkmcnt(3)\n\t" \
  "v_smfmac_f32_16x16x64_bf16 v[92:95], v[56:59], v[0:7], v106\n\t" \
  "ds_read_b128 v[20:23], v103 offset:50176\n\t" \
  "v_permlane16_swap_b32 v69, v71\n\t" \
  "v_smfmac_f32_16x16x64_bf16 v[88:91], v[60:63], v[176:183], v106\n\t" \
  "ds_read_b128 v[24:27], v103 offset:51200\n\t" \
  "buffer_store_dwordx4 v[68:71], v108, %[ru], s76 offen sc1\n\t" \
  "s_waitcnt lgkmcnt(3)\n\t" \
  "v_smfmac_f32_16x16x64_bf16 v[92:95], v[60:63], v[8:15], v106\n\t" \
  "ds_read_b128 v[28:31], v103 offset:52224\n\t" \
  "v_smfmac_f32_16x16x64_bf16 v[32:35], v[96:99], v[200:207], v106\n\t" \
  "v_cvt_pk_bf16_f32 v72, v88, v89\n\t" \
  "v_smfmac_f32_16x16x64_bf16 v[36:39], v[96:99], v[232:239], v106\n\t" \
  "v_cvt_pk_bf16_f32 v73, v90, v91\n\t" \
  "v_smfmac_f32_16x16x64_bf16 v[32:35], v[52:55], v[208:215], v106\n\t" \
  "v_cvt_pk_bf16_f32 v74, v92, v93\n\t" \
  "v_smfmac_f32_16x16x64_bf16 v[36:39], v[52:55], v[240:247], v106\n\t" \
  "v_cvt_pk_bf16_f32 v75, v94, v95\n\t" \
  "v_smfmac_f32_16x16x64_bf16 v[32:35], v[56:59], v[216:223], v106\n\t" \
  "v_permlane16_swap_b32 v72, v74\n\t" \
  "v_smfmac_f32_16x16x64_bf16 v[32:35], v[60:63], v[224:231], v106\n\t" \
  "v_permlane16_swap_b32 v73, v75\n\t" \
  "s_waitcnt lgkmcnt(2)\n\t" \
  "v_smfmac_f32_16x16x64_bf16 v[36:39], v[56:59], v[16:23], v106\n\t" \
  "buffer_store_dwordx4 v[72:75], v108, %[ru], s77 offen sc1\n\t" \
  "s_waitcnt lgkmcnt(0)\n\t" \
  "v_smfmac_f32_16x16x64_bf16 v[36:39], v[60:63], v[24:31], v106\n\t" \
  "v_cvt_pk_bf16_f32 v76, v32, v33\n\t" \
  "v_cvt_pk_bf16_f32 v77, v34, v35\n\t" \
  "s_nop 5\n\t" \
  "v_cvt_pk_bf16_f32 v78, v36, v37\n\t" \
  "v_cvt_pk_bf16_f32 v79, v38, v39\n\t" \
  "s_nop 0\n\t" \
  "v_permlane16_swap_b32 v76, v78\n\t" \
  "v_permlane16_swap_b32 v77, v79\n\t" \
  "buffer_store_dwordx4 v[76:79], v108, %[ru], s78 offen sc1\n\t" \
  "s_memtime s[76:77]\n\t" \
  "s_mov_b32 s42, 0\n\t" \
  "s_waitcnt lgkmcnt(0)\n\t" \
  "s_mov_b32 s57, s76\n\t" \
  "s_add_i32 s41, s56, 1950\n\t" \
  "s_sub_i32 s76, s92, s51\n\t" \
  "s_sub_i32 s77, s93, s53\n\t" \
  "s_cmp_eq_u32 s76, 0\n\t" \
  "s_cbranch_scc1 21f\n\t" \
  "v_cvt_f32_u32 v117, s76\n\t" \
  "v_cvt_f32_u32 v116, s77\n\t" \
  "v_rcp_f32 v117, v117\n\t" \
  "s_mov_b32 s51, s92\n\t" \
  "v_mul_f32 v117, v117, v116\n\t" \
  "v_max_f32 v117, 0x3f028f5c, v117\n\t" \
  "v_min_f32 v117, 0x3ffeb852, v117\n\t" \
  "v_rcp_f32 v116, v117\n\t" \
  "v_mul_f32 v117, 0x4f000000, v117\n\t" \
  "v_mul_f32 v116, 0x47800000, v116\n\t" \
  "v_cvt_u32_f32 v117, v117\n\t" \
  "v_cvt_u32_f32 v116, v116\n\t" \
  "s_mov_b32 s53, s93\n\t" \
  "v_readfirstlane_b32 s46, v117\n\t" \
  "v_readfirstlane_b32 s52, v116\n\t" \
  "21:\n\t" \
  "s_add_i32 s44, s44, 1\n\t" \
  "s_and_b32 s58, s58, 4\n\t" \
  "s_and_b32 s76, s44, 64\n\t" \
  "s_or_b32 s42, s42, s76\n\t" \
  "s_cbranch_scc0 20b\n\t" \
  "s_branch 99f\n\t" \
  "14:\n\t" \
  "s_sleep 3\n\t" \
  "s_add_u32 s59, s59, 0x10000\n\t" \
  "s_cmp_lt_u32 s59, 0x100000\n\t" \
  "s_cbranch_scc1 13b\n\t" \
  "s_mov_b64 exec, -1\n\t" \
  "s_mov_b32 s42, 16\n\t" \
  "s_branch 99f\n\t" \
  "8:\n\t" \
  "s_bitcmp1_b32 s58, 1\n\t" \
  "s_cbranch_scc1 4b\n\t" \
  "s_bitcmp1_b32 s58, 2\n\t" \
  "s_cbranch_scc1 5b\n\t" \
  "v_max3_u32 v116, v32, v33, v34\n\t" \
  "v_max3_u32 v117, v35, v36, v37\n\t" \
  "v_max3_u32 v116, v116, v38, v39\n\t" \
  "v_max_u32 v116, v116, v117\n\t" \
  "v_cmp_eq_u32 vcc, -1, v116\n\t" \
  "s_cbranch_vccz 5b\n\t" \
  "s_branch 0f\n\t" \
  "9:\n\t" \
  "s_bitcmp1_b32 s58, 2\n\t" \
  "s_cbranch_scc1 6b\n\t" \
  "v_max3_u32 v116, v40, v41, v42\n\t" \
  "v_max3_u32 v117, v43, v44, v45\n\t" \
  "v_max3_u32 v116, v116, v46, v47\n\t" \
  "v_max_u32 v116, v116, v117\n\t" \
  "v_cmp_eq_u32 vcc, -1, v116\n\t" \
  "s_cbranch_vccz 6b\n\t" \
  "s_or_b32 s58, s58, 2\n\t" \
  "0:\n\t" \
  "s_or_b32 s58, s58, 1\n\t" \
  "11:\n\t" \
  "buffer_load_dwordx4 v[32:35], v100, %[rh], s40 offen sc1\n\t" \
  "buffer_load_dwordx4 v[36:39], v100, %[rh], s76 offen sc1\n\t" \
  "buffer_load_dwordx4 v[40:43], v100, %[rh], s77 offen sc1\n\t" \
  "buffer_load_dwordx4 v[44:47], v100, %[rh], s78 offen sc1\n\t" \
  "s_waitcnt lgkmcnt(0)\n\t" \
  "s_branch 10b\n\t" \
  "17:\n\t" \
  "s_add_u32 s59, s59, 1\n\t" \
  "s_bitcmp1_b32 s58, 2\n\t" \
  "s_cbranch_scc1 3b\n\t" \
  "s_waitcnt vmcnt(0)\n\t" \
  "v_max_u32 v116, v32, v33\n\t" \
  "v_max3_u32 v116, v116, v34, v35\n\t" \
  "v_max3_u32 v116, v116, v36, v37\n\t" \
  "v_max3_u32 v116, v116, v38, v39\n\t" \
  "v_max3_u32 v116, v116, v40, v41\n\t" \
  "v_max3_u32 v116, v116, v42, v43\n\t" \
  "v_max3_u32 v116, v116, v44, v45\n\t" \
  "v_max3_u32 v116, v116, v46, v47\n\t" \
  "v_cmp_eq_u32 vcc, -1, v116\n\t" \
  "s_cbranch_vccz 3b\n\t" \
  "s_bitcmp1_b32 s59, 6\n\t" \
  "s_cbranch_scc1 12f\n\t" \
  "buffer_load_dwordx4 v[32:35], v100, %[rh], s40 offen sc1\n\t" \
  "buffer_load_dwordx4 v[36:39], v100, %[rh], s76 offen sc1\n\t" \
  "buffer_load_dwordx4 v[40:43], v100, %[rh], s77 offen sc1\n\t" \
  "buffer_load_dwordx4 v[44:47], v100, %[rh], s78 offen sc1\n\t" \
  "s_branch 17b\n\t" \
  "12:\n\t" \
  "s_mov_b32 s42, s58\n\t" \
  "s_branch 99f\n\t" \
  "92:\n\t" \
  "s_mov_b32 s58, s42\n\t" \
  "20:\n\t" \
  "s_and_b32 s76, s44, 7\n\t" \
  "s_lshl_b32 s76, s76, 15\n\t" \
  "s_add_i32 s40, s54, s76\n\t" \
  "s_and_b32 s76, s44, 3\n\t" \
  "s_lshl_b32 s45, s76, 14\n\t" \
  "s_lshl_b32 s47, s76, 20\n\t" \
  "s_add_i32 s47, s47, s55\n\t" \
  "s_lshr_b32 s76, s47, 21\n\t" \
  "s_bitcmp1_b64 %[sel], s76\n\t" \
  "s_cselect_b32 s76, 0x2000, 0\n\t" \
  "s_add_i32 s47, s47, s76\n\t" \
  "s_add_i32 s49, s44, -1\n\t" \
  "s_and_b32 s48, s49, 2\n\t" \
  "s_lshl_b32 s48, s48, 13\n\t" \
  "s_and_b32 s76, s49, 1\n\t" \
  "s_lshl_b32 s76, s76, 12\n\t" \
  "s_add_i32 s48, s48, s76\n\t" \
  "s_lshl_b32 s50, s53, 8\n\t" \
  "s_mov_b32 s59, 0\n\t" \
  "s_mov_b32 s92, s51\n\t" \
  "s_mov_b32 s93, s53\n\t" \
  "s_xor_b32 s76, s40, 0x400\n\t" \
  "s_xor_b32 s77, s40, 0x800\n\t" \
  "s_xor_b32 s78, s40, 0xc00\n\t" \
  "s_cmp_lg_u32 s58, 0\n\t" \
  "s_cbranch_scc1 11f\n\t" \
  "10:\n\t" \
  "ds_read_b128 v[48:51], v102 offset:32768\n\t" \
  "ds_read_b128 v[52:55], v102 offset:33792\n\t" \
  "ds_read_b128 v[56:59], v102 offset:34816\n\t" \
  "ds_read_b128 v[60:63], v102 offset:35840\n\t" \
  "ds_read_b128 v[0:3], v102 offset:49152\n\t" \
  "ds_read_b128 v[4:7], v102 offset:50176\n\t" \
  "ds_read_b128 v[8:11], v102 offset:51200\n\t" \
  "ds_read_b128 v[12:15], v102 offset:52224\n\t" \
  "ds_read_b128 v[16:19], v102 offset:0\n\t" \
  "ds_read_b128 v[20:23], v102 offset:1024\n\t" \
  "ds_read_b128 v[24:27], v102 offset:2048\n\t" \
  "ds_read_b128 v[28:31], v102 offset:3072\n\t" \
  "s_waitcnt lgkmcnt(4)\n\t" \
  "ds_read_b128 v[80:83], v102 offset:16384\n\t" \
  "ds_read_b128 v[84:87], v102 offset:17408\n\t" \
  "ds_read_b128 v[88:91], v102 offset:18432\n\t" \
  "ds_read_b128 v[92:95], v102 offset:19456\n\t" \
  "46:\n\t" \
  "s_bitcmp1_b32 s58, 1\n\t" \
  "s_cselect_b64 s[80:81], -1, 0\n\t" \
  "s_cmp_lg_u32 s58, 0\n\t" \
  "s_cbranch_scc1 17f\n\t" \
  "s_mov_b32 exec_hi, 0\n\t" \
  "16:\n\t" \
  "s_memtime vcc\n\t" \
  "s_waitcnt lgkmcnt(0)\n\t" \
  "s_sub_i32 s79, s41, vcc_lo\n\t" \
  "s_cmp_gt_i32 s79, 64\n\t" \
  "s_cbranch_scc1 16b\n\t" \
  "13:\n\t" \
  "buffer_load_dword v116, v112, %[rh], s48 offen sc1\n\t" \
  "s_waitcnt vmcnt(0)\n\t" \
  "15:\n\t" \
  "v_mov_b32 v117, 20000\n\t" \
  "v_cmp_ne_u32_sdwa vcc, v116, s49 src0_sel:BYTE_0 src1_sel:DWORD\n\t" \
  "v_subrev_u32 v116, s50, v116\n\t" \
  "v_ashrrev_i32 v116, 8, v116\n\t" \
  "s_nop 1\n\t" \
  "v_max_i32_dpp v116, v116, v116 quad_perm:[1,0,3,2] row_mask:0xf bank_mask:0xf\n\t" \
  "s_cbranch_vccnz 14f\n\t" \
  "s_nop 0\n\t" \
  "v_max_i32_dpp v116, v116, v116 quad_perm:[2,3,0,1] row_mask:0xf bank_mask:0xf\n\t" \
  "s_nop 1\n\t" \
  "v_max_i32_dpp v116, v116, v116 row_half_mirror row_mask:0xf bank_mask:0xf\n\t" \
  "s_nop 1\n\t" \
  "v_max_i32_dpp v116, v116, v116 row_mirror row_mask:0xf bank_mask:0xf\n\t" \
  "v_med3_i32 v116, v116, 0, v117\n\t" \
  "v_mul_u32_u24 v116, s52, v116\n\t" \
  "v_lshrrev_b32 v116, 16, v116\n\t" \
  "s_mov_b64 exec, -1\n\t" \
  "v_readfirstlane_b32 s84, v116\n\t" \
  "v_readlane_b32 s85, v116, 16\n\t" \
  "s_max_u32 s84, s84, s85\n\t" \
  "s_add_i32 s84, s84, s51\n\t" \
  "1:\n\t" \
  "s_memtime vcc\n\t" \
  "s_waitcnt lgkmcnt(0)\n\t" \
  "s_sub_i32 s79, s84, vcc_lo\n\t" \
  "s_cmp_gt_i32 s79, 128\n\t" \
  "s_cbranch_scc1 1b\n\t" \
  ".p2align 4\n\t" \
  "2:\n\t" \
  "s_sub_i32 s79, s79, 33\n\t" \
  "s_cmp_gt_i32 s79, 56\n\t" \
  "s_cbranch_scc1 2b\n\t" \
  "buffer_load_dwordx4 v[32:35], v100, %[rh], s40 offen sc1\n\t" \
  "s_nop 12\n\t" \
  "buffer_load_dwordx4 v[36:39], v100, %[rh], s76 offen sc1\n\t" \
  "s_nop 12\n\t" \
  "buffer_load_dwordx4 v[40:43], v100, %[rh], s77 offen sc1\n\t" \
  "s_nop 12\n\t" \
  "buffer_load_dwordx4 v[44:47], v100, %[rh], s78 offen sc1\n\t" \
  "s_memtime vcc\n\t" \
  "s_memrealtime s[82:83]\n\t" \
  "s_nop 4\n\t" \
  "s_memrealtime s[84:85]\n\t" \
  "s_nop 4\n\t" \
  "s_memrealtime s[56:57]\n\t" \
  "s_nop 4\n\t" \
  "s_memrealtime s[86:87]\n\t" \
  "s_nop 4\n\t" \
  "s_memrealtime s[88:89]\n\t" \
  "s_nop 4\n\t" \
  "s_memrealtime s[90:91]\n\t" \
  "s_mov_b32 s92, 0\n\t" \
  "s_waitcnt lgkmcnt(0)\n\t" \
  "s_cmp_eq_u32 s82, s84\n\t" \
  "s_addc_u32 s92, s92, 0\n\t" \
  "s_cmp_eq_u32 s82, s56\n\t" \
  "s_addc_u32 s92, s92, 0\n\t" \
  "s_cmp_eq_u32 s82, s86\n\t" \
  "s_addc_u32 s92, s92, 0\n\t" \
  "s_cmp_eq_u32 s82, s88\n\t" \
  "s_addc_u32 s92, s92, 0\n\t" \
  "s_cmp_eq_u32 s82, s90\n\t" \
  "s_addc_u32 s92, s92, 0\n\t" \
  "s_mul_i32 s92, s92, 24\n\t" \
  "s_add_i32 s92, s92, vcc_lo\n\t" \
  "s_lshl4_add_u32 s93, s82, 64\n\t" \
  "3:\n\t" \
  "v_mov_b64 v[72:73], 0\n\t" \
  "v_mov_b64 v[74:75], 0\n\t" \
  "v_mov_b64 v[76:77], 0\n\t" \
  "v_mov_b64 v[78:79], 0\n\t" \
  "s_waitcnt lgkmcnt(0)\n\t" \
  "s_waitcnt vmcnt(3)\n\t" \
  "v_smfmac_f32_16x16x64_bf16 v[72:75], v[32:35], v[48:55], v104\n\t" \
  "ds_write_b128 v101, v[32:35] offset:8192\n\t" \
  "v_smfmac_f32_16x16x64_bf16 v[76:79], v[32:35], v[56:63], v104\n\t" \
  "s_waitcnt vmcnt(2)\n\t" \
  "ds_write_b128 v101, v[36:39] offset:9216\n\t" \
  "v_smfmac_f32_16x16x64_bf16 v[72:75], v[36:39], a[96:103], v104\n\t" \
  "v_smfmac_f32_16x16x64_bf16 v[76:79], v[36:39], a[104:111], v104\n\t" \
  "s_nop 3\n\t" \
  "v_cmp_u_f32 vcc, v72, v73\n\t" \
  "v_cmp_u_f32 s[82:83], v74, v75\n\t" \
  "s_waitcnt vmcnt(1)\n\t" \
  "v_smfmac_f32_16x16x64_bf16 v[72:75], v[40:43], a[112:119], v104\n\t" \
  "s_or_b64 vcc, vcc, s[82:83]\n\t" \
  "s_or_b64 vcc, vcc, s[80:81]\n\t" \
  "v_smfmac_f32_16x16x64_bf16 v[76:79], v[40:43], a[120:127], v104\n\t" \
  "s_waitcnt lgkmcnt(0)\n\t" \
  "s_cbranch_scc1 8f\n\t" \
  "5:\n\t" \
  "s_barrier\n\t" \
  "4:\n\t" \
  "s_memtime s[56:57]\n\t" \
  "ds_read_b128 v[48:51], v101 offset:12288\n\t" \
  "ds_read_b128 v[52:55], v101 offset:13312\n\t" \
  "ds_read_b128 v[56:59], v101 offset:1024\n\t" \
  "s_waitcnt vmcnt(0)\n\t" \
  "v_smfmac_f32_16x16x64_bf16 v[72:75], v[44:47], a[128:135], v104\n\t" \
  "ds_read_b128 v[60:63], v101 offset:0\n\t" \
  "ds_read_b128 v[64:67], v101 offset:5120\n\t" \
  "v_smfmac_f32_16x16x64_bf16 v[76:79], v[44:47], a[136:143], v104\n\t" \
  "ds_read_b128 v[68:71], v101 offset:4096\n\t" \
  "ds_write_b128 v101, v[40:43] offset:10240\n\t" \
  "s_nop 1\n\t" \
  "v_cmp_u_f32 vcc, v72, v73\n\t" \
  "v_cmp_u_f32 s[82:83], v74, v75\n\t" \
  "s_waitcnt lgkmcnt(6)\n\t" \
  "v_smfmac_f32_16x16x64_bf16 v[72:75], v[48:51], v[0:7], v104\n\t" \
  "s_or_b64 vcc, vcc, s[82:83]\n\t" \
  "v_smfmac_f32_16x16x64_bf16 v[76:79], v[48:51], v[8:15], v104\n\t" \
  "s_cbranch_scc1 9f\n\t" \
  "6:\n\t" \
  "s_waitcnt lgkmcnt(5)\n\t" \
  "v_smfmac_f32_16x16x64_bf16 v[72:75], v[52:55], a[144:151], v104\n\t" \
  "ds_write_b128 v101, v[44:47] offset:11264\n\t" \
  "v_smfmac_f32_16x16x64_bf16 v[76:79], v[52:55], a[152:159], v104\n\t" \
  "v_mov_b64 v[40:41], 0\n\t" \
  "s_waitcnt lgkmcnt(5)\n\t" \
  "v_smfmac_f32_16x16x64_bf16 v[72:75], v[56:59], a[0:7], v104\n\t" \
  "v_mov_b64 v[42:43], 0\n\t" \
  "v_smfmac_f32_16x16x64_bf16 v[76:79], v[56:59], a[8:15], v104\n\t" \
  "s_waitcnt lgkmcnt(4)\n\t" \
  "v_smfmac_f32_16x16x64_bf16 v[72:75], v[60:63], v[16:23], v104\n\t" \
  "s_waitcnt lgkmcnt(0)\n\t" \
  "s_barrier\n\t" \
  "v_smfmac_f32_16x16x64_bf16 v[76:79], v[60:63], v[24:31], v104\n\t" \
  "ds_read_b128 v[48:51], v101 offset:14336\n\t" \
  "ds_read_b128 v[52:55], v101 offset:2048\n\t" \
  "v_smfmac_f32_16x16x64_bf16 v[72:75], v[64:67], a[48:55], v104\n\t" \
  "ds_read_b128 v[56:59], v101 offset:6144\n\t" \
  "ds_read_b128 v[60:63], v101 offset:15360\n\t" \
  "v_smfmac_f32_16x16x64_bf16 v[76:79], v[64:67], a[56:63], v104\n\t" \
  "ds_read_b128 v[32:35], v101 offset:3072\n\t" \
  "ds_read_b128 v[36:39], v101 offset:7168\n\t" \
  "v_smfmac_f32_16x16x64_bf16 v[72:75], v[68:71], v[80:87], v104\n\t" \
  "ds_read_b128 v[0:3], v103 offset:0\n\t" \
  "v_mov_b64 v[44:45], 0\n\t" \
  "v_smfmac_f32_16x16x64_bf16 v[76:79], v[68:71], v[88:95], v104\n\t" \
  "v_mov_b64 v[46:47], 0\n\t" \
  "s_waitcnt lgkmcnt(5)\n\t" \
  "v_smfmac_f32_16x16x64_bf16 v[72:75], v[48:51], a[160:167], v104\n\t" \
  "ds_read_b128 v[4:7], v103 offset:1024\n\t" \
  "v_mov_b64 v[80:81], 0\n\t" \
  "v_smfmac_f32_16x16x64_bf16 v[72:75], v[52:55], a[16:23], v104\n\t" \
  "v_mov_b64 v[82:83], 0\n\t" \
  "s_waitcnt lgkmcnt(2)\n\t" \
  "v_smfmac_f32_16x16x64_bf16 v[72:75], v[56:59], a[64:71], v104\n\t" \
  "ds_read_b128 v[8:11], v103 offset:2048\n\t" \
  "v_mov_b64 v[84:85], 0\n\t" \
  "v_smfmac_f32_16x16x64_bf16 v[72:75], v[60:63], a[176:183], v104\n\t" \
  "ds_read_b128 v[12:15], v103 offset:3072\n\t" \
  "v_mov_b64 v[86:87], 0\n\t" \
  "v_smfmac_f32_16x16x64_bf16 v[72:75], v[32:35], a[32:39], v104\n\t" \
  "ds_read_b128 v[16:19], v103 offset:16384\n\t" \
  "v_mov_b64 v[88:89], 0\n\t" \
  "v_smfmac_f32_16x16x64_bf16 v[72:75], v[36:39], a[80:87], v104\n\t" \
  "ds_read_b128 v[20:23], v103 offset:17408\n\t" \
  "v_mov_b64 v[90:91], 0\n\t" \
  "v_smfmac_f32_16x16x64_bf16 v[76:79], v[48:51], a[168:175], v104\n\t" \
  "ds_read_b128 v[24:27], v103 offset:18432\n\t" \
  "v_mov_b64 v[92:93], 0\n\t" \
  "v_smfmac_f32_16x16x64_bf16 v[76:79], v[52:55], a[24:31], v104\n\t" \
  "v_add_f32 v96, v72, v74\n\t" \
  "v_add_f32 v97, v73, v75\n\t" \
  "v_cvt_pk_bf16_f32 v96, v96, v97\n\t" \
  "ds_write_b32 v105, v96\n\t" \
  "v_smfmac_f32_16x16x64_bf16 v[76:79], v[56:59], a[72:79], v104\n\t" \
  "ds_read_b128 v[28:31], v103 offset:19456\n\t" \
  "v_mov_b64 v[94:95], 0\n\t" \
  "v_smfmac_f32_16x16x64_bf16 v[76:79], v[60:63], a[184:191], v104\n\t" \
  "v_smfmac_f32_16x16x64_bf16 v[76:79], v[32:35], a[40:47], v104\n\t" \
  "v_smfmac_f32_16x16x64_bf16 v[76:79], v[36:39], a[88:95], v104\n\t" \
  "s_waitcnt lgkmcnt(1)\n\t" \
  "s_barrier\n\t" \
  "ds_read_b64_tr_b16 v[96:97], v107 offset:0\n\t" \
  "ds_read_b64_tr_b16 v[98:99], v107 offset:64\n\t" \
  "ds_read_b64_tr_b16 v[56:57], v107 offset:1024\n\t" \
  "ds_read_b64_tr_b16 v[58:59], v107 offset:1088\n\t" \
  "s_nop 1\n\t" \
  "v_add_f32 v116, v76, v78\n\t" \
  "v_add_f32 v117, v77, v79\n\t" \
  "v_cvt_pk_bf16_f32 v116, v116, v117\n\t" \
  "ds_write_b32 v105, v116 offset:256\n\t" \
  "s_waitcnt lgkmcnt(3)\n\t" \
  "v_smfmac_f32_16x16x64_bf16 v[40:43], v[96:99], a[192:199], v106\n\t" \
  "v_smfmac_f32_16x16x64_bf16 v[44:47], v[96:99], a[224:231], v106\n\t" \
  "s_waitcnt lgkmcnt(0)\n\t" \
  "s_barrier\n\t" \
  "ds_read_b64_tr_b16 v[52:53], v107 offset:256\n\t" \
  "ds_read_b64_tr_b16 v[54:55], v107 offset:320\n\t" \
  "v_smfmac_f32_16x16x64_bf16 v[40:43], v[56:59], a[208:215], v106\n\t" \
  "ds_read_b64_tr_b16 v[60:61], v107 offset:1280\n\t" \
  "ds_read_b64_tr_b16 v[62:63], v107 offset:1344\n\t" \
  "v_smfmac_f32_16x16x64_bf16 v[44:47], v[56:59], v[0:7], v106\n\t" \
  "v_smfmac_f32_16x16x64_bf16 v[80:83], v[96:99], a[240:247], v106\n\t" \
  "v_mov_b64 v[32:33], 0\n\t" \
  "v_smfmac_f32_16x16x64_bf16 v[84:87], v[96:99], v[136:143], v106\n\t" \
  "v_mov_b64 v[34:35], 0\n\t" \
  "v_smfmac_f32_16x16x64_bf16 v[80:83], v[56:59], v[120:127], v106\n\t" \
  "v_mov_b64 v[36:37], 0\n\t" \
  "v_smfmac_f32_16x16x64_bf16 v[84:87], v[56:59], v[16:23], v106\n\t" \
  "s_waitcnt lgkmcnt(2)\n\t" \
  "v_smfmac_f32_16x16x64_bf16 v[40:43], v[52:55], a[200:207], v106\n\t" \
  "v_smfmac_f32_16x16x64_bf16 v[44:47], v[52:55], a[232:239], v106\n\t" \
  "s_xor_b32 s76, s47, 0x40000\n\t" \
  "s_waitcnt lgkmcnt(0)\n\t" \
  "v_smfmac_f32_16x16x64_bf16 v[40:43], v[60:63], a[216:223], v106\n\t" \
  "v_mov_b64 v[38:39], 0\n\t" \
  "s_xor_b32 s77, s47, 0x80000\n\t" \
  "v_smfmac_f32_16x16x64_bf16 v[44:47], v[60:63], v[8:15], v106\n\t" \
  "s_xor_b32 s78, s47, 0xc0000\n\t" \
  "v_smfmac_f32_16x16x64_bf16 v[80:83], v[52:55], a[248:255], v106\n\t" \
  "ds_read_b128 v[0:3], v103 offset:32768\n\t" \
  "v_cvt_pk_bf16_f32 v64, v40, v41\n\t" \
  "v_smfmac_f32_16x16x64_bf16 v[84:87], v[52:55], v[144:151], v106\n\t" \
  "ds_read_b128 v[4:7], v103 offset:33792\n\t" \
  "v_cvt_pk_bf16_f32 v65, v42, v43\n\t" \
  "v_smfmac_f32_16x16x64_bf16 v[80:83], v[60:63], v[128:135], v106\n\t" \
  "ds_read_b128 v[8:11], v103 offset:34816\n\t" \
  "v_cvt_pk_bf16_f32 v66, v44, v45\n\t" \
  "v_smfmac_f32_16x16x64_bf16 v[84:87], v[60:63], v[24:31], v106\n\t" \
  "ds_read_b128 v[12:15], v103 offset:35840\n\t" \
  "v_cvt_pk_bf16_f32 v67, v46, v47\n\t" \
  "v_smfmac_f32_16x16x64_bf16 v[88:91], v[96:99], v[152:159], v106\n\t" \
  "v_permlane16_swap_b32 v64, v66\n\t" \
  "v_cvt_pk_bf16_f32 v68, v80, v81\n\t" \
  "v_smfmac_f32_16x16x64_bf16 v[92:95], v[96:99], v[184:191], v106\n\t" \
  "v_permlane16_swap_b32 v65, v67\n\t" \
  "v_cvt_pk_bf16_f32 v69, v82, v83\n\t" \
  "v_smfmac_f32_16x16x64_bf16 v[88:91], v[52:55], v[160:167], v106\n\t" \
  "v_cvt_pk_bf16_f32 v70, v84, v85\n\t" \
  "v_smfmac_f32_16x16x64_bf16 v[92:95], v[52:55], v[192:199], v106\n\t" \
  "buffer_store_dwordx4 v[64:67], v108, %[ru], s47 offen sc1\n\t" \
  "v_cvt_pk_bf16_f32 v71, v86, v87\n\t" \
  "v_smfmac_f32_16x16x64_bf16 v[88:91], v[56:59], v[168:175], v106\n\t" \
  "ds_read_b128 v[16:19], v103 offset:49152\n\t" \
  "v_permlane16_swap_b32 v68, v70\n\t" \
  "s_waitcnt lgkmcnt(3)\n\t" \
  "v_smfmac_f32_16x16x64_bf16 v[92:95], v[56:59], v[0:7], v106\n\t" \
  "ds_read_b128 v[20:23], v103 offset:50176\n\t" \
  "v_permlane16_swap_b32 v69, v71\n\t" \
  "v_smfmac_f32_16x16x64_bf16 v[88:91], v[60:63], v[176:183], v106\n\t" \
  "ds_read_b128 v[24:27], v103 offset:51200\n\t" \
  "s_waitcnt lgkmcnt(3)\n\t" \
  "v_smfmac_f32_16x16x64_bf16 v[92:95], v[60:63], v[8:15], v106\n\t" \
  "ds_read_b128 v[28:31], v103 offset:52224\n\t" \
  "buffer_store_dwordx4 v[68:71], v108, %[ru], s76 offen sc1\n\t" \
  "v_smfmac_f32_16x16x64_bf16 v[32:35], v[96:99], v[200:207], v106\n\t" \
  "v_cvt_pk_bf16_f32 v72, v88, v89\n\t" \
  "v_smfmac_f32_16x16x64_bf16 v[36:39], v[96:99], v[232:239], v106\n\t" \
  "v_cvt_pk_bf16_f32 v73, v90, v91\n\t" \
  "v_smfmac_f32_16x16x64_bf16 v[32:35], v[52:55], v[208:215], v106\n\t" \
  "v_cvt_pk_bf16_f32 v74, v92, v93\n\t" \
  "v_smfmac_f32_16x16x64_bf16 v[36:39], v[52:55], v[240:247], v106\n\t" \
  "v_cvt_pk_bf16_f32 v75, v94, v95\n\t" \
  "v_smfmac_f32_16x16x64_bf16 v[32:35], v[56:59], v[216:223], v106\n\t" \
  "v_permlane16_swap_b32 v72, v74\n\t" \
  "v_smfmac_f32_16x16x64_bf16 v[32:35], v[60:63], v[224:231], v106\n\t" \
  "v_permlane16_swap_b32 v73, v75\n\t" \
  "s_waitcnt lgkmcnt(2)\n\t" \
  "v_smfmac_f32_16x16x64_bf16 v[36:39], v[56:59], v[16:23], v106\n\t" \
  "s_waitcnt lgkmcnt(0)\n\t" \
  "v_smfmac_f32_16x16x64_bf16 v[36:39], v[60:63], v[24:31], v106\n\t" \
  "buffer_store_dwordx4 v[72:75], v108, %[ru], s77 offen sc1\n\t" \
  "v_cvt_pk_bf16_f32 v76, v32, v33\n\t" \
  "v_cvt_pk_bf16_f32 v77, v34, v35\n\t" \
  "s_nop 5\n\t" \
  "v_cvt_pk_bf16_f32 v78, v36, v37\n\t" \
  "v_cvt_pk_bf16_f32 v79, v38, v39\n\t" \
  "s_nop 0\n\t" \
  "v_permlane16_swap_b32 v76, v78\n\t" \
  "v_permlane16_swap_b32 v77, v79\n\t" \
  "buffer_store_dwordx4 v[76:79], v108, %[ru], s78 offen sc1\n\t" \
  "s_memtime s[76:77]\n\t" \
  "s_mov_b32 s42, 0\n\t" \
  "s_waitcnt lgkmcnt(0)\n\t" \
  "s_mov_b32 s57, s76\n\t" \
  "s_add_i32 s41, s56, 1950\n\t" \
  "s_sub_i32 s76, s92, s51\n\t" \
  "s_sub_i32 s77, s93, s53\n\t" \
  "s_cmp_eq_u32 s76, 0\n\t" \
  "s_cbranch_scc1 21f\n\t" \
  "v_cvt_f32_u32 v117, s76\n\t" \
  "v_cvt_f32_u32 v116, s77\n\t" \
  "v_rcp_f32 v117, v117\n\t" \
  "s_mov_b32 s51, s92\n\t" \
  "v_mul_f32 v117, v117, v116\n\t" \
  "v_max_f32 v117, 0x3f028f5c, v117\n\t" \
  "v_min_f32 v117, 0x3ffeb852, v117\n\t" \
  "v_rcp_f32 v116, v117\n\t" \
  "v_mul_f32 v117, 0x4f000000, v117\n\t" \
  "v_mul_f32 v116, 0x47800000, v116\n\t" \
  "v_cvt_u32_f32 v117, v117\n\t" \
  "v_cvt_u32_f32 v116, v116\n\t" \
  "s_mov_b32 s53, s93\n\t" \
  "v_readfirstlane_b32 s46, v117\n\t" \
  "v_readfirstlane_b32 s52, v116\n\t" \
  "21:\n\t" \
  "s_add_i32 s44, s44, 1\n\t" \
  "s_and_b32 s58, s58, 4\n\t" \
  "s_and_b32 s76, s44, 64\n\t" \
  "s_or_b32 s42, s42, s76\n\t" \
  "s_cbranch_scc0 20b\n\t" \
  "s_branch 99f\n\t" \
  "14:\n\t" \
  "s_sleep 3\n\t" \
  "s_add_u32 s59, s59, 0x10000\n\t" \
  "s_cmp_lt_u32 s59, 0x100000\n\t" \
  "s_cbranch_scc1 13b\n\t" \
  "s_mov_b64 exec, -1\n\t" \
  "s_mov_b32 s42, 16\n\t" \
  "s_branch 99f\n\t" \
  "8:\n\t" \
  "s_bitcmp1_b32 s58, 1\n\t" \
  "s_cbranch_scc1 4b\n\t" \
  "s_bitcmp1_b32 s58, 2\n\t" \
  "s_cbranch_scc1 5b\n\t" \
  "v_max3_u32 v116, v32, v33, v34\n\t" \
  "v_max3_u32 v117, v35, v36, v37\n\t" \
  "v_max3_u32 v116, v116, v38, v39\n\t" \
  "v_max_u32 v116, v116, v117\n\t" \
  "v_cmp_eq_u32 vcc, -1, v116\n\t" \
  "s_cbranch_vccz 5b\n\t" \
  "s_branch 0f\n\t" \
  "9:\n\t" \
  "s_bitcmp1_b32 s58, 2\n\t" \
  "s_cbranch_scc1 6b\n\t" \
  "v_max3_u32 v116, v40, v41, v42\n\t" \
  "v_max3_u32 v117, v43, v44, v45\n\t" \
  "v_max3_u32 v116, v116, v46, v47\n\t" \
  "v_max_u32 v116, v116, v117\n\t" \
  "v_cmp_eq_u32 vcc, -1, v116\n\t" \
  "s_cbranch_vccz 6b\n\t" \
  "s_or_b32 s58, s58, 2\n\t" \
  "0:\n\t" \
  "s_or_b32 s58, s58, 1\n\t" \
  "11:\n\t" \
  "buffer_load_dwordx4 v[32:35], v100, %[rh], s40 offen sc1\n\t" \
  "buffer_load_dwordx4 v[36:39], v100, %[rh], s76 offen sc1\n\t" \
  "buffer_load_dwordx4 v[40:43], v100, %[rh], s77 offen sc1\n\t" \
  "buffer_load_dwordx4 v[44:47], v100, %[rh], s78 offen sc1\n\t" \
  "s_waitcnt lgkmcnt(0)\n\t" \
  "s_branch 10b\n\t" \
  "17:\n\t" \
  "s_add_u32 s59, s59, 1\n\t" \
  "s_bitcmp1_b32 s58, 2\n\t" \
  "s_cbranch_scc1 3b\n\t" \
  "s_waitcnt vmcnt(0)\n\t" \
  "v_max_u32 v116, v32, v33\n\t" \
  "v_max3_u32 v116, v116, v34, v35\n\t" \
  "v_max3_u32 v116, v116, v36, v37\n\t" \
  "v_max3_u32 v116, v116, v38, v39\n\t" \
  "v_max3_u32 v116, v116, v40, v41\n\t" \
  "v_max3_u32 v116, v116, v42, v43\n\t" \
  "v_max3_u32 v116, v116, v44, v45\n\t" \
  "v_max3_u32 v116, v116, v46, v47\n\t" \
  "v_cmp_eq_u32 vcc, -1, v116\n\t" \
  "s_cbranch_vccz 3b\n\t" \
  "s_bitcmp1_b32 s59, 6\n\t" \
  "s_cbranch_scc1 12f\n\t" \
  "buffer_load_dwordx4 v[32:35], v100, %[rh], s40 offen sc1\n\t" \
  "buffer_load_dwordx4 v[36:39], v100, %[rh], s76 offen sc1\n\t" \
  "buffer_load_dwordx4 v[40:43], v100, %[rh], s77 offen sc1\n\t" \
  "buffer_load_dwordx4 v[44:47], v100, %[rh], s78 offen sc1\n\t" \
  "s_branch 17b\n\t" \
  "12:\n\t" \
  "s_mov_b32 s42, s58\n\t" \
  "s_branch 99f\n\t" \
  "93:\n\t" \
  "s_mov_b32 s58, s42\n\t" \
  "20:\n\t" \
  "s_and_b32 s76, s44, 7\n\t" \
  "s_lshl_b32 s76, s76, 15\n\t" \
  "s_add_i32 s40, s54, s76\n\t" \
  "s_and_b32 s76, s44, 3\n\t" \
  "s_lshl_b32 s45, s76, 14\n\t" \
  "s_lshl_b32 s47, s76, 20\n\t" \
  "s_add_i32 s47, s47, s55\n\t" \
  "s_lshr_b32 s76, s47, 21\n\t" \
  "s_bitcmp1_b64 %[sel], s76\n\t" \
  "s_cselect_b32 s76, 0x2000, 0\n\t" \
  "s_add_i32 s47, s47, s76\n\t" \
  "s_add_i32 s49, s44, -1\n\t" \
  "s_and_b32 s48, s49, 2\n\t" \
  "s_lshl_b32 s48, s48, 13\n\t" \
  "s_and_b32 s76, s49, 1\n\t" \
  "s_lshl_b32 s76, s76, 12\n\t" \
  "s_add_i32 s48, s48, s76\n\t" \
  "s_lshl_b32 s50, s53, 8\n\t" \
  "s_mov_b32 s59, 0\n\t" \
  "s_mov_b32 s92, s51\n\t" \
  "s_mov_b32 s93, s53\n\t" \
  "s_xor_b32 s76, s40, 0x400\n\t" \
  "s_xor_b32 s77, s40, 0x800\n\t" \
  "s_xor_b32 s78, s40, 0xc00\n\t" \
  "s_cmp_lg_u32 s58, 0\n\t" \
  "s_cbranch_scc1 11f\n\t" \
  "10:\n\t" \
  "ds_read_b128 v[48:51], v102 offset:49152\n\t" \
  "ds_read_b128 v[52:55], v102 offset:50176\n\t" \
  "ds_read_b128 v[56:59], v102 offset:51200\n\t" \
  "ds_read_b128 v[60:63], v102 offset:52224\n\t" \
  "ds_read_b128 v[0:3], v102 offset:0\n\t" \
  "ds_read_b128 v[4:7], v102 offset:1024\n\t" \
  "ds_read_b128 v[8:11], v102 offset:2048\n\t" \
  "ds_read_b128 v[12:15], v102 offset:3072\n\t" \
  "ds_read_b128 v[16:19], v102 offset:16384\n\t" \
  "ds_read_b128 v[20:23], v102 offset:17408\n\t" \
  "ds_read_b128 v[24:27], v102 offset:18432\n\t" \
  "ds_read_b128 v[28:31], v102 offset:19456\n\t" \
  "s_waitcnt lgkmcnt(4)\n\t" \
  "ds_read_b128 v[80:83], v102 offset:32768\n\t" \
  "ds_read_b128 v[84:87], v102 offset:33792\n\t" \
  "ds_read_b128 v[88:91], v102 offset:34816\n\t" \
  "ds_read_b128 v[92:95], v102 offset:35840\n\t" \
  "46:\n\t" \
  "s_bitcmp1_b32 s58, 1\n\t" \
  "s_cselect_b64 s[80:81], -1, 0\n\t" \
  "s_cmp_lg_u32 s58, 0\n\t" \
  "s_cbranch_scc1 17f\n\t" \
  "s_mov_b32 exec_hi, 0\n\t" \
  "16:\n\t" \
  "s_memtime vcc\n\t" \
  "s_waitcnt lgkmcnt(0)\n\t" \
  "s_sub_i32 s79, s41, vcc_lo\n\t" \
  "s_cmp_gt_i32 s79, 64\n\t" \
  "s_cbranch_scc1 16b\n\t" \
  "13:\n\t" \
  "buffer_load_dword v116, v112, %[rh], s48 offen sc1\n\t" \
  "s_waitcnt vmcnt(0)\n\t" \
  "15:\n\t" \
  "v_mov_b32 v117, 20000\n\t" \
  "v_cmp_ne_u32_sdwa vcc, v116, s49 src0_sel:BYTE_0 src1_sel:DWORD\n\t" \
  "v_subrev_u32 v116, s50, v116\n\t" \
  "v_ashrrev_i32 v116, 8, v116\n\t" \
  "s_nop 1\n\t" \
  "v_max_i32_dpp v116, v116, v116 quad_perm:[1,0,3,2] row_mask:0xf bank_mask:0xf\n\t" \
  "s_cbranch_vccnz 14f\n\t" \
  "s_nop 0\n\t" \
  "v_max_i32_dpp v116, v116, v116 quad_perm:[2,3,0,1] row_mask:0xf bank_mask:0xf\n\t" \
  "s_nop 1\n\t" \
  "v_max_i32_dpp v116, v116, v116 row_half_mirror row_mask:0xf bank_mask:0xf\n\t" \
  "s_nop 1\n\t" \
  "v_max_i32_dpp v116, v116, v116 row_mirror row_mask:0xf bank_mask:0xf\n\t" \
  "v_med3_i32 v116, v116, 0, v117\n\t" \
  "v_mul_u32_u24 v116, s52, v116\n\t" \
  "v_lshrrev_b32 v116, 16, v116\n\t" \
  "s_mov_b64 exec, -1\n\t" \
  "v_readfirstlane_b32 s84, v116\n\t" \
  "v_readlane_b32 s85, v116, 16\n\t" \
  "s_max_u32 s84, s84, s85\n\t" \
  "s_add_i32 s84, s84, s51\n\t" \
  "1:\n\t" \
  "s_memtime vcc\n\t" \
  "s_waitcnt lgkmcnt(0)\n\t" \
  "s_sub_i32 s79, s84, vcc_lo\n\t" \
  "s_cmp_gt_i32 s79, 128\n\t" \
  "s_cbranch_scc1 1b\n\t" \
  ".p2align 4\n\t" \
  "2:\n\t" \
  "s_sub_i32 s79, s79, 33\n\t" \
  "s_cmp_gt_i32 s79, 56\n\t" \
  "s_cbranch_scc1 2b\n\t" \
  "buffer_load_dwordx4 v[32:35], v100, %[rh], s40 offen sc1\n\t" \
  "s_nop 12\n\t" \
  "buffer_load_dwordx4 v[36:39], v100, %[rh], s76 offen sc1\n\t" \
  "s_nop 12\n\t" \
  "buffer_load_dwordx4 v[40:43], v100, %[rh], s77 offen sc1\n\t" \
  "s_nop 12\n\t" \
  "buffer_load_dwordx4 v[44:47], v100, %[rh], s78 offen sc1\n\t" \
  "s_memtime vcc\n\t" \
  "s_memrealtime s[82:83]\n\t" \
  "s_nop 4\n\t" \
  "s_memrealtime s[84:85]\n\t" \
  "s_nop 4\n\t" \
  "s_memrealtime s[56:57]\n\t" \
  "s_nop 4\n\t" \
  "s_memrealtime s[86:87]\n\t" \
  "s_nop 4\n\t" \
  "s_memrealtime s[88:89]\n\t" \
  "s_nop 4\n\t" \
  "s_memrealtime s[90:91]\n\t" \
  "s_mov_b32 s92, 0\n\t" \
  "s_waitcnt lgkmcnt(0)\n\t" \
  "s_cmp_eq_u32 s82, s84\n\t" \
  "s_addc_u32 s92, s92, 0\n\t" \
  "s_cmp_eq_u32 s82, s56\n\t" \
  "s_addc_u32 s92, s92, 0\n\t" \
  "s_cmp_eq_u32 s82, s86\n\t" \
  "s_addc_u32 s92, s92, 0\n\t" \
  "s_cmp_eq_u32 s82, s88\n\t" \
  "s_addc_u32 s92, s92, 0\n\t" \
  "s_cmp_eq_u32 s82, s90\n\t" \
  "s_addc_u32 s92, s92, 0\n\t" \
  "s_mul_i32 s92, s92, 24\n\t" \
  "s_add_i32 s92, s92, vcc_lo\n\t" \
  "s_lshl4_add_u32 s93, s82, 64\n\t" \
  "3:\n\t" \
  "v_mov_b64 v[72:73], 0\n\t" \
  "v_mov_b64 v[74:75], 0\n\t" \
  "v_mov_b64 v[76:77], 0\n\t" \
  "v_mov_b64 v[78:79], 0\n\t" \
  "s_waitcnt lgkmcnt(0)\n\t" \
  "s_waitcnt vmcnt(3)\n\t" \
  "v_smfmac_f32_16x16x64_bf16 v[72:75], v[32:35], v[48:55], v104\n\t" \
  "ds_write_b128 v101, v[32:35] offset:12288\n\t" \
  "v_smfmac_f32_16x16x64_bf16 v[76:79], v[32:35], v[56:63], v104\n\t" \
  "s_waitcnt vmcnt(2)\n\t" \
  "ds_write_b128 v101, v[36:39] offset:13312\n\t" \
  "v_smfmac_f32_16x16x64_bf16 v[72:75], v[36:39], a[144:151], v104\n\t" \
  "v_smfmac_f32_16x16x64_bf16 v[76:79], v[36:39], a[152:159], v104\n\t" \
  "s_nop 3\n\t" \
  "v_cmp_u_f32 vcc, v72, v73\n\t" \
  "v_cmp_u_f32 s[82:83], v74, v75\n\t" \
  "s_waitcnt vmcnt(1)\n\t" \
  "v_smfmac_f32_16x16x64_bf16 v[72:75], v[40:43], a[160:167], v104\n\t" \
  "s_or_b64 vcc, vcc, s[82:83]\n\t" \
  "s_or_b64 vcc, vcc, s[80:81]\n\t" \
  "v_smfmac_f32_16x16x64_bf16 v[76:79], v[40:43], a[168:175], v104\n\t" \
  "s_waitcnt lgkmcnt(0)\n\t" \
  "s_cbranch_scc1 8f\n\t" \
  "5:\n\t" \
  "s_barrier\n\t" \
  "4:\n\t" \
  "s_memtime s[56:57]\n\t" \
  "ds_read_b128 v[48:51], v101 offset:0\n\t" \
  "ds_read_b128 v[52:55], v101 offset:1024\n\t" \
  "ds_read_b128 v[56:59], v101 offset:5120\n\t" \
  "s_waitcnt vmcnt(0)\n\t" \
  "v_smfmac_f32_16x16x64_bf16 v[72:75], v[44:47], a[176:183], v104\n\t" \
  "ds_read_b128 v[60:63], v101 offset:4096\n\t" \
  "ds_read_b128 v[64:67], v101 offset:9216\n\t" \
  "v_smfmac_f32_16x16x64_bf16 v[76:79], v[44:47], a[184:191], v104\n\t" \
  "ds_read_b128 v[68:71], v101 offset:8192\n\t" \
  "ds_write_b128 v101, v[40:43] offset:14336\n\t" \
  "s_nop 1\n\t" \
  "v_cmp_u_f32 vcc, v72, v73\n\t" \
  "v_cmp_u_f32 s[82:83], v74, v75\n\t" \
  "s_waitcnt lgkmcnt(6)\n\t" \
  "v_smfmac_f32_16x16x64_bf16 v[72:75], v[48:51], v[0:7], v104\n\t" \
  "s_or_b64 vcc, vcc, s[82:83]\n\t" \
  "v_smfmac_f32_16x16x64_bf16 v[76:79], v[48:51], v[8:15], v104\n\t" \
  "s_cbranch_scc1 9f\n\t" \
  "6:\n\t" \
  "s_waitcnt lgkmcnt(5)\n\t" \
  "v_smfmac_f32_16x16x64_bf16 v[72:75], v[52:55], a[0:7], v104\n\t" \
  "ds_write_b128 v101, v[44:47] offset:15360\n\t" \
  "v_smfmac_f32_16x16x64_bf16 v[76:79], v[52:55], a[8:15], v104\n\t" \
  "v_mov_b64 v[40:41], 0\n\t" \
  "s_waitcnt lgkmcnt(5)\n\t" \
  "v_smfmac_f32_16x16x64_bf16 v[72:75], v[56:59], a[48:55], v104\n\t" \
  "v_mov_b64 v[42:43], 0\n\t" \
  "v_smfmac_f32_16x16x64_bf16 v[76:79], v[56:59], a[56:63], v104\n\t" \
  "s_waitcnt lgkmcnt(4)\n\t" \
  "v_smfmac_f32_16x16x64_bf16 v[72:75], v[60:63], v[16:23], v104\n\t" \
  "s_waitcnt lgkmcnt(0)\n\t" \
  "s_barrier\n\t" \
  "v_smfmac_f32_16x16x64_bf16 v[76:79], v[60:63], v[24:31], v104\n\t" \
  "ds_read_b128 v[48:51], v101 offset:2048\n\t" \
  "ds_read_b128 v[52:55], v101 offset:6144\n\t" \
  "v_smfmac_f32_16x16x64_bf16 v[72:75], v[64:67], a[96:103], v104\n\t" \
  "ds_read_b128 v[56:59], v101 offset:10240\n\t" \
  "ds_read_b128 v[60:63], v101 offset:3072\n\t" \
  "v_smfmac_f32_16x16x64_bf16 v[76:79], v[64:67], a[104:111], v104\n\t" \
  "ds_read_b128 v[32:35], v101 offset:7168\n\t" \
  "ds_read_b128 v[36:39], v101 offset:11264\n\t" \
  "v_smfmac_f32_16x16x64_bf16 v[72:75], v[68:71], v[80:87], v104\n\t" \
  "ds_read_b128 v[0:3], v103 offset:0\n\t" \
  "v_mov_b64 v[44:45], 0\n\t" \
  "v_smfmac_f32_16x16x64_bf16 v[76:79], v[68:71], v[88:95], v104\n\t" \
  "v_mov_b64 v[46:47], 0\n\t" \
  "s_waitcnt lgkmcnt(5)\n\t" \
  "v_smfmac_f32_16x16x64_bf16 v[72:75], v[48:51], a[16:23], v104\n\t" \
  "ds_read_b128 v[4:7], v103 offset:1024\n\t" \
  "v_mov_b64 v[80:81], 0\n\t" \
  "v_smfmac_f32_16x16x64_bf16 v[72:75], v[52:55], a[64:71], v104\n\t" \
  "v_mov_b64 v[82:83], 0\n\t" \
  "s_waitcnt lgkmcnt(2)\n\t" \
  "v_smfmac_f32_16x16x64_bf16 v[72:75], v[56:59], a[112:119], v104\n\t" \
  "ds_read_b128 v[8:11], v103 offset:2048\n\t" \
  "v_mov_b64 v[84:85], 0\n\t" \
  "v_smfmac_f32_16x16x64_bf16 v[72:75], v[60:63], a[32:39], v104\n\t" \
  "ds_read_b128 v[12:15], v103 offset:3072\n\t" \
  "v_mov_b64 v[86:87], 0\n\t" \
  "v_smfmac_f32_16x16x64_bf16 v[72:75], v[32:35], a[80:87], v104\n\t" \
  "ds_read_b128 v[16:19], v103 offset:16384\n\t" \
  "v_mov_b64 v[88:89], 0\n\t" \
  "v_smfmac_f32_16x16x64_bf16 v[72:75], v[36:39], a[128:135], v104\n\t" \
  "ds_read_b128 v[20:23], v103 offset:17408\n\t" \
  "v_mov_b64 v[90:91], 0\n\t" \
  "v_smfmac_f32_16x16x64_bf16 v[76:79], v[48:51], a[24:31], v104\n\t" \
  "ds_read_b128 v[24:27], v103 offset:18432\n\t" \
  "v_mov_b64 v[92:93], 0\n\t" \
  "v_smfmac_f32_16x16x64_bf16 v[76:79], v[52:55], a[72:79], v104\n\t" \
  "v_add_f32 v96, v72, v74\n\t" \
  "v_add_f32 v97, v73, v75\n\t" \
  "v_cvt_pk_bf16_f32 v96, v96, v97\n\t" \
  "ds_write_b32 v105, v96\n\t" \
  "v_smfmac_f32_16x16x64_bf16 v[76:79], v[56:59], a[120:127], v104\n\t" \
  "ds_read_b128 v[28:31], v103 offset:19456\n\t" \
  "v_mov_b64 v[94:95], 0\n\t" \
  "v_smfmac_f32_16x16x64_bf16 v[76:79], v[60:63], a[40:47], v104\n\t" \
  "v_smfmac_f32_16x16x64_bf16 v[76:79], v[32:35], a[88:95], v104\n\t" \
  "v_smfmac_f32_16x16x64_bf16 v[76:79], v[36:39], a[136:143], v104\n\t" \
  "s_waitcnt lgkmcnt(1)\n\t" \
  "s_barrier\n\t" \
  "ds_read_b64_tr_b16 v[96:97], v107 offset:0\n\t" \
  "ds_read_b64_tr_b16 v[98:99], v107 offset:64\n\t" \
  "ds_read_b64_tr_b16 v[56:57], v107 offset:1024\n\t" \
  "ds_read_b64_tr_b16 v[58:59], v107 offset:1088\n\t" \
  "s_nop 1\n\t" \
  "v_add_f32 v116, v76, v78\n\t" \
  "v_add_f32 v117, v77, v79\n\t" \
  "v_cvt_pk_bf16_f32 v116, v116, v117\n\t" \
  "ds_write_b32 v105, v116 offset:256\n\t" \
  "s_waitcnt lgkmcnt(3)\n\t" \
  "v_smfmac_f32_16x16x64_bf16 v[40:43], v[96:99], a[192:199], v106\n\t" \
  "v_smfmac_f32_16x16x64_bf16 v[44:47], v[96:99], a[224:231], v106\n\t" \
  "s_waitcnt lgkmcnt(0)\n\t" \
  "s_barrier\n\t" \
  "ds_read_b64_tr_b16 v[52:53], v107 offset:256\n\t" \
  "ds_read_b64_tr_b16 v[54:55], v107 offset:320\n\t" \
  "v_smfmac_f32_16x16x64_bf16 v[40:43], v[56:59], a[208:215], v106\n\t" \
  "ds_read_b64_tr_b16 v[60:61], v107 offset:1280\n\t" \
  "ds_read_b64_tr_b16 v[62:63], v107 offset:1344\n\t" \
  "v_smfmac_f32_16x16x64_bf16 v[44:47], v[56:59], v[0:7], v106\n\t" \
  "v_smfmac_f32_16x16x64_bf16 v[80:83], v[96:99], a[240:247], v106\n\t" \
  "v_mov_b64 v[32:33], 0\n\t" \
  "v_smfmac_f32_16x16x64_bf16 v[84:87], v[96:99], v[136:143], v106\n\t" \
  "v_mov_b64 v[34:35], 0\n\t" \
  "v_smfmac_f32_16x16x64_bf16 v[80:83], v[56:59], v[120:127], v106\n\t" \
  "v_mov_b64 v[36:37], 0\n\t" \
  "v_smfmac_f32_16x16x64_bf16 v[84:87], v[56:59], v[16:23], v106\n\t" \
  "s_waitcnt lgkmcnt(2)\n\t" \
  "v_smfmac_f32_16x16x64_bf16 v[40:43], v[52:55], a[200:207], v106\n\t" \
  "v_smfmac_f32_16x16x64_bf16 v[44:47], v[52:55], a[232:239], v106\n\t" \
  "s_xor_b32 s76, s47, 0x40000\n\t" \
  "s_waitcnt lgkmcnt(0)\n\t" \
  "v_smfmac_f32_16x16x64_bf16 v[40:43], v[60:63], a[216:223], v106\n\t" \
  "v_mov_b64 v[38:39], 0\n\t" \
  "s_xor_b32 s77, s47, 0x80000\n\t" \
  "v_smfmac_f32_16x16x64_bf16 v[44:47], v[60:63], v[8:15], v106\n\t" \
  "s_xor_b32 s78, s47, 0xc0000\n\t" \
  "v_smfmac_f32_16x16x64_bf16 v[80:83], v[52:55], a[248:255], v106\n\t" \
  "ds_read_b128 v[0:3], v103 offset:32768\n\t" \
  "v_cvt_pk_bf16_f32 v64, v40, v41\n\t" \
  "v_smfmac_f32_16x16x64_bf16 v[84:87], v[52:55], v[144:151], v106\n\t" \
  "ds_read_b128 v[4:7], v103 offset:33792\n\t" \
  "v_cvt_pk_bf16_f32 v65, v42, v43\n\t" \
  "v_smfmac_f32_16x16x64_bf16 v[80:83], v[60:63], v[128:135], v106\n\t" \
  "ds_read_b128 v[8:11], v103 offset:34816\n\t" \
  "v_cvt_pk_bf16_f32 v66, v44, v45\n\t" \
  "v_smfmac_f32_16x16x64_bf16 v[84:87], v[60:63], v[24:31], v106\n\t" \
  "ds_read_b128 v[12:15], v103 offset:35840\n\t" \
  "v_cvt_pk_bf16_f32 v67, v46, v47\n\t" \
  "v_smfmac_f32_16x16x64_bf16 v[88:91], v[96:99], v[152:159], v106\n\t" \
  "v_permlane16_swap_b32 v64, v66\n\t" \
  "v_cvt_pk_bf16_f32 v68, v80, v81\n\t" \
  "v_smfmac_f32_16x16x64_bf16 v[92:95], v[96:99], v[184:191], v106\n\t" \
  "v_permlane16_swap_b32 v65, v67\n\t" \
  "v_cvt_pk_bf16_f32 v69, v82, v83\n\t" \
  "v_smfmac_f32_16x16x64_bf16 v[88:91], v[52:55], v[160:167], v106\n\t" \
  "v_cvt_pk_bf16_f32 v70, v84, v85\n\t" \
  "v_smfmac_f32_16x16x64_bf16 v[92:95], v[52:55], v[192:199], v106\n\t" \
  "v_cvt_pk_bf16_f32 v71, v86, v87\n\t" \
  "v_smfmac_f32_16x16x64_bf16 v[88:91], v[56:59], v[168:175], v106\n\t" \
  "ds_read_b128 v[16:19], v103 offset:49152\n\t" \
  "buffer_store_dwordx4 v[64:67], v108, %[ru], s47 offen sc1\n\t" \
  "v_permlane16_swap_b32 v68, v70\n\t" \
  "s_waitcnt lgkmcnt(3)\n\t" \
  "v_smfmac_f32_16x16x64_bf16 v[92:95], v[56:59], v[0:7], v106\n\t" \
  "ds_read_b128 v[20:23], v103 offset:50176\n\t" \
  "v_permlane16_swap_b32 v69, v71\n\t" \
  "v_smfmac_f32_16x16x64_bf16 v[88:91], v[60:63], v[176:183], v106\n\t" \
  "ds_read_b128 v[24:27], v103 offset:51200\n\t" \
  "s_waitcnt lgkmcnt(3)\n\t" \
  "v_smfmac_f32_16x16x64_bf16 v[92:95], v[60:63], v[8:15], v106\n\t" \
  "ds_read_b128 v[28:31], v103 offset:52224\n\t" \
  "v_smfmac_f32_16x16x64_bf16 v[32:35], v[96:99], v[200:207], v106\n\t" \
  "buffer_store_dwordx4 v[68:71], v108, %[ru], s76 offen sc1\n\t" \
  "v_cvt_pk_bf16_f32 v72, v88, v89\n\t" \
  "v_smfmac_f32_16x16x64_bf16 v[36:39], v[96:99], v[232:239], v106\n\t" \
  "v_cvt_pk_bf16_f32 v73, v90, v91\n\t" \
  "v_smfmac_f32_16x16x64_bf16 v[32:35], v[52:55], v[208:215], v106\n\t" \
  "v_cvt_pk_bf16_f32 v74, v92, v93\n\t" \
  "v_smfmac_f32_16x16x64_bf16 v[36:39], v[52:55], v[240:247], v106\n\t" \
  "v_cvt_pk_bf16_f32 v75, v94, v95\n\t" \
  "v_smfmac_f32_16x16x64_bf16 v[32:35], v[56:59], v[216:223], v106\n\t" \
  "v_permlane16_swap_b32 v72, v74\n\t" \
  "v_smfmac_f32_16x16x64_bf16 v[32:35], v[60:63], v[224:231], v106\n\t" \
  "v_permlane16_swap_b32 v73, v75\n\t" \
  "s_waitcnt lgkmcnt(2)\n\t" \
  "v_smfmac_f32_16x16x64_bf16 v[36:39], v[56:59], v[16:23], v106\n\t" \
  "s_waitcnt lgkmcnt(0)\n\t" \
  "v_smfmac_f32_16x16x64_bf16 v[36:39], v[60:63], v[24:31], v106\n\t" \
  "buffer_store_dwordx4 v[72:75], v108, %[ru], s77 offen sc1\n\t" \
  "v_cvt_pk_bf16_f32 v76, v32, v33\n\t" \
  "v_cvt_pk_bf16_f32 v77, v34, v35\n\t" \
  "s_nop 5\n\t" \
  "v_cvt_pk_bf16_f32 v78, v36, v37\n\t" \
  "v_cvt_pk_bf16_f32 v79, v38, v39\n\t" \
  "s_nop 0\n\t" \
  "v_permlane16_swap_b32 v76, v78\n\t" \
  "v_permlane16_swap_b32 v77, v79\n\t" \
  "buffer_store_dwordx4 v[76:79], v108, %[ru], s78 offen sc1\n\t" \
  "s_memtime s[76:77]\n\t" \
  "s_mov_b32 s42, 0\n\t" \
  "s_waitcnt lgkmcnt(0)\n\t" \
  "s_mov_b32 s57, s76\n\t" \
  "s_add_i32 s41, s56, 1950\n\t" \
  "s_sub_i32 s76, s92, s51\n\t" \
  "s_sub_i32 s77, s93, s53\n\t" \
  "s_cmp_eq_u32 s76, 0\n\t" \
  "s_cbranch_scc1 21f\n\t" \
  "v_cvt_f32_u32 v117, s76\n\t" \
  "v_cvt_f32_u32 v116, s77\n\t" \
  "v_rcp_f32 v117, v117\n\t" \
  "s_mov_b32 s51, s92\n\t" \
  "v_mul_f32 v117, v117, v116\n\t" \
  "v_max_f32 v117, 0x3f028f5c, v117\n\t" \
  "v_min_f32 v117, 0x3ffeb852, v117\n\t" \
  "v_rcp_f32 v116, v117\n\t" \
  "v_mul_f32 v117, 0x4f000000, v117\n\t" \
  "v_mul_f32 v116, 0x47800000, v116\n\t" \
  "v_cvt_u32_f32 v117, v117\n\t" \
  "v_cvt_u32_f32 v116, v116\n\t" \
  "s_mov_b32 s53, s93\n\t" \
  "v_readfirstlane_b32 s46, v117\n\t" \
  "v_readfirstlane_b32 s52, v116\n\t" \
  "21:\n\t" \
  "s_add_i32 s44, s44, 1\n\t" \
  "s_and_b32 s58, s58, 4\n\t" \
  "s_and_b32 s76, s44, 64\n\t" \
  "s_or_b32 s42, s42, s76\n\t" \
  "s_cbranch_scc0 20b\n\t" \
  "s_branch 99f\n\t" \
  "14:\n\t" \
  "s_sleep 3\n\t" \
  "s_add_u32 s59, s59, 0x10000\n\t" \
  "s_cmp_lt_u32 s59, 0x100000\n\t" \
  "s_cbranch_scc1 13b\n\t" \
  "s_mov_b64 exec, -1\n\t" \
  "s_mov_b32 s42, 16\n\t" \
  "s_branch 99f\n\t" \
  "8:\n\t" \
  "s_bitcmp1_b32 s58, 1\n\t" \
  "s_cbranch_scc1 4b\n\t" \
  "s_bitcmp1_b32 s58, 2\n\t" \
  "s_cbranch_scc1 5b\n\t" \
  "v_max3_u32 v116, v32, v33, v34\n\t" \
  "v_max3_u32 v117, v35, v36, v37\n\t" \
  "v_max3_u32 v116, v116, v38, v39\n\t" \
  "v_max_u32 v116, v116, v117\n\t" \
  "v_cmp_eq_u32 vcc, -1, v116\n\t" \
  "s_cbranch_vccz 5b\n\t" \
  "s_branch 0f\n\t" \
  "9:\n\t" \
  "s_bitcmp1_b32 s58, 2\n\t" \
  "s_cbranch_scc1 6b\n\t" \
  "v_max3_u32 v116, v40, v41, v42\n\t" \
  "v_max3_u32 v117, v43, v44, v45\n\t" \
  "v_max3_u32 v116, v116, v46, v47\n\t" \
  "v_max_u32 v116, v116, v117\n\t" \
  "v_cmp_eq_u32 vcc, -1, v116\n\t" \
  "s_cbranch_vccz 6b\n\t" \
  "s_or_b32 s58, s58, 2\n\t" \
  "0:\n\t" \
  "s_or_b32 s58, s58, 1\n\t" \
  "11:\n\t" \
  "buffer_load_dwordx4 v[32:35], v100, %[rh], s40 offen sc1\n\t" \
  "buffer_load_dwordx4 v[36:39], v100, %[rh], s76 offen sc1\n\t" \
  "buffer_load_dwordx4 v[40:43], v100, %[rh], s77 offen sc1\n\t" \
  "buffer_load_dwordx4 v[44:47], v100, %[rh], s78 offen sc1\n\t" \
  "s_waitcnt lgkmcnt(0)\n\t" \
  "s_branch 10b\n\t" \
  "17:\n\t" \
  "s_add_u32 s59, s59, 1\n\t" \
  "s_bitcmp1_b32 s58, 2\n\t" \
  "s_cbranch_scc1 3b\n\t" \
  "s_waitcnt vmcnt(0)\n\t" \
  "v_max_u32 v116, v32, v33\n\t" \
  "v_max3_u32 v116, v116, v34, v35\n\t" \
  "v_max3_u32 v116, v116, v36, v37\n\t" \
  "v_max3_u32 v116, v116, v38, v39\n\t" \
  "v_max3_u32 v116, v116, v40, v41\n\t" \
  "v_max3_u32 v116, v116, v42, v43\n\t" \
  "v_max3_u32 v116, v116, v44, v45\n\t" \
  "v_max3_u32 v116, v116, v46, v47\n\t" \
  "v_cmp_eq_u32 vcc, -1, v116\n\t" \
  "s_cbranch_vccz 3b\n\t" \
  "s_bitcmp1_b32 s59, 6\n\t" \
  "s_cbranch_scc1 12f\n\t" \
  "buffer_load_dwordx4 v[32:35], v100, %[rh], s40 offen sc1\n\t" \
  "buffer_load_dwordx4 v[36:39], v100, %[rh], s76 offen sc1\n\t" \
  "buffer_load_dwordx4 v[40:43], v100, %[rh], s77 offen sc1\n\t" \
  "buffer_load_dwordx4 v[44:47], v100, %[rh], s78 offen sc1\n\t" \
  "s_branch 17b\n\t" \
  "12:\n\t" \
  "s_mov_b32 s42, s58\n\t" \
  "s_branch 99f\n\t" \
  "99:\n\t" \
  "s_waitcnt vmcnt(0) lgkmcnt(0)\n\t" \
  ".Lchain_last:\n\t" \
  ""
#define CB_BODY \
  "s_cmp_gt_u32 %[wv], 1\n\t" \
  "s_cbranch_scc1 91f\n\t" \
  "s_mov_b32 s58, s42\n\t" \
  "s_mov_b32 s59, 0\n\t" \
  "s_bitcmp1_b32 s58, 4\n\t" \
  "s_cbranch_scc1 26f\n\t" \
  "s_bitcmp1_b32 s58, 3\n\t" \
  "s_cbranch_scc1 34f\n\t" \
  "s_bitcmp1_b32 s58, 1\n\t" \
  "s_cbranch_scc1 18f\n\t" \
  "17:\n\t" \
  "19:\n\t" \
  "s_add_i32 s85, s40, -1\n\t" \
  "s_and_b32 s82, s85, 2\n\t" \
  "s_lshl_b32 s82, s82, 13\n\t" \
  "s_and_b32 s80, s85, 1\n\t" \
  "s_lshl_b32 s80, s80, 12\n\t" \
  "s_add_i32 s82, s82, s80\n\t" \
  "s_add_i32 s82, s82, 0x84000\n\t" \
  "s_mov_b32 exec_hi, 0\n\t" \
  "buffer_load_dword v184, v200, %[rh], s82 offen sc1\n\t" \
  "buffer_load_dword v186, v203, %[ru], 0 offen sc1\n\t" \
  "s_mov_b64 exec, -1\n\t" \
  "20:\n\t" \
  "s_and_b32 s80, s40, 3\n\t" \
  "s_mul_i32 s77, s80, 0xc000\n\t" \
  "s_add_i32 s77, s77, 0x40000\n\t" \
  "s_lshl_b32 s78, s80, 14\n\t" \
  "s_add_i32 s78, s78, 0x70000\n\t" \
  "s_mov_b32 s57, 0\n\t" \
  "s_add_i32 s96, s77, 0x1000\n\t" \
  "s_lshl_b32 s88, s80, 20\n\t" \
  "s_add_i32 s88, s88, s48\n\t" \
  "s_lshr_b32 s80, s88, 21\n\t" \
  "s_bitcmp1_b64 %[sel], s80\n\t" \
  "s_cselect_b32 s80, 0x2000, 0\n\t" \
  "s_add_i32 s88, s88, s80\n\t" \
  "s_xor_b32 s89, s88, 0x800\n\t" \
  "s_xor_b32 s90, s88, 0x1000\n\t" \
  "s_xor_b32 s91, s88, 0x1800\n\t" \
  "s_add_i32 s92, s77, s50\n\t" \
  "s_xor_b32 s93, s92, 0x100\n\t" \
  "s_xor_b32 s94, s92, 0x200\n\t" \
  "s_xor_b32 s95, s92, 0x300\n\t" \
  "s_add_i32 s97, s40, 1\n\t" \
  "s_and_b32 s97, s97, 7\n\t" \
  "s_lshl_b32 s97, s97, 15\n\t" \
  "s_add_i32 s97, s97, s49\n\t" \
  "s_memtime vcc\n\t" \
  "s_memrealtime s[86:87]\n\t" \
  "s_nop 4\n\t" \
  "s_memrealtime s[60:61]\n\t" \
  "s_nop 4\n\t" \
  "s_memrealtime s[62:63]\n\t" \
  "s_nop 4\n\t" \
  "s_memrealtime s[64:65]\n\t" \
  "s_nop 4\n\t" \
  "s_memrealtime s[66:67]\n\t" \
  "s_nop 4\n\t" \
  "s_memrealtime s[68:69]\n\t" \
  "s_mov_b32 s79, 0\n\t" \
  "s_waitcnt lgkmcnt(0)\n\t" \
  "s_cmp_eq_u32 s86, s60\n\t" \
  "s_addc_u32 s79, s79, 0\n\t" \
  "s_cmp_eq_u32 s86, s62\n\t" \
  "s_addc_u32 s79, s79, 0\n\t" \
  "s_cmp_eq_u32 s86, s64\n\t" \
  "s_addc_u32 s79, s79, 0\n\t" \
  "s_cmp_eq_u32 s86, s66\n\t" \
  "s_addc_u32 s79, s79, 0\n\t" \
  "s_cmp_eq_u32 s86, s68\n\t" \
  "s_addc_u32 s79, s79, 0\n\t" \
  "s_mul_i32 s79, s79, 24\n\t" \
  "s_add_i32 s79, s79, vcc_lo\n\t" \
  "s_lshl4_add_u32 s80, s86, 64\n\t" \
  "s_sub_i32 s86, s79, s41\n\t" \
  "s_sub_i32 s87, s80, s43\n\t" \
  "s_cmp_lt_u32 s86, 4000\n\t" \
  "s_cbranch_scc1 21f\n\t" \
  "v_cvt_f32_u32 v165, s86\n\t" \
  "v_cvt_f32_u32 v164, s87\n\t" \
  "v_rcp_f32 v165, v165\n\t" \
  "s_nop 0\n\t" \
  "v_mul_f32 v165, v165, v164\n\t" \
  "v_max_f32 v165, 0x3f028f5c, v165\n\t" \
  "v_min_f32 v165, 0x3ffeb852, v165\n\t" \
  "v_rcp_f32 v164, v165\n\t" \
  "v_mul_f32 v165, 0x4f000000, v165\n\t" \
  "v_mul_f32 v164, 0x47800000, v164\n\t" \
  "v_cvt_u32_f32 v165, v165\n\t" \
  "v_cvt_u32_f32 v164, v164\n\t" \
  "s_nop 0\n\t" \
  "v_readfirstlane_b32 s45, v165\n\t" \
  "v_readfirstlane_b32 s44, v164\n\t" \
  "21:\n\t" \
  "s_mov_b32 s41, s79\n\t" \
  "s_mov_b32 s43, s80\n\t" \
  "s_lshl_b32 s83, s43, 8\n\t" \
  "s_mov_b32 s84, 570\n\t" \
  "s_mov_b32 exec_hi, 0\n\t" \
  "s_waitcnt vmcnt(0)\n\t" \
  "v_cmp_ne_u32_sdwa vcc, v184, s85 src0_sel:BYTE_0 src1_sel:DWORD\n\t" \
  "v_subrev_u32 v184, s83, v184\n\t" \
  "v_ashrrev_i32 v184, 8, v184\n\t" \
  "s_cbranch_vccnz 22f\n\t" \
  "s_nop 0\n\t" \
  "v_max_i32_dpp v184, v184, v184 quad_perm:[1,0,3,2] row_mask:0xf bank_mask:0xf\n\t" \
  "s_nop 1\n\t" \
  "v_max_i32_dpp v184, v184, v184 quad_perm:[2,3,0,1] row_mask:0xf bank_mask:0xf\n\t" \
  "s_nop 1\n\t" \
  "v_max_i32_dpp v184, v184, v184 row_half_mirror row_mask:0xf bank_mask:0xf\n\t" \
  "s_nop 1\n\t" \
  "v_max_i32_dpp v184, v184, v184 row_mirror row_mask:0xf bank_mask:0xf\n\t" \
  "s_nop 0\n\t" \
  "v_readlane_b32 s81, v184, 0\n\t" \
  "v_readlane_b32 s82, v184, 16\n\t" \
  "s_max_i32 s81, s81, s82\n\t" \
  "s_add_i32 s76, s81, s43\n\t" \
  "s_add_i32 s81, s81, s53\n\t" \
  "s_max_i32 s81, s81, 0\n\t" \
  "s_min_i32 s81, s81, 20000\n\t" \
  "s_mul_i32 s81, s81, s44\n\t" \
  "s_lshr_b32 s81, s81, 16\n\t" \
  "s_add_i32 s81, s81, -900\n\t" \
  "24:\n\t" \
  "s_add_i32 s81, s81, s41\n\t" \
  "1:\n\t" \
  "s_memtime vcc\n\t" \
  "s_waitcnt lgkmcnt(0)\n\t" \
  "s_sub_i32 s79, s81, vcc_lo\n\t" \
  "s_cmp_gt_i32 s79, 128\n\t" \
  "s_cbranch_scc1 1b\n\t" \
  ".p2align 4\n\t" \
  "2:\n\t" \
  "s_sub_i32 s79, s79, 33\n\t" \
  "s_cmp_gt_i32 s79, 56\n\t" \
  "s_cbranch_scc1 2b\n\t" \
  "buffer_load_dword v187, v200, %[rh], s78 offen sc1\n\t" \
  "s_memtime vcc\n\t" \
  "s_memrealtime s[86:87]\n\t" \
  "s_nop 4\n\t" \
  "s_memrealtime s[60:61]\n\t" \
  "s_nop 4\n\t" \
  "s_memrealtime s[62:63]\n\t" \
  "s_nop 4\n\t" \
  "s_memrealtime s[64:65]\n\t" \
  "s_nop 4\n\t" \
  "s_memrealtime s[66:67]\n\t" \
  "s_nop 4\n\t" \
  "s_memrealtime s[68:69]\n\t" \
  "s_mov_b32 s79, 0\n\t" \
  "s_waitcnt lgkmcnt(0)\n\t" \
  "s_cmp_eq_u32 s86, s60\n\t" \
  "s_addc_u32 s79, s79, 0\n\t" \
  "s_cmp_eq_u32 s86, s62\n\t" \
  "s_addc_u32 s79, s79, 0\n\t" \
  "s_cmp_eq_u32 s86, s64\n\t" \
  "s_addc_u32 s79, s79, 0\n\t" \
  "s_cmp_eq_u32 s86, s66\n\t" \
  "s_addc_u32 s79, s79, 0\n\t" \
  "s_cmp_eq_u32 s86, s68\n\t" \
  "s_addc_u32 s79, s79, 0\n\t" \
  "s_mul_i32 s79, s79, 24\n\t" \
  "s_add_i32 s79, s79, vcc_lo\n\t" \
  "s_lshl4_add_u32 s80, s86, 64\n\t" \
  "s_mov_b32 s41, s79\n\t" \
  "s_mov_b32 s43, s80\n\t" \
  "s_lshl_b32 s83, s43, 8\n\t" \
  "13:\n\t" \
  "s_waitcnt vmcnt(0)\n\t" \
  "s_memtime s[86:87]\n\t" \
  "v_cmp_ne_u32_sdwa vcc, v187, s40 src0_sel:BYTE_0 src1_sel:DWORD\n\t" \
  "v_subrev_u32 v184, s83, v187\n\t" \
  "v_ashrrev_i32 v184, 8, v184\n\t" \
  "s_nop 1\n\t" \
  "v_max_i32_dpp v184, v184, v184 quad_perm:[1,0,3,2] row_mask:0xf bank_mask:0xf\n\t" \
  "s_nop 1\n\t" \
  "v_max_i32_dpp v184, v184, v184 quad_perm:[2,3,0,1] row_mask:0xf bank_mask:0xf\n\t" \
  "s_cbranch_vccnz 14f\n\t" \
  "15:\n\t" \
  "s_nop 0\n\t" \
  "v_max_i32_dpp v184, v184, v184 row_half_mirror row_mask:0xf bank_mask:0xf\n\t" \
  "s_nop 1\n\t" \
  "v_max_i32_dpp v184, v184, v184 row_mirror row_mask:0xf bank_mask:0xf\n\t" \
  "s_mov_b64 exec, -1\n\t" \
  "v_readlane_b32 s57, v184, 0\n\t" \
  "v_readlane_b32 s82, v184, 16\n\t" \
  "s_waitcnt lgkmcnt(0)\n\t" \
  "s_sub_i32 s79, s41, s86\n\t" \
  "s_max_i32 s57, s57, s82\n\t" \
  "s_add_i32 s81, s57, 212\n\t" \
  "s_max_i32 s81, s81, 0\n\t" \
  "s_min_i32 s81, s81, 20000\n\t" \
  "s_mul_i32 s81, s81, s44\n\t" \
  "s_lshr_b32 s81, s81, 16\n\t" \
  "s_add_i32 s79, s79, s81\n\t" \
  "s_add_i32 s79, s79, -310\n\t" \
  ".p2align 4\n\t" \
  "3:\n\t" \
  "s_sub_i32 s79, s79, 33\n\t" \
  "s_cmp_gt_i32 s79, 56\n\t" \
  "s_cbranch_scc1 3b\n\t" \
  "buffer_load_dwordx4 v[72:75], v192, %[ru], s88 offen offset:0 sc1\n\t" \
  "s_nop 4\n\t" \
  "buffer_load_dwordx4 v[76:79], v192, %[ru], s88 offen offset:1024 sc1\n\t" \
  "s_nop 4\n\t" \
  "s_add_i32 s79, s79, 12\n\t" \
  ".p2align 4\n\t" \
  "4:\n\t" \
  "s_sub_i32 s79, s79, 33\n\t" \
  "s_cmp_gt_i32 s79, 56\n\t" \
  "s_cbranch_scc1 4b\n\t" \
  "buffer_load_dwordx4 v[80:83], v192, %[ru], s89 offen offset:0 sc1\n\t" \
  "s_nop 4\n\t" \
  "buffer_load_dwordx4 v[84:87], v192, %[ru], s89 offen offset:1024 sc1\n\t" \
  "s_nop 4\n\t" \
  "s_add_i32 s79, s79, 22\n\t" \
  ".p2align 4\n\t" \
  "5:\n\t" \
  "s_sub_i32 s79, s79, 33\n\t" \
  "s_cmp_gt_i32 s79, 56\n\t" \
  "s_cbranch_scc1 5b\n\t" \
  "buffer_load_dwordx4 v[88:91], v192, %[ru], s90 offen offset:0 sc1\n\t" \
  "buffer_load_dwordx4 v[92:95], v192, %[ru], s90 offen offset:1024 sc1\n\t" \
  "s_add_i32 s79, s79, 102\n\t" \
  ".p2align 4\n\t" \
  "9:\n\t" \
  "s_sub_i32 s79, s79, 33\n\t" \
  "s_cmp_gt_i32 s79, 56\n\t" \
  "s_cbranch_scc1 9b\n\t" \
  "buffer_load_dwordx4 v[104:107], v194, %[rh], s92 offen sc1\n\t" \
  "s_nop 4\n\t" \
  "buffer_load_dwordx4 v[108:111], v194, %[rh], s93 offen sc1\n\t" \
  "s_nop 4\n\t" \
  "buffer_load_dwordx4 v[112:115], v194, %[rh], s94 offen sc1\n\t" \
  "s_nop 4\n\t" \
  "buffer_load_dwordx4 v[116:119], v194, %[rh], s95 offen sc1\n\t" \
  "s_nop 4\n\t" \
  "buffer_load_dwordx4 v[96:99], v192, %[ru], s91 offen offset:0 sc1\n\t" \
  "s_nop 4\n\t" \
  "buffer_load_dwordx4 v[100:103], v192, %[ru], s91 offen offset:1024 sc1\n\t" \
  "s_nop 4\n\t" \
  "10:\n\t" \
  "s_waitcnt vmcnt(5)\n\t" \
  "v_mfma_f32_16x16x32_bf16 v[136:139], v[104:107], v[72:75], 0\n\t" \
  "v_mfma_f32_16x16x32_bf16 v[140:143], v[104:107], v[76:79], 0\n\t" \
  "s_waitcnt vmcnt(4)\n\t" \
  "v_mfma_f32_16x16x32_bf16 v[136:139], v[108:111], v[80:83], v[136:139]\n\t" \
  "v_mfma_f32_16x16x32_bf16 v[140:143], v[108:111], v[84:87], v[140:143]\n\t" \
  "s_waitcnt vmcnt(3)\n\t" \
  "v_mfma_f32_16x16x32_bf16 v[136:139], v[112:115], v[88:91], v[136:139]\n\t" \
  "v_mfma_f32_16x16x32_bf16 v[140:143], v[112:115], v[92:95], v[140:143]\n\t" \
  "s_waitcnt vmcnt(1)\n\t" \
  "v_mfma_f32_16x16x32_bf16 v[136:139], v[116:119], v[96:99], v[136:139]\n\t" \
  "s_waitcnt vmcnt(0)\n\t" \
  "v_mfma_f32_16x16x32_bf16 v[140:143], v[116:119], v[100:103], v[140:143]\n\t" \
  "s_barrier\n\t" \
  "ds_read2st64_b64 v[144:147], v195 offset1:1\n\t" \
  "s_nop 1\n\t" \
  "v_permlane32_swap_b32 v136, v138\n\t" \
  "v_permlane32_swap_b32 v137, v139\n\t" \
  "s_nop 1\n\t" \
  "v_permlane32_swap_b32 v140, v142\n\t" \
  "v_permlane32_swap_b32 v141, v143\n\t" \
  "v_pk_add_f32 v[136:137], v[136:137], v[142:143]\n\t" \
  "s_waitcnt lgkmcnt(0)\n\t" \
  "v_pk_add_f32 v[136:137], v[136:137], v[144:145]\n\t" \
  "v_pk_add_f32 v[136:137], v[136:137], v[146:147]\n\t" \
  "v_cvt_pk_bf16_f32 v148, v136, v137\n\t" \
  "s_nop 1\n\t" \
  "v_mov_b32_dpp v149, v148 row_half_mirror row_mask:0xf bank_mask:0xf\n\t" \
  "v_perm_b32 v150, v148, v149, v205\n\t" \
  "v_cmpx_o_f16_sdwa vcc, v150, v150 src0_sel:WORD_0 src1_sel:WORD_1\n\t" \
  "6:\n\t" \
  "buffer_store_dword v150, v201, %[rh], s97 offen sc1\n\t" \
  "s_memtime s[82:83]\n\t" \
  "s_mov_b64 exec, -1\n\t" \
  "s_cmp_eq_u64 vcc, -1\n\t" \
  "s_cbranch_scc0 8f\n\t" \
  "v_mov_b32 v151, 0\n\t" \
  "5:\n\t" \
  "ds_write_b32 v206, v151\n\t" \
  "s_waitcnt lgkmcnt(0)\n\t" \
  "s_mov_b32 s56, s82\n\t" \
  "s_barrier\n\t" \
  "ds_read2_b32 v[148:149], v207 offset1:8\n\t" \
  "s_waitcnt lgkmcnt(0)\n\t" \
  "v_or_b32 v148, v148, v149\n\t" \
  "v_cmp_ne_u32 vcc, 0, v148\n\t" \
  "s_cbranch_vccnz 12f\n\t" \
  "s_and_b32 s58, s58, 4\n\t" \
  "s_add_i32 s80, s57, s43\n\t" \
  "s_sub_i32 s80, s80, s76\n\t" \
  "s_add_i32 s53, s53, -12\n\t" \
  "s_max_i32 s80, s80, s53\n\t" \
  "s_add_i32 s53, s53, 24\n\t" \
  "s_min_i32 s53, s53, s80\n\t" \
  "25:\n\t" \
  "s_add_i32 s82, s40, 2\n\t" \
  "s_min_i32 s83, s82, 63\n\t" \
  "s_mov_b32 exec_hi, 0\n\t" \
  "v_cmp_gt_i32 vcc, s83, v186\n\t" \
  "s_mov_b64 exec, -1\n\t" \
  "s_and_b32 s81, s82, 15\n\t" \
  "s_lshl_b32 s81, s81, 22\n\t" \
  "s_add_i32 s81, s81, s51\n\t" \
  "s_lshr_b32 s80, s81, 21\n\t" \
  "s_bitcmp0_b64 %[sel], s80\n\t" \
  "s_cselect_b32 s80, 0x2000, 0\n\t" \
  "s_add_i32 s81, s81, s80\n\t" \
  "s_cbranch_vccnz 27f\n\t" \
  "28:\n\t" \
  "s_cmp_ge_i32 s40, 62\n\t" \
  "s_cbranch_scc1 37f\n\t" \
  "s_and_b32 s76, s40, 3\n\t" \
  "s_lshl_b32 s76, s76, 18\n\t" \
  "buffer_load_dwordx4 v[0:3], v210, %[ru], s81 offen offset:0 sc1\n\t" \
  "buffer_load_dwordx4 v[4:7], v210, %[ru], s81 offen offset:1024 sc1\n\t" \
  "s_add_i32 s81, s81, 0x20000\n\t" \
  "buffer_load_dwordx4 v[8:11], v210, %[ru], s81 offen offset:0 sc1\n\t" \
  "buffer_load_dwordx4 v[12:15], v210, %[ru], s81 offen offset:1024 sc1\n\t" \
  "s_add_i32 s81, s81, 0x20000\n\t" \
  "buffer_load_dwordx4 v[16:19], v210, %[ru], s81 offen offset:0 sc1\n\t" \
  "buffer_load_dwordx4 v[20:23], v210, %[ru], s81 offen offset:1024 sc1\n\t" \
  "s_add_i32 s81, s81, 0x20000\n\t" \
  "buffer_load_dwordx4 v[24:27], v210, %[ru], s81 offen offset:0 sc1\n\t" \
  "buffer_load_dwordx4 v[28:31], v210, %[ru], s81 offen offset:1024 sc1\n\t" \
  "s_add_i32 s81, s81, 0x20000\n\t" \
  "s_bitcmp1_b32 s58, 4\n\t" \
  "s_cbranch_scc1 29f\n\t" \
  "s_sub_i32 s80, s56, s41\n\t" \
  "s_mul_hi_u32 s80, s80, s45\n\t" \
  "s_lshl_b32 s80, s80, 1\n\t" \
  "s_sub_i32 s80, s80, s57\n\t" \
  "s_min_u32 s47, s47, s46\n\t" \
  "s_min_u32 s47, s47, s80\n\t" \
  "v_mov_b32 v188, s47\n\t" \
  "s_mov_b32 s47, s46\n\t" \
  "s_mov_b32 s46, s80\n\t" \
  "buffer_store_dword v188, v202, %[rh], s52 offen sc1\n\t" \
  "v_mov_b64 v[188:189], -1\n\t" \
  "v_mov_b64 v[190:191], -1\n\t" \
  "v_mov_b32 v187, s40\n\t" \
  "s_add_i32 s80, s40, -1\n\t" \
  "s_and_b32 s80, s80, 7\n\t" \
  "s_lshl_b32 s80, s80, 15\n\t" \
  "s_add_i32 s80, s80, s49\n\t" \
  "buffer_store_dwordx4 v[188:191], v208, %[rh], s80 offen sc1\n\t" \
  "buffer_store_dword v187, v209, %[ru], 0 offen sc1\n\t" \
  "ds_write_b128 v197, v[72:75] offset:16384\n\t" \
  "ds_write_b128 v197, v[76:79] offset:17408\n\t" \
  "ds_write_b128 v197, v[80:83] offset:18432\n\t" \
  "ds_write_b128 v197, v[84:87] offset:19456\n\t" \
  "ds_write_b128 v197, v[88:91] offset:20480\n\t" \
  "ds_write_b128 v197, v[92:95] offset:21504\n\t" \
  "ds_write_b128 v197, v[96:99] offset:22528\n\t" \
  "ds_write_b128 v197, v[100:103] offset:23552\n\t" \
  "29:\n\t" \
  "s_and_b32 s58, s58, 36\n\t" \
  "buffer_load_dwordx4 v[32:35], v210, %[ru], s81 offen offset:0 sc1\n\t" \
  "buffer_load_dwordx4 v[36:39], v210, %[ru], s81 offen offset:1024 sc1\n\t" \
  "s_add_i32 s81, s81, 0x20000\n\t" \
  "buffer_load_dwordx4 v[40:43], v210, %[ru], s81 offen offset:0 sc1\n\t" \
  "buffer_load_dwordx4 v[44:47], v210, %[ru], s81 offen offset:1024 sc1\n\t" \
  "s_add_i32 s81, s81, 0x20000\n\t" \
  "buffer_load_dwordx4 a[0:3], v211, %[rh], s76 offen offset:0 sc1\n\t" \
  "buffer_load_dwordx4 a[4:7], v211, %[rh], s76 offen offset:1024 sc1\n\t" \
  "buffer_load_dwordx4 v[48:51], v210, %[ru], s81 offen offset:0 sc1\n\t" \
  "buffer_load_dwordx4 v[52:55], v210, %[ru], s81 offen offset:1024 sc1\n\t" \
  "s_add_i32 s81, s81, 0x20000\n\t" \
  "buffer_load_dwordx4 v[56:59], v210, %[ru], s81 offen offset:0 sc1\n\t" \
  "buffer_load_dwordx4 v[60:63], v210, %[ru], s81 offen offset:1024 sc1\n\t" \
  "s_add_i32 s81, s81, 0x20000\n\t" \
  "buffer_load_dwordx4 a[8:11], v212, %[rh], s76 offen offset:0 sc1\n\t" \
  "buffer_load_dwordx4 a[12:15], v212, %[rh], s76 offen offset:1024 sc1\n\t" \
  "s_waitcnt lgkmcnt(0)\n\t" \
  "s_barrier\n\t" \
  "buffer_load_dwordx4 v[64:67], v210, %[ru], s81 offen offset:0 sc1\n\t" \
  "buffer_load_dwordx4 v[68:71], v210, %[ru], s81 offen offset:1024 sc1\n\t" \
  "s_add_i32 s81, s81, 0x20000\n\t" \
  "buffer_load_dwordx4 v[104:107], v210, %[ru], s81 offen offset:0 sc1\n\t" \
  "buffer_load_dwordx4 v[108:111], v210, %[ru], s81 offen offset:1024 sc1\n\t" \
  "s_add_i32 s81, s81, 0x20000\n\t" \
  "buffer_load_dwordx4 a[16:19], v213, %[rh], s76 offen offset:0 sc1\n\t" \
  "buffer_load_dwordx4 a[20:23], v213, %[rh], s76 offen offset:1024 sc1\n\t" \
  "buffer_load_dwordx4 v[112:115], v210, %[ru], s81 offen offset:0 sc1\n\t" \
  "buffer_load_dwordx4 v[116:119], v210, %[ru], s81 offen offset:1024 sc1\n\t" \
  "s_add_i32 s81, s81, 0x20000\n\t" \
  "buffer_load_dwordx4 v[120:123], v210, %[ru], s81 offen offset:0 sc1\n\t" \
  "buffer_load_dwordx4 v[124:127], v210, %[ru], s81 offen offset:1024 sc1\n\t" \
  "s_add_i32 s81, s81, 0x20000\n\t" \
  "buffer_load_dwordx4 a[24:27], v214, %[rh], s76 offen offset:0 sc1\n\t" \
  "buffer_load_dwordx4 a[28:31], v214, %[rh], s76 offen offset:1024 sc1\n\t" \
  "ds_read_b128 v[160:163], v193 offset:0\n\t" \
  "ds_read_b128 v[164:167], v193 offset:256\n\t" \
  "ds_read_b128 v[168:171], v193 offset:2048\n\t" \
  "ds_read_b128 v[172:175], v193 offset:2304\n\t" \
  "ds_read_b128 v[176:179], v193 offset:4096\n\t" \
  "ds_read_b128 v[180:183], v193 offset:4352\n\t" \
  "s_waitcnt lgkmcnt(2)\n\t" \
  "v_pk_add_f32 v[160:161], v[160:161], v[168:169]\n\t" \
  "v_pk_add_f32 v[162:163], v[162:163], v[170:171]\n\t" \
  "v_pk_add_f32 v[164:165], v[164:165], v[172:173]\n\t" \
  "v_pk_add_f32 v[166:167], v[166:167], v[174:175]\n\t" \
  "s_waitcnt lgkmcnt(0)\n\t" \
  "v_pk_add_f32 v[160:161], v[160:161], v[176:177]\n\t" \
  "v_pk_add_f32 v[162:163], v[162:163], v[178:179]\n\t" \
  "v_pk_add_f32 v[164:165], v[164:165], v[180:181]\n\t" \
  "v_pk_add_f32 v[166:167], v[166:167], v[182:183]\n\t" \
  "v_cvt_pk_bf16_f32 v160, v160, v161\n\t" \
  "v_cvt_pk_bf16_f32 v161, v162, v163\n\t" \
  "v_cvt_pk_bf16_f32 v162, v164, v165\n\t" \
  "v_cvt_pk_bf16_f32 v163, v166, v167\n\t" \
  "buffer_load_dwordx4 v[128:131], v210, %[ru], s81 offen offset:0 sc1\n\t" \
  "buffer_load_dwordx4 v[132:135], v210, %[ru], s81 offen offset:1024 sc1\n\t" \
  "s_add_i32 s81, s81, 0x20000\n\t" \
  "buffer_load_dwordx4 v[136:139], v210, %[ru], s81 offen offset:0 sc1\n\t" \
  "buffer_load_dwordx4 v[140:143], v210, %[ru], s81 offen offset:1024 sc1\n\t" \
  "s_add_i32 s81, s81, 0x20000\n\t" \
  "s_waitcnt vmcnt(32)\n\t" \
  "v_swap_b32 v1, v8\n\t" \
  "v_swap_b32 v3, v10\n\t" \
  "v_swap_b32 v5, v12\n\t" \
  "v_swap_b32 v7, v14\n\t" \
  "ds_write_b128 v204, v[0:3] offset:0\n\t" \
  "ds_write_b128 v204, v[8:11] offset:1024\n\t" \
  "ds_write_b128 v204, v[4:7] offset:2048\n\t" \
  "ds_write_b128 v204, v[12:15] offset:3072\n\t" \
  "buffer_load_dwordx4 v[144:147], v210, %[ru], s81 offen offset:0 sc1\n\t" \
  "buffer_load_dwordx4 v[148:151], v210, %[ru], s81 offen offset:1024 sc1\n\t" \
  "s_add_i32 s81, s81, 0x20000\n\t" \
  "buffer_load_dwordx4 v[152:155], v210, %[ru], s81 offen offset:0 sc1\n\t" \
  "buffer_load_dwordx4 v[156:159], v210, %[ru], s81 offen offset:1024 sc1\n\t" \
  "s_add_i32 s81, s81, 0x20000\n\t" \
  "s_waitcnt vmcnt(32)\n\t" \
  "v_swap_b32 v17, v24\n\t" \
  "v_swap_b32 v19, v26\n\t" \
  "v_swap_b32 v21, v28\n\t" \
  "v_swap_b32 v23, v30\n\t" \
  "ds_write_b128 v204, v[16:19] offset:4096\n\t" \
  "ds_write_b128 v204, v[24:27] offset:5120\n\t" \
  "ds_write_b128 v204, v[20:23] offset:6144\n\t" \
  "ds_write_b128 v204, v[28:31] offset:7168\n\t" \
  "s_waitcnt vmcnt(28)\n\t" \
  "v_swap_b32 v33, v40\n\t" \
  "v_swap_b32 v35, v42\n\t" \
  "v_swap_b32 v37, v44\n\t" \
  "v_swap_b32 v39, v46\n\t" \
  "ds_write_b128 v204, v[32:35] offset:8192\n\t" \
  "ds_write_b128 v204, v[40:43] offset:9216\n\t" \
  "ds_write_b128 v204, v[36:39] offset:10240\n\t" \
  "ds_write_b128 v204, v[44:47] offset:11264\n\t" \
  "s_waitcnt vmcnt(22)\n\t" \
  "v_swap_b32 v49, v56\n\t" \
  "v_swap_b32 v51, v58\n\t" \
  "v_swap_b32 v53, v60\n\t" \
  "v_swap_b32 v55, v62\n\t" \
  "ds_write_b128 v204, v[48:51] offset:12288\n\t" \
  "ds_write_b128 v204, v[56:59] offset:13312\n\t" \
  "ds_write_b128 v204, v[52:55] offset:14336\n\t" \
  "ds_write_b128 v204, v[60:63] offset:15360\n\t" \
  "s_waitcnt vmcnt(16)\n\t" \
  "v_swap_b32 v65, v104\n\t" \
  "v_swap_b32 v67, v106\n\t" \
  "v_swap_b32 v69, v108\n\t" \
  "v_swap_b32 v71, v110\n\t" \
  "ds_write_b128 v204, v[64:67] offset:16384\n\t" \
  "ds_write_b128 v204, v[104:107] offset:17408\n\t" \
  "ds_write_b128 v204, v[68:71] offset:18432\n\t" \
  "ds_write_b128 v204, v[108:111] offset:19456\n\t" \
  "s_waitcnt vmcnt(10)\n\t" \
  "v_swap_b32 v113, v120\n\t" \
  "v_swap_b32 v115, v122\n\t" \
  "v_swap_b32 v117, v124\n\t" \
  "v_swap_b32 v119, v126\n\t" \
  "ds_write_b128 v204, v[112:115] offset:20480\n\t" \
  "ds_write_b128 v204, v[120:123] offset:21504\n\t" \
  "ds_write_b128 v204, v[116:119] offset:22528\n\t" \
  "ds_write_b128 v204, v[124:127] offset:23552\n\t" \
  "s_waitcnt vmcnt(4)\n\t" \
  "v_swap_b32 v129, v136\n\t" \
  "v_swap_b32 v131, v138\n\t" \
  "v_swap_b32 v133, v140\n\t" \
  "v_swap_b32 v135, v142\n\t" \
  "ds_write_b128 v204, v[128:131] offset:24576\n\t" \
  "ds_write_b128 v204, v[136:139] offset:25600\n\t" \
  "ds_write_b128 v204, v[132:135] offset:26624\n\t" \
  "ds_write_b128 v204, v[140:143] offset:27648\n\t" \
  "s_waitcnt vmcnt(0)\n\t" \
  "v_swap_b32 v145, v152\n\t" \
  "v_swap_b32 v147, v154\n\t" \
  "v_swap_b32 v149, v156\n\t" \
  "v_swap_b32 v151, v158\n\t" \
  "ds_write_b128 v204, v[144:147] offset:28672\n\t" \
  "ds_write_b128 v204, v[152:155] offset:29696\n\t" \
  "ds_write_b128 v204, v[148:151] offset:30720\n\t" \
  "ds_write_b128 v204, v[156:159] offset:31744\n\t" \
  "s_waitcnt lgkmcnt(0)\n\t" \
  "s_cmp_lt_i32 s40, 0\n\t" \
  "s_cbranch_scc1 39f\n\t" \
  "38:\n\t" \
  "s_add_i32 s80, s40, -2\n\t" \
  "s_lshl_b32 s80, s80, 17\n\t" \
  "s_min_u32 s80, s80, 0x70000000\n\t" \
  "buffer_store_dwordx4 v[160:163], v196, %[ro], s80 offen sc1\n\t" \
  "ds_read_b128 a[32:35], v198 offset:0\n\t" \
  "ds_read_b128 a[36:39], v198 offset:1024\n\t" \
  "ds_read_b128 a[40:43], v198 offset:2048\n\t" \
  "ds_read_b128 a[44:47], v198 offset:3072\n\t" \
  "ds_read_b128 a[48:51], v198 offset:4096\n\t" \
  "ds_read_b128 a[52:55], v198 offset:5120\n\t" \
  "ds_read_b128 a[56:59], v198 offset:6144\n\t" \
  "ds_read_b128 a[60:63], v198 offset:7168\n\t" \
  "ds_read_b128 a[64:67], v198 offset:8192\n\t" \
  "ds_read_b128 a[68:71], v198 offset:9216\n\t" \
  "ds_read_b128 a[72:75], v198 offset:10240\n\t" \
  "ds_read_b128 a[76:79], v198 offset:11264\n\t" \
  "ds_read_b128 a[80:83], v198 offset:12288\n\t" \
  "ds_read_b128 a[84:87], v198 offset:13312\n\t" \
  "ds_read_b128 a[88:91], v198 offset:14336\n\t" \
  "ds_read_b128 a[92:95], v198 offset:15360\n\t" \
  "s_waitcnt vmcnt(1) lgkmcnt(0)\n\t" \
  "31:\n\t" \
  "v_mfma_f32_16x16x32_bf16 v[168:171], a[32:35], a[0:3], 0\n\t" \
  "v_mfma_f32_16x16x32_bf16 v[172:175], a[64:67], a[0:3], 0\n\t" \
  "v_mfma_f32_16x16x32_bf16 v[168:171], a[36:39], a[4:7], v[168:171]\n\t" \
  "v_mfma_f32_16x16x32_bf16 v[172:175], a[68:71], a[4:7], v[172:175]\n\t" \
  "v_mfma_f32_16x16x32_bf16 v[168:171], a[40:43], a[8:11], v[168:171]\n\t" \
  "v_mfma_f32_16x16x32_bf16 v[172:175], a[72:75], a[8:11], v[172:175]\n\t" \
  "v_mfma_f32_16x16x32_bf16 v[168:171], a[44:47], a[12:15], v[168:171]\n\t" \
  "v_mfma_f32_16x16x32_bf16 v[172:175], a[76:79], a[12:15], v[172:175]\n\t" \
  "v_mfma_f32_16x16x32_bf16 v[168:171], a[48:51], a[16:19], v[168:171]\n\t" \
  "v_mfma_f32_16x16x32_bf16 v[172:175], a[80:83], a[16:19], v[172:175]\n\t" \
  "v_mfma_f32_16x16x32_bf16 v[168:171], a[52:55], a[20:23], v[168:171]\n\t" \
  "v_mfma_f32_16x16x32_bf16 v[172:175], a[84:87], a[20:23], v[172:175]\n\t" \
  "v_mfma_f32_16x16x32_bf16 v[168:171], a[56:59], a[24:27], v[168:171]\n\t" \
  "v_mfma_f32_16x16x32_bf16 v[172:175], a[88:91], a[24:27], v[172:175]\n\t" \
  "v_mfma_f32_16x16x32_bf16 v[168:171], a[60:63], a[28:31], v[168:171]\n\t" \
  "v_mfma_f32_16x16x32_bf16 v[172:175], a[92:95], a[28:31], v[172:175]\n\t" \
  "s_nop 7\n\t" \
  "39:\n\t" \
  "s_add_i32 s40, s40, 2\n\t" \
  "v_cmp_u_f32 vcc, v168, v168\n\t" \
  "s_cbranch_vccnz 32f\n\t" \
  "33:\n\t" \
  "s_barrier\n\t" \
  "s_add_i32 s85, s40, -1\n\t" \
  "s_cmp_ge_i32 s40, 64\n\t" \
  "s_cbranch_scc1 47f\n\t" \
  "s_and_b32 s82, s85, 2\n\t" \
  "s_lshl_b32 s82, s82, 13\n\t" \
  "s_and_b32 s80, s85, 1\n\t" \
  "s_lshl_b32 s80, s80, 12\n\t" \
  "s_add_i32 s82, s82, s80\n\t" \
  "s_add_i32 s82, s82, 0x84000\n\t" \
  "s_mov_b32 exec_hi, 0\n\t" \
  "buffer_load_dword v184, v200, %[rh], s82 offen sc1\n\t" \
  "buffer_load_dword v186, v203, %[ru], 0 offen sc1\n\t" \
  "s_mov_b64 exec, -1\n\t" \
  "47:\n\t" \
  "ds_write_b128 v199, v[168:171] offset:0\n\t" \
  "ds_write_b128 v199, v[172:175] offset:1024\n\t" \
  "s_mov_b32 s59, 0\n\t" \
  "s_and_b32 s58, s58, 4\n\t" \
  "s_cmp_lt_i32 s40, 64\n\t" \
  "s_cbranch_scc1 20b\n\t" \
  "s_waitcnt lgkmcnt(0)\n\t" \
  "s_barrier\n\t" \
  "ds_read_b128 v[160:163], v193 offset:0\n\t" \
  "ds_read_b128 v[164:167], v193 offset:256\n\t" \
  "ds_read_b128 v[168:171], v193 offset:2048\n\t" \
  "ds_read_b128 v[172:175], v193 offset:2304\n\t" \
  "ds_read_b128 v[176:179], v193 offset:4096\n\t" \
  "ds_read_b128 v[180:183], v193 offset:4352\n\t" \
  "s_waitcnt lgkmcnt(2)\n\t" \
  "v_pk_add_f32 v[160:161], v[160:161], v[168:169]\n\t" \
  "v_pk_add_f32 v[162:163], v[162:163], v[170:171]\n\t" \
  "v_pk_add_f32 v[164:165], v[164:165], v[172:173]\n\t" \
  "v_pk_add_f32 v[166:167], v[166:167], v[174:175]\n\t" \
  "s_waitcnt lgkmcnt(0)\n\t" \
  "v_pk_add_f32 v[160:161], v[160:161], v[176:177]\n\t" \
  "v_pk_add_f32 v[162:163], v[162:163], v[178:179]\n\t" \
  "v_pk_add_f32 v[164:165], v[164:165], v[180:181]\n\t" \
  "v_pk_add_f32 v[166:167], v[166:167], v[182:183]\n\t" \
  "v_cvt_pk_bf16_f32 v160, v160, v161\n\t" \
  "v_cvt_pk_bf16_f32 v161, v162, v163\n\t" \
  "v_cvt_pk_bf16_f32 v162, v164, v165\n\t" \
  "v_cvt_pk_bf16_f32 v163, v166, v167\n\t" \
  "s_add_i32 s80, s40, -2\n\t" \
  "s_lshl_b32 s80, s80, 17\n\t" \
  "s_min_u32 s80, s80, 0x70000000\n\t" \
  "buffer_store_dwordx4 v[160:163], v196, %[ro], s80 offen sc1\n\t" \
  "s_mov_b32 s58, 64\n\t" \
  "s_branch 7f\n\t" \
  "22:\n\t" \
  "s_mov_b32 s81, 0\n\t" \
  "s_cmp_eq_u32 s40, 0\n\t" \
  "s_cbranch_scc1 24b\n\t" \
  "s_bitcmp1_b32 s58, 2\n\t" \
  "s_cbranch_scc1 24b\n\t" \
  "s_mov_b64 exec, -1\n\t" \
  "s_sleep 10\n\t" \
  "s_add_u32 s59, s59, 0x10000\n\t" \
  "s_cmp_lt_u32 s59, 0x100000\n\t" \
  "s_cbranch_scc1 19b\n\t" \
  "s_or_b32 s58, s58, 1\n\t" \
  "s_branch 7f\n\t" \
  "14:\n\t" \
  "v_mov_b32 v184, 0\n\t" \
  "s_bitcmp1_b32 s58, 2\n\t" \
  "s_nop 0\n\t" \
  "s_cbranch_scc1 15b\n\t" \
  "s_mov_b32 s84, 870\n\t" \
  "s_add_u32 s59, s59, 0x10000\n\t" \
  "s_cmp_lt_u32 s59, 0x100000\n\t" \
  "s_mov_b64 exec, vcc\n\t" \
  "buffer_load_dword v187, v200, %[rh], s78 offen sc1\n\t" \
  "s_mov_b32 exec_lo, -1\n\t" \
  "s_cbranch_scc1 13b\n\t" \
  "s_mov_b64 exec, -1\n\t" \
  "s_or_b32 s58, s58, 1\n\t" \
  "s_branch 7f\n\t" \
  "27:\n\t" \
  "s_bitcmp1_b32 s58, 2\n\t" \
  "s_cbranch_scc1 28b\n\t" \
  "s_bitcmp1_b32 s58, 4\n\t" \
  "s_cbranch_scc1 23f\n\t" \
  "s_sub_i32 s80, s56, s41\n\t" \
  "s_mul_hi_u32 s80, s80, s45\n\t" \
  "s_lshl_b32 s80, s80, 1\n\t" \
  "s_sub_i32 s80, s80, s57\n\t" \
  "s_min_u32 s47, s47, s46\n\t" \
  "s_min_u32 s47, s47, s80\n\t" \
  "v_mov_b32 v188, s47\n\t" \
  "s_mov_b32 s47, s46\n\t" \
  "s_mov_b32 s46, s80\n\t" \
  "buffer_store_dword v188, v202, %[rh], s52 offen sc1\n\t" \
  "v_mov_b64 v[188:189], -1\n\t" \
  "v_mov_b64 v[190:191], -1\n\t" \
  "v_mov_b32 v187, s40\n\t" \
  "s_add_i32 s80, s40, -1\n\t" \
  "s_and_b32 s80, s80, 7\n\t" \
  "s_lshl_b32 s80, s80, 15\n\t" \
  "s_add_i32 s80, s80, s49\n\t" \
  "buffer_store_dwordx4 v[188:191], v208, %[rh], s80 offen sc1\n\t" \
  "buffer_store_dword v187, v209, %[ru], 0 offen sc1\n\t" \
  "ds_write_b128 v197, v[72:75] offset:16384\n\t" \
  "ds_write_b128 v197, v[76:79] offset:17408\n\t" \
  "ds_write_b128 v197, v[80:83] offset:18432\n\t" \
  "ds_write_b128 v197, v[84:87] offset:19456\n\t" \
  "ds_write_b128 v197, v[88:91] offset:20480\n\t" \
  "ds_write_b128 v197, v[92:95] offset:21504\n\t" \
  "ds_write_b128 v197, v[96:99] offset:22528\n\t" \
  "ds_write_b128 v197, v[100:103] offset:23552\n\t" \
  "s_or_b32 s58, s58, 16\n\t" \
  "23:\n\t" \
  "s_add_u32 s59, s59, 0x1000000\n\t" \
  "s_cmp_lt_u32 s59, 0x40000000\n\t" \
  "s_cbranch_scc1 26f\n\t" \
  "s_or_b32 s58, s58, 1\n\t" \
  "s_branch 7f\n\t" \
  "26:\n\t" \
  "buffer_load_dword v186, v203, %[ru], 0 offen sc1\n\t" \
  "s_waitcnt vmcnt(0)\n\t" \
  "s_branch 25b\n\t" \
  "37:\n\t" \
  "s_and_b32 s76, s40, 3\n\t" \
  "s_lshl_b32 s76, s76, 18\n\t" \
  "buffer_load_dwordx4 a[0:3], v211, %[rh], s76 offen offset:0 sc1\n\t" \
  "buffer_load_dwordx4 a[4:7], v211, %[rh], s76 offen offset:1024 sc1\n\t" \
  "buffer_load_dwordx4 a[8:11], v212, %[rh], s76 offen offset:0 sc1\n\t" \
  "buffer_load_dwordx4 a[12:15], v212, %[rh], s76 offen offset:1024 sc1\n\t" \
  "buffer_load_dwordx4 a[16:19], v213, %[rh], s76 offen offset:0 sc1\n\t" \
  "buffer_load_dwordx4 a[20:23], v213, %[rh], s76 offen offset:1024 sc1\n\t" \
  "buffer_load_dwordx4 a[24:27], v214, %[rh], s76 offen offset:0 sc1\n\t" \
  "buffer_load_dwordx4 a[28:31], v214, %[rh], s76 offen offset:1024 sc1\n\t" \
  "s_bitcmp1_b32 s58, 4\n\t" \
  "s_cbranch_scc1 29f\n\t" \
  "s_sub_i32 s80, s56, s41\n\t" \
  "s_mul_hi_u32 s80, s80, s45\n\t" \
  "s_lshl_b32 s80, s80, 1\n\t" \
  "s_sub_i32 s80, s80, s57\n\t" \
  "s_min_u32 s47, s47, s46\n\t" \
  "s_min_u32 s47, s47, s80\n\t" \
  "v_mov_b32 v188, s47\n\t" \
  "s_mov_b32 s47, s46\n\t" \
  "s_mov_b32 s46, s80\n\t" \
  "buffer_store_dword v188, v202, %[rh], s52 offen sc1\n\t" \
  "v_mov_b64 v[188:189], -1\n\t" \
  "v_mov_b64 v[190:191], -1\n\t" \
  "v_mov_b32 v187, s40\n\t" \
  "s_add_i32 s80, s40, -1\n\t" \
  "s_and_b32 s80, s80, 7\n\t" \
  "s_lshl_b32 s80, s80, 15\n\t" \
  "s_add_i32 s80, s80, s49\n\t" \
  "buffer_store_dwordx4 v[188:191], v208, %[rh], s80 offen sc1\n\t" \
  "buffer_store_dword v187, v209, %[ru], 0 offen sc1\n\t" \
  "ds_write_b128 v197, v[72:75] offset:16384\n\t" \
  "ds_write_b128 v197, v[76:79] offset:17408\n\t" \
  "ds_write_b128 v197, v[80:83] offset:18432\n\t" \
  "ds_write_b128 v197, v[84:87] offset:19456\n\t" \
  "ds_write_b128 v197, v[88:91] offset:20480\n\t" \
  "ds_write_b128 v197, v[92:95] offset:21504\n\t" \
  "ds_write_b128 v197, v[96:99] offset:22528\n\t" \
  "ds_write_b128 v197, v[100:103] offset:23552\n\t" \
  "29:\n\t" \
  "s_and_b32 s58, s58, 36\n\t" \
  "s_waitcnt lgkmcnt(0)\n\t" \
  "s_barrier\n\t" \
  "ds_read_b128 v[160:163], v193 offset:0\n\t" \
  "ds_read_b128 v[164:167], v193 offset:256\n\t" \
  "ds_read_b128 v[168:171], v193 offset:2048\n\t" \
  "ds_read_b128 v[172:175], v193 offset:2304\n\t" \
  "ds_read_b128 v[176:179], v193 offset:4096\n\t" \
  "ds_read_b128 v[180:183], v193 offset:4352\n\t" \
  "s_waitcnt lgkmcnt(2)\n\t" \
  "v_pk_add_f32 v[160:161], v[160:161], v[168:169]\n\t" \
  "v_pk_add_f32 v[162:163], v[162:163], v[170:171]\n\t" \
  "v_pk_add_f32 v[164:165], v[164:165], v[172:173]\n\t" \
  "v_pk_add_f32 v[166:167], v[166:167], v[174:175]\n\t" \
  "s_waitcnt lgkmcnt(0)\n\t" \
  "v_pk_add_f32 v[160:161], v[160:161], v[176:177]\n\t" \
  "v_pk_add_f32 v[162:163], v[162:163], v[178:179]\n\t" \
  "v_pk_add_f32 v[164:165], v[164:165], v[180:181]\n\t" \
  "v_pk_add_f32 v[166:167], v[166:167], v[182:183]\n\t" \
  "v_cvt_pk_bf16_f32 v160, v160, v161\n\t" \
  "v_cvt_pk_bf16_f32 v161, v162, v163\n\t" \
  "v_cvt_pk_bf16_f32 v162, v164, v165\n\t" \
  "v_cvt_pk_bf16_f32 v163, v166, v167\n\t" \
  "s_branch 38b\n\t" \
  "8:\n\t" \
  "s_mov_b64 vcc, -1\n\t" \
  "s_and_b32 s80, s58, 6\n\t" \
  "s_cbranch_scc1 6b\n\t" \
  "v_mov_b32 v151, 1\n\t" \
  "s_branch 5b\n\t" \
  "32:\n\t" \
  "s_and_b32 s80, s58, 36\n\t" \
  "s_cbranch_scc1 33b\n\t" \
  "s_add_i32 s40, s40, -2\n\t" \
  "34:\n\t" \
  "s_or_b32 s59, s59, 128\n\t" \
  "s_and_b32 s76, s40, 3\n\t" \
  "s_lshl_b32 s76, s76, 18\n\t" \
  "35:\n\t" \
  "buffer_load_dwordx4 a[0:3], v211, %[rh], s76 offen offset:0 sc1\n\t" \
  "buffer_load_dwordx4 a[4:7], v211, %[rh], s76 offen offset:1024 sc1\n\t" \
  "buffer_load_dwordx4 a[8:11], v212, %[rh], s76 offen offset:0 sc1\n\t" \
  "buffer_load_dwordx4 a[12:15], v212, %[rh], s76 offen offset:1024 sc1\n\t" \
  "buffer_load_dwordx4 a[16:19], v213, %[rh], s76 offen offset:0 sc1\n\t" \
  "buffer_load_dwordx4 a[20:23], v213, %[rh], s76 offen offset:1024 sc1\n\t" \
  "buffer_load_dwordx4 a[24:27], v214, %[rh], s76 offen offset:0 sc1\n\t" \
  "buffer_load_dwordx4 a[28:31], v214, %[rh], s76 offen offset:1024 sc1\n\t" \
  "s_waitcnt vmcnt(0)\n\t" \
  "s_bitcmp1_b32 s58, 2\n\t" \
  "s_cbranch_scc1 36f\n\t" \
  "v_accvgpr_read_b32 v0, a0\n\t" \
  "v_accvgpr_read_b32 v1, a1\n\t" \
  "v_accvgpr_read_b32 v2, a2\n\t" \
  "v_accvgpr_read_b32 v3, a3\n\t" \
  "v_accvgpr_read_b32 v4, a4\n\t" \
  "v_accvgpr_read_b32 v5, a5\n\t" \
  "v_accvgpr_read_b32 v6, a6\n\t" \
  "v_accvgpr_read_b32 v7, a7\n\t" \
  "v_accvgpr_read_b32 v8, a8\n\t" \
  "v_accvgpr_read_b32 v9, a9\n\t" \
  "v_accvgpr_read_b32 v10, a10\n\t" \
  "v_accvgpr_read_b32 v11, a11\n\t" \
  "v_accvgpr_read_b32 v12, a12\n\t" \
  "v_accvgpr_read_b32 v13, a13\n\t" \
  "v_accvgpr_read_b32 v14, a14\n\t" \
  "v_accvgpr_read_b32 v15, a15\n\t" \
  "v_accvgpr_read_b32 v16, a16\n\t" \
  "v_accvgpr_read_b32 v17, a17\n\t" \
  "v_accvgpr_read_b32 v18, a18\n\t" \
  "v_accvgpr_read_b32 v19, a19\n\t" \
  "v_accvgpr_read_b32 v20, a20\n\t" \
  "v_accvgpr_read_b32 v21, a21\n\t" \
  "v_accvgpr_read_b32 v22, a22\n\t" \
  "v_accvgpr_read_b32 v23, a23\n\t" \
  "v_accvgpr_read_b32 v24, a24\n\t" \
  "v_accvgpr_read_b32 v25, a25\n\t" \
  "v_accvgpr_read_b32 v26, a26\n\t" \
  "v_accvgpr_read_b32 v27, a27\n\t" \
  "v_accvgpr_read_b32 v28, a28\n\t" \
  "v_accvgpr_read_b32 v29, a29\n\t" \
  "v_accvgpr_read_b32 v30, a30\n\t" \
  "v_accvgpr_read_b32 v31, a31\n\t" \
  "v_max_u32 v148, v0, v1\n\t" \
  "v_max3_u32 v148, v148, v2, v3\n\t" \
  "v_max3_u32 v148, v148, v4, v5\n\t" \
  "v_max3_u32 v148, v148, v6, v7\n\t" \
  "v_max3_u32 v148, v148, v8, v9\n\t" \
  "v_max3_u32 v148, v148, v10, v11\n\t" \
  "v_max3_u32 v148, v148, v12, v13\n\t" \
  "v_max3_u32 v148, v148, v14, v15\n\t" \
  "v_max3_u32 v148, v148, v16, v17\n\t" \
  "v_max3_u32 v148, v148, v18, v19\n\t" \
  "v_max3_u32 v148, v148, v20, v21\n\t" \
  "v_max3_u32 v148, v148, v22, v23\n\t" \
  "v_max3_u32 v148, v148, v24, v25\n\t" \
  "v_max3_u32 v148, v148, v26, v27\n\t" \
  "v_max3_u32 v148, v148, v28, v29\n\t" \
  "v_max3_u32 v148, v148, v30, v31\n\t" \
  "v_cmp_eq_u32 vcc, -1, v148\n\t" \
  "s_cbranch_vccz 36f\n\t" \
  "s_add_u32 s59, s59, 0x1000000\n\t" \
  "s_cmp_lt_u32 s59, 0x10000000\n\t" \
  "s_cbranch_scc1 35b\n\t" \
  "s_or_b32 s58, s58, 9\n\t" \
  "s_branch 7f\n\t" \
  "36:\n\t" \
  "s_and_b32 s58, s58, 4\n\t" \
  "s_or_b32 s58, s58, 32\n\t" \
  "s_branch 31b\n\t" \
  "18:\n\t" \
  "v_mov_b32 v186, -1\n\t" \
  "s_and_b32 s80, s40, 3\n\t" \
  "s_mul_i32 s77, s80, 0xc000\n\t" \
  "s_add_i32 s77, s77, 0x40000\n\t" \
  "s_lshl_b32 s78, s80, 14\n\t" \
  "s_add_i32 s78, s78, 0x70000\n\t" \
  "s_mov_b32 s57, 0\n\t" \
  "s_add_i32 s96, s77, 0x1000\n\t" \
  "s_lshl_b32 s88, s80, 20\n\t" \
  "s_add_i32 s88, s88, s48\n\t" \
  "s_lshr_b32 s80, s88, 21\n\t" \
  "s_bitcmp1_b64 %[sel], s80\n\t" \
  "s_cselect_b32 s80, 0x2000, 0\n\t" \
  "s_add_i32 s88, s88, s80\n\t" \
  "s_xor_b32 s89, s88, 0x800\n\t" \
  "s_xor_b32 s90, s88, 0x1000\n\t" \
  "s_xor_b32 s91, s88, 0x1800\n\t" \
  "s_add_i32 s92, s77, s50\n\t" \
  "s_xor_b32 s93, s92, 0x100\n\t" \
  "s_xor_b32 s94, s92, 0x200\n\t" \
  "s_xor_b32 s95, s92, 0x300\n\t" \
  "s_add_i32 s97, s40, 1\n\t" \
  "s_and_b32 s97, s97, 7\n\t" \
  "s_lshl_b32 s97, s97, 15\n\t" \
  "s_add_i32 s97, s97, s49\n\t" \
  "12:\n\t" \
  "s_or_b32 s58, s58, 3\n\t" \
  "11:\n\t" \
  "buffer_load_dwordx4 v[72:75], v192, %[ru], s88 offen offset:0 sc1\n\t" \
  "buffer_load_dwordx4 v[76:79], v192, %[ru], s88 offen offset:1024 sc1\n\t" \
  "buffer_load_dwordx4 v[80:83], v192, %[ru], s89 offen offset:0 sc1\n\t" \
  "buffer_load_dwordx4 v[84:87], v192, %[ru], s89 offen offset:1024 sc1\n\t" \
  "buffer_load_dwordx4 v[88:91], v192, %[ru], s90 offen offset:0 sc1\n\t" \
  "buffer_load_dwordx4 v[92:95], v192, %[ru], s90 offen offset:1024 sc1\n\t" \
  "buffer_load_dwordx4 v[96:99], v192, %[ru], s91 offen offset:0 sc1\n\t" \
  "buffer_load_dwordx4 v[100:103], v192, %[ru], s91 offen offset:1024 sc1\n\t" \
  "buffer_load_dwordx4 v[104:107], v194, %[rh], s92 offen sc1\n\t" \
  "buffer_load_dwordx4 v[108:111], v194, %[rh], s93 offen sc1\n\t" \
  "buffer_load_dwordx4 v[112:115], v194, %[rh], s94 offen sc1\n\t" \
  "buffer_load_dwordx4 v[116:119], v194, %[rh], s95 offen sc1\n\t" \
  "s_waitcnt vmcnt(0)\n\t" \
  "s_add_u32 s59, s59, 1\n\t" \
  "s_bitcmp1_b32 s58, 2\n\t" \
  "s_cbranch_scc1 10b\n\t" \
  "v_max_u32 v148, v72, v73\n\t" \
  "v_max3_u32 v148, v148, v74, v75\n\t" \
  "v_max3_u32 v148, v148, v76, v77\n\t" \
  "v_max3_u32 v148, v148, v78, v79\n\t" \
  "v_max3_u32 v148, v148, v80, v81\n\t" \
  "v_max3_u32 v148, v148, v82, v83\n\t" \
  "v_max3_u32 v148, v148, v84, v85\n\t" \
  "v_max3_u32 v148, v148, v86, v87\n\t" \
  "v_max3_u32 v148, v148, v88, v89\n\t" \
  "v_max3_u32 v148, v148, v90, v91\n\t" \
  "v_max3_u32 v148, v148, v92, v93\n\t" \
  "v_max3_u32 v148, v148, v94, v95\n\t" \
  "v_max3_u32 v148, v148, v96, v97\n\t" \
  "v_max3_u32 v148, v148, v98, v99\n\t" \
  "v_max3_u32 v148, v148, v100, v101\n\t" \
  "v_max3_u32 v148, v148, v102, v103\n\t" \
  "v_max3_u32 v148, v148, v104, v105\n\t" \
  "v_max3_u32 v148, v148, v106, v107\n\t" \
  "v_max3_u32 v148, v148, v108, v109\n\t" \
  "v_max3_u32 v148, v148, v110, v111\n\t" \
  "v_max3_u32 v148, v148, v112, v113\n\t" \
  "v_max3_u32 v148, v148, v114, v115\n\t" \
  "v_max3_u32 v148, v148, v116, v117\n\t" \
  "v_max3_u32 v148, v148, v118, v119\n\t" \
  "v_cmp_eq_u32 vcc, -1, v148\n\t" \
  "s_cbranch_vccz 10b\n\t" \
  "s_bitcmp1_b32 s59, 6\n\t" \
  "s_cbranch_scc0 11b\n\t" \
  "7:\n\t" \
  "s_branch 92f\n\t" \
  "91:\n\t" \
  "s_mov_b32 s58, s42\n\t" \
  "s_mov_b32 s59, 0\n\t" \
  "s_bitcmp1_b32 s58, 4\n\t" \
  "s_cbranch_scc1 37f\n\t" \
  "s_bitcmp1_b32 s58, 3\n\t" \
  "s_cbranch_scc1 34f\n\t" \
  "s_bitcmp1_b32 s58, 1\n\t" \
  "s_cbranch_scc1 18f\n\t" \
  "17:\n\t" \
  "v_mov_b64 v[40:41], 0\n\t" \
  "v_mov_b64 v[42:43], 0\n\t" \
  "v_mov_b64 v[44:45], 0\n\t" \
  "v_mov_b64 v[46:47], 0\n\t" \
  "v_mov_b64 v[48:49], 0\n\t" \
  "v_mov_b64 v[50:51], 0\n\t" \
  "v_mov_b64 v[52:53], 0\n\t" \
  "v_mov_b64 v[54:55], 0\n\t" \
  "v_mov_b64 v[56:57], 0\n\t" \
  "v_mov_b64 v[58:59], 0\n\t" \
  "v_mov_b64 v[60:61], 0\n\t" \
  "v_mov_b64 v[62:63], 0\n\t" \
  "v_mov_b64 v[64:65], 0\n\t" \
  "v_mov_b64 v[66:67], 0\n\t" \
  "v_mov_b64 v[68:69], 0\n\t" \
  "v_mov_b64 v[70:71], 0\n\t" \
  "v_mov_b64 v[72:73], 0\n\t" \
  "v_mov_b64 v[74:75], 0\n\t" \
  "v_mov_b64 v[76:77], 0\n\t" \
  "v_mov_b64 v[78:79], 0\n\t" \
  "v_mov_b64 v[80:81], 0\n\t" \
  "v_mov_b64 v[82:83], 0\n\t" \
  "v_mov_b64 v[84:85], 0\n\t" \
  "v_mov_b64 v[86:87], 0\n\t" \
  "v_mov_b64 v[88:89], 0\n\t" \
  "v_mov_b64 v[90:91], 0\n\t" \
  "v_mov_b64 v[92:93], 0\n\t" \
  "v_mov_b64 v[94:95], 0\n\t" \
  "v_mov_b64 v[96:97], 0\n\t" \
  "v_mov_b64 v[98:99], 0\n\t" \
  "v_mov_b64 v[100:101], 0\n\t" \
  "v_mov_b64 v[102:103], 0\n\t" \
  "v_mov_b64 v[136:137], 0\n\t" \
  "v_mov_b64 v[138:139], 0\n\t" \
  "v_mov_b64 v[140:141], 0\n\t" \
  "v_mov_b64 v[142:143], 0\n\t" \
  "19:\n\t" \
  "s_add_i32 s85, s40, -1\n\t" \
  "s_and_b32 s82, s85, 2\n\t" \
  "s_lshl_b32 s82, s82, 13\n\t" \
  "s_and_b32 s80, s85, 1\n\t" \
  "s_lshl_b32 s80, s80, 12\n\t" \
  "s_add_i32 s82, s82, s80\n\t" \
  "s_add_i32 s82, s82, 0x84000\n\t" \
  "s_mov_b32 exec_hi, 0\n\t" \
  "buffer_load_dword v184, v200, %[rh], s82 offen sc1\n\t" \
  "buffer_load_dword v185, v200, %[rh], s52 offen sc1\n\t" \
  "s_mov_b64 exec, -1\n\t" \
  "20:\n\t" \
  "s_and_b32 s80, s40, 3\n\t" \
  "s_mul_i32 s77, s80, 0xc000\n\t" \
  "s_add_i32 s77, s77, 0x40000\n\t" \
  "s_lshl_b32 s78, s80, 14\n\t" \
  "s_add_i32 s78, s78, 0x70000\n\t" \
  "s_mov_b32 s57, 0\n\t" \
  "s_add_i32 s96, s77, 0x1000\n\t" \
  "s_and_b32 s98, s40, 2\n\t" \
  "s_lshl_b32 s98, s98, 13\n\t" \
  "s_and_b32 s80, s40, 1\n\t" \
  "s_lshl_b32 s80, s80, 12\n\t" \
  "s_add_i32 s98, s98, s80\n\t" \
  "s_add_i32 s98, s98, 0x84000\n\t" \
  "s_memtime vcc\n\t" \
  "s_memrealtime s[86:87]\n\t" \
  "s_nop 4\n\t" \
  "s_memrealtime s[60:61]\n\t" \
  "s_nop 4\n\t" \
  "s_memrealtime s[62:63]\n\t" \
  "s_nop 4\n\t" \
  "s_memrealtime s[64:65]\n\t" \
  "s_nop 4\n\t" \
  "s_memrealtime s[66:67]\n\t" \
  "s_nop 4\n\t" \
  "s_memrealtime s[68:69]\n\t" \
  "s_mov_b32 s79, 0\n\t" \
  "s_waitcnt lgkmcnt(0)\n\t" \
  "s_cmp_eq_u32 s86, s60\n\t" \
  "s_addc_u32 s79, s79, 0\n\t" \
  "s_cmp_eq_u32 s86, s62\n\t" \
  "s_addc_u32 s79, s79, 0\n\t" \
  "s_cmp_eq_u32 s86, s64\n\t" \
  "s_addc_u32 s79, s79, 0\n\t" \
  "s_cmp_eq_u32 s86, s66\n\t" \
  "s_addc_u32 s79, s79, 0\n\t" \
  "s_cmp_eq_u32 s86, s68\n\t" \
  "s_addc_u32 s79, s79, 0\n\t" \
  "s_mul_i32 s79, s79, 24\n\t" \
  "s_add_i32 s79, s79, vcc_lo\n\t" \
  "s_lshl4_add_u32 s80, s86, 64\n\t" \
  "s_sub_i32 s86, s79, s41\n\t" \
  "s_sub_i32 s87, s80, s43\n\t" \
  "s_cmp_lt_u32 s86, 4000\n\t" \
  "s_cbranch_scc1 21f\n\t" \
  "v_cvt_f32_u32 v165, s86\n\t" \
  "v_cvt_f32_u32 v164, s87\n\t" \
  "v_rcp_f32 v165, v165\n\t" \
  "s_nop 0\n\t" \
  "v_mul_f32 v165, v165, v164\n\t" \
  "v_max_f32 v165, 0x3f028f5c, v165\n\t" \
  "v_min_f32 v165, 0x3ffeb852, v165\n\t" \
  "v_rcp_f32 v164, v165\n\t" \
  "v_mul_f32 v165, 0x4f000000, v165\n\t" \
  "v_mul_f32 v164, 0x47800000, v164\n\t" \
  "v_cvt_u32_f32 v165, v165\n\t" \
  "v_cvt_u32_f32 v164, v164\n\t" \
  "s_nop 0\n\t" \
  "v_readfirstlane_b32 s45, v165\n\t" \
  "v_readfirstlane_b32 s44, v164\n\t" \
  "21:\n\t" \
  "s_mov_b32 s41, s79\n\t" \
  "s_mov_b32 s43, s80\n\t" \
  "s_lshl_b32 s83, s43, 8\n\t" \
  "s_mov_b32 s84, 570\n\t" \
  "s_mov_b32 exec_hi, 0\n\t" \
  "s_waitcnt vmcnt(0)\n\t" \
  "v_cmp_ne_u32_sdwa vcc, v184, s85 src0_sel:BYTE_0 src1_sel:DWORD\n\t" \
  "v_subrev_u32 v184, s83, v184\n\t" \
  "v_ashrrev_i32 v184, 8, v184\n\t" \
  "s_cbranch_vccnz 22f\n\t" \
  "s_nop 0\n\t" \
  "v_max_i32_dpp v184, v184, v184 quad_perm:[1,0,3,2] row_mask:0xf bank_mask:0xf\n\t" \
  "s_nop 1\n\t" \
  "v_max_i32_dpp v184, v184, v184 quad_perm:[2,3,0,1] row_mask:0xf bank_mask:0xf\n\t" \
  "s_nop 1\n\t" \
  "v_max_i32_dpp v184, v184, v184 row_half_mirror row_mask:0xf bank_mask:0xf\n\t" \
  "s_nop 1\n\t" \
  "v_max_i32_dpp v184, v184, v184 row_mirror row_mask:0xf bank_mask:0xf\n\t" \
  "s_nop 0\n\t" \
  "v_readlane_b32 s81, v184, 0\n\t" \
  "v_readlane_b32 s82, v184, 16\n\t" \
  "s_max_i32 s81, s81, s82\n\t" \
  "s_add_i32 s76, s81, s43\n\t" \
  "s_add_i32 s81, s81, s53\n\t" \
  "s_max_i32 s81, s81, 0\n\t" \
  "s_min_i32 s81, s81, 20000\n\t" \
  "s_mul_i32 s81, s81, s44\n\t" \
  "s_lshr_b32 s81, s81, 16\n\t" \
  "s_add_i32 s81, s81, -1020\n\t" \
  "24:\n\t" \
  "s_add_i32 s81, s81, s41\n\t" \
  "v_min_u32 v185, 1760, v185\n\t" \
  "s_nop 1\n\t" \
  "v_max_i32_dpp v185, v185, v185 quad_perm:[1,0,3,2] row_mask:0xf bank_mask:0xf\n\t" \
  "s_nop 1\n\t" \
  "v_max_i32_dpp v185, v185, v185 quad_perm:[2,3,0,1] row_mask:0xf bank_mask:0xf\n\t" \
  "s_nop 1\n\t" \
  "v_max_i32_dpp v185, v185, v185 row_half_mirror row_mask:0xf bank_mask:0xf\n\t" \
  "s_nop 1\n\t" \
  "v_max_i32_dpp v185, v185, v185 row_mirror row_mask:0xf bank_mask:0xf\n\t" \
  "s_nop 0\n\t" \
  "v_readlane_b32 s54, v185, 0\n\t" \
  "v_readlane_b32 s82, v185, 16\n\t" \
  "s_max_i32 s54, s54, s82\n\t" \
  "s_add_i32 s54, s54, 235\n\t" \
  "1:\n\t" \
  "s_memtime vcc\n\t" \
  "s_waitcnt lgkmcnt(0)\n\t" \
  "s_sub_i32 s79, s81, vcc_lo\n\t" \
  "s_cmp_gt_i32 s79, 128\n\t" \
  "s_cbranch_scc1 1b\n\t" \
  ".p2align 4\n\t" \
  "2:\n\t" \
  "s_sub_i32 s79, s79, 33\n\t" \
  "s_cmp_gt_i32 s79, 56\n\t" \
  "s_cbranch_scc1 2b\n\t" \
  "buffer_load_dword v187, v200, %[rh], s78 offen sc1\n\t" \
  "s_memtime vcc\n\t" \
  "s_memrealtime s[86:87]\n\t" \
  "s_nop 4\n\t" \
  "s_memrealtime s[60:61]\n\t" \
  "s_nop 4\n\t" \
  "s_memrealtime s[62:63]\n\t" \
  "s_nop 4\n\t" \
  "s_memrealtime s[64:65]\n\t" \
  "s_nop 4\n\t" \
  "s_memrealtime s[66:67]\n\t" \
  "s_nop 4\n\t" \
  "s_memrealtime s[68:69]\n\t" \
  "s_mov_b32 s79, 0\n\t" \
  "s_waitcnt lgkmcnt(0)\n\t" \
  "s_mov_b64 exec, -1\n\t" \
  "ds_read_b128 a[0:3], v194 offset:0\n\t" \
  "ds_read_b128 a[4:7], v194 offset:1024\n\t" \
  "ds_read_b128 a[8:11], v194 offset:2048\n\t" \
  "ds_read_b128 a[12:15], v194 offset:3072\n\t" \
  "ds_read_b128 a[16:19], v194 offset:4096\n\t" \
  "ds_read_b128 a[20:23], v194 offset:5120\n\t" \
  "ds_read_b128 a[24:27], v194 offset:6144\n\t" \
  "ds_read_b128 a[28:31], v194 offset:7168\n\t" \
  "ds_read_b128 a[32:35], v194 offset:8192\n\t" \
  "ds_read_b128 a[36:39], v194 offset:9216\n\t" \
  "ds_read_b128 a[40:43], v194 offset:10240\n\t" \
  "ds_read_b128 a[44:47], v194 offset:11264\n\t" \
  "ds_read_b128 a[48:51], v194 offset:12288\n\t" \
  "ds_read_b128 a[52:55], v194 offset:13312\n\t" \
  "ds_read_b128 a[56:59], v194 offset:14336\n\t" \
  "ds_read_b128 a[60:63], v194 offset:15360\n\t" \
  "ds_read_b128 a[64:67], v194 offset:16384\n\t" \
  "ds_read_b128 a[68:71], v194 offset:17408\n\t" \
  "ds_read_b128 a[72:75], v194 offset:18432\n\t" \
  "ds_read_b128 a[76:79], v194 offset:19456\n\t" \
  "ds_read_b128 a[80:83], v194 offset:20480\n\t" \
  "ds_read_b128 a[84:87], v194 offset:21504\n\t" \
  "ds_read_b128 a[88:91], v194 offset:22528\n\t" \
  "ds_read_b128 a[92:95], v194 offset:23552\n\t" \
  "ds_read_b128 a[96:99], v194 offset:24576\n\t" \
  "ds_read_b128 a[100:103], v194 offset:25600\n\t" \
  "ds_read_b128 a[104:107], v194 offset:26624\n\t" \
  "ds_read_b128 a[108:111], v194 offset:27648\n\t" \
  "ds_read_b128 a[112:115], v194 offset:28672\n\t" \
  "ds_read_b128 a[116:119], v194 offset:29696\n\t" \
  "ds_read_b128 a[120:123], v194 offset:30720\n\t" \
  "ds_read_b128 a[124:127], v194 offset:31744\n\t" \
  "s_mov_b32 exec_hi, 0\n\t" \
  "s_cmp_eq_u32 s86, s60\n\t" \
  "s_addc_u32 s79, s79, 0\n\t" \
  "s_cmp_eq_u32 s86, s62\n\t" \
  "s_addc_u32 s79, s79, 0\n\t" \
  "s_cmp_eq_u32 s86, s64\n\t" \
  "s_addc_u32 s79, s79, 0\n\t" \
  "s_cmp_eq_u32 s86, s66\n\t" \
  "s_addc_u32 s79, s79, 0\n\t" \
  "s_cmp_eq_u32 s86, s68\n\t" \
  "s_addc_u32 s79, s79, 0\n\t" \
  "s_mul_i32 s79, s79, 24\n\t" \
  "s_add_i32 s79, s79, vcc_lo\n\t" \
  "s_lshl4_add_u32 s80, s86, 64\n\t" \
  "s_mov_b32 s41, s79\n\t" \
  "s_mov_b32 s43, s80\n\t" \
  "s_lshl_b32 s83, s43, 8\n\t" \
  "s_add_i32 s54, s54, s43\n\t" \
  "13:\n\t" \
  "s_waitcnt vmcnt(0)\n\t" \
  "s_memtime s[86:87]\n\t" \
  "v_cmp_ne_u32_sdwa vcc, v187, s40 src0_sel:BYTE_0 src1_sel:DWORD\n\t" \
  "v_subrev_u32 v184, s83, v187\n\t" \
  "v_ashrrev_i32 v184, 8, v184\n\t" \
  "s_nop 1\n\t" \
  "v_max_i32_dpp v184, v184, v184 quad_perm:[1,0,3,2] row_mask:0xf bank_mask:0xf\n\t" \
  "s_nop 1\n\t" \
  "v_max_i32_dpp v184, v184, v184 quad_perm:[2,3,0,1] row_mask:0xf bank_mask:0xf\n\t" \
  "s_cbranch_vccnz 14f\n\t" \
  "15:\n\t" \
  "s_nop 0\n\t" \
  "v_max_i32_dpp v184, v184, v184 row_half_mirror row_mask:0xf bank_mask:0xf\n\t" \
  "s_nop 1\n\t" \
  "v_max_i32_dpp v184, v184, v184 row_mirror row_mask:0xf bank_mask:0xf\n\t" \
  "s_mov_b64 exec, -1\n\t" \
  "v_readlane_b32 s57, v184, 0\n\t" \
  "v_readlane_b32 s82, v184, 16\n\t" \
  "s_waitcnt lgkmcnt(0)\n\t" \
  "s_sub_i32 s79, s41, s86\n\t" \
  "s_max_i32 s57, s57, s82\n\t" \
  "s_add_i32 s81, s57, 212\n\t" \
  "s_max_i32 s81, s81, 0\n\t" \
  "s_min_i32 s81, s81, 20000\n\t" \
  "s_mul_i32 s81, s81, s44\n\t" \
  "s_lshr_b32 s81, s81, 16\n\t" \
  "s_add_i32 s79, s79, s81\n\t" \
  "s_sub_i32 s85, s84, s79\n\t" \
  "s_max_i32 s85, s85, 0\n\t" \
  "s_mul_hi_u32 s85, s85, s45\n\t" \
  "s_lshl1_add_u32 s85, s85, s57\n\t" \
  "s_add_i32 s85, s85, s54\n\t" \
  "s_lshl_b32 s85, s85, 8\n\t" \
  "s_or_b32 s85, s85, s40\n\t" \
  "v_mov_b32 v184, s85\n\t" \
  "buffer_store_dword v184, v202, %[rh], s98 offen sc1\n\t" \
  "s_add_i32 s79, s79, -570\n\t" \
  ".p2align 4\n\t" \
  "3:\n\t" \
  "s_sub_i32 s79, s79, 33\n\t" \
  "s_cmp_gt_i32 s79, 56\n\t" \
  "s_cbranch_scc1 3b\n\t" \
  "buffer_load_dwordx4 v[0:3], v193, %[rh], s77 offen offset:0 sc1\n\t" \
  "s_nop 4\n\t" \
  "buffer_load_dwordx4 v[4:7], v193, %[rh], s77 offen offset:1024 sc1\n\t" \
  "s_nop 4\n\t" \
  "buffer_load_dwordx4 v[8:11], v193, %[rh], s77 offen offset:2048 sc1\n\t" \
  "s_nop 4\n\t" \
  "buffer_load_dwordx4 v[12:15], v193, %[rh], s77 offen offset:3072 sc1\n\t" \
  "s_nop 4\n\t" \
  "buffer_load_dwordx4 v[16:19], v193, %[rh], s96 offen offset:0 sc1\n\t" \
  "s_nop 4\n\t" \
  "buffer_load_dwordx4 v[20:23], v193, %[rh], s96 offen offset:1024 sc1\n\t" \
  "s_nop 4\n\t" \
  "buffer_load_dwordx4 v[24:27], v193, %[rh], s96 offen offset:2048 sc1\n\t" \
  "s_nop 4\n\t" \
  "buffer_load_dwordx4 v[28:31], v193, %[rh], s96 offen offset:3072 sc1\n\t" \
  "s_add_i32 s79, s79, 217\n\t" \
  ".p2align 4\n\t" \
  "4:\n\t" \
  "s_sub_i32 s79, s79, 33\n\t" \
  "s_cmp_gt_i32 s79, 56\n\t" \
  "s_cbranch_scc1 4b\n\t" \
  "buffer_load_dwordx4 v[32:35], v195, %[rh], s77 offen offset:0 sc1\n\t" \
  "buffer_load_dwordx4 v[36:39], v195, %[rh], s77 offen offset:2048 sc1\n\t" \
  "10:\n\t" \
  "s_waitcnt vmcnt(9)\n\t" \
  "v_smfmac_f32_16x16x64_bf16 v[40:43], v[0:3], a[0:7], v203\n\t" \
  "v_smfmac_f32_16x16x64_bf16 v[56:59], v[0:3], a[8:15], v203\n\t" \
  "s_waitcnt vmcnt(8)\n\t" \
  "v_smfmac_f32_16x16x64_bf16 v[44:47], v[4:7], a[16:23], v203\n\t" \
  "v_cvt_pk_bf16_f32 v40, v40, v41\n\t" \
  "v_cvt_pk_bf16_f32 v41, v42, v43\n\t" \
  "v_smfmac_f32_16x16x64_bf16 v[60:63], v[4:7], a[24:31], v203\n\t" \
  "v_cvt_pk_bf16_f32 v56, v56, v57\n\t" \
  "v_cvt_pk_bf16_f32 v57, v58, v59\n\t" \
  "s_waitcnt vmcnt(7)\n\t" \
  "v_smfmac_f32_16x16x64_bf16 v[48:51], v[8:11], a[32:39], v203\n\t" \
  "v_cvt_pk_bf16_f32 v42, v44, v45\n\t" \
  "v_cvt_pk_bf16_f32 v43, v46, v47\n\t" \
  "v_smfmac_f32_16x16x64_bf16 v[64:67], v[8:11], a[40:47], v203\n\t" \
  "v_cvt_pk_bf16_f32 v58, v60, v61\n\t" \
  "v_cvt_pk_bf16_f32 v59, v62, v63\n\t" \
  "s_waitcnt vmcnt(6)\n\t" \
  "v_smfmac_f32_16x16x64_bf16 v[52:55], v[12:15], a[48:55], v203\n\t" \
  "v_cvt_pk_bf16_f32 v44, v48, v49\n\t" \
  "v_cvt_pk_bf16_f32 v45, v50, v51\n\t" \
  "v_smfmac_f32_16x16x64_bf16 v[68:71], v[12:15], a[56:63], v203\n\t" \
  "v_cvt_pk_bf16_f32 v60, v64, v65\n\t" \
  "v_cvt_pk_bf16_f32 v61, v66, v67\n\t" \
  "s_waitcnt vmcnt(5)\n\t" \
  "v_smfmac_f32_16x16x64_bf16 v[72:75], v[16:19], a[64:71], v203\n\t" \
  "v_cvt_pk_bf16_f32 v46, v52, v53\n\t" \
  "v_cvt_pk_bf16_f32 v47, v54, v55\n\t" \
  "v_smfmac_f32_16x16x64_bf16 v[88:91], v[16:19], a[72:79], v203\n\t" \
  "v_cvt_pk_bf16_f32 v62, v68, v69\n\t" \
  "v_cvt_pk_bf16_f32 v63, v70, v71\n\t" \
  "s_waitcnt vmcnt(4)\n\t" \
  "v_smfmac_f32_16x16x64_bf16 v[76:79], v[20:23], a[80:87], v203\n\t" \
  "v_cvt_pk_bf16_f32 v72, v72, v73\n\t" \
  "v_cvt_pk_bf16_f32 v73, v74, v75\n\t" \
  "v_smfmac_f32_16x16x64_bf16 v[92:95], v[20:23], a[88:95], v203\n\t" \
  "v_cvt_pk_bf16_f32 v88, v88, v89\n\t" \
  "v_cvt_pk_bf16_f32 v89, v90, v91\n\t" \
  "s_waitcnt vmcnt(3)\n\t" \
  "v_smfmac_f32_16x16x64_bf16 v[80:83], v[24:27], a[96:103], v203\n\t" \
  "v_cvt_pk_bf16_f32 v74, v76, v77\n\t" \
  "v_cvt_pk_bf16_f32 v75, v78, v79\n\t" \
  "v_smfmac_f32_16x16x64_bf16 v[96:99], v[24:27], a[104:111], v203\n\t" \
  "v_cvt_pk_bf16_f32 v90, v92, v93\n\t" \
  "v_cvt_pk_bf16_f32 v91, v94, v95\n\t" \
  "s_waitcnt vmcnt(2)\n\t" \
  "v_smfmac_f32_16x16x64_bf16 v[84:87], v[28:31], a[112:119], v203\n\t" \
  "v_cvt_pk_bf16_f32 v76, v80, v81\n\t" \
  "v_cvt_pk_bf16_f32 v77, v82, v83\n\t" \
  "v_smfmac_f32_16x16x64_bf16 v[100:103], v[28:31], a[120:127], v203\n\t" \
  "v_cvt_pk_bf16_f32 v92, v96, v97\n\t" \
  "v_cvt_pk_bf16_f32 v93, v98, v99\n\t" \
  "s_nop 1\n\t" \
  "v_cvt_pk_bf16_f32 v78, v84, v85\n\t" \
  "v_cvt_pk_bf16_f32 v79, v86, v87\n\t" \
  "s_nop 1\n\t" \
  "v_cvt_pk_bf16_f32 v94, v100, v101\n\t" \
  "v_cvt_pk_bf16_f32 v95, v102, v103\n\t" \
  "s_waitcnt vmcnt(1)\n\t" \
  "v_smfmac_f32_16x16x64_bf16 v[136:139], v[32:35], v[40:47], v204\n\t" \
  "v_smfmac_f32_16x16x64_bf16 v[140:143], v[32:35], v[56:63], v204\n\t" \
  "s_waitcnt vmcnt(0)\n\t" \
  "v_smfmac_f32_16x16x64_bf16 v[136:139], v[36:39], v[72:79], v204\n\t" \
  "v_smfmac_f32_16x16x64_bf16 v[140:143], v[36:39], v[88:95], v204\n\t" \
  "s_nop 3\n\t" \
  "v_permlane32_swap_b32 v136, v138\n\t" \
  "v_permlane32_swap_b32 v137, v139\n\t" \
  "v_pk_add_f32 v[136:137], v[136:137], v[138:139]\n\t" \
  "s_nop 0\n\t" \
  "v_permlane32_swap_b32 v140, v142\n\t" \
  "v_permlane32_swap_b32 v141, v143\n\t" \
  "v_pk_add_f32 v[138:139], v[140:141], v[142:143]\n\t" \
  "ds_write2st64_b64 v197, v[136:137], v[138:139] offset1:2\n\t" \
  "s_waitcnt lgkmcnt(0)\n\t" \
  "s_barrier\n\t" \
  "s_barrier\n\t" \
  "ds_read2_b32 v[148:149], v207 offset1:8\n\t" \
  "s_waitcnt lgkmcnt(0)\n\t" \
  "v_or_b32 v148, v148, v149\n\t" \
  "v_cmp_ne_u32 vcc, 0, v148\n\t" \
  "s_cbranch_vccnz 12f\n\t" \
  "s_and_b32 s58, s58, 4\n\t" \
  "s_add_i32 s80, s57, s43\n\t" \
  "s_sub_i32 s80, s80, s76\n\t" \
  "s_add_i32 s53, s53, -12\n\t" \
  "s_max_i32 s80, s80, s53\n\t" \
  "s_add_i32 s53, s53, 24\n\t" \
  "s_min_i32 s53, s53, s80\n\t" \
  "s_and_b32 s76, s40, 3\n\t" \
  "s_lshl_b32 s76, s76, 18\n\t" \
  "s_add_i32 s77, s76, 0x10000\n\t" \
  "s_add_i32 s78, s76, 0x1000\n\t" \
  "s_add_i32 s79, s76, 0x11000\n\t" \
  "s_mov_b32 s97, 1\n\t" \
  "s_cmp_ge_i32 s40, 62\n\t" \
  "s_cbranch_scc1 31f\n\t" \
  "s_and_b32 s82, s40, 3\n\t" \
  "s_lshl_b32 s82, s82, 20\n\t" \
  "s_add_i32 s82, s82, s48\n\t" \
  "s_andn2_b32 s82, s82, 0x1800\n\t" \
  "s_lshr_b32 s80, s82, 21\n\t" \
  "s_bitcmp1_b64 %[sel], s80\n\t" \
  "s_cselect_b32 s80, 0x2000, 0\n\t" \
  "s_add_i32 s82, s82, s80\n\t" \
  "s_add_i32 s83, s82, 0x1000\n\t" \
  "v_mov_b64 v[188:189], -1\n\t" \
  "v_mov_b64 v[190:191], -1\n\t" \
  "s_sleep 0\n\t" \
  "buffer_store_dwordx4 v[188:191], v192, %[ru], s82 offen offset:0 sc1\n\t" \
  "buffer_store_dwordx4 v[188:191], v192, %[ru], s82 offen offset:1024 sc1\n\t" \
  "buffer_store_dwordx4 v[188:191], v192, %[ru], s82 offen offset:2048 sc1\n\t" \
  "buffer_store_dwordx4 v[188:191], v192, %[ru], s82 offen offset:3072 sc1\n\t" \
  "buffer_store_dwordx4 v[188:191], v192, %[ru], s83 offen offset:0 sc1\n\t" \
  "buffer_store_dwordx4 v[188:191], v192, %[ru], s83 offen offset:1024 sc1\n\t" \
  "buffer_store_dwordx4 v[188:191], v192, %[ru], s83 offen offset:2048 sc1\n\t" \
  "buffer_store_dwordx4 v[188:191], v192, %[ru], s83 offen offset:3072 sc1\n\t" \
  "31:\n\t" \
  "buffer_load_dwordx4 v[0:3], v210, %[rh], s76 offen offset:0 sc1\n\t" \
  "s_nop 10\n\t" \
  "buffer_load_dwordx4 v[4:7], v210, %[rh], s76 offen offset:1024 sc1\n\t" \
  "s_nop 10\n\t" \
  "buffer_load_dwordx4 v[8:11], v210, %[rh], s77 offen offset:0 sc1\n\t" \
  "s_nop 10\n\t" \
  "buffer_load_dwordx4 v[12:15], v210, %[rh], s77 offen offset:1024 sc1\n\t" \
  "s_cmp_eq_u32 s97, 0\n\t" \
  "s_cbranch_scc1 26f\n\t" \
  "s_barrier\n\t" \
  "s_mov_b32 s97, 0\n\t" \
  "26:\n\t" \
  "buffer_load_dwordx4 v[16:19], v210, %[rh], s76 offen offset:2048 sc1\n\t" \
  "s_nop 10\n\t" \
  "buffer_load_dwordx4 v[20:23], v210, %[rh], s76 offen offset:3072 sc1\n\t" \
  "s_nop 10\n\t" \
  "buffer_load_dwordx4 v[24:27], v210, %[rh], s77 offen offset:2048 sc1\n\t" \
  "s_nop 10\n\t" \
  "buffer_load_dwordx4 v[28:31], v210, %[rh], s77 offen offset:3072 sc1\n\t" \
  "s_nop 10\n\t" \
  "buffer_load_dwordx4 v[32:35], v210, %[rh], s78 offen offset:0 sc1\n\t" \
  "s_nop 10\n\t" \
  "buffer_load_dwordx4 v[36:39], v210, %[rh], s78 offen offset:1024 sc1\n\t" \
  "s_nop 10\n\t" \
  "buffer_load_dwordx4 v[40:43], v210, %[rh], s79 offen offset:0 sc1\n\t" \
  "s_nop 10\n\t" \
  "buffer_load_dwordx4 v[44:47], v210, %[rh], s79 offen offset:1024 sc1\n\t" \
  "s_nop 10\n\t" \
  "buffer_load_dwordx4 v[48:51], v210, %[rh], s78 offen offset:2048 sc1\n\t" \
  "s_nop 10\n\t" \
  "buffer_load_dwordx4 v[52:55], v210, %[rh], s78 offen offset:3072 sc1\n\t" \
  "s_nop 10\n\t" \
  "buffer_load_dwordx4 v[56:59], v210, %[rh], s79 offen offset:2048 sc1\n\t" \
  "s_nop 10\n\t" \
  "buffer_load_dwordx4 v[60:63], v210, %[rh], s79 offen offset:3072 sc1\n\t" \
  "s_nop 10\n\t" \
  "buffer_load_dwordx4 v[64:67], v211, %[rh], s76 offen offset:0 sc1\n\t" \
  "s_nop 10\n\t" \
  "buffer_load_dwordx4 v[68:71], v211, %[rh], s76 offen offset:1024 sc1\n\t" \
  "s_nop 10\n\t" \
  "buffer_load_dwordx4 v[72:75], v211, %[rh], s77 offen offset:0 sc1\n\t" \
  "s_nop 10\n\t" \
  "buffer_load_dwordx4 v[76:79], v211, %[rh], s77 offen offset:1024 sc1\n\t" \
  "s_nop 10\n\t" \
  "buffer_load_dwordx4 v[80:83], v211, %[rh], s76 offen offset:2048 sc1\n\t" \
  "s_nop 10\n\t" \
  "buffer_load_dwordx4 v[84:87], v211, %[rh], s76 offen offset:3072 sc1\n\t" \
  "s_nop 10\n\t" \
  "v_mov_b64 v[136:137], 0\n\t" \
  "v_mov_b64 v[138:139], 0\n\t" \
  "v_mov_b64 v[140:141], 0\n\t" \
  "v_mov_b64 v[142:143], 0\n\t" \
  "s_waitcnt vmcnt(20)\n\t" \
  "v_swap_b32 v1, v4\n\t" \
  "v_swap_b32 v3, v6\n\t" \
  "s_nop 1\n\t" \
  "v_mfma_f32_16x16x32_bf16 v[168:171], a[0:3], v[0:3], 0\n\t" \
  "buffer_load_dwordx4 v[88:91], v211, %[rh], s77 offen offset:2048 sc1\n\t" \
  "v_mfma_f32_16x16x32_bf16 v[172:175], a[8:11], v[0:3], 0\n\t" \
  "s_waitcnt vmcnt(19)\n\t" \
  "v_swap_b32 v9, v12\n\t" \
  "v_mfma_f32_16x16x32_bf16 v[168:171], a[4:7], v[4:7], v[168:171]\n\t" \
  "v_swap_b32 v11, v14\n\t" \
  "v_mfma_f32_16x16x32_bf16 v[172:175], a[12:15], v[4:7], v[172:175]\n\t" \
  "buffer_load_dwordx4 v[92:95], v211, %[rh], s77 offen offset:3072 sc1\n\t" \
  "v_mfma_f32_16x16x32_bf16 v[176:179], a[0:3], v[8:11], 0\n\t" \
  "buffer_load_dwordx4 v[96:99], v211, %[rh], s78 offen offset:0 sc1\n\t" \
  "v_mfma_f32_16x16x32_bf16 v[180:183], a[8:11], v[8:11], 0\n\t" \
  "s_waitcnt vmcnt(19)\n\t" \
  "v_swap_b32 v17, v20\n\t" \
  "v_mfma_f32_16x16x32_bf16 v[176:179], a[4:7], v[12:15], v[176:179]\n\t" \
  "v_swap_b32 v19, v22\n\t" \
  "v_mfma_f32_16x16x32_bf16 v[180:183], a[12:15], v[12:15], v[180:183]\n\t" \
  "buffer_load_dwordx4 v[100:103], v211, %[rh], s78 offen offset:1024 sc1\n\t" \
  "v_mfma_f32_16x16x32_bf16 v[168:171], a[16:19], v[16:19], v[168:171]\n\t" \
  "buffer_load_dwordx4 v[104:107], v211, %[rh], s79 offen offset:0 sc1\n\t" \
  "v_mfma_f32_16x16x32_bf16 v[172:175], a[24:27], v[16:19], v[172:175]\n\t" \
  "s_waitcnt vmcnt(19)\n\t" \
  "v_swap_b32 v25, v28\n\t" \
  "v_mfma_f32_16x16x32_bf16 v[168:171], a[20:23], v[20:23], v[168:171]\n\t" \
  "v_swap_b32 v27, v30\n\t" \
  "v_mfma_f32_16x16x32_bf16 v[172:175], a[28:31], v[20:23], v[172:175]\n\t" \
  "buffer_load_dwordx4 v[108:111], v211, %[rh], s79 offen offset:1024 sc1\n\t" \
  "v_mfma_f32_16x16x32_bf16 v[176:179], a[16:19], v[24:27], v[176:179]\n\t" \
  "buffer_load_dwordx4 v[112:115], v211, %[rh], s78 offen offset:2048 sc1\n\t" \
  "v_mfma_f32_16x16x32_bf16 v[180:183], a[24:27], v[24:27], v[180:183]\n\t" \
  "s_waitcnt vmcnt(19)\n\t" \
  "v_swap_b32 v33, v36\n\t" \
  "v_mfma_f32_16x16x32_bf16 v[176:179], a[20:23], v[28:31], v[176:179]\n\t" \
  "v_swap_b32 v35, v38\n\t" \
  "v_mfma_f32_16x16x32_bf16 v[180:183], a[28:31], v[28:31], v[180:183]\n\t" \
  "buffer_load_dwordx4 v[116:119], v211, %[rh], s78 offen offset:3072 sc1\n\t" \
  "v_mfma_f32_16x16x32_bf16 v[168:171], a[32:35], v[32:35], v[168:171]\n\t" \
  "buffer_load_dwordx4 v[120:123], v211, %[rh], s79 offen offset:2048 sc1\n\t" \
  "v_mfma_f32_16x16x32_bf16 v[172:175], a[40:43], v[32:35], v[172:175]\n\t" \
  "s_waitcnt vmcnt(19)\n\t" \
  "v_swap_b32 v41, v44\n\t" \
  "v_mfma_f32_16x16x32_bf16 v[168:171], a[36:39], v[36:39], v[168:171]\n\t" \
  "v_swap_b32 v43, v46\n\t" \
  "v_mfma_f32_16x16x32_bf16 v[172:175], a[44:47], v[36:39], v[172:175]\n\t" \
  "buffer_load_dwordx4 v[124:127], v211, %[rh], s79 offen offset:3072 sc1\n\t" \
  "v_mfma_f32_16x16x32_bf16 v[176:179], a[32:35], v[40:43], v[176:179]\n\t" \
  "v_mfma_f32_16x16x32_bf16 v[180:183], a[40:43], v[40:43], v[180:183]\n\t" \
  "s_waitcnt vmcnt(18)\n\t" \
  "v_swap_b32 v49, v52\n\t" \
  "v_mfma_f32_16x16x32_bf16 v[176:179], a[36:39], v[44:47], v[176:179]\n\t" \
  "v_swap_b32 v51, v54\n\t" \
  "v_mfma_f32_16x16x32_bf16 v[180:183], a[44:47], v[44:47], v[180:183]\n\t" \
  "v_mov_b64 v[40:41], 0\n\t" \
  "v_mov_b64 v[42:43], 0\n\t" \
  "v_mfma_f32_16x16x32_bf16 v[168:171], a[48:51], v[48:51], v[168:171]\n\t" \
  "v_mov_b64 v[44:45], 0\n\t" \
  "v_mov_b64 v[46:47], 0\n\t" \
  "v_mfma_f32_16x16x32_bf16 v[172:175], a[56:59], v[48:51], v[172:175]\n\t" \
  "s_waitcnt vmcnt(16)\n\t" \
  "v_swap_b32 v57, v60\n\t" \
  "v_mfma_f32_16x16x32_bf16 v[168:171], a[52:55], v[52:55], v[168:171]\n\t" \
  "v_swap_b32 v59, v62\n\t" \
  "v_mfma_f32_16x16x32_bf16 v[172:175], a[60:63], v[52:55], v[172:175]\n\t" \
  "v_mov_b64 v[48:49], 0\n\t" \
  "v_mov_b64 v[50:51], 0\n\t" \
  "v_mfma_f32_16x16x32_bf16 v[176:179], a[48:51], v[56:59], v[176:179]\n\t" \
  "v_mov_b64 v[52:53], 0\n\t" \
  "v_mov_b64 v[54:55], 0\n\t" \
  "v_mfma_f32_16x16x32_bf16 v[180:183], a[56:59], v[56:59], v[180:183]\n\t" \
  "s_waitcnt vmcnt(14)\n\t" \
  "v_swap_b32 v65, v68\n\t" \
  "v_mfma_f32_16x16x32_bf16 v[176:179], a[52:55], v[60:63], v[176:179]\n\t" \
  "v_swap_b32 v67, v70\n\t" \
  "v_mfma_f32_16x16x32_bf16 v[180:183], a[60:63], v[60:63], v[180:183]\n\t" \
  "v_mov_b64 v[56:57], 0\n\t" \
  "v_mov_b64 v[58:59], 0\n\t" \
  "v_mfma_f32_16x16x32_bf16 v[168:171], a[64:67], v[64:67], v[168:171]\n\t" \
  "v_mov_b64 v[60:61], 0\n\t" \
  "v_mov_b64 v[62:63], 0\n\t" \
  "v_mfma_f32_16x16x32_bf16 v[172:175], a[72:75], v[64:67], v[172:175]\n\t" \
  "s_waitcnt vmcnt(12)\n\t" \
  "v_swap_b32 v73, v76\n\t" \
  "v_mfma_f32_16x16x32_bf16 v[168:171], a[68:71], v[68:71], v[168:171]\n\t" \
  "v_swap_b32 v75, v78\n\t" \
  "v_mfma_f32_16x16x32_bf16 v[172:175], a[76:79], v[68:71], v[172:175]\n\t" \
  "v_mov_b64 v[64:65], 0\n\t" \
  "v_mov_b64 v[66:67], 0\n\t" \
  "v_mfma_f32_16x16x32_bf16 v[176:179], a[64:67], v[72:75], v[176:179]\n\t" \
  "v_mov_b64 v[68:69], 0\n\t" \
  "v_mov_b64 v[70:71], 0\n\t" \
  "v_mfma_f32_16x16x32_bf16 v[180:183], a[72:75], v[72:75], v[180:183]\n\t" \
  "s_waitcnt vmcnt(10)\n\t" \
  "v_swap_b32 v81, v84\n\t" \
  "v_mfma_f32_16x16x32_bf16 v[176:179], a[68:71], v[76:79], v[176:179]\n\t" \
  "v_swap_b32 v83, v86\n\t" \
  "v_mfma_f32_16x16x32_bf16 v[180:183], a[76:79], v[76:79], v[180:183]\n\t" \
  "v_mov_b64 v[72:73], 0\n\t" \
  "v_mov_b64 v[74:75], 0\n\t" \
  "v_mfma_f32_16x16x32_bf16 v[168:171], a[80:83], v[80:83], v[168:171]\n\t" \
  "v_mov_b64 v[76:77], 0\n\t" \
  "v_mov_b64 v[78:79], 0\n\t" \
  "v_mfma_f32_16x16x32_bf16 v[172:175], a[88:91], v[80:83], v[172:175]\n\t" \
  "s_waitcnt vmcnt(8)\n\t" \
  "v_swap_b32 v89, v92\n\t" \
  "v_mfma_f32_16x16x32_bf16 v[168:171], a[84:87], v[84:87], v[168:171]\n\t" \
  "v_swap_b32 v91, v94\n\t" \
  "v_mfma_f32_16x16x32_bf16 v[172:175], a[92:95], v[84:87], v[172:175]\n\t" \
  "v_mov_b64 v[80:81], 0\n\t" \
  "v_mov_b64 v[82:83], 0\n\t" \
  "v_mfma_f32_16x16x32_bf16 v[176:179], a[80:83], v[88:91], v[176:179]\n\t" \
  "v_mov_b64 v[84:85], 0\n\t" \
  "v_mov_b64 v[86:87], 0\n\t" \
  "v_mfma_f32_16x16x32_bf16 v[180:183], a[88:91], v[88:91], v[180:183]\n\t" \
  "s_waitcnt vmcnt(6)\n\t" \
  "v_swap_b32 v97, v100\n\t" \
  "v_mfma_f32_16x16x32_bf16 v[176:179], a[84:87], v[92:95], v[176:179]\n\t" \
  "v_swap_b32 v99, v102\n\t" \
  "v_mfma_f32_16x16x32_bf16 v[180:183], a[92:95], v[92:95], v[180:183]\n\t" \
  "v_mov_b64 v[88:89], 0\n\t" \
  "v_mov_b64 v[90:91], 0\n\t" \
  "v_mfma_f32_16x16x32_bf16 v[168:171], a[96:99], v[96:99], v[168:171]\n\t" \
  "v_mov_b64 v[92:93], 0\n\t" \
  "v_mov_b64 v[94:95], 0\n\t" \
  "v_mfma_f32_16x16x32_bf16 v[172:175], a[104:107], v[96:99], v[172:175]\n\t" \
  "s_waitcnt vmcnt(4)\n\t" \
  "v_swap_b32 v105, v108\n\t" \
  "v_mfma_f32_16x16x32_bf16 v[168:171], a[100:103], v[100:103], v[168:171]\n\t" \
  "v_swap_b32 v107, v110\n\t" \
  "v_mfma_f32_16x16x32_bf16 v[172:175], a[108:111], v[100:103], v[172:175]\n\t" \
  "v_mov_b64 v[96:97], 0\n\t" \
  "v_mov_b64 v[98:99], 0\n\t" \
  "v_mfma_f32_16x16x32_bf16 v[176:179], a[96:99], v[104:107], v[176:179]\n\t" \
  "v_mov_b64 v[100:101], 0\n\t" \
  "v_mov_b64 v[102:103], 0\n\t" \
  "v_mfma_f32_16x16x32_bf16 v[180:183], a[104:107], v[104:107], v[180:183]\n\t" \
  "s_waitcnt vmcnt(2)\n\t" \
  "v_swap_b32 v113, v116\n\t" \
  "v_mfma_f32_16x16x32_bf16 v[176:179], a[100:103], v[108:111], v[176:179]\n\t" \
  "v_swap_b32 v115, v118\n\t" \
  "v_mfma_f32_16x16x32_bf16 v[180:183], a[108:111], v[108:111], v[180:183]\n\t" \
  "s_nop 0\n\t" \
  "v_mfma_f32_16x16x32_bf16 v[168:171], a[112:115], v[112:115], v[168:171]\n\t" \
  "v_mfma_f32_16x16x32_bf16 v[172:175], a[120:123], v[112:115], v[172:175]\n\t" \
  "s_waitcnt vmcnt(0)\n\t" \
  "v_swap_b32 v121, v124\n\t" \
  "v_mfma_f32_16x16x32_bf16 v[168:171], a[116:119], v[116:119], v[168:171]\n\t" \
  "v_swap_b32 v123, v126\n\t" \
  "v_mfma_f32_16x16x32_bf16 v[172:175], a[124:127], v[116:119], v[172:175]\n\t" \
  "s_nop 0\n\t" \
  "v_mfma_f32_16x16x32_bf16 v[176:179], a[112:115], v[120:123], v[176:179]\n\t" \
  "v_mfma_f32_16x16x32_bf16 v[180:183], a[120:123], v[120:123], v[180:183]\n\t" \
  "v_mfma_f32_16x16x32_bf16 v[176:179], a[116:119], v[124:127], v[176:179]\n\t" \
  "v_mfma_f32_16x16x32_bf16 v[180:183], a[124:127], v[124:127], v[180:183]\n\t" \
  "s_nop 6\n\t" \
  "39:\n\t" \
  "s_add_i32 s40, s40, 2\n\t" \
  "v_cmp_u_f32 vcc, v168, v176\n\t" \
  "s_cbranch_vccnz 32f\n\t" \
  "33:\n\t" \
  "s_barrier\n\t" \
  "s_add_i32 s85, s40, -1\n\t" \
  "s_cmp_ge_i32 s40, 64\n\t" \
  "s_cbranch_scc1 47f\n\t" \
  "s_and_b32 s82, s85, 2\n\t" \
  "s_lshl_b32 s82, s82, 13\n\t" \
  "s_and_b32 s80, s85, 1\n\t" \
  "s_lshl_b32 s80, s80, 12\n\t" \
  "s_add_i32 s82, s82, s80\n\t" \
  "s_add_i32 s82, s82, 0x84000\n\t" \
  "s_mov_b32 exec_hi, 0\n\t" \
  "buffer_load_dword v184, v200, %[rh], s82 offen sc1\n\t" \
  "buffer_load_dword v185, v200, %[rh], s52 offen sc1\n\t" \
  "s_mov_b64 exec, -1\n\t" \
  "47:\n\t" \
  "ds_write_b128 v199, v[168:171] offset:0\n\t" \
  "ds_write_b128 v199, v[172:175] offset:1024\n\t" \
  "ds_write_b128 v199, v[176:179] offset:8192\n\t" \
  "ds_write_b128 v199, v[180:183] offset:9216\n\t" \
  "s_mov_b32 s59, 0\n\t" \
  "s_and_b32 s58, s58, 4\n\t" \
  "s_cmp_lt_i32 s40, 64\n\t" \
  "s_cbranch_scc1 20b\n\t" \
  "s_waitcnt lgkmcnt(0)\n\t" \
  "s_barrier\n\t" \
  "s_mov_b32 s58, 64\n\t" \
  "s_branch 7f\n\t" \
  "22:\n\t" \
  "s_mov_b32 s81, 0\n\t" \
  "s_cmp_eq_u32 s40, 0\n\t" \
  "s_cbranch_scc1 24b\n\t" \
  "s_bitcmp1_b32 s58, 2\n\t" \
  "s_cbranch_scc1 24b\n\t" \
  "s_mov_b64 exec, -1\n\t" \
  "s_sleep 10\n\t" \
  "s_add_u32 s59, s59, 0x10000\n\t" \
  "s_cmp_lt_u32 s59, 0x100000\n\t" \
  "s_cbranch_scc1 19b\n\t" \
  "s_or_b32 s58, s58, 1\n\t" \
  "s_branch 7f\n\t" \
  "14:\n\t" \
  "v_mov_b32 v184, 0\n\t" \
  "s_bitcmp1_b32 s58, 2\n\t" \
  "s_nop 0\n\t" \
  "s_cbranch_scc1 15b\n\t" \
  "s_mov_b32 s84, 870\n\t" \
  "s_add_u32 s59, s59, 0x10000\n\t" \
  "s_cmp_lt_u32 s59, 0x100000\n\t" \
  "s_mov_b64 exec, vcc\n\t" \
  "buffer_load_dword v187, v200, %[rh], s78 offen sc1\n\t" \
  "s_mov_b32 exec_lo, -1\n\t" \
  "s_cbranch_scc1 13b\n\t" \
  "s_mov_b64 exec, -1\n\t" \
  "s_or_b32 s58, s58, 1\n\t" \
  "s_branch 7f\n\t" \
  "37:\n\t" \
  "s_barrier\n\t" \
  "v_mov_b64 v[40:41], 0\n\t" \
  "v_mov_b64 v[42:43], 0\n\t" \
  "v_mov_b64 v[44:45], 0\n\t" \
  "v_mov_b64 v[46:47], 0\n\t" \
  "v_mov_b64 v[48:49], 0\n\t" \
  "v_mov_b64 v[50:51], 0\n\t" \
  "v_mov_b64 v[52:53], 0\n\t" \
  "v_mov_b64 v[54:55], 0\n\t" \
  "v_mov_b64 v[56:57], 0\n\t" \
  "v_mov_b64 v[58:59], 0\n\t" \
  "v_mov_b64 v[60:61], 0\n\t" \
  "v_mov_b64 v[62:63], 0\n\t" \
  "v_mov_b64 v[64:65], 0\n\t" \
  "v_mov_b64 v[66:67], 0\n\t" \
  "v_mov_b64 v[68:69], 0\n\t" \
  "v_mov_b64 v[70:71], 0\n\t" \
  "v_mov_b64 v[72:73], 0\n\t" \
  "v_mov_b64 v[74:75], 0\n\t" \
  "v_mov_b64 v[76:77], 0\n\t" \
  "v_mov_b64 v[78:79], 0\n\t" \
  "v_mov_b64 v[80:81], 0\n\t" \
  "v_mov_b64 v[82:83], 0\n\t" \
  "v_mov_b64 v[84:85], 0\n\t" \
  "v_mov_b64 v[86:87], 0\n\t" \
  "v_mov_b64 v[88:89], 0\n\t" \
  "v_mov_b64 v[90:91], 0\n\t" \
  "v_mov_b64 v[92:93], 0\n\t" \
  "v_mov_b64 v[94:95], 0\n\t" \
  "v_mov_b64 v[96:97], 0\n\t" \
  "v_mov_b64 v[98:99], 0\n\t" \
  "v_mov_b64 v[100:101], 0\n\t" \
  "v_mov_b64 v[102:103], 0\n\t" \
  "v_mov_b64 v[136:137], 0\n\t" \
  "v_mov_b64 v[138:139], 0\n\t" \
  "v_mov_b64 v[140:141], 0\n\t" \
  "v_mov_b64 v[142:143], 0\n\t" \
  "s_add_i32 s40, s40, 2\n\t" \
  "s_branch 33b\n\t" \
  "32:\n\t" \
  "s_and_b32 s80, s58, 36\n\t" \
  "s_cbranch_scc1 33b\n\t" \
  "s_add_i32 s40, s40, -2\n\t" \
  "34:\n\t" \
  "s_or_b32 s59, s59, 128\n\t" \
  "s_and_b32 s76, s40, 3\n\t" \
  "s_lshl_b32 s76, s76, 18\n\t" \
  "s_add_i32 s77, s76, 0x10000\n\t" \
  "s_add_i32 s78, s76, 0x1000\n\t" \
  "s_add_i32 s79, s76, 0x11000\n\t" \
  "35:\n\t" \
  "buffer_load_dwordx4 v[0:3], v210, %[rh], s76 offen offset:0 sc1\n\t" \
  "buffer_load_dwordx4 v[4:7], v210, %[rh], s76 offen offset:1024 sc1\n\t" \
  "buffer_load_dwordx4 v[8:11], v210, %[rh], s77 offen offset:0 sc1\n\t" \
  "buffer_load_dwordx4 v[12:15], v210, %[rh], s77 offen offset:1024 sc1\n\t" \
  "buffer_load_dwordx4 v[16:19], v210, %[rh], s76 offen offset:2048 sc1\n\t" \
  "buffer_load_dwordx4 v[20:23], v210, %[rh], s76 offen offset:3072 sc1\n\t" \
  "buffer_load_dwordx4 v[24:27], v210, %[rh], s77 offen offset:2048 sc1\n\t" \
  "buffer_load_dwordx4 v[28:31], v210, %[rh], s77 offen offset:3072 sc1\n\t" \
  "buffer_load_dwordx4 v[32:35], v210, %[rh], s78 offen offset:0 sc1\n\t" \
  "buffer_load_dwordx4 v[36:39], v210, %[rh], s78 offen offset:1024 sc1\n\t" \
  "buffer_load_dwordx4 v[40:43], v210, %[rh], s79 offen offset:0 sc1\n\t" \
  "buffer_load_dwordx4 v[44:47], v210, %[rh], s79 offen offset:1024 sc1\n\t" \
  "buffer_load_dwordx4 v[48:51], v210, %[rh], s78 offen offset:2048 sc1\n\t" \
  "buffer_load_dwordx4 v[52:55], v210, %[rh], s78 offen offset:3072 sc1\n\t" \
  "buffer_load_dwordx4 v[56:59], v210, %[rh], s79 offen offset:2048 sc1\n\t" \
  "buffer_load_dwordx4 v[60:63], v210, %[rh], s79 offen offset:3072 sc1\n\t" \
  "buffer_load_dwordx4 v[64:67], v211, %[rh], s76 offen offset:0 sc1\n\t" \
  "buffer_load_dwordx4 v[68:71], v211, %[rh], s76 offen offset:1024 sc1\n\t" \
  "buffer_load_dwordx4 v[72:75], v211, %[rh], s77 offen offset:0 sc1\n\t" \
  "buffer_load_dwordx4 v[76:79], v211, %[rh], s77 offen offset:1024 sc1\n\t" \
  "buffer_load_dwordx4 v[80:83], v211, %[rh], s76 offen offset:2048 sc1\n\t" \
  "buffer_load_dwordx4 v[84:87], v211, %[rh], s76 offen offset:3072 sc1\n\t" \
  "buffer_load_dwordx4 v[88:91], v211, %[rh], s77 offen offset:2048 sc1\n\t" \
  "buffer_load_dwordx4 v[92:95], v211, %[rh], s77 offen offset:3072 sc1\n\t" \
  "buffer_load_dwordx4 v[96:99], v211, %[rh], s78 offen offset:0 sc1\n\t" \
  "buffer_load_dwordx4 v[100:103], v211, %[rh], s78 offen offset:1024 sc1\n\t" \
  "buffer_load_dwordx4 v[104:107], v211, %[rh], s79 offen offset:0 sc1\n\t" \
  "buffer_load_dwordx4 v[108:111], v211, %[rh], s79 offen offset:1024 sc1\n\t" \
  "buffer_load_dwordx4 v[112:115], v211, %[rh], s78 offen offset:2048 sc1\n\t" \
  "buffer_load_dwordx4 v[116:119], v211, %[rh], s78 offen offset:3072 sc1\n\t" \
  "buffer_load_dwordx4 v[120:123], v211, %[rh], s79 offen offset:2048 sc1\n\t" \
  "buffer_load_dwordx4 v[124:127], v211, %[rh], s79 offen offset:3072 sc1\n\t" \
  "s_waitcnt vmcnt(0)\n\t" \
  "s_bitcmp1_b32 s58, 2\n\t" \
  "s_cbranch_scc1 36f\n\t" \
  "v_max_u32 v148, v0, v1\n\t" \
  "v_max3_u32 v148, v148, v2, v3\n\t" \
  "v_max3_u32 v148, v148, v4, v5\n\t" \
  "v_max3_u32 v148, v148, v6, v7\n\t" \
  "v_max3_u32 v148, v148, v8, v9\n\t" \
  "v_max3_u32 v148, v148, v10, v11\n\t" \
  "v_max3_u32 v148, v148, v12, v13\n\t" \
  "v_max3_u32 v148, v148, v14, v15\n\t" \
  "v_max3_u32 v148, v148, v16, v17\n\t" \
  "v_max3_u32 v148, v148, v18, v19\n\t" \
  "v_max3_u32 v148, v148, v20, v21\n\t" \
  "v_max3_u32 v148, v148, v22, v23\n\t" \
  "v_max3_u32 v148, v148, v24, v25\n\t" \
  "v_max3_u32 v148, v148, v26, v27\n\t" \
  "v_max3_u32 v148, v148, v28, v29\n\t" \
  "v_max3_u32 v148, v148, v30, v31\n\t" \
  "v_max3_u32 v148, v148, v32, v33\n\t" \
  "v_max3_u32 v148, v148, v34, v35\n\t" \
  "v_max3_u32 v148, v148, v36, v37\n\t" \
  "v_max3_u32 v148, v148, v38, v39\n\t" \
  "v_max3_u32 v148, v148, v40, v41\n\t" \
  "v_max3_u32 v148, v148, v42, v43\n\t" \
  "v_max3_u32 v148, v148, v44, v45\n\t" \
  "v_max3_u32 v148, v148, v46, v47\n\t" \
  "v_max3_u32 v148, v148, v48, v49\n\t" \
  "v_max3_u32 v148, v148, v50, v51\n\t" \
  "v_max3_u32 v148, v148, v52, v53\n\t" \
  "v_max3_u32 v148, v148, v54, v55\n\t" \
  "v_max3_u32 v148, v148, v56, v57\n\t" \
  "v_max3_u32 v148, v148, v58, v59\n\t" \
  "v_max3_u32 v148, v148, v60, v61\n\t" \
  "v_max3_u32 v148, v148, v62, v63\n\t" \
  "v_max3_u32 v148, v148, v64, v65\n\t" \
  "v_max3_u32 v148, v148, v66, v67\n\t" \
  "v_max3_u32 v148, v148, v68, v69\n\t" \
  "v_max3_u32 v148, v148, v70, v71\n\t" \
  "v_max3_u32 v148, v148, v72, v73\n\t" \
  "v_max3_u32 v148, v148, v74, v75\n\t" \
  "v_max3_u32 v148, v148, v76, v77\n\t" \
  "v_max3_u32 v148, v148, v78, v79\n\t" \
  "v_max3_u32 v148, v148, v80, v81\n\t" \
  "v_max3_u32 v148, v148, v82, v83\n\t" \
  "v_max3_u32 v148, v148, v84, v85\n\t" \
  "v_max3_u32 v148, v148, v86, v87\n\t" \
  "v_max3_u32 v148, v148, v88, v89\n\t" \
  "v_max3_u32 v148, v148, v90, v91\n\t" \
  "v_max3_u32 v148, v148, v92, v93\n\t" \
  "v_max3_u32 v148, v148, v94, v95\n\t" \
  "v_max3_u32 v148, v148, v96, v97\n\t" \
  "v_max3_u32 v148, v148, v98, v99\n\t" \
  "v_max3_u32 v148, v148, v100, v101\n\t" \
  "v_max3_u32 v148, v148, v102, v103\n\t" \
  "v_max3_u32 v148, v148, v104, v105\n\t" \
  "v_max3_u32 v148, v148, v106, v107\n\t" \
  "v_max3_u32 v148, v148, v108, v109\n\t" \
  "v_max3_u32 v148, v148, v110, v111\n\t" \
  "v_max3_u32 v148, v148, v112, v113\n\t" \
  "v_max3_u32 v148, v148, v114, v115\n\t" \
  "v_max3_u32 v148, v148, v116, v117\n\t" \
  "v_max3_u32 v148, v148, v118, v119\n\t" \
  "v_max3_u32 v148, v148, v120, v121\n\t" \
  "v_max3_u32 v148, v148, v122, v123\n\t" \
  "v_max3_u32 v148, v148, v124, v125\n\t" \
  "v_max3_u32 v148, v148, v126, v127\n\t" \
  "v_cmp_eq_u32 vcc, -1, v148\n\t" \
  "s_cbranch_vccz 36f\n\t" \
  "s_add_u32 s59, s59, 0x1000000\n\t" \
  "s_cmp_lt_u32 s59, 0x10000000\n\t" \
  "s_cbranch_scc1 35b\n\t" \
  "s_or_b32 s58, s58, 9\n\t" \
  "s_branch 7f\n\t" \
  "36:\n\t" \
  "s_mov_b32 s97, 0\n\t" \
  "s_and_b32 s58, s58, 4\n\t" \
  "s_or_b32 s58, s58, 32\n\t" \
  "s_branch 31b\n\t" \
  "18:\n\t" \
  "v_mov_b32 v186, -1\n\t" \
  "s_and_b32 s80, s40, 3\n\t" \
  "s_mul_i32 s77, s80, 0xc000\n\t" \
  "s_add_i32 s77, s77, 0x40000\n\t" \
  "s_lshl_b32 s78, s80, 14\n\t" \
  "s_add_i32 s78, s78, 0x70000\n\t" \
  "s_mov_b32 s57, 0\n\t" \
  "s_add_i32 s96, s77, 0x1000\n\t" \
  "s_and_b32 s98, s40, 2\n\t" \
  "s_lshl_b32 s98, s98, 13\n\t" \
  "s_and_b32 s80, s40, 1\n\t" \
  "s_lshl_b32 s80, s80, 12\n\t" \
  "s_add_i32 s98, s98, s80\n\t" \
  "s_add_i32 s98, s98, 0x84000\n\t" \
  "12:\n\t" \
  "s_or_b32 s58, s58, 3\n\t" \
  "11:\n\t" \
  "buffer_load_dwordx4 v[0:3], v193, %[rh], s77 offen offset:0 sc1\n\t" \
  "buffer_load_dwordx4 v[4:7], v193, %[rh], s77 offen offset:1024 sc1\n\t" \
  "buffer_load_dwordx4 v[8:11], v193, %[rh], s77 offen offset:2048 sc1\n\t" \
  "buffer_load_dwordx4 v[12:15], v193, %[rh], s77 offen offset:3072 sc1\n\t" \
  "buffer_load_dwordx4 v[16:19], v193, %[rh], s96 offen offset:0 sc1\n\t" \
  "buffer_load_dwordx4 v[20:23], v193, %[rh], s96 offen offset:1024 sc1\n\t" \
  "buffer_load_dwordx4 v[24:27], v193, %[rh], s96 offen offset:2048 sc1\n\t" \
  "buffer_load_dwordx4 v[28:31], v193, %[rh], s96 offen offset:3072 sc1\n\t" \
  "buffer_load_dwordx4 v[32:35], v195, %[rh], s77 offen offset:0 sc1\n\t" \
  "buffer_load_dwordx4 v[36:39], v195, %[rh], s77 offen offset:2048 sc1\n\t" \
  "v_mov_b64 v[40:41], 0\n\t" \
  "v_mov_b64 v[42:43], 0\n\t" \
  "v_mov_b64 v[44:45], 0\n\t" \
  "v_mov_b64 v[46:47], 0\n\t" \
  "v_mov_b64 v[48:49], 0\n\t" \
  "v_mov_b64 v[50:51], 0\n\t" \
  "v_mov_b64 v[52:53], 0\n\t" \
  "v_mov_b64 v[54:55], 0\n\t" \
  "v_mov_b64 v[56:57], 0\n\t" \
  "v_mov_b64 v[58:59], 0\n\t" \
  "v_mov_b64 v[60:61], 0\n\t" \
  "v_mov_b64 v[62:63], 0\n\t" \
  "v_mov_b64 v[64:65], 0\n\t" \
  "v_mov_b64 v[66:67], 0\n\t" \
  "v_mov_b64 v[68:69], 0\n\t" \
  "v_mov_b64 v[70:71], 0\n\t" \
  "v_mov_b64 v[72:73], 0\n\t" \
  "v_mov_b64 v[74:75], 0\n\t" \
  "v_mov_b64 v[76:77], 0\n\t" \
  "v_mov_b64 v[78:79], 0\n\t" \
  "v_mov_b64 v[80:81], 0\n\t" \
  "v_mov_b64 v[82:83], 0\n\t" \
  "v_mov_b64 v[84:85], 0\n\t" \
  "v_mov_b64 v[86:87], 0\n\t" \
  "v_mov_b64 v[88:89], 0\n\t" \
  "v_mov_b64 v[90:91], 0\n\t" \
  "v_mov_b64 v[92:93], 0\n\t" \
  "v_mov_b64 v[94:95], 0\n\t" \
  "v_mov_b64 v[96:97], 0\n\t" \
  "v_mov_b64 v[98:99], 0\n\t" \
  "v_mov_b64 v[100:101], 0\n\t" \
  "v_mov_b64 v[102:103], 0\n\t" \
  "v_mov_b64 v[136:137], 0\n\t" \
  "v_mov_b64 v[138:139], 0\n\t" \
  "v_mov_b64 v[140:141], 0\n\t" \
  "v_mov_b64 v[142:143], 0\n\t" \
  "s_waitcnt vmcnt(0)\n\t" \
  "s_add_u32 s59, s59, 1\n\t" \
  "s_bitcmp1_b32 s58, 2\n\t" \
  "s_cbranch_scc1 10b\n\t" \
  "v_max_u32 v148, v0, v1\n\t" \
  "v_max3_u32 v148, v148, v2, v3\n\t" \
  "v_max3_u32 v148, v148, v4, v5\n\t" \
  "v_max3_u32 v148, v148, v6, v7\n\t" \
  "v_max3_u32 v148, v148, v8, v9\n\t" \
  "v_max3_u32 v148, v148, v10, v11\n\t" \
  "v_max3_u32 v148, v148, v12, v13\n\t" \
  "v_max3_u32 v148, v148, v14, v15\n\t" \
  "v_max3_u32 v148, v148, v16, v17\n\t" \
  "v_max3_u32 v148, v148, v18, v19\n\t" \
  "v_max3_u32 v148, v148, v20, v21\n\t" \
  "v_max3_u32 v148, v148, v22, v23\n\t" \
  "v_max3_u32 v148, v148, v24, v25\n\t" \
  "v_max3_u32 v148, v148, v26, v27\n\t" \
  "v_max3_u32 v148, v148, v28, v29\n\t" \
  "v_max3_u32 v148, v148, v30, v31\n\t" \
  "v_max3_u32 v148, v148, v32, v33\n\t" \
  "v_max3_u32 v148, v148, v34, v35\n\t" \
  "v_max3_u32 v148, v148, v36, v37\n\t" \
  "v_max3_u32 v148, v148, v38, v39\n\t" \
  "v_cmp_eq_u32 vcc, -1, v148\n\t" \
  "s_cbranch_vccz 10b\n\t" \
  "s_bitcmp1_b32 s59, 6\n\t" \
  "s_cbranch_scc0 11b\n\t" \
  "7:\n\t" \
  "92:\n\t" \
  "s_mov_b32 s42, s58\n\t" \
  "s_waitcnt vmcnt(0) lgkmcnt(0)\n\t" \
  ""

#define V53 "v0", "v1", "v2", "v3", "v4", "v5", "v6", "v7", "v8", "v9", "v10", "v11", "v12", "v13", "v14", "v15", "v16", "v17", \
  "v18", "v19", "v20", "v21", "v22", "v23", "v24", "v25", "v26", "v27", "v28", "v29", "v30", "v31", "v32", "v33", "v34", "v35", \
  "v36", "v37", "v38", "v39", "v40", "v41", "v42", "v43", "v44", "v45", "v46", "v47", "v48", "v49", "v50", "v51", "v52"
#define V100 V53, "v53", "v54", "v55", "v56", "v57", "v58", "v59", "v60", "v61", "v62", "v63", "v64", "v65", "v66", "v67", "v68", "v69", "v70", \
  "v71", "v72", "v73", "v74", "v75", "v76", "v77", "v78", "v79", "v80", "v81", "v82", "v83", "v84", "v85", "v86", "v87", "v88", \
  "v89", "v90", "v91", "v116", "v117", "v118", "v119"
#define STEP_PV
#define CB_CL "s60", "s61", "s62", "s63", "s64", "s65", "s66", "s67", "s68", "s69",
#define STEPS(BODY, ...) asm volatile(BODY : "=&{v[92:99]}"(X), "+{s[40:55]}"(P) STEP_PV \
  : "{v[120:151]}"(W.v[0]), "{v[152:183]}"(W.v[1]), "{v[184:215]}"(W.v[2]), "{v[216:247]}"(W.v[3]), "{v[100:115]}"(C), [rh] "s"(rh), [wv] "s"(w), [ru] "s"(rum), [sel] "s"(sel) __VA_ARGS__ \
  : "memory", "vcc", "scc", "s56", "s57", "s58", "s59", "s76", "s77", "s78", "s79", "s80", "s81", "s82", "s83", "s84", "s85", "s86", "s87", "s88", "s89", \
    "s90", "s91", "s92", "s93", "s94", "s95", "s96", "s97", V100)
__device__ __forceinline__ void keep_a(const Wts& W) {
#pragma unroll
  for (int i = 0; i < 32; ++i) asm volatile("" :: "a"(W.a[i]));
}
__device__ __forceinline__ char* lds_base(char* smem, int off) { asm volatile("" : "+v"(off)); return smem + off; }
__device__ __forceinline__ u32x2 tr4(unsigned p) { return __builtin_bit_cast(u32x2, __builtin_amdgcn_ds_read_tr16_b64_v4i16((LAS bf16x4*)(uintptr_t)p)); }
__device__ __forceinline__ float rb1(float x) { return __uint_as_float(pk2(0.f, x)); }

__device__ __forceinline__ void steps_init(u32x16& P, int h, int w, unsigned place) {
#pragma unroll
  for (int i = 0; i < 16; ++i) P[i] = 0;
  P[2] = 1; P[3] = CDUR; P[14] = H_MEM + (w >> 1) * CHK + (w & 1) * 4096 + (h & RHO) * 1024; P[15] = place;
}
__device__ __forceinline__ void steps_enter(u32x16& P, Clk& ck, bool dead) {
  ck.sync();
  P[2] = (P[2] & 3u) | (dead ? 4u : 0u); P[6] = ck.c2t << 15; P[11] = ck.ce; P[12] = ck.t2c; P[13] = ck.te;
#pragma unroll
  for (int i = 0; i < 16; ++i) P[i] = __builtin_amdgcn_readfirstlane(P[i]);
}

__device__ __forceinline__ void role_qk(const ChainArgs& a, int h, char* smem, const int w, Gate& G) {
  int lane = opaque_tid(0);
  const int b = h; (void)b;
  u64* prof = a.prof; (void)prof;
  PDECL(L_PRF, 4);
  const float CS = 0.08838834764831845f * 1.4426950408889634f;
  GAS unsigned* abortw = (GAS unsigned*)(a.sync + SY_ABORT);
  bool dead = false;
  Wts W;
  Clk ck; ck.init();
  u32x4 gv;
  if (QK_D) while (__builtin_amdgcn_s_memrealtime() - G.entry < QK_D) __builtin_amdgcn_s_sleep(8);
  load_weights<0>(a, h, w, lane, smem, W, G, gv);
  TP(1);
  if (!G.look(a.sync, gv)) return;
  PZERO;
  const Sel sel = G.sel;
  const __amdgpu_buffer_rsrc_t rh = rsrc_hot(a.sync, sel), rum = rsrc_u(a.sync);
  auto pm_o = [&](int l) { return 2 * CHK + h * 256 + (l & 1) * 128 + (l >> 3) * 16 + (l & 6) * 2; };
  auto fac = [&](int l, int hf) { return ((l & 7) >> 1) == 2 * (l >> 5) + ((h >> 1) & 1) && ((l >> 3) & 1) == hf ? 1.f : 0.f; };
  u32x16 C, P;
  {
    const int r = lane & 15, g = lane >> 4;
    const unsigned l0 = (unsigned)(uintptr_t)(LAS char*)smem;
    C[0] = lane * 16; C[1] = l0 + L_MEM + lane * 16; C[2] = l0 + L_WTS + w * 4096 + lane * 16; C[3] = C[2] + 65536;
    C[4] = (lane & 2) ? 0xEEEE : 0x4444;
    C[5] = l0 + L_XCH + (16 * w + 2 * (r >> 2) + (r & 1)) * 32 + g * 8 + ((r >> 1) & 1) * 4;
    C[6] = l0 + L_XK + (32 * w + r) * 16 + g * 4;
    C[7] = w == 3 ? H_PX + (h >> 3) * CHK + (h & 7) * 1024 + lane * 16 : w ? (2 * h + 1) * CHK + (w - 1) * 4096 + lane * 16 : l0 + L_XCH + (8 * g + (r >> 2)) * 32 + (r & 3) * 8;
    C[8] = l0 + L_XK + (8 * g + (r >> 2)) * 16 + (r & 1) * 8;
    C[9] = w ? (lane & 1) * 4096 : (lane == 0 ? H_ANN + h * 128 : 0x7fffffffu);
    C[10] = w == 0 ? (h >> 4) * CHK + (h & 15) * 512 + ((lane >> 1) & 1) * 256 + (lane >> 5) * 128 + (lane & 1) * 64 + ((lane >> 3) & 3) * 16 + ((lane >> 2) & 1) * 8 :
            w < 3 ? H_PX + ((w - 1) * 4 + (h >> 3)) * CHK + (h & 7) * 1024 + lane * 16 :
            lane < 32 ? (h >> 4) * CHK + (h & 15) * 512 + lane * 16 : (lane < 48 ? 2 * CHK + h * 256 + (lane - 32) * 16 : 0x7fffffffu);
    C[11] = w == 0 ? pm_o(lane) : (lane < (w == 3 ? 32 : 16) ? H_PX + (w == 3 ? 8 + g : 7 + w) * CHK + hpos(h) * 256 + r * 16 : 0x7fffffffu);
    C[12] = H_TR + (lane & 31) * 128; C[13] = l0 + L_PRF + w * 1024 + (lane >> 6);
    unsigned mk = 0;
#pragma unroll
    for (int i = 0; i < 8; ++i) mk |= (16 * ((i >> 1) & 1) + 8 * (g & 1) + 4 * (g >> 1) + 2 * (i & 1) + (i >> 2) > 16 * (w - 1) + r ? 1u : 0u) << i;
    C[14] = w ? mk : __float_as_uint(fac(lane, 0)); C[15] = __float_as_uint(fac(lane, 1));
    asm volatile("" : "+v"(C));
  }
  steps_init(P, h, w, 2 * h * CHK + (3 * w - 3) * 1024);
  u32x4 k0[2];
  start_wait(a.sync, abortw, lane, dead, CT_Q + ct_near(sel) + h, 1u, [&]() { for (int i = 0; i < 2; ++i) k0[i] = ld16(rum, x_slot(1, 0, h, sel) + (2 * w + i) * 1024 + lane * 16); });
  G.arrive(a.sync);
#pragma unroll
  for (int i = 0; i < 2; ++i) *(u32x4*)(smem + L_KX + (2 * w + i) * 1024 + lane * 16) = k0[i];
  SSTAMP(96 + h);
  TP(2);
  GAS unsigned* kcp = ctr(a, CT_Q + ct_near(sel) + h);
  unsigned spins = 0;
  while (true) {
    u32x8 X; u32x16 pv; (void)pv;
    steps_enter(P, ck, dead);
    STEPS(QK_BODY, , [cs] "s"(CS), [kc] "s"(kcp));
    keep_a(W);
    const unsigned res = __builtin_amdgcn_readfirstlane(P[2]), s = __builtin_amdgcn_readfirstlane(P[4]);
    lane = opaque_tid(0);
    if (res & 8u) {
      auto sw = [](float v, int k) { return __int_as_float(k == 0 ? __builtin_amdgcn_mov_dpp(__float_as_int(v), 0xB1, 0xf, 0xf, false) :
                                                           k == 1 ? __builtin_amdgcn_mov_dpp(__float_as_int(v), 0x4E, 0xf, 0xf, false) :
                                                                    __builtin_amdgcn_mov_dpp(__float_as_int(v), 0x141, 0xf, 0xf, false)); };
      float x[5], d = fmaxf(__uint_as_float(X[4]), 0.f);
      d = fmaxf(d, sw(d, 0)); d = fmaxf(d, sw(d, 1)); d = fmaxf(d, sw(d, 2));
#pragma unroll
      for (int i = 0; i < 5; ++i) x[i] = __builtin_amdgcn_exp2f(__uint_as_float(X[i]) - d);
      float z = ((x[0] + x[1]) + (x[2] + x[3])) + x[4];
      z += sw(z, 0); z += sw(z, 1); z += sw(z, 2);
      z = __builtin_amdgcn_rcpf(z);
      const float y = sw(x[4], 0), f = __builtin_amdgcn_exp2f(-d) * z;
      const unsigned pm = (lane & 1) ? pk2(fac(lane, 0) * f, fac(lane, 1) * f) : pk2(x[4] * z, y * z);
      if (!(__uint_as_float(X[5]) < 1.0e18f))
        __builtin_amdgcn_raw_buffer_store_b32(nosent(pm), rh, pm_o(lane), H_P + ((s - 1) & 3) * 3 * CHK, ST_AUX);
    }
    if (res & 64u) break;
    if (res & 32u) {
      u32x4 k[3];
      start_wait(a.sync, abortw, lane, dead, CT_Q + ct_near(sel) + h, s + 1, [&]() { for (int i = 0; i < 3; ++i) k[i] = ld16(rum, x_slot(1, s, h, sel) + min(3 * w - 3 + i, 7) * 1024 + lane * 16); });
#pragma unroll
      for (int i = 0; i < 3; ++i) *(u32x4*)(smem + L_KX + min(3 * w - 3 + i, 7) * 1024 + lane * 16) = k[i];
    }
    if (res & 17u) {
      spins |= 63u;
      if (dead || poll_fail(spins, abortw, lane)) dead = true;
    }
  }
  PDUMP(0);
}

__device__ __forceinline__ void role_vo(const ChainArgs& a, int h, char* smem, const int w, Gate& G) {
  int lane = opaque_tid(0);
  const int b = 32 + h; (void)b;
  u64* prof = a.prof; (void)prof;
  PDECL(L_PRF, 4);
  GAS unsigned* abortw = (GAS unsigned*)(a.sync + SY_ABORT);
  bool dead = false;
  Wts W;
  Clk ck; ck.init();
  u32x4 gv;
  load_weights<1>(a, h, w, lane, smem, W, G, gv);
  if (!G.look(a.sync, gv)) return;
  PZERO;
  const Sel sel = G.sel;
  const __amdgpu_buffer_rsrc_t rh = rsrc_hot(a.sync, sel), rum = rsrc_u(a.sync);
  u32x16 C, P;
  {
    const int r = lane & 15, g = lane >> 4;
    const unsigned l0 = (unsigned)(uintptr_t)(LAS char*)smem;
#pragma unroll
    for (int i = 0; i < 16; ++i) C[i] = H_TR + (lane & 31) * 128;
    C[0] = lane * 16; C[1] = l0 + L_MEM + lane * 16; C[2] = l0 + L_WTS + w * 4096 + lane * 16; C[3] = C[2] + 65536;
    C[4] = (lane & 2) ? 0xEEEE : 0x4444;
    C[5] = l0 + L_XCH + (32 * w + r) * 16 + g * 4;
    C[6] = (lane & 8) ? 0xEEEE : 0x4444;
    C[7] = l0 + L_XCH + (32 * (g >> 1) + 8 * (g & 1) + (r >> 2)) * 16 + (r & 1) * 8;
    C[8] = ((g & 1) * 8 + (g >> 1)) * CHK + r * 16;
    C[13] = l0 + L_PRF + w * 1024 + (lane >> 6);
    asm volatile("" : "+v"(C));
  }
  start_wait(a.sync, abortw, lane, dead, -1, 0u, [] {});
  G.arrive(a.sync);
  SSTAMP(128 + h);
  steps_init(P, h, w, SY_UM + (16 * (h & 3) + 2 * w) * CHK + hpos(h) * 256);
  unsigned spins = 0;
  while (true) {
    u32x8 X; u32x16 pv; (void)pv;
    steps_enter(P, ck, dead);
    STEPS(VO_BODY);
    keep_a(W);
    lane = opaque_tid(0);
    if (__builtin_amdgcn_readfirstlane(P[2]) & 64u) break;
    spins |= 63u;
    if (dead || poll_fail(spins, abortw, lane)) dead = true;
  }
  PDUMP(0);
}

__device__ __forceinline__ void calibrate(const ChainArgs& a, int cal, int slot, u64 entry) {
  const u32x4 rs = own_rsrc(a.sync, 64 * PAGE);
  const unsigned so = cal * PAGE + (slot >> 1) * CHK + (slot & 1) * 4096;
  unsigned best[2] = {1u << 30, 1u << 30};
  bool sure;
  {
    unsigned z = 0;
    asm volatile("s_mov_b64 exec, 0xffff\n\tbuffer_atomic_or %0, %1, %2, %3 offen sc0 sc1\n\ts_mov_b64 exec, -1\n\ts_waitcnt vmcnt(0)"
                 : "+v"(z) : "v"((opaque_tid(0) & 8) * 1024 + (opaque_tid(0) & 7) * 256), "s"(rs), "s"(so) : "memory");
  }
  for (int i = 0;; ++i) {
    sure = min(best[0], best[1]) < CALT && max(best[0], best[1]) - min(best[0], best[1]) > 110u;
    if (i >= CALN && (sure || __builtin_amdgcn_s_memrealtime() - entry > CALM)) break;
#pragma unroll
    for (int hf = 0; hf < 2; ++hf) {
      u64 c0, c1;
      unsigned z = 0;
      asm volatile("s_mov_b64 exec, 1\n\ts_memtime %0\n\ts_waitcnt lgkmcnt(0)\n\tbuffer_atomic_or %2, off, %3, %4 sc0 sc1\n\ts_waitcnt vmcnt(0)\n\ts_memtime %1\n\ts_mov_b64 exec, -1\n\ts_waitcnt lgkmcnt(0)"
                   : "=&s"(c0), "=&s"(c1), "+v"(z) : "s"(rs), "s"(so + hf * 8192 + (i & 7) * 256) : "memory");
      best[hf] = min(best[hf], (unsigned)(c1 - c0));
    }
  }
  const u64 hs = (__builtin_amdgcn_s_memrealtime() & ~3ull) | (sure ? 2u : 0u) | ((best[1] < best[0] ? 1u : 0u) ^ die_half());
  if (opaque_tid(0) == 0) __hip_atomic_store((GAS u64*)ctr(a, CT_CAL + (slot >> 1)) + (slot & 1), hs, __ATOMIC_RELAXED, __HIP_MEMORY_SCOPE_AGENT);
}
__device__ __forceinline__ u32x4 pk8(f32x4 a, f32x4 b) { return (u32x4){pk2(a[0], a[1]), pk2(a[2], a[3]), pk2(b[0], b[1]), pk2(b[2], b[3])}; }
__device__ __forceinline__ unsigned pk2v(float a, float b) { unsigned r; asm volatile("v_cvt_pk_bf16_f32 %0, %1, %2" : "=v"(r) : "v"(a), "v"(b)); return r; }
__device__ __forceinline__ u32x4 pk8v(f32x4 a, f32x4 b) { asm volatile("s_nop 7"); return (u32x4){pk2v(a[0], a[1]), pk2v(a[2], a[3]), pk2v(b[0], b[1]), pk2v(b[2], b[3])}; }
__device__ __forceinline__ void st16g(u16* p, u32x4 v) { asm volatile("global_store_dwordx4 %0, %1, off " OUTP "\n\ts_nop 1" :: "v"(p), "v"(v) : "memory"); }
__device__ __forceinline__ void role_cb(const ChainArgs& a, int cbi, char* smem, const int w, Gate& G) {
  int lane = opaque_tid(0);
  const int b = 64 + cbi; (void)b;
  const int par = cbi >> 5, ci = cbi & 31, J = ci >> 3;
  u64* prof = a.prof; (void)prof;
  PDECL(65536, 8); PZERO;
  GAS unsigned* abortw = (GAS unsigned*)(a.sync + SY_ABORT);
  bool dead = false;
  const int tw = 2 * ci + (w & 1);
  const unsigned mo = (tw >> 5) * CHK + (tw & 31) * 256;
  Sel sel = 0;
  auto start = [&]() {
    lane = opaque_tid(0);
    SSTAMP(0 + ci);
    unsigned m0 = 0;
    if (par == 0 && w < 2) {
      const u16* m = a.mem0 + (2 * (lane >> 4) + ((lane >> 2) & 1)) * DM + 16 * tw + (lane & 8);
      m0 = m[lane & 3] | (unsigned)m[7 - (lane & 3)] << 16;
    }
    if (!G.look(a.sync, (u32x4){0u, 0u, 0u, 0u})) return false;
    sel = G.sel;
    SSTAMP(64 + ci);
    const __amdgpu_buffer_rsrc_t rh = rsrc_hot(a.sync, sel), rum = rsrc_u(a.sync);
    const unsigned s1v = ~(unsigned)(lane >> 6);
    const u32x4 sv = {s1v, s1v, s1v, s1v};
    auto um = [&](int k) {
#pragma unroll
      for (int i = 0; i < 4; ++i) st16(rum, near_half(SY_UM + (64 * k + tw) * CHK, sel) + (w >> 1) * 4096 + i * 1024 + lane * 16, sv);
    };
    um(par);
    const unsigned dv = cbi == H_DLT / CHK ? K_DEF * TU : s1v;
#pragma unroll
    for (int i = 0; i < 2; ++i) st16(rh, cbi >= 2 && cbi < 45 ? (cbi < 35 ? cbi * CHK : H_PX + (cbi - 35) * CHK) + w * 2048 + i * 1024 + lane * 16 : 0x7fffffffu, (u32x4){dv, s1v, s1v, s1v});
    if (par == 0 && w < 2) st4(rh, H_MEM + mo + lane * 4, nosent(m0));
    asm volatile("s_waitcnt vmcnt(0)" ::: "memory");
    lds_barrier();
    if (w == 0 && lane == 0) ctr_add(a, CT_INIT + J, 1u);
    SSTAMP(256 + ci);
    TP(6);
    G.arrive(a.sync);
    um(par + 2);
    {
      const __amdgpu_buffer_rsrc_t rx = mk_rsrc((void*)a.X, 4u << 20);
      u32x4 v[16];
#pragma unroll
      for (int i = 0; i < 16; ++i) v[i] = __builtin_amdgcn_raw_buffer_load_b128(rx, (unsigned)(cbi * 4096 + w * 1024 + lane * 16), (unsigned)(i * 262144), 0);
#pragma unroll
      for (int i = 0; i < 16; ++i) asm volatile("" :: "v"(v[i]));
    }
    SSTAMP(224 + ci);
    start_wait(a.sync, abortw, lane, dead, -1, 0u, [] {});
    return true;
  };
  if (!start()) return;
  Wts W;
#pragma unroll
  for (int i = 0; i < 32; ++i) asm volatile("" : "=a"(W.a[i]));
  u32x16 C, P; u32x8 C2;
  {
    const unsigned l0 = (unsigned)(uintptr_t)(LAS char*)smem, oor = 0x7fffffffu;
    const int r = lane & 7, t = (lane >> 3) & 1, g = lane >> 4, e = w & 1;
    const bool L = w < 2;
    C[0] = lane * 16;
    C[1] = L ? l0 + 32768 + e * 8192 + (lane & 2) * 512 + ((lane & 1) * 32 + (lane >> 2)) * 16 : e * CHK + t * 512 + (g & 1) * 256 + (r >> 2) * 128 + (g >> 1) * 64 + (r & 3) * 16;
    C[2] = L ? (4 * t + g) * 1024 + r * 16 : l0 + 81920 + e * 32768 + lane * 16;
    C[3] = L ? l0 + e * 1024 + lane * 8 : 2 * CHK + e * 4096 + (4 * (g >> 1) + 2 * t + (g & 1)) * 256 + 128 + r * 16;
    C[4] = ((16 * e + (lane >> 2)) * OUTW + DM + 32 * ci + 8 * (lane & 3)) * 2;
    C[5] = l0 + (L ? e * 8192 + lane * 16 : e * 512 + lane * 8); C[6] = l0 + 16384 + lane * 16; C[7] = l0 + 32768 + (L ? e * 8192 + 4096 : e * 2048) + lane * 16;
    C[8] = (lane & 31) * 128;
    C[9] = (8 * (g & 1) + 4 * (g >> 1) + 2 * t + (r >> 2)) * 16 + 4 * (r < 4 ? r : 7 - r);
    C[10] = (lane | e) ? oor : ci * 128;
    C[11] = L ? SY_CT + (CT_U + ct_near(sel) + 16 * e + (lane & 15)) * 128 : (t ? 0xEEEE : 0x4444); C[12] = L ? l0 + 81920 + e * 32768 + lane * 16 : ((lane & 2) ? 0xEEEE : 0x4444); C[13] = r < 4 ? 0x01000504u : 0x07060302u;
    C[14] = l0 + 49152 + 32 * e + (lane >> 6); C[15] = l0 + 49152 + (lane >> 6);
    C2[0] = L ? (lane < 16 ? lane * 16 : oor) : ux_tile(2 * ci) + lane * 16;
    C2[1] = lane == 0 && e && ci < 8 ? SY_CT + (CT_DONE + ct_near(~sel) + 8 * par + ci) * 128 : oor;
    C2[2] = (L ? ux_tile(2 * ci) : H_PX + 2 * e * CHK) + lane * 16;
#pragma unroll
    for (int n = 0; n < 4; ++n) C2[3 + n] = L ? H_PX + (8 + e) * CHK + (J ^ n) * 2048 + lane * 16 : C2[2] + CHK;
    C2[7] = l0 + 65536 + w * 2048 - 64 + (lane >> 6);
    asm volatile("" : "+v"(C), "+v"(C2));
#pragma unroll
    for (int i = 0; i < 16; ++i) P[i] = par - 2;
    P[2] = 48; P[6] = P[7] = K_DEF * TU; P[8] = SY_UM + tw * CHK + J * 2048; P[9] = H_MEM + mo; P[10] = 2 * CHK + J * 256; P[11] = e * 16 * UXH; P[13] = 1780; P[12] = H_DLT + par * 4096;
  }
  Clk ck; ck.init();
  unsigned spins = 0;
  while (true) {
    u32x16 pv; (void)pv;
    unsigned res = __builtin_amdgcn_readfirstlane(P[2]);
    if (res & 64u) break;
    const u32x4 rh2 = own_rsrc(a.sync + near_half(SY_HOT, sel), H_BYTES - 8192), ra = own_rsrc(a.sync, SY_BYTES), ro = own_rsrc(a.out, S_LEN * OUTW * 2);
    if (res & 1u) {
      spins |= 63u;
      if (dead || poll_fail(spins, abortw, opaque_tid(0))) dead = true;
    }
    ck.sync();
    P[2] = (res & 58u) | (dead ? 4u : 0u); P[1] = ck.ce; P[3] = ck.te; P[4] = ck.t2c; P[5] = ck.c2t << 15;
#pragma unroll
    for (int i = 0; i < 16; ++i) P[i] = __builtin_amdgcn_readfirstlane(P[i]);
    asm volatile(CB_BODY : "+{s[40:55]}"(P) STEP_PV
                 : "{v[192:207]}"(C), "{v[208:215]}"(C2), [rh] "s"(rh2), [ru] "s"(ra), [ro] "s"(ro), [wv] "s"(w), [sel] "s"(sel)
                 : "memory", "vcc", "scc", CB_CL "s56", "s57", "s58", "s59", "s76", "s77", "s78", "s79", "s80", "s81", "s82", "s83", "s84", "s85", "s86", "s87", "s88", "s89", "s90", "s91", "s92", "s93", "s94", "s95", "s96", "s97", "s98", "s99", "s100", "s101", V53, "v53", "v54", "v55", "v56", "v57", "v58", "v59", "v60", "v61", "v62", "v63", "v64", "v65", "v66", "v67", "v68", "v69", "v70", "v71", "v72", "v73", "v74", "v75", "v76", "v77", "v78", "v79", "v80", "v81", "v82", "v83", "v84", "v85", "v86", "v87", "v88", "v89", "v90", "v91", "v92", "v93", "v94", "v95", "v96", "v97", "v98", "v99", "v100", "v101", "v102", "v103", "v104", "v105", "v106", "v107", "v108", "v109", "v110", "v111", "v112", "v113", "v114", "v115", "v116", "v117", "v118", "v119", "v120", "v121", "v122", "v123", "v124", "v125", "v126", "v127", "v128", "v129", "v130", "v131", "v132", "v133", "v134", "v135", "v136", "v137", "v138", "v139", "v140", "v141", "v142", "v143", "v144", "v145", "v146", "v147", "v148", "v149", "v150", "v151", "v152", "v153", "v154", "v155", "v156", "v157", "v158", "v159", "v160", "v161", "v162", "v163", "v164", "v165", "v166", "v167", "v168", "v169", "v170", "v171", "v172", "v173", "v174", "v175", "v176", "v177", "v178", "v179", "v180", "v181", "v182", "v183", "v184", "v185", "v186", "v187", "v188", "v189", "v190", "v191");
    keep_a(W);
  }
  PDUMP(2);
}

__device__ __forceinline__ void bg_drain() {
  asm volatile("s_waitcnt vmcnt(0)" ::: "memory");
  __builtin_amdgcn_s_barrier();
  asm volatile("" ::: "memory");
}
#define BG_SLP 16
#define A3_SLP 48
__device__ __forceinline__ float blo(unsigned d) { return __uint_as_float(d << 16); }
__device__ __forceinline__ float bhi(unsigned d) { return __uint_as_float(d & 0xffff0000u); }
struct A1 {
  u32x4 kk[11], vv[8], sh[4];
  __device__ __forceinline__ void load(const ChainArgs& a, int x, int n) {
    const int lane = opaque_tid(0);
    const int d = (4 * x + (n & 3)) * 64 + 8 * (lane & 7), tb = (n >> 2) * 64 + 8 * (lane >> 3);
    const __amdgpu_buffer_rsrc_t rk = mk_rsrc(a.Ak, 4u << 20), rv = mk_rsrc(a.Av, 4u << 20), rs = mk_rsrc(a.Ashift, 8192);
#pragma unroll
    for (int i = 0; i < 4; ++i) sh[i] = __builtin_amdgcn_raw_buffer_load_b128(rs, d * 8 + 16 * i, 0, 0);
#pragma unroll
    for (int i = 0; i < 11; ++i) kk[i] = __builtin_amdgcn_raw_buffer_load_b128(rk, tb - 3 + i < 0 ? 0x7fffffffu : ((tb - 3 + i) * D_A + d) * 2, 0, 2);
#pragma unroll
    for (int i = 0; i < 8; ++i) vv[i] = __builtin_amdgcn_raw_buffer_load_b128(rv, ((tb + i) * D_A + d) * 2, 0, 2);
  }
  __device__ __forceinline__ void finish(const ChainArgs& a, int x, int n, char* T, Sel sel) {
    const int lane = opaque_tid(0);
#pragma unroll
    for (int c = 0; c < 8; ++c) {
      const unsigned t01 = sh[c >> 1][2 * (c & 1)], t23 = sh[c >> 1][2 * (c & 1) + 1];
      const float s0 = blo(t01), s1 = bhi(t01), s2 = blo(t23), s3 = bhi(t23);
      float kf[11];
#pragma unroll
      for (int i = 0; i < 11; ++i) kf[i] = (c & 1) ? bhi(kk[i][c >> 1]) : blo(kk[i][c >> 1]);
      u32x4 o;
#pragma unroll
      for (int i = 0; i < 8; i += 2) {
        float r[2];
#pragma unroll
        for (int j = 0; j < 2; ++j)
          r[j] = fmaf(kf[i + j + 3], s0, fmaf(kf[i + j + 2], s1, fmaf(kf[i + j + 1], s2, kf[i + j] * s3))) * ((c & 1) ? bhi(vv[i + j][c >> 1]) : blo(vv[i + j][c >> 1]));
        o[i >> 1] = pk2(r[0], r[1]);
      }
      *(u32x4*)(T + (8 * (lane & 7) + c) * 128 + (((lane >> 3) ^ (lane & 7)) * 16)) = o;
    }
    const __amdgpu_buffer_rsrc_t rkv = rsrc_u(a.sync);
#pragma unroll
    for (int i = 0; i < 8; ++i)
      st16(rkv, kv_row((4 * x + (n & 3)) * 64, sel) + (4 * i + (lane >> 4)) * CHK + ((lane >> 3) & 1) * 4096 + (n >> 2) * 128 + (lane & 7) * 16,
           *(const u32x4*)(T + (8 * i + (lane >> 3)) * 128 + (((lane & 7) ^ i) * 16)));
  }
};
#define WAH 4
#define DSP 4
#define SIGQ 2
__device__ __forceinline__ void task_w(const ChainArgs& a, int kind, int h, char* smem, int wv, Gate& G) {
  const int lane = opaque_tid(0), w = wv, r = lane & 15, g = lane >> 4;
  const __amdgpu_buffer_rsrc_t rw = mk_rsrc(kind == 0 ? a.Wq : (kind == 1 ? a.Wk : a.Wv), 8u << 20), rx = mk_rsrc(a.X, 4u << 20);
  const unsigned lds0 = (unsigned)(uintptr_t)(LAS char*)smem + w * 4096;
  unsigned vx[2], vw[4];
#pragma unroll
  for (int j = 0; j < 2; ++j) {
    const int rr = 8 * j + (lane >> 3);
    const int tb = kind == 0 ? rr : (kind == 1 ? 16 * ((rr >> 1) & 1) + 8 * ((rr >> 2) & 1) + 4 * (rr >> 3) + 2 * (rr & 1) : 8 * (rr >> 2) + (rr & 3));
    vx[j] = tb * DM * 2 + ((lane & 7) ^ (rr >> 1)) * 16;
  }
#pragma unroll
  for (int j = 0; j < 4; ++j) { const int row = (w * 4 + j) * 4 + g; vw[j] = row * HD * 2 + (r ^ swz_b(row)) * 16; }
  const u64 wo = kind == 2 ? bg_order(2) : (kind ? bg_order(1) : bg_order(0));
  auto xo = [&](int q) { return ((int)((wo >> (16 * q + 4 * w)) & 15) ^ (h & RHO)) * 128; };
  unsigned dT = (kind == 0 ? 16 : (kind == 1 ? 1 : 4)) * DM * 2;
  asm volatile("s_nop 4" : "+s"(dT));
  const unsigned vx0 = ((w & 1) ? vx[1] : vx[0]) + (w >> 1) * dT;
  auto st_w = [&](int n) {
#pragma unroll
    for (int j = 0; j < 4; ++j) dma16(rw, vw[j], (64 * ((int)((wo >> (4 * n)) & 15) ^ (h & RHO)) * HD + 128 * h) * 2, lds0 + (n & 7) * 16384 + j * 1024, W_NT & 16);
    dma16(rx, vx0, ((int)((wo >> (4 * n)) & 15) ^ (h & RHO)) * 128, lds0 - 3072 * w + 131072 + (n & 7) * 4096);
  };
  auto st_x = [&](int s, int b, int q, int j, bool pad) { dma16(rx, vx[j & 1], s * 65536 + (j >> 1) * dT + xo(q), lds0 + (b * 4 + q) * 16384 + j * 1024, false, pad); };
  if (KQ_D && kind < 2) while (__builtin_amdgcn_s_memrealtime() - G.entry < KQ_D) __builtin_amdgcn_s_sleep(8);
  __builtin_amdgcn_s_waitcnt(0x0F70);
  u32x4 W[32][2], gv;
  char* xb[3][2];
#pragma unroll
  for (int i = 0; i < 6; ++i) xb[i >> 1][i & 1] = lds_base(smem, (i >> 1) * 65536 + (r >> 3) * 1024 + (r & 7) * 128 + ((4 * (i & 1) + g) ^ (r >> 1)) * 16);
  f32x4 acc[2][4];
#pragma unroll
  for (int i = 0; i < 4; ++i) acc[1][i] = vzero();
#pragma unroll
  for (int n = 0; n < WAH; ++n) st_w(n);
#pragma unroll
  for (int n = 0; n < 16; ++n) {
    asm volatile("s_waitcnt vmcnt(%0)" :: "n"(4 * WAH - 4 + (n < 16 - WAH ? WAH - 1 : 15 - n)) : "memory");
    lds_barrier();
    u32x4 f[4], y[4];
#pragma unroll
    for (int i = 0; i < 4; ++i) {
#pragma unroll
      for (int q = 0; q < 2; ++q) {
        const int row = (i & 1) * 32 + 8 * g + 4 * q + (r >> 2);
        const u32x2 u = __builtin_bit_cast(u32x2, __builtin_amdgcn_ds_read_tr16_b64_v4i16((LAS bf16x4*)(smem + (n & 7) * 16384 + row * 256 + (((4 * w + (r & 3)) ^ swz_b(row)) * 16) + 8 * (i >> 1))));
        f[i][2 * q] = u[0]; f[i][2 * q + 1] = u[1];
      }
      y[i] = *(const u32x4*)(xb[2][i & 1] + (n & 7) * 4096 + (i >> 1) * 2048);
    }
    SB0;
    if (n == 16 - WAH) G.ask(a.sync, gv);
    if (n == 15) asm volatile("" : "+v"(gv));
    if (n + WAH < 16) st_w(n + WAH);
    else {
#pragma unroll
      for (int j = 0; j < 4; ++j) st_x(1 + ((n + WAH - 16) >> 2), (n + WAH - 16) >> 2, (n + WAH) & 3, j, true);
    }
#pragma unroll
    for (int i = 0; i < 8; ++i)
      asm volatile("s_nop 1\n\tv_mfma_f32_16x16x32_bf16 %0, %1, %2, %0" : "+v"(acc[1][i & 3]) : "v"(f[2 * (i & 1) + (i >> 2)]), "v"(y[(i & 2) + (i >> 2)]));
#pragma unroll
    for (int i = 0; i < 4; ++i) { asm volatile("" : "+a"(f[i])); W[2 * n + (i & 1)][i >> 1] = f[i]; }
  }
  if (!G.look(a.sync, gv)) { bg_drain(); return; }
  TP(4);
  SSTAMP(160 + h);
  const Sel sel = G.sel;
  const __amdgpu_buffer_rsrc_t ru = mk_rsrc(a.sync, SY_BYTES);
  const unsigned sgo = (w | lane) ? 0x7fffffffu : SY_CT + (kind == 2 ? CT_V + ct_near(~sel) : CT_Q + 32 * kind + ct_near(sel)) * 128 + h * 128;
#pragma unroll
  for (int T = 0; T < 2; ++T) st16s(pk8v(acc[1][2 * T], acc[1][2 * T + 1]), ru, lane * 16 + T * 4096, x_slot(kind, 0, h, sel) + w * 1024);
  SB0;
  const u32x4 rx2 = own_rsrc(a.X, 4u << 20);
  unsigned vq[4][4];
#pragma unroll
  for (int i = 0; i < 16; ++i) { vq[i >> 2][i & 3] = vx[i & 1] + ((i >> 1) & 1) * dT + xo(i >> 2); asm volatile("" : "+v"(vq[i >> 2][i & 3])); }
  u32x4 xf[4][2], o;
  auto rd = [&](int b, int k, int T) { xf[k & 3][T] = *(const u32x4*)(xb[b][k & 1] + (k >> 1) * 4096 + T * 2048); };
  auto signal = [&](int s) {
    st4(ru, sgo, s);
    if (w == 0 && lane == 0 && s >= 0 && (s < PSEG || (s & 7) == 0 || ((s & 15) == 2 && PSEG > 2) || s > 61)) {
    }
  };
  if (kind < KQ_NOW) { bg_drain(); signal(0); }
  G.arrive(a.sync);
#pragma unroll
  for (int i = 4 * WAH; i < 28; ++i) st_x(1 + (i >> 4), i >> 4, (i >> 2) & 3, i & 3, true);
  wait_vm<26>();
  lds_barrier();
#pragma unroll
  for (int i = 0; i < 6; ++i) rd(0, i >> 1, i & 1);
  WST(kind == 2, 18);
  auto seg = [&](int s, int b) {
    const unsigned so = x_slot(kind, s - 1, h, sel) + w * 1024;
    WST(kind == 2 && s > 0 && s < 4, 18 + s);
#pragma unroll
    for (int k = 0; k < 32; ++k)
#pragma unroll
      for (int i = 0; i < 4; ++i) {
        if (k == 0) asm volatile("v_mfma_f32_16x16x32_bf16 %0, %1, %2, 0" : "=&v"(acc[b][i]) : "a"(W[k][i & 1]), "v"(xf[k & 3][i >> 1]));
        else asm volatile("v_mfma_f32_16x16x32_bf16 %0, %1, %2, %0" : "+v"(acc[b][i]) : "a"(W[k][i & 1]), "v"(xf[k & 3][i >> 1]));
        SB0;
        const int q = k >> 3, n = 4 * k + i;
        if ((k & 7) == 5 && i == 0) {
          if (q == SIGQ) wait_vm<4 * SIGQ>(); else wait_vm<20>();
          if (q == 1 && __builtin_expect(s - 2u < W_NOW, 0)) { wait_vm<4>(); __builtin_amdgcn_s_barrier(); signal(s - 1); }
          else if (q == 0 && b == 0 && __builtin_expect(s == 1, 0)) { wait_vm<32 - 4 * WAH>(); __builtin_amdgcn_s_barrier(); signal(0); }
          else __builtin_amdgcn_s_barrier();
          asm volatile("" ::: "memory");
        }
        if (i == 1 || i == 2) rd(k + 3 < 32 ? b : b ^ 1, (k + 3) & 31, i - 1);
        {
          const int m = (n + 128 - 23) & 127, j = (m & 31) / DSP, q0 = m >> 5;
          if (j < 4 && (m & 31) % DSP == 0) asm volatile("s_add_i32 m0, %0, %1" :: "s"(lds0), "n"(((b ^ (q0 == 0) ^ (n < 23)) * 4 + ((q0 + 3) & 3)) * 16384 + j * 1024) : "m0", "scc");
          if (j < 4 && (m & 31) % DSP == 1)
            asm volatile("buffer_load_dwordx4 %0, %1, %2 offen lds" :: "v"(vq[(q0 + 3) & 3][j & 3]), "s"(rx2), "s"((s + 2 - (q0 == 0) - (n < 23)) * 65536) : "memory");
        }
        if (k == 8 * SIGQ + 5 && i == 2) signal(s - 1);
        if (k >= 1 && k < 5 && (i == 0 || i == 3)) { const f32x4 x = acc[b ^ 1][k - 1]; o[2 * (~k & 1) + (i & 1)] = pk2v(x[2 * (i & 1)], x[2 * (i & 1) + 1]); }
        if ((k == 2 || k == 4) && i == 3) { __builtin_amdgcn_raw_buffer_store_b128(o, ru, (unsigned)(lane * 16 + (k >> 2) * 4096), so, ST_AUX); asm volatile("s_nop 0" :: "v"(o)); }
        SB0;
      }
  };
  A1 a1;
#pragma unroll
  for (int i = 0; i < 11; ++i) asm volatile("" : "=v"(a1.kk[i]), "=v"(a1.vv[i & 7]), "=v"(a1.sh[i & 3]));
  const int wi = 32 * (2 - kind) + 4 * (h & 7) + w;
  for (int s = 1, ph = 0;; ++ph) {
    const int n = ph == 1 ? wi : (ph == 2 && wi >= 64 ? 32 + wi : 256);
    for (const int e = s + (ph ? A1_D : A1_0); s < e || ph == 4; s += 2) {
      seg(s, 0);
      if (s == NSEG - 1) break;
      seg(s + 1, 1);
    }
    if (ph == 4) break;
    if (ph == 3 && lane == 0) ctr_add(a, CT_A1 + ct_near(~sel) + (h >> 3), 1u);
    if (n < 128) a1.finish(a, h >> 3, n, smem + 131072 + w * 8192, sel);
    a1.load(a, h >> 3, ph == 0 ? wi : (ph == 1 && wi >= 64 ? 32 + wi : 256));
    SB0;
  }
#pragma unroll
  for (int T = 0; T < 2; ++T) st16s(pk8v(acc[0][2 * T], acc[0][2 * T + 1]), ru, lane * 16 + T * 4096, x_slot(kind, NSEG - 1, h, sel) + w * 1024);
  bg_drain();
  signal(NSEG - 1);
}
constexpr int A2_CSTR = 4160;
constexpr int A2_UOFF = 4 * A2_CSTR + 16 * 80;
constexpr int A2_USTR = 80 * 80;
constexpr int A2_WLDS = 4 * A2_CSTR + 2 * A2_USTR;
__device__ __forceinline__ void a2_wave(const ChainArgs& a, int d0, int d1, char* smem, int w, Sel sel) {
  const int lane = opaque_tid(0);
  char* Wb = smem + 1024 + w * A2_WLDS;
  char* U = Wb + A2_UOFF;
  const __amdgpu_buffer_rsrc_t rf = mk_rsrc((void*)a.Assm, 4u << 20), rd_ = mk_rsrc((void*)a.AD, 2048);
  const __amdgpu_buffer_rsrc_t rkv = rsrc_u(a.sync);
  u32x4 hi[4], uv[2][4];
  u32x2 lo[4];
  unsigned dn;
  auto ld_h = [&](int d) {
#pragma unroll
    for (int i = 0; i < 4; ++i) {
      hi[i] = __builtin_amdgcn_raw_buffer_load_b128(rf, (unsigned)((255 - 64 * i - lane) * 16), (unsigned)(d * 4096), 2);
      lo[i] = __builtin_amdgcn_raw_buffer_load_b64(rf, i == 3 && lane == 63 ? 0x7fffffffu : (255 - 64 * i - lane) * 16 - 8, (unsigned)(d * 4096), 2);
    }
    dn = __builtin_amdgcn_raw_buffer_load_b16(rd_, 0, 2 * d, 0);
  };
  auto ld_u = [&](int d) {
#pragma unroll
    for (int i = 0; i < 8; ++i) uv[i & 1][i >> 1] = __builtin_amdgcn_raw_buffer_load_b128(rkv, (unsigned)max(((i >> 1) * 64 + lane) * 16 - 8 * (i & 1), 0), kv_row(d, sel), LD_AUX);
  };
  ld_h(d0);
  {
    bool dead = false;
    ctr_wait<BG_SLP>(ctr(a, CT_A1 + ct_near(~sel) + (d0 >> 8)), 96u, (GAS unsigned*)(a.sync + SY_ABORT), lane, dead);
  }
  ld_u(d0);
  if (lane < 12) *(f32x4*)(Wb + (lane / 3) * A2_CSTR + (256 + lane % 3) * 16) = vzero();
#pragma unroll
  for (int i = 0; i < 4; ++i) if (64 * (i & 1) + lane < 80) *(f32x4*)(U + (i >> 1) * A2_USTR - 16 * 80 + (64 * (i & 1) + lane) * 16) = vzero();
  const int r = lane & 15, g = lane >> 4;
#pragma unroll 1
  for (int d = d0; d < d1; d += 96) {
    u64 at[6]; (void)at;
    AST(0);
#pragma unroll
    for (int i = 0; i < 4; ++i) {
      const unsigned src[6] = {lo[i][0], lo[i][1], hi[i][0], hi[i][1], hi[i][2], hi[i][3]};
      unsigned f[10];
#pragma unroll
      for (int x = 0; x < 10; ++x) {
        if ((x & 1) == 0) f[x] = __builtin_amdgcn_alignbit(src[5 - x / 2], src[5 - x / 2], 16);
        else f[x] = __builtin_amdgcn_perm(src[5 - (x + 1) / 2], src[5 - (x - 1) / 2], 0x07060100u);
      }
#pragma unroll
      for (int m = 0; m < 4; ++m) *(u32x4*)(Wb + m * A2_CSTR + (64 * i + lane) * 16) = (u32x4){f[3 - m], f[5 - m], f[7 - m], f[9 - m]};
    }
    if (lane == 0) uv[1][0] = (u32x4){0u, 0u, uv[1][0][0], uv[1][0][1]};
#pragma unroll
    for (int i = 0; i < 8; ++i) {
      const int ch = (i >> 1) * 64 + lane;
      *(u32x4*)(U + (i & 1) * A2_USTR + (ch >> 2) * 80 + (ch & 3) * 16) = uv[i & 1][i >> 1];
    }
    const float Dd = blo(dn);
    asm volatile("" :: "v"(Dd) : "memory");
    AST(1);
    ld_h(d + 96 < d1 ? d + 96 : D_A); ld_u(d + 96 < d1 ? d + 96 : D_A);
    f32x4 acc[4][2];
#pragma unroll
    for (int i = 0; i < 8; ++i) acc[i >> 1][i & 1] = vzero();
    const char* pa = Wb + (r & 3) * A2_CSTR + 16 * (255 - (r >> 2) + g) - 3 * 1024 - 64;
    const char* pb = U + (r - 1) * 80 + 16 * g;
    bf16x8 fa[2][4], fb[2][8];
    auto rd = [&](int b, int n, int k) {
      if (n == 0 || n > 8) fa[b][n ? n - 8 : 0] = *(const bf16x8*)(pa + 1024 * (3 - (n ? n - 8 : 0)) + 64 * (1 - k));
      else fb[b][n - 1] = *(const bf16x8*)(pb + ((n - 1) & 1) * A2_USTR + 80 * (16 * ((n - 1) >> 1) + 1 - k));
    };
#pragma unroll
    for (int n = 0; n < 12; ++n) rd(0, n, 0);
    AST(2);
#pragma unroll 1
    for (int k = 0; k < 16; k += 2) {
#pragma unroll
      for (int b = 0; b < 2; ++b) {
        int q = 0;
#pragma unroll
        for (int j = 0; j < 4; ++j)
#pragma unroll
          for (int i = j; i < 4; ++i)
#pragma unroll
            for (int mt = 0; mt < 2; ++mt) {
              asm volatile("v_mfma_f32_16x16x32_bf16 %0, %1, %2, %0" : "+v"(acc[i][mt]) : "v"(fa[b][j]), "v"(fb[b][2 * (i - j) + mt]));
              SB0;
              if (q < 12) rd(b ^ 1, q, 1);
              if (q == 12) { pa -= 64; pb -= 80; }
              ++q;
              SB0;
            }
      }
    }
    AST(3);
    u32x4 kv[4];
#pragma unroll
    for (int i = 0; i < 4; ++i) kv[i] = *(const u32x4*)(U + (16 * i + r) * 80 + 16 * g);
    asm volatile("s_nop 7\n\ts_waitcnt vmcnt(0)" ::: "memory");
#pragma unroll
    for (int i = 0; i < 8; ++i) asm volatile("" : "+v"(acc[i >> 1][i & 1]));
    if (lane == 0 && d > d0) ctr_add(a, CT_A2 + ct_near(~sel) + ((d - 96) >> 6), 1u);
#pragma unroll
    for (int i = 0; i < 4; ++i) {
      const f32x4 x = acc[i][1], y = acc[i][0];
      st16s((u32x4){pk2(fmaf(blo(kv[i][0]), Dd, x[0]), fmaf(bhi(kv[i][0]), Dd, x[1])), pk2(fmaf(blo(kv[i][1]), Dd, x[2]), fmaf(bhi(kv[i][1]), Dd, x[3])),
                    pk2(fmaf(blo(kv[i][2]), Dd, y[0]), fmaf(bhi(kv[i][2]), Dd, y[1])), pk2(fmaf(blo(kv[i][3]), Dd, y[2]), fmaf(bhi(kv[i][3]), Dd, y[3]))},
            rkv, (unsigned)(i * 1024 + r * 64 + g * 16), kv_row(d, sel));
    }
    AST(4);
  }
  wait_vm<0>();
  if (lane == 0) ctr_add(a, CT_A2 + ct_near(~sel) + ((d0 + (d1 - 1 - d0) / 96 * 96) >> 6), 1u);
}
__device__ __forceinline__ void task_u(const ChainArgs& a, int h, char* smem, int wv, Gate& G) {
  const int lane = opaque_tid(0);
  const int w = wv;
  GAS unsigned* abortw = (GAS unsigned*)(a.sync + SY_ABORT);
  bool dead = false;
  char* sb = smem + w * 32768;
  const __amdgpu_buffer_rsrc_t rwo = mk_rsrc(a.Wo, 8u << 20);
  const unsigned lds0 = (unsigned)(uintptr_t)(LAS char*)sb;
  unsigned wo4[4];
#pragma unroll
  for (int i = 0; i < 4; ++i) { const int row = i * 4 + (lane >> 4); wo4[i] = row * DM * 2 + (((lane & 15) ^ swz_b(row)) * 16); }
  asm volatile("s_nop 4");
  auto stage = [&](int n) {
    const int p = (U_ORD >> (4 * n)) & 3;
#pragma unroll
    for (int i = 0; i < 16; ++i)
      dma16(rwo, wo4[i & 3], (unsigned)(((128 * h + 64 * (p & 1) + 16 * (i >> 2)) * DM + 256 * w + 128 * (p >> 1)) * 2), lds0 + (n & 1) * 16384 + i * 1024, W_NT & 2);
  };
  __builtin_amdgcn_s_waitcnt(0x0F70);
  u32x4 W[16][4];
  stage(0); stage(1);
#pragma unroll
  for (int n = 0; n < 4; ++n) {
    const int p = (U_ORD >> (4 * n)) & 3;
    if (n < 3) wait_vm<16>(); else wait_vm<0>();
#pragma unroll
    for (int t = 0; t < 8; ++t) {
      const u32x8 f = tr_frag(sb + (n & 1) * 16384, t, lane);
      u32x4 f0 = {f[0], f[1], f[2], f[3]}, f1 = {f[4], f[5], f[6], f[7]};
      asm volatile("" : "+a"(f0), "+a"(f1));
      W[8 * (p >> 1) + t][2 * (p & 1)] = f0; W[8 * (p >> 1) + t][2 * (p & 1) + 1] = f1;
    }
    asm volatile("s_waitcnt lgkmcnt(0)" ::: "memory");
    if (n + 2 < 4) stage(n + 2);
  }
  lds_barrier();
  if (!G.pass(a.sync)) return;
  SSTAMP(192 + h);
  const Sel sel = G.sel;
  const u32x4 ru = own_rsrc(a.sync, SY_BYTES);
  u32x4 V[2][8], pc[2], o[2];
  f32x4 c[2][2];
  unsigned cv, cw, vo[8], lk[2], sgo = (w | lane) ? 0x7fffffffu : SY_CT + (CT_U + ct_near(sel) + h) * 128, nd = (lane & 1) ? 3 - URING : 2;
#pragma unroll
  for (int i = 0; i < 8; ++i) { vo[i] = lane * 16 + ux_tile(16 * w + 2 * i); asm volatile("" : "+v"(vo[i])); }
#pragma unroll
  for (int i = 0; i < 2; ++i) lk[i] = SY_CT + (ct_near(~sel) + ((lane & 1) ? CT_DONE + 8 * i + (h & 7) : CT_V + h)) * 128;
  char* vl = lds_base(smem, lane * 16);
  const unsigned vl2 = lane * 16 + w * 2048;
  auto ld_v = [&](int s, bool pad = false) {
    if (pad) asm volatile("s_nop 4\n\tbuffer_load_dwordx4 %0, %2, %3, %4 offen sc1\n\tbuffer_load_dwordx4 %1, %2, %3, %4 offen offset:1024 sc1" : "=&v"(pc[0]), "=&v"(pc[1]) : "v"(vl2), "s"(ru), "s"(x_slot(2, s, h, sel)) : "memory");
    else asm volatile("buffer_load_dwordx4 %0, %2, %3, %4 offen sc1\n\tbuffer_load_dwordx4 %1, %2, %3, %4 offen offset:1024 sc1" : "=&v"(pc[0]), "=&v"(pc[1]) : "v"(vl2), "s"(ru), "s"(x_slot(2, s, h, sel)) : "memory");
  };
  auto signal = [&](int s) {
    asm volatile("buffer_store_dword %0, %1, %2, 0 offen sc1" :: "v"(s), "v"(sgo), "s"(ru) : "memory");
  };
  auto st_u = [&](int n, unsigned ub) { asm volatile("buffer_store_dwordx4 %0, %1, %2, %3 offen offset:%4 sc1" :: "v"(o[n & 1]), "v"(vo[n >> 1]), "s"(ru), "s"(ub), "n"((n & 1) * 1024) : "memory"); };
  auto fetch = [&](int s) {
    for (unsigned spins = 0;;) {
      asm volatile("buffer_load_dword %0, %1, %2, 0 offen sc1" : "=&v"(cw) : "v"(lk[0]), "s"(ru) : "memory");
      ld_v(s, true);
      asm volatile("s_waitcnt vmcnt(0)" : "+v"(pc[0]), "+v"(pc[1]), "+v"(cw) :: "memory");
      if (s && spins == 0) { __builtin_amdgcn_s_barrier(); signal(0); }
      if ((int)__builtin_amdgcn_readfirstlane(cw) >= s) break;
      __builtin_amdgcn_s_sleep(2);
      if (poll_fail(spins, abortw, lane)) { dead = true; break; }
    }
    WST(s == 0, 24);
    if (s == 0) TP(3);
#pragma unroll
    for (int i = 0; i < 2; ++i) *(u32x4*)(vl + 16384 + w * 2048 + i * 1024) = pc[i];
    lds_barrier();
#pragma unroll
    for (int i = 0; i < 8; ++i) V[s][i] = *(const u32x4*)(vl + 16384 + i * 1024);
  };
#pragma unroll
  for (int i = 0; i < 8; ++i) asm volatile("" : "=v"(V[0][i]));
  u64 bad = 0;
  unsigned up = 0x7f000000u;
  c[1][0] = c[1][1] = vzero();
  auto seg = [&](int s, int b) {
    const unsigned ub = s < 0 ? 0x7f000000u : ux_base(s, h, sel);
    WST(s >= 0 && s < 4, 25 + s);
#pragma unroll
    for (int n = 0; n < 16; ++n)
#pragma unroll
      for (int q = 0; q < 8; ++q) {
        const int kp = q >> 1, m = q & 1;
        if (kp == 0) asm volatile("v_mfma_f32_16x16x32_bf16 %0, %1, %2, 0" : "=&v"(c[n & 1][m]) : "v"(V[b][4 * m + kp]), "a"(W[n][kp]));
        else asm volatile("v_mfma_f32_16x16x32_bf16 %0, %1, %2, %0" : "+v"(c[n & 1][m]) : "v"(V[b][4 * m + kp]), "a"(W[n][kp]));
        SB0;
        if (q >= 2 && q < 6) { const f32x4 x = c[~n & 1][(q - 2) >> 1]; o[~n & 1][q - 2] = pk2v(x[2 * (q & 1)], x[2 * (q & 1) + 1]); }
        if (q == 6) st_u((n - 1) & 15, n ? ub : up);
        if (q == 5) asm volatile("" :: "v"(o[n & 1]));
        if (n == U_LD && q == 0) {
          if (__builtin_expect(bad != 0, 0)) {
            if (s <= U_NOW) {
              asm volatile("s_waitcnt vmcnt(%1)" : "+v"(cw) : "n"(U_LD - 2) : "memory");
              __builtin_amdgcn_s_barrier();
              signal(s - 1);
              bad = (int)__builtin_amdgcn_readfirstlane(cw) <= s;
            }
            if (bad & 1) ctr_wait<2>(ctr(a, CT_V + ct_near(~sel) + h), min(s + 2, NSEG), abortw, lane, dead);
            if (bad & 2) {
              ctr_wait<U_SLP>(ctr(a, CT_DONE + ct_near(~sel) + 8 * b + (h & 7)), max(s + 3 - URING, 0), abortw, lane, dead);
              for (int i = 0; i < U_STG * (h & 7) + U_ST0; ++i) __builtin_amdgcn_s_sleep(1);
            }
          }
          ld_v(min(s + 1u, NSEG - 1u));
        }
        if (n == 1 && q == 7) asm volatile("buffer_load_dword %0, %1, %2, 0 offen sc1" : "=&v"(cw) : "v"(s <= U_NOW ? lk[0] : 0x7fffffffu), "s"(ru) : "memory");
        if (n == U_LD && q == 7) asm volatile("buffer_load_dword %0, %1, %2, 0 offen sc1" : "=&v"(cv) : "v"(lk[b ^ 1]), "s"(ru) : "memory");
        if (n == U_SIG2 && q == 0) asm volatile("s_waitcnt vmcnt(%4)" : "+v"(pc[0]), "+v"(pc[1]), "+v"(cv), "+v"(cw) : "n"(U_SIG2 - U_LD - 1) : "memory");
        if (n == U_SIG2 && (q == 1 || q == 7)) *(u32x4*)(vl + (b ^ 1) * 8192 + w * 2048 + (q >> 2) * 1024) = pc[q >> 2];
        if (n == U_SIG2 + 1 && q == 0) { bad = __builtin_amdgcn_ballot_w64(s >= 0 && (int)cv < (s < U_NOW && lane > 1 ? NSEG : min(s + (int)nd, NSEG - 1))); asm volatile("" : "+s"(bad)); }
        if (n == U_SIG2 + 1 && q == 7) lds_barrier();
        if (n == U_SIG2 + 2 && q == 0) signal(s - 1);
        if ((n == U_SIG2 + 2 || n == U_SIG2 + 3) && (q & 1)) V[b ^ 1][4 * (n - U_SIG2 - 2) + (q >> 1)] = *(const u32x4*)(vl + (b ^ 1) * 8192 + (4 * (n - U_SIG2 - 2) + (q >> 1)) * 1024);
        SB0;
      }
    up = ub;
  };
  for (int s = -U_DRY; s < NSEG; s += 2) {
    if (__builtin_expect(s == 0, 0)) fetch(0);
    seg(s, 0);
    if (__builtin_expect(s == 0, 0)) {
      o[1] = pk8v(c[1][0], c[1][1]);
      st_u(15, up);
      fetch(1);
    }
    seg(s + 1, 1);
  }
  o[1] = pk8v(c[1][0], c[1][1]);
  st_u(15, up);
  bg_drain();
  signal(NSEG - 1);
}

__device__ __forceinline__ void task_a3(const ChainArgs& a, int dg, int t0, char* T, Sel sel) {
  const int lane = opaque_tid(0);
  const int d = dg * 64 + 8 * (lane & 7);
  const int tb = t0 + 8 * (lane >> 3);
  const __amdgpu_buffer_rsrc_t rz = rsrc_u(a.sync);
  u32x4 qq[16], zz[8][2];
#pragma unroll
  for (int i = 0; i < 16; ++i) qq[i] = LDNT((const u32x4*)(a.Aq + (long)(tb + 64 * (i >> 3) + (i & 7)) * D_A + d));
  {
    u32x4 zr[16];
    bool dead = false;
    ctr_wait<A3_SLP>(ctr(a, CT_A2 + ct_near(~sel) + dg), 64u, (GAS unsigned*)(a.sync + SY_ABORT), lane, dead);
  #pragma unroll
    for (int i = 0; i < 16; ++i) zr[i] = ld16(rz, kv_row(dg * 64, sel) + (2 * i + (lane >> 5)) * CHK + ((lane >> 4) & 1) * 4096 + (t0 + (lane & 15) * 8) * 2);
#pragma unroll
    for (int i = 0; i < 16; ++i) {
      const int row = 4 * i + (lane >> 4);
      *(u32x4*)(T + row * 256 + (((lane & 15) ^ (row >> 3)) * 16)) = zr[i];
    }
  }
#pragma unroll
  for (int c = 0; c < 8; ++c)
#pragma unroll
    for (int gq = 0; gq < 2; ++gq) zz[c][gq] = *(const u32x4*)(T + (8 * (lane & 7) + c) * 256 + (((gq * 8 + (lane >> 3)) ^ (lane & 7)) * 16));
#pragma unroll
  for (int i = 0; i < 16; ++i) {
    u32x4 o;
#pragma unroll
    for (int c = 0; c < 8; c += 2) {
      const unsigned z0 = zz[c][i >> 3][(i >> 1) & 3], z1 = zz[c + 1][i >> 3][(i >> 1) & 3];
      o[c >> 1] = pk2(blo(qq[i][c >> 1]) * ((i & 1) ? bhi(z0) : blo(z0)), bhi(qq[i][c >> 1]) * ((i & 1) ? bhi(z1) : blo(z1)));
    }
    st16g(a.out + (long)(tb + 64 * (i >> 3) + (i & 7)) * OUTW + d, o);
  }
}

__device__ __forceinline__ void part_a(const ChainArgs& a, char* smem, int x, int wi, int w, Sel sel) {
  char* T = smem + 1024 + w * A2_WLDS;
  a2_wave(a, 256 * x + wi, 256 * x + 256, smem, w, sel);
  while (true) {
    unsigned t = 0;
    if (opaque_tid(0) == 0) t = __hip_atomic_fetch_add(ctr(a, CT_TK + ct_near(~sel) + x), 1u, __ATOMIC_RELAXED, __HIP_MEMORY_SCOPE_AGENT) + 1u;
    t = __builtin_amdgcn_readfirstlane(t);
    if (t >= 64) break;
    task_a3(a, 4 * x + (t >> 4), (t & 15) * 128, T, sel);
  }
}

__global__ __launch_bounds__(256, 1) void chain_kernel(ChainArgs a) {
  extern __shared__ __attribute__((aligned(16))) char smem[];
  const int b = blockIdx.x;
  {
    u32x4 rc;
    asm volatile("s_getpc_b64 s[60:61]\n\t.Lchain_here:\n\ts_sub_u32 s60, s60, .Lchain_here-_Z12chain_kernel9ChainArgs\n\ts_subb_u32 s61, s61, 0\n\ts_and_b32 s61, s61, 0xffff\n\t"
                 "s_mov_b32 s62, .Lchain_end-_Z12chain_kernel9ChainArgs\n\ts_mov_b32 s63, 0x20000\n\t"
                 "buffer_load_dwordx4 a[252:255], %1, s[60:63], 0 offen\n\tbuffer_load_dwordx4 a[252:255], %1, s[60:63], s63 offen"
                 : "={s[60:63]}"(rc) : "v"((b >> 3) * 4096 + threadIdx.x * 16) : "scc", "a252", "a253", "a254", "a255");
  }
  int w = __builtin_amdgcn_readfirstlane(threadIdx.x >> 6);
  Gate G;
  G.entry = __builtin_amdgcn_s_memrealtime(); G.open = false; G.arr = w == 0 ? b & 7 : -1;
  G.rot = ((unsigned)__builtin_amdgcn_s_getreg(20 | (3 << 11)) - b + WRONG) & 7u;
  {
    volatile LAS unsigned* t = (volatile LAS unsigned*)smem;
    if (w == 0) { t[0] = (unsigned)G.entry; t[1] = (unsigned)(G.entry >> 32); }
    __syncthreads();
    G.entry = (u64)(unsigned)__builtin_amdgcn_readfirstlane(t[0]) | ((u64)(unsigned)__builtin_amdgcn_readfirstlane(t[1]) << 32);
    __syncthreads();
  }
  G.tx = G.entry;
  if (((b + G.rot) & 4) == 0 && b < 128) {
    const int c = (b >> 6) * 32 + 8 * ((b + G.rot) & 3) + ((b >> 3) & 7);
    if (w < 2) calibrate(a, w ? 32 + (0x3200 >> (4 * (c & 3)) & 3) : c, 2 * c + w, G.entry);
    __builtin_amdgcn_s_barrier();
  }
  if (b == 0) {
    const int lane = opaque_tid(0);
    const __amdgpu_buffer_rsrc_t rs = mk_rsrc(a.sync + SY_SET, 3 * CHK);
    st16(rs, CHK + w * 8192 + lane * 128, (u32x4){SENT, SENT, SENT, SENT});
    if (w == 0 && lane >= 2) st16(rs, lane * 128, (u32x4){SENT, SENT, SENT, SENT});
    if (w == 3) {
      u32x4 v;
      bool ok;
      do {
        v = ld16(mk_rsrc(a.sync + SY_CT + CT_CAL * 128, 8192), lane * 128);
        ok = ((u64)v[1] << 32 | v[0]) - G.entry + 2000u < (1ull << 31) && ((u64)v[3] << 32 | v[2]) - G.entry + 2000u < (1ull << 31);
      } while (!__all(ok) && __builtin_amdgcn_s_memrealtime() - G.entry < 2 * CALM);
      const u64 k = lane == 32 ? 0x3333333333333333ull : (lane == 34 || lane == 35 ? 0x1111111111111111ull << (lane & 3) : 0ull);
      const int n1 = __builtin_popcountll(k & __builtin_amdgcn_ballot_w64(ok && (v[2] & 3u) == 3u)), n0 = __builtin_popcountll(k & __builtin_amdgcn_ballot_w64(ok && (v[2] & 3u) == 2u));
      if (!ok) v[0] = 0;
      if (n0 != n1) v[0] = n1 > n0 ? 3u : 2u;
      const unsigned t = (v[0] ^ lane) & 1u;
      const bool most = __builtin_popcountll(__builtin_amdgcn_ballot_w64((v[0] & 2u) && t)) > __builtin_popcountll(__builtin_amdgcn_ballot_w64((v[0] & 2u) && !t));
      const u64 m = __builtin_amdgcn_ballot_w64((v[0] & 2u) ? v[0] & 1u : (lane ^ most) & 1u);
      if (lane == 0) __hip_atomic_store((GAS u64*)(a.sync + SY_SEL), m, __ATOMIC_RELAXED, __HIP_MEMORY_SCOPE_AGENT);
    }
    bg_drain();
    if (w == 0 && lane == 0) __hip_atomic_store((GAS u64*)(a.sync + SY_EPOCH), (__builtin_amdgcn_s_memrealtime() & ~7ull) | G.rot, __ATOMIC_RELAXED, __HIP_MEMORY_SCOPE_AGENT);
  }
  while (true) {
    const unsigned rot = G.rot;
    asm volatile("" : "+s"(w));
    const int x = (b + rot) & 7, j = b >> 3, h = 8 * (x & 3) + (j & 7), role = (x & 4) + (j >> 3);
    if ((role == 0 || role == 7) && w >= 2) {
      const int n = opaque_tid(w - 2);
      const unsigned x0 = __builtin_amdgcn_raw_buffer_load_b32(mk_rsrc(a.sync, 106u << 20), n * PAGE + 4096 + (j & 31) * 128, 0, 2);
      const unsigned x1 = __builtin_amdgcn_raw_buffer_load_b32(mk_rsrc(a.out, 8u << 20), n * PAGE + (j & 31) * 128, 0, 2);
      asm volatile("" :: "v"(x0), "v"(x1));
    }
    if (role < 2) role_cb(a, 32 * role + h, smem, w, G);
    else if (role == 2) role_qk(a, h, smem, w, G);
    else if (role == 3) role_vo(a, h, smem, w, G);
    else if (role == 4) task_u(a, h, smem, w, G);
    else task_w(a, 7 - role, h, smem, w, G);
    if (G.rot == rot && role > 4) part_a(a, smem, x & 3, 4 * (j - 8) + w, w, G.sel);
    if (G.rot == rot) break;
    __syncthreads();
  }
  asm volatile(".Lchain_end:");
}

extern "C" void kernel_launch(void* const* d_in, const int* in_sizes, int n_in,
                              void* d_out, int out_size, void* d_ws, size_t ws_size,
                              hipStream_t stream) {
  const u16* A_q = (const u16*)d_in[0];
  const u16* A_k = (const u16*)d_in[1];
  const u16* A_v = (const u16*)d_in[2];
  const u16* A_shift = (const u16*)d_in[3];
  const u16* A_ssmk = (const u16*)d_in[4];
  const u16* A_D = (const u16*)d_in[5];
  const u16* B_x = (const u16*)d_in[6];
  const u16* B_mem0 = (const u16*)d_in[7];
  const u16* B_Wq = (const u16*)d_in[8];
  const u16* B_Wk = (const u16*)d_in[9];
  const u16* B_Wv = (const u16*)d_in[10];
  const u16* B_Wo = (const u16*)d_in[11];
  u16* out = (u16*)d_out;
  char* ws = (char*)(((uintptr_t)d_ws + (2u << 20) - 1) & ~(uintptr_t)((2u << 20) - 1)) + WSOFF * MB;
  hipFuncSetAttribute((const void*)chain_kernel, hipFuncAttributeMaxDynamicSharedMemorySize, CHAIN_LDS);
  ChainArgs ca;
  ca.prof = (u64*)((char*)d_ws + PROF_AT);
  ca.mem0 = B_mem0; ca.Wq = B_Wq; ca.Wk = B_Wk; ca.Wv = B_Wv; ca.Wo = B_Wo; ca.X = B_x;
  ca.out = out; ca.sync = ws;
  ca.Aq = A_q; ca.Ak = A_k; ca.Av = A_v; ca.Ashift = A_shift; ca.Assm = A_ssmk; ca.AD = A_D;
  chain_kernel<<<256, 256, CHAIN_LDS, stream>>>(ca);
}
```

```cpp
#include <hip/hip_runtime.h>
#include <hip/hip_bf16.h>
#include <stdint.h>
#include <type_traits>

constexpr int S_LEN = 2048;
constexpr int D_A   = 1024;
constexpr int DM    = 1024;
constexpr int NH    = 32;
constexpr int DH    = 128;
constexpr int HD    = NH * DH;
constexpr int MEMR  = 8;
constexpr int SEG   = 32;
constexpr int NSEG  = S_LEN / SEG;
constexpr int OUTW  = 2048;

typedef unsigned short u16;
typedef unsigned long long u64;
typedef short bf16x8 __attribute__((ext_vector_type(8)));
typedef short bf16x4 __attribute__((ext_vector_type(4)));
typedef float f32x4 __attribute__((ext_vector_type(4)));
typedef float f32x16 __attribute__((ext_vector_type(16)));
typedef unsigned u32x4 __attribute__((ext_vector_type(4)));
typedef unsigned u32x2 __attribute__((ext_vector_type(2)));
#define LAS __attribute__((address_space(3)))
#define GAS __attribute__((address_space(1)))

constexpr size_t MB = 1u << 20;
constexpr size_t PROF_AT = 0;

__device__ __forceinline__ float bf2f(u16 b) { return __uint_as_float(((unsigned)b) << 16); }
__device__ __forceinline__ u16 f2bf(float f) { __bf16 b = (__bf16)f; return __builtin_bit_cast(u16, b); }
__device__ __forceinline__ float rbf(float f) { return bf2f(f2bf(f)); }
__device__ __forceinline__ unsigned pk2(float a, float b) {
  typedef float f2 __attribute__((ext_vector_type(2)));
  typedef __bf16 b2 __attribute__((ext_vector_type(2)));
  f2 v = {a, b}; b2 r = __builtin_convertvector(v, b2); return __builtin_bit_cast(unsigned, r);
}

__device__ __forceinline__ int opaque_tid(int wv) {
  int l;
  asm volatile("v_mbcnt_lo_u32_b32 %0, -1, 0\n\tv_mbcnt_hi_u32_b32 %0, -1, %0" : "=v"(l));
  return wv * 64 + l;
}
__device__ __forceinline__ int swz_b(int row) { return ((row & 3) << 2) | ((row >> 2) & 3); }

#define KNOB(name, v) constexpr int name = v
KNOB(K_DEF, 70);
KNOB(W_STEP, 33); KNOB(W_OFF, 56);
#define RHOV 3
KNOB(RHO, RHOV);
#define CDUR 1490
#define U_SLP 8
#define U_STG 0
#define U_ST0 0
#define WSOFF 122
#define START_SLP 3
#define QK_D 0
#define U_DRY 0
#define A1_0 20
#define A1_D 4
#define PSEG 1
#define KQ_NOW 2
#define NOA 0
#define TRW 1
#define CALN 3
#define CALM 500u
#define CALT 680u
#define KQ_D 350
#define W_NOW 4
#define U_SIG2 11
#define U_LD 5
#define U_NOW 6
#define WRONG 0
#define OUTP "sc1"
#define LDNT(p) __builtin_nontemporal_load(p)
constexpr int CHK      = 16384;
constexpr int PAGE     = 2 << 20;
constexpr int SY_UM    = 34 * PAGE;
constexpr int SY_HOT   = 32 * PAGE;
constexpr int H_MEM    = 0;
constexpr int H_P      = 16 * CHK;
constexpr int H_ANN    = 28 * CHK;
constexpr int H_DLT    = 32 * CHK;
constexpr int H_TR     = 33 * CHK;
constexpr int H_PX     = 64 * CHK;
constexpr int H_BYTES  = 128 * CHK;
constexpr int URING    = 16;
constexpr int SY_SET   = 52 * PAGE;
constexpr int SY_EPOCH = SY_SET + 128;
constexpr int SY_ABORT = SY_SET + 256;
constexpr int SY_SEL   = SY_EPOCH + 8;
constexpr int SY_CT    = SY_SET + CHK;
constexpr int CT_Q = 0, CT_K = 32, CT_U = 128;
constexpr int CT_V = 0, CT_DONE = 32, CT_A2 = 48, CT_A1 = 128, CT_TK = 132;
constexpr int CT_ARR = -124, CT_INIT = -116, CT_CAL = 256;
constexpr int SY_KV    = 48 * PAGE;
constexpr int SY_BYTES = SY_CT + (CT_CAL + 64) * 128;
constexpr unsigned SENT = 0xFFFFFFFFu;
constexpr unsigned SPIN_CAP = 1u << 19;
constexpr int NLF = 16;
constexpr int L_WTS = 0;
constexpr int L_MEM = L_WTS + 4 * NLF * 2048;
constexpr int L_XCH = L_MEM + 16384;
constexpr int L_XK  = L_XCH + 2048;
constexpr int L_KX  = L_XCH + 4096;
constexpr int L_PRF = L_KX + 8192;
constexpr int XRS = 40;
constexpr int CHAIN_LDS = 160 * 1024;
static_assert(L_XCH + 128 * XRS <= L_PRF && L_PRF + 4096 <= CHAIN_LDS, "lds");

typedef __bf16 bfv8 __attribute__((ext_vector_type(8)));
typedef __bf16 bfv16 __attribute__((ext_vector_type(16)));
typedef unsigned u32x8 __attribute__((ext_vector_type(8)));
typedef unsigned u32x12 __attribute__((ext_vector_type(12)));
typedef unsigned u32x16 __attribute__((ext_vector_type(16)));
typedef unsigned u32x32 __attribute__((ext_vector_type(32)));
struct Wts { u32x8 a[32]; u32x32 v[4]; };

#define STAMPL(i) do {} while (0)
#define PVAL(i, v) do {} while (0)
#define PDECL(base, str) do {} while (0)
#define PZERO do {} while (0)
#define PDUMP(mask) do {} while (0)
#define TA(x) do {} while (0)
#define TB(x) TS(x)
#define TS(x) do {} while (0)
#define SSTAMP(i) do {} while (0)
#define WST(c, i) do {} while (0)
#define AST(i) do {} while (0)
struct ChainArgs {
  u64* prof;
  const u16* mem0; const u16* Wq; const u16* Wk; const u16* Wv; const u16* Wo; const u16* X;
  u16* out; char* sync;
  const u16* Aq; const u16* Ak; const u16* Av; const u16* Ashift; const u16* Assm; const u16* AD;
};
__device__ __forceinline__ GAS unsigned* ctr(const ChainArgs& a, int i) { return (GAS unsigned*)(a.sync + SY_CT + i * 128); }
__device__ __forceinline__ void ctr_add(const ChainArgs& a, int i, unsigned v) {
  __hip_atomic_fetch_add(ctr(a, i), v, __ATOMIC_RELAXED, __HIP_MEMORY_SCOPE_AGENT);
}

__device__ __forceinline__ __amdgpu_buffer_rsrc_t mk_rsrc(const void* p, unsigned bytes) {
  const u64 v = (u64)p;
  const unsigned lo = (unsigned)__builtin_amdgcn_readfirstlane((unsigned)v), hi = (unsigned)__builtin_amdgcn_readfirstlane((unsigned)(v >> 32));
  const u64 u = (u64)lo | ((u64)hi << 32);
  return __builtin_amdgcn_make_buffer_rsrc((void*)u, 0, bytes, 0x00020000);
}
__device__ __forceinline__ u32x4 own_rsrc(const void* p, unsigned bytes) {
  u32x4 r = {(unsigned)__builtin_amdgcn_readfirstlane((unsigned)(u64)p), (unsigned)__builtin_amdgcn_readfirstlane((unsigned)((u64)p >> 32)) & 0xffffu, bytes, 0x00020000u};
  asm volatile("s_nop 4" : "+s"(r));
  return r;
}
constexpr int LD_AUX = 16, ST_AUX = 16;
__device__ __forceinline__ u32x4 ld16(__amdgpu_buffer_rsrc_t rs, unsigned off) {
  return __builtin_amdgcn_raw_buffer_load_b128(rs, off, 0, LD_AUX);
}
__device__ __forceinline__ void st16(__amdgpu_buffer_rsrc_t rs, unsigned off, u32x4 v) {
  __builtin_amdgcn_raw_buffer_store_b128(v, rs, off, 0, ST_AUX);
}
__device__ __forceinline__ void st16s(u32x4 v, __amdgpu_buffer_rsrc_t rs, unsigned voff, unsigned soff) {
  __builtin_amdgcn_raw_buffer_store_b128(v, rs, voff, soff, ST_AUX);
  asm volatile("s_nop 1" :: "v"(v) : "memory");
}
__device__ __forceinline__ void st8(__amdgpu_buffer_rsrc_t rs, unsigned off, u32x2 v) {
  __builtin_amdgcn_raw_buffer_store_b64(v, rs, off, 0, ST_AUX);
}
__device__ __forceinline__ void st4(__amdgpu_buffer_rsrc_t rs, unsigned off, unsigned v) {
  __builtin_amdgcn_raw_buffer_store_b32(v, rs, off, 0, ST_AUX);
}
__device__ __forceinline__ unsigned ld4(__amdgpu_buffer_rsrc_t rs, unsigned off) {
  return __builtin_amdgcn_raw_buffer_load_b32(rs, off, 0, LD_AUX);
}
constexpr int TU = 16;
__device__ __forceinline__ unsigned cyc32() { return (unsigned)__builtin_amdgcn_s_memtime(); }
__device__ __forceinline__ unsigned to_sgpr(unsigned x) {
  unsigned r;
  asm volatile("s_nop 0\n\tv_readfirstlane_b32 %0, %1\n\ts_nop 4" : "=s"(r) : "v"(x));
  return r;
}
struct Clk {
  unsigned ce, te;
  unsigned c2t, t2c;
  __device__ __forceinline__ void edge(unsigned& c, unsigned& t) {
    u64 c0, r0, r1, r2, r3, r4, r5;
    asm volatile("s_memtime %0\n\ts_memrealtime %1\n\ts_nop 4\n\ts_memrealtime %2\n\ts_nop 4\n\ts_memrealtime %3\n\ts_nop 4\n\t"
                 "s_memrealtime %4\n\ts_nop 4\n\ts_memrealtime %5\n\ts_nop 4\n\ts_memrealtime %6\n\ts_waitcnt lgkmcnt(0)"
                 : "=&s"(c0), "=&s"(r0), "=&s"(r1), "=&s"(r2), "=&s"(r3), "=&s"(r4), "=&s"(r5) :: "memory");
    const unsigned x = (unsigned)r0;
    const unsigned n = ((unsigned)r1 == x) + ((unsigned)r2 == x) + ((unsigned)r3 == x) + ((unsigned)r4 == x) + ((unsigned)r5 == x);
    c = to_sgpr((unsigned)c0 + 24u * n); t = (x + 4u) * TU;
  }
  __device__ __forceinline__ void init() { edge(ce, te); c2t = 44000u; t2c = 97600u; }
  __device__ __forceinline__ void sync() {
    unsigned c, t;
    edge(c, t);
    set(c, t);
  }
  __device__ __forceinline__ void set(unsigned c, unsigned t) {
    if (c == ce) return;
    const float q = (float)(t - te) * __builtin_amdgcn_rcpf((float)(c - ce));
    const float qc = fminf(fmaxf(q, 0.51f), 1.99f);
    c2t = to_sgpr((unsigned)(qc * 65536.f));
    t2c = to_sgpr((unsigned)(__builtin_amdgcn_rcpf(qc) * 65536.f));
    ce = c; te = t;
  }
  __device__ __forceinline__ unsigned time(unsigned c) const { return te + ((min(c - ce, 30000u) * c2t) >> 16); }
  __device__ __forceinline__ unsigned now() const { return time(cyc32()); }
  __device__ __forceinline__ unsigned target(unsigned t) const {
    return to_sgpr(ce + (((unsigned)min(max((int)(t - te), 0), 20000) * t2c) >> 16));
  }
  __device__ __forceinline__ void wait(unsigned t) const { wait_c(target(t)); }
  __device__ __forceinline__ void wait_c(unsigned c) const {
    unsigned y;
    asm volatile("1:\n\ts_memtime vcc\n\ts_waitcnt lgkmcnt(0)\n\ts_sub_i32 %0, %1, vcc_lo\n\ts_cmp_gt_i32 %0, 128\n\ts_cbranch_scc1 1b\n\t"
                 "2:\n\ts_sub_i32 %0, %0, %2\n\ts_cmp_gt_i32 %0, %3\n\ts_cbranch_scc1 2b"
                 : "=&s"(y) : "s"(c), "n"(W_STEP), "n"(W_OFF) : "memory", "scc", "vcc");
  }
};
__device__ __forceinline__ unsigned nosent(unsigned d) { return min(d, SENT - 1u); }
__device__ __forceinline__ int arow(int row, int half) { return 4 * (row >> 1) + 2 * half + (row & 1); }
__device__ __forceinline__ unsigned umax4(u32x4 v) { return max(max(v[0], v[1]), max(v[2], v[3])); }
__device__ __forceinline__ void stamp_again(GAS u64* p) {
  __hip_atomic_store(p, (__builtin_amdgcn_s_memrealtime() & ~7ull) | (__hip_atomic_load(p, __ATOMIC_RELAXED, __HIP_MEMORY_SCOPE_AGENT) & 7ull),
                     __ATOMIC_RELAXED, __HIP_MEMORY_SCOPE_AGENT);
}
__device__ __forceinline__ bool poll_fail(unsigned& spins, GAS unsigned* abortw, int lane) {
  ++spins;
  if (spins > SPIN_CAP) {
    if (lane == 0) __hip_atomic_store(abortw, 1u, __ATOMIC_RELAXED, __HIP_MEMORY_SCOPE_AGENT);
    return true;
  }
  if ((spins & 511u) == 0u && lane == 0) {
    unsigned n = 0;
    for (int i = 0; i < 8; ++i) n += __hip_atomic_load(abortw + (SY_CT - SY_ABORT + (CT_ARR + i) * 128) / 4, __ATOMIC_RELAXED, __HIP_MEMORY_SCOPE_AGENT) + 1u;
    if (n < 256u) stamp_again((GAS u64*)(abortw - 32));
  }
  if ((spins & 63u) == 0u)
    return __builtin_amdgcn_readfirstlane(__hip_atomic_load(abortw, __ATOMIC_RELAXED, __HIP_MEMORY_SCOPE_AGENT)) != SENT;
  return false;
}
#define TP(k) do {} while (0)
struct Gate {
  u64 tx; u64 entry, sel; int arr; unsigned rot; bool open;
  __device__ __forceinline__ bool look(char* sync, u32x4 v) {
    if (open) return true;
    for (unsigned spins = 1;; ++spins) {
#pragma unroll
      for (int i = 0; i < 4; ++i) v[i] = __builtin_amdgcn_readfirstlane(v[i]);
      if (((u64)v[1] << 32 | v[0]) - entry + 2000u < (1ull << 31) || spins > 4 * SPIN_CAP) break;
      if (spins > 1) __builtin_amdgcn_s_sleep(8);
      v = ld16(mk_rsrc(sync + SY_EPOCH, 16), 0);
    }
    asm volatile("" ::: "memory");
    sel = (u64)v[3] << 32 | v[2];
    const bool same = rot == (v[0] & 7u);
    rot = v[0] & 7u; open = true;
    return same;
  }
  __device__ __forceinline__ void ask(char* sync, u32x4& v) { asm volatile("s_nop 4\n\tbuffer_load_dwordx4 %0, off, %1, 0 sc1" : "=v"(v) : "s"(own_rsrc(sync + SY_EPOCH, 16)) : "memory"); }
  __device__ __forceinline__ void arrive(char* sync) {
    if (arr >= 0 && opaque_tid(0) == 0) __hip_atomic_fetch_add((GAS unsigned*)(sync + SY_CT + (CT_ARR + arr) * 128), 1u, __ATOMIC_RELAXED, __HIP_MEMORY_SCOPE_AGENT);
    arr = -1;
  }
  __device__ __forceinline__ bool pass(char* sync, u32x4 v = {0u, 0u, 0u, 0u}) { const bool r = look(sync, v); arrive(sync); return r; }
};
__device__ __forceinline__ unsigned ann_val(unsigned t, int s) { return (t << 8) | (unsigned)s; }
__device__ __forceinline__ unsigned tr_place(int s) { return (s & 2) * (CHK / 2) + (s & 1) * 4096; }
__device__ __forceinline__ unsigned die_half() { return ((unsigned)__builtin_amdgcn_s_getreg(20 | (3 << 11)) >> 2) & 1u; }
typedef u64 Sel;
template <class F>
__device__ __forceinline__ void start_wait(char* sync, GAS unsigned* abortw, int lane, bool& dead, int ci, unsigned n, F f) {
  unsigned spins = 0, v;
  const bool m = lane < 4 || ci < 0;
  while (true) {
    v = ld4(mk_rsrc(sync + SY_SET, SY_BYTES - SY_SET), lane < 6 ? CHK + (m ? CT_INIT + (lane & 3) : ci + 32 * (lane & 1)) * 128 : 0x7fffffffu) + 1u;
    asm volatile("" ::: "memory");
    f();
    if (__all(lane > 5 || v >= (m ? 16u : n))) break;
    __builtin_amdgcn_s_sleep(START_SLP);
    if (dead || poll_fail(spins, abortw, lane)) { dead = true; break; }
  }
}
__device__ __forceinline__ __amdgpu_buffer_rsrc_t rsrc_u(char* sync) { return mk_rsrc((void*)sync, SY_SET); }
__device__ __forceinline__ unsigned near_half(unsigned off, u64 sel) { return off + ((unsigned)(sel >> ((off >> 21) & 63)) & 1u) * 8192; }
__device__ __forceinline__ unsigned x_slot(int kind, int s, int h, Sel sel) {
  return kind == 2 ? near_half(32 * PAGE + (s * 32 + h) * CHK, ~sel) : near_half((s * 64 + 2 * h + 1 - kind) * CHK, sel);
}
__device__ __forceinline__ int ct_near(Sel sel) { return 64 * ((unsigned)(sel >> (SY_SET / PAGE)) & 1u); }
__device__ __forceinline__ unsigned kv_row(int d, Sel sel) { return near_half(SY_KV + (d >> 1) * CHK, ~sel) + (d & 1) * 4096; }
__device__ __forceinline__ int hpos(int h) { return (h & 3) * 8 + (h >> 2); }
constexpr unsigned UXH = 8 * CHK;
__device__ __forceinline__ unsigned ux_base(int s, int h, Sel sel) { return near_half(((s & (URING - 1)) * 32 + h) * UXH, ~sel); }
__device__ __forceinline__ constexpr unsigned ux_tile(int t) { return (t >> 3) * CHK + (t & 7) * 1024; }
__device__ __forceinline__ __amdgpu_buffer_rsrc_t rsrc_hot(char* sync, Sel sel) {
  return mk_rsrc((void*)(sync + near_half(SY_HOT, sel)), H_BYTES - 8192);
}
template <int SLP = 4>
__device__ __forceinline__ void ctr_wait(GAS unsigned* c, unsigned need, GAS unsigned* abortw, int lane, bool& dead) {
  unsigned spins = 0;
  while (true) {
    unsigned v = __builtin_amdgcn_readfirstlane(__hip_atomic_load(c, __ATOMIC_RELAXED, __HIP_MEMORY_SCOPE_AGENT)) + 1u;
    if (v >= need) break;
    __builtin_amdgcn_s_sleep(SLP);
    if (dead || poll_fail(spins, abortw, lane)) { dead = true; break; }
  }
}
template <int N> __device__ __forceinline__ void wait_vm() { asm volatile("s_waitcnt vmcnt(%0)" :: "n"(N) : "memory"); }
__device__ __forceinline__ void lds_barrier() {
  asm volatile("s_waitcnt lgkmcnt(0)" ::: "memory");
  __builtin_amdgcn_s_barrier();
  asm volatile("" ::: "memory");
}
__device__ __forceinline__ f32x4 vzero() { float z; asm volatile("v_mov_b32 %0, 0" : "=v"(z)); f32x4 r = {z, z, z, z}; asm volatile("" : "+v"(r)); return r; }
__device__ __forceinline__ f32x4 smf(u32x4 a, u32x8 b, f32x4 c, int idx) {
  return __builtin_amdgcn_smfmac_f32_16x16x64_bf16(__builtin_bit_cast(bfv8, a), __builtin_bit_cast(bfv16, b), c, idx, 0, 0);
}
__device__ __forceinline__ float red16max(float x) { auto r = __builtin_amdgcn_permlane16_swap(__float_as_uint(x), __float_as_uint(x), false, false); return fmaxf(__uint_as_float(r[0]), __uint_as_float(r[1])); }
__device__ __forceinline__ float red32max(float x) { auto r = __builtin_amdgcn_permlane32_swap(__float_as_uint(x), __float_as_uint(x), false, false); return fmaxf(__uint_as_float(r[0]), __uint_as_float(r[1])); }
__device__ __forceinline__ float red16add(float x) { auto r = __builtin_amdgcn_permlane16_swap(__float_as_uint(x), __float_as_uint(x), false, false); return __uint_as_float(r[0]) + __uint_as_float(r[1]); }
__device__ __forceinline__ float red32add(float x) { auto r = __builtin_amdgcn_permlane32_swap(__float_as_uint(x), __float_as_uint(x), false, false); return __uint_as_float(r[0]) + __uint_as_float(r[1]); }
constexpr bool blk_in_lds(int R, int b) {
  return R == 0 ? ((b & 7) == 1 || (b & 7) == 4) : (b < 16 ? (b & 3) == 0 : (b & 3) == 3);
}
constexpr int lds_index(int R, int b) { int n = 0; for (int i = 0; i < b; ++i) if (blk_in_lds(R, i)) ++n; return n; }
constexpr bool frag_in_lds(int R, int f) { return blk_in_lds(R, f >> 1); }
constexpr int frag_reg_slot(int R, int f) { int n = 0; for (int i = 0; i < f; ++i) if (!frag_in_lds(R, i)) ++n; return n; }

constexpr int m_block(int R, int m, int i) {
  return R == 0 ? 2 * (i < 12 ? (m ? i + i / 3 + 1 : i + (i + 1) / 3) : 4 * (i - 12) + 2 - 2 * m) + m : m ? 16 + 4 * (i & 3) + (i >> 2) : (i < 12 ? i + i / 3 + 1 : 4 * (i - 12));
}
#define QK_REV 0
#define W_BV 0xba9876543210fedcull
#define VO_ORD 3
constexpr int w_block(int R, int i) {
  if (R && VO_ORD == 2) return i < 12 ? m_block(1, 0, 11 - i) : m_block(1, 1, i - 12);
  if (R && VO_ORD == 3) return i & 1 ? m_block(1, 1, i >> 1) : m_block(1, 0, 11 - (i >> 1));
  if (!R && QK_REV) return m_block(0, i & 1, 11 - (i >> 1));
  return R && VO_ORD ? m_block(R, i < 12, i % 12) : m_block(R, i & 1, i >> 1);
}
#define W_BG 0xfedcba9876543210ull
#define W_NT 0
#define U_ORD 0x3210
constexpr u64 bg_order(int m) { u64 r = 0; for (int n = 0; n < 16; ++n) r |= (u64)(m_block(0, m > 0, ((m == 2 ? W_BV : W_BG) >> (4 * n)) & 15) >> 1) << (4 * n); return r; }
struct WSched { int ord[32]; int slot[32]; int issue[32]; int out[32]; int reg[32]; };
constexpr WSched make_wsched(int R) {
  WSched S{};
  int n = 0;
  for (int i = 0; i < 24; ++i) S.ord[n++] = w_block(R, i);
  for (int i = 0; i < n; ++i) S.reg[i] = frag_reg_slot(R, 2 * S.ord[i]);
  const int npure = n;
  for (int b = 0; b < 32; ++b) if (blk_in_lds(R, b)) S.ord[n++] = b;
  int seq[32] = {};
  int nissued = 0, next = 0;
  for (int i = 0; i < 10; ++i) { S.slot[i] = (i + 10 - npure % 10) % 10; S.issue[i] = -1; seq[i] = nissued++; }
  next = 10;
  S.out[0] = nissued - 1;
  for (int c = 0; c <= npure; ++c) {
    if (c >= 1) {
      const int k = S.slot[c - 1];
      if (next < npure) { S.slot[next] = k; S.issue[next] = c; seq[next] = nissued++; ++next; }
      else if (k < NLF / 2) { S.slot[npure + k] = k; S.issue[npure + k] = c; seq[npure + k] = nissued++; }
    }
    if (c + 1 < npure) S.out[c + 1] = nissued - 1 - seq[c + 1];
  }
  return S;
}
__device__ __forceinline__ int kcol(int k) { return 8 * ((k >> 1) & 1) + ((k & 1) ? 7 - (k >> 2) : k >> 2); }
template <bool PERM = false>
__device__ __forceinline__ u32x8 tr_frag(const char* sb, int tile, int lane) {
  u32x8 f;
#pragma unroll
  for (int q = 0; q < 4; ++q) {
    int row = (q >> 1) * 32 + 8 * (lane >> 4) + 4 * (q & 1) + ((lane & 15) >> 2);
    if (PERM) row = (row & ~15) + kcol(row & 15);
    int p = lane & 3;
    int lc = 2 * tile + (p >> 1);
    int addr = row * 256 + ((lc ^ swz_b(row)) * 16) + 8 * (p & 1);
    bf16x4 v = __builtin_amdgcn_ds_read_tr16_b64_v4i16((LAS bf16x4*)(sb + addr));
    u32x2 u = __builtin_bit_cast(u32x2, v);
    f[2 * q] = u[0]; f[2 * q + 1] = u[1];
  }
  return f;
}
__device__ __forceinline__ void dma16(__amdgpu_buffer_rsrc_t rs, unsigned voff, unsigned soff, unsigned lds, bool nt = false, bool pad = true) {
  if (nt) asm volatile("s_mov_b32 m0, %0\n\ts_nop 3\n\tbuffer_load_dwordx4 %1, %2, %3 offen nt lds" :: "s"(lds), "v"(voff), "s"(rs), "s"(soff) : "m0", "memory");
  else if (pad) asm volatile("s_mov_b32 m0, %0\n\ts_nop 3\n\tbuffer_load_dwordx4 %1, %2, %3 offen lds" :: "s"(lds), "v"(voff), "s"(rs), "s"(soff) : "m0", "memory");
  else asm volatile("s_mov_b32 m0, %0\n\ts_nop 0\n\tbuffer_load_dwordx4 %1, %2, %3 offen lds" :: "s"(lds), "v"(voff), "s"(rs), "s"(soff) : "m0", "memory");
}
__device__ __forceinline__ u32x8 tr_frag2(const char* sb, int tile, int f, int lane) {
  u32x8 r;
#pragma unroll
  for (int c = 0; c < 2; ++c)
#pragma unroll
    for (int t = 0; t < 2; ++t) {
      const int row = 32 * c + 16 * f + 4 * (lane >> 4) + ((lane & 15) >> 2);
      const int p = lane & 3, lc = 2 * (tile + t) + (p >> 1);
      const u32x2 u = __builtin_bit_cast(u32x2, __builtin_amdgcn_ds_read_tr16_b64_v4i16((LAS bf16x4*)(sb + row * 256 + ((lc ^ swz_b(row)) * 16) + 8 * (p & 1))));
      r[4 * c + t] = u[0]; r[4 * c + 2 + t] = u[1];
    }
  return r;
}
template <int ROLE>
__device__ __forceinline__ void load_weights(const ChainArgs& a, int h, int w, int lane, char* smem, Wts& W, Gate& G, u32x4& gv) {
#pragma unroll
  for (int i = 0; i < 4; ++i) asm volatile("" : "=v"(W.v[i]));
#pragma unroll
  for (int i = 0; i < 32; ++i) asm volatile("" : "=a"(W.a[i]));
  constexpr WSched S = make_wsched(ROLE);
  constexpr int NP = 32 - NLF / 2;
  const __amdgpu_buffer_rsrc_t r0 = mk_rsrc(ROLE == 0 ? a.Wq : a.Wv, 8u << 20), r1 = mk_rsrc(ROLE == 0 ? a.Wk : a.Wo, 8u << 20);
  const unsigned lds0 = (unsigned)(uintptr_t)(LAS char*)smem;
  unsigned vo[2][4];
#pragma unroll
  for (int j = 0; j < 4; ++j) {
    const int row = (w * 4 + j) * 4 + (lane >> 4), lc = (lane & 15) ^ swz_b(row);
    vo[0][j] = row * HD * 2 + lc * 16; vo[1][j] = row * DM * 2 + lc * 16;
  }
  asm volatile("s_nop 4");
  auto stage = [&](int i) {
    const int blk = S.ord[i];
    const bool second = ROLE == 0 ? (blk & 1) : blk >= 16, wo = ROLE == 1 && second;
    const unsigned so = wo ? (unsigned)(((128 * h + 64 * (blk & 1)) * DM + 128 * (((blk - 16) >> 1) ^ (2 * (h & 3)))) * 2)
                           : (unsigned)((64 * ((ROLE == 0 ? blk >> 1 : blk) ^ (h & RHO)) * HD + 128 * h) * 2);
#pragma unroll
    for (int j = 0; j < 4; ++j) dma16(second ? r1 : r0, vo[wo][j], so, lds0 + 16384 * S.slot[i] + (w * 4 + j) * 1024, W_NT & (ROLE ? 8 : 1));
  };
#pragma unroll
  for (int i = 0; i < 32; ++i) if (S.issue[i] == -1) stage(i);
#pragma unroll
  for (int c = 0; c < NP; ++c) {
    asm volatile("s_waitcnt vmcnt(%0)" :: "n"(4 * S.out[c]) : "memory");
    lds_barrier();
#pragma unroll
    for (int i = 0; i < 32; ++i) if (S.issue[i] == c) stage(i);
    const char* sb = smem + 16384 * S.slot[c];
#pragma unroll
    for (int t = 0; t < 2; ++t) {
      u32x8 f = (ROLE == 1 && S.ord[c] >= 16) ? tr_frag2(sb, 2 * w, t, lane) : tr_frag<true>(sb, 2 * w + t, lane);
      if (S.reg[c] < 32) {
#pragma unroll
        for (int i = 0; i < 8; ++i) asm volatile("v_accvgpr_write_b32 a[%1], %0" :: "v"(f[i]), "n"(8 * ((S.reg[c] + t) & 31) + i));
      }
      else {
#pragma unroll
        for (int i = 0; i < 8; ++i) W.v[(S.reg[c] >> 2) & 3][8 * ((S.reg[c] + t) & 3) + i] = f[i];
      }
    }
    if (S.reg[c] >= 32) asm volatile("" : "+v"(W.v[(S.reg[c] >> 2) & 3]));
  }
  static_assert(S.issue[31] < NP && S.issue[NP] < S.issue[31], "resident blocks are issued in slot order inside the loop");
  G.ask(a.sync, gv);
#pragma unroll
  for (int j = 0; j < NLF / 2; ++j) {
    asm volatile("s_waitcnt vmcnt(%0)" :: "n"(4 * (NLF / 2 - 1 - j) + 1) : "memory");
    lds_barrier();
    const bool two = ROLE == 1 && S.ord[NP + j] >= 16;
    u32x8 f0 = two ? tr_frag2(smem + 16384 * j, 2 * w, 0, lane) : tr_frag<true>(smem + 16384 * j, 2 * w, lane);
    u32x8 f1 = two ? tr_frag2(smem + 16384 * j, 2 * w, 1, lane) : tr_frag<true>(smem + 16384 * j, 2 * w + 1, lane);
    lds_barrier();
    char* d = smem + 16384 * j + w * 4096 + lane * 16;
    *(u32x4*)d = (u32x4){f0[0], f0[1], f0[2], f0[3]};
    *(u32x4*)(d + 1024) = (u32x4){f0[4], f0[5], f0[6], f0[7]};
    *(u32x4*)(d + 2048) = (u32x4){f1[0], f1[1], f1[2], f1[3]};
    *(u32x4*)(d + 3072) = (u32x4){f1[4], f1[5], f1[6], f1[7]};
  }
  asm volatile("s_waitcnt vmcnt(0)" : "+v"(gv) :: "memory");
  lds_barrier();
}
template <int... Is> struct iseq {};
template <int N, int... Is> struct mkseq : mkseq<N - 1, N - 1, Is...> {};
template <int... Is> struct mkseq<0, Is...> { typedef iseq<Is...> type; };

#define SB0 __builtin_amdgcn_sched_barrier(0)
#define CAT8(x, y) ((u32x8){x[0], x[1], x[2], x[3], y[0], y[1], y[2], y[3]})
template <class F, int... I>
__device__ __forceinline__ void for_slots(iseq<I...>, F f) { (f(std::integral_constant<int, I>{}), ...); }
#define QK_BODY \
  "s_cmp_eq_u32 %[wv], 1\n\t" \
  "s_cbranch_scc1 91f\n\t" \
  "s_cmp_eq_u32 %[wv], 2\n\t" \
  "s_cbranch_scc1 92f\n\t" \
  "s_cmp_eq_u32 %[wv], 3\n\t" \
  "s_cbranch_scc1 93f\n\t" \
  "90:\n\t" \
  "s_mov_b32 s58, s42\n\t" \
  "20:\n\t" \
  "s_and_b32 s76, s44, 7\n\t" \
  "s_lshl_b32 s76, s76, 15\n\t" \
  "s_add_i32 s40, s54, s76\n\t" \
  "s_and_b32 s76, s44, 3\n\t" \
  "s_lshl_b32 s45, s76, 14\n\t" \
  "s_mul_i32 s47, s76, 0xc000\n\t" \
  "s_add_i32 s47, s47, 0x40000\n\t" \
  "s_add_i32 s49, s44, -1\n\t" \
  "s_and_b32 s48, s49, 2\n\t" \
  "s_lshl_b32 s48, s48, 13\n\t" \
  "s_and_b32 s76, s49, 1\n\t" \
  "s_lshl_b32 s76, s76, 12\n\t" \
  "s_add_i32 s48, s48, s76\n\t" \
  "s_lshl_b32 s50, s53, 8\n\t" \
  "s_mov_b32 s59, 0\n\t" \
  "s_mov_b32 s92, s51\n\t" \
  "s_mov_b32 s93, s53\n\t" \
  "s_xor_b32 s76, s40, 0x400\n\t" \
  "s_xor_b32 s77, s40, 0x800\n\t" \
  "s_xor_b32 s78, s40, 0xc00\n\t" \
  "s_cmp_lg_u32 s58, 0\n\t" \
  "s_cbranch_scc1 11f\n\t" \
  "10:\n\t" \
  "ds_read_b128 v[48:51], v102 offset:0\n\t" \
  "ds_read_b128 v[52:55], v102 offset:1024\n\t" \
  "ds_read_b128 v[56:59], v102 offset:2048\n\t" \
  "ds_read_b128 v[60:63], v102 offset:3072\n\t" \
  "ds_read_b128 v[16:19], v102 offset:16384\n\t" \
  "ds_read_b128 v[20:23], v102 offset:17408\n\t" \
  "ds_read_b128 v[24:27], v102 offset:18432\n\t" \
  "ds_read_b128 v[28:31], v102 offset:19456\n\t" \
  "ds_read_b128 v[84:87], v102 offset:49152\n\t" \
  "ds_read_b128 v[88:91], v102 offset:50176\n\t" \
  "ds_read_b128 v[92:95], v102 offset:51200\n\t" \
  "ds_read_b128 v[96:99], v102 offset:52224\n\t" \
  "s_waitcnt lgkmcnt(4)\n\t" \
  "ds_read_b128 v[0:3], v103 offset:16384\n\t" \
  "ds_read_b128 v[4:7], v103 offset:17408\n\t" \
  "ds_read_b128 v[8:11], v103 offset:18432\n\t" \
  "ds_read_b128 v[12:15], v103 offset:19456\n\t" \
  "46:\n\t" \
  "s_bitcmp1_b32 s58, 1\n\t" \
  "s_cselect_b64 s[80:81], -1, 0\n\t" \
  "s_cmp_lg_u32 s58, 0\n\t" \
  "s_cbranch_scc1 17f\n\t" \
  "s_mov_b32 exec_hi, 0\n\t" \
  "16:\n\t" \
  "s_memtime vcc\n\t" \
  "s_waitcnt lgkmcnt(0)\n\t" \
  "s_sub_i32 s79, s41, vcc_lo\n\t" \
  "s_cmp_gt_i32 s79, 64\n\t" \
  "s_cbranch_scc1 16b\n\t" \
  "13:\n\t" \
  "buffer_load_dword v116, v112, %[rh], s48 offen sc1\n\t" \
  "s_waitcnt vmcnt(0)\n\t" \
  "15:\n\t" \
  "v_mov_b32 v117, 20000\n\t" \
  "v_cmp_ne_u32_sdwa vcc, v116, s49 src0_sel:BYTE_0 src1_sel:DWORD\n\t" \
  "v_subrev_u32 v116, s50, v116\n\t" \
  "v_ashrrev_i32 v116, 8, v116\n\t" \
  "s_nop 1\n\t" \
  "v_max_i32_dpp v116, v116, v116 quad_perm:[1,0,3,2] row_mask:0xf bank_mask:0xf\n\t" \
  "s_cbranch_vccnz 14f\n\t" \
  "s_nop 0\n\t" \
  "v_max_i32_dpp v116, v116, v116 quad_perm:[2,3,0,1] row_mask:0xf bank_mask:0xf\n\t" \
  "s_nop 1\n\t" \
  "v_max_i32_dpp v116, v116, v116 row_half_mirror row_mask:0xf bank_mask:0xf\n\t" \
  "s_nop 1\n\t" \
  "v_max_i32_dpp v116, v116, v116 row_mirror row_mask:0xf bank_mask:0xf\n\t" \
  "v_med3_i32 v116, v116, 0, v117\n\t" \
  "v_mul_u32_u24 v116, s52, v116\n\t" \
  "v_lshrrev_b32 v116, 16, v116\n\t" \
  "s_mov_b64 exec, -1\n\t" \
  "v_readfirstlane_b32 s84, v116\n\t" \
  "v_readlane_b32 s85, v116, 16\n\t" \
  "s_max_u32 s84, s84, s85\n\t" \
  "s_add_i32 s84, s84, s51\n\t" \
  ".p2align 6\n\t" \
  "s_nop 0\n\t" \
  "s_nop 0\n\t" \
  "s_nop 0\n\t" \
  "s_nop 0\n\t" \
  "s_nop 0\n\t" \
  "s_nop 0\n\t" \
  "s_nop 0\n\t" \
  "s_nop 0\n\t" \
  "1:\n\t" \
  "s_memtime vcc\n\t" \
  "s_waitcnt lgkmcnt(0)\n\t" \
  "s_sub_i32 s79, s84, vcc_lo\n\t" \
  "s_cmp_gt_i32 s79, 128\n\t" \
  "s_cbranch_scc1 1b\n\t" \
  ".p2align 4\n\t" \
  "2:\n\t" \
  "s_sub_i32 s79, s79, 33\n\t" \
  "s_cmp_gt_i32 s79, 56\n\t" \
  "s_cbranch_scc1 2b\n\t" \
  "buffer_load_dwordx4 v[32:35], v100, %[rh], s40 offen sc1\n\t" \
  "s_nop 12\n\t" \
  "buffer_load_dwordx4 v[36:39], v100, %[rh], s76 offen sc1\n\t" \
  "s_nop 12\n\t" \
  "buffer_load_dwordx4 v[40:43], v100, %[rh], s77 offen sc1\n\t" \
  "s_nop 12\n\t" \
  "buffer_load_dwordx4 v[44:47], v100, %[rh], s78 offen sc1\n\t" \
  "s_memtime vcc\n\t" \
  "s_memrealtime s[82:83]\n\t" \
  "s_nop 4\n\t" \
  "s_memrealtime s[84:85]\n\t" \
  "s_nop 4\n\t" \
  "s_memrealtime s[56:57]\n\t" \
  "s_nop 4\n\t" \
  "s_memrealtime s[86:87]\n\t" \
  "s_nop 4\n\t" \
  "s_memrealtime s[88:89]\n\t" \
  "s_nop 4\n\t" \
  "s_memrealtime s[90:91]\n\t" \
  "s_mov_b32 s92, 0\n\t" \
  "s_waitcnt lgkmcnt(0)\n\t" \
  "s_cmp_eq_u32 s82, s84\n\t" \
  "s_addc_u32 s92, s92, 0\n\t" \
  "s_cmp_eq_u32 s82, s56\n\t" \
  "s_addc_u32 s92, s92, 0\n\t" \
  "s_cmp_eq_u32 s82, s86\n\t" \
  "s_addc_u32 s92, s92, 0\n\t" \
  "s_cmp_eq_u32 s82, s88\n\t" \
  "s_addc_u32 s92, s92, 0\n\t" \
  "s_cmp_eq_u32 s82, s90\n\t" \
  "s_addc_u32 s92, s92, 0\n\t" \
  "s_mul_i32 s92, s92, 24\n\t" \
  "s_add_i32 s92, s92, vcc_lo\n\t" \
  "s_lshl4_add_u32 s93, s82, 64\n\t" \
  "3:\n\t" \
  "s_sub_i32 s84, s92, s43\n\t" \
  "s_lshl_b32 s85, s93, 8\n\t" \
  "s_or_b32 s85, s85, s44\n\t" \
  "v_mov_b32 v118, s85\n\t" \
  "v_mov_b64 v[68:69], 0\n\t" \
  "v_mov_b64 v[70:71], 0\n\t" \
  "v_mov_b64 v[72:73], 0\n\t" \
  "v_mov_b64 v[74:75], 0\n\t" \
  "v_mov_b64 v[76:77], 0\n\t" \
  "v_mov_b64 v[78:79], 0\n\t" \
  "v_mov_b64 v[80:81], 0\n\t" \
  "v_mov_b64 v[82:83], 0\n\t" \
  "s_waitcnt lgkmcnt(0)\n\t" \
  "s_waitcnt vmcnt(3)\n\t" \
  "v_smfmac_f32_16x16x64_bf16 v[68:71], v[32:35], a[0:7], v104\n\t" \
  "ds_write_b128 v101, v[32:35] offset:0\n\t" \
  "v_smfmac_f32_16x16x64_bf16 v[72:75], v[32:35], a[8:15], v104\n\t" \
  "v_smfmac_f32_16x16x64_bf16 v[76:79], v[32:35], v[48:55], v104\n\t" \
  "v_smfmac_f32_16x16x64_bf16 v[80:83], v[32:35], v[56:63], v104\n\t" \
  "s_waitcnt vmcnt(2)\n\t" \
  "v_smfmac_f32_16x16x64_bf16 v[68:71], v[36:39], a[16:23], v104\n\t" \
  "ds_write_b128 v101, v[36:39] offset:1024\n\t" \
  "v_smfmac_f32_16x16x64_bf16 v[72:75], v[36:39], a[24:31], v104\n\t" \
  "v_smfmac_f32_16x16x64_bf16 v[76:79], v[36:39], a[32:39], v104\n\t" \
  "v_cmp_u_f32 vcc, v68, v69\n\t" \
  "v_smfmac_f32_16x16x64_bf16 v[80:83], v[36:39], a[40:47], v104\n\t" \
  "v_cmp_u_f32 s[82:83], v70, v71\n\t" \
  "s_waitcnt vmcnt(1)\n\t" \
  "v_smfmac_f32_16x16x64_bf16 v[68:71], v[40:43], v[16:23], v104\n\t" \
  "ds_write_b128 v101, v[40:43] offset:2048\n\t" \
  "v_smfmac_f32_16x16x64_bf16 v[72:75], v[40:43], v[24:31], v104\n\t" \
  "s_or_b64 vcc, vcc, s[82:83]\n\t" \
  "s_or_b64 vcc, vcc, s[80:81]\n\t" \
  "s_waitcnt lgkmcnt(1)\n\t" \
  "v_smfmac_f32_16x16x64_bf16 v[76:79], v[40:43], a[48:55], v104\n\t" \
  "s_cbranch_scc1 8f\n\t" \
  "5:\n\t" \
  "s_barrier\n\t" \
  "4:\n\t" \
  "s_memtime s[56:57]\n\t" \
  "ds_read_b128 v[48:51], v101 offset:4096\n\t" \
  "v_smfmac_f32_16x16x64_bf16 v[80:83], v[40:43], a[56:63], v104\n\t" \
  "ds_read_b128 v[52:55], v101 offset:5120\n\t" \
  "ds_read_b128 v[56:59], v101 offset:8192\n\t" \
  "s_waitcnt vmcnt(0)\n\t" \
  "v_smfmac_f32_16x16x64_bf16 v[68:71], v[44:47], a[64:71], v104\n\t" \
  "ds_write_b128 v101, v[44:47] offset:3072\n\t" \
  "v_smfmac_f32_16x16x64_bf16 v[72:75], v[44:47], a[72:79], v104\n\t" \
  "v_smfmac_f32_16x16x64_bf16 v[76:79], v[44:47], a[80:87], v104\n\t" \
  "v_cmp_u_f32 vcc, v68, v69\n\t" \
  "v_smfmac_f32_16x16x64_bf16 v[80:83], v[44:47], a[88:95], v104\n\t" \
  "v_cmp_u_f32 s[82:83], v70, v71\n\t" \
  "s_waitcnt lgkmcnt(3)\n\t" \
  "v_smfmac_f32_16x16x64_bf16 v[68:71], v[48:51], a[96:103], v104\n\t" \
  "v_smfmac_f32_16x16x64_bf16 v[72:75], v[48:51], a[104:111], v104\n\t" \
  "s_or_b64 vcc, vcc, s[82:83]\n\t" \
  "s_waitcnt lgkmcnt(0)\n\t" \
  "v_smfmac_f32_16x16x64_bf16 v[68:71], v[52:55], a[112:119], v104\n\t" \
  "s_cbranch_scc1 9f\n\t" \
  "6:\n\t" \
  "s_barrier\n\t" \
  "ds_read_b128 v[60:63], v101 offset:9216\n\t" \
  "v_smfmac_f32_16x16x64_bf16 v[72:75], v[52:55], a[120:127], v104\n\t" \
  "ds_read_b128 v[64:67], v101 offset:12288\n\t" \
  "s_sub_i32 s79, s56, s84\n\t" \
  "s_mul_hi_u32 s79, s79, s46\n\t" \
  "v_smfmac_f32_16x16x64_bf16 v[68:71], v[56:59], a[192:199], v104\n\t" \
  "v_lshl_add_u32 v116, s79, 9, v118\n\t" \
  "buffer_store_dword v116, v109, %[rh], s45 offen sc1\n\t" \
  "v_smfmac_f32_16x16x64_bf16 v[72:75], v[56:59], a[200:207], v104\n\t" \
  "ds_read_b128 v[48:51], v101 offset:13312\n\t" \
  "s_waitcnt lgkmcnt(2)\n\t" \
  "v_smfmac_f32_16x16x64_bf16 v[68:71], v[60:63], a[208:215], v104\n\t" \
  "ds_read_b128 v[52:55], v101 offset:6144\n\t" \
  "v_smfmac_f32_16x16x64_bf16 v[72:75], v[60:63], a[216:223], v104\n\t" \
  "ds_read_b128 v[56:59], v101 offset:7168\n\t" \
  "s_waitcnt lgkmcnt(3)\n\t" \
  "v_smfmac_f32_16x16x64_bf16 v[68:71], v[64:67], v[152:159], v104\n\t" \
  "ds_read_b128 v[60:63], v101 offset:10240\n\t" \
  "v_smfmac_f32_16x16x64_bf16 v[72:75], v[64:67], v[160:167], v104\n\t" \
  "ds_read_b128 v[32:35], v101 offset:20480\n\t" \
  "s_waitcnt lgkmcnt(4)\n\t" \
  "v_smfmac_f32_16x16x64_bf16 v[68:71], v[48:51], v[168:175], v104\n\t" \
  "ds_read_b128 v[64:67], v101 offset:11264\n\t" \
  "ds_read_b128 v[16:19], v103 offset:49152\n\t" \
  "v_smfmac_f32_16x16x64_bf16 v[72:75], v[48:51], v[176:183], v104\n\t" \
  "ds_read_b128 v[36:39], v101 offset:21504\n\t" \
  "s_waitcnt lgkmcnt(6)\n\t" \
  "v_smfmac_f32_16x16x64_bf16 v[68:71], v[52:55], v[84:91], v104\n\t" \
  "ds_read_b128 v[48:51], v101 offset:15360\n\t" \
  "ds_read_b128 v[20:23], v103 offset:50176\n\t" \
  "v_smfmac_f32_16x16x64_bf16 v[72:75], v[52:55], v[92:99], v104\n\t" \
  "ds_read_b128 v[40:43], v101 offset:22528\n\t" \
  "s_waitcnt lgkmcnt(8)\n\t" \
  "v_smfmac_f32_16x16x64_bf16 v[68:71], v[56:59], a[160:167], v104\n\t" \
  "ds_read_b128 v[52:55], v101 offset:14336\n\t" \
  "ds_read_b128 v[24:27], v103 offset:51200\n\t" \
  "v_smfmac_f32_16x16x64_bf16 v[72:75], v[56:59], a[168:175], v104\n\t" \
  "ds_read_b128 v[28:31], v103 offset:52224\n\t" \
  "s_waitcnt lgkmcnt(10)\n\t" \
  "v_smfmac_f32_16x16x64_bf16 v[68:71], v[60:63], v[0:7], v104\n\t" \
  "ds_read_b128 v[44:47], v101 offset:23552\n\t" \
  "v_smfmac_f32_16x16x64_bf16 v[72:75], v[60:63], v[8:15], v104\n\t" \
  "v_mov_b64 v[92:93], 0\n\t" \
  "s_waitcnt lgkmcnt(9)\n\t" \
  "v_smfmac_f32_16x16x64_bf16 v[68:71], v[64:67], v[120:127], v104\n\t" \
  "ds_read_b128 v[0:3], v101 offset:24576\n\t" \
  "v_smfmac_f32_16x16x64_bf16 v[72:75], v[64:67], v[128:135], v104\n\t" \
  "ds_read_b128 v[4:7], v101 offset:25600\n\t" \
  "s_waitcnt lgkmcnt(8)\n\t" \
  "v_smfmac_f32_16x16x64_bf16 v[68:71], v[48:51], v[216:223], v104\n\t" \
  "v_mov_b64 v[94:95], 0\n\t" \
  "s_waitcnt lgkmcnt(5)\n\t" \
  "v_smfmac_f32_16x16x64_bf16 v[68:71], v[52:55], v[16:23], v104\n\t" \
  "ds_read_b128 v[8:11], v101 offset:26624\n\t" \
  "v_smfmac_f32_16x16x64_bf16 v[72:75], v[48:51], v[224:231], v104\n\t" \
  "ds_read_b128 v[12:15], v101 offset:27648\n\t" \
  "s_waitcnt lgkmcnt(5)\n\t" \
  "v_smfmac_f32_16x16x64_bf16 v[72:75], v[52:55], v[24:31], v104\n\t" \
  "v_add_f32 v96, v68, v70\n\t" \
  "v_smfmac_f32_16x16x64_bf16 v[76:79], v[56:59], a[176:183], v104\n\t" \
  "v_add_f32 v97, v69, v71\n\t" \
  "v_cvt_pk_bf16_f32 v96, v96, v97\n\t" \
  "v_smfmac_f32_16x16x64_bf16 v[80:83], v[56:59], a[184:191], v104\n\t" \
  "v_add_f32 v98, v72, v74\n\t" \
  "v_add_f32 v99, v73, v75\n\t" \
  "v_smfmac_f32_16x16x64_bf16 v[76:79], v[60:63], a[240:247], v104\n\t" \
  "v_cvt_pk_bf16_f32 v98, v98, v99\n\t" \
  "ds_write2st64_b32 v105, v96, v98 offset1:1\n\t" \
  "v_smfmac_f32_16x16x64_bf16 v[80:83], v[60:63], a[248:255], v104\n\t" \
  "ds_read_b128 v[56:59], v101 offset:9216\n\t" \
  "v_mov_b64 v[68:69], 0\n\t" \
  "v_smfmac_f32_16x16x64_bf16 v[76:79], v[64:67], v[136:143], v104\n\t" \
  "ds_read_b128 v[60:63], v101 offset:13312\n\t" \
  "v_mov_b64 v[70:71], 0\n\t" \
  "v_smfmac_f32_16x16x64_bf16 v[80:83], v[64:67], v[144:151], v104\n\t" \
  "v_smfmac_f32_16x16x64_bf16 v[76:79], v[48:51], v[232:239], v104\n\t" \
  "s_waitcnt lgkmcnt(2)\n\t" \
  "s_barrier\n\t" \
  "ds_read_b64_tr_b16 v[84:85], v107 offset:0\n\t" \
  "v_smfmac_f32_16x16x64_bf16 v[80:83], v[48:51], v[240:247], v104\n\t" \
  "ds_read_b64_tr_b16 v[86:87], v107 offset:128\n\t" \
  "ds_read_b64_tr_b16 v[88:89], v107 offset:1024\n\t" \
  "v_smfmac_f32_16x16x64_bf16 v[76:79], v[52:55], v[200:207], v104\n\t" \
  "ds_read_b64_tr_b16 v[90:91], v107 offset:1152\n\t" \
  "v_smfmac_f32_16x16x64_bf16 v[80:83], v[52:55], v[208:215], v104\n\t" \
  "ds_read_b128 v[64:67], v101 offset:6144\n\t" \
  "s_waitcnt lgkmcnt(5)\n\t" \
  "v_smfmac_f32_16x16x64_bf16 v[76:79], v[60:63], v[184:191], v104\n\t" \
  "ds_read_b128 v[52:55], v101 offset:5120\n\t" \
  "s_waitcnt lgkmcnt(4)\n\t" \
  "v_smfmac_f32_16x16x64_bf16 v[92:95], v[84:87], v[32:39], v104\n\t" \
  "s_waitcnt lgkmcnt(2)\n\t" \
  "v_smfmac_f32_16x16x64_bf16 v[92:95], v[88:91], v[40:47], v104\n\t" \
  "v_smfmac_f32_16x16x64_bf16 v[68:71], v[84:87], v[0:7], v104\n\t" \
  "v_smfmac_f32_16x16x64_bf16 v[68:71], v[88:91], v[8:15], v104\n\t" \
  "v_add_f32 v92, v92, v94\n\t" \
  "v_add_f32 v94, v93, v95\n\t" \
  "v_smfmac_f32_16x16x64_bf16 v[80:83], v[60:63], v[192:199], v104\n\t" \
  "v_mov_b32 v97, v94\n\t" \
  "v_mov_b32_dpp v94, v92 row_ror:8 row_mask:0xf bank_mask:0x3\n\t" \
  "s_nop 0\n\t" \
  "v_mov_b32_dpp v92, v97 row_ror:8 row_mask:0xf bank_mask:0xc\n\t" \
  "s_waitcnt lgkmcnt(1)\n\t" \
  "v_smfmac_f32_16x16x64_bf16 v[76:79], v[64:67], a[144:151], v104\n\t" \
  "v_add_f32 v93, v68, v70\n\t" \
  "v_add_f32 v95, v69, v71\n\t" \
  "v_max_f32 v98, v92, v94\n\t" \
  "v_mov_b32 v97, v95\n\t" \
  "v_mov_b32_dpp v95, v93 row_ror:8 row_mask:0xf bank_mask:0x3\n\t" \
  "v_smfmac_f32_16x16x64_bf16 v[80:83], v[64:67], a[152:159], v104\n\t" \
  "v_mov_b32_dpp v93, v97 row_ror:8 row_mask:0xf bank_mask:0xc\n\t" \
  "v_max3_f32 v98, v98, v93, v95\n\t" \
  "s_waitcnt lgkmcnt(0)\n\t" \
  "v_smfmac_f32_16x16x64_bf16 v[76:79], v[52:55], a[128:135], v104\n\t" \
  "v_max_f32_dpp v98, v98, v98 quad_perm:[1,0,3,2] row_mask:0xf bank_mask:0xf\n\t" \
  "v_smfmac_f32_16x16x64_bf16 v[76:79], v[56:59], a[224:231], v104\n\t" \
  "s_nop 0\n\t" \
  "v_max_f32_dpp v98, v98, v98 quad_perm:[2,3,0,1] row_mask:0xf bank_mask:0xf\n\t" \
  "v_smfmac_f32_16x16x64_bf16 v[80:83], v[52:55], a[136:143], v104\n\t" \
  "s_nop 0\n\t" \
  "v_max_f32_dpp v98, v98, v98 row_half_mirror row_mask:0xf bank_mask:0xf\n\t" \
  "v_mul_f32 v98, %[cs], v98\n\t" \
  "v_fma_f32 v92, v92, %[cs], -v98\n\t" \
  "v_fma_f32 v93, v93, %[cs], -v98\n\t" \
  "v_fma_f32 v94, v94, %[cs], -v98\n\t" \
  "v_fma_f32 v95, v95, %[cs], -v98\n\t" \
  "v_exp_f32 v72, v92\n\t" \
  "v_exp_f32 v73, v93\n\t" \
  "v_exp_f32 v74, v94\n\t" \
  "v_smfmac_f32_16x16x64_bf16 v[80:83], v[56:59], a[232:239], v104\n\t" \
  "v_exp_f32 v75, v95\n\t" \
  "v_cvt_pk_bf16_f32 v68, v72, v73\n\t" \
  "v_cvt_pk_bf16_f32 v69, v74, v75\n\t" \
  "buffer_store_dwordx2 v[68:69], v110, %[rh], s47 offen sc1\n\t" \
  "v_add_f32 v20, v76, v78\n\t" \
  "v_add_f32 v21, v77, v79\n\t" \
  "v_cvt_pk_bf16_f32 v20, v20, v21\n\t" \
  "ds_write_b32 v106, v20\n\t" \
  "v_add_f32 v23, v80, v82\n\t" \
  "v_add_f32 v21, v81, v83\n\t" \
  "v_cvt_pk_bf16_f32 v21, v23, v21\n\t" \
  "ds_write_b32 v106, v21 offset:256\n\t" \
  "v_mov_b64 v[0:1], 0\n\t" \
  "v_mov_b64 v[2:3], 0\n\t" \
  "v_mov_b64 v[36:37], 0\n\t" \
  "v_mov_b64 v[38:39], 0\n\t" \
  "s_waitcnt lgkmcnt(0)\n\t" \
  "s_barrier\n\t" \
  "ds_read_b64_tr_b16 v[32:33], v108 offset:12288\n\t" \
  "ds_read_b64_tr_b16 v[34:35], v108 offset:12352\n\t" \
  "ds_read_b64_tr_b16 v[4:5], v108\n\t" \
  "ds_read_b64_tr_b16 v[6:7], v108 offset:64\n\t" \
  "ds_read_b64_tr_b16 v[8:9], v108 offset:512\n\t" \
  "ds_read_b64_tr_b16 v[10:11], v108 offset:576\n\t" \
  "ds_read_b64_tr_b16 v[12:13], v108 offset:1024\n\t" \
  "ds_read_b64_tr_b16 v[14:15], v108 offset:1088\n\t" \
  "ds_read_b64_tr_b16 v[16:17], v108 offset:1536\n\t" \
  "ds_read_b64_tr_b16 v[18:19], v108 offset:1600\n\t" \
  "v_add_f32 v29, v72, v73\n\t" \
  "v_add_f32 v29, v29, v74\n\t" \
  "s_waitcnt lgkmcnt(8)\n\t" \
  "v_smfmac_f32_16x16x64_bf16 v[0:3], v[84:87], v[32:39], v104\n\t" \
  "s_waitcnt lgkmcnt(4)\n\t" \
  "v_smfmac_f32_16x16x64_bf16 v[0:3], v[84:87], v[4:11], v104\n\t" \
  "s_waitcnt lgkmcnt(0)\n\t" \
  "v_smfmac_f32_16x16x64_bf16 v[0:3], v[88:91], v[12:19], v104\n\t" \
  "v_add_f32 v29, v29, v75\n\t" \
  "s_mov_b32 s76, 0xaaaaaaaa\n\t" \
  "s_mov_b32 s77, s76\n\t" \
  "s_nop 4\n\t" \
  "v_add_f32 v96, v0, v2\n\t" \
  "v_add_f32_dpp v96, v1, v3 quad_perm:[0,1,2,3] row_mask:0xf bank_mask:0xc\n\t" \
  "v_fma_f32 v96, v96, %[cs], -v98\n\t" \
  "v_exp_f32 v24, v96\n\t" \
  "s_nop 0\n\t" \
  "v_add_f32 v29, v29, v24\n\t" \
  "s_nop 0\n\t" \
  "v_mov_b32_dpp v25, v24 quad_perm:[1,0,3,2] row_mask:0xf bank_mask:0xf\n\t" \
  "v_add_f32_dpp v29, v29, v29 quad_perm:[1,0,3,2] row_mask:0xf bank_mask:0xf\n\t" \
  "v_cndmask_b32_e64 v24, v24, v114, s[76:77]\n\t" \
  "v_cndmask_b32_e64 v25, v25, v115, s[76:77]\n\t" \
  "v_add_f32_dpp v29, v29, v29 quad_perm:[2,3,0,1] row_mask:0xf bank_mask:0xf\n\t" \
  "s_nop 1\n\t" \
  "v_add_f32_dpp v29, v29, v29 row_half_mirror row_mask:0xf bank_mask:0xf\n\t" \
  "v_rcp_f32 v26, v29\n\t" \
  "v_cmpx_gt_f32 vcc, 0x5d5e0b6b, v29\n\t" \
  "v_mul_f32 v24, v24, v26\n\t" \
  "v_mul_f32 v25, v25, v26\n\t" \
  "v_cvt_pk_bf16_f32 v24, v24, v25\n\t" \
  "buffer_store_dword v24, v111, %[rh], s47 offen sc1\n\t" \
  "s_memtime s[76:77]\n\t" \
  "s_mov_b64 exec, -1\n\t" \
  "v_mov_b32 v97, v29\n\t" \
  "s_cmp_eq_u64 vcc, -1\n\t" \
  "s_cselect_b32 s42, 0, 8\n\t" \
  "s_waitcnt lgkmcnt(0)\n\t" \
  "s_mov_b32 s57, s76\n\t" \
  "s_barrier\n\t" \
  "s_add_i32 s41, s56, 1950\n\t" \
  "s_sub_i32 s76, s57, s56\n\t" \
  "s_add_i32 s77, s43, 8\n\t" \
  "s_min_u32 s76, s76, s77\n\t" \
  "s_sub_i32 s43, s43, 4\n\t" \
  "s_max_u32 s43, s43, s76\n\t" \
  "s_add_i32 s76, s45, 0x4000\n\t" \
  "s_and_b32 s76, s76, 0xc000\n\t" \
  "v_mov_b32 v116, -1\n\t" \
  "buffer_store_dword v116, v109, %[rh], s76 offen sc1\n\t" \
  "s_sub_i32 s76, s92, s51\n\t" \
  "s_sub_i32 s77, s93, s53\n\t" \
  "s_cmp_eq_u32 s76, 0\n\t" \
  "s_cbranch_scc1 21f\n\t" \
  "v_cvt_f32_u32 v117, s76\n\t" \
  "v_cvt_f32_u32 v116, s77\n\t" \
  "v_rcp_f32 v117, v117\n\t" \
  "s_mov_b32 s51, s92\n\t" \
  "v_mul_f32 v117, v117, v116\n\t" \
  "v_max_f32 v117, 0x3f028f5c, v117\n\t" \
  "v_min_f32 v117, 0x3ffeb852, v117\n\t" \
  "v_rcp_f32 v116, v117\n\t" \
  "v_mul_f32 v117, 0x4f000000, v117\n\t" \
  "v_mul_f32 v116, 0x47800000, v116\n\t" \
  "v_cvt_u32_f32 v117, v117\n\t" \
  "v_cvt_u32_f32 v116, v116\n\t" \
  "s_mov_b32 s53, s93\n\t" \
  "v_readfirstlane_b32 s46, v117\n\t" \
  "v_readfirstlane_b32 s52, v116\n\t" \
  "21:\n\t" \
  "s_add_i32 s44, s44, 1\n\t" \
  "s_and_b32 s58, s58, 4\n\t" \
  "s_and_b32 s76, s44, 64\n\t" \
  "s_or_b32 s42, s42, s76\n\t" \
  "s_cbranch_scc0 20b\n\t" \
  "s_branch 99f\n\t" \
  "14:\n\t" \
  "s_sleep 3\n\t" \
  "s_add_u32 s59, s59, 0x10000\n\t" \
  "s_cmp_lt_u32 s59, 0x100000\n\t" \
  "s_cbranch_scc1 13b\n\t" \
  "s_mov_b64 exec, -1\n\t" \
  "s_mov_b32 s42, 16\n\t" \
  "s_branch 99f\n\t" \
  "8:\n\t" \
  "s_bitcmp1_b32 s58, 1\n\t" \
  "s_cbranch_scc1 4b\n\t" \
  "s_bitcmp1_b32 s58, 2\n\t" \
  "s_cbranch_scc1 5b\n\t" \
  "v_max3_u32 v116, v32, v33, v34\n\t" \
  "v_max3_u32 v117, v35, v36, v37\n\t" \
  "v_max3_u32 v116, v116, v38, v39\n\t" \
  "v_max_u32 v116, v116, v117\n\t" \
  "v_cmp_eq_u32 vcc, -1, v116\n\t" \
  "s_cbranch_vccz 5b\n\t" \
  "s_branch 0f\n\t" \
  "9:\n\t" \
  "s_bitcmp1_b32 s58, 2\n\t" \
  "s_cbranch_scc1 6b\n\t" \
  "v_max3_u32 v116, v40, v41, v42\n\t" \
  "v_max3_u32 v117, v43, v44, v45\n\t" \
  "v_max3_u32 v116, v116, v46, v47\n\t" \
  "v_max_u32 v116, v116, v117\n\t" \
  "v_cmp_eq_u32 vcc, -1, v116\n\t" \
  "s_cbranch_vccz 6b\n\t" \
  "s_or_b32 s58, s58, 2\n\t" \
  "0:\n\t" \
  "s_or_b32 s58, s58, 1\n\t" \
  "11:\n\t" \
  "buffer_load_dwordx4 v[32:35], v100, %[rh], s40 offen sc1\n\t" \
  "buffer_load_dwordx4 v[36:39], v100, %[rh], s76 offen sc1\n\t" \
  "buffer_load_dwordx4 v[40:43], v100, %[rh], s77 offen sc1\n\t" \
  "buffer_load_dwordx4 v[44:47], v100, %[rh], s78 offen sc1\n\t" \
  "s_waitcnt lgkmcnt(0)\n\t" \
  "s_branch 10b\n\t" \
  "17:\n\t" \
  "s_add_u32 s59, s59, 1\n\t" \
  "s_bitcmp1_b32 s58, 2\n\t" \
  "s_cbranch_scc1 3b\n\t" \
  "s_waitcnt vmcnt(0)\n\t" \
  "v_max_u32 v116, v32, v33\n\t" \
  "v_max3_u32 v116, v116, v34, v35\n\t" \
  "v_max3_u32 v116, v116, v36, v37\n\t" \
  "v_max3_u32 v116, v116, v38, v39\n\t" \
  "v_max3_u32 v116, v116, v40, v41\n\t" \
  "v_max3_u32 v116, v116, v42, v43\n\t" \
  "v_max3_u32 v116, v116, v44, v45\n\t" \
  "v_max3_u32 v116, v116, v46, v47\n\t" \
  "v_cmp_eq_u32 vcc, -1, v116\n\t" \
  "s_cbranch_vccz 3b\n\t" \
  "s_bitcmp1_b32 s59, 6\n\t" \
  "s_cbranch_scc1 12f\n\t" \
  "buffer_load_dwordx4 v[32:35], v100, %[rh], s40 offen sc1\n\t" \
  "buffer_load_dwordx4 v[36:39], v100, %[rh], s76 offen sc1\n\t" \
  "buffer_load_dwordx4 v[40:43], v100, %[rh], s77 offen sc1\n\t" \
  "buffer_load_dwordx4 v[44:47], v100, %[rh], s78 offen sc1\n\t" \
  "s_branch 17b\n\t" \
  "12:\n\t" \
  "s_mov_b32 s42, s58\n\t" \
  "s_branch 99f\n\t" \
  "91:\n\t" \
  "s_mov_b32 s58, s42\n\t" \
  "s_mov_b32 s95, 0\n\t" \
  "20:\n\t" \
  "s_and_b32 s76, s44, 7\n\t" \
  "s_lshl_b32 s76, s76, 15\n\t" \
  "s_add_i32 s40, s54, s76\n\t" \
  "s_and_b32 s76, s44, 3\n\t" \
  "s_lshl_b32 s45, s76, 14\n\t" \
  "s_add_i32 s47, s44, 1\n\t" \
  "s_min_u32 s47, s47, 63\n\t" \
  "s_lshr_b32 s76, s47, 1\n\t" \
  "s_lshl_b32 s47, s47, 20\n\t" \
  "s_add_i32 s47, s47, s55\n\t" \
  "s_bitcmp1_b64 %[sel], s76\n\t" \
  "s_cselect_b32 s76, 0x2000, 0\n\t" \
  "s_add_i32 s47, s47, s76\n\t" \
  "s_lshr_b32 s76, s44, 1\n\t" \
  "s_lshl_b32 s94, s44, 20\n\t" \
  "s_bitcmp1_b64 %[sel], s76\n\t" \
  "s_cselect_b32 s76, 0x2000, 0\n\t" \
  "s_add_i32 s94, s94, s76\n\t" \
  "s_add_i32 s49, s44, -1\n\t" \
  "s_and_b32 s48, s49, 2\n\t" \
  "s_lshl_b32 s48, s48, 13\n\t" \
  "s_and_b32 s76, s49, 1\n\t" \
  "s_lshl_b32 s76, s76, 12\n\t" \
  "s_add_i32 s48, s48, s76\n\t" \
  "s_lshl_b32 s50, s53, 8\n\t" \
  "s_mov_b32 s59, 0\n\t" \
  "s_mov_b32 s92, s51\n\t" \
  "s_mov_b32 s93, s53\n\t" \
  "s_xor_b32 s76, s40, 0x400\n\t" \
  "s_xor_b32 s77, s40, 0x800\n\t" \
  "s_xor_b32 s78, s40, 0xc00\n\t" \
  "s_cmp_lg_u32 s58, 0\n\t" \
  "s_cbranch_scc1 11f\n\t" \
  "s_cmp_lg_u32 s95, 0\n\t" \
  "s_cbranch_scc1 46f\n\t" \
  "10:\n\t" \
  "ds_read_b128 v[48:51], v102 offset:32768\n\t" \
  "ds_read_b128 v[52:55], v102 offset:33792\n\t" \
  "ds_read_b128 v[56:59], v102 offset:34816\n\t" \
  "ds_read_b128 v[60:63], v102 offset:35840\n\t" \
  "ds_read_b128 v[16:19], v102 offset:49152\n\t" \
  "ds_read_b128 v[20:23], v102 offset:50176\n\t" \
  "ds_read_b128 v[24:27], v102 offset:51200\n\t" \
  "ds_read_b128 v[28:31], v102 offset:52224\n\t" \
  "ds_read_b128 v[84:87], v103 offset:16384\n\t" \
  "ds_read_b128 v[88:91], v103 offset:17408\n\t" \
  "ds_read_b128 v[92:95], v103 offset:18432\n\t" \
  "ds_read_b128 v[96:99], v103 offset:19456\n\t" \
  "s_waitcnt lgkmcnt(4)\n\t" \
  "ds_read_b128 v[0:3], v103 offset:49152\n\t" \
  "ds_read_b128 v[4:7], v103 offset:50176\n\t" \
  "ds_read_b128 v[8:11], v103 offset:51200\n\t" \
  "ds_read_b128 v[12:15], v103 offset:52224\n\t" \
  "46:\n\t" \
  "s_bitcmp1_b32 s58, 1\n\t" \
  "s_cselect_b64 s[80:81], -1, 0\n\t" \
  "s_cmp_lg_u32 s58, 0\n\t" \
  "s_cbranch_scc1 17f\n\t" \
  "s_mov_b32 exec_hi, 0\n\t" \
  "s_cmp_eq_u32 s95, 0\n\t" \
  "s_cbranch_scc1 16f\n\t" \
  "s_waitcnt vmcnt(2)\n\t" \
  "v_mov_b32 v116, v118\n\t" \
  "s_branch 15f\n\t" \
  "16:\n\t" \
  "s_memtime vcc\n\t" \
  "s_waitcnt lgkmcnt(0)\n\t" \
  "s_sub_i32 s79, s41, vcc_lo\n\t" \
  "s_cmp_gt_i32 s79, 64\n\t" \
  "s_cbranch_scc1 16b\n\t" \
  "13:\n\t" \
  "buffer_load_dword v116, v112, %[rh], s48 offen sc1\n\t" \
  "s_waitcnt vmcnt(0)\n\t" \
  "15:\n\t" \
  "v_mov_b32 v117, 20000\n\t" \
  "v_cmp_ne_u32_sdwa vcc, v116, s49 src0_sel:BYTE_0 src1_sel:DWORD\n\t" \
  "v_subrev_u32 v116, s50, v116\n\t" \
  "v_ashrrev_i32 v116, 8, v116\n\t" \
  "s_nop 1\n\t" \
  "v_max_i32_dpp v116, v116, v116 quad_perm:[1,0,3,2] row_mask:0xf bank_mask:0xf\n\t" \
  "s_cbranch_vccnz 14f\n\t" \
  "s_nop 0\n\t" \
  "v_max_i32_dpp v116, v116, v116 quad_perm:[2,3,0,1] row_mask:0xf bank_mask:0xf\n\t" \
  "s_nop 1\n\t" \
  "v_max_i32_dpp v116, v116, v116 row_half_mirror row_mask:0xf bank_mask:0xf\n\t" \
  "s_nop 1\n\t" \
  "v_max_i32_dpp v116, v116, v116 row_mirror row_mask:0xf bank_mask:0xf\n\t" \
  "v_med3_i32 v116, v116, 0, v117\n\t" \
  "v_mul_u32_u24 v116, s52, v116\n\t" \
  "v_lshrrev_b32 v116, 16, v116\n\t" \
  "s_mov_b64 exec, -1\n\t" \
  "v_readfirstlane_b32 s84, v116\n\t" \
  "v_readlane_b32 s85, v116, 16\n\t" \
  "s_max_u32 s84, s84, s85\n\t" \
  "s_add_i32 s84, s84, s51\n\t" \
  ".p2align 6\n\t" \
  "s_nop 0\n\t" \
  "s_nop 0\n\t" \
  "s_nop 0\n\t" \
  "s_nop 0\n\t" \
  "s_nop 0\n\t" \
  "s_nop 0\n\t" \
  "s_nop 0\n\t" \
  "s_nop 0\n\t" \
  "1:\n\t" \
  "s_memtime vcc\n\t" \
  "s_waitcnt lgkmcnt(0)\n\t" \
  "s_sub_i32 s79, s84, vcc_lo\n\t" \
  "s_cmp_gt_i32 s79, 128\n\t" \
  "s_cbranch_scc1 1b\n\t" \
  ".p2align 4\n\t" \
  "2:\n\t" \
  "s_sub_i32 s79, s79, 33\n\t" \
  "s_cmp_gt_i32 s79, 56\n\t" \
  "s_cbranch_scc1 2b\n\t" \
  "buffer_load_dwordx4 v[32:35], v100, %[rh], s40 offen sc1\n\t" \
  "s_nop 12\n\t" \
  "buffer_load_dwordx4 v[36:39], v100, %[rh], s76 offen sc1\n\t" \
  "s_nop 12\n\t" \
  "buffer_load_dwordx4 v[40:43], v100, %[rh], s77 offen sc1\n\t" \
  "s_nop 12\n\t" \
  "buffer_load_dwordx4 v[44:47], v100, %[rh], s78 offen sc1\n\t" \
  "s_memtime vcc\n\t" \
  "s_memrealtime s[82:83]\n\t" \
  "s_nop 4\n\t" \
  "s_memrealtime s[84:85]\n\t" \
  "s_nop 4\n\t" \
  "s_memrealtime s[56:57]\n\t" \
  "s_nop 4\n\t" \
  "s_memrealtime s[86:87]\n\t" \
  "s_nop 4\n\t" \
  "s_memrealtime s[88:89]\n\t" \
  "s_nop 4\n\t" \
  "s_memrealtime s[90:91]\n\t" \
  "s_mov_b32 s92, 0\n\t" \
  "s_waitcnt lgkmcnt(0)\n\t" \
  "s_cmp_eq_u32 s82, s84\n\t" \
  "s_addc_u32 s92, s92, 0\n\t" \
  "s_cmp_eq_u32 s82, s56\n\t" \
  "s_addc_u32 s92, s92, 0\n\t" \
  "s_cmp_eq_u32 s82, s86\n\t" \
  "s_addc_u32 s92, s92, 0\n\t" \
  "s_cmp_eq_u32 s82, s88\n\t" \
  "s_addc_u32 s92, s92, 0\n\t" \
  "s_cmp_eq_u32 s82, s90\n\t" \
  "s_addc_u32 s92, s92, 0\n\t" \
  "s_mul_i32 s92, s92, 24\n\t" \
  "s_add_i32 s92, s92, vcc_lo\n\t" \
  "s_lshl4_add_u32 s93, s82, 64\n\t" \
  "3:\n\t" \
  "v_mov_b64 v[68:69], 0\n\t" \
  "v_mov_b64 v[70:71], 0\n\t" \
  "v_mov_b64 v[72:73], 0\n\t" \
  "v_mov_b64 v[74:75], 0\n\t" \
  "v_mov_b64 v[76:77], 0\n\t" \
  "v_mov_b64 v[78:79], 0\n\t" \
  "v_mov_b64 v[80:81], 0\n\t" \
  "v_mov_b64 v[82:83], 0\n\t" \
  "s_waitcnt lgkmcnt(0)\n\t" \
  "s_waitcnt vmcnt(3)\n\t" \
  "v_smfmac_f32_16x16x64_bf16 v[68:71], v[32:35], a[96:103], v104\n\t" \
  "ds_write_b128 v101, v[32:35] offset:4096\n\t" \
  "v_smfmac_f32_16x16x64_bf16 v[72:75], v[32:35], a[104:111], v104\n\t" \
  "v_smfmac_f32_16x16x64_bf16 v[76:79], v[32:35], v[48:55], v104\n\t" \
  "v_smfmac_f32_16x16x64_bf16 v[80:83], v[32:35], v[56:63], v104\n\t" \
  "s_waitcnt vmcnt(2)\n\t" \
  "v_smfmac_f32_16x16x64_bf16 v[68:71], v[36:39], a[112:119], v104\n\t" \
  "ds_write_b128 v101, v[36:39] offset:5120\n\t" \
  "v_smfmac_f32_16x16x64_bf16 v[72:75], v[36:39], a[120:127], v104\n\t" \
  "v_smfmac_f32_16x16x64_bf16 v[76:79], v[36:39], a[128:135], v104\n\t" \
  "v_cmp_u_f32 vcc, v68, v69\n\t" \
  "v_smfmac_f32_16x16x64_bf16 v[80:83], v[36:39], a[136:143], v104\n\t" \
  "v_cmp_u_f32 s[82:83], v70, v71\n\t" \
  "s_waitcnt vmcnt(1)\n\t" \
  "v_smfmac_f32_16x16x64_bf16 v[68:71], v[40:43], v[16:23], v104\n\t" \
  "ds_write_b128 v101, v[40:43] offset:6144\n\t" \
  "v_smfmac_f32_16x16x64_bf16 v[72:75], v[40:43], v[24:31], v104\n\t" \
  "s_or_b64 vcc, vcc, s[82:83]\n\t" \
  "s_or_b64 vcc, vcc, s[80:81]\n\t" \
  "s_waitcnt lgkmcnt(1)\n\t" \
  "v_smfmac_f32_16x16x64_bf16 v[76:79], v[40:43], a[144:151], v104\n\t" \
  "s_cbranch_scc1 8f\n\t" \
  "5:\n\t" \
  "s_barrier\n\t" \
  "4:\n\t" \
  "s_memtime s[56:57]\n\t" \
  "ds_read_b128 v[48:51], v101 offset:8192\n\t" \
  "v_smfmac_f32_16x16x64_bf16 v[80:83], v[40:43], a[152:159], v104\n\t" \
  "ds_read_b128 v[52:55], v101 offset:9216\n\t" \
  "ds_read_b128 v[56:59], v101 offset:12288\n\t" \
  "s_waitcnt vmcnt(0)\n\t" \
  "v_smfmac_f32_16x16x64_bf16 v[68:71], v[44:47], a[160:167], v104\n\t" \
  "ds_write_b128 v101, v[44:47] offset:7168\n\t" \
  "v_smfmac_f32_16x16x64_bf16 v[72:75], v[44:47], a[168:175], v104\n\t" \
  "v_smfmac_f32_16x16x64_bf16 v[76:79], v[44:47], a[176:183], v104\n\t" \
  "v_cmp_u_f32 vcc, v68, v69\n\t" \
  "v_smfmac_f32_16x16x64_bf16 v[80:83], v[44:47], a[184:191], v104\n\t" \
  "v_cmp_u_f32 s[82:83], v70, v71\n\t" \
  "s_waitcnt lgkmcnt(3)\n\t" \
  "v_smfmac_f32_16x16x64_bf16 v[68:71], v[48:51], a[192:199], v104\n\t" \
  "v_smfmac_f32_16x16x64_bf16 v[72:75], v[48:51], a[200:207], v104\n\t" \
  "s_or_b64 vcc, vcc, s[82:83]\n\t" \
  "s_waitcnt lgkmcnt(0)\n\t" \
  "v_smfmac_f32_16x16x64_bf16 v[68:71], v[52:55], a[208:215], v104\n\t" \
  "s_cbranch_scc1 9f\n\t" \
  "6:\n\t" \
  "s_barrier\n\t" \
  "ds_read_b128 v[60:63], v101 offset:13312\n\t" \
  "v_smfmac_f32_16x16x64_bf16 v[72:75], v[52:55], a[216:223], v104\n\t" \
  "ds_read_b128 v[64:67], v101 offset:0\n\t" \
  "v_smfmac_f32_16x16x64_bf16 v[68:71], v[56:59], v[152:159], v104\n\t" \
  "v_smfmac_f32_16x16x64_bf16 v[72:75], v[56:59], v[160:167], v104\n\t" \
  "ds_read_b128 v[48:51], v101 offset:1024\n\t" \
  "s_waitcnt lgkmcnt(2)\n\t" \
  "v_smfmac_f32_16x16x64_bf16 v[68:71], v[60:63], v[168:175], v104\n\t" \
  "ds_read_b128 v[52:55], v101 offset:10240\n\t" \
  "v_smfmac_f32_16x16x64_bf16 v[72:75], v[60:63], v[176:183], v104\n\t" \
  "ds_read_b128 v[56:59], v101 offset:11264\n\t" \
  "s_waitcnt lgkmcnt(3)\n\t" \
  "v_smfmac_f32_16x16x64_bf16 v[68:71], v[64:67], a[0:7], v104\n\t" \
  "ds_read_b128 v[60:63], v101 offset:14336\n\t" \
  "v_smfmac_f32_16x16x64_bf16 v[72:75], v[64:67], a[8:15], v104\n\t" \
  "s_waitcnt lgkmcnt(3)\n\t" \
  "v_smfmac_f32_16x16x64_bf16 v[68:71], v[48:51], a[16:23], v104\n\t" \
  "ds_read_b128 v[64:67], v101 offset:15360\n\t" \
  "ds_read_b128 v[16:19], v102 offset:16384\n\t" \
  "v_smfmac_f32_16x16x64_bf16 v[72:75], v[48:51], a[24:31], v104\n\t" \
  "s_waitcnt lgkmcnt(4)\n\t" \
  "v_smfmac_f32_16x16x64_bf16 v[68:71], v[52:55], v[84:91], v104\n\t" \
  "ds_read_b128 v[48:51], v101 offset:3072\n\t" \
  "ds_read_b128 v[20:23], v102 offset:17408\n\t" \
  "v_smfmac_f32_16x16x64_bf16 v[72:75], v[52:55], v[92:99], v104\n\t" \
  "s_waitcnt lgkmcnt(5)\n\t" \
  "v_smfmac_f32_16x16x64_bf16 v[68:71], v[56:59], v[120:127], v104\n\t" \
  "ds_read_b128 v[52:55], v101 offset:2048\n\t" \
  "ds_read_b128 v[24:27], v102 offset:18432\n\t" \
  "v_smfmac_f32_16x16x64_bf16 v[72:75], v[56:59], v[128:135], v104\n\t" \
  "ds_read_b128 v[28:31], v102 offset:19456\n\t" \
  "s_waitcnt lgkmcnt(7)\n\t" \
  "v_smfmac_f32_16x16x64_bf16 v[68:71], v[60:63], v[0:7], v104\n\t" \
  "ds_read_b128 v[36:39], v102 offset:28672\n\t" \
  "v_smfmac_f32_16x16x64_bf16 v[72:75], v[60:63], v[8:15], v104\n\t" \
  "buffer_load_dwordx4 v[84:87], v107, %[ru], s94 offen offset:0 sc1\n\t" \
  "s_waitcnt lgkmcnt(7)\n\t" \
  "v_smfmac_f32_16x16x64_bf16 v[68:71], v[64:67], v[216:223], v104\n\t" \
  "ds_read_b128 v[0:3], v103 offset:0\n\t" \
  "v_smfmac_f32_16x16x64_bf16 v[72:75], v[64:67], v[224:231], v104\n\t" \
  "ds_read_b128 v[4:7], v103 offset:1024\n\t" \
  "s_waitcnt lgkmcnt(7)\n\t" \
  "v_smfmac_f32_16x16x64_bf16 v[68:71], v[48:51], a[64:71], v104\n\t" \
  "buffer_load_dwordx4 v[88:91], v107, %[ru], s94 offen offset:1024 sc1\n\t" \
  "s_waitcnt lgkmcnt(5)\n\t" \
  "v_smfmac_f32_16x16x64_bf16 v[68:71], v[52:55], v[16:23], v104\n\t" \
  "ds_read_b128 v[8:11], v103 offset:2048\n\t" \
  "v_smfmac_f32_16x16x64_bf16 v[72:75], v[48:51], a[72:79], v104\n\t" \
  "ds_read_b128 v[12:15], v103 offset:3072\n\t" \
  "s_waitcnt lgkmcnt(5)\n\t" \
  "v_smfmac_f32_16x16x64_bf16 v[72:75], v[52:55], v[24:31], v104\n\t" \
  "v_add_f32 v44, v68, v70\n\t" \
  "ds_read_b128 v[40:43], v102 offset:29696\n\t" \
  "v_smfmac_f32_16x16x64_bf16 v[76:79], v[56:59], v[136:143], v104\n\t" \
  "v_add_f32 v45, v69, v71\n\t" \
  "v_cvt_pk_bf16_f32 v44, v44, v45\n\t" \
  "v_smfmac_f32_16x16x64_bf16 v[80:83], v[56:59], v[144:151], v104\n\t" \
  "v_add_f32 v46, v72, v74\n\t" \
  "v_add_f32 v47, v73, v75\n\t" \
  "v_smfmac_f32_16x16x64_bf16 v[76:79], v[60:63], v[200:207], v104\n\t" \
  "v_cvt_pk_bf16_f32 v46, v46, v47\n\t" \
  "ds_write2st64_b32 v105, v44, v46 offset1:1\n\t" \
  "v_smfmac_f32_16x16x64_bf16 v[80:83], v[60:63], v[208:215], v104\n\t" \
  "ds_read_b128 v[56:59], v101 offset:8192\n\t" \
  "v_mov_b64 v[68:69], 0\n\t" \
  "v_smfmac_f32_16x16x64_bf16 v[76:79], v[64:67], v[232:239], v104\n\t" \
  "ds_read_b128 v[16:19], v103 offset:32768\n\t" \
  "v_mov_b64 v[70:71], 0\n\t" \
  "v_smfmac_f32_16x16x64_bf16 v[80:83], v[64:67], v[240:247], v104\n\t" \
  "ds_read_b128 v[20:23], v103 offset:33792\n\t" \
  "v_smfmac_f32_16x16x64_bf16 v[76:79], v[48:51], a[80:87], v104\n\t" \
  "s_waitcnt lgkmcnt(3)\n\t" \
  "s_barrier\n\t" \
  "v_smfmac_f32_16x16x64_bf16 v[80:83], v[48:51], a[88:95], v104\n\t" \
  "buffer_load_dwordx4 v[92:95], v107, %[ru], s94 offen offset:2048 sc1\n\t" \
  "v_smfmac_f32_16x16x64_bf16 v[68:71], v[32:35], v[36:43], v104\n\t" \
  "ds_read_b128 v[36:39], v102 offset:61440\n\t" \
  "ds_read_b128 v[40:43], v102 offset:62464\n\t" \
  "v_smfmac_f32_16x16x64_bf16 v[76:79], v[52:55], a[48:55], v104\n\t" \
  "ds_read_b128 v[60:63], v101 offset:1024\n\t" \
  "ds_read_b128 v[24:27], v103 offset:34816\n\t" \
  "v_smfmac_f32_16x16x64_bf16 v[80:83], v[52:55], a[56:63], v104\n\t" \
  "ds_read_b128 v[64:67], v101 offset:10240\n\t" \
  "buffer_load_dwordx4 v[96:99], v107, %[ru], s94 offen offset:3072 sc1\n\t" \
  "s_waitcnt lgkmcnt(7)\n\t" \
  "v_smfmac_f32_16x16x64_bf16 v[76:79], v[56:59], v[0:7], v104\n\t" \
  "ds_read_b128 v[48:51], v101 offset:12288\n\t" \
  "ds_read_b128 v[28:31], v103 offset:35840\n\t" \
  "global_load_dword v119, v109, %[kc] sc1\n\t" \
  "v_smfmac_f32_16x16x64_bf16 v[80:83], v[56:59], v[8:15], v104\n\t" \
  "s_waitcnt lgkmcnt(4)\n\t" \
  "v_smfmac_f32_16x16x64_bf16 v[76:79], v[60:63], a[32:39], v104\n\t" \
  "ds_read_b128 v[52:55], v101 offset:9216\n\t" \
  "buffer_load_dwordx4 v[32:35], v100, %[ru], s47 offen offset:0 sc1\n\t" \
  "v_smfmac_f32_16x16x64_bf16 v[80:83], v[60:63], a[40:47], v104\n\t" \
  "ds_read_b128 v[0:3], v102 offset:0\n\t" \
  "v_smfmac_f32_16x16x64_bf16 v[68:71], v[56:59], v[36:43], v104\n\t" \
  "ds_read_b128 v[36:39], v103 offset:28672\n\t" \
  "ds_read_b128 v[40:43], v103 offset:29696\n\t" \
  "s_waitcnt lgkmcnt(6)\n\t" \
  "v_smfmac_f32_16x16x64_bf16 v[76:79], v[64:67], a[240:247], v104\n\t" \
  "ds_read_b128 v[56:59], v101 offset:13312\n\t" \
  "v_smfmac_f32_16x16x64_bf16 v[80:83], v[64:67], a[248:255], v104\n\t" \
  "ds_read_b128 v[60:63], v101 offset:0\n\t" \
  "ds_read_b128 v[4:7], v102 offset:1024\n\t" \
  "buffer_load_dwordx4 v[44:47], v100, %[ru], s47 offen offset:1024 sc1\n\t" \
  "s_waitcnt lgkmcnt(8)\n\t" \
  "v_smfmac_f32_16x16x64_bf16 v[76:79], v[48:51], v[16:23], v104\n\t" \
  "ds_read_b128 v[8:11], v102 offset:2048\n\t" \
  "s_waitcnt lgkmcnt(8)\n\t" \
  "v_smfmac_f32_16x16x64_bf16 v[80:83], v[48:51], v[24:31], v104\n\t" \
  "ds_read_b128 v[12:15], v102 offset:3072\n\t" \
  "s_waitcnt lgkmcnt(8)\n\t" \
  "v_smfmac_f32_16x16x64_bf16 v[76:79], v[52:55], a[224:231], v104\n\t" \
  "s_waitcnt lgkmcnt(5)\n\t" \
  "v_smfmac_f32_16x16x64_bf16 v[68:71], v[48:51], v[36:43], v104\n\t" \
  "v_smfmac_f32_16x16x64_bf16 v[80:83], v[52:55], a[232:239], v104\n\t" \
  "s_waitcnt lgkmcnt(4)\n\t" \
  "v_smfmac_f32_16x16x64_bf16 v[76:79], v[56:59], v[184:191], v104\n\t" \
  "v_add_f32 v72, v68, v70\n\t" \
  "s_waitcnt lgkmcnt(2)\n\t" \
  "v_smfmac_f32_16x16x64_bf16 v[76:79], v[60:63], v[0:7], v104\n\t" \
  "v_add_f32 v73, v69, v71\n\t" \
  "v_smfmac_f32_16x16x64_bf16 v[80:83], v[56:59], v[192:199], v104\n\t" \
  "v_cvt_pk_bf16_f32 v72, v72, v73\n\t" \
  "s_waitcnt lgkmcnt(0)\n\t" \
  "v_smfmac_f32_16x16x64_bf16 v[80:83], v[60:63], v[8:15], v104\n\t" \
  "ds_write_b32 v106, v72 offset:11776\n\t" \
  "v_add_f32 v20, v76, v78\n\t" \
  "v_add_f32 v21, v77, v79\n\t" \
  "v_cvt_pk_bf16_f32 v20, v20, v21\n\t" \
  "ds_write_b32 v106, v20\n\t" \
  "s_nop 3\n\t" \
  "v_add_f32 v23, v80, v82\n\t" \
  "v_add_f32 v21, v81, v83\n\t" \
  "v_cvt_pk_bf16_f32 v21, v23, v21\n\t" \
  "ds_write_b32 v106, v21 offset:256\n\t" \
  "s_waitcnt lgkmcnt(0)\n\t" \
  "s_barrier\n\t" \
  "v_bfe_i32 v64, v114, 0, 1\n\t" \
  "v_and_b32 v64, 0xff800000, v64\n\t" \
  "v_bfe_i32 v65, v114, 1, 1\n\t" \
  "v_and_b32 v65, 0xff800000, v65\n\t" \
  "v_bfe_i32 v66, v114, 2, 1\n\t" \
  "v_and_b32 v66, 0xff800000, v66\n\t" \
  "v_bfe_i32 v67, v114, 3, 1\n\t" \
  "v_and_b32 v67, 0xff800000, v67\n\t" \
  "v_bfe_i32 v68, v114, 4, 1\n\t" \
  "v_and_b32 v68, 0xff800000, v68\n\t" \
  "v_bfe_i32 v69, v114, 5, 1\n\t" \
  "v_and_b32 v69, 0xff800000, v69\n\t" \
  "v_bfe_i32 v70, v114, 6, 1\n\t" \
  "v_and_b32 v70, 0xff800000, v70\n\t" \
  "v_bfe_i32 v71, v114, 7, 1\n\t" \
  "v_and_b32 v71, 0xff800000, v71\n\t" \
  "s_lshl_b32 s94, s45, 4\n\t" \
  "s_and_b32 s96, s44, 2\n\t" \
  "s_lshl_b32 s96, s96, 13\n\t" \
  "s_and_b32 s76, s44, 1\n\t" \
  "s_lshl_b32 s76, s76, 12\n\t" \
  "s_add_i32 s96, s96, s76\n\t" \
  "s_mov_b32 s95, 1\n\t" \
  "s_add_i32 s41, s56, 1950\n\t" \
  "s_sub_i32 s76, s92, s51\n\t" \
  "s_sub_i32 s77, s93, s53\n\t" \
  "s_cmp_eq_u32 s76, 0\n\t" \
  "s_cbranch_scc1 21f\n\t" \
  "v_cvt_f32_u32 v117, s76\n\t" \
  "v_cvt_f32_u32 v116, s77\n\t" \
  "v_rcp_f32 v117, v117\n\t" \
  "s_mov_b32 s51, s92\n\t" \
  "v_mul_f32 v117, v117, v116\n\t" \
  "v_max_f32 v117, 0x3f028f5c, v117\n\t" \
  "v_min_f32 v117, 0x3ffeb852, v117\n\t" \
  "v_rcp_f32 v116, v117\n\t" \
  "v_mul_f32 v117, 0x4f000000, v117\n\t" \
  "v_mul_f32 v116, 0x47800000, v116\n\t" \
  "v_cvt_u32_f32 v117, v117\n\t" \
  "v_cvt_u32_f32 v116, v116\n\t" \
  "s_mov_b32 s53, s93\n\t" \
  "v_readfirstlane_b32 s46, v117\n\t" \
  "v_readfirstlane_b32 s52, v116\n\t" \
  "21:\n\t" \
  "ds_read_b128 v[0:3], v101 offset:20480\n\t" \
  "ds_read_b128 v[4:7], v101 offset:21504\n\t" \
  "ds_read_b128 v[8:11], v101 offset:22528\n\t" \
  "ds_read_b128 v[12:15], v101 offset:23552\n\t" \
  "ds_read_b128 v[16:19], v101 offset:24576\n\t" \
  "ds_read_b128 v[20:23], v101 offset:25600\n\t" \
  "ds_read_b128 v[24:27], v101 offset:26624\n\t" \
  "ds_read_b128 v[28:31], v101 offset:27648\n\t" \
  "ds_read_b64_tr_b16 v[48:49], v108 offset:0\n\t" \
  "ds_read_b64_tr_b16 v[50:51], v108 offset:64\n\t" \
  "ds_read_b64_tr_b16 v[52:53], v108 offset:512\n\t" \
  "ds_read_b64_tr_b16 v[54:55], v108 offset:576\n\t" \
  "ds_read_b64_tr_b16 v[56:57], v108 offset:1024\n\t" \
  "ds_read_b64_tr_b16 v[58:59], v108 offset:1088\n\t" \
  "ds_read_b64_tr_b16 v[60:61], v108 offset:1536\n\t" \
  "ds_read_b64_tr_b16 v[62:63], v108 offset:1600\n\t" \
  "ds_read_b64_tr_b16 v[76:77], v108 offset:12288\n\t" \
  "ds_read_b64_tr_b16 v[78:79], v108 offset:12352\n\t" \
  "s_waitcnt vmcnt(3) lgkmcnt(10)\n\t" \
  "v_mfma_f32_16x16x32_bf16 v[64:67], v[0:3], v[84:87], v[64:67]\n\t" \
  "v_mfma_f32_16x16x32_bf16 v[68:71], v[16:19], v[84:87], v[68:71]\n\t" \
  "v_mfma_f32_16x16x32_bf16 v[64:67], v[4:7], v[88:91], v[64:67]\n\t" \
  "v_mfma_f32_16x16x32_bf16 v[68:71], v[20:23], v[88:91], v[68:71]\n\t" \
  "v_mfma_f32_16x16x32_bf16 v[64:67], v[8:11], v[92:95], v[64:67]\n\t" \
  "v_mfma_f32_16x16x32_bf16 v[68:71], v[24:27], v[92:95], v[68:71]\n\t" \
  "v_mfma_f32_16x16x32_bf16 v[64:67], v[12:15], v[96:99], v[64:67]\n\t" \
  "v_mfma_f32_16x16x32_bf16 v[68:71], v[28:31], v[96:99], v[68:71]\n\t" \
  "s_waitcnt lgkmcnt(0)\n\t" \
  "s_barrier\n\t" \
  "v_mfma_f32_16x16x32_bf16 v[72:75], v[48:51], v[84:87], 0\n\t" \
  "v_mfma_f32_16x16x32_bf16 v[72:75], v[52:55], v[88:91], v[72:75]\n\t" \
  "v_mfma_f32_16x16x32_bf16 v[72:75], v[56:59], v[92:95], v[72:75]\n\t" \
  "v_mfma_f32_16x16x32_bf16 v[72:75], v[60:63], v[96:99], v[72:75]\n\t" \
  "v_mfma_f32_16x16x32_bf16 v[72:75], v[76:79], v[84:87], v[72:75]\n\t" \
  "s_waitcnt vmcnt(0)\n\t" \
  "ds_write_b128 v101, v[32:35] offset:20480\n\t" \
  "ds_write_b128 v101, v[44:47] offset:21504\n\t" \
  "ds_read_b128 v[48:51], v102 offset:32768\n\t" \
  "ds_read_b128 v[52:55], v102 offset:33792\n\t" \
  "ds_read_b128 v[56:59], v102 offset:34816\n\t" \
  "ds_read_b128 v[60:63], v102 offset:35840\n\t" \
  "ds_read_b128 v[16:19], v102 offset:49152\n\t" \
  "ds_read_b128 v[20:23], v102 offset:50176\n\t" \
  "ds_read_b128 v[24:27], v102 offset:51200\n\t" \
  "ds_read_b128 v[28:31], v102 offset:52224\n\t" \
  "ds_read_b128 v[84:87], v103 offset:16384\n\t" \
  "ds_read_b128 v[88:91], v103 offset:17408\n\t" \
  "ds_read_b128 v[92:95], v103 offset:18432\n\t" \
  "ds_read_b128 v[96:99], v103 offset:19456\n\t" \
  "ds_read_b128 v[0:3], v103 offset:49152\n\t" \
  "ds_read_b128 v[4:7], v103 offset:50176\n\t" \
  "ds_read_b128 v[8:11], v103 offset:51200\n\t" \
  "ds_read_b128 v[12:15], v103 offset:52224\n\t" \
  "v_max3_f32 v76, v64, v65, v66\n\t" \
  "v_max3_f32 v77, v67, v68, v69\n\t" \
  "v_max3_f32 v76, v76, v70, v71\n\t" \
  "v_max3_f32 v77, v77, v72, v73\n\t" \
  "v_max3_f32 v76, v76, v74, v75\n\t" \
  "v_max_f32 v76, v76, v77\n\t" \
  "v_mov_b32 v77, v76\n\t" \
  "s_nop 1\n\t" \
  "v_permlane16_swap_b32 v76, v77\n\t" \
  "v_max_f32 v76, v76, v77\n\t" \
  "v_mov_b32 v77, v76\n\t" \
  "s_nop 1\n\t" \
  "v_permlane32_swap_b32 v76, v77\n\t" \
  "v_max_f32 v76, v76, v77\n\t" \
  "v_mul_f32 v76, %[cs], v76\n\t" \
  "v_fma_f32 v64, v64, %[cs], -v76\n\t" \
  "v_fma_f32 v65, v65, %[cs], -v76\n\t" \
  "v_fma_f32 v66, v66, %[cs], -v76\n\t" \
  "v_fma_f32 v67, v67, %[cs], -v76\n\t" \
  "v_fma_f32 v68, v68, %[cs], -v76\n\t" \
  "v_fma_f32 v69, v69, %[cs], -v76\n\t" \
  "v_fma_f32 v70, v70, %[cs], -v76\n\t" \
  "v_fma_f32 v71, v71, %[cs], -v76\n\t" \
  "v_fma_f32 v72, v72, %[cs], -v76\n\t" \
  "v_fma_f32 v73, v73, %[cs], -v76\n\t" \
  "v_fma_f32 v74, v74, %[cs], -v76\n\t" \
  "v_fma_f32 v75, v75, %[cs], -v76\n\t" \
  "v_exp_f32 v64, v64\n\t" \
  "v_exp_f32 v65, v65\n\t" \
  "v_exp_f32 v66, v66\n\t" \
  "v_exp_f32 v67, v67\n\t" \
  "v_exp_f32 v68, v68\n\t" \
  "v_exp_f32 v69, v69\n\t" \
  "v_exp_f32 v70, v70\n\t" \
  "v_exp_f32 v71, v71\n\t" \
  "v_exp_f32 v72, v72\n\t" \
  "v_exp_f32 v73, v73\n\t" \
  "v_exp_f32 v74, v74\n\t" \
  "v_exp_f32 v75, v75\n\t" \
  "v_add_f32 v76, v72, v73\n\t" \
  "v_add_f32 v77, v74, v75\n\t" \
  "v_add_f32 v78, v64, v65\n\t" \
  "v_add_f32 v79, v66, v67\n\t" \
  "v_add_f32 v76, v76, v77\n\t" \
  "v_add_f32 v78, v78, v68\n\t" \
  "v_add_f32 v79, v79, v69\n\t" \
  "v_add_f32 v78, v78, v70\n\t" \
  "v_add_f32 v79, v79, v71\n\t" \
  "v_fma_f32 v76, v76, 0.5, v78\n\t" \
  "v_add_f32 v76, v76, v79\n\t" \
  "v_mov_b32 v77, v76\n\t" \
  "s_nop 1\n\t" \
  "v_permlane16_swap_b32 v76, v77\n\t" \
  "v_add_f32 v76, v76, v77\n\t" \
  "v_mov_b32 v77, v76\n\t" \
  "s_nop 1\n\t" \
  "v_permlane32_swap_b32 v76, v77\n\t" \
  "v_add_f32 v76, v76, v77\n\t" \
  "v_rcp_f32 v76, v76\n\t" \
  "s_mov_b32 exec_hi, 0\n\t" \
  "buffer_load_dword v118, v112, %[rh], s96 offen sc1\n\t" \
  "s_mov_b64 exec, -1\n\t" \
  "v_mul_f32 v64, v64, v76\n\t" \
  "v_mul_f32 v65, v65, v76\n\t" \
  "v_mul_f32 v66, v66, v76\n\t" \
  "v_mul_f32 v67, v67, v76\n\t" \
  "v_mul_f32 v68, v68, v76\n\t" \
  "v_mul_f32 v69, v69, v76\n\t" \
  "v_mul_f32 v70, v70, v76\n\t" \
  "v_mul_f32 v71, v71, v76\n\t" \
  "v_mul_f32 v72, v72, v76\n\t" \
  "v_mul_f32 v73, v73, v76\n\t" \
  "v_mul_f32 v74, v74, v76\n\t" \
  "v_mul_f32 v75, v75, v76\n\t" \
  "v_cvt_pk_bf16_f32 v76, v64, v68\n\t" \
  "v_cvt_pk_bf16_f32 v77, v65, v69\n\t" \
  "v_cvt_pk_bf16_f32 v78, v66, v70\n\t" \
  "v_cvt_pk_bf16_f32 v79, v67, v71\n\t" \
  "v_cvt_pk_bf16_f32 v80, v72, v73\n\t" \
  "v_cvt_pk_bf16_f32 v81, v74, v75\n\t" \
  "v_permlane32_swap_b32 v76, v78\n\t" \
  "v_permlane32_swap_b32 v77, v79\n\t" \
  "v_mov_b64 v[82:83], v[80:81]\n\t" \
  "buffer_store_dwordx4 v[76:79], v110, %[rh], s94 offen sc1\n\t" \
  "s_nop 0\n\t" \
  "v_permlane16_swap_b32 v80, v82\n\t" \
  "v_permlane16_swap_b32 v81, v83\n\t" \
  "buffer_store_dwordx4 v[80:83], v111, %[rh], s94 offen sc1\n\t" \
  "s_add_i32 s77, s44, 1\n\t" \
  "s_min_u32 s77, s77, 63\n\t" \
  "v_cmp_gt_i32 vcc, s77, v119\n\t" \
  "s_cmp_lg_u64 vcc, 0\n\t" \
  "s_cselect_b32 s42, 32, 0\n\t" \
  "s_add_i32 s44, s44, 1\n\t" \
  "s_and_b32 s58, s58, 4\n\t" \
  "s_and_b32 s76, s44, 64\n\t" \
  "s_or_b32 s42, s42, s76\n\t" \
  "s_cbranch_scc0 20b\n\t" \
  "s_branch 99f\n\t" \
  "14:\n\t" \
  "s_sleep 3\n\t" \
  "s_add_u32 s59, s59, 0x10000\n\t" \
  "s_cmp_lt_u32 s59, 0x100000\n\t" \
  "s_cbranch_scc1 13b\n\t" \
  "s_mov_b64 exec, -1\n\t" \
  "s_mov_b32 s42, 16\n\t" \
  "s_branch 99f\n\t" \
  "8:\n\t" \
  "s_bitcmp1_b32 s58, 1\n\t" \
  "s_cbranch_scc1 4b\n\t" \
  "s_bitcmp1_b32 s58, 2\n\t" \
  "s_cbranch_scc1 5b\n\t" \
  "v_max3_u32 v116, v32, v33, v34\n\t" \
  "v_max3_u32 v117, v35, v36, v37\n\t" \
  "v_max3_u32 v116, v116, v38, v39\n\t" \
  "v_max_u32 v116, v116, v117\n\t" \
  "v_cmp_eq_u32 vcc, -1, v116\n\t" \
  "s_cbranch_vccz 5b\n\t" \
  "s_branch 0f\n\t" \
  "9:\n\t" \
  "s_bitcmp1_b32 s58, 2\n\t" \
  "s_cbranch_scc1 6b\n\t" \
  "v_max3_u32 v116, v40, v41, v42\n\t" \
  "v_max3_u32 v117, v43, v44, v45\n\t" \
  "v_max3_u32 v116, v116, v46, v47\n\t" \
  "v_max_u32 v116, v116, v117\n\t" \
  "v_cmp_eq_u32 vcc, -1, v116\n\t" \
  "s_cbranch_vccz 6b\n\t" \
  "s_or_b32 s58, s58, 2\n\t" \
  "0:\n\t" \
  "s_or_b32 s58, s58, 1\n\t" \
  "11:\n\t" \
  "buffer_load_dwordx4 v[32:35], v100, %[rh], s40 offen sc1\n\t" \
  "buffer_load_dwordx4 v[36:39], v100, %[rh], s76 offen sc1\n\t" \
  "buffer_load_dwordx4 v[40:43], v100, %[rh], s77 offen sc1\n\t" \
  "buffer_load_dwordx4 v[44:47], v100, %[rh], s78 offen sc1\n\t" \
  "s_waitcnt lgkmcnt(0)\n\t" \
  "s_branch 10b\n\t" \
  "17:\n\t" \
  "s_add_u32 s59, s59, 1\n\t" \
  "s_bitcmp1_b32 s58, 2\n\t" \
  "s_cbranch_scc1 3b\n\t" \
  "s_waitcnt vmcnt(0)\n\t" \
  "v_max_u32 v116, v32, v33\n\t" \
  "v_max3_u32 v116, v116, v34, v35\n\t" \
  "v_max3_u32 v116, v116, v36, v37\n\t" \
  "v_max3_u32 v116, v116, v38, v39\n\t" \
  "v_max3_u32 v116, v116, v40, v41\n\t" \
  "v_max3_u32 v116, v116, v42, v43\n\t" \
  "v_max3_u32 v116, v116, v44, v45\n\t" \
  "v_max3_u32 v116, v116, v46, v47\n\t" \
  "v_cmp_eq_u32 vcc, -1, v116\n\t" \
  "s_cbranch_vccz 3b\n\t" \
  "s_bitcmp1_b32 s59, 6\n\t" \
  "s_cbranch_scc1 12f\n\t" \
  "buffer_load_dwordx4 v[32:35], v100, %[rh], s40 offen sc1\n\t" \
  "buffer_load_dwordx4 v[36:39], v100, %[rh], s76 offen sc1\n\t" \
  "buffer_load_dwordx4 v[40:43], v100, %[rh], s77 offen sc1\n\t" \
  "buffer_load_dwordx4 v[44:47], v100, %[rh], s78 offen sc1\n\t" \
  "s_branch 17b\n\t" \
  "12:\n\t" \
  "s_mov_b32 s42, s58\n\t" \
  "s_branch 99f\n\t" \
  "92:\n\t" \
  "s_mov_b32 s58, s42\n\t" \
  "s_mov_b32 s95, 0\n\t" \
  "20:\n\t" \
  "s_and_b32 s76, s44, 7\n\t" \
  "s_lshl_b32 s76, s76, 15\n\t" \
  "s_add_i32 s40, s54, s76\n\t" \
  "s_and_b32 s76, s44, 3\n\t" \
  "s_lshl_b32 s45, s76, 14\n\t" \
  "s_add_i32 s47, s44, 1\n\t" \
  "s_min_u32 s47, s47, 63\n\t" \
  "s_lshr_b32 s76, s47, 1\n\t" \
  "s_lshl_b32 s47, s47, 20\n\t" \
  "s_add_i32 s47, s47, s55\n\t" \
  "s_bitcmp1_b64 %[sel], s76\n\t" \
  "s_cselect_b32 s76, 0x2000, 0\n\t" \
  "s_add_i32 s47, s47, s76\n\t" \
  "s_lshr_b32 s76, s44, 1\n\t" \
  "s_lshl_b32 s94, s44, 20\n\t" \
  "s_bitcmp1_b64 %[sel], s76\n\t" \
  "s_cselect_b32 s76, 0x2000, 0\n\t" \
  "s_add_i32 s94, s94, s76\n\t" \
  "s_add_i32 s49, s44, -1\n\t" \
  "s_and_b32 s48, s49, 2\n\t" \
  "s_lshl_b32 s48, s48, 13\n\t" \
  "s_and_b32 s76, s49, 1\n\t" \
  "s_lshl_b32 s76, s76, 12\n\t" \
  "s_add_i32 s48, s48, s76\n\t" \
  "s_lshl_b32 s50, s53, 8\n\t" \
  "s_mov_b32 s59, 0\n\t" \
  "s_mov_b32 s92, s51\n\t" \
  "s_mov_b32 s93, s53\n\t" \
  "s_xor_b32 s76, s40, 0x400\n\t" \
  "s_xor_b32 s77, s40, 0x800\n\t" \
  "s_xor_b32 s78, s40, 0xc00\n\t" \
  "s_cmp_lg_u32 s58, 0\n\t" \
  "s_cbranch_scc1 11f\n\t" \
  "s_cmp_lg_u32 s95, 0\n\t" \
  "s_cbranch_scc1 46f\n\t" \
  "10:\n\t" \
  "ds_read_b128 v[48:51], v103 offset:0\n\t" \
  "ds_read_b128 v[52:55], v103 offset:1024\n\t" \
  "ds_read_b128 v[56:59], v103 offset:2048\n\t" \
  "ds_read_b128 v[60:63], v103 offset:3072\n\t" \
  "ds_read_b128 v[16:19], v103 offset:16384\n\t" \
  "ds_read_b128 v[20:23], v103 offset:17408\n\t" \
  "ds_read_b128 v[24:27], v103 offset:18432\n\t" \
  "ds_read_b128 v[28:31], v103 offset:19456\n\t" \
  "ds_read_b128 v[84:87], v103 offset:49152\n\t" \
  "ds_read_b128 v[88:91], v103 offset:50176\n\t" \
  "ds_read_b128 v[92:95], v103 offset:51200\n\t" \
  "ds_read_b128 v[96:99], v103 offset:52224\n\t" \
  "s_waitcnt lgkmcnt(4)\n\t" \
  "ds_read_b128 v[0:3], v102 offset:16384\n\t" \
  "ds_read_b128 v[4:7], v102 offset:17408\n\t" \
  "ds_read_b128 v[8:11], v102 offset:18432\n\t" \
  "ds_read_b128 v[12:15], v102 offset:19456\n\t" \
  "46:\n\t" \
  "s_bitcmp1_b32 s58, 1\n\t" \
  "s_cselect_b64 s[80:81], -1, 0\n\t" \
  "s_cmp_lg_u32 s58, 0\n\t" \
  "s_cbranch_scc1 17f\n\t" \
  "s_mov_b32 exec_hi, 0\n\t" \
  "s_cmp_eq_u32 s95, 0\n\t" \
  "s_cbranch_scc1 16f\n\t" \
  "s_waitcnt vmcnt(2)\n\t" \
  "v_mov_b32 v116, v118\n\t" \
  "s_branch 15f\n\t" \
  "16:\n\t" \
  "s_memtime vcc\n\t" \
  "s_waitcnt lgkmcnt(0)\n\t" \
  "s_sub_i32 s79, s41, vcc_lo\n\t" \
  "s_cmp_gt_i32 s79, 64\n\t" \
  "s_cbranch_scc1 16b\n\t" \
  "13:\n\t" \
  "buffer_load_dword v116, v112, %[rh], s48 offen sc1\n\t" \
  "s_waitcnt vmcnt(0)\n\t" \
  "15:\n\t" \
  "v_mov_b32 v117, 20000\n\t" \
  "v_cmp_ne_u32_sdwa vcc, v116, s49 src0_sel:BYTE_0 src1_sel:DWORD\n\t" \
  "v_subrev_u32 v116, s50, v116\n\t" \
  "v_ashrrev_i32 v116, 8, v116\n\t" \
  "s_nop 1\n\t" \
  "v_max_i32_dpp v116, v116, v116 quad_perm:[1,0,3,2] row_mask:0xf bank_mask:0xf\n\t" \
  "s_cbranch_vccnz 14f\n\t" \
  "s_nop 0\n\t" \
  "v_max_i32_dpp v116, v116, v116 quad_perm:[2,3,0,1] row_mask:0xf bank_mask:0xf\n\t" \
  "s_nop 1\n\t" \
  "v_max_i32_dpp v116, v116, v116 row_half_mirror row_mask:0xf bank_mask:0xf\n\t" \
  "s_nop 1\n\t" \
  "v_max_i32_dpp v116, v116, v116 row_mirror row_mask:0xf bank_mask:0xf\n\t" \
  "v_med3_i32 v116, v116, 0, v117\n\t" \
  "v_mul_u32_u24 v116, s52, v116\n\t" \
  "v_lshrrev_b32 v116, 16, v116\n\t" \
  "s_mov_b64 exec, -1\n\t" \
  "v_readfirstlane_b32 s84, v116\n\t" \
  "v_readlane_b32 s85, v116, 16\n\t" \
  "s_max_u32 s84, s84, s85\n\t" \
  "s_add_i32 s84, s84, s51\n\t" \
  ".p2align 6\n\t" \
  "s_nop 0\n\t" \
  "s_nop 0\n\t" \
  "s_nop 0\n\t" \
  "s_nop 0\n\t" \
  "s_nop 0\n\t" \
  "s_nop 0\n\t" \
  "s_nop 0\n\t" \
  "s_nop 0\n\t" \
  "1:\n\t" \
  "s_memtime vcc\n\t" \
  "s_waitcnt lgkmcnt(0)\n\t" \
  "s_sub_i32 s79, s84, vcc_lo\n\t" \
  "s_cmp_gt_i32 s79, 128\n\t" \
  "s_cbranch_scc1 1b\n\t" \
  ".p2align 4\n\t" \
  "2:\n\t" \
  "s_sub_i32 s79, s79, 33\n\t" \
  "s_cmp_gt_i32 s79, 56\n\t" \
  "s_cbranch_scc1 2b\n\t" \
  "buffer_load_dwordx4 v[32:35], v100, %[rh], s40 offen sc1\n\t" \
  "s_nop 12\n\t" \
  "buffer_load_dwordx4 v[36:39], v100, %[rh], s76 offen sc1\n\t" \
  "s_nop 12\n\t" \
  "buffer_load_dwordx4 v[40:43], v100, %[rh], s77 offen sc1\n\t" \
  "s_nop 12\n\t" \
  "buffer_load_dwordx4 v[44:47], v100, %[rh], s78 offen sc1\n\t" \
  "s_memtime vcc\n\t" \
  "s_memrealtime s[82:83]\n\t" \
  "s_nop 4\n\t" \
  "s_memrealtime s[84:85]\n\t" \
  "s_nop 4\n\t" \
  "s_memrealtime s[56:57]\n\t" \
  "s_nop 4\n\t" \
  "s_memrealtime s[86:87]\n\t" \
  "s_nop 4\n\t" \
  "s_memrealtime s[88:89]\n\t" \
  "s_nop 4\n\t" \
  "s_memrealtime s[90:91]\n\t" \
  "s_mov_b32 s92, 0\n\t" \
  "s_waitcnt lgkmcnt(0)\n\t" \
  "s_cmp_eq_u32 s82, s84\n\t" \
  "s_addc_u32 s92, s92, 0\n\t" \
  "s_cmp_eq_u32 s82, s56\n\t" \
  "s_addc_u32 s92, s92, 0\n\t" \
  "s_cmp_eq_u32 s82, s86\n\t" \
  "s_addc_u32 s92, s92, 0\n\t" \
  "s_cmp_eq_u32 s82, s88\n\t" \
  "s_addc_u32 s92, s92, 0\n\t" \
  "s_cmp_eq_u32 s82, s90\n\t" \
  "s_addc_u32 s92, s92, 0\n\t" \
  "s_mul_i32 s92, s92, 24\n\t" \
  "s_add_i32 s92, s92, vcc_lo\n\t" \
  "s_lshl4_add_u32 s93, s82, 64\n\t" \
  "3:\n\t" \
  "v_mov_b64 v[68:69], 0\n\t" \
  "v_mov_b64 v[70:71], 0\n\t" \
  "v_mov_b64 v[72:73], 0\n\t" \
  "v_mov_b64 v[74:75], 0\n\t" \
  "v_mov_b64 v[76:77], 0\n\t" \
  "v_mov_b64 v[78:79], 0\n\t" \
  "v_mov_b64 v[80:81], 0\n\t" \
  "v_mov_b64 v[82:83], 0\n\t" \
  "s_waitcnt lgkmcnt(0)\n\t" \
  "s_waitcnt vmcnt(3)\n\t" \
  "v_smfmac_f32_16x16x64_bf16 v[68:71], v[32:35], a[192:199], v104\n\t" \
  "ds_write_b128 v101, v[32:35] offset:8192\n\t" \
  "v_smfmac_f32_16x16x64_bf16 v[72:75], v[32:35], a[200:207], v104\n\t" \
  "v_smfmac_f32_16x16x64_bf16 v[76:79], v[32:35], v[48:55], v104\n\t" \
  "v_smfmac_f32_16x16x64_bf16 v[80:83], v[32:35], v[56:63], v104\n\t" \
  "s_waitcnt vmcnt(2)\n\t" \
  "v_smfmac_f32_16x16x64_bf16 v[68:71], v[36:39], a[208:215], v104\n\t" \
  "ds_write_b128 v101, v[36:39] offset:9216\n\t" \
  "v_smfmac_f32_16x16x64_bf16 v[72:75], v[36:39], a[216:223], v104\n\t" \
  "v_smfmac_f32_16x16x64_bf16 v[76:79], v[36:39], a[224:231], v104\n\t" \
  "v_cmp_u_f32 vcc, v68, v69\n\t" \
  "v_smfmac_f32_16x16x64_bf16 v[80:83], v[36:39], a[232:239], v104\n\t" \
  "v_cmp_u_f32 s[82:83], v70, v71\n\t" \
  "s_waitcnt vmcnt(1)\n\t" \
  "v_smfmac_f32_16x16x64_bf16 v[68:71], v[40:43], v[16:23], v104\n\t" \
  "ds_write_b128 v101, v[40:43] offset:10240\n\t" \
  "v_smfmac_f32_16x16x64_bf16 v[72:75], v[40:43], v[24:31], v104\n\t" \
  "s_or_b64 vcc, vcc, s[82:83]\n\t" \
  "s_or_b64 vcc, vcc, s[80:81]\n\t" \
  "s_waitcnt lgkmcnt(1)\n\t" \
  "v_smfmac_f32_16x16x64_bf16 v[76:79], v[40:43], a[240:247], v104\n\t" \
  "s_cbranch_scc1 8f\n\t" \
  "5:\n\t" \
  "s_barrier\n\t" \
  "4:\n\t" \
  "s_memtime s[56:57]\n\t" \
  "ds_read_b128 v[48:51], v101 offset:12288\n\t" \
  "v_smfmac_f32_16x16x64_bf16 v[80:83], v[40:43], a[248:255], v104\n\t" \
  "ds_read_b128 v[52:55], v101 offset:13312\n\t" \
  "ds_read_b128 v[56:59], v101 offset:0\n\t" \
  "s_waitcnt vmcnt(0)\n\t" \
  "v_smfmac_f32_16x16x64_bf16 v[68:71], v[44:47], v[120:127], v104\n\t" \
  "v_smfmac_f32_16x16x64_bf16 v[72:75], v[44:47], v[128:135], v104\n\t" \
  "ds_write_b128 v101, v[44:47] offset:11264\n\t" \
  "v_smfmac_f32_16x16x64_bf16 v[76:79], v[44:47], v[136:143], v104\n\t" \
  "v_cmp_u_f32 vcc, v68, v69\n\t" \
  "v_smfmac_f32_16x16x64_bf16 v[80:83], v[44:47], v[144:151], v104\n\t" \
  "v_cmp_u_f32 s[82:83], v70, v71\n\t" \
  "s_waitcnt lgkmcnt(3)\n\t" \
  "v_smfmac_f32_16x16x64_bf16 v[68:71], v[48:51], v[152:159], v104\n\t" \
  "v_smfmac_f32_16x16x64_bf16 v[72:75], v[48:51], v[160:167], v104\n\t" \
  "s_or_b64 vcc, vcc, s[82:83]\n\t" \
  "s_waitcnt lgkmcnt(0)\n\t" \
  "v_smfmac_f32_16x16x64_bf16 v[68:71], v[52:55], v[168:175], v104\n\t" \
  "s_cbranch_scc1 9f\n\t" \
  "6:\n\t" \
  "s_barrier\n\t" \
  "ds_read_b128 v[60:63], v101 offset:1024\n\t" \
  "v_smfmac_f32_16x16x64_bf16 v[72:75], v[52:55], v[176:183], v104\n\t" \
  "ds_read_b128 v[64:67], v101 offset:4096\n\t" \
  "v_smfmac_f32_16x16x64_bf16 v[68:71], v[56:59], a[0:7], v104\n\t" \
  "v_smfmac_f32_16x16x64_bf16 v[72:75], v[56:59], a[8:15], v104\n\t" \
  "ds_read_b128 v[48:51], v101 offset:5120\n\t" \
  "s_waitcnt lgkmcnt(2)\n\t" \
  "v_smfmac_f32_16x16x64_bf16 v[68:71], v[60:63], a[16:23], v104\n\t" \
  "ds_read_b128 v[52:55], v101 offset:14336\n\t" \
  "v_smfmac_f32_16x16x64_bf16 v[72:75], v[60:63], a[24:31], v104\n\t" \
  "ds_read_b128 v[56:59], v101 offset:15360\n\t" \
  "s_waitcnt lgkmcnt(3)\n\t" \
  "v_smfmac_f32_16x16x64_bf16 v[68:71], v[64:67], a[96:103], v104\n\t" \
  "ds_read_b128 v[60:63], v101 offset:2048\n\t" \
  "v_smfmac_f32_16x16x64_bf16 v[72:75], v[64:67], a[104:111], v104\n\t" \
  "s_waitcnt lgkmcnt(3)\n\t" \
  "v_smfmac_f32_16x16x64_bf16 v[68:71], v[48:51], a[112:119], v104\n\t" \
  "ds_read_b128 v[64:67], v101 offset:3072\n\t" \
  "ds_read_b128 v[16:19], v102 offset:49152\n\t" \
  "v_smfmac_f32_16x16x64_bf16 v[72:75], v[48:51], a[120:127], v104\n\t" \
  "s_waitcnt lgkmcnt(4)\n\t" \
  "v_smfmac_f32_16x16x64_bf16 v[68:71], v[52:55], v[84:91], v104\n\t" \
  "ds_read_b128 v[48:51], v101 offset:7168\n\t" \
  "ds_read_b128 v[20:23], v102 offset:50176\n\t" \
  "v_smfmac_f32_16x16x64_bf16 v[72:75], v[52:55], v[92:99], v104\n\t" \
  "s_waitcnt lgkmcnt(5)\n\t" \
  "v_smfmac_f32_16x16x64_bf16 v[68:71], v[56:59], v[216:223], v104\n\t" \
  "ds_read_b128 v[52:55], v101 offset:6144\n\t" \
  "ds_read_b128 v[24:27], v102 offset:51200\n\t" \
  "v_smfmac_f32_16x16x64_bf16 v[72:75], v[56:59], v[224:231], v104\n\t" \
  "ds_read_b128 v[28:31], v102 offset:52224\n\t" \
  "s_waitcnt lgkmcnt(7)\n\t" \
  "v_smfmac_f32_16x16x64_bf16 v[68:71], v[60:63], v[0:7], v104\n\t" \
  "v_smfmac_f32_16x16x64_bf16 v[72:75], v[60:63], v[8:15], v104\n\t" \
  "buffer_load_dwordx4 v[84:87], v107, %[ru], s94 offen offset:0 sc1\n\t" \
  "s_waitcnt lgkmcnt(6)\n\t" \
  "v_smfmac_f32_16x16x64_bf16 v[68:71], v[64:67], a[64:71], v104\n\t" \
  "ds_read_b128 v[0:3], v103 offset:32768\n\t" \
  "v_smfmac_f32_16x16x64_bf16 v[72:75], v[64:67], a[72:79], v104\n\t" \
  "ds_read_b128 v[4:7], v103 offset:33792\n\t" \
  "s_waitcnt lgkmcnt(6)\n\t" \
  "v_smfmac_f32_16x16x64_bf16 v[68:71], v[48:51], a[160:167], v104\n\t" \
  "buffer_load_dwordx4 v[88:91], v107, %[ru], s94 offen offset:1024 sc1\n\t" \
  "s_waitcnt lgkmcnt(4)\n\t" \
  "v_smfmac_f32_16x16x64_bf16 v[68:71], v[52:55], v[16:23], v104\n\t" \
  "ds_read_b128 v[8:11], v103 offset:34816\n\t" \
  "v_smfmac_f32_16x16x64_bf16 v[72:75], v[48:51], a[168:175], v104\n\t" \
  "ds_read_b128 v[12:15], v103 offset:35840\n\t" \
  "s_waitcnt lgkmcnt(4)\n\t" \
  "v_smfmac_f32_16x16x64_bf16 v[72:75], v[52:55], v[24:31], v104\n\t" \
  "v_add_f32 v44, v68, v70\n\t" \
  "v_smfmac_f32_16x16x64_bf16 v[76:79], v[56:59], v[232:239], v104\n\t" \
  "v_add_f32 v45, v69, v71\n\t" \
  "v_cvt_pk_bf16_f32 v44, v44, v45\n\t" \
  "v_smfmac_f32_16x16x64_bf16 v[80:83], v[56:59], v[240:247], v104\n\t" \
  "v_add_f32 v46, v72, v74\n\t" \
  "v_add_f32 v47, v73, v75\n\t" \
  "v_smfmac_f32_16x16x64_bf16 v[76:79], v[60:63], a[48:55], v104\n\t" \
  "v_cvt_pk_bf16_f32 v46, v46, v47\n\t" \
  "ds_write2st64_b32 v105, v44, v46 offset1:1\n\t" \
  "v_smfmac_f32_16x16x64_bf16 v[80:83], v[60:63], a[56:63], v104\n\t" \
  "ds_read_b128 v[56:59], v101 offset:12288\n\t" \
  "v_smfmac_f32_16x16x64_bf16 v[76:79], v[64:67], a[80:87], v104\n\t" \
  "ds_read_b128 v[16:19], v102 offset:0\n\t" \
  "v_smfmac_f32_16x16x64_bf16 v[80:83], v[64:67], a[88:95], v104\n\t" \
  "ds_read_b128 v[20:23], v102 offset:1024\n\t" \
  "v_smfmac_f32_16x16x64_bf16 v[76:79], v[48:51], a[176:183], v104\n\t" \
  "s_waitcnt lgkmcnt(3)\n\t" \
  "s_barrier\n\t" \
  "v_smfmac_f32_16x16x64_bf16 v[80:83], v[48:51], a[184:191], v104\n\t" \
  "buffer_load_dwordx4 v[92:95], v107, %[ru], s94 offen offset:2048 sc1\n\t" \
  "v_smfmac_f32_16x16x64_bf16 v[76:79], v[52:55], a[144:151], v104\n\t" \
  "ds_read_b128 v[60:63], v101 offset:5120\n\t" \
  "ds_read_b128 v[24:27], v102 offset:2048\n\t" \
  "v_smfmac_f32_16x16x64_bf16 v[80:83], v[52:55], a[152:159], v104\n\t" \
  "ds_read_b128 v[64:67], v101 offset:14336\n\t" \
  "buffer_load_dwordx4 v[96:99], v107, %[ru], s94 offen offset:3072 sc1\n\t" \
  "s_waitcnt lgkmcnt(5)\n\t" \
  "v_smfmac_f32_16x16x64_bf16 v[76:79], v[56:59], v[0:7], v104\n\t" \
  "ds_read_b128 v[48:51], v101 offset:0\n\t" \
  "ds_read_b128 v[28:31], v102 offset:3072\n\t" \
  "v_smfmac_f32_16x16x64_bf16 v[80:83], v[56:59], v[8:15], v104\n\t" \
  "global_load_dword v119, v109, %[kc] sc1\n\t" \
  "s_waitcnt lgkmcnt(4)\n\t" \
  "v_smfmac_f32_16x16x64_bf16 v[76:79], v[60:63], a[128:135], v104\n\t" \
  "ds_read_b128 v[52:55], v101 offset:13312\n\t" \
  "v_smfmac_f32_16x16x64_bf16 v[80:83], v[60:63], a[136:143], v104\n\t" \
  "ds_read_b128 v[0:3], v102 offset:32768\n\t" \
  "buffer_load_dwordx4 v[32:35], v100, %[ru], s47 offen offset:0 sc1\n\t" \
  "s_waitcnt lgkmcnt(4)\n\t" \
  "v_smfmac_f32_16x16x64_bf16 v[76:79], v[64:67], v[200:207], v104\n\t" \
  "ds_read_b128 v[56:59], v101 offset:1024\n\t" \
  "v_smfmac_f32_16x16x64_bf16 v[80:83], v[64:67], v[208:215], v104\n\t" \
  "ds_read_b128 v[60:63], v101 offset:4096\n\t" \
  "ds_read_b128 v[4:7], v102 offset:33792\n\t" \
  "s_waitcnt lgkmcnt(6)\n\t" \
  "v_smfmac_f32_16x16x64_bf16 v[76:79], v[48:51], v[16:23], v104\n\t" \
  "ds_read_b128 v[8:11], v102 offset:34816\n\t" \
  "buffer_load_dwordx4 v[36:39], v100, %[ru], s47 offen offset:1024 sc1\n\t" \
  "s_waitcnt lgkmcnt(6)\n\t" \
  "v_smfmac_f32_16x16x64_bf16 v[80:83], v[48:51], v[24:31], v104\n\t" \
  "ds_read_b128 v[12:15], v102 offset:35840\n\t" \
  "s_waitcnt lgkmcnt(6)\n\t" \
  "v_smfmac_f32_16x16x64_bf16 v[76:79], v[52:55], v[184:191], v104\n\t" \
  "v_smfmac_f32_16x16x64_bf16 v[80:83], v[52:55], v[192:199], v104\n\t" \
  "buffer_load_dwordx4 v[40:43], v100, %[ru], s47 offen offset:2048 sc1\n\t" \
  "s_waitcnt lgkmcnt(4)\n\t" \
  "v_smfmac_f32_16x16x64_bf16 v[76:79], v[56:59], a[32:39], v104\n\t" \
  "s_waitcnt lgkmcnt(2)\n\t" \
  "v_smfmac_f32_16x16x64_bf16 v[76:79], v[60:63], v[0:7], v104\n\t" \
  "v_smfmac_f32_16x16x64_bf16 v[80:83], v[56:59], a[40:47], v104\n\t" \
  "s_waitcnt lgkmcnt(0)\n\t" \
  "v_smfmac_f32_16x16x64_bf16 v[80:83], v[60:63], v[8:15], v104\n\t" \
  "v_add_f32 v20, v76, v78\n\t" \
  "v_add_f32 v21, v77, v79\n\t" \
  "v_cvt_pk_bf16_f32 v20, v20, v21\n\t" \
  "ds_write_b32 v106, v20\n\t" \
  "s_nop 3\n\t" \
  "v_add_f32 v23, v80, v82\n\t" \
  "v_add_f32 v21, v81, v83\n\t" \
  "v_cvt_pk_bf16_f32 v21, v23, v21\n\t" \
  "ds_write_b32 v106, v21 offset:256\n\t" \
  "s_waitcnt lgkmcnt(0)\n\t" \
  "s_barrier\n\t" \
  "v_bfe_i32 v64, v114, 0, 1\n\t" \
  "v_and_b32 v64, 0xff800000, v64\n\t" \
  "v_bfe_i32 v65, v114, 1, 1\n\t" \
  "v_and_b32 v65, 0xff800000, v65\n\t" \
  "v_bfe_i32 v66, v114, 2, 1\n\t" \
  "v_and_b32 v66, 0xff800000, v66\n\t" \
  "v_bfe_i32 v67, v114, 3, 1\n\t" \
  "v_and_b32 v67, 0xff800000, v67\n\t" \
  "v_bfe_i32 v68, v114, 4, 1\n\t" \
  "v_and_b32 v68, 0xff800000, v68\n\t" \
  "v_bfe_i32 v69, v114, 5, 1\n\t" \
  "v_and_b32 v69, 0xff800000, v69\n\t" \
  "v_bfe_i32 v70, v114, 6, 1\n\t" \
  "v_and_b32 v70, 0xff800000, v70\n\t" \
  "v_bfe_i32 v71, v114, 7, 1\n\t" \
  "v_and_b32 v71, 0xff800000, v71\n\t" \
  "s_lshl_b32 s94, s45, 4\n\t" \
  "s_and_b32 s96, s44, 2\n\t" \
  "s_lshl_b32 s96, s96, 13\n\t" \
  "s_and_b32 s76, s44, 1\n\t" \
  "s_lshl_b32 s76, s76, 12\n\t" \
  "s_add_i32 s96, s96, s76\n\t" \
  "s_mov_b32 s95, 1\n\t" \
  "s_add_i32 s41, s56, 1950\n\t" \
  "s_sub_i32 s76, s92, s51\n\t" \
  "s_sub_i32 s77, s93, s53\n\t" \
  "s_cmp_eq_u32 s76, 0\n\t" \
  "s_cbranch_scc1 21f\n\t" \
  "v_cvt_f32_u32 v117, s76\n\t" \
  "v_cvt_f32_u32 v116, s77\n\t" \
  "v_rcp_f32 v117, v117\n\t" \
  "s_mov_b32 s51, s92\n\t" \
  "v_mul_f32 v117, v117, v116\n\t" \
  "v_max_f32 v117, 0x3f028f5c, v117\n\t" \
  "v_min_f32 v117, 0x3ffeb852, v117\n\t" \
  "v_rcp_f32 v116, v117\n\t" \
  "v_mul_f32 v117, 0x4f000000, v117\n\t" \
  "v_mul_f32 v116, 0x47800000, v116\n\t" \
  "v_cvt_u32_f32 v117, v117\n\t" \
  "v_cvt_u32_f32 v116, v116\n\t" \
  "s_mov_b32 s53, s93\n\t" \
  "v_readfirstlane_b32 s46, v117\n\t" \
  "v_readfirstlane_b32 s52, v116\n\t" \
  "21:\n\t" \
  "ds_read_b128 v[0:3], v101 offset:20480\n\t" \
  "ds_read_b128 v[4:7], v101 offset:21504\n\t" \
  "ds_read_b128 v[8:11], v101 offset:22528\n\t" \
  "ds_read_b128 v[12:15], v101 offset:23552\n\t" \
  "ds_read_b128 v[16:19], v101 offset:24576\n\t" \
  "ds_read_b128 v[20:23], v101 offset:25600\n\t" \
  "ds_read_b128 v[24:27], v101 offset:26624\n\t" \
  "ds_read_b128 v[28:31], v101 offset:27648\n\t" \
  "ds_read_b64_tr_b16 v[48:49], v108 offset:0\n\t" \
  "ds_read_b64_tr_b16 v[50:51], v108 offset:64\n\t" \
  "ds_read_b64_tr_b16 v[52:53], v108 offset:512\n\t" \
  "ds_read_b64_tr_b16 v[54:55], v108 offset:576\n\t" \
  "ds_read_b64_tr_b16 v[56:57], v108 offset:1024\n\t" \
  "ds_read_b64_tr_b16 v[58:59], v108 offset:1088\n\t" \
  "ds_read_b64_tr_b16 v[60:61], v108 offset:1536\n\t" \
  "ds_read_b64_tr_b16 v[62:63], v108 offset:1600\n\t" \
  "ds_read_b64_tr_b16 v[76:77], v108 offset:12288\n\t" \
  "ds_read_b64_tr_b16 v[78:79], v108 offset:12352\n\t" \
  "s_waitcnt vmcnt(4) lgkmcnt(10)\n\t" \
  "v_mfma_f32_16x16x32_bf16 v[64:67], v[0:3], v[84:87], v[64:67]\n\t" \
  "v_mfma_f32_16x16x32_bf16 v[68:71], v[16:19], v[84:87], v[68:71]\n\t" \
  "v_mfma_f32_16x16x32_bf16 v[64:67], v[4:7], v[88:91], v[64:67]\n\t" \
  "v_mfma_f32_16x16x32_bf16 v[68:71], v[20:23], v[88:91], v[68:71]\n\t" \
  "v_mfma_f32_16x16x32_bf16 v[64:67], v[8:11], v[92:95], v[64:67]\n\t" \
  "v_mfma_f32_16x16x32_bf16 v[68:71], v[24:27], v[92:95], v[68:71]\n\t" \
  "v_mfma_f32_16x16x32_bf16 v[64:67], v[12:15], v[96:99], v[64:67]\n\t" \
  "v_mfma_f32_16x16x32_bf16 v[68:71], v[28:31], v[96:99], v[68:71]\n\t" \
  "s_waitcnt lgkmcnt(0)\n\t" \
  "s_barrier\n\t" \
  "v_mfma_f32_16x16x32_bf16 v[72:75], v[48:51], v[84:87], 0\n\t" \
  "v_mfma_f32_16x16x32_bf16 v[72:75], v[52:55], v[88:91], v[72:75]\n\t" \
  "v_mfma_f32_16x16x32_bf16 v[72:75], v[56:59], v[92:95], v[72:75]\n\t" \
  "v_mfma_f32_16x16x32_bf16 v[72:75], v[60:63], v[96:99], v[72:75]\n\t" \
  "v_mfma_f32_16x16x32_bf16 v[72:75], v[76:79], v[84:87], v[72:75]\n\t" \
  "s_waitcnt vmcnt(0)\n\t" \
  "ds_write_b128 v101, v[32:35] offset:22528\n\t" \
  "ds_write_b128 v101, v[36:39] offset:23552\n\t" \
  "ds_write_b128 v101, v[40:43] offset:24576\n\t" \
  "ds_read_b128 v[48:51], v103 offset:0\n\t" \
  "ds_read_b128 v[52:55], v103 offset:1024\n\t" \
  "ds_read_b128 v[56:59], v103 offset:2048\n\t" \
  "ds_read_b128 v[60:63], v103 offset:3072\n\t" \
  "ds_read_b128 v[16:19], v103 offset:16384\n\t" \
  "ds_read_b128 v[20:23], v103 offset:17408\n\t" \
  "ds_read_b128 v[24:27], v103 offset:18432\n\t" \
  "ds_read_b128 v[28:31], v103 offset:19456\n\t" \
  "ds_read_b128 v[84:87], v103 offset:49152\n\t" \
  "ds_read_b128 v[88:91], v103 offset:50176\n\t" \
  "ds_read_b128 v[92:95], v103 offset:51200\n\t" \
  "ds_read_b128 v[96:99], v103 offset:52224\n\t" \
  "ds_read_b128 v[0:3], v102 offset:16384\n\t" \
  "ds_read_b128 v[4:7], v102 offset:17408\n\t" \
  "ds_read_b128 v[8:11], v102 offset:18432\n\t" \
  "ds_read_b128 v[12:15], v102 offset:19456\n\t" \
  "v_max3_f32 v76, v64, v65, v66\n\t" \
  "v_max3_f32 v77, v67, v68, v69\n\t" \
  "v_max3_f32 v76, v76, v70, v71\n\t" \
  "v_max3_f32 v77, v77, v72, v73\n\t" \
  "v_max3_f32 v76, v76, v74, v75\n\t" \
  "v_max_f32 v76, v76, v77\n\t" \
  "v_mov_b32 v77, v76\n\t" \
  "s_nop 1\n\t" \
  "v_permlane16_swap_b32 v76, v77\n\t" \
  "v_max_f32 v76, v76, v77\n\t" \
  "v_mov_b32 v77, v76\n\t" \
  "s_nop 1\n\t" \
  "v_permlane32_swap_b32 v76, v77\n\t" \
  "v_max_f32 v76, v76, v77\n\t" \
  "v_mul_f32 v76, %[cs], v76\n\t" \
  "v_fma_f32 v64, v64, %[cs], -v76\n\t" \
  "v_fma_f32 v65, v65, %[cs], -v76\n\t" \
  "v_fma_f32 v66, v66, %[cs], -v76\n\t" \
  "v_fma_f32 v67, v67, %[cs], -v76\n\t" \
  "v_fma_f32 v68, v68, %[cs], -v76\n\t" \
  "v_fma_f32 v69, v69, %[cs], -v76\n\t" \
  "v_fma_f32 v70, v70, %[cs], -v76\n\t" \
  "v_fma_f32 v71, v71, %[cs], -v76\n\t" \
  "v_fma_f32 v72, v72, %[cs], -v76\n\t" \
  "v_fma_f32 v73, v73, %[cs], -v76\n\t" \
  "v_fma_f32 v74, v74, %[cs], -v76\n\t" \
  "v_fma_f32 v75, v75, %[cs], -v76\n\t" \
  "v_exp_f32 v64, v64\n\t" \
  "v_exp_f32 v65, v65\n\t" \
  "v_exp_f32 v66, v66\n\t" \
  "v_exp_f32 v67, v67\n\t" \
  "v_exp_f32 v68, v68\n\t" \
  "v_exp_f32 v69, v69\n\t" \
  "v_exp_f32 v70, v70\n\t" \
  "v_exp_f32 v71, v71\n\t" \
  "v_exp_f32 v72, v72\n\t" \
  "v_exp_f32 v73, v73\n\t" \
  "v_exp_f32 v74, v74\n\t" \
  "v_exp_f32 v75, v75\n\t" \
  "v_add_f32 v76, v72, v73\n\t" \
  "v_add_f32 v77, v74, v75\n\t" \
  "v_add_f32 v78, v64, v65\n\t" \
  "v_add_f32 v79, v66, v67\n\t" \
  "v_add_f32 v76, v76, v77\n\t" \
  "v_add_f32 v78, v78, v68\n\t" \
  "v_add_f32 v79, v79, v69\n\t" \
  "v_add_f32 v78, v78, v70\n\t" \
  "v_add_f32 v79, v79, v71\n\t" \
  "v_fma_f32 v76, v76, 0.5, v78\n\t" \
  "v_add_f32 v76, v76, v79\n\t" \
  "v_mov_b32 v77, v76\n\t" \
  "s_nop 1\n\t" \
  "v_permlane16_swap_b32 v76, v77\n\t" \
  "v_add_f32 v76, v76, v77\n\t" \
  "v_mov_b32 v77, v76\n\t" \
  "s_nop 1\n\t" \
  "v_permlane32_swap_b32 v76, v77\n\t" \
  "v_add_f32 v76, v76, v77\n\t" \
  "v_rcp_f32 v76, v76\n\t" \
  "s_mov_b32 exec_hi, 0\n\t" \
  "buffer_load_dword v118, v112, %[rh], s96 offen sc1\n\t" \
  "s_mov_b64 exec, -1\n\t" \
  "v_mul_f32 v64, v64, v76\n\t" \
  "v_mul_f32 v65, v65, v76\n\t" \
  "v_mul_f32 v66, v66, v76\n\t" \
  "v_mul_f32 v67, v67, v76\n\t" \
  "v_mul_f32 v68, v68, v76\n\t" \
  "v_mul_f32 v69, v69, v76\n\t" \
  "v_mul_f32 v70, v70, v76\n\t" \
  "v_mul_f32 v71, v71, v76\n\t" \
  "v_mul_f32 v72, v72, v76\n\t" \
  "v_mul_f32 v73, v73, v76\n\t" \
  "v_mul_f32 v74, v74, v76\n\t" \
  "v_mul_f32 v75, v75, v76\n\t" \
  "v_cvt_pk_bf16_f32 v76, v64, v68\n\t" \
  "v_cvt_pk_bf16_f32 v77, v65, v69\n\t" \
  "v_cvt_pk_bf16_f32 v78, v66, v70\n\t" \
  "v_cvt_pk_bf16_f32 v79, v67, v71\n\t" \
  "v_cvt_pk_bf16_f32 v80, v72, v73\n\t" \
  "v_cvt_pk_bf16_f32 v81, v74, v75\n\t" \
  "v_permlane32_swap_b32 v76, v78\n\t" \
  "v_permlane32_swap_b32 v77, v79\n\t" \
  "v_mov_b64 v[82:83], v[80:81]\n\t" \
  "buffer_store_dwordx4 v[76:79], v110, %[rh], s94 offen sc1\n\t" \
  "s_nop 0\n\t" \
  "v_permlane16_swap_b32 v80, v82\n\t" \
  "v_permlane16_swap_b32 v81, v83\n\t" \
  "buffer_store_dwordx4 v[80:83], v111, %[rh], s94 offen sc1\n\t" \
  "s_add_i32 s77, s44, 1\n\t" \
  "s_min_u32 s77, s77, 63\n\t" \
  "v_cmp_gt_i32 vcc, s77, v119\n\t" \
  "s_cmp_lg_u64 vcc, 0\n\t" \
  "s_cselect_b32 s42, 32, 0\n\t" \
  "s_add_i32 s44, s44, 1\n\t" \
  "s_and_b32 s58, s58, 4\n\t" \
  "s_and_b32 s76, s44, 64\n\t" \
  "s_or_b32 s42, s42, s76\n\t" \
  "s_cbranch_scc0 20b\n\t" \
  "s_branch 99f\n\t" \
  "14:\n\t" \
  "s_sleep 3\n\t" \
  "s_add_u32 s59, s59, 0x10000\n\t" \
  "s_cmp_lt_u32 s59, 0x100000\n\t" \
  "s_cbranch_scc1 13b\n\t" \
  "s_mov_b64 exec, -1\n\t" \
  "s_mov_b32 s42, 16\n\t" \
  "s_branch 99f\n\t" \
  "8:\n\t" \
  "s_bitcmp1_b32 s58, 1\n\t" \
  "s_cbranch_scc1 4b\n\t" \
  "s_bitcmp1_b32 s58, 2\n\t" \
  "s_cbranch_scc1 5b\n\t" \
  "v_max3_u32 v116, v32, v33, v34\n\t" \
  "v_max3_u32 v117, v35, v36, v37\n\t" \
  "v_max3_u32 v116, v116, v38, v39\n\t" \
  "v_max_u32 v116, v116, v117\n\t" \
  "v_cmp_eq_u32 vcc, -1, v116\n\t" \
  "s_cbranch_vccz 5b\n\t" \
  "s_branch 0f\n\t" \
  "9:\n\t" \
  "s_bitcmp1_b32 s58, 2\n\t" \
  "s_cbranch_scc1 6b\n\t" \
  "v_max3_u32 v116, v40, v41, v42\n\t" \
  "v_max3_u32 v117, v43, v44, v45\n\t" \
  "v_max3_u32 v116, v116, v46, v47\n\t" \
  "v_max_u32 v116, v116, v117\n\t" \
  "v_cmp_eq_u32 vcc, -1, v116\n\t" \
  "s_cbranch_vccz 6b\n\t" \
  "s_or_b32 s58, s58, 2\n\t" \
  "0:\n\t" \
  "s_or_b32 s58, s58, 1\n\t" \
  "11:\n\t" \
  "buffer_load_dwordx4 v[32:35], v100, %[rh], s40 offen sc1\n\t" \
  "buffer_load_dwordx4 v[36:39], v100, %[rh], s76 offen sc1\n\t" \
  "buffer_load_dwordx4 v[40:43], v100, %[rh], s77 offen sc1\n\t" \
  "buffer_load_dwordx4 v[44:47], v100, %[rh], s78 offen sc1\n\t" \
  "s_waitcnt lgkmcnt(0)\n\t" \
  "s_branch 10b\n\t" \
  "17:\n\t" \
  "s_add_u32 s59, s59, 1\n\t" \
  "s_bitcmp1_b32 s58, 2\n\t" \
  "s_cbranch_scc1 3b\n\t" \
  "s_waitcnt vmcnt(0)\n\t" \
  "v_max_u32 v116, v32, v33\n\t" \
  "v_max3_u32 v116, v116, v34, v35\n\t" \
  "v_max3_u32 v116, v116, v36, v37\n\t" \
  "v_max3_u32 v116, v116, v38, v39\n\t" \
  "v_max3_u32 v116, v116, v40, v41\n\t" \
  "v_max3_u32 v116, v116, v42, v43\n\t" \
  "v_max3_u32 v116, v116, v44, v45\n\t" \
  "v_max3_u32 v116, v116, v46, v47\n\t" \
  "v_cmp_eq_u32 vcc, -1, v116\n\t" \
  "s_cbranch_vccz 3b\n\t" \
  "s_bitcmp1_b32 s59, 6\n\t" \
  "s_cbranch_scc1 12f\n\t" \
  "buffer_load_dwordx4 v[32:35], v100, %[rh], s40 offen sc1\n\t" \
  "buffer_load_dwordx4 v[36:39], v100, %[rh], s76 offen sc1\n\t" \
  "buffer_load_dwordx4 v[40:43], v100, %[rh], s77 offen sc1\n\t" \
  "buffer_load_dwordx4 v[44:47], v100, %[rh], s78 offen sc1\n\t" \
  "s_branch 17b\n\t" \
  "12:\n\t" \
  "s_mov_b32 s42, s58\n\t" \
  "s_branch 99f\n\t" \
  "93:\n\t" \
  "s_mov_b32 s58, s42\n\t" \
  "20:\n\t" \
  "s_and_b32 s76, s44, 7\n\t" \
  "s_lshl_b32 s76, s76, 15\n\t" \
  "s_add_i32 s40, s54, s76\n\t" \
  "s_and_b32 s76, s44, 3\n\t" \
  "s_lshl_b32 s45, s76, 14\n\t" \
  "s_add_i32 s47, s44, 1\n\t" \
  "s_min_u32 s47, s47, 63\n\t" \
  "s_lshr_b32 s76, s47, 1\n\t" \
  "s_lshl_b32 s47, s47, 20\n\t" \
  "s_add_i32 s47, s47, s55\n\t" \
  "s_bitcmp1_b64 %[sel], s76\n\t" \
  "s_cselect_b32 s76, 0x2000, 0\n\t" \
  "s_add_i32 s47, s47, s76\n\t" \
  "s_add_i32 s49, s44, -1\n\t" \
  "s_and_b32 s48, s49, 2\n\t" \
  "s_lshl_b32 s48, s48, 13\n\t" \
  "s_and_b32 s76, s49, 1\n\t" \
  "s_lshl_b32 s76, s76, 12\n\t" \
  "s_add_i32 s48, s48, s76\n\t" \
  "s_lshl_b32 s50, s53, 8\n\t" \
  "s_mov_b32 s59, 0\n\t" \
  "s_mov_b32 s92, s51\n\t" \
  "s_mov_b32 s93, s53\n\t" \
  "s_xor_b32 s76, s40, 0x400\n\t" \
  "s_xor_b32 s77, s40, 0x800\n\t" \
  "s_xor_b32 s78, s40, 0xc00\n\t" \
  "s_cmp_lg_u32 s58, 0\n\t" \
  "s_cbranch_scc1 11f\n\t" \
  "10:\n\t" \
  "ds_read_b128 v[48:51], v103 offset:32768\n\t" \
  "ds_read_b128 v[52:55], v103 offset:33792\n\t" \
  "ds_read_b128 v[56:59], v103 offset:34816\n\t" \
  "ds_read_b128 v[60:63], v103 offset:35840\n\t" \
  "ds_read_b128 v[16:19], v103 offset:49152\n\t" \
  "ds_read_b128 v[20:23], v103 offset:50176\n\t" \
  "ds_read_b128 v[24:27], v103 offset:51200\n\t" \
  "ds_read_b128 v[28:31], v103 offset:52224\n\t" \
  "ds_read_b128 v[84:87], v102 offset:16384\n\t" \
  "ds_read_b128 v[88:91], v102 offset:17408\n\t" \
  "ds_read_b128 v[92:95], v102 offset:18432\n\t" \
  "ds_read_b128 v[96:99], v102 offset:19456\n\t" \
  "s_waitcnt lgkmcnt(4)\n\t" \
  "ds_read_b128 v[0:3], v102 offset:49152\n\t" \
  "ds_read_b128 v[4:7], v102 offset:50176\n\t" \
  "ds_read_b128 v[8:11], v102 offset:51200\n\t" \
  "ds_read_b128 v[12:15], v102 offset:52224\n\t" \
  "46:\n\t" \
  "s_bitcmp1_b32 s58, 1\n\t" \
  "s_cselect_b64 s[80:81], -1, 0\n\t" \
  "s_cmp_lg_u32 s58, 0\n\t" \
  "s_cbranch_scc1 17f\n\t" \
  "s_mov_b32 exec_hi, 0\n\t" \
  "16:\n\t" \
  "s_memtime vcc\n\t" \
  "s_waitcnt lgkmcnt(0)\n\t" \
  "s_sub_i32 s79, s41, vcc_lo\n\t" \
  "s_cmp_gt_i32 s79, 64\n\t" \
  "s_cbranch_scc1 16b\n\t" \
  "13:\n\t" \
  "buffer_load_dword v116, v112, %[rh], s48 offen sc1\n\t" \
  "s_waitcnt vmcnt(0)\n\t" \
  "15:\n\t" \
  "v_mov_b32 v117, 20000\n\t" \
  "v_cmp_ne_u32_sdwa vcc, v116, s49 src0_sel:BYTE_0 src1_sel:DWORD\n\t" \
  "v_subrev_u32 v116, s50, v116\n\t" \
  "v_ashrrev_i32 v116, 8, v116\n\t" \
  "s_nop 1\n\t" \
  "v_max_i32_dpp v116, v116, v116 quad_perm:[1,0,3,2] row_mask:0xf bank_mask:0xf\n\t" \
  "s_cbranch_vccnz 14f\n\t" \
  "s_nop 0\n\t" \
  "v_max_i32_dpp v116, v116, v116 quad_perm:[2,3,0,1] row_mask:0xf bank_mask:0xf\n\t" \
  "s_nop 1\n\t" \
  "v_max_i32_dpp v116, v116, v116 row_half_mirror row_mask:0xf bank_mask:0xf\n\t" \
  "s_nop 1\n\t" \
  "v_max_i32_dpp v116, v116, v116 row_mirror row_mask:0xf bank_mask:0xf\n\t" \
  "v_med3_i32 v116, v116, 0, v117\n\t" \
  "v_mul_u32_u24 v116, s52, v116\n\t" \
  "v_lshrrev_b32 v116, 16, v116\n\t" \
  "s_mov_b64 exec, -1\n\t" \
  "v_readfirstlane_b32 s84, v116\n\t" \
  "v_readlane_b32 s85, v116, 16\n\t" \
  "s_max_u32 s84, s84, s85\n\t" \
  "s_add_i32 s84, s84, s51\n\t" \
  ".p2align 6\n\t" \
  "s_nop 0\n\t" \
  "s_nop 0\n\t" \
  "s_nop 0\n\t" \
  "s_nop 0\n\t" \
  "s_nop 0\n\t" \
  "s_nop 0\n\t" \
  "s_nop 0\n\t" \
  "s_nop 0\n\t" \
  "1:\n\t" \
  "s_memtime vcc\n\t" \
  "s_waitcnt lgkmcnt(0)\n\t" \
  "s_sub_i32 s79, s84, vcc_lo\n\t" \
  "s_cmp_gt_i32 s79, 128\n\t" \
  "s_cbranch_scc1 1b\n\t" \
  ".p2align 4\n\t" \
  "2:\n\t" \
  "s_sub_i32 s79, s79, 33\n\t" \
  "s_cmp_gt_i32 s79, 56\n\t" \
  "s_cbranch_scc1 2b\n\t" \
  "buffer_load_dwordx4 v[32:35], v100, %[rh], s40 offen sc1\n\t" \
  "s_nop 12\n\t" \
  "buffer_load_dwordx4 v[36:39], v100, %[rh], s76 offen sc1\n\t" \
  "s_nop 12\n\t" \
  "buffer_load_dwordx4 v[40:43], v100, %[rh], s77 offen sc1\n\t" \
  "s_nop 12\n\t" \
  "buffer_load_dwordx4 v[44:47], v100, %[rh], s78 offen sc1\n\t" \
  "s_memtime vcc\n\t" \
  "s_memrealtime s[82:83]\n\t" \
  "s_nop 4\n\t" \
  "s_memrealtime s[84:85]\n\t" \
  "s_nop 4\n\t" \
  "s_memrealtime s[56:57]\n\t" \
  "s_nop 4\n\t" \
  "s_memrealtime s[86:87]\n\t" \
  "s_nop 4\n\t" \
  "s_memrealtime s[88:89]\n\t" \
  "s_nop 4\n\t" \
  "s_memrealtime s[90:91]\n\t" \
  "s_mov_b32 s92, 0\n\t" \
  "s_waitcnt lgkmcnt(0)\n\t" \
  "s_cmp_eq_u32 s82, s84\n\t" \
  "s_addc_u32 s92, s92, 0\n\t" \
  "s_cmp_eq_u32 s82, s56\n\t" \
  "s_addc_u32 s92, s92, 0\n\t" \
  "s_cmp_eq_u32 s82, s86\n\t" \
  "s_addc_u32 s92, s92, 0\n\t" \
  "s_cmp_eq_u32 s82, s88\n\t" \
  "s_addc_u32 s92, s92, 0\n\t" \
  "s_cmp_eq_u32 s82, s90\n\t" \
  "s_addc_u32 s92, s92, 0\n\t" \
  "s_mul_i32 s92, s92, 24\n\t" \
  "s_add_i32 s92, s92, vcc_lo\n\t" \
  "s_lshl4_add_u32 s93, s82, 64\n\t" \
  "3:\n\t" \
  "v_mov_b64 v[68:69], 0\n\t" \
  "v_mov_b64 v[70:71], 0\n\t" \
  "v_mov_b64 v[72:73], 0\n\t" \
  "v_mov_b64 v[74:75], 0\n\t" \
  "v_mov_b64 v[76:77], 0\n\t" \
  "v_mov_b64 v[78:79], 0\n\t" \
  "v_mov_b64 v[80:81], 0\n\t" \
  "v_mov_b64 v[82:83], 0\n\t" \
  "s_waitcnt lgkmcnt(0)\n\t" \
  "s_waitcnt vmcnt(3)\n\t" \
  "v_smfmac_f32_16x16x64_bf16 v[68:71], v[32:35], v[152:159], v104\n\t" \
  "ds_write_b128 v101, v[32:35] offset:12288\n\t" \
  "v_smfmac_f32_16x16x64_bf16 v[72:75], v[32:35], v[160:167], v104\n\t" \
  "v_smfmac_f32_16x16x64_bf16 v[76:79], v[32:35], v[48:55], v104\n\t" \
  "v_smfmac_f32_16x16x64_bf16 v[80:83], v[32:35], v[56:63], v104\n\t" \
  "s_waitcnt vmcnt(2)\n\t" \
  "v_smfmac_f32_16x16x64_bf16 v[68:71], v[36:39], v[168:175], v104\n\t" \
  "ds_write_b128 v101, v[36:39] offset:13312\n\t" \
  "v_smfmac_f32_16x16x64_bf16 v[72:75], v[36:39], v[176:183], v104\n\t" \
  "v_smfmac_f32_16x16x64_bf16 v[76:79], v[36:39], v[184:191], v104\n\t" \
  "v_cmp_u_f32 vcc, v68, v69\n\t" \
  "v_smfmac_f32_16x16x64_bf16 v[80:83], v[36:39], v[192:199], v104\n\t" \
  "v_cmp_u_f32 s[82:83], v70, v71\n\t" \
  "s_waitcnt vmcnt(1)\n\t" \
  "v_smfmac_f32_16x16x64_bf16 v[68:71], v[40:43], v[16:23], v104\n\t" \
  "ds_write_b128 v101, v[40:43] offset:14336\n\t" \
  "v_smfmac_f32_16x16x64_bf16 v[72:75], v[40:43], v[24:31], v104\n\t" \
  "s_or_b64 vcc, vcc, s[82:83]\n\t" \
  "s_or_b64 vcc, vcc, s[80:81]\n\t" \
  "s_waitcnt lgkmcnt(1)\n\t" \
  "v_smfmac_f32_16x16x64_bf16 v[76:79], v[40:43], v[200:207], v104\n\t" \
  "s_cbranch_scc1 8f\n\t" \
  "5:\n\t" \
  "s_barrier\n\t" \
  "4:\n\t" \
  "s_memtime s[56:57]\n\t" \
  "ds_read_b128 v[48:51], v101 offset:0\n\t" \
  "v_smfmac_f32_16x16x64_bf16 v[80:83], v[40:43], v[208:215], v104\n\t" \
  "ds_read_b128 v[52:55], v101 offset:1024\n\t" \
  "ds_read_b128 v[56:59], v101 offset:4096\n\t" \
  "s_waitcnt vmcnt(0)\n\t" \
  "v_smfmac_f32_16x16x64_bf16 v[68:71], v[44:47], v[216:223], v104\n\t" \
  "v_smfmac_f32_16x16x64_bf16 v[72:75], v[44:47], v[224:231], v104\n\t" \
  "ds_write_b128 v101, v[44:47] offset:15360\n\t" \
  "v_smfmac_f32_16x16x64_bf16 v[76:79], v[44:47], v[232:239], v104\n\t" \
  "v_cmp_u_f32 vcc, v68, v69\n\t" \
  "v_smfmac_f32_16x16x64_bf16 v[80:83], v[44:47], v[240:247], v104\n\t" \
  "v_cmp_u_f32 s[82:83], v70, v71\n\t" \
  "s_waitcnt lgkmcnt(3)\n\t" \
  "v_smfmac_f32_16x16x64_bf16 v[68:71], v[48:51], a[0:7], v104\n\t" \
  "v_smfmac_f32_16x16x64_bf16 v[72:75], v[48:51], a[8:15], v104\n\t" \
  "s_or_b64 vcc, vcc, s[82:83]\n\t" \
  "s_waitcnt lgkmcnt(0)\n\t" \
  "v_smfmac_f32_16x16x64_bf16 v[68:71], v[52:55], a[16:23], v104\n\t" \
  "s_cbranch_scc1 9f\n\t" \
  "6:\n\t" \
  "s_barrier\n\t" \
  "ds_read_b128 v[60:63], v101 offset:5120\n\t" \
  "v_smfmac_f32_16x16x64_bf16 v[72:75], v[52:55], a[24:31], v104\n\t" \
  "ds_read_b128 v[64:67], v101 offset:8192\n\t" \
  "v_smfmac_f32_16x16x64_bf16 v[68:71], v[56:59], a[96:103], v104\n\t" \
  "v_smfmac_f32_16x16x64_bf16 v[72:75], v[56:59], a[104:111], v104\n\t" \
  "ds_read_b128 v[48:51], v101 offset:9216\n\t" \
  "s_waitcnt lgkmcnt(2)\n\t" \
  "v_smfmac_f32_16x16x64_bf16 v[68:71], v[60:63], a[112:119], v104\n\t" \
  "ds_read_b128 v[52:55], v101 offset:2048\n\t" \
  "v_smfmac_f32_16x16x64_bf16 v[72:75], v[60:63], a[120:127], v104\n\t" \
  "ds_read_b128 v[56:59], v101 offset:3072\n\t" \
  "s_waitcnt lgkmcnt(3)\n\t" \
  "v_smfmac_f32_16x16x64_bf16 v[68:71], v[64:67], a[192:199], v104\n\t" \
  "ds_read_b128 v[60:63], v101 offset:6144\n\t" \
  "v_smfmac_f32_16x16x64_bf16 v[72:75], v[64:67], a[200:207], v104\n\t" \
  "s_waitcnt lgkmcnt(3)\n\t" \
  "v_smfmac_f32_16x16x64_bf16 v[68:71], v[48:51], a[208:215], v104\n\t" \
  "ds_read_b128 v[64:67], v101 offset:7168\n\t" \
  "ds_read_b128 v[16:19], v103 offset:16384\n\t" \
  "v_smfmac_f32_16x16x64_bf16 v[72:75], v[48:51], a[216:223], v104\n\t" \
  "s_waitcnt lgkmcnt(4)\n\t" \
  "v_smfmac_f32_16x16x64_bf16 v[68:71], v[52:55], v[84:91], v104\n\t" \
  "ds_read_b128 v[48:51], v101 offset:11264\n\t" \
  "ds_read_b128 v[20:23], v103 offset:17408\n\t" \
  "v_smfmac_f32_16x16x64_bf16 v[72:75], v[52:55], v[92:99], v104\n\t" \
  "s_waitcnt lgkmcnt(5)\n\t" \
  "v_smfmac_f32_16x16x64_bf16 v[68:71], v[56:59], a[64:71], v104\n\t" \
  "ds_read_b128 v[52:55], v101 offset:10240\n\t" \
  "ds_read_b128 v[24:27], v103 offset:18432\n\t" \
  "v_smfmac_f32_16x16x64_bf16 v[72:75], v[56:59], a[72:79], v104\n\t" \
  "ds_read_b128 v[28:31], v103 offset:19456\n\t" \
  "s_waitcnt lgkmcnt(7)\n\t" \
  "v_smfmac_f32_16x16x64_bf16 v[68:71], v[60:63], v[0:7], v104\n\t" \
  "ds_read_b128 v[84:87], v103 offset:22528\n\t" \
  "v_smfmac_f32_16x16x64_bf16 v[72:75], v[60:63], v[8:15], v104\n\t" \
  "ds_read_b128 v[88:91], v103 offset:23552\n\t" \
  "s_waitcnt lgkmcnt(8)\n\t" \
  "v_smfmac_f32_16x16x64_bf16 v[68:71], v[64:67], a[160:167], v104\n\t" \
  "ds_read_b128 v[0:3], v102 offset:0\n\t" \
  "v_smfmac_f32_16x16x64_bf16 v[72:75], v[64:67], a[168:175], v104\n\t" \
  "ds_read_b128 v[4:7], v102 offset:1024\n\t" \
  "s_waitcnt lgkmcnt(8)\n\t" \
  "v_smfmac_f32_16x16x64_bf16 v[68:71], v[48:51], v[120:127], v104\n\t" \
  "ds_read_b128 v[92:95], v102 offset:22528\n\t" \
  "s_waitcnt lgkmcnt(7)\n\t" \
  "v_smfmac_f32_16x16x64_bf16 v[68:71], v[52:55], v[16:23], v104\n\t" \
  "ds_read_b128 v[8:11], v102 offset:2048\n\t" \
  "v_smfmac_f32_16x16x64_bf16 v[72:75], v[48:51], v[128:135], v104\n\t" \
  "ds_read_b128 v[12:15], v102 offset:3072\n\t" \
  "s_waitcnt lgkmcnt(7)\n\t" \
  "v_smfmac_f32_16x16x64_bf16 v[72:75], v[52:55], v[24:31], v104\n\t" \
  "v_add_f32 v44, v68, v70\n\t" \
  "ds_read_b128 v[96:99], v102 offset:23552\n\t" \
  "v_smfmac_f32_16x16x64_bf16 v[76:79], v[56:59], a[80:87], v104\n\t" \
  "v_add_f32 v45, v69, v71\n\t" \
  "v_cvt_pk_bf16_f32 v44, v44, v45\n\t" \
  "v_smfmac_f32_16x16x64_bf16 v[80:83], v[56:59], a[88:95], v104\n\t" \
  "v_add_f32 v46, v72, v74\n\t" \
  "v_add_f32 v47, v73, v75\n\t" \
  "v_smfmac_f32_16x16x64_bf16 v[76:79], v[60:63], a[144:151], v104\n\t" \
  "v_cvt_pk_bf16_f32 v46, v46, v47\n\t" \
  "ds_write2st64_b32 v105, v44, v46 offset1:1\n\t" \
  "v_smfmac_f32_16x16x64_bf16 v[80:83], v[60:63], a[152:159], v104\n\t" \
  "ds_read_b128 v[56:59], v101 offset:0\n\t" \
  "v_mov_b64 v[68:69], 0\n\t" \
  "v_smfmac_f32_16x16x64_bf16 v[76:79], v[64:67], a[176:183], v104\n\t" \
  "ds_read_b128 v[16:19], v102 offset:32768\n\t" \
  "v_mov_b64 v[70:71], 0\n\t" \
  "v_smfmac_f32_16x16x64_bf16 v[80:83], v[64:67], a[184:191], v104\n\t" \
  "ds_read_b128 v[20:23], v102 offset:33792\n\t" \
  "v_smfmac_f32_16x16x64_bf16 v[76:79], v[48:51], v[136:143], v104\n\t" \
  "s_waitcnt lgkmcnt(3)\n\t" \
  "s_barrier\n\t" \
  "v_smfmac_f32_16x16x64_bf16 v[80:83], v[48:51], v[144:151], v104\n\t" \
  "v_smfmac_f32_16x16x64_bf16 v[68:71], v[32:35], v[84:91], v104\n\t" \
  "v_smfmac_f32_16x16x64_bf16 v[76:79], v[52:55], a[240:247], v104\n\t" \
  "ds_read_b128 v[60:63], v101 offset:9216\n\t" \
  "ds_read_b128 v[24:27], v102 offset:34816\n\t" \
  "v_smfmac_f32_16x16x64_bf16 v[80:83], v[52:55], a[248:255], v104\n\t" \
  "ds_read_b128 v[64:67], v101 offset:2048\n\t" \
  "s_waitcnt lgkmcnt(5)\n\t" \
  "v_smfmac_f32_16x16x64_bf16 v[76:79], v[56:59], v[0:7], v104\n\t" \
  "ds_read_b128 v[48:51], v101 offset:4096\n\t" \
  "ds_read_b128 v[28:31], v102 offset:35840\n\t" \
  "v_smfmac_f32_16x16x64_bf16 v[80:83], v[56:59], v[8:15], v104\n\t" \
  "ds_read_b128 v[84:87], v102 offset:55296\n\t" \
  "global_load_dword v119, v109, %[kc] sc1\n\t" \
  "s_waitcnt lgkmcnt(5)\n\t" \
  "v_smfmac_f32_16x16x64_bf16 v[76:79], v[60:63], a[224:231], v104\n\t" \
  "ds_read_b128 v[52:55], v101 offset:1024\n\t" \
  "ds_read_b128 v[88:91], v102 offset:56320\n\t" \
  "v_smfmac_f32_16x16x64_bf16 v[80:83], v[60:63], a[232:239], v104\n\t" \
  "ds_read_b128 v[0:3], v103 offset:0\n\t" \
  "s_waitcnt lgkmcnt(6)\n\t" \
  "v_smfmac_f32_16x16x64_bf16 v[76:79], v[64:67], a[48:55], v104\n\t" \
  "ds_read_b128 v[56:59], v101 offset:5120\n\t" \
  "buffer_load_dwordx4 v[32:35], v100, %[ru], s47 offen offset:0 sc1\n\t" \
  "v_smfmac_f32_16x16x64_bf16 v[80:83], v[64:67], a[56:63], v104\n\t" \
  "ds_read_b128 v[60:63], v101 offset:8192\n\t" \
  "ds_read_b128 v[4:7], v103 offset:1024\n\t" \
  "s_waitcnt lgkmcnt(8)\n\t" \
  "v_smfmac_f32_16x16x64_bf16 v[76:79], v[48:51], v[16:23], v104\n\t" \
  "ds_read_b128 v[8:11], v103 offset:2048\n\t" \
  "s_waitcnt lgkmcnt(8)\n\t" \
  "v_smfmac_f32_16x16x64_bf16 v[80:83], v[48:51], v[24:31], v104\n\t" \
  "ds_read_b128 v[12:15], v103 offset:3072\n\t" \
  "buffer_load_dwordx4 v[36:39], v100, %[ru], s47 offen offset:1024 sc1\n\t" \
  "v_smfmac_f32_16x16x64_bf16 v[68:71], v[48:51], v[92:99], v104\n\t" \
  "s_waitcnt lgkmcnt(7)\n\t" \
  "v_smfmac_f32_16x16x64_bf16 v[76:79], v[52:55], a[32:39], v104\n\t" \
  "v_smfmac_f32_16x16x64_bf16 v[80:83], v[52:55], a[40:47], v104\n\t" \
  "s_waitcnt lgkmcnt(4)\n\t" \
  "v_smfmac_f32_16x16x64_bf16 v[76:79], v[56:59], a[128:135], v104\n\t" \
  "buffer_load_dwordx4 v[40:43], v100, %[ru], s47 offen offset:2048 sc1\n\t" \
  "s_waitcnt lgkmcnt(2)\n\t" \
  "v_smfmac_f32_16x16x64_bf16 v[76:79], v[60:63], v[0:7], v104\n\t" \
  "v_smfmac_f32_16x16x64_bf16 v[68:71], v[60:63], v[84:91], v104\n\t" \
  "v_smfmac_f32_16x16x64_bf16 v[80:83], v[56:59], a[136:143], v104\n\t" \
  "s_waitcnt lgkmcnt(0)\n\t" \
  "v_smfmac_f32_16x16x64_bf16 v[80:83], v[60:63], v[8:15], v104\n\t" \
  "v_add_f32 v20, v76, v78\n\t" \
  "v_add_f32 v21, v77, v79\n\t" \
  "v_cvt_pk_bf16_f32 v20, v20, v21\n\t" \
  "ds_write_b32 v106, v20\n\t" \
  "v_add_f32 v72, v68, v70\n\t" \
  "v_add_f32 v73, v69, v71\n\t" \
  "v_cvt_pk_bf16_f32 v72, v72, v73\n\t" \
  "ds_write_b32 v106, v72 offset:11008\n\t" \
  "v_add_f32 v23, v80, v82\n\t" \
  "v_add_f32 v21, v81, v83\n\t" \
  "v_cvt_pk_bf16_f32 v21, v23, v21\n\t" \
  "ds_write_b32 v106, v21 offset:256\n\t" \
  "s_waitcnt lgkmcnt(0)\n\t" \
  "s_barrier\n\t" \
  "s_waitcnt vmcnt(0)\n\t" \
  "s_barrier\n\t" \
  "ds_write_b128 v101, v[32:35] offset:25600\n\t" \
  "ds_write_b128 v101, v[36:39] offset:26624\n\t" \
  "ds_write_b128 v101, v[40:43] offset:27648\n\t" \
  "s_add_i32 s77, s44, 1\n\t" \
  "s_min_u32 s77, s77, 63\n\t" \
  "v_cmp_gt_i32 vcc, s77, v119\n\t" \
  "s_cmp_lg_u64 vcc, 0\n\t" \
  "s_cselect_b32 s42, 32, 0\n\t" \
  "s_add_i32 s41, s56, 1950\n\t" \
  "s_sub_i32 s76, s92, s51\n\t" \
  "s_sub_i32 s77, s93, s53\n\t" \
  "s_cmp_eq_u32 s76, 0\n\t" \
  "s_cbranch_scc1 21f\n\t" \
  "v_cvt_f32_u32 v117, s76\n\t" \
  "v_cvt_f32_u32 v116, s77\n\t" \
  "v_rcp_f32 v117, v117\n\t" \
  "s_mov_b32 s51, s92\n\t" \
  "v_mul_f32 v117, v117, v116\n\t" \
  "v_max_f32 v117, 0x3f028f5c, v117\n\t" \
  "v_min_f32 v117, 0x3ffeb852, v117\n\t" \
  "v_rcp_f32 v116, v117\n\t" \
  "v_mul_f32 v117, 0x4f000000, v117\n\t" \
  "v_mul_f32 v116, 0x47800000, v116\n\t" \
  "v_cvt_u32_f32 v117, v117\n\t" \
  "v_cvt_u32_f32 v116, v116\n\t" \
  "s_mov_b32 s53, s93\n\t" \
  "v_readfirstlane_b32 s46, v117\n\t" \
  "v_readfirstlane_b32 s52, v116\n\t" \
  "21:\n\t" \
  "v_mov_b32 v0, -1\n\t" \
  "v_mov_b32 v1, -1\n\t" \
  "v_mov_b32 v2, -1\n\t" \
  "v_mov_b32 v3, -1\n\t" \
  "s_and_b32 s76, s49, 3\n\t" \
  "s_mul_i32 s76, s76, 0xc000\n\t" \
  "s_add_i32 s76, s76, 0x40000\n\t" \
  "buffer_store_dwordx4 v[0:3], v110, %[rh], s76 offen sc1\n\t" \
  "s_add_i32 s76, s44, 1\n\t" \
  "s_and_b32 s76, s76, 3\n\t" \
  "s_lshl_b32 s76, s76, 18\n\t" \
  "s_add_i32 s77, s76, 0x10000\n\t" \
  "buffer_store_dwordx4 v[0:3], v107, %[rh], s76 offen sc1\n\t" \
  "buffer_store_dwordx4 v[0:3], v107, %[rh], s77 offen sc1\n\t" \
  "buffer_store_dwordx4 v[0:3], v111, %[rh], s76 offen sc1\n\t" \
  "s_add_i32 s44, s44, 1\n\t" \
  "s_and_b32 s58, s58, 4\n\t" \
  "s_and_b32 s76, s44, 64\n\t" \
  "s_or_b32 s42, s42, s76\n\t" \
  "s_cbranch_scc0 20b\n\t" \
  "s_branch 99f\n\t" \
  "14:\n\t" \
  "s_sleep 3\n\t" \
  "s_add_u32 s59, s59, 0x10000\n\t" \
  "s_cmp_lt_u32 s59, 0x100000\n\t" \
  "s_cbranch_scc1 13b\n\t" \
  "s_mov_b64 exec, -1\n\t" \
  "s_mov_b32 s42, 16\n\t" \
  "s_branch 99f\n\t" \
  "8:\n\t" \
  "s_bitcmp1_b32 s58, 1\n\t" \
  "s_cbranch_scc1 4b\n\t" \
  "s_bitcmp1_b32 s58, 2\n\t" \
  "s_cbranch_scc1 5b\n\t" \
  "v_max3_u32 v116, v32, v33, v34\n\t" \
  "v_max3_u32 v117, v35, v36, v37\n\t" \
  "v_max3_u32 v116, v116, v38, v39\n\t" \
  "v_max_u32 v116, v116, v117\n\t" \
  "v_cmp_eq_u32 vcc, -1, v116\n\t" \
  "s_cbranch_vccz 5b\n\t" \
  "s_branch 0f\n\t" \
  "9:\n\t" \
  "s_bitcmp1_b32 s58, 2\n\t" \
  "s_cbranch_scc1 6b\n\t" \
  "v_max3_u32 v116, v40, v41, v42\n\t" \
  "v_max3_u32 v117, v43, v44, v45\n\t" \
  "v_max3_u32 v116, v116, v46, v47\n\t" \
  "v_max_u32 v116, v116, v117\n\t" \
  "v_cmp_eq_u32 vcc, -1, v116\n\t" \
  "s_cbranch_vccz 6b\n\t" \
  "s_or_b32 s58, s58, 2\n\t" \
  "0:\n\t" \
  "s_or_b32 s58, s58, 1\n\t" \
  "11:\n\t" \
  "buffer_load_dwordx4 v[32:35], v100, %[rh], s40 offen sc1\n\t" \
  "buffer_load_dwordx4 v[36:39], v100, %[rh], s76 offen sc1\n\t" \
  "buffer_load_dwordx4 v[40:43], v100, %[rh], s77 offen sc1\n\t" \
  "buffer_load_dwordx4 v[44:47], v100, %[rh], s78 offen sc1\n\t" \
  "s_waitcnt lgkmcnt(0)\n\t" \
  "s_branch 10b\n\t" \
  "17:\n\t" \
  "s_add_u32 s59, s59, 1\n\t" \
  "s_bitcmp1_b32 s58, 2\n\t" \
  "s_cbranch_scc1 3b\n\t" \
  "s_waitcnt vmcnt(0)\n\t" \
  "v_max_u32 v116, v32, v33\n\t" \
  "v_max3_u32 v116, v116, v34, v35\n\t" \
  "v_max3_u32 v116, v116, v36, v37\n\t" \
  "v_max3_u32 v116, v116, v38, v39\n\t" \
  "v_max3_u32 v116, v116, v40, v41\n\t" \
  "v_max3_u32 v116, v116, v42, v43\n\t" \
  "v_max3_u32 v116, v116, v44, v45\n\t" \
  "v_max3_u32 v116, v116, v46, v47\n\t" \
  "v_cmp_eq_u32 vcc, -1, v116\n\t" \
  "s_cbranch_vccz 3b\n\t" \
  "s_bitcmp1_b32 s59, 6\n\t" \
  "s_cbranch_scc1 12f\n\t" \
  "buffer_load_dwordx4 v[32:35], v100, %[rh], s40 offen sc1\n\t" \
  "buffer_load_dwordx4 v[36:39], v100, %[rh], s76 offen sc1\n\t" \
  "buffer_load_dwordx4 v[40:43], v100, %[rh], s77 offen sc1\n\t" \
  "buffer_load_dwordx4 v[44:47], v100, %[rh], s78 offen sc1\n\t" \
  "s_branch 17b\n\t" \
  "12:\n\t" \
  "s_mov_b32 s42, s58\n\t" \
  "s_branch 99f\n\t" \
  "99:\n\t" \
  "s_waitcnt vmcnt(0) lgkmcnt(0)\n\t" \
  ""
#define VO_BODY \
  "s_cmp_eq_u32 %[wv], 1\n\t" \
  "s_cbranch_scc1 91f\n\t" \
  "s_cmp_eq_u32 %[wv], 2\n\t" \
  "s_cbranch_scc1 92f\n\t" \
  "s_cmp_eq_u32 %[wv], 3\n\t" \
  "s_cbranch_scc1 93f\n\t" \
  "90:\n\t" \
  "s_mov_b32 s58, s42\n\t" \
  "20:\n\t" \
  "s_and_b32 s76, s44, 7\n\t" \
  "s_lshl_b32 s76, s76, 15\n\t" \
  "s_add_i32 s40, s54, s76\n\t" \
  "s_and_b32 s76, s44, 3\n\t" \
  "s_lshl_b32 s45, s76, 14\n\t" \
  "s_lshl_b32 s47, s76, 20\n\t" \
  "s_add_i32 s47, s47, s55\n\t" \
  "s_lshr_b32 s76, s47, 21\n\t" \
  "s_bitcmp1_b64 %[sel], s76\n\t" \
  "s_cselect_b32 s76, 0x2000, 0\n\t" \
  "s_add_i32 s47, s47, s76\n\t" \
  "s_add_i32 s49, s44, -1\n\t" \
  "s_and_b32 s48, s49, 2\n\t" \
  "s_lshl_b32 s48, s48, 13\n\t" \
  "s_and_b32 s76, s49, 1\n\t" \
  "s_lshl_b32 s76, s76, 12\n\t" \
  "s_add_i32 s48, s48, s76\n\t" \
  "s_lshl_b32 s50, s53, 8\n\t" \
  "s_mov_b32 s59, 0\n\t" \
  "s_mov_b32 s92, s51\n\t" \
  "s_mov_b32 s93, s53\n\t" \
  "s_xor_b32 s76, s40, 0x400\n\t" \
  "s_xor_b32 s77, s40, 0x800\n\t" \
  "s_xor_b32 s78, s40, 0xc00\n\t" \
  "s_cmp_lg_u32 s58, 0\n\t" \
  "s_cbranch_scc1 11f\n\t" \
  "10:\n\t" \
  "ds_read_b128 v[48:51], v102 offset:0\n\t" \
  "ds_read_b128 v[52:55], v102 offset:1024\n\t" \
  "ds_read_b128 v[56:59], v102 offset:2048\n\t" \
  "ds_read_b128 v[60:63], v102 offset:3072\n\t" \
  "ds_read_b128 v[0:3], v102 offset:16384\n\t" \
  "ds_read_b128 v[4:7], v102 offset:17408\n\t" \
  "ds_read_b128 v[8:11], v102 offset:18432\n\t" \
  "ds_read_b128 v[12:15], v102 offset:19456\n\t" \
  "ds_read_b128 v[16:19], v102 offset:32768\n\t" \
  "ds_read_b128 v[20:23], v102 offset:33792\n\t" \
  "ds_read_b128 v[24:27], v102 offset:34816\n\t" \
  "ds_read_b128 v[28:31], v102 offset:35840\n\t" \
  "s_waitcnt lgkmcnt(4)\n\t" \
  "ds_read_b128 v[80:83], v102 offset:49152\n\t" \
  "ds_read_b128 v[84:87], v102 offset:50176\n\t" \
  "ds_read_b128 v[88:91], v102 offset:51200\n\t" \
  "ds_read_b128 v[92:95], v102 offset:52224\n\t" \
  "46:\n\t" \
  "s_bitcmp1_b32 s58, 1\n\t" \
  "s_cselect_b64 s[80:81], -1, 0\n\t" \
  "s_cmp_lg_u32 s58, 0\n\t" \
  "s_cbranch_scc1 17f\n\t" \
  "s_mov_b32 exec_hi, 0\n\t" \
  "16:\n\t" \
  "s_memtime vcc\n\t" \
  "s_waitcnt lgkmcnt(0)\n\t" \
  "s_sub_i32 s79, s41, vcc_lo\n\t" \
  "s_cmp_gt_i32 s79, 64\n\t" \
  "s_cbranch_scc1 16b\n\t" \
  "13:\n\t" \
  "buffer_load_dword v116, v112, %[rh], s48 offen sc1\n\t" \
  "s_waitcnt vmcnt(0)\n\t" \
  "15:\n\t" \
  "v_mov_b32 v117, 20000\n\t" \
  "v_cmp_ne_u32_sdwa vcc, v116, s49 src0_sel:BYTE_0 src1_sel:DWORD\n\t" \
  "v_subrev_u32 v116, s50, v116\n\t" \
  "v_ashrrev_i32 v116, 8, v116\n\t" \
  "s_nop 1\n\t" \
  "v_max_i32_dpp v116, v116, v116 quad_perm:[1,0,3,2] row_mask:0xf bank_mask:0xf\n\t" \
  "s_cbranch_vccnz 14f\n\t" \
  "s_nop 0\n\t" \
  "v_max_i32_dpp v116, v116, v116 quad_perm:[2,3,0,1] row_mask:0xf bank_mask:0xf\n\t" \
  "s_nop 1\n\t" \
  "v_max_i32_dpp v116, v116, v116 row_half_mirror row_mask:0xf bank_mask:0xf\n\t" \
  "s_nop 1\n\t" \
  "v_max_i32_dpp v116, v116, v116 row_mirror row_mask:0xf bank_mask:0xf\n\t" \
  "v_med3_i32 v116, v116, 0, v117\n\t" \
  "v_mul_u32_u24 v116, s52, v116\n\t" \
  "v_lshrrev_b32 v116, 16, v116\n\t" \
  "s_mov_b64 exec, -1\n\t" \
  "v_readfirstlane_b32 s84, v116\n\t" \
  "v_readlane_b32 s85, v116, 16\n\t" \
  "s_max_u32 s84, s84, s85\n\t" \
  "s_add_i32 s84, s84, s51\n\t" \
  ".p2align 6\n\t" \
  "s_nop 0\n\t" \
  "s_nop 0\n\t" \
  "s_nop 0\n\t" \
  "s_nop 0\n\t" \
  "s_nop 0\n\t" \
  "s_nop 0\n\t" \
  "s_nop 0\n\t" \
  "s_nop 0\n\t" \
  "1:\n\t" \
  "s_memtime vcc\n\t" \
  "s_waitcnt lgkmcnt(0)\n\t" \
  "s_sub_i32 s79, s84, vcc_lo\n\t" \
  "s_cmp_gt_i32 s79, 128\n\t" \
  "s_cbranch_scc1 1b\n\t" \
  ".p2align 4\n\t" \
  "2:\n\t" \
  "s_sub_i32 s79, s79, 33\n\t" \
  "s_cmp_gt_i32 s79, 56\n\t" \
  "s_cbranch_scc1 2b\n\t" \
  "buffer_load_dwordx4 v[32:35], v100, %[rh], s40 offen sc1\n\t" \
  "s_nop 12\n\t" \
  "buffer_load_dwordx4 v[36:39], v100, %[rh], s76 offen sc1\n\t" \
  "s_nop 12\n\t" \
  "buffer_load_dwordx4 v[40:43], v100, %[rh], s77 offen sc1\n\t" \
  "s_nop 12\n\t" \
  "buffer_load_dwordx4 v[44:47], v100, %[rh], s78 offen sc1\n\t" \
  "s_memtime vcc\n\t" \
  "s_memrealtime s[82:83]\n\t" \
  "s_nop 4\n\t" \
  "s_memrealtime s[84:85]\n\t" \
  "s_nop 4\n\t" \
  "s_memrealtime s[56:57]\n\t" \
  "s_nop 4\n\t" \
  "s_memrealtime s[86:87]\n\t" \
  "s_nop 4\n\t" \
  "s_memrealtime s[88:89]\n\t" \
  "s_nop 4\n\t" \
  "s_memrealtime s[90:91]\n\t" \
  "s_mov_b32 s92, 0\n\t" \
  "s_waitcnt lgkmcnt(0)\n\t" \
  "s_cmp_eq_u32 s82, s84\n\t" \
  "s_addc_u32 s92, s92, 0\n\t" \
  "s_cmp_eq_u32 s82, s56\n\t" \
  "s_addc_u32 s92, s92, 0\n\t" \
  "s_cmp_eq_u32 s82, s86\n\t" \
  "s_addc_u32 s92, s92, 0\n\t" \
  "s_cmp_eq_u32 s82, s88\n\t" \
  "s_addc_u32 s92, s92, 0\n\t" \
  "s_cmp_eq_u32 s82, s90\n\t" \
  "s_addc_u32 s92, s92, 0\n\t" \
  "s_mul_i32 s92, s92, 24\n\t" \
  "s_add_i32 s92, s92, vcc_lo\n\t" \
  "s_lshl4_add_u32 s93, s82, 64\n\t" \
  "3:\n\t" \
  "v_mov_b64 v[72:73], 0\n\t" \
  "v_mov_b64 v[74:75], 0\n\t" \
  "v_mov_b64 v[76:77], 0\n\t" \
  "v_mov_b64 v[78:79], 0\n\t" \
  "s_waitcnt lgkmcnt(0)\n\t" \
  "s_waitcnt vmcnt(3)\n\t" \
  "v_smfmac_f32_16x16x64_bf16 v[72:75], v[32:35], v[48:55], v104\n\t" \
  "ds_write_b128 v101, v[32:35] offset:0\n\t" \
  "v_smfmac_f32_16x16x64_bf16 v[76:79], v[32:35], v[56:63], v104\n\t" \
  "s_waitcnt vmcnt(2)\n\t" \
  "ds_write_b128 v101, v[36:39] offset:1024\n\t" \
  "v_smfmac_f32_16x16x64_bf16 v[72:75], v[36:39], a[0:7], v104\n\t" \
  "v_smfmac_f32_16x16x64_bf16 v[76:79], v[36:39], a[8:15], v104\n\t" \
  "s_nop 3\n\t" \
  "v_cmp_u_f32 vcc, v72, v73\n\t" \
  "v_cmp_u_f32 s[82:83], v74, v75\n\t" \
  "s_waitcnt vmcnt(1)\n\t" \
  "v_smfmac_f32_16x16x64_bf16 v[72:75], v[40:43], a[16:23], v104\n\t" \
  "s_or_b64 vcc, vcc, s[82:83]\n\t" \
  "s_or_b64 vcc, vcc, s[80:81]\n\t" \
  "v_smfmac_f32_16x16x64_bf16 v[76:79], v[40:43], a[24:31], v104\n\t" \
  "s_waitcnt lgkmcnt(0)\n\t" \
  "s_cbranch_scc1 8f\n\t" \
  "5:\n\t" \
  "s_barrier\n\t" \
  "4:\n\t" \
  "s_memtime s[56:57]\n\t" \
  "ds_read_b128 v[48:51], v101 offset:4096\n\t" \
  "ds_read_b128 v[52:55], v101 offset:5120\n\t" \
  "ds_read_b128 v[56:59], v101 offset:9216\n\t" \
  "s_waitcnt vmcnt(0)\n\t" \
  "v_smfmac_f32_16x16x64_bf16 v[72:75], v[44:47], a[32:39], v104\n\t" \
  "ds_read_b128 v[60:63], v101 offset:8192\n\t" \
  "ds_read_b128 v[64:67], v101 offset:13312\n\t" \
  "v_smfmac_f32_16x16x64_bf16 v[76:79], v[44:47], a[40:47], v104\n\t" \
  "ds_read_b128 v[68:71], v101 offset:12288\n\t" \
  "ds_write_b128 v101, v[40:43] offset:2048\n\t" \
  "s_nop 1\n\t" \
  "v_cmp_u_f32 vcc, v72, v73\n\t" \
  "v_cmp_u_f32 s[82:83], v74, v75\n\t" \
  "s_waitcnt lgkmcnt(6)\n\t" \
  "v_smfmac_f32_16x16x64_bf16 v[72:75], v[48:51], v[0:7], v104\n\t" \
  "s_or_b64 vcc, vcc, s[82:83]\n\t" \
  "v_smfmac_f32_16x16x64_bf16 v[76:79], v[48:51], v[8:15], v104\n\t" \
  "s_cbranch_scc1 9f\n\t" \
  "6:\n\t" \
  "s_waitcnt lgkmcnt(5)\n\t" \
  "v_smfmac_f32_16x16x64_bf16 v[72:75], v[52:55], a[48:55], v104\n\t" \
  "ds_write_b128 v101, v[44:47] offset:3072\n\t" \
  "v_smfmac_f32_16x16x64_bf16 v[76:79], v[52:55], a[56:63], v104\n\t" \
  "v_mov_b64 v[40:41], 0\n\t" \
  "s_waitcnt lgkmcnt(5)\n\t" \
  "v_smfmac_f32_16x16x64_bf16 v[72:75], v[56:59], a[96:103], v104\n\t" \
  "v_mov_b64 v[42:43], 0\n\t" \
  "v_smfmac_f32_16x16x64_bf16 v[76:79], v[56:59], a[104:111], v104\n\t" \
  "s_waitcnt lgkmcnt(4)\n\t" \
  "v_smfmac_f32_16x16x64_bf16 v[72:75], v[60:63], v[16:23], v104\n\t" \
  "s_waitcnt lgkmcnt(0)\n\t" \
  "s_barrier\n\t" \
  "v_smfmac_f32_16x16x64_bf16 v[76:79], v[60:63], v[24:31], v104\n\t" \
  "ds_read_b128 v[48:51], v101 offset:6144\n\t" \
  "ds_read_b128 v[52:55], v101 offset:10240\n\t" \
  "v_smfmac_f32_16x16x64_bf16 v[72:75], v[64:67], a[144:151], v104\n\t" \
  "ds_read_b128 v[56:59], v101 offset:14336\n\t" \
  "ds_read_b128 v[60:63], v101 offset:7168\n\t" \
  "v_smfmac_f32_16x16x64_bf16 v[76:79], v[64:67], a[152:159], v104\n\t" \
  "ds_read_b128 v[32:35], v101 offset:11264\n\t" \
  "ds_read_b128 v[36:39], v101 offset:15360\n\t" \
  "v_smfmac_f32_16x16x64_bf16 v[72:75], v[68:71], v[80:87], v104\n\t" \
  "ds_read_b128 v[0:3], v103 offset:0\n\t" \
  "v_mov_b64 v[44:45], 0\n\t" \
  "v_smfmac_f32_16x16x64_bf16 v[76:79], v[68:71], v[88:95], v104\n\t" \
  "v_mov_b64 v[46:47], 0\n\t" \
  "s_waitcnt lgkmcnt(5)\n\t" \
  "v_smfmac_f32_16x16x64_bf16 v[72:75], v[48:51], a[64:71], v104\n\t" \
  "ds_read_b128 v[4:7], v103 offset:1024\n\t" \
  "v_mov_b64 v[80:81], 0\n\t" \
  "v_smfmac_f32_16x16x64_bf16 v[72:75], v[52:55], a[112:119], v104\n\t" \
  "v_mov_b64 v[82:83], 0\n\t" \
  "s_waitcnt lgkmcnt(2)\n\t" \
  "v_smfmac_f32_16x16x64_bf16 v[72:75], v[56:59], a[160:167], v104\n\t" \
  "ds_read_b128 v[8:11], v103 offset:2048\n\t" \
  "v_mov_b64 v[84:85], 0\n\t" \
  "v_smfmac_f32_16x16x64_bf16 v[72:75], v[60:63], a[80:87], v104\n\t" \
  "ds_read_b128 v[12:15], v103 offset:3072\n\t" \
  "v_mov_b64 v[86:87], 0\n\t" \
  "v_smfmac_f32_16x16x64_bf16 v[72:75], v[32:35], a[128:135], v104\n\t" \
  "ds_read_b128 v[16:19], v103 offset:16384\n\t" \
  "v_mov_b64 v[88:89], 0\n\t" \
  "v_smfmac_f32_16x16x64_bf16 v[72:75], v[36:39], a[176:183], v104\n\t" \
  "ds_read_b128 v[20:23], v103 offset:17408\n\t" \
  "v_mov_b64 v[90:91], 0\n\t" \
  "v_smfmac_f32_16x16x64_bf16 v[76:79], v[48:51], a[72:79], v104\n\t" \
  "ds_read_b128 v[24:27], v103 offset:18432\n\t" \
  "v_mov_b64 v[92:93], 0\n\t" \
  "v_smfmac_f32_16x16x64_bf16 v[76:79], v[52:55], a[120:127], v104\n\t" \
  "v_add_f32 v96, v72, v74\n\t" \
  "v_add_f32 v97, v73, v75\n\t" \
  "v_cvt_pk_bf16_f32 v96, v96, v97\n\t" \
  "ds_write_b32 v105, v96\n\t" \
  "v_smfmac_f32_16x16x64_bf16 v[76:79], v[56:59], a[168:175], v104\n\t" \
  "ds_read_b128 v[28:31], v103 offset:19456\n\t" \
  "v_mov_b64 v[94:95], 0\n\t" \
  "v_smfmac_f32_16x16x64_bf16 v[76:79], v[60:63], a[88:95], v104\n\t" \
  "v_smfmac_f32_16x16x64_bf16 v[76:79], v[32:35], a[136:143], v104\n\t" \
  "v_smfmac_f32_16x16x64_bf16 v[76:79], v[36:39], a[184:191], v104\n\t" \
  "s_waitcnt lgkmcnt(1)\n\t" \
  "s_barrier\n\t" \
  "ds_read_b64_tr_b16 v[96:97], v107 offset:0\n\t" \
  "ds_read_b64_tr_b16 v[98:99], v107 offset:64\n\t" \
  "ds_read_b64_tr_b16 v[56:57], v107 offset:1024\n\t" \
  "ds_read_b64_tr_b16 v[58:59], v107 offset:1088\n\t" \
  "s_nop 1\n\t" \
  "v_add_f32 v116, v76, v78\n\t" \
  "v_add_f32 v117, v77, v79\n\t" \
  "v_cvt_pk_bf16_f32 v116, v116, v117\n\t" \
  "ds_write_b32 v105, v116 offset:256\n\t" \
  "s_waitcnt lgkmcnt(3)\n\t" \
  "v_smfmac_f32_16x16x64_bf16 v[40:43], v[96:99], a[192:199], v106\n\t" \
  "v_smfmac_f32_16x16x64_bf16 v[44:47], v[96:99], a[224:231], v106\n\t" \
  "s_waitcnt lgkmcnt(0)\n\t" \
  "s_barrier\n\t" \
  "ds_read_b64_tr_b16 v[52:53], v107 offset:256\n\t" \
  "ds_read_b64_tr_b16 v[54:55], v107 offset:320\n\t" \
  "v_smfmac_f32_16x16x64_bf16 v[40:43], v[56:59], a[208:215], v106\n\t" \
  "ds_read_b64_tr_b16 v[60:61], v107 offset:1280\n\t" \
  "ds_read_b64_tr_b16 v[62:63], v107 offset:1344\n\t" \
  "v_smfmac_f32_16x16x64_bf16 v[44:47], v[56:59], v[0:7], v106\n\t" \
  "v_smfmac_f32_16x16x64_bf16 v[80:83], v[96:99], a[240:247], v106\n\t" \
  "v_mov_b64 v[32:33], 0\n\t" \
  "v_smfmac_f32_16x16x64_bf16 v[84:87], v[96:99], v[136:143], v106\n\t" \
  "v_mov_b64 v[34:35], 0\n\t" \
  "v_smfmac_f32_16x16x64_bf16 v[80:83], v[56:59], v[120:127], v106\n\t" \
  "v_mov_b64 v[36:37], 0\n\t" \
  "v_smfmac_f32_16x16x64_bf16 v[84:87], v[56:59], v[16:23], v106\n\t" \
  "s_waitcnt lgkmcnt(2)\n\t" \
  "v_smfmac_f32_16x16x64_bf16 v[40:43], v[52:55], a[200:207], v106\n\t" \
  "v_smfmac_f32_16x16x64_bf16 v[44:47], v[52:55], a[232:239], v106\n\t" \
  "s_xor_b32 s76, s47, 0x40000\n\t" \
  "s_waitcnt lgkmcnt(0)\n\t" \
  "v_smfmac_f32_16x16x64_bf16 v[40:43], v[60:63], a[216:223], v106\n\t" \
  "v_mov_b64 v[38:39], 0\n\t" \
  "s_xor_b32 s77, s47, 0x80000\n\t" \
  "v_smfmac_f32_16x16x64_bf16 v[44:47], v[60:63], v[8:15], v106\n\t" \
  "s_xor_b32 s78, s47, 0xc0000\n\t" \
  "v_smfmac_f32_16x16x64_bf16 v[80:83], v[52:55], a[248:255], v106\n\t" \
  "ds_read_b128 v[0:3], v103 offset:32768\n\t" \
  "v_cvt_pk_bf16_f32 v64, v40, v41\n\t" \
  "v_smfmac_f32_16x16x64_bf16 v[84:87], v[52:55], v[144:151], v106\n\t" \
  "ds_read_b128 v[4:7], v103 offset:33792\n\t" \
  "v_cvt_pk_bf16_f32 v65, v42, v43\n\t" \
  "v_smfmac_f32_16x16x64_bf16 v[80:83], v[60:63], v[128:135], v106\n\t" \
  "ds_read_b128 v[8:11], v103 offset:34816\n\t" \
  "v_cvt_pk_bf16_f32 v66, v44, v45\n\t" \
  "v_smfmac_f32_16x16x64_bf16 v[84:87], v[60:63], v[24:31], v106\n\t" \
  "ds_read_b128 v[12:15], v103 offset:35840\n\t" \
  "v_cvt_pk_bf16_f32 v67, v46, v47\n\t" \
  "v_smfmac_f32_16x16x64_bf16 v[88:91], v[96:99], v[152:159], v106\n\t" \
  "v_permlane16_swap_b32 v64, v66\n\t" \
  "v_cvt_pk_bf16_f32 v68, v80, v81\n\t" \
  "v_smfmac_f32_16x16x64_bf16 v[92:95], v[96:99], v[184:191], v106\n\t" \
  "v_permlane16_swap_b32 v65, v67\n\t" \
  "buffer_store_dwordx4 v[64:67], v108, %[ru], s47 offen sc1\n\t" \
  "v_cvt_pk_bf16_f32 v69, v82, v83\n\t" \
  "v_smfmac_f32_16x16x64_bf16 v[88:91], v[52:55], v[160:167], v106\n\t" \
  "v_cvt_pk_bf16_f32 v70, v84, v85\n\t" \
  "v_smfmac_f32_16x16x64_bf16 v[92:95], v[52:55], v[192:199], v106\n\t" \
  "v_cvt_pk_bf16_f32 v71, v86, v87\n\t" \
  "v_smfmac_f32_16x16x64_bf16 v[88:91], v[56:59], v[168:175], v106\n\t" \
  "ds_read_b128 v[16:19], v103 offset:49152\n\t" \
  "v_permlane16_swap_b32 v68, v70\n\t" \
  "s_waitcnt lgkmcnt(3)\n\t" \
  "v_smfmac_f32_16x16x64_bf16 v[92:95], v[56:59], v[0:7], v106\n\t" \
  "ds_read_b128 v[20:23], v103 offset:50176\n\t" \
  "v_permlane16_swap_b32 v69, v71\n\t" \
  "buffer_store_dwordx4 v[68:71], v108, %[ru], s76 offen sc1\n\t" \
  "v_smfmac_f32_16x16x64_bf16 v[88:91], v[60:63], v[176:183], v106\n\t" \
  "ds_read_b128 v[24:27], v103 offset:51200\n\t" \
  "s_waitcnt lgkmcnt(3)\n\t" \
  "v_smfmac_f32_16x16x64_bf16 v[92:95], v[60:63], v[8:15], v106\n\t" \
  "ds_read_b128 v[28:31], v103 offset:52224\n\t" \
  "v_smfmac_f32_16x16x64_bf16 v[32:35], v[96:99], v[200:207], v106\n\t" \
  "v_cvt_pk_bf16_f32 v72, v88, v89\n\t" \
  "v_smfmac_f32_16x16x64_bf16 v[36:39], v[96:99], v[232:239], v106\n\t" \
  "v_cvt_pk_bf16_f32 v73, v90, v91\n\t" \
  "v_smfmac_f32_16x16x64_bf16 v[32:35], v[52:55], v[208:215], v106\n\t" \
  "v_cvt_pk_bf16_f32 v74, v92, v93\n\t" \
  "v_smfmac_f32_16x16x64_bf16 v[36:39], v[52:55], v[240:247], v106\n\t" \
  "v_cvt_pk_bf16_f32 v75, v94, v95\n\t" \
  "v_smfmac_f32_16x16x64_bf16 v[32:35], v[56:59], v[216:223], v106\n\t" \
  "v_permlane16_swap_b32 v72, v74\n\t" \
  "v_smfmac_f32_16x16x64_bf16 v[32:35], v[60:63], v[224:231], v106\n\t" \
  "v_permlane16_swap_b32 v73, v75\n\t" \
  "buffer_store_dwordx4 v[72:75], v108, %[ru], s77 offen sc1\n\t" \
  "s_waitcnt lgkmcnt(2)\n\t" \
  "v_smfmac_f32_16x16x64_bf16 v[36:39], v[56:59], v[16:23], v106\n\t" \
  "s_waitcnt lgkmcnt(0)\n\t" \
  "v_smfmac_f32_16x16x64_bf16 v[36:39], v[60:63], v[24:31], v106\n\t" \
  "v_cvt_pk_bf16_f32 v76, v32, v33\n\t" \
  "v_cvt_pk_bf16_f32 v77, v34, v35\n\t" \
  "s_nop 5\n\t" \
  "v_cvt_pk_bf16_f32 v78, v36, v37\n\t" \
  "v_cvt_pk_bf16_f32 v79, v38, v39\n\t" \
  "s_nop 0\n\t" \
  "v_permlane16_swap_b32 v76, v78\n\t" \
  "v_permlane16_swap_b32 v77, v79\n\t" \
  "buffer_store_dwordx4 v[76:79], v108, %[ru], s78 offen sc1\n\t" \
  "s_memtime s[76:77]\n\t" \
  "s_mov_b32 s42, 0\n\t" \
  "s_waitcnt lgkmcnt(0)\n\t" \
  "s_mov_b32 s57, s76\n\t" \
  "s_add_i32 s41, s56, 1950\n\t" \
  "s_sub_i32 s76, s92, s51\n\t" \
  "s_sub_i32 s77, s93, s53\n\t" \
  "s_cmp_eq_u32 s76, 0\n\t" \
  "s_cbranch_scc1 21f\n\t" \
  "v_cvt_f32_u32 v117, s76\n\t" \
  "v_cvt_f32_u32 v116, s77\n\t" \
  "v_rcp_f32 v117, v117\n\t" \
  "s_mov_b32 s51, s92\n\t" \
  "v_mul_f32 v117, v117, v116\n\t" \
  "v_max_f32 v117, 0x3f028f5c, v117\n\t" \
  "v_min_f32 v117, 0x3ffeb852, v117\n\t" \
  "v_rcp_f32 v116, v117\n\t" \
  "v_mul_f32 v117, 0x4f000000, v117\n\t" \
  "v_mul_f32 v116, 0x47800000, v116\n\t" \
  "v_cvt_u32_f32 v117, v117\n\t" \
  "v_cvt_u32_f32 v116, v116\n\t" \
  "s_mov_b32 s53, s93\n\t" \
  "v_readfirstlane_b32 s46, v117\n\t" \
  "v_readfirstlane_b32 s52, v116\n\t" \
  "21:\n\t" \
  "s_add_i32 s44, s44, 1\n\t" \
  "s_and_b32 s58, s58, 4\n\t" \
  "s_and_b32 s76, s44, 64\n\t" \
  "s_or_b32 s42, s42, s76\n\t" \
  "s_cbranch_scc0 20b\n\t" \
  "s_branch 99f\n\t" \
  "14:\n\t" \
  "s_sleep 3\n\t" \
  "s_add_u32 s59, s59, 0x10000\n\t" \
  "s_cmp_lt_u32 s59, 0x100000\n\t" \
  "s_cbranch_scc1 13b\n\t" \
  "s_mov_b64 exec, -1\n\t" \
  "s_mov_b32 s42, 16\n\t" \
  "s_branch 99f\n\t" \
  "8:\n\t" \
  "s_bitcmp1_b32 s58, 1\n\t" \
  "s_cbranch_scc1 4b\n\t" \
  "s_bitcmp1_b32 s58, 2\n\t" \
  "s_cbranch_scc1 5b\n\t" \
  "v_max3_u32 v116, v32, v33, v34\n\t" \
  "v_max3_u32 v117, v35, v36, v37\n\t" \
  "v_max3_u32 v116, v116, v38, v39\n\t" \
  "v_max_u32 v116, v116, v117\n\t" \
  "v_cmp_eq_u32 vcc, -1, v116\n\t" \
  "s_cbranch_vccz 5b\n\t" \
  "s_branch 0f\n\t" \
  "9:\n\t" \
  "s_bitcmp1_b32 s58, 2\n\t" \
  "s_cbranch_scc1 6b\n\t" \
  "v_max3_u32 v116, v40, v41, v42\n\t" \
  "v_max3_u32 v117, v43, v44, v45\n\t" \
  "v_max3_u32 v116, v116, v46, v47\n\t" \
  "v_max_u32 v116, v116, v117\n\t" \
  "v_cmp_eq_u32 vcc, -1, v116\n\t" \
  "s_cbranch_vccz 6b\n\t" \
  "s_or_b32 s58, s58, 2\n\t" \
  "0:\n\t" \
  "s_or_b32 s58, s58, 1\n\t" \
  "11:\n\t" \
  "buffer_load_dwordx4 v[32:35], v100, %[rh], s40 offen sc1\n\t" \
  "buffer_load_dwordx4 v[36:39], v100, %[rh], s76 offen sc1\n\t" \
  "buffer_load_dwordx4 v[40:43], v100, %[rh], s77 offen sc1\n\t" \
  "buffer_load_dwordx4 v[44:47], v100, %[rh], s78 offen sc1\n\t" \
  "s_waitcnt lgkmcnt(0)\n\t" \
  "s_branch 10b\n\t" \
  "17:\n\t" \
  "s_add_u32 s59, s59, 1\n\t" \
  "s_bitcmp1_b32 s58, 2\n\t" \
  "s_cbranch_scc1 3b\n\t" \
  "s_waitcnt vmcnt(0)\n\t" \
  "v_max_u32 v116, v32, v33\n\t" \
  "v_max3_u32 v116, v116, v34, v35\n\t" \
  "v_max3_u32 v116, v116, v36, v37\n\t" \
  "v_max3_u32 v116, v116, v38, v39\n\t" \
  "v_max3_u32 v116, v116, v40, v41\n\t" \
  "v_max3_u32 v116, v116, v42, v43\n\t" \
  "v_max3_u32 v116, v116, v44, v45\n\t" \
  "v_max3_u32 v116, v116, v46, v47\n\t" \
  "v_cmp_eq_u32 vcc, -1, v116\n\t" \
  "s_cbranch_vccz 3b\n\t" \
  "s_bitcmp1_b32 s59, 6\n\t" \
  "s_cbranch_scc1 12f\n\t" \
  "buffer_load_dwordx4 v[32:35], v100, %[rh], s40 offen sc1\n\t" \
  "buffer_load_dwordx4 v[36:39], v100, %[rh], s76 offen sc1\n\t" \
  "buffer_load_dwordx4 v[40:43], v100, %[rh], s77 offen sc1\n\t" \
  "buffer_load_dwordx4 v[44:47], v100, %[rh], s78 offen sc1\n\t" \
  "s_branch 17b\n\t" \
  "12:\n\t" \
  "s_mov_b32 s42, s58\n\t" \
  "s_branch 99f\n\t" \
  "91:\n\t" \
  "s_mov_b32 s58, s42\n\t" \
  "20:\n\t" \
  "s_and_b32 s76, s44, 7\n\t" \
  "s_lshl_b32 s76, s76, 15\n\t" \
  "s_add_i32 s40, s54, s76\n\t" \
  "s_and_b32 s76, s44, 3\n\t" \
  "s_lshl_b32 s45, s76, 14\n\t" \
  "s_lshl_b32 s47, s76, 20\n\t" \
  "s_add_i32 s47, s47, s55\n\t" \
  "s_lshr_b32 s76, s47, 21\n\t" \
  "s_bitcmp1_b64 %[sel], s76\n\t" \
  "s_cselect_b32 s76, 0x2000, 0\n\t" \
  "s_add_i32 s47, s47, s76\n\t" \
  "s_add_i32 s49, s44, -1\n\t" \
  "s_and_b32 s48, s49, 2\n\t" \
  "s_lshl_b32 s48, s48, 13\n\t" \
  "s_and_b32 s76, s49, 1\n\t" \
  "s_lshl_b32 s76, s76, 12\n\t" \
  "s_add_i32 s48, s48, s76\n\t" \
  "s_lshl_b32 s50, s53, 8\n\t" \
  "s_mov_b32 s59, 0\n\t" \
  "s_mov_b32 s92, s51\n\t" \
  "s_mov_b32 s93, s53\n\t" \
  "s_xor_b32 s76, s40, 0x400\n\t" \
  "s_xor_b32 s77, s40, 0x800\n\t" \
  "s_xor_b32 s78, s40, 0xc00\n\t" \
  "s_cmp_lg_u32 s58, 0\n\t" \
  "s_cbranch_scc1 11f\n\t" \
  "10:\n\t" \
  "ds_read_b128 v[48:51], v102 offset:16384\n\t" \
  "ds_read_b128 v[52:55], v102 offset:17408\n\t" \
  "ds_read_b128 v[56:59], v102 offset:18432\n\t" \
  "ds_read_b128 v[60:63], v102 offset:19456\n\t" \
  "ds_read_b128 v[0:3], v102 offset:32768\n\t" \
  "ds_read_b128 v[4:7], v102 offset:33792\n\t" \
  "ds_read_b128 v[8:11], v102 offset:34816\n\t" \
  "ds_read_b128 v[12:15], v102 offset:35840\n\t" \
  "ds_read_b128 v[16:19], v102 offset:49152\n\t" \
  "ds_read_b128 v[20:23], v102 offset:50176\n\t" \
  "ds_read_b128 v[24:27], v102 offset:51200\n\t" \
  "ds_read_b128 v[28:31], v102 offset:52224\n\t" \
  "s_waitcnt lgkmcnt(4)\n\t" \
  "ds_read_b128 v[80:83], v102 offset:0\n\t" \
  "ds_read_b128 v[84:87], v102 offset:1024\n\t" \
  "ds_read_b128 v[88:91], v102 offset:2048\n\t" \
  "ds_read_b128 v[92:95], v102 offset:3072\n\t" \
  "46:\n\t" \
  "s_bitcmp1_b32 s58, 1\n\t" \
  "s_cselect_b64 s[80:81], -1, 0\n\t" \
  "s_cmp_lg_u32 s58, 0\n\t" \
  "s_cbranch_scc1 17f\n\t" \
  "s_mov_b32 exec_hi, 0\n\t" \
  "16:\n\t" \
  "s_memtime vcc\n\t" \
  "s_waitcnt lgkmcnt(0)\n\t" \
  "s_sub_i32 s79, s41, vcc_lo\n\t" \
  "s_cmp_gt_i32 s79, 64\n\t" \
  "s_cbranch_scc1 16b\n\t" \
  "13:\n\t" \
  "buffer_load_dword v116, v112, %[rh], s48 offen sc1\n\t" \
  "s_waitcnt vmcnt(0)\n\t" \
  "15:\n\t" \
  "v_mov_b32 v117, 20000\n\t" \
  "v_cmp_ne_u32_sdwa vcc, v116, s49 src0_sel:BYTE_0 src1_sel:DWORD\n\t" \
  "v_subrev_u32 v116, s50, v116\n\t" \
  "v_ashrrev_i32 v116, 8, v116\n\t" \
  "s_nop 1\n\t" \
  "v_max_i32_dpp v116, v116, v116 quad_perm:[1,0,3,2] row_mask:0xf bank_mask:0xf\n\t" \
  "s_cbranch_vccnz 14f\n\t" \
  "s_nop 0\n\t" \
  "v_max_i32_dpp v116, v116, v116 quad_perm:[2,3,0,1] row_mask:0xf bank_mask:0xf\n\t" \
  "s_nop 1\n\t" \
  "v_max_i32_dpp v116, v116, v116 row_half_mirror row_mask:0xf bank_mask:0xf\n\t" \
  "s_nop 1\n\t" \
  "v_max_i32_dpp v116, v116, v116 row_mirror row_mask:0xf bank_mask:0xf\n\t" \
  "v_med3_i32 v116, v116, 0, v117\n\t" \
  "v_mul_u32_u24 v116, s52, v116\n\t" \
  "v_lshrrev_b32 v116, 16, v116\n\t" \
  "s_mov_b64 exec, -1\n\t" \
  "v_readfirstlane_b32 s84, v116\n\t" \
  "v_readlane_b32 s85, v116, 16\n\t" \
  "s_max_u32 s84, s84, s85\n\t" \
  "s_add_i32 s84, s84, s51\n\t" \
  ".p2align 6\n\t" \
  "s_nop 0\n\t" \
  "s_nop 0\n\t" \
  "s_nop 0\n\t" \
  "s_nop 0\n\t" \
  "s_nop 0\n\t" \
  "s_nop 0\n\t" \
  "s_nop 0\n\t" \
  "s_nop 0\n\t" \
  "1:\n\t" \
  "s_memtime vcc\n\t" \
  "s_waitcnt lgkmcnt(0)\n\t" \
  "s_sub_i32 s79, s84, vcc_lo\n\t" \
  "s_cmp_gt_i32 s79, 128\n\t" \
  "s_cbranch_scc1 1b\n\t" \
  ".p2align 4\n\t" \
  "2:\n\t" \
  "s_sub_i32 s79, s79, 33\n\t" \
  "s_cmp_gt_i32 s79, 56\n\t" \
  "s_cbranch_scc1 2b\n\t" \
  "buffer_load_dwordx4 v[32:35], v100, %[rh], s40 offen sc1\n\t" \
  "s_nop 12\n\t" \
  "buffer_load_dwordx4 v[36:39], v100, %[rh], s76 offen sc1\n\t" \
  "s_nop 12\n\t" \
  "buffer_load_dwordx4 v[40:43], v100, %[rh], s77 offen sc1\n\t" \
  "s_nop 12\n\t" \
  "buffer_load_dwordx4 v[44:47], v100, %[rh], s78 offen sc1\n\t" \
  "s_memtime vcc\n\t" \
  "s_memrealtime s[82:83]\n\t" \
  "s_nop 4\n\t" \
  "s_memrealtime s[84:85]\n\t" \
  "s_nop 4\n\t" \
  "s_memrealtime s[56:57]\n\t" \
  "s_nop 4\n\t" \
  "s_memrealtime s[86:87]\n\t" \
  "s_nop 4\n\t" \
  "s_memrealtime s[88:89]\n\t" \
  "s_nop 4\n\t" \
  "s_memrealtime s[90:91]\n\t" \
  "s_mov_b32 s92, 0\n\t" \
  "s_waitcnt lgkmcnt(0)\n\t" \
  "s_cmp_eq_u32 s82, s84\n\t" \
  "s_addc_u32 s92, s92, 0\n\t" \
  "s_cmp_eq_u32 s82, s56\n\t" \
  "s_addc_u32 s92, s92, 0\n\t" \
  "s_cmp_eq_u32 s82, s86\n\t" \
  "s_addc_u32 s92, s92, 0\n\t" \
  "s_cmp_eq_u32 s82, s88\n\t" \
  "s_addc_u32 s92, s92, 0\n\t" \
  "s_cmp_eq_u32 s82, s90\n\t" \
  "s_addc_u32 s92, s92, 0\n\t" \
  "s_mul_i32 s92, s92, 24\n\t" \
  "s_add_i32 s92, s92, vcc_lo\n\t" \
  "s_lshl4_add_u32 s93, s82, 64\n\t" \
  "3:\n\t" \
  "v_mov_b64 v[72:73], 0\n\t" \
  "v_mov_b64 v[74:75], 0\n\t" \
  "v_mov_b64 v[76:77], 0\n\t" \
  "v_mov_b64 v[78:79], 0\n\t" \
  "s_waitcnt lgkmcnt(0)\n\t" \
  "s_waitcnt vmcnt(3)\n\t" \
  "v_smfmac_f32_16x16x64_bf16 v[72:75], v[32:35], v[48:55], v104\n\t" \
  "ds_write_b128 v101, v[32:35] offset:4096\n\t" \
  "v_smfmac_f32_16x16x64_bf16 v[76:79], v[32:35], v[56:63], v104\n\t" \
  "s_waitcnt vmcnt(2)\n\t" \
  "ds_write_b128 v101, v[36:39] offset:5120\n\t" \
  "v_smfmac_f32_16x16x64_bf16 v[72:75], v[36:39], a[48:55], v104\n\t" \
  "v_smfmac_f32_16x16x64_bf16 v[76:79], v[36:39], a[56:63], v104\n\t" \
  "s_nop 3\n\t" \
  "v_cmp_u_f32 vcc, v72, v73\n\t" \
  "v_cmp_u_f32 s[82:83], v74, v75\n\t" \
  "s_waitcnt vmcnt(1)\n\t" \
  "v_smfmac_f32_16x16x64_bf16 v[72:75], v[40:43], a[64:71], v104\n\t" \
  "s_or_b64 vcc, vcc, s[82:83]\n\t" \
  "s_or_b64 vcc, vcc, s[80:81]\n\t" \
  "v_smfmac_f32_16x16x64_bf16 v[76:79], v[40:43], a[72:79], v104\n\t" \
  "s_waitcnt lgkmcnt(0)\n\t" \
  "s_cbranch_scc1 8f\n\t" \
  "5:\n\t" \
  "s_barrier\n\t" \
  "4:\n\t" \
  "s_memtime s[56:57]\n\t" \
  "ds_read_b128 v[48:51], v101 offset:8192\n\t" \
  "ds_read_b128 v[52:55], v101 offset:9216\n\t" \
  "ds_read_b128 v[56:59], v101 offset:13312\n\t" \
  "s_waitcnt vmcnt(0)\n\t" \
  "v_smfmac_f32_16x16x64_bf16 v[72:75], v[44:47], a[80:87], v104\n\t" \
  "ds_read_b128 v[60:63], v101 offset:12288\n\t" \
  "ds_read_b128 v[64:67], v101 offset:1024\n\t" \
  "v_smfmac_f32_16x16x64_bf16 v[76:79], v[44:47], a[88:95], v104\n\t" \
  "ds_read_b128 v[68:71], v101 offset:0\n\t" \
  "ds_write_b128 v101, v[40:43] offset:6144\n\t" \
  "s_nop 1\n\t" \
  "v_cmp_u_f32 vcc, v72, v73\n\t" \
  "v_cmp_u_f32 s[82:83], v74, v75\n\t" \
  "s_waitcnt lgkmcnt(6)\n\t" \
  "v_smfmac_f32_16x16x64_bf16 v[72:75], v[48:51], v[0:7], v104\n\t" \
  "s_or_b64 vcc, vcc, s[82:83]\n\t" \
  "v_smfmac_f32_16x16x64_bf16 v[76:79], v[48:51], v[8:15], v104\n\t" \
  "s_cbranch_scc1 9f\n\t" \
  "6:\n\t" \
  "s_waitcnt lgkmcnt(5)\n\t" \
  "v_smfmac_f32_16x16x64_bf16 v[72:75], v[52:55], a[96:103], v104\n\t" \
  "ds_write_b128 v101, v[44:47] offset:7168\n\t" \
  "v_smfmac_f32_16x16x64_bf16 v[76:79], v[52:55], a[104:111], v104\n\t" \
  "v_mov_b64 v[40:41], 0\n\t" \
  "s_waitcnt lgkmcnt(5)\n\t" \
  "v_smfmac_f32_16x16x64_bf16 v[72:75], v[56:59], a[144:151], v104\n\t" \
  "v_mov_b64 v[42:43], 0\n\t" \
  "v_smfmac_f32_16x16x64_bf16 v[76:79], v[56:59], a[152:159], v104\n\t" \
  "s_waitcnt lgkmcnt(4)\n\t" \
  "v_smfmac_f32_16x16x64_bf16 v[72:75], v[60:63], v[16:23], v104\n\t" \
  "s_waitcnt lgkmcnt(0)\n\t" \
  "s_barrier\n\t" \
  "v_smfmac_f32_16x16x64_bf16 v[76:79], v[60:63], v[24:31], v104\n\t" \
  "ds_read_b128 v[48:51], v101 offset:10240\n\t" \
  "ds_read_b128 v[52:55], v101 offset:14336\n\t" \
  "v_smfmac_f32_16x16x64_bf16 v[72:75], v[64:67], a[0:7], v104\n\t" \
  "ds_read_b128 v[56:59], v101 offset:2048\n\t" \
  "ds_read_b128 v[60:63], v101 offset:11264\n\t" \
  "v_smfmac_f32_16x16x64_bf16 v[76:79], v[64:67], a[8:15], v104\n\t" \
  "ds_read_b128 v[32:35], v101 offset:15360\n\t" \
  "ds_read_b128 v[36:39], v101 offset:3072\n\t" \
  "v_smfmac_f32_16x16x64_bf16 v[72:75], v[68:71], v[80:87], v104\n\t" \
  "ds_read_b128 v[0:3], v103 offset:0\n\t" \
  "v_mov_b64 v[44:45], 0\n\t" \
  "v_smfmac_f32_16x16x64_bf16 v[76:79], v[68:71], v[88:95], v104\n\t" \
  "v_mov_b64 v[46:47], 0\n\t" \
  "s_waitcnt lgkmcnt(5)\n\t" \
  "v_smfmac_f32_16x16x64_bf16 v[72:75], v[48:51], a[112:119], v104\n\t" \
  "ds_read_b128 v[4:7], v103 offset:1024\n\t" \
  "v_mov_b64 v[80:81], 0\n\t" \
  "v_smfmac_f32_16x16x64_bf16 v[72:75], v[52:55], a[160:167], v104\n\t" \
  "v_mov_b64 v[82:83], 0\n\t" \
  "s_waitcnt lgkmcnt(2)\n\t" \
  "v_smfmac_f32_16x16x64_bf16 v[72:75], v[56:59], a[16:23], v104\n\t" \
  "ds_read_b128 v[8:11], v103 offset:2048\n\t" \
  "v_mov_b64 v[84:85], 0\n\t" \
  "v_smfmac_f32_16x16x64_bf16 v[72:75], v[60:63], a[128:135], v104\n\t" \
  "ds_read_b128 v[12:15], v103 offset:3072\n\t" \
  "v_mov_b64 v[86:87], 0\n\t" \
  "v_smfmac_f32_16x16x64_bf16 v[72:75], v[32:35], a[176:183], v104\n\t" \
  "ds_read_b128 v[16:19], v103 offset:16384\n\t" \
  "v_mov_b64 v[88:89], 0\n\t" \
  "v_smfmac_f32_16x16x64_bf16 v[72:75], v[36:39], a[32:39], v104\n\t" \
  "ds_read_b128 v[20:23], v103 offset:17408\n\t" \
  "v_mov_b64 v[90:91], 0\n\t" \
  "v_smfmac_f32_16x16x64_bf16 v[76:79], v[48:51], a[120:127], v104\n\t" \
  "ds_read_b128 v[24:27], v103 offset:18432\n\t" \
  "v_mov_b64 v[92:93], 0\n\t" \
  "v_smfmac_f32_16x16x64_bf16 v[76:79], v[52:55], a[168:175], v104\n\t" \
  "v_add_f32 v96, v72, v74\n\t" \
  "v_add_f32 v97, v73, v75\n\t" \
  "v_cvt_pk_bf16_f32 v96, v96, v97\n\t" \
  "ds_write_b32 v105, v96\n\t" \
  "v_smfmac_f32_16x16x64_bf16 v[76:79], v[56:59], a[24:31], v104\n\t" \
  "ds_read_b128 v[28:31], v103 offset:19456\n\t" \
  "v_mov_b64 v[94:95], 0\n\t" \
  "v_smfmac_f32_16x16x64_bf16 v[76:79], v[60:63], a[136:143], v104\n\t" \
  "v_smfmac_f32_16x16x64_bf16 v[76:79], v[32:35], a[184:191], v104\n\t" \
  "v_smfmac_f32_16x16x64_bf16 v[76:79], v[36:39], a[40:47], v104\n\t" \
  "s_waitcnt lgkmcnt(1)\n\t" \
  "s_barrier\n\t" \
  "ds_read_b64_tr_b16 v[96:97], v107 offset:0\n\t" \
  "ds_read_b64_tr_b16 v[98:99], v107 offset:64\n\t" \
  "ds_read_b64_tr_b16 v[56:57], v107 offset:1024\n\t" \
  "ds_read_b64_tr_b16 v[58:59], v107 offset:1088\n\t" \
  "s_nop 1\n\t" \
  "v_add_f32 v116, v76, v78\n\t" \
  "v_add_f32 v117, v77, v79\n\t" \
  "v_cvt_pk_bf16_f32 v116, v116, v117\n\t" \
  "ds_write_b32 v105, v116 offset:256\n\t" \
  "s_waitcnt lgkmcnt(3)\n\t" \
  "v_smfmac_f32_16x16x64_bf16 v[40:43], v[96:99], a[192:199], v106\n\t" \
  "v_smfmac_f32_16x16x64_bf16 v[44:47], v[96:99], a[224:231], v106\n\t" \
  "s_waitcnt lgkmcnt(0)\n\t" \
  "s_barrier\n\t" \
  "ds_read_b64_tr_b16 v[52:53], v107 offset:256\n\t" \
  "ds_read_b64_tr_b16 v[54:55], v107 offset:320\n\t" \
  "v_smfmac_f32_16x16x64_bf16 v[40:43], v[56:59], a[208:215], v106\n\t" \
  "ds_read_b64_tr_b16 v[60:61], v107 offset:1280\n\t" \
  "ds_read_b64_tr_b16 v[62:63], v107 offset:1344\n\t" \
  "v_smfmac_f32_16x16x64_bf16 v[44:47], v[56:59], v[0:7], v106\n\t" \
  "v_smfmac_f32_16x16x64_bf16 v[80:83], v[96:99], a[240:247], v106\n\t" \
  "v_mov_b64 v[32:33], 0\n\t" \
  "v_smfmac_f32_16x16x64_bf16 v[84:87], v[96:99], v[136:143], v106\n\t" \
  "v_mov_b64 v[34:35], 0\n\t" \
  "v_smfmac_f32_16x16x64_bf16 v[80:83], v[56:59], v[120:127], v106\n\t" \
  "v_mov_b64 v[36:37], 0\n\t" \
  "v_smfmac_f32_16x16x64_bf16 v[84:87], v[56:59], v[16:23], v106\n\t" \
  "s_waitcnt lgkmcnt(2)\n\t" \
  "v_smfmac_f32_16x16x64_bf16 v[40:43], v[52:55], a[200:207], v106\n\t" \
  "v_smfmac_f32_16x16x64_bf16 v[44:47], v[52:55], a[232:239], v106\n\t" \
  "s_xor_b32 s76, s47, 0x40000\n\t" \
  "s_waitcnt lgkmcnt(0)\n\t" \
  "v_smfmac_f32_16x16x64_bf16 v[40:43], v[60:63], a[216:223], v106\n\t" \
  "v_mov_b64 v[38:39], 0\n\t" \
  "s_xor_b32 s77, s47, 0x80000\n\t" \
  "v_smfmac_f32_16x16x64_bf16 v[44:47], v[60:63], v[8:15], v106\n\t" \
  "s_xor_b32 s78, s47, 0xc0000\n\t" \
  "v_smfmac_f32_16x16x64_bf16 v[80:83], v[52:55], a[248:255], v106\n\t" \
  "ds_read_b128 v[0:3], v103 offset:32768\n\t" \
  "v_cvt_pk_bf16_f32 v64, v40, v41\n\t" \
  "v_smfmac_f32_16x16x64_bf16 v[84:87], v[52:55], v[144:151], v106\n\t" \
  "ds_read_b128 v[4:7], v103 offset:33792\n\t" \
  "v_cvt_pk_bf16_f32 v65, v42, v43\n\t" \
  "v_smfmac_f32_16x16x64_bf16 v[80:83], v[60:63], v[128:135], v106\n\t" \
  "ds_read_b128 v[8:11], v103 offset:34816\n\t" \
  "v_cvt_pk_bf16_f32 v66, v44, v45\n\t" \
  "v_smfmac_f32_16x16x64_bf16 v[84:87], v[60:63], v[24:31], v106\n\t" \
  "ds_read_b128 v[12:15], v103 offset:35840\n\t" \
  "v_cvt_pk_bf16_f32 v67, v46, v47\n\t" \
  "v_smfmac_f32_16x16x64_bf16 v[88:91], v[96:99], v[152:159], v106\n\t" \
  "v_permlane16_swap_b32 v64, v66\n\t" \
  "v_cvt_pk_bf16_f32 v68, v80, v81\n\t" \
  "v_smfmac_f32_16x16x64_bf16 v[92:95], v[96:99], v[184:191], v106\n\t" \
  "v_permlane16_swap_b32 v65, v67\n\t" \
  "v_cvt_pk_bf16_f32 v69, v82, v83\n\t" \
  "v_smfmac_f32_16x16x64_bf16 v[88:91], v[52:55], v[160:167], v106\n\t" \
  "buffer_store_dwordx4 v[64:67], v108, %[ru], s47 offen sc1\n\t" \
  "v_cvt_pk_bf16_f32 v70, v84, v85\n\t" \
  "v_smfmac_f32_16x16x64_bf16 v[92:95], v[52:55], v[192:199], v106\n\t" \
  "v_cvt_pk_bf16_f32 v71, v86, v87\n\t" \
  "v_smfmac_f32_16x16x64_bf16 v[88:91], v[56:59], v[168:175], v106\n\t" \
  "ds_read_b128 v[16:19], v103 offset:49152\n\t" \
  "v_permlane16_swap_b32 v68, v70\n\t" \
  "s_waitcnt lgkmcnt(3)\n\t" \
  "v_smfmac_f32_16x16x64_bf16 v[92:95], v[56:59], v[0:7], v106\n\t" \
  "ds_read_b128 v[20:23], v103 offset:50176\n\t" \
  "v_permlane16_swap_b32 v69, v71\n\t" \
  "v_smfmac_f32_16x16x64_bf16 v[88:91], v[60:63], v[176:183], v106\n\t" \
  "ds_read_b128 v[24:27], v103 offset:51200\n\t" \
  "buffer_store_dwordx4 v[68:71], v108, %[ru], s76 offen sc1\n\t" \
  "s_waitcnt lgkmcnt(3)\n\t" \
  "v_smfmac_f32_16x16x64_bf16 v[92:95], v[60:63], v[8:15], v106\n\t" \
  "ds_read_b128 v[28:31], v103 offset:52224\n\t" \
  "v_smfmac_f32_16x16x64_bf16 v[32:35], v[96:99], v[200:207], v106\n\t" \
  "v_cvt_pk_bf16_f32 v72, v88, v89\n\t" \
  "v_smfmac_f32_16x16x64_bf16 v[36:39], v[96:99], v[232:239], v106\n\t" \
  "v_cvt_pk_bf16_f32 v73, v90, v91\n\t" \
  "v_smfmac_f32_16x16x64_bf16 v[32:35], v[52:55], v[208:215], v106\n\t" \
  "v_cvt_pk_bf16_f32 v74, v92, v93\n\t" \
  "v_smfmac_f32_16x16x64_bf16 v[36:39], v[52:55], v[240:247], v106\n\t" \
  "v_cvt_pk_bf16_f32 v75, v94, v95\n\t" \
  "v_smfmac_f32_16x16x64_bf16 v[32:35], v[56:59], v[216:223], v106\n\t" \
  "v_permlane16_swap_b32 v72, v74\n\t" \
  "v_smfmac_f32_16x16x64_bf16 v[32:35], v[60:63], v[224:231], v106\n\t" \
  "v_permlane16_swap_b32 v73, v75\n\t" \
  "s_waitcnt lgkmcnt(2)\n\t" \
  "v_smfmac_f32_16x16x64_bf16 v[36:39], v[56:59], v[16:23], v106\n\t" \
  "buffer_store_dwordx4 v[72:75], v108, %[ru], s77 offen sc1\n\t" \
  "s_waitcnt lgkmcnt(0)\n\t" \
  "v_smfmac_f32_16x16x64_bf16 v[36:39], v[60:63], v[24:31], v106\n\t" \
  "v_cvt_pk_bf16_f32 v76, v32, v33\n\t" \
  "v_cvt_pk_bf16_f32 v77, v34, v35\n\t" \
  "s_nop 5\n\t" \
  "v_cvt_pk_bf16_f32 v78, v36, v37\n\t" \
  "v_cvt_pk_bf16_f32 v79, v38, v39\n\t" \
  "s_nop 0\n\t" \
  "v_permlane16_swap_b32 v76, v78\n\t" \
  "v_permlane16_swap_b32 v77, v79\n\t" \
  "buffer_store_dwordx4 v[76:79], v108, %[ru], s78 offen sc1\n\t" \
  "s_memtime s[76:77]\n\t" \
  "s_mov_b32 s42, 0\n\t" \
  "s_waitcnt lgkmcnt(0)\n\t" \
  "s_mov_b32 s57, s76\n\t" \
  "s_add_i32 s41, s56, 1950\n\t" \
  "s_sub_i32 s76, s92, s51\n\t" \
  "s_sub_i32 s77, s93, s53\n\t" \
  "s_cmp_eq_u32 s76, 0\n\t" \
  "s_cbranch_scc1 21f\n\t" \
  "v_cvt_f32_u32 v117, s76\n\t" \
  "v_cvt_f32_u32 v116, s77\n\t" \
  "v_rcp_f32 v117, v117\n\t" \
  "s_mov_b32 s51, s92\n\t" \
  "v_mul_f32 v117, v117, v116\n\t" \
  "v_max_f32 v117, 0x3f028f5c, v117\n\t" \
  "v_min_f32 v117, 0x3ffeb852, v117\n\t" \
  "v_rcp_f32 v116, v117\n\t" \
  "v_mul_f32 v117, 0x4f000000, v117\n\t" \
  "v_mul_f32 v116, 0x47800000, v116\n\t" \
  "v_cvt_u32_f32 v117, v117\n\t" \
  "v_cvt_u32_f32 v116, v116\n\t" \
  "s_mov_b32 s53, s93\n\t" \
  "v_readfirstlane_b32 s46, v117\n\t" \
  "v_readfirstlane_b32 s52, v116\n\t" \
  "21:\n\t" \
  "s_add_i32 s44, s44, 1\n\t" \
  "s_and_b32 s58, s58, 4\n\t" \
  "s_and_b32 s76, s44, 64\n\t" \
  "s_or_b32 s42, s42, s76\n\t" \
  "s_cbranch_scc0 20b\n\t" \
  "s_branch 99f\n\t" \
  "14:\n\t" \
  "s_sleep 3\n\t" \
  "s_add_u32 s59, s59, 0x10000\n\t" \
  "s_cmp_lt_u32 s59, 0x100000\n\t" \
  "s_cbranch_scc1 13b\n\t" \
  "s_mov_b64 exec, -1\n\t" \
  "s_mov_b32 s42, 16\n\t" \
  "s_branch 99f\n\t" \
  "8:\n\t" \
  "s_bitcmp1_b32 s58, 1\n\t" \
  "s_cbranch_scc1 4b\n\t" \
  "s_bitcmp1_b32 s58, 2\n\t" \
  "s_cbranch_scc1 5b\n\t" \
  "v_max3_u32 v116, v32, v33, v34\n\t" \
  "v_max3_u32 v117, v35, v36, v37\n\t" \
  "v_max3_u32 v116, v116, v38, v39\n\t" \
  "v_max_u32 v116, v116, v117\n\t" \
  "v_cmp_eq_u32 vcc, -1, v116\n\t" \
  "s_cbranch_vccz 5b\n\t" \
  "s_branch 0f\n\t" \
  "9:\n\t" \
  "s_bitcmp1_b32 s58, 2\n\t" \
  "s_cbranch_scc1 6b\n\t" \
  "v_max3_u32 v116, v40, v41, v42\n\t" \
  "v_max3_u32 v117, v43, v44, v45\n\t" \
  "v_max3_u32 v116, v116, v46, v47\n\t" \
  "v_max_u32 v116, v116, v117\n\t" \
  "v_cmp_eq_u32 vcc, -1, v116\n\t" \
  "s_cbranch_vccz 6b\n\t" \
  "s_or_b32 s58, s58, 2\n\t" \
  "0:\n\t" \
  "s_or_b32 s58, s58, 1\n\t" \
  "11:\n\t" \
  "buffer_load_dwordx4 v[32:35], v100, %[rh], s40 offen sc1\n\t" \
  "buffer_load_dwordx4 v[36:39], v100, %[rh], s76 offen sc1\n\t" \
  "buffer_load_dwordx4 v[40:43], v100, %[rh], s77 offen sc1\n\t" \
  "buffer_load_dwordx4 v[44:47], v100, %[rh], s78 offen sc1\n\t" \
  "s_waitcnt lgkmcnt(0)\n\t" \
  "s_branch 10b\n\t" \
  "17:\n\t" \
  "s_add_u32 s59, s59, 1\n\t" \
  "s_bitcmp1_b32 s58, 2\n\t" \
  "s_cbranch_scc1 3b\n\t" \
  "s_waitcnt vmcnt(0)\n\t" \
  "v_max_u32 v116, v32, v33\n\t" \
  "v_max3_u32 v116, v116, v34, v35\n\t" \
  "v_max3_u32 v116, v116, v36, v37\n\t" \
  "v_max3_u32 v116, v116, v38, v39\n\t" \
  "v_max3_u32 v116, v116, v40, v41\n\t" \
  "v_max3_u32 v116, v116, v42, v43\n\t" \
  "v_max3_u32 v116, v116, v44, v45\n\t" \
  "v_max3_u32 v116, v116, v46, v47\n\t" \
  "v_cmp_eq_u32 vcc, -1, v116\n\t" \
  "s_cbranch_vccz 3b\n\t" \
  "s_bitcmp1_b32 s59, 6\n\t" \
  "s_cbranch_scc1 12f\n\t" \
  "buffer_load_dwordx4 v[32:35], v100, %[rh], s40 offen sc1\n\t" \
  "buffer_load_dwordx4 v[36:39], v100, %[rh], s76 offen sc1\n\t" \
  "buffer_load_dwordx4 v[40:43], v100, %[rh], s77 offen sc1\n\t" \
  "buffer_load_dwordx4 v[44:47], v100, %[rh], s78 offen sc1\n\t" \
  "s_branch 17b\n\t" \
  "12:\n\t" \
  "s_mov_b32 s42, s58\n\t" \
  "s_branch 99f\n\t" \
  "92:\n\t" \
  "s_mov_b32 s58, s42\n\t" \
  "20:\n\t" \
  "s_and_b32 s76, s44, 7\n\t" \
  "s_lshl_b32 s76, s76, 15\n\t" \
  "s_add_i32 s40, s54, s76\n\t" \
  "s_and_b32 s76, s44, 3\n\t" \
  "s_lshl_b32 s45, s76, 14\n\t" \
  "s_lshl_b32 s47, s76, 20\n\t" \
  "s_add_i32 s47, s47, s55\n\t" \
  "s_lshr_b32 s76, s47, 21\n\t" \
  "s_bitcmp1_b64 %[sel], s76\n\t" \
  "s_cselect_b32 s76, 0x2000, 0\n\t" \
  "s_add_i32 s47, s47, s76\n\t" \
  "s_add_i32 s49, s44, -1\n\t" \
  "s_and_b32 s48, s49, 2\n\t" \
  "s_lshl_b32 s48, s48, 13\n\t" \
  "s_and_b32 s76, s49, 1\n\t" \
  "s_lshl_b32 s76, s76, 12\n\t" \
  "s_add_i32 s48, s48, s76\n\t" \
  "s_lshl_b32 s50, s53, 8\n\t" \
  "s_mov_b32 s59, 0\n\t" \
  "s_mov_b32 s92, s51\n\t" \
  "s_mov_b32 s93, s53\n\t" \
  "s_xor_b32 s76, s40, 0x400\n\t" \
  "s_xor_b32 s77, s40, 0x800\n\t" \
  "s_xor_b32 s78, s40, 0xc00\n\t" \
  "s_cmp_lg_u32 s58, 0\n\t" \
  "s_cbranch_scc1 11f\n\t" \
  "10:\n\t" \
  "ds_read_b128 v[48:51], v102 offset:32768\n\t" \
  "ds_read_b128 v[52:55], v102 offset:33792\n\t" \
  "ds_read_b128 v[56:59], v102 offset:34816\n\t" \
  "ds_read_b128 v[60:63], v102 offset:35840\n\t" \
  "ds_read_b128 v[0:3], v102 offset:49152\n\t" \
  "ds_read_b128 v[4:7], v102 offset:50176\n\t" \
  "ds_read_b128 v[8:11], v102 offset:51200\n\t" \
  "ds_read_b128 v[12:15], v102 offset:52224\n\t" \
  "ds_read_b128 v[16:19], v102 offset:0\n\t" \
  "ds_read_b128 v[20:23], v102 offset:1024\n\t" \
  "ds_read_b128 v[24:27], v102 offset:2048\n\t" \
  "ds_read_b128 v[28:31], v102 offset:3072\n\t" \
  "s_waitcnt lgkmcnt(4)\n\t" \
  "ds_read_b128 v[80:83], v102 offset:16384\n\t" \
  "ds_read_b128 v[84:87], v102 offset:17408\n\t" \
  "ds_read_b128 v[88:91], v102 offset:18432\n\t" \
  "ds_read_b128 v[92:95], v102 offset:19456\n\t" \
  "46:\n\t" \
  "s_bitcmp1_b32 s58, 1\n\t" \
  "s_cselect_b64 s[80:81], -1, 0\n\t" \
  "s_cmp_lg_u32 s58, 0\n\t" \
  "s_cbranch_scc1 17f\n\t" \
  "s_mov_b32 exec_hi, 0\n\t" \
  "16:\n\t" \
  "s_memtime vcc\n\t" \
  "s_waitcnt lgkmcnt(0)\n\t" \
  "s_sub_i32 s79, s41, vcc_lo\n\t" \
  "s_cmp_gt_i32 s79, 64\n\t" \
  "s_cbranch_scc1 16b\n\t" \
  "13:\n\t" \
  "buffer_load_dword v116, v112, %[rh], s48 offen sc1\n\t" \
  "s_waitcnt vmcnt(0)\n\t" \
  "15:\n\t" \
  "v_mov_b32 v117, 20000\n\t" \
  "v_cmp_ne_u32_sdwa vcc, v116, s49 src0_sel:BYTE_0 src1_sel:DWORD\n\t" \
  "v_subrev_u32 v116, s50, v116\n\t" \
  "v_ashrrev_i32 v116, 8, v116\n\t" \
  "s_nop 1\n\t" \
  "v_max_i32_dpp v116, v116, v116 quad_perm:[1,0,3,2] row_mask:0xf bank_mask:0xf\n\t" \
  "s_cbranch_vccnz 14f\n\t" \
  "s_nop 0\n\t" \
  "v_max_i32_dpp v116, v116, v116 quad_perm:[2,3,0,1] row_mask:0xf bank_mask:0xf\n\t" \
  "s_nop 1\n\t" \
  "v_max_i32_dpp v116, v116, v116 row_half_mirror row_mask:0xf bank_mask:0xf\n\t" \
  "s_nop 1\n\t" \
  "v_max_i32_dpp v116, v116, v116 row_mirror row_mask:0xf bank_mask:0xf\n\t" \
  "v_med3_i32 v116, v116, 0, v117\n\t" \
  "v_mul_u32_u24 v116, s52, v116\n\t" \
  "v_lshrrev_b32 v116, 16, v116\n\t" \
  "s_mov_b64 exec, -1\n\t" \
  "v_readfirstlane_b32 s84, v116\n\t" \
  "v_readlane_b32 s85, v116, 16\n\t" \
  "s_max_u32 s84, s84, s85\n\t" \
  "s_add_i32 s84, s84, s51\n\t" \
  ".p2align 6\n\t" \
  "s_nop 0\n\t" \
  "s_nop 0\n\t" \
  "s_nop 0\n\t" \
  "s_nop 0\n\t" \
  "s_nop 0\n\t" \
  "s_nop 0\n\t" \
  "s_nop 0\n\t" \
  "s_nop 0\n\t" \
  "1:\n\t" \
  "s_memtime vcc\n\t" \
  "s_waitcnt lgkmcnt(0)\n\t" \
  "s_sub_i32 s79, s84, vcc_lo\n\t" \
  "s_cmp_gt_i32 s79, 128\n\t" \
  "s_cbranch_scc1 1b\n\t" \
  ".p2align 4\n\t" \
  "2:\n\t" \
  "s_sub_i32 s79, s79, 33\n\t" \
  "s_cmp_gt_i32 s79, 56\n\t" \
  "s_cbranch_scc1 2b\n\t" \
  "buffer_load_dwordx4 v[32:35], v100, %[rh], s40 offen sc1\n\t" \
  "s_nop 12\n\t" \
  "buffer_load_dwordx4 v[36:39], v100, %[rh], s76 offen sc1\n\t" \
  "s_nop 12\n\t" \
  "buffer_load_dwordx4 v[40:43], v100, %[rh], s77 offen sc1\n\t" \
  "s_nop 12\n\t" \
  "buffer_load_dwordx4 v[44:47], v100, %[rh], s78 offen sc1\n\t" \
  "s_memtime vcc\n\t" \
  "s_memrealtime s[82:83]\n\t" \
  "s_nop 4\n\t" \
  "s_memrealtime s[84:85]\n\t" \
  "s_nop 4\n\t" \
  "s_memrealtime s[56:57]\n\t" \
  "s_nop 4\n\t" \
  "s_memrealtime s[86:87]\n\t" \
  "s_nop 4\n\t" \
  "s_memrealtime s[88:89]\n\t" \
  "s_nop 4\n\t" \
  "s_memrealtime s[90:91]\n\t" \
  "s_mov_b32 s92, 0\n\t" \
  "s_waitcnt lgkmcnt(0)\n\t" \
  "s_cmp_eq_u32 s82, s84\n\t" \
  "s_addc_u32 s92, s92, 0\n\t" \
  "s_cmp_eq_u32 s82, s56\n\t" \
  "s_addc_u32 s92, s92, 0\n\t" \
  "s_cmp_eq_u32 s82, s86\n\t" \
  "s_addc_u32 s92, s92, 0\n\t" \
  "s_cmp_eq_u32 s82, s88\n\t" \
  "s_addc_u32 s92, s92, 0\n\t" \
  "s_cmp_eq_u32 s82, s90\n\t" \
  "s_addc_u32 s92, s92, 0\n\t" \
  "s_mul_i32 s92, s92, 24\n\t" \
  "s_add_i32 s92, s92, vcc_lo\n\t" \
  "s_lshl4_add_u32 s93, s82, 64\n\t" \
  "3:\n\t" \
  "v_mov_b64 v[72:73], 0\n\t" \
  "v_mov_b64 v[74:75], 0\n\t" \
  "v_mov_b64 v[76:77], 0\n\t" \
  "v_mov_b64 v[78:79], 0\n\t" \
  "s_waitcnt lgkmcnt(0)\n\t" \
  "s_waitcnt vmcnt(3)\n\t" \
  "v_smfmac_f32_16x16x64_bf16 v[72:75], v[32:35], v[48:55], v104\n\t" \
  "ds_write_b128 v101, v[32:35] offset:8192\n\t" \
  "v_smfmac_f32_16x16x64_bf16 v[76:79], v[32:35], v[56:63], v104\n\t" \
  "s_waitcnt vmcnt(2)\n\t" \
  "ds_write_b128 v101, v[36:39] offset:9216\n\t" \
  "v_smfmac_f32_16x16x64_bf16 v[72:75], v[36:39], a[96:103], v104\n\t" \
  "v_smfmac_f32_16x16x64_bf16 v[76:79], v[36:39], a[104:111], v104\n\t" \
  "s_nop 3\n\t" \
  "v_cmp_u_f32 vcc, v72, v73\n\t" \
  "v_cmp_u_f32 s[82:83], v74, v75\n\t" \
  "s_waitcnt vmcnt(1)\n\t" \
  "v_smfmac_f32_16x16x64_bf16 v[72:75], v[40:43], a[112:119], v104\n\t" \
  "s_or_b64 vcc, vcc, s[82:83]\n\t" \
  "s_or_b64 vcc, vcc, s[80:81]\n\t" \
  "v_smfmac_f32_16x16x64_bf16 v[76:79], v[40:43], a[120:127], v104\n\t" \
  "s_waitcnt lgkmcnt(0)\n\t" \
  "s_cbranch_scc1 8f\n\t" \
  "5:\n\t" \
  "s_barrier\n\t" \
  "4:\n\t" \
  "s_memtime s[56:57]\n\t" \
  "ds_read_b128 v[48:51], v101 offset:12288\n\t" \
  "ds_read_b128 v[52:55], v101 offset:13312\n\t" \
  "ds_read_b128 v[56:59], v101 offset:1024\n\t" \
  "s_waitcnt vmcnt(0)\n\t" \
  "v_smfmac_f32_16x16x64_bf16 v[72:75], v[44:47], a[128:135], v104\n\t" \
  "ds_read_b128 v[60:63], v101 offset:0\n\t" \
  "ds_read_b128 v[64:67], v101 offset:5120\n\t" \
  "v_smfmac_f32_16x16x64_bf16 v[76:79], v[44:47], a[136:143], v104\n\t" \
  "ds_read_b128 v[68:71], v101 offset:4096\n\t" \
  "ds_write_b128 v101, v[40:43] offset:10240\n\t" \
  "s_nop 1\n\t" \
  "v_cmp_u_f32 vcc, v72, v73\n\t" \
  "v_cmp_u_f32 s[82:83], v74, v75\n\t" \
  "s_waitcnt lgkmcnt(6)\n\t" \
  "v_smfmac_f32_16x16x64_bf16 v[72:75], v[48:51], v[0:7], v104\n\t" \
  "s_or_b64 vcc, vcc, s[82:83]\n\t" \
  "v_smfmac_f32_16x16x64_bf16 v[76:79], v[48:51], v[8:15], v104\n\t" \
  "s_cbranch_scc1 9f\n\t" \
  "6:\n\t" \
  "s_waitcnt lgkmcnt(5)\n\t" \
  "v_smfmac_f32_16x16x64_bf16 v[72:75], v[52:55], a[144:151], v104\n\t" \
  "ds_write_b128 v101, v[44:47] offset:11264\n\t" \
  "v_smfmac_f32_16x16x64_bf16 v[76:79], v[52:55], a[152:159], v104\n\t" \
  "v_mov_b64 v[40:41], 0\n\t" \
  "s_waitcnt lgkmcnt(5)\n\t" \
  "v_smfmac_f32_16x16x64_bf16 v[72:75], v[56:59], a[0:7], v104\n\t" \
  "v_mov_b64 v[42:43], 0\n\t" \
  "v_smfmac_f32_16x16x64_bf16 v[76:79], v[56:59], a[8:15], v104\n\t" \
  "s_waitcnt lgkmcnt(4)\n\t" \
  "v_smfmac_f32_16x16x64_bf16 v[72:75], v[60:63], v[16:23], v104\n\t" \
  "s_waitcnt lgkmcnt(0)\n\t" \
  "s_barrier\n\t" \
  "v_smfmac_f32_16x16x64_bf16 v[76:79], v[60:63], v[24:31], v104\n\t" \
  "ds_read_b128 v[48:51], v101 offset:14336\n\t" \
  "ds_read_b128 v[52:55], v101 offset:2048\n\t" \
  "v_smfmac_f32_16x16x64_bf16 v[72:75], v[64:67], a[48:55], v104\n\t" \
  "ds_read_b128 v[56:59], v101 offset:6144\n\t" \
  "ds_read_b128 v[60:63], v101 offset:15360\n\t" \
  "v_smfmac_f32_16x16x64_bf16 v[76:79], v[64:67], a[56:63], v104\n\t" \
  "ds_read_b128 v[32:35], v101 offset:3072\n\t" \
  "ds_read_b128 v[36:39], v101 offset:7168\n\t" \
  "v_smfmac_f32_16x16x64_bf16 v[72:75], v[68:71], v[80:87], v104\n\t" \
  "ds_read_b128 v[0:3], v103 offset:0\n\t" \
  "v_mov_b64 v[44:45], 0\n\t" \
  "v_smfmac_f32_16x16x64_bf16 v[76:79], v[68:71], v[88:95], v104\n\t" \
  "v_mov_b64 v[46:47], 0\n\t" \
  "s_waitcnt lgkmcnt(5)\n\t" \
  "v_smfmac_f32_16x16x64_bf16 v[72:75], v[48:51], a[160:167], v104\n\t" \
  "ds_read_b128 v[4:7], v103 offset:1024\n\t" \
  "v_mov_b64 v[80:81], 0\n\t" \
  "v_smfmac_f32_16x16x64_bf16 v[72:75], v[52:55], a[16:23], v104\n\t" \
  "v_mov_b64 v[82:83], 0\n\t" \
  "s_waitcnt lgkmcnt(2)\n\t" \
  "v_smfmac_f32_16x16x64_bf16 v[72:75], v[56:59], a[64:71], v104\n\t" \
  "ds_read_b128 v[8:11], v103 offset:2048\n\t" \
  "v_mov_b64 v[84:85], 0\n\t" \
  "v_smfmac_f32_16x16x64_bf16 v[72:75], v[60:63], a[176:183], v104\n\t" \
  "ds_read_b128 v[12:15], v103 offset:3072\n\t" \
  "v_mov_b64 v[86:87], 0\n\t" \
  "v_smfmac_f32_16x16x64_bf16 v[72:75], v[32:35], a[32:39], v104\n\t" \
  "ds_read_b128 v[16:19], v103 offset:16384\n\t" \
  "v_mov_b64 v[88:89], 0\n\t" \
  "v_smfmac_f32_16x16x64_bf16 v[72:75], v[36:39], a[80:87], v104\n\t" \
  "ds_read_b128 v[20:23], v103 offset:17408\n\t" \
  "v_mov_b64 v[90:91], 0\n\t" \
  "v_smfmac_f32_16x16x64_bf16 v[76:79], v[48:51], a[168:175], v104\n\t" \
  "ds_read_b128 v[24:27], v103 offset:18432\n\t" \
  "v_mov_b64 v[92:93], 0\n\t" \
  "v_smfmac_f32_16x16x64_bf16 v[76:79], v[52:55], a[24:31], v104\n\t" \
  "v_add_f32 v96, v72, v74\n\t" \
  "v_add_f32 v97, v73, v75\n\t" \
  "v_cvt_pk_bf16_f32 v96, v96, v97\n\t" \
  "ds_write_b32 v105, v96\n\t" \
  "v_smfmac_f32_16x16x64_bf16 v[76:79], v[56:59], a[72:79], v104\n\t" \
  "ds_read_b128 v[28:31], v103 offset:19456\n\t" \
  "v_mov_b64 v[94:95], 0\n\t" \
  "v_smfmac_f32_16x16x64_bf16 v[76:79], v[60:63], a[184:191], v104\n\t" \
  "v_smfmac_f32_16x16x64_bf16 v[76:79], v[32:35], a[40:47], v104\n\t" \
  "v_smfmac_f32_16x16x64_bf16 v[76:79], v[36:39], a[88:95], v104\n\t" \
  "s_waitcnt lgkmcnt(1)\n\t" \
  "s_barrier\n\t" \
  "ds_read_b64_tr_b16 v[96:97], v107 offset:0\n\t" \
  "ds_read_b64_tr_b16 v[98:99], v107 offset:64\n\t" \
  "ds_read_b64_tr_b16 v[56:57], v107 offset:1024\n\t" \
  "ds_read_b64_tr_b16 v[58:59], v107 offset:1088\n\t" \
  "s_nop 1\n\t" \
  "v_add_f32 v116, v76, v78\n\t" \
  "v_add_f32 v117, v77, v79\n\t" \
  "v_cvt_pk_bf16_f32 v116, v116, v117\n\t" \
  "ds_write_b32 v105, v116 offset:256\n\t" \
  "s_waitcnt lgkmcnt(3)\n\t" \
  "v_smfmac_f32_16x16x64_bf16 v[40:43], v[96:99], a[192:199], v106\n\t" \
  "v_smfmac_f32_16x16x64_bf16 v[44:47], v[96:99], a[224:231], v106\n\t" \
  "s_waitcnt lgkmcnt(0)\n\t" \
  "s_barrier\n\t" \
  "ds_read_b64_tr_b16 v[52:53], v107 offset:256\n\t" \
  "ds_read_b64_tr_b16 v[54:55], v107 offset:320\n\t" \
  "v_smfmac_f32_16x16x64_bf16 v[40:43], v[56:59], a[208:215], v106\n\t" \
  "ds_read_b64_tr_b16 v[60:61], v107 offset:1280\n\t" \
  "ds_read_b64_tr_b16 v[62:63], v107 offset:1344\n\t" \
  "v_smfmac_f32_16x16x64_bf16 v[44:47], v[56:59], v[0:7], v106\n\t" \
  "v_smfmac_f32_16x16x64_bf16 v[80:83], v[96:99], a[240:247], v106\n\t" \
  "v_mov_b64 v[32:33], 0\n\t" \
  "v_smfmac_f32_16x16x64_bf16 v[84:87], v[96:99], v[136:143], v106\n\t" \
  "v_mov_b64 v[34:35], 0\n\t" \
  "v_smfmac_f32_16x16x64_bf16 v[80:83], v[56:59], v[120:127], v106\n\t" \
  "v_mov_b64 v[36:37], 0\n\t" \
  "v_smfmac_f32_16x16x64_bf16 v[84:87], v[56:59], v[16:23], v106\n\t" \
  "s_waitcnt lgkmcnt(2)\n\t" \
  "v_smfmac_f32_16x16x64_bf16 v[40:43], v[52:55], a[200:207], v106\n\t" \
  "v_smfmac_f32_16x16x64_bf16 v[44:47], v[52:55], a[232:239], v106\n\t" \
  "s_xor_b32 s76, s47, 0x40000\n\t" \
  "s_waitcnt lgkmcnt(0)\n\t" \
  "v_smfmac_f32_16x16x64_bf16 v[40:43], v[60:63], a[216:223], v106\n\t" \
  "v_mov_b64 v[38:39], 0\n\t" \
  "s_xor_b32 s77, s47, 0x80000\n\t" \
  "v_smfmac_f32_16x16x64_bf16 v[44:47], v[60:63], v[8:15], v106\n\t" \
  "s_xor_b32 s78, s47, 0xc0000\n\t" \
  "v_smfmac_f32_16x16x64_bf16 v[80:83], v[52:55], a[248:255], v106\n\t" \
  "ds_read_b128 v[0:3], v103 offset:32768\n\t" \
  "v_cvt_pk_bf16_f32 v64, v40, v41\n\t" \
  "v_smfmac_f32_16x16x64_bf16 v[84:87], v[52:55], v[144:151], v106\n\t" \
  "ds_read_b128 v[4:7], v103 offset:33792\n\t" \
  "v_cvt_pk_bf16_f32 v65, v42, v43\n\t" \
  "v_smfmac_f32_16x16x64_bf16 v[80:83], v[60:63], v[128:135], v106\n\t" \
  "ds_read_b128 v[8:11], v103 offset:34816\n\t" \
  "v_cvt_pk_bf16_f32 v66, v44, v45\n\t" \
  "v_smfmac_f32_16x16x64_bf16 v[84:87], v[60:63], v[24:31], v106\n\t" \
  "ds_read_b128 v[12:15], v103 offset:35840\n\t" \
  "v_cvt_pk_bf16_f32 v67, v46, v47\n\t" \
  "v_smfmac_f32_16x16x64_bf16 v[88:91], v[96:99], v[152:159], v106\n\t" \
  "v_permlane16_swap_b32 v64, v66\n\t" \
  "v_cvt_pk_bf16_f32 v68, v80, v81\n\t" \
  "v_smfmac_f32_16x16x64_bf16 v[92:95], v[96:99], v[184:191], v106\n\t" \
  "v_permlane16_swap_b32 v65, v67\n\t" \
  "v_cvt_pk_bf16_f32 v69, v82, v83\n\t" \
  "v_smfmac_f32_16x16x64_bf16 v[88:91], v[52:55], v[160:167], v106\n\t" \
  "v_cvt_pk_bf16_f32 v70, v84, v85\n\t" \
  "v_smfmac_f32_16x16x64_bf16 v[92:95], v[52:55], v[192:199], v106\n\t" \
  "buffer_store_dwordx4 v[64:67], v108, %[ru], s47 offen sc1\n\t" \
  "v_cvt_pk_bf16_f32 v71, v86, v87\n\t" \
  "v_smfmac_f32_16x16x64_bf16 v[88:91], v[56:59], v[168:175], v106\n\t" \
  "ds_read_b128 v[16:19], v103 offset:49152\n\t" \
  "v_permlane16_swap_b32 v68, v70\n\t" \
  "s_waitcnt lgkmcnt(3)\n\t" \
  "v_smfmac_f32_16x16x64_bf16 v[92:95], v[56:59], v[0:7], v106\n\t" \
  "ds_read_b128 v[20:23], v103 offset:50176\n\t" \
  "v_permlane16_swap_b32 v69, v71\n\t" \
  "v_smfmac_f32_16x16x64_bf16 v[88:91], v[60:63], v[176:183], v106\n\t" \
  "ds_read_b128 v[24:27], v103 offset:51200\n\t" \
  "s_waitcnt lgkmcnt(3)\n\t" \
  "v_smfmac_f32_16x16x64_bf16 v[92:95], v[60:63], v[8:15], v106\n\t" \
  "ds_read_b128 v[28:31], v103 offset:52224\n\t" \
  "buffer_store_dwordx4 v[68:71], v108, %[ru], s76 offen sc1\n\t" \
  "v_smfmac_f32_16x16x64_bf16 v[32:35], v[96:99], v[200:207], v106\n\t" \
  "v_cvt_pk_bf16_f32 v72, v88, v89\n\t" \
  "v_smfmac_f32_16x16x64_bf16 v[36:39], v[96:99], v[232:239], v106\n\t" \
  "v_cvt_pk_bf16_f32 v73, v90, v91\n\t" \
  "v_smfmac_f32_16x16x64_bf16 v[32:35], v[52:55], v[208:215], v106\n\t" \
  "v_cvt_pk_bf16_f32 v74, v92, v93\n\t" \
  "v_smfmac_f32_16x16x64_bf16 v[36:39], v[52:55], v[240:247], v106\n\t" \
  "v_cvt_pk_bf16_f32 v75, v94, v95\n\t" \
  "v_smfmac_f32_16x16x64_bf16 v[32:35], v[56:59], v[216:223], v106\n\t" \
  "v_permlane16_swap_b32 v72, v74\n\t" \
  "v_smfmac_f32_16x16x64_bf16 v[32:35], v[60:63], v[224:231], v106\n\t" \
  "v_permlane16_swap_b32 v73, v75\n\t" \
  "s_waitcnt lgkmcnt(2)\n\t" \
  "v_smfmac_f32_16x16x64_bf16 v[36:39], v[56:59], v[16:23], v106\n\t" \
  "s_waitcnt lgkmcnt(0)\n\t" \
  "v_smfmac_f32_16x16x64_bf16 v[36:39], v[60:63], v[24:31], v106\n\t" \
  "buffer_store_dwordx4 v[72:75], v108, %[ru], s77 offen sc1\n\t" \
  "v_cvt_pk_bf16_f32 v76, v32, v33\n\t" \
  "v_cvt_pk_bf16_f32 v77, v34, v35\n\t" \
  "s_nop 5\n\t" \
  "v_cvt_pk_bf16_f32 v78, v36, v37\n\t" \
  "v_cvt_pk_bf16_f32 v79, v38, v39\n\t" \
  "s_nop 0\n\t" \
  "v_permlane16_swap_b32 v76, v78\n\t" \
  "v_permlane16_swap_b32 v77, v79\n\t" \
  "buffer_store_dwordx4 v[76:79], v108, %[ru], s78 offen sc1\n\t" \
  "s_memtime s[76:77]\n\t" \
  "s_mov_b32 s42, 0\n\t" \
  "s_waitcnt lgkmcnt(0)\n\t" \
  "s_mov_b32 s57, s76\n\t" \
  "s_add_i32 s41, s56, 1950\n\t" \
  "s_sub_i32 s76, s92, s51\n\t" \
  "s_sub_i32 s77, s93, s53\n\t" \
  "s_cmp_eq_u32 s76, 0\n\t" \
  "s_cbranch_scc1 21f\n\t" \
  "v_cvt_f32_u32 v117, s76\n\t" \
  "v_cvt_f32_u32 v116, s77\n\t" \
  "v_rcp_f32 v117, v117\n\t" \
  "s_mov_b32 s51, s92\n\t" \
  "v_mul_f32 v117, v117, v116\n\t" \
  "v_max_f32 v117, 0x3f028f5c, v117\n\t" \
  "v_min_f32 v117, 0x3ffeb852, v117\n\t" \
  "v_rcp_f32 v116, v117\n\t" \
  "v_mul_f32 v117, 0x4f000000, v117\n\t" \
  "v_mul_f32 v116, 0x47800000, v116\n\t" \
  "v_cvt_u32_f32 v117, v117\n\t" \
  "v_cvt_u32_f32 v116, v116\n\t" \
  "s_mov_b32 s53, s93\n\t" \
  "v_readfirstlane_b32 s46, v117\n\t" \
  "v_readfirstlane_b32 s52, v116\n\t" \
  "21:\n\t" \
  "s_add_i32 s44, s44, 1\n\t" \
  "s_and_b32 s58, s58, 4\n\t" \
  "s_and_b32 s76, s44, 64\n\t" \
  "s_or_b32 s42, s42, s76\n\t" \
  "s_cbranch_scc0 20b\n\t" \
  "s_branch 99f\n\t" \
  "14:\n\t" \
  "s_sleep 3\n\t" \
  "s_add_u32 s59, s59, 0x10000\n\t" \
  "s_cmp_lt_u32 s59, 0x100000\n\t" \
  "s_cbranch_scc1 13b\n\t" \
  "s_mov_b64 exec, -1\n\t" \
  "s_mov_b32 s42, 16\n\t" \
  "s_branch 99f\n\t" \
  "8:\n\t" \
  "s_bitcmp1_b32 s58, 1\n\t" \
  "s_cbranch_scc1 4b\n\t" \
  "s_bitcmp1_b32 s58, 2\n\t" \
  "s_cbranch_scc1 5b\n\t" \
  "v_max3_u32 v116, v32, v33, v34\n\t" \
  "v_max3_u32 v117, v35, v36, v37\n\t" \
  "v_max3_u32 v116, v116, v38, v39\n\t" \
  "v_max_u32 v116, v116, v117\n\t" \
  "v_cmp_eq_u32 vcc, -1, v116\n\t" \
  "s_cbranch_vccz 5b\n\t" \
  "s_branch 0f\n\t" \
  "9:\n\t" \
  "s_bitcmp1_b32 s58, 2\n\t" \
  "s_cbranch_scc1 6b\n\t" \
  "v_max3_u32 v116, v40, v41, v42\n\t" \
  "v_max3_u32 v117, v43, v44, v45\n\t" \
  "v_max3_u32 v116, v116, v46, v47\n\t" \
  "v_max_u32 v116, v116, v117\n\t" \
  "v_cmp_eq_u32 vcc, -1, v116\n\t" \
  "s_cbranch_vccz 6b\n\t" \
  "s_or_b32 s58, s58, 2\n\t" \
  "0:\n\t" \
  "s_or_b32 s58, s58, 1\n\t" \
  "11:\n\t" \
  "buffer_load_dwordx4 v[32:35], v100, %[rh], s40 offen sc1\n\t" \
  "buffer_load_dwordx4 v[36:39], v100, %[rh], s76 offen sc1\n\t" \
  "buffer_load_dwordx4 v[40:43], v100, %[rh], s77 offen sc1\n\t" \
  "buffer_load_dwordx4 v[44:47], v100, %[rh], s78 offen sc1\n\t" \
  "s_waitcnt lgkmcnt(0)\n\t" \
  "s_branch 10b\n\t" \
  "17:\n\t" \
  "s_add_u32 s59, s59, 1\n\t" \
  "s_bitcmp1_b32 s58, 2\n\t" \
  "s_cbranch_scc1 3b\n\t" \
  "s_waitcnt vmcnt(0)\n\t" \
  "v_max_u32 v116, v32, v33\n\t" \
  "v_max3_u32 v116, v116, v34, v35\n\t" \
  "v_max3_u32 v116, v116, v36, v37\n\t" \
  "v_max3_u32 v116, v116, v38, v39\n\t" \
  "v_max3_u32 v116, v116, v40, v41\n\t" \
  "v_max3_u32 v116, v116, v42, v43\n\t" \
  "v_max3_u32 v116, v116, v44, v45\n\t" \
  "v_max3_u32 v116, v116, v46, v47\n\t" \
  "v_cmp_eq_u32 vcc, -1, v116\n\t" \
  "s_cbranch_vccz 3b\n\t" \
  "s_bitcmp1_b32 s59, 6\n\t" \
  "s_cbranch_scc1 12f\n\t" \
  "buffer_load_dwordx4 v[32:35], v100, %[rh], s40 offen sc1\n\t" \
  "buffer_load_dwordx4 v[36:39], v100, %[rh], s76 offen sc1\n\t" \
  "buffer_load_dwordx4 v[40:43], v100, %[rh], s77 offen sc1\n\t" \
  "buffer_load_dwordx4 v[44:47], v100, %[rh], s78 offen sc1\n\t" \
  "s_branch 17b\n\t" \
  "12:\n\t" \
  "s_mov_b32 s42, s58\n\t" \
  "s_branch 99f\n\t" \
  "93:\n\t" \
  "s_mov_b32 s58, s42\n\t" \
  "20:\n\t" \
  "s_and_b32 s76, s44, 7\n\t" \
  "s_lshl_b32 s76, s76, 15\n\t" \
  "s_add_i32 s40, s54, s76\n\t" \
  "s_and_b32 s76, s44, 3\n\t" \
  "s_lshl_b32 s45, s76, 14\n\t" \
  "s_lshl_b32 s47, s76, 20\n\t" \
  "s_add_i32 s47, s47, s55\n\t" \
  "s_lshr_b32 s76, s47, 21\n\t" \
  "s_bitcmp1_b64 %[sel], s76\n\t" \
  "s_cselect_b32 s76, 0x2000, 0\n\t" \
  "s_add_i32 s47, s47, s76\n\t" \
  "s_add_i32 s49, s44, -1\n\t" \
  "s_and_b32 s48, s49, 2\n\t" \
  "s_lshl_b32 s48, s48, 13\n\t" \
  "s_and_b32 s76, s49, 1\n\t" \
  "s_lshl_b32 s76, s76, 12\n\t" \
  "s_add_i32 s48, s48, s76\n\t" \
  "s_lshl_b32 s50, s53, 8\n\t" \
  "s_mov_b32 s59, 0\n\t" \
  "s_mov_b32 s92, s51\n\t" \
  "s_mov_b32 s93, s53\n\t" \
  "s_xor_b32 s76, s40, 0x400\n\t" \
  "s_xor_b32 s77, s40, 0x800\n\t" \
  "s_xor_b32 s78, s40, 0xc00\n\t" \
  "s_cmp_lg_u32 s58, 0\n\t" \
  "s_cbranch_scc1 11f\n\t" \
  "10:\n\t" \
  "ds_read_b128 v[48:51], v102 offset:49152\n\t" \
  "ds_read_b128 v[52:55], v102 offset:50176\n\t" \
  "ds_read_b128 v[56:59], v102 offset:51200\n\t" \
  "ds_read_b128 v[60:63], v102 offset:52224\n\t" \
  "ds_read_b128 v[0:3], v102 offset:0\n\t" \
  "ds_read_b128 v[4:7], v102 offset:1024\n\t" \
  "ds_read_b128 v[8:11], v102 offset:2048\n\t" \
  "ds_read_b128 v[12:15], v102 offset:3072\n\t" \
  "ds_read_b128 v[16:19], v102 offset:16384\n\t" \
  "ds_read_b128 v[20:23], v102 offset:17408\n\t" \
  "ds_read_b128 v[24:27], v102 offset:18432\n\t" \
  "ds_read_b128 v[28:31], v102 offset:19456\n\t" \
  "s_waitcnt lgkmcnt(4)\n\t" \
  "ds_read_b128 v[80:83], v102 offset:32768\n\t" \
  "ds_read_b128 v[84:87], v102 offset:33792\n\t" \
  "ds_read_b128 v[88:91], v102 offset:34816\n\t" \
  "ds_read_b128 v[92:95], v102 offset:35840\n\t" \
  "46:\n\t" \
  "s_bitcmp1_b32 s58, 1\n\t" \
  "s_cselect_b64 s[80:81], -1, 0\n\t" \
  "s_cmp_lg_u32 s58, 0\n\t" \
  "s_cbranch_scc1 17f\n\t" \
  "s_mov_b32 exec_hi, 0\n\t" \
  "16:\n\t" \
  "s_memtime vcc\n\t" \
  "s_waitcnt lgkmcnt(0)\n\t" \
  "s_sub_i32 s79, s41, vcc_lo\n\t" \
  "s_cmp_gt_i32 s79, 64\n\t" \
  "s_cbranch_scc1 16b\n\t" \
  "13:\n\t" \
  "buffer_load_dword v116, v112, %[rh], s48 offen sc1\n\t" \
  "s_waitcnt vmcnt(0)\n\t" \
  "15:\n\t" \
  "v_mov_b32 v117, 20000\n\t" \
  "v_cmp_ne_u32_sdwa vcc, v116, s49 src0_sel:BYTE_0 src1_sel:DWORD\n\t" \
  "v_subrev_u32 v116, s50, v116\n\t" \
  "v_ashrrev_i32 v116, 8, v116\n\t" \
  "s_nop 1\n\t" \
  "v_max_i32_dpp v116, v116, v116 quad_perm:[1,0,3,2] row_mask:0xf bank_mask:0xf\n\t" \
  "s_cbranch_vccnz 14f\n\t" \
  "s_nop 0\n\t" \
  "v_max_i32_dpp v116, v116, v116 quad_perm:[2,3,0,1] row_mask:0xf bank_mask:0xf\n\t" \
  "s_nop 1\n\t" \
  "v_max_i32_dpp v116, v116, v116 row_half_mirror row_mask:0xf bank_mask:0xf\n\t" \
  "s_nop 1\n\t" \
  "v_max_i32_dpp v116, v116, v116 row_mirror row_mask:0xf bank_mask:0xf\n\t" \
  "v_med3_i32 v116, v116, 0, v117\n\t" \
  "v_mul_u32_u24 v116, s52, v116\n\t" \
  "v_lshrrev_b32 v116, 16, v116\n\t" \
  "s_mov_b64 exec, -1\n\t" \
  "v_readfirstlane_b32 s84, v116\n\t" \
  "v_readlane_b32 s85, v116, 16\n\t" \
  "s_max_u32 s84, s84, s85\n\t" \
  "s_add_i32 s84, s84, s51\n\t" \
  ".p2align 6\n\t" \
  "s_nop 0\n\t" \
  "s_nop 0\n\t" \
  "s_nop 0\n\t" \
  "s_nop 0\n\t" \
  "s_nop 0\n\t" \
  "s_nop 0\n\t" \
  "s_nop 0\n\t" \
  "s_nop 0\n\t" \
  "1:\n\t" \
  "s_memtime vcc\n\t" \
  "s_waitcnt lgkmcnt(0)\n\t" \
  "s_sub_i32 s79, s84, vcc_lo\n\t" \
  "s_cmp_gt_i32 s79, 128\n\t" \
  "s_cbranch_scc1 1b\n\t" \
  ".p2align 4\n\t" \
  "2:\n\t" \
  "s_sub_i32 s79, s79, 33\n\t" \
  "s_cmp_gt_i32 s79, 56\n\t" \
  "s_cbranch_scc1 2b\n\t" \
  "buffer_load_dwordx4 v[32:35], v100, %[rh], s40 offen sc1\n\t" \
  "s_nop 12\n\t" \
  "buffer_load_dwordx4 v[36:39], v100, %[rh], s76 offen sc1\n\t" \
  "s_nop 12\n\t" \
  "buffer_load_dwordx4 v[40:43], v100, %[rh], s77 offen sc1\n\t" \
  "s_nop 12\n\t" \
  "buffer_load_dwordx4 v[44:47], v100, %[rh], s78 offen sc1\n\t" \
  "s_memtime vcc\n\t" \
  "s_memrealtime s[82:83]\n\t" \
  "s_nop 4\n\t" \
  "s_memrealtime s[84:85]\n\t" \
  "s_nop 4\n\t" \
  "s_memrealtime s[56:57]\n\t" \
  "s_nop 4\n\t" \
  "s_memrealtime s[86:87]\n\t" \
  "s_nop 4\n\t" \
  "s_memrealtime s[88:89]\n\t" \
  "s_nop 4\n\t" \
  "s_memrealtime s[90:91]\n\t" \
  "s_mov_b32 s92, 0\n\t" \
  "s_waitcnt lgkmcnt(0)\n\t" \
  "s_cmp_eq_u32 s82, s84\n\t" \
  "s_addc_u32 s92, s92, 0\n\t" \
  "s_cmp_eq_u32 s82, s56\n\t" \
  "s_addc_u32 s92, s92, 0\n\t" \
  "s_cmp_eq_u32 s82, s86\n\t" \
  "s_addc_u32 s92, s92, 0\n\t" \
  "s_cmp_eq_u32 s82, s88\n\t" \
  "s_addc_u32 s92, s92, 0\n\t" \
  "s_cmp_eq_u32 s82, s90\n\t" \
  "s_addc_u32 s92, s92, 0\n\t" \
  "s_mul_i32 s92, s92, 24\n\t" \
  "s_add_i32 s92, s92, vcc_lo\n\t" \
  "s_lshl4_add_u32 s93, s82, 64\n\t" \
  "3:\n\t" \
  "v_mov_b64 v[72:73], 0\n\t" \
  "v_mov_b64 v[74:75], 0\n\t" \
  "v_mov_b64 v[76:77], 0\n\t" \
  "v_mov_b64 v[78:79], 0\n\t" \
  "s_waitcnt lgkmcnt(0)\n\t" \
  "s_waitcnt vmcnt(3)\n\t" \
  "v_smfmac_f32_16x16x64_bf16 v[72:75], v[32:35], v[48:55], v104\n\t" \
  "ds_write_b128 v101, v[32:35] offset:12288\n\t" \
  "v_smfmac_f32_16x16x64_bf16 v[76:79], v[32:35], v[56:63], v104\n\t" \
  "s_waitcnt vmcnt(2)\n\t" \
  "ds_write_b128 v101, v[36:39] offset:13312\n\t" \
  "v_smfmac_f32_16x16x64_bf16 v[72:75], v[36:39], a[144:151], v104\n\t" \
  "v_smfmac_f32_16x16x64_bf16 v[76:79], v[36:39], a[152:159], v104\n\t" \
  "s_nop 3\n\t" \
  "v_cmp_u_f32 vcc, v72, v73\n\t" \
  "v_cmp_u_f32 s[82:83], v74, v75\n\t" \
  "s_waitcnt vmcnt(1)\n\t" \
  "v_smfmac_f32_16x16x64_bf16 v[72:75], v[40:43], a[160:167], v104\n\t" \
  "s_or_b64 vcc, vcc, s[82:83]\n\t" \
  "s_or_b64 vcc, vcc, s[80:81]\n\t" \
  "v_smfmac_f32_16x16x64_bf16 v[76:79], v[40:43], a[168:175], v104\n\t" \
  "s_waitcnt lgkmcnt(0)\n\t" \
  "s_cbranch_scc1 8f\n\t" \
  "5:\n\t" \
  "s_barrier\n\t" \
  "4:\n\t" \
  "s_memtime s[56:57]\n\t" \
  "ds_read_b128 v[48:51], v101 offset:0\n\t" \
  "ds_read_b128 v[52:55], v101 offset:1024\n\t" \
  "ds_read_b128 v[56:59], v101 offset:5120\n\t" \
  "s_waitcnt vmcnt(0)\n\t" \
  "v_smfmac_f32_16x16x64_bf16 v[72:75], v[44:47], a[176:183], v104\n\t" \
  "ds_read_b128 v[60:63], v101 offset:4096\n\t" \
  "ds_read_b128 v[64:67], v101 offset:9216\n\t" \
  "v_smfmac_f32_16x16x64_bf16 v[76:79], v[44:47], a[184:191], v104\n\t" \
  "ds_read_b128 v[68:71], v101 offset:8192\n\t" \
  "ds_write_b128 v101, v[40:43] offset:14336\n\t" \
  "s_nop 1\n\t" \
  "v_cmp_u_f32 vcc, v72, v73\n\t" \
  "v_cmp_u_f32 s[82:83], v74, v75\n\t" \
  "s_waitcnt lgkmcnt(6)\n\t" \
  "v_smfmac_f32_16x16x64_bf16 v[72:75], v[48:51], v[0:7], v104\n\t" \
  "s_or_b64 vcc, vcc, s[82:83]\n\t" \
  "v_smfmac_f32_16x16x64_bf16 v[76:79], v[48:51], v[8:15], v104\n\t" \
  "s_cbranch_scc1 9f\n\t" \
  "6:\n\t" \
  "s_waitcnt lgkmcnt(5)\n\t" \
  "v_smfmac_f32_16x16x64_bf16 v[72:75], v[52:55], a[0:7], v104\n\t" \
  "ds_write_b128 v101, v[44:47] offset:15360\n\t" \
  "v_smfmac_f32_16x16x64_bf16 v[76:79], v[52:55], a[8:15], v104\n\t" \
  "v_mov_b64 v[40:41], 0\n\t" \
  "s_waitcnt lgkmcnt(5)\n\t" \
  "v_smfmac_f32_16x16x64_bf16 v[72:75], v[56:59], a[48:55], v104\n\t" \
  "v_mov_b64 v[42:43], 0\n\t" \
  "v_smfmac_f32_16x16x64_bf16 v[76:79], v[56:59], a[56:63], v104\n\t" \
  "s_waitcnt lgkmcnt(4)\n\t" \
  "v_smfmac_f32_16x16x64_bf16 v[72:75], v[60:63], v[16:23], v104\n\t" \
  "s_waitcnt lgkmcnt(0)\n\t" \
  "s_barrier\n\t" \
  "v_smfmac_f32_16x16x64_bf16 v[76:79], v[60:63], v[24:31], v104\n\t" \
  "ds_read_b128 v[48:51], v101 offset:2048\n\t" \
  "ds_read_b128 v[52:55], v101 offset:6144\n\t" \
  "v_smfmac_f32_16x16x64_bf16 v[72:75], v[64:67], a[96:103], v104\n\t" \
  "ds_read_b128 v[56:59], v101 offset:10240\n\t" \
  "ds_read_b128 v[60:63], v101 offset:3072\n\t" \
  "v_smfmac_f32_16x16x64_bf16 v[76:79], v[64:67], a[104:111], v104\n\t" \
  "ds_read_b128 v[32:35], v101 offset:7168\n\t" \
  "ds_read_b128 v[36:39], v101 offset:11264\n\t" \
  "v_smfmac_f32_16x16x64_bf16 v[72:75], v[68:71], v[80:87], v104\n\t" \
  "ds_read_b128 v[0:3], v103 offset:0\n\t" \
  "v_mov_b64 v[44:45], 0\n\t" \
  "v_smfmac_f32_16x16x64_bf16 v[76:79], v[68:71], v[88:95], v104\n\t" \
  "v_mov_b64 v[46:47], 0\n\t" \
  "s_waitcnt lgkmcnt(5)\n\t" \
  "v_smfmac_f32_16x16x64_bf16 v[72:75], v[48:51], a[16:23], v104\n\t" \
  "ds_read_b128 v[4:7], v103 offset:1024\n\t" \
  "v_mov_b64 v[80:81], 0\n\t" \
  "v_smfmac_f32_16x16x64_bf16 v[72:75], v[52:55], a[64:71], v104\n\t" \
  "v_mov_b64 v[82:83], 0\n\t" \
  "s_waitcnt lgkmcnt(2)\n\t" \
  "v_smfmac_f32_16x16x64_bf16 v[72:75], v[56:59], a[112:119], v104\n\t" \
  "ds_read_b128 v[8:11], v103 offset:2048\n\t" \
  "v_mov_b64 v[84:85], 0\n\t" \
  "v_smfmac_f32_16x16x64_bf16 v[72:75], v[60:63], a[32:39], v104\n\t" \
  "ds_read_b128 v[12:15], v103 offset:3072\n\t" \
  "v_mov_b64 v[86:87], 0\n\t" \
  "v_smfmac_f32_16x16x64_bf16 v[72:75], v[32:35], a[80:87], v104\n\t" \
  "ds_read_b128 v[16:19], v103 offset:16384\n\t" \
  "v_mov_b64 v[88:89], 0\n\t" \
  "v_smfmac_f32_16x16x64_bf16 v[72:75], v[36:39], a[128:135], v104\n\t" \
  "ds_read_b128 v[20:23], v103 offset:17408\n\t" \
  "v_mov_b64 v[90:91], 0\n\t" \
  "v_smfmac_f32_16x16x64_bf16 v[76:79], v[48:51], a[24:31], v104\n\t" \
  "ds_read_b128 v[24:27], v103 offset:18432\n\t" \
  "v_mov_b64 v[92:93], 0\n\t" \
  "v_smfmac_f32_16x16x64_bf16 v[76:79], v[52:55], a[72:79], v104\n\t" \
  "v_add_f32 v96, v72, v74\n\t" \
  "v_add_f32 v97, v73, v75\n\t" \
  "v_cvt_pk_bf16_f32 v96, v96, v97\n\t" \
  "ds_write_b32 v105, v96\n\t" \
  "v_smfmac_f32_16x16x64_bf16 v[76:79], v[56:59], a[120:127], v104\n\t" \
  "ds_read_b128 v[28:31], v103 offset:19456\n\t" \
  "v_mov_b64 v[94:95], 0\n\t" \
  "v_smfmac_f32_16x16x64_bf16 v[76:79], v[60:63], a[40:47], v104\n\t" \
  "v_smfmac_f32_16x16x64_bf16 v[76:79], v[32:35], a[88:95], v104\n\t" \
  "v_smfmac_f32_16x16x64_bf16 v[76:79], v[36:39], a[136:143], v104\n\t" \
  "s_waitcnt lgkmcnt(1)\n\t" \
  "s_barrier\n\t" \
  "ds_read_b64_tr_b16 v[96:97], v107 offset:0\n\t" \
  "ds_read_b64_tr_b16 v[98:99], v107 offset:64\n\t" \
  "ds_read_b64_tr_b16 v[56:57], v107 offset:1024\n\t" \
  "ds_read_b64_tr_b16 v[58:59], v107 offset:1088\n\t" \
  "s_nop 1\n\t" \
  "v_add_f32 v116, v76, v78\n\t" \
  "v_add_f32 v117, v77, v79\n\t" \
  "v_cvt_pk_bf16_f32 v116, v116, v117\n\t" \
  "ds_write_b32 v105, v116 offset:256\n\t" \
  "s_waitcnt lgkmcnt(3)\n\t" \
  "v_smfmac_f32_16x16x64_bf16 v[40:43], v[96:99], a[192:199], v106\n\t" \
  "v_smfmac_f32_16x16x64_bf16 v[44:47], v[96:99], a[224:231], v106\n\t" \
  "s_waitcnt lgkmcnt(0)\n\t" \
  "s_barrier\n\t" \
  "ds_read_b64_tr_b16 v[52:53], v107 offset:256\n\t" \
  "ds_read_b64_tr_b16 v[54:55], v107 offset:320\n\t" \
  "v_smfmac_f32_16x16x64_bf16 v[40:43], v[56:59], a[208:215], v106\n\t" \
  "ds_read_b64_tr_b16 v[60:61], v107 offset:1280\n\t" \
  "ds_read_b64_tr_b16 v[62:63], v107 offset:1344\n\t" \
  "v_smfmac_f32_16x16x64_bf16 v[44:47], v[56:59], v[0:7], v106\n\t" \
  "v_smfmac_f32_16x16x64_bf16 v[80:83], v[96:99], a[240:247], v106\n\t" \
  "v_mov_b64 v[32:33], 0\n\t" \
  "v_smfmac_f32_16x16x64_bf16 v[84:87], v[96:99], v[136:143], v106\n\t" \
  "v_mov_b64 v[34:35], 0\n\t" \
  "v_smfmac_f32_16x16x64_bf16 v[80:83], v[56:59], v[120:127], v106\n\t" \
  "v_mov_b64 v[36:37], 0\n\t" \
  "v_smfmac_f32_16x16x64_bf16 v[84:87], v[56:59], v[16:23], v106\n\t" \
  "s_waitcnt lgkmcnt(2)\n\t" \
  "v_smfmac_f32_16x16x64_bf16 v[40:43], v[52:55], a[200:207], v106\n\t" \
  "v_smfmac_f32_16x16x64_bf16 v[44:47], v[52:55], a[232:239], v106\n\t" \
  "s_xor_b32 s76, s47, 0x40000\n\t" \
  "s_waitcnt lgkmcnt(0)\n\t" \
  "v_smfmac_f32_16x16x64_bf16 v[40:43], v[60:63], a[216:223], v106\n\t" \
  "v_mov_b64 v[38:39], 0\n\t" \
  "s_xor_b32 s77, s47, 0x80000\n\t" \
  "v_smfmac_f32_16x16x64_bf16 v[44:47], v[60:63], v[8:15], v106\n\t" \
  "s_xor_b32 s78, s47, 0xc0000\n\t" \
  "v_smfmac_f32_16x16x64_bf16 v[80:83], v[52:55], a[248:255], v106\n\t" \
  "ds_read_b128 v[0:3], v103 offset:32768\n\t" \
  "v_cvt_pk_bf16_f32 v64, v40, v41\n\t" \
  "v_smfmac_f32_16x16x64_bf16 v[84:87], v[52:55], v[144:151], v106\n\t" \
  "ds_read_b128 v[4:7], v103 offset:33792\n\t" \
  "v_cvt_pk_bf16_f32 v65, v42, v43\n\t" \
  "v_smfmac_f32_16x16x64_bf16 v[80:83], v[60:63], v[128:135], v106\n\t" \
  "ds_read_b128 v[8:11], v103 offset:34816\n\t" \
  "v_cvt_pk_bf16_f32 v66, v44, v45\n\t" \
  "v_smfmac_f32_16x16x64_bf16 v[84:87], v[60:63], v[24:31], v106\n\t" \
  "ds_read_b128 v[12:15], v103 offset:35840\n\t" \
  "v_cvt_pk_bf16_f32 v67, v46, v47\n\t" \
  "v_smfmac_f32_16x16x64_bf16 v[88:91], v[96:99], v[152:159], v106\n\t" \
  "v_permlane16_swap_b32 v64, v66\n\t" \
  "v_cvt_pk_bf16_f32 v68, v80, v81\n\t" \
  "v_smfmac_f32_16x16x64_bf16 v[92:95], v[96:99], v[184:191], v106\n\t" \
  "v_permlane16_swap_b32 v65, v67\n\t" \
  "v_cvt_pk_bf16_f32 v69, v82, v83\n\t" \
  "v_smfmac_f32_16x16x64_bf16 v[88:91], v[52:55], v[160:167], v106\n\t" \
  "v_cvt_pk_bf16_f32 v70, v84, v85\n\t" \
  "v_smfmac_f32_16x16x64_bf16 v[92:95], v[52:55], v[192:199], v106\n\t" \
  "v_cvt_pk_bf16_f32 v71, v86, v87\n\t" \
  "v_smfmac_f32_16x16x64_bf16 v[88:91], v[56:59], v[168:175], v106\n\t" \
  "ds_read_b128 v[16:19], v103 offset:49152\n\t" \
  "buffer_store_dwordx4 v[64:67], v108, %[ru], s47 offen sc1\n\t" \
  "v_permlane16_swap_b32 v68, v70\n\t" \
  "s_waitcnt lgkmcnt(3)\n\t" \
  "v_smfmac_f32_16x16x64_bf16 v[92:95], v[56:59], v[0:7], v106\n\t" \
  "ds_read_b128 v[20:23], v103 offset:50176\n\t" \
  "v_permlane16_swap_b32 v69, v71\n\t" \
  "v_smfmac_f32_16x16x64_bf16 v[88:91], v[60:63], v[176:183], v106\n\t" \
  "ds_read_b128 v[24:27], v103 offset:51200\n\t" \
  "s_waitcnt lgkmcnt(3)\n\t" \
  "v_smfmac_f32_16x16x64_bf16 v[92:95], v[60:63], v[8:15], v106\n\t" \
  "ds_read_b128 v[28:31], v103 offset:52224\n\t" \
  "v_smfmac_f32_16x16x64_bf16 v[32:35], v[96:99], v[200:207], v106\n\t" \
  "buffer_store_dwordx4 v[68:71], v108, %[ru], s76 offen sc1\n\t" \
  "v_cvt_pk_bf16_f32 v72, v88, v89\n\t" \
  "v_smfmac_f32_16x16x64_bf16 v[36:39], v[96:99], v[232:239], v106\n\t" \
  "v_cvt_pk_bf16_f32 v73, v90, v91\n\t" \
  "v_smfmac_f32_16x16x64_bf16 v[32:35], v[52:55], v[208:215], v106\n\t" \
  "v_cvt_pk_bf16_f32 v74, v92, v93\n\t" \
  "v_smfmac_f32_16x16x64_bf16 v[36:39], v[52:55], v[240:247], v106\n\t" \
  "v_cvt_pk_bf16_f32 v75, v94, v95\n\t" \
  "v_smfmac_f32_16x16x64_bf16 v[32:35], v[56:59], v[216:223], v106\n\t" \
  "v_permlane16_swap_b32 v72, v74\n\t" \
  "v_smfmac_f32_16x16x64_bf16 v[32:35], v[60:63], v[224:231], v106\n\t" \
  "v_permlane16_swap_b32 v73, v75\n\t" \
  "s_waitcnt lgkmcnt(2)\n\t" \
  "v_smfmac_f32_16x16x64_bf16 v[36:39], v[56:59], v[16:23], v106\n\t" \
  "s_waitcnt lgkmcnt(0)\n\t" \
  "v_smfmac_f32_16x16x64_bf16 v[36:39], v[60:63], v[24:31], v106\n\t" \
  "buffer_store_dwordx4 v[72:75], v108, %[ru], s77 offen sc1\n\t" \
  "v_cvt_pk_bf16_f32 v76, v32, v33\n\t" \
  "v_cvt_pk_bf16_f32 v77, v34, v35\n\t" \
  "s_nop 5\n\t" \
  "v_cvt_pk_bf16_f32 v78, v36, v37\n\t" \
  "v_cvt_pk_bf16_f32 v79, v38, v39\n\t" \
  "s_nop 0\n\t" \
  "v_permlane16_swap_b32 v76, v78\n\t" \
  "v_permlane16_swap_b32 v77, v79\n\t" \
  "buffer_store_dwordx4 v[76:79], v108, %[ru], s78 offen sc1\n\t" \
  "s_memtime s[76:77]\n\t" \
  "s_mov_b32 s42, 0\n\t" \
  "s_waitcnt lgkmcnt(0)\n\t" \
  "s_mov_b32 s57, s76\n\t" \
  "s_add_i32 s41, s56, 1950\n\t" \
  "s_sub_i32 s76, s92, s51\n\t" \
  "s_sub_i32 s77, s93, s53\n\t" \
  "s_cmp_eq_u32 s76, 0\n\t" \
  "s_cbranch_scc1 21f\n\t" \
  "v_cvt_f32_u32 v117, s76\n\t" \
  "v_cvt_f32_u32 v116, s77\n\t" \
  "v_rcp_f32 v117, v117\n\t" \
  "s_mov_b32 s51, s92\n\t" \
  "v_mul_f32 v117, v117, v116\n\t" \
  "v_max_f32 v117, 0x3f028f5c, v117\n\t" \
  "v_min_f32 v117, 0x3ffeb852, v117\n\t" \
  "v_rcp_f32 v116, v117\n\t" \
  "v_mul_f32 v117, 0x4f000000, v117\n\t" \
  "v_mul_f32 v116, 0x47800000, v116\n\t" \
  "v_cvt_u32_f32 v117, v117\n\t" \
  "v_cvt_u32_f32 v116, v116\n\t" \
  "s_mov_b32 s53, s93\n\t" \
  "v_readfirstlane_b32 s46, v117\n\t" \
  "v_readfirstlane_b32 s52, v116\n\t" \
  "21:\n\t" \
  "s_add_i32 s44, s44, 1\n\t" \
  "s_and_b32 s58, s58, 4\n\t" \
  "s_and_b32 s76, s44, 64\n\t" \
  "s_or_b32 s42, s42, s76\n\t" \
  "s_cbranch_scc0 20b\n\t" \
  "s_branch 99f\n\t" \
  "14:\n\t" \
  "s_sleep 3\n\t" \
  "s_add_u32 s59, s59, 0x10000\n\t" \
  "s_cmp_lt_u32 s59, 0x100000\n\t" \
  "s_cbranch_scc1 13b\n\t" \
  "s_mov_b64 exec, -1\n\t" \
  "s_mov_b32 s42, 16\n\t" \
  "s_branch 99f\n\t" \
  "8:\n\t" \
  "s_bitcmp1_b32 s58, 1\n\t" \
  "s_cbranch_scc1 4b\n\t" \
  "s_bitcmp1_b32 s58, 2\n\t" \
  "s_cbranch_scc1 5b\n\t" \
  "v_max3_u32 v116, v32, v33, v34\n\t" \
  "v_max3_u32 v117, v35, v36, v37\n\t" \
  "v_max3_u32 v116, v116, v38, v39\n\t" \
  "v_max_u32 v116, v116, v117\n\t" \
  "v_cmp_eq_u32 vcc, -1, v116\n\t" \
  "s_cbranch_vccz 5b\n\t" \
  "s_branch 0f\n\t" \
  "9:\n\t" \
  "s_bitcmp1_b32 s58, 2\n\t" \
  "s_cbranch_scc1 6b\n\t" \
  "v_max3_u32 v116, v40, v41, v42\n\t" \
  "v_max3_u32 v117, v43, v44, v45\n\t" \
  "v_max3_u32 v116, v116, v46, v47\n\t" \
  "v_max_u32 v116, v116, v117\n\t" \
  "v_cmp_eq_u32 vcc, -1, v116\n\t" \
  "s_cbranch_vccz 6b\n\t" \
  "s_or_b32 s58, s58, 2\n\t" \
  "0:\n\t" \
  "s_or_b32 s58, s58, 1\n\t" \
  "11:\n\t" \
  "buffer_load_dwordx4 v[32:35], v100, %[rh], s40 offen sc1\n\t" \
  "buffer_load_dwordx4 v[36:39], v100, %[rh], s76 offen sc1\n\t" \
  "buffer_load_dwordx4 v[40:43], v100, %[rh], s77 offen sc1\n\t" \
  "buffer_load_dwordx4 v[44:47], v100, %[rh], s78 offen sc1\n\t" \
  "s_waitcnt lgkmcnt(0)\n\t" \
  "s_branch 10b\n\t" \
  "17:\n\t" \
  "s_add_u32 s59, s59, 1\n\t" \
  "s_bitcmp1_b32 s58, 2\n\t" \
  "s_cbranch_scc1 3b\n\t" \
  "s_waitcnt vmcnt(0)\n\t" \
  "v_max_u32 v116, v32, v33\n\t" \
  "v_max3_u32 v116, v116, v34, v35\n\t" \
  "v_max3_u32 v116, v116, v36, v37\n\t" \
  "v_max3_u32 v116, v116, v38, v39\n\t" \
  "v_max3_u32 v116, v116, v40, v41\n\t" \
  "v_max3_u32 v116, v116, v42, v43\n\t" \
  "v_max3_u32 v116, v116, v44, v45\n\t" \
  "v_max3_u32 v116, v116, v46, v47\n\t" \
  "v_cmp_eq_u32 vcc, -1, v116\n\t" \
  "s_cbranch_vccz 3b\n\t" \
  "s_bitcmp1_b32 s59, 6\n\t" \
  "s_cbranch_scc1 12f\n\t" \
  "buffer_load_dwordx4 v[32:35], v100, %[rh], s40 offen sc1\n\t" \
  "buffer_load_dwordx4 v[36:39], v100, %[rh], s76 offen sc1\n\t" \
  "buffer_load_dwordx4 v[40:43], v100, %[rh], s77 offen sc1\n\t" \
  "buffer_load_dwordx4 v[44:47], v100, %[rh], s78 offen sc1\n\t" \
  "s_branch 17b\n\t" \
  "12:\n\t" \
  "s_mov_b32 s42, s58\n\t" \
  "s_branch 99f\n\t" \
  "99:\n\t" \
  "s_waitcnt vmcnt(0) lgkmcnt(0)\n\t" \
  ".Lchain_last:\n\t" \
  ""
#define CB_BODY \
  "s_cmp_gt_u32 %[wv], 1\n\t" \
  "s_cbranch_scc1 91f\n\t" \
  "s_mov_b32 s58, s42\n\t" \
  "s_mov_b32 s59, 0\n\t" \
  "s_bitcmp1_b32 s58, 4\n\t" \
  "s_cbranch_scc1 26f\n\t" \
  "s_bitcmp1_b32 s58, 3\n\t" \
  "s_cbranch_scc1 34f\n\t" \
  "s_bitcmp1_b32 s58, 1\n\t" \
  "s_cbranch_scc1 18f\n\t" \
  "17:\n\t" \
  "19:\n\t" \
  "s_add_i32 s85, s40, -1\n\t" \
  "s_and_b32 s82, s85, 2\n\t" \
  "s_lshl_b32 s82, s82, 13\n\t" \
  "s_and_b32 s80, s85, 1\n\t" \
  "s_lshl_b32 s80, s80, 12\n\t" \
  "s_add_i32 s82, s82, s80\n\t" \
  "s_add_i32 s82, s82, 0x84000\n\t" \
  "s_mov_b32 exec_hi, 0\n\t" \
  "buffer_load_dword v184, v200, %[rh], s82 offen sc1\n\t" \
  "buffer_load_dword v186, v203, %[ru], 0 offen sc1\n\t" \
  "s_mov_b64 exec, -1\n\t" \
  "20:\n\t" \
  "s_and_b32 s80, s40, 3\n\t" \
  "s_mul_i32 s77, s80, 0xc000\n\t" \
  "s_add_i32 s77, s77, 0x40000\n\t" \
  "s_lshl_b32 s78, s80, 14\n\t" \
  "s_add_i32 s78, s78, 0x70000\n\t" \
  "s_mov_b32 s57, 0\n\t" \
  "s_add_i32 s96, s77, 0x1000\n\t" \
  "s_lshl_b32 s88, s80, 20\n\t" \
  "s_add_i32 s88, s88, s48\n\t" \
  "s_lshr_b32 s80, s88, 21\n\t" \
  "s_bitcmp1_b64 %[sel], s80\n\t" \
  "s_cselect_b32 s80, 0x2000, 0\n\t" \
  "s_add_i32 s88, s88, s80\n\t" \
  "s_xor_b32 s89, s88, 0x800\n\t" \
  "s_xor_b32 s90, s88, 0x1000\n\t" \
  "s_xor_b32 s91, s88, 0x1800\n\t" \
  "s_add_i32 s92, s77, s50\n\t" \
  "s_xor_b32 s93, s92, 0x100\n\t" \
  "s_xor_b32 s94, s92, 0x200\n\t" \
  "s_xor_b32 s95, s92, 0x300\n\t" \
  "s_add_i32 s97, s40, 1\n\t" \
  "s_and_b32 s97, s97, 7\n\t" \
  "s_lshl_b32 s97, s97, 15\n\t" \
  "s_add_i32 s97, s97, s49\n\t" \
  "s_memtime vcc\n\t" \
  "s_memrealtime s[86:87]\n\t" \
  "s_nop 4\n\t" \
  "s_memrealtime s[60:61]\n\t" \
  "s_nop 4\n\t" \
  "s_memrealtime s[62:63]\n\t" \
  "s_nop 4\n\t" \
  "s_memrealtime s[64:65]\n\t" \
  "s_nop 4\n\t" \
  "s_memrealtime s[66:67]\n\t" \
  "s_nop 4\n\t" \
  "s_memrealtime s[68:69]\n\t" \
  "s_mov_b32 s79, 0\n\t" \
  "s_waitcnt lgkmcnt(0)\n\t" \
  "s_cmp_eq_u32 s86, s60\n\t" \
  "s_addc_u32 s79, s79, 0\n\t" \
  "s_cmp_eq_u32 s86, s62\n\t" \
  "s_addc_u32 s79, s79, 0\n\t" \
  "s_cmp_eq_u32 s86, s64\n\t" \
  "s_addc_u32 s79, s79, 0\n\t" \
  "s_cmp_eq_u32 s86, s66\n\t" \
  "s_addc_u32 s79, s79, 0\n\t" \
  "s_cmp_eq_u32 s86, s68\n\t" \
  "s_addc_u32 s79, s79, 0\n\t" \
  "s_mul_i32 s79, s79, 24\n\t" \
  "s_add_i32 s79, s79, vcc_lo\n\t" \
  "s_lshl4_add_u32 s80, s86, 64\n\t" \
  "s_sub_i32 s86, s79, s41\n\t" \
  "s_sub_i32 s87, s80, s43\n\t" \
  "s_cmp_lt_u32 s86, 4000\n\t" \
  "s_cbranch_scc1 21f\n\t" \
  "v_cvt_f32_u32 v165, s86\n\t" \
  "v_cvt_f32_u32 v164, s87\n\t" \
  "v_rcp_f32 v165, v165\n\t" \
  "s_nop 0\n\t" \
  "v_mul_f32 v165, v165, v164\n\t" \
  "v_max_f32 v165, 0x3f028f5c, v165\n\t" \
  "v_min_f32 v165, 0x3ffeb852, v165\n\t" \
  "v_rcp_f32 v164, v165\n\t" \
  "v_mul_f32 v165, 0x4f000000, v165\n\t" \
  "v_mul_f32 v164, 0x47800000, v164\n\t" \
  "v_cvt_u32_f32 v165, v165\n\t" \
  "v_cvt_u32_f32 v164, v164\n\t" \
  "s_nop 0\n\t" \
  "v_readfirstlane_b32 s45, v165\n\t" \
  "v_readfirstlane_b32 s44, v164\n\t" \
  "21:\n\t" \
  "s_mov_b32 s41, s79\n\t" \
  "s_mov_b32 s43, s80\n\t" \
  "s_lshl_b32 s83, s43, 8\n\t" \
  "s_mov_b32 s84, 570\n\t" \
  "s_mov_b32 exec_hi, 0\n\t" \
  "s_waitcnt vmcnt(0)\n\t" \
  "v_cmp_ne_u32_sdwa vcc, v184, s85 src0_sel:BYTE_0 src1_sel:DWORD\n\t" \
  "v_subrev_u32 v184, s83, v184\n\t" \
  "v_ashrrev_i32 v184, 8, v184\n\t" \
  "s_cbranch_vccnz 22f\n\t" \
  "s_nop 0\n\t" \
  "v_max_i32_dpp v184, v184, v184 quad_perm:[1,0,3,2] row_mask:0xf bank_mask:0xf\n\t" \
  "s_nop 1\n\t" \
  "v_max_i32_dpp v184, v184, v184 quad_perm:[2,3,0,1] row_mask:0xf bank_mask:0xf\n\t" \
  "s_nop 1\n\t" \
  "v_max_i32_dpp v184, v184, v184 row_half_mirror row_mask:0xf bank_mask:0xf\n\t" \
  "s_nop 1\n\t" \
  "v_max_i32_dpp v184, v184, v184 row_mirror row_mask:0xf bank_mask:0xf\n\t" \
  "s_nop 0\n\t" \
  "v_readlane_b32 s81, v184, 0\n\t" \
  "v_readlane_b32 s82, v184, 16\n\t" \
  "s_max_i32 s81, s81, s82\n\t" \
  "s_add_i32 s76, s81, s43\n\t" \
  "s_add_i32 s81, s81, s53\n\t" \
  "s_max_i32 s81, s81, 0\n\t" \
  "s_min_i32 s81, s81, 20000\n\t" \
  "s_mul_i32 s81, s81, s44\n\t" \
  "s_lshr_b32 s81, s81, 16\n\t" \
  "s_add_i32 s81, s81, -900\n\t" \
  "24:\n\t" \
  "s_add_i32 s81, s81, s41\n\t" \
  ".p2align 6\n\t" \
  "s_nop 0\n\t" \
  "s_nop 0\n\t" \
  "s_nop 0\n\t" \
  "s_nop 0\n\t" \
  "s_nop 0\n\t" \
  "s_nop 0\n\t" \
  "s_nop 0\n\t" \
  "s_nop 0\n\t" \
  "1:\n\t" \
  "s_memtime vcc\n\t" \
  "s_waitcnt lgkmcnt(0)\n\t" \
  "s_sub_i32 s79, s81, vcc_lo\n\t" \
  "s_cmp_gt_i32 s79, 128\n\t" \
  "s_cbranch_scc1 1b\n\t" \
  ".p2align 4\n\t" \
  "2:\n\t" \
  "s_sub_i32 s79, s79, 33\n\t" \
  "s_cmp_gt_i32 s79, 56\n\t" \
  "s_cbranch_scc1 2b\n\t" \
  "buffer_load_dword v187, v200, %[rh], s78 offen sc1\n\t" \
  "s_memtime vcc\n\t" \
  "s_memrealtime s[86:87]\n\t" \
  "s_nop 4\n\t" \
  "s_memrealtime s[60:61]\n\t" \
  "s_nop 4\n\t" \
  "s_memrealtime s[62:63]\n\t" \
  "s_nop 4\n\t" \
  "s_memrealtime s[64:65]\n\t" \
  "s_nop 4\n\t" \
  "s_memrealtime s[66:67]\n\t" \
  "s_nop 4\n\t" \
  "s_memrealtime s[68:69]\n\t" \
  "s_mov_b32 s79, 0\n\t" \
  "s_waitcnt lgkmcnt(0)\n\t" \
  "s_cmp_eq_u32 s86, s60\n\t" \
  "s_addc_u32 s79, s79, 0\n\t" \
  "s_cmp_eq_u32 s86, s62\n\t" \
  "s_addc_u32 s79, s79, 0\n\t" \
  "s_cmp_eq_u32 s86, s64\n\t" \
  "s_addc_u32 s79, s79, 0\n\t" \
  "s_cmp_eq_u32 s86, s66\n\t" \
  "s_addc_u32 s79, s79, 0\n\t" \
  "s_cmp_eq_u32 s86, s68\n\t" \
  "s_addc_u32 s79, s79, 0\n\t" \
  "s_mul_i32 s79, s79, 24\n\t" \
  "s_add_i32 s79, s79, vcc_lo\n\t" \
  "s_lshl4_add_u32 s80, s86, 64\n\t" \
  "s_mov_b32 s41, s79\n\t" \
  "s_mov_b32 s43, s80\n\t" \
  "s_lshl_b32 s83, s43, 8\n\t" \
  "s_add_i32 s82, s40, 2\n\t" \
  "v_cmp_gt_i32 vcc, s82, v186\n\t" \
  "s_and_b32 s54, s82, 15\n\t" \
  "s_lshl_b32 s54, s54, 22\n\t" \
  "s_add_i32 s54, s54, s51\n\t" \
  "s_lshr_b32 s80, s54, 21\n\t" \
  "s_bitcmp0_b64 %[sel], s80\n\t" \
  "s_cselect_b32 s80, 0x2000, 0\n\t" \
  "s_add_i32 s54, s54, s80\n\t" \
  "s_cmp_eq_u64 vcc, 0\n\t" \
  "s_cselect_b32 s54, s54, 0x70000000\n\t" \
  "13:\n\t" \
  "s_waitcnt vmcnt(0)\n\t" \
  "s_memtime s[86:87]\n\t" \
  "v_cmp_ne_u32_sdwa vcc, v187, s40 src0_sel:BYTE_0 src1_sel:DWORD\n\t" \
  "v_subrev_u32 v184, s83, v187\n\t" \
  "v_ashrrev_i32 v184, 8, v184\n\t" \
  "s_nop 1\n\t" \
  "v_max_i32_dpp v184, v184, v184 quad_perm:[1,0,3,2] row_mask:0xf bank_mask:0xf\n\t" \
  "s_nop 1\n\t" \
  "v_max_i32_dpp v184, v184, v184 quad_perm:[2,3,0,1] row_mask:0xf bank_mask:0xf\n\t" \
  "s_cbranch_vccnz 14f\n\t" \
  "15:\n\t" \
  "s_nop 0\n\t" \
  "v_max_i32_dpp v184, v184, v184 row_half_mirror row_mask:0xf bank_mask:0xf\n\t" \
  "s_nop 1\n\t" \
  "v_max_i32_dpp v184, v184, v184 row_mirror row_mask:0xf bank_mask:0xf\n\t" \
  "s_mov_b64 exec, -1\n\t" \
  "v_readlane_b32 s57, v184, 0\n\t" \
  "v_readlane_b32 s82, v184, 16\n\t" \
  "s_waitcnt lgkmcnt(0)\n\t" \
  "s_sub_i32 s79, s41, s86\n\t" \
  "s_max_i32 s57, s57, s82\n\t" \
  "s_add_i32 s81, s57, 212\n\t" \
  "s_max_i32 s81, s81, 0\n\t" \
  "s_min_i32 s81, s81, 20000\n\t" \
  "s_mul_i32 s81, s81, s44\n\t" \
  "s_lshr_b32 s81, s81, 16\n\t" \
  "s_add_i32 s79, s79, s81\n\t" \
  "s_add_i32 s79, s79, -310\n\t" \
  ".p2align 4\n\t" \
  "3:\n\t" \
  "s_sub_i32 s79, s79, 33\n\t" \
  "s_cmp_gt_i32 s79, 56\n\t" \
  "s_cbranch_scc1 3b\n\t" \
  "buffer_load_dwordx4 v[72:75], v192, %[ru], s88 offen offset:0 sc1\n\t" \
  "s_nop 4\n\t" \
  "buffer_load_dwordx4 v[76:79], v192, %[ru], s88 offen offset:1024 sc1\n\t" \
  "s_nop 4\n\t" \
  "s_add_i32 s79, s79, 12\n\t" \
  ".p2align 4\n\t" \
  "4:\n\t" \
  "s_sub_i32 s79, s79, 33\n\t" \
  "s_cmp_gt_i32 s79, 56\n\t" \
  "s_cbranch_scc1 4b\n\t" \
  "buffer_load_dwordx4 v[80:83], v192, %[ru], s89 offen offset:0 sc1\n\t" \
  "s_nop 4\n\t" \
  "buffer_load_dwordx4 v[84:87], v192, %[ru], s89 offen offset:1024 sc1\n\t" \
  "s_nop 4\n\t" \
  "s_add_i32 s79, s79, 22\n\t" \
  ".p2align 4\n\t" \
  "5:\n\t" \
  "s_sub_i32 s79, s79, 33\n\t" \
  "s_cmp_gt_i32 s79, 56\n\t" \
  "s_cbranch_scc1 5b\n\t" \
  "buffer_load_dwordx4 v[88:91], v192, %[ru], s90 offen offset:0 sc1\n\t" \
  "buffer_load_dwordx4 v[92:95], v192, %[ru], s90 offen offset:1024 sc1\n\t" \
  "s_add_i32 s79, s79, 102\n\t" \
  ".p2align 4\n\t" \
  "9:\n\t" \
  "s_sub_i32 s79, s79, 33\n\t" \
  "s_cmp_gt_i32 s79, 56\n\t" \
  "s_cbranch_scc1 9b\n\t" \
  "buffer_load_dwordx4 v[104:107], v194, %[rh], s92 offen sc1\n\t" \
  "s_nop 4\n\t" \
  "buffer_load_dwordx4 v[108:111], v194, %[rh], s93 offen sc1\n\t" \
  "s_nop 4\n\t" \
  "buffer_load_dwordx4 v[112:115], v194, %[rh], s94 offen sc1\n\t" \
  "s_nop 4\n\t" \
  "buffer_load_dwordx4 v[116:119], v194, %[rh], s95 offen sc1\n\t" \
  "s_nop 4\n\t" \
  "buffer_load_dwordx4 v[96:99], v192, %[ru], s91 offen offset:0 sc1\n\t" \
  "s_nop 4\n\t" \
  "buffer_load_dwordx4 v[100:103], v192, %[ru], s91 offen offset:1024 sc1\n\t" \
  "s_nop 4\n\t" \
  "10:\n\t" \
  "s_waitcnt vmcnt(5)\n\t" \
  "v_mfma_f32_16x16x32_bf16 v[136:139], v[104:107], v[72:75], 0\n\t" \
  "v_mfma_f32_16x16x32_bf16 v[140:143], v[104:107], v[76:79], 0\n\t" \
  "s_waitcnt vmcnt(4)\n\t" \
  "v_mfma_f32_16x16x32_bf16 v[136:139], v[108:111], v[80:83], v[136:139]\n\t" \
  "v_mfma_f32_16x16x32_bf16 v[140:143], v[108:111], v[84:87], v[140:143]\n\t" \
  "s_waitcnt vmcnt(3)\n\t" \
  "v_mfma_f32_16x16x32_bf16 v[136:139], v[112:115], v[88:91], v[136:139]\n\t" \
  "v_mfma_f32_16x16x32_bf16 v[140:143], v[112:115], v[92:95], v[140:143]\n\t" \
  "s_waitcnt vmcnt(1)\n\t" \
  "v_mfma_f32_16x16x32_bf16 v[136:139], v[116:119], v[96:99], v[136:139]\n\t" \
  "s_waitcnt vmcnt(0)\n\t" \
  "v_mfma_f32_16x16x32_bf16 v[140:143], v[116:119], v[100:103], v[140:143]\n\t" \
  "s_barrier\n\t" \
  "ds_read2st64_b64 v[144:147], v195 offset1:1\n\t" \
  "s_nop 1\n\t" \
  "v_permlane32_swap_b32 v136, v138\n\t" \
  "v_permlane32_swap_b32 v137, v139\n\t" \
  "s_nop 1\n\t" \
  "v_permlane32_swap_b32 v140, v142\n\t" \
  "v_permlane32_swap_b32 v141, v143\n\t" \
  "v_pk_add_f32 v[136:137], v[136:137], v[142:143]\n\t" \
  "s_waitcnt lgkmcnt(0)\n\t" \
  "v_pk_add_f32 v[136:137], v[136:137], v[144:145]\n\t" \
  "v_pk_add_f32 v[136:137], v[136:137], v[146:147]\n\t" \
  "v_cvt_pk_bf16_f32 v148, v136, v137\n\t" \
  "s_nop 1\n\t" \
  "v_mov_b32_dpp v149, v148 row_half_mirror row_mask:0xf bank_mask:0xf\n\t" \
  "v_perm_b32 v150, v148, v149, v205\n\t" \
  "v_cmpx_o_f16_sdwa vcc, v150, v150 src0_sel:WORD_0 src1_sel:WORD_1\n\t" \
  "6:\n\t" \
  "buffer_store_dword v150, v201, %[rh], s97 offen sc1\n\t" \
  "s_memtime s[82:83]\n\t" \
  "s_mov_b64 exec, -1\n\t" \
  "s_cmp_eq_u64 vcc, -1\n\t" \
  "s_cbranch_scc0 8f\n\t" \
  "v_mov_b32 v151, 0\n\t" \
  "5:\n\t" \
  "ds_write_b32 v206, v151\n\t" \
  "buffer_load_dwordx4 v[0:3], v210, %[ru], s54 offen offset:0 sc1\n\t" \
  "buffer_load_dwordx4 v[4:7], v210, %[ru], s54 offen offset:1024 sc1\n\t" \
  "s_add_i32 s54, s54, 0x20000\n\t" \
  "buffer_load_dwordx4 v[8:11], v210, %[ru], s54 offen offset:0 sc1\n\t" \
  "buffer_load_dwordx4 v[12:15], v210, %[ru], s54 offen offset:1024 sc1\n\t" \
  "s_add_i32 s54, s54, 0x20000\n\t" \
  "s_waitcnt lgkmcnt(0)\n\t" \
  "s_mov_b32 s56, s82\n\t" \
  "s_barrier\n\t" \
  "ds_read2_b32 v[148:149], v207 offset1:8\n\t" \
  "s_waitcnt lgkmcnt(0)\n\t" \
  "v_or_b32 v148, v148, v149\n\t" \
  "v_cmp_ne_u32 vcc, 0, v148\n\t" \
  "s_cbranch_vccnz 12f\n\t" \
  "s_and_b32 s58, s58, 4\n\t" \
  "s_add_i32 s80, s57, s43\n\t" \
  "s_sub_i32 s80, s80, s76\n\t" \
  "s_add_i32 s53, s53, -12\n\t" \
  "s_max_i32 s80, s80, s53\n\t" \
  "s_add_i32 s53, s53, 24\n\t" \
  "s_min_i32 s53, s53, s80\n\t" \
  "s_cmp_lt_u32 s54, 0x70000000\n\t" \
  "s_cbranch_scc1 48f\n\t" \
  "25:\n\t" \
  "s_add_i32 s82, s40, 2\n\t" \
  "s_min_i32 s83, s82, 63\n\t" \
  "s_mov_b32 exec_hi, 0\n\t" \
  "v_cmp_gt_i32 vcc, s83, v186\n\t" \
  "s_mov_b64 exec, -1\n\t" \
  "s_and_b32 s54, s82, 15\n\t" \
  "s_lshl_b32 s54, s54, 22\n\t" \
  "s_add_i32 s54, s54, s51\n\t" \
  "s_lshr_b32 s80, s54, 21\n\t" \
  "s_bitcmp0_b64 %[sel], s80\n\t" \
  "s_cselect_b32 s80, 0x2000, 0\n\t" \
  "s_add_i32 s54, s54, s80\n\t" \
  "s_cbranch_vccnz 27f\n\t" \
  "28:\n\t" \
  "s_cmp_ge_i32 s40, 62\n\t" \
  "s_cbranch_scc1 37f\n\t" \
  "buffer_load_dwordx4 v[0:3], v210, %[ru], s54 offen offset:0 sc1\n\t" \
  "buffer_load_dwordx4 v[4:7], v210, %[ru], s54 offen offset:1024 sc1\n\t" \
  "s_add_i32 s54, s54, 0x20000\n\t" \
  "buffer_load_dwordx4 v[8:11], v210, %[ru], s54 offen offset:0 sc1\n\t" \
  "buffer_load_dwordx4 v[12:15], v210, %[ru], s54 offen offset:1024 sc1\n\t" \
  "s_add_i32 s54, s54, 0x20000\n\t" \
  "48:\n\t" \
  "s_and_b32 s76, s40, 3\n\t" \
  "s_lshl_b32 s76, s76, 18\n\t" \
  "s_bitcmp1_b32 s58, 4\n\t" \
  "s_cbranch_scc1 29f\n\t" \
  "s_sub_i32 s80, s56, s41\n\t" \
  "s_mul_hi_u32 s80, s80, s45\n\t" \
  "s_lshl_b32 s80, s80, 1\n\t" \
  "s_sub_i32 s80, s80, s57\n\t" \
  "s_min_u32 s47, s47, s46\n\t" \
  "s_min_u32 s47, s47, s80\n\t" \
  "v_mov_b32 v188, s47\n\t" \
  "s_mov_b32 s47, s46\n\t" \
  "s_mov_b32 s46, s80\n\t" \
  "buffer_store_dword v188, v202, %[rh], s52 offen sc1\n\t" \
  "v_mov_b64 v[188:189], -1\n\t" \
  "v_mov_b64 v[190:191], -1\n\t" \
  "v_mov_b32 v187, s40\n\t" \
  "s_add_i32 s80, s40, -1\n\t" \
  "s_and_b32 s80, s80, 7\n\t" \
  "s_lshl_b32 s80, s80, 15\n\t" \
  "s_add_i32 s80, s80, s49\n\t" \
  "buffer_store_dwordx4 v[188:191], v208, %[rh], s80 offen sc1\n\t" \
  "buffer_store_dword v187, v209, %[ru], 0 offen sc1\n\t" \
  "ds_write_b128 v197, v[72:75] offset:16384\n\t" \
  "ds_write_b128 v197, v[76:79] offset:17408\n\t" \
  "ds_write_b128 v197, v[80:83] offset:18432\n\t" \
  "ds_write_b128 v197, v[84:87] offset:19456\n\t" \
  "ds_write_b128 v197, v[88:91] offset:20480\n\t" \
  "ds_write_b128 v197, v[92:95] offset:21504\n\t" \
  "ds_write_b128 v197, v[96:99] offset:22528\n\t" \
  "ds_write_b128 v197, v[100:103] offset:23552\n\t" \
  "29:\n\t" \
  "s_and_b32 s58, s58, 36\n\t" \
  "buffer_load_dwordx4 v[16:19], v210, %[ru], s54 offen offset:0 sc1\n\t" \
  "buffer_load_dwordx4 v[20:23], v210, %[ru], s54 offen offset:1024 sc1\n\t" \
  "s_add_i32 s54, s54, 0x20000\n\t" \
  "buffer_load_dwordx4 v[24:27], v210, %[ru], s54 offen offset:0 sc1\n\t" \
  "buffer_load_dwordx4 v[28:31], v210, %[ru], s54 offen offset:1024 sc1\n\t" \
  "s_add_i32 s54, s54, 0x20000\n\t" \
  "buffer_load_dwordx4 v[32:35], v210, %[ru], s54 offen offset:0 sc1\n\t" \
  "buffer_load_dwordx4 v[36:39], v210, %[ru], s54 offen offset:1024 sc1\n\t" \
  "s_add_i32 s54, s54, 0x20000\n\t" \
  "buffer_load_dwordx4 v[40:43], v210, %[ru], s54 offen offset:0 sc1\n\t" \
  "buffer_load_dwordx4 v[44:47], v210, %[ru], s54 offen offset:1024 sc1\n\t" \
  "s_add_i32 s54, s54, 0x20000\n\t" \
  "buffer_load_dwordx4 a[0:3], v211, %[rh], s76 offen offset:0 sc1\n\t" \
  "buffer_load_dwordx4 a[4:7], v211, %[rh], s76 offen offset:1024 sc1\n\t" \
  "buffer_load_dwordx4 v[48:51], v210, %[ru], s54 offen offset:0 sc1\n\t" \
  "buffer_load_dwordx4 v[52:55], v210, %[ru], s54 offen offset:1024 sc1\n\t" \
  "s_add_i32 s54, s54, 0x20000\n\t" \
  "buffer_load_dwordx4 v[56:59], v210, %[ru], s54 offen offset:0 sc1\n\t" \
  "buffer_load_dwordx4 v[60:63], v210, %[ru], s54 offen offset:1024 sc1\n\t" \
  "s_add_i32 s54, s54, 0x20000\n\t" \
  "buffer_load_dwordx4 a[8:11], v212, %[rh], s76 offen offset:0 sc1\n\t" \
  "buffer_load_dwordx4 a[12:15], v212, %[rh], s76 offen offset:1024 sc1\n\t" \
  "s_waitcnt lgkmcnt(0)\n\t" \
  "s_barrier\n\t" \
  "buffer_load_dwordx4 v[64:67], v210, %[ru], s54 offen offset:0 sc1\n\t" \
  "buffer_load_dwordx4 v[68:71], v210, %[ru], s54 offen offset:1024 sc1\n\t" \
  "s_add_i32 s54, s54, 0x20000\n\t" \
  "buffer_load_dwordx4 v[104:107], v210, %[ru], s54 offen offset:0 sc1\n\t" \
  "buffer_load_dwordx4 v[108:111], v210, %[ru], s54 offen offset:1024 sc1\n\t" \
  "s_add_i32 s54, s54, 0x20000\n\t" \
  "buffer_load_dwordx4 a[16:19], v213, %[rh], s76 offen offset:0 sc1\n\t" \
  "buffer_load_dwordx4 a[20:23], v213, %[rh], s76 offen offset:1024 sc1\n\t" \
  "buffer_load_dwordx4 v[112:115], v210, %[ru], s54 offen offset:0 sc1\n\t" \
  "buffer_load_dwordx4 v[116:119], v210, %[ru], s54 offen offset:1024 sc1\n\t" \
  "s_add_i32 s54, s54, 0x20000\n\t" \
  "buffer_load_dwordx4 v[120:123], v210, %[ru], s54 offen offset:0 sc1\n\t" \
  "buffer_load_dwordx4 v[124:127], v210, %[ru], s54 offen offset:1024 sc1\n\t" \
  "s_add_i32 s54, s54, 0x20000\n\t" \
  "buffer_load_dwordx4 a[24:27], v214, %[rh], s76 offen offset:0 sc1\n\t" \
  "buffer_load_dwordx4 a[28:31], v214, %[rh], s76 offen offset:1024 sc1\n\t" \
  "ds_read_b128 v[160:163], v193 offset:0\n\t" \
  "ds_read_b128 v[164:167], v193 offset:256\n\t" \
  "ds_read_b128 v[168:171], v193 offset:2048\n\t" \
  "ds_read_b128 v[172:175], v193 offset:2304\n\t" \
  "ds_read_b128 v[176:179], v193 offset:4096\n\t" \
  "ds_read_b128 v[180:183], v193 offset:4352\n\t" \
  "s_waitcnt lgkmcnt(2)\n\t" \
  "v_pk_add_f32 v[160:161], v[160:161], v[168:169]\n\t" \
  "v_pk_add_f32 v[162:163], v[162:163], v[170:171]\n\t" \
  "v_pk_add_f32 v[164:165], v[164:165], v[172:173]\n\t" \
  "v_pk_add_f32 v[166:167], v[166:167], v[174:175]\n\t" \
  "s_waitcnt lgkmcnt(0)\n\t" \
  "v_pk_add_f32 v[160:161], v[160:161], v[176:177]\n\t" \
  "v_pk_add_f32 v[162:163], v[162:163], v[178:179]\n\t" \
  "v_pk_add_f32 v[164:165], v[164:165], v[180:181]\n\t" \
  "v_pk_add_f32 v[166:167], v[166:167], v[182:183]\n\t" \
  "v_cvt_pk_bf16_f32 v160, v160, v161\n\t" \
  "v_cvt_pk_bf16_f32 v161, v162, v163\n\t" \
  "v_cvt_pk_bf16_f32 v162, v164, v165\n\t" \
  "v_cvt_pk_bf16_f32 v163, v166, v167\n\t" \
  "buffer_load_dwordx4 v[128:131], v210, %[ru], s54 offen offset:0 sc1\n\t" \
  "buffer_load_dwordx4 v[132:135], v210, %[ru], s54 offen offset:1024 sc1\n\t" \
  "s_add_i32 s54, s54, 0x20000\n\t" \
  "buffer_load_dwordx4 v[136:139], v210, %[ru], s54 offen offset:0 sc1\n\t" \
  "buffer_load_dwordx4 v[140:143], v210, %[ru], s54 offen offset:1024 sc1\n\t" \
  "s_add_i32 s54, s54, 0x20000\n\t" \
  "s_waitcnt vmcnt(32)\n\t" \
  "v_swap_b32 v1, v8\n\t" \
  "v_swap_b32 v3, v10\n\t" \
  "v_swap_b32 v5, v12\n\t" \
  "v_swap_b32 v7, v14\n\t" \
  "ds_write_b128 v204, v[0:3] offset:0\n\t" \
  "ds_write_b128 v204, v[8:11] offset:1024\n\t" \
  "ds_write_b128 v204, v[4:7] offset:2048\n\t" \
  "ds_write_b128 v204, v[12:15] offset:3072\n\t" \
  "buffer_load_dwordx4 v[144:147], v210, %[ru], s54 offen offset:0 sc1\n\t" \
  "buffer_load_dwordx4 v[148:151], v210, %[ru], s54 offen offset:1024 sc1\n\t" \
  "s_add_i32 s54, s54, 0x20000\n\t" \
  "buffer_load_dwordx4 v[152:155], v210, %[ru], s54 offen offset:0 sc1\n\t" \
  "buffer_load_dwordx4 v[156:159], v210, %[ru], s54 offen offset:1024 sc1\n\t" \
  "s_add_i32 s54, s54, 0x20000\n\t" \
  "s_waitcnt vmcnt(32)\n\t" \
  "v_swap_b32 v17, v24\n\t" \
  "v_swap_b32 v19, v26\n\t" \
  "v_swap_b32 v21, v28\n\t" \
  "v_swap_b32 v23, v30\n\t" \
  "ds_write_b128 v204, v[16:19] offset:4096\n\t" \
  "ds_write_b128 v204, v[24:27] offset:5120\n\t" \
  "ds_write_b128 v204, v[20:23] offset:6144\n\t" \
  "ds_write_b128 v204, v[28:31] offset:7168\n\t" \
  "s_waitcnt vmcnt(28)\n\t" \
  "v_swap_b32 v33, v40\n\t" \
  "v_swap_b32 v35, v42\n\t" \
  "v_swap_b32 v37, v44\n\t" \
  "v_swap_b32 v39, v46\n\t" \
  "ds_write_b128 v204, v[32:35] offset:8192\n\t" \
  "ds_write_b128 v204, v[40:43] offset:9216\n\t" \
  "ds_write_b128 v204, v[36:39] offset:10240\n\t" \
  "ds_write_b128 v204, v[44:47] offset:11264\n\t" \
  "s_waitcnt vmcnt(22)\n\t" \
  "v_swap_b32 v49, v56\n\t" \
  "v_swap_b32 v51, v58\n\t" \
  "v_swap_b32 v53, v60\n\t" \
  "v_swap_b32 v55, v62\n\t" \
  "ds_write_b128 v204, v[48:51] offset:12288\n\t" \
  "ds_write_b128 v204, v[56:59] offset:13312\n\t" \
  "ds_write_b128 v204, v[52:55] offset:14336\n\t" \
  "ds_write_b128 v204, v[60:63] offset:15360\n\t" \
  "s_waitcnt vmcnt(16)\n\t" \
  "v_swap_b32 v65, v104\n\t" \
  "v_swap_b32 v67, v106\n\t" \
  "v_swap_b32 v69, v108\n\t" \
  "v_swap_b32 v71, v110\n\t" \
  "ds_write_b128 v204, v[64:67] offset:16384\n\t" \
  "ds_write_b128 v204, v[104:107] offset:17408\n\t" \
  "ds_write_b128 v204, v[68:71] offset:18432\n\t" \
  "ds_write_b128 v204, v[108:111] offset:19456\n\t" \
  "s_waitcnt vmcnt(10)\n\t" \
  "v_swap_b32 v113, v120\n\t" \
  "v_swap_b32 v115, v122\n\t" \
  "v_swap_b32 v117, v124\n\t" \
  "v_swap_b32 v119, v126\n\t" \
  "ds_write_b128 v204, v[112:115] offset:20480\n\t" \
  "ds_write_b128 v204, v[120:123] offset:21504\n\t" \
  "ds_write_b128 v204, v[116:119] offset:22528\n\t" \
  "ds_write_b128 v204, v[124:127] offset:23552\n\t" \
  "s_waitcnt vmcnt(4)\n\t" \
  "v_swap_b32 v129, v136\n\t" \
  "v_swap_b32 v131, v138\n\t" \
  "v_swap_b32 v133, v140\n\t" \
  "v_swap_b32 v135, v142\n\t" \
  "ds_write_b128 v204, v[128:131] offset:24576\n\t" \
  "ds_write_b128 v204, v[136:139] offset:25600\n\t" \
  "ds_write_b128 v204, v[132:135] offset:26624\n\t" \
  "ds_write_b128 v204, v[140:143] offset:27648\n\t" \
  "s_waitcnt vmcnt(0)\n\t" \
  "v_swap_b32 v145, v152\n\t" \
  "v_swap_b32 v147, v154\n\t" \
  "v_swap_b32 v149, v156\n\t" \
  "v_swap_b32 v151, v158\n\t" \
  "ds_write_b128 v204, v[144:147] offset:28672\n\t" \
  "ds_write_b128 v204, v[152:155] offset:29696\n\t" \
  "ds_write_b128 v204, v[148:151] offset:30720\n\t" \
  "ds_write_b128 v204, v[156:159] offset:31744\n\t" \
  "s_waitcnt lgkmcnt(0)\n\t" \
  "s_cmp_lt_i32 s40, 0\n\t" \
  "s_cbranch_scc1 39f\n\t" \
  "38:\n\t" \
  "s_add_i32 s80, s40, -2\n\t" \
  "s_lshl_b32 s80, s80, 17\n\t" \
  "s_min_u32 s80, s80, 0x70000000\n\t" \
  "buffer_store_dwordx4 v[160:163], v196, %[ro], s80 offen sc1\n\t" \
  "ds_read_b128 a[32:35], v198 offset:0\n\t" \
  "ds_read_b128 a[36:39], v198 offset:1024\n\t" \
  "ds_read_b128 a[40:43], v198 offset:2048\n\t" \
  "ds_read_b128 a[44:47], v198 offset:3072\n\t" \
  "ds_read_b128 a[48:51], v198 offset:4096\n\t" \
  "ds_read_b128 a[52:55], v198 offset:5120\n\t" \
  "ds_read_b128 a[56:59], v198 offset:6144\n\t" \
  "ds_read_b128 a[60:63], v198 offset:7168\n\t" \
  "ds_read_b128 a[64:67], v198 offset:8192\n\t" \
  "ds_read_b128 a[68:71], v198 offset:9216\n\t" \
  "ds_read_b128 a[72:75], v198 offset:10240\n\t" \
  "ds_read_b128 a[76:79], v198 offset:11264\n\t" \
  "ds_read_b128 a[80:83], v198 offset:12288\n\t" \
  "ds_read_b128 a[84:87], v198 offset:13312\n\t" \
  "ds_read_b128 a[88:91], v198 offset:14336\n\t" \
  "ds_read_b128 a[92:95], v198 offset:15360\n\t" \
  "s_waitcnt vmcnt(1) lgkmcnt(0)\n\t" \
  "31:\n\t" \
  "v_mfma_f32_16x16x32_bf16 v[168:171], a[32:35], a[0:3], 0\n\t" \
  "v_mfma_f32_16x16x32_bf16 v[172:175], a[64:67], a[0:3], 0\n\t" \
  "v_mfma_f32_16x16x32_bf16 v[168:171], a[36:39], a[4:7], v[168:171]\n\t" \
  "v_mfma_f32_16x16x32_bf16 v[172:175], a[68:71], a[4:7], v[172:175]\n\t" \
  "v_mfma_f32_16x16x32_bf16 v[168:171], a[40:43], a[8:11], v[168:171]\n\t" \
  "v_mfma_f32_16x16x32_bf16 v[172:175], a[72:75], a[8:11], v[172:175]\n\t" \
  "v_mfma_f32_16x16x32_bf16 v[168:171], a[44:47], a[12:15], v[168:171]\n\t" \
  "v_mfma_f32_16x16x32_bf16 v[172:175], a[76:79], a[12:15], v[172:175]\n\t" \
  "v_mfma_f32_16x16x32_bf16 v[168:171], a[48:51], a[16:19], v[168:171]\n\t" \
  "v_mfma_f32_16x16x32_bf16 v[172:175], a[80:83], a[16:19], v[172:175]\n\t" \
  "v_mfma_f32_16x16x32_bf16 v[168:171], a[52:55], a[20:23], v[168:171]\n\t" \
  "v_mfma_f32_16x16x32_bf16 v[172:175], a[84:87], a[20:23], v[172:175]\n\t" \
  "v_mfma_f32_16x16x32_bf16 v[168:171], a[56:59], a[24:27], v[168:171]\n\t" \
  "v_mfma_f32_16x16x32_bf16 v[172:175], a[88:91], a[24:27], v[172:175]\n\t" \
  "v_mfma_f32_16x16x32_bf16 v[168:171], a[60:63], a[28:31], v[168:171]\n\t" \
  "v_mfma_f32_16x16x32_bf16 v[172:175], a[92:95], a[28:31], v[172:175]\n\t" \
  "s_nop 7\n\t" \
  "39:\n\t" \
  "s_add_i32 s40, s40, 2\n\t" \
  "v_cmp_u_f32 vcc, v168, v168\n\t" \
  "s_cbranch_vccnz 32f\n\t" \
  "33:\n\t" \
  "s_barrier\n\t" \
  "s_add_i32 s85, s40, -1\n\t" \
  "s_cmp_ge_i32 s40, 64\n\t" \
  "s_cbranch_scc1 47f\n\t" \
  "s_and_b32 s82, s85, 2\n\t" \
  "s_lshl_b32 s82, s82, 13\n\t" \
  "s_and_b32 s80, s85, 1\n\t" \
  "s_lshl_b32 s80, s80, 12\n\t" \
  "s_add_i32 s82, s82, s80\n\t" \
  "s_add_i32 s82, s82, 0x84000\n\t" \
  "s_mov_b32 exec_hi, 0\n\t" \
  "buffer_load_dword v184, v200, %[rh], s82 offen sc1\n\t" \
  "buffer_load_dword v186, v203, %[ru], 0 offen sc1\n\t" \
  "s_mov_b64 exec, -1\n\t" \
  "47:\n\t" \
  "ds_write_b128 v199, v[168:171] offset:0\n\t" \
  "ds_write_b128 v199, v[172:175] offset:1024\n\t" \
  "s_mov_b32 s59, 0\n\t" \
  "s_and_b32 s58, s58, 4\n\t" \
  "s_cmp_lt_i32 s40, 64\n\t" \
  "s_cbranch_scc1 20b\n\t" \
  "s_waitcnt lgkmcnt(0)\n\t" \
  "s_barrier\n\t" \
  "ds_read_b128 v[160:163], v193 offset:0\n\t" \
  "ds_read_b128 v[164:167], v193 offset:256\n\t" \
  "ds_read_b128 v[168:171], v193 offset:2048\n\t" \
  "ds_read_b128 v[172:175], v193 offset:2304\n\t" \
  "ds_read_b128 v[176:179], v193 offset:4096\n\t" \
  "ds_read_b128 v[180:183], v193 offset:4352\n\t" \
  "s_waitcnt lgkmcnt(2)\n\t" \
  "v_pk_add_f32 v[160:161], v[160:161], v[168:169]\n\t" \
  "v_pk_add_f32 v[162:163], v[162:163], v[170:171]\n\t" \
  "v_pk_add_f32 v[164:165], v[164:165], v[172:173]\n\t" \
  "v_pk_add_f32 v[166:167], v[166:167], v[174:175]\n\t" \
  "s_waitcnt lgkmcnt(0)\n\t" \
  "v_pk_add_f32 v[160:161], v[160:161], v[176:177]\n\t" \
  "v_pk_add_f32 v[162:163], v[162:163], v[178:179]\n\t" \
  "v_pk_add_f32 v[164:165], v[164:165], v[180:181]\n\t" \
  "v_pk_add_f32 v[166:167], v[166:167], v[182:183]\n\t" \
  "v_cvt_pk_bf16_f32 v160, v160, v161\n\t" \
  "v_cvt_pk_bf16_f32 v161, v162, v163\n\t" \
  "v_cvt_pk_bf16_f32 v162, v164, v165\n\t" \
  "v_cvt_pk_bf16_f32 v163, v166, v167\n\t" \
  "s_add_i32 s80, s40, -2\n\t" \
  "s_lshl_b32 s80, s80, 17\n\t" \
  "s_min_u32 s80, s80, 0x70000000\n\t" \
  "buffer_store_dwordx4 v[160:163], v196, %[ro], s80 offen sc1\n\t" \
  "s_mov_b32 s58, 64\n\t" \
  "s_branch 7f\n\t" \
  "22:\n\t" \
  "s_mov_b32 s81, 0\n\t" \
  "s_cmp_eq_u32 s40, 0\n\t" \
  "s_cbranch_scc1 24b\n\t" \
  "s_bitcmp1_b32 s58, 2\n\t" \
  "s_cbranch_scc1 24b\n\t" \
  "s_mov_b64 exec, -1\n\t" \
  "s_sleep 10\n\t" \
  "s_add_u32 s59, s59, 0x10000\n\t" \
  "s_cmp_lt_u32 s59, 0x100000\n\t" \
  "s_cbranch_scc1 19b\n\t" \
  "s_or_b32 s58, s58, 1\n\t" \
  "s_branch 7f\n\t" \
  "14:\n\t" \
  "v_mov_b32 v184, 0\n\t" \
  "s_bitcmp1_b32 s58, 2\n\t" \
  "s_nop 0\n\t" \
  "s_cbranch_scc1 15b\n\t" \
  "s_mov_b32 s84, 870\n\t" \
  "s_add_u32 s59, s59, 0x10000\n\t" \
  "s_cmp_lt_u32 s59, 0x100000\n\t" \
  "s_mov_b64 exec, vcc\n\t" \
  "buffer_load_dword v187, v200, %[rh], s78 offen sc1\n\t" \
  "s_mov_b32 exec_lo, -1\n\t" \
  "s_cbranch_scc1 13b\n\t" \
  "s_mov_b64 exec, -1\n\t" \
  "s_or_b32 s58, s58, 1\n\t" \
  "s_branch 7f\n\t" \
  "27:\n\t" \
  "s_bitcmp1_b32 s58, 2\n\t" \
  "s_cbranch_scc1 28b\n\t" \
  "s_bitcmp1_b32 s58, 4\n\t" \
  "s_cbranch_scc1 23f\n\t" \
  "s_sub_i32 s80, s56, s41\n\t" \
  "s_mul_hi_u32 s80, s80, s45\n\t" \
  "s_lshl_b32 s80, s80, 1\n\t" \
  "s_sub_i32 s80, s80, s57\n\t" \
  "s_min_u32 s47, s47, s46\n\t" \
  "s_min_u32 s47, s47, s80\n\t" \
  "v_mov_b32 v188, s47\n\t" \
  "s_mov_b32 s47, s46\n\t" \
  "s_mov_b32 s46, s80\n\t" \
  "buffer_store_dword v188, v202, %[rh], s52 offen sc1\n\t" \
  "v_mov_b64 v[188:189], -1\n\t" \
  "v_mov_b64 v[190:191], -1\n\t" \
  "v_mov_b32 v187, s40\n\t" \
  "s_add_i32 s80, s40, -1\n\t" \
  "s_and_b32 s80, s80, 7\n\t" \
  "s_lshl_b32 s80, s80, 15\n\t" \
  "s_add_i32 s80, s80, s49\n\t" \
  "buffer_store_dwordx4 v[188:191], v208, %[rh], s80 offen sc1\n\t" \
  "buffer_store_dword v187, v209, %[ru], 0 offen sc1\n\t" \
  "ds_write_b128 v197, v[72:75] offset:16384\n\t" \
  "ds_write_b128 v197, v[76:79] offset:17408\n\t" \
  "ds_write_b128 v197, v[80:83] offset:18432\n\t" \
  "ds_write_b128 v197, v[84:87] offset:19456\n\t" \
  "ds_write_b128 v197, v[88:91] offset:20480\n\t" \
  "ds_write_b128 v197, v[92:95] offset:21504\n\t" \
  "ds_write_b128 v197, v[96:99] offset:22528\n\t" \
  "ds_write_b128 v197, v[100:103] offset:23552\n\t" \
  "s_or_b32 s58, s58, 16\n\t" \
  "23:\n\t" \
  "s_add_u32 s59, s59, 0x1000000\n\t" \
  "s_cmp_lt_u32 s59, 0x40000000\n\t" \
  "s_cbranch_scc1 26f\n\t" \
  "s_or_b32 s58, s58, 1\n\t" \
  "s_branch 7f\n\t" \
  "26:\n\t" \
  "buffer_load_dword v186, v203, %[ru], 0 offen sc1\n\t" \
  "s_waitcnt vmcnt(0)\n\t" \
  "s_branch 25b\n\t" \
  "37:\n\t" \
  "s_and_b32 s76, s40, 3\n\t" \
  "s_lshl_b32 s76, s76, 18\n\t" \
  "buffer_load_dwordx4 a[0:3], v211, %[rh], s76 offen offset:0 sc1\n\t" \
  "buffer_load_dwordx4 a[4:7], v211, %[rh], s76 offen offset:1024 sc1\n\t" \
  "buffer_load_dwordx4 a[8:11], v212, %[rh], s76 offen offset:0 sc1\n\t" \
  "buffer_load_dwordx4 a[12:15], v212, %[rh], s76 offen offset:1024 sc1\n\t" \
  "buffer_load_dwordx4 a[16:19], v213, %[rh], s76 offen offset:0 sc1\n\t" \
  "buffer_load_dwordx4 a[20:23], v213, %[rh], s76 offen offset:1024 sc1\n\t" \
  "buffer_load_dwordx4 a[24:27], v214, %[rh], s76 offen offset:0 sc1\n\t" \
  "buffer_load_dwordx4 a[28:31], v214, %[rh], s76 offen offset:1024 sc1\n\t" \
  "s_bitcmp1_b32 s58, 4\n\t" \
  "s_cbranch_scc1 29f\n\t" \
  "s_sub_i32 s80, s56, s41\n\t" \
  "s_mul_hi_u32 s80, s80, s45\n\t" \
  "s_lshl_b32 s80, s80, 1\n\t" \
  "s_sub_i32 s80, s80, s57\n\t" \
  "s_min_u32 s47, s47, s46\n\t" \
  "s_min_u32 s47, s47, s80\n\t" \
  "v_mov_b32 v188, s47\n\t" \
  "s_mov_b32 s47, s46\n\t" \
  "s_mov_b32 s46, s80\n\t" \
  "buffer_store_dword v188, v202, %[rh], s52 offen sc1\n\t" \
  "v_mov_b64 v[188:189], -1\n\t" \
  "v_mov_b64 v[190:191], -1\n\t" \
  "v_mov_b32 v187, s40\n\t" \
  "s_add_i32 s80, s40, -1\n\t" \
  "s_and_b32 s80, s80, 7\n\t" \
  "s_lshl_b32 s80, s80, 15\n\t" \
  "s_add_i32 s80, s80, s49\n\t" \
  "buffer_store_dwordx4 v[188:191], v208, %[rh], s80 offen sc1\n\t" \
  "buffer_store_dword v187, v209, %[ru], 0 offen sc1\n\t" \
  "ds_write_b128 v197, v[72:75] offset:16384\n\t" \
  "ds_write_b128 v197, v[76:79] offset:17408\n\t" \
  "ds_write_b128 v197, v[80:83] offset:18432\n\t" \
  "ds_write_b128 v197, v[84:87] offset:19456\n\t" \
  "ds_write_b128 v197, v[88:91] offset:20480\n\t" \
  "ds_write_b128 v197, v[92:95] offset:21504\n\t" \
  "ds_write_b128 v197, v[96:99] offset:22528\n\t" \
  "ds_write_b128 v197, v[100:103] offset:23552\n\t" \
  "29:\n\t" \
  "s_and_b32 s58, s58, 36\n\t" \
  "s_waitcnt lgkmcnt(0)\n\t" \
  "s_barrier\n\t" \
  "ds_read_b128 v[160:163], v193 offset:0\n\t" \
  "ds_read_b128 v[164:167], v193 offset:256\n\t" \
  "ds_read_b128 v[168:171], v193 offset:2048\n\t" \
  "ds_read_b128 v[172:175], v193 offset:2304\n\t" \
  "ds_read_b128 v[176:179], v193 offset:4096\n\t" \
  "ds_read_b128 v[180:183], v193 offset:4352\n\t" \
  "s_waitcnt lgkmcnt(2)\n\t" \
  "v_pk_add_f32 v[160:161], v[160:161], v[168:169]\n\t" \
  "v_pk_add_f32 v[162:163], v[162:163], v[170:171]\n\t" \
  "v_pk_add_f32 v[164:165], v[164:165], v[172:173]\n\t" \
  "v_pk_add_f32 v[166:167], v[166:167], v[174:175]\n\t" \
  "s_waitcnt lgkmcnt(0)\n\t" \
  "v_pk_add_f32 v[160:161], v[160:161], v[176:177]\n\t" \
  "v_pk_add_f32 v[162:163], v[162:163], v[178:179]\n\t" \
  "v_pk_add_f32 v[164:165], v[164:165], v[180:181]\n\t" \
  "v_pk_add_f32 v[166:167], v[166:167], v[182:183]\n\t" \
  "v_cvt_pk_bf16_f32 v160, v160, v161\n\t" \
  "v_cvt_pk_bf16_f32 v161, v162, v163\n\t" \
  "v_cvt_pk_bf16_f32 v162, v164, v165\n\t" \
  "v_cvt_pk_bf16_f32 v163, v166, v167\n\t" \
  "s_branch 38b\n\t" \
  "8:\n\t" \
  "s_mov_b64 vcc, -1\n\t" \
  "s_and_b32 s80, s58, 6\n\t" \
  "s_cbranch_scc1 6b\n\t" \
  "v_mov_b32 v151, 1\n\t" \
  "s_branch 5b\n\t" \
  "32:\n\t" \
  "s_and_b32 s80, s58, 36\n\t" \
  "s_cbranch_scc1 33b\n\t" \
  "s_add_i32 s40, s40, -2\n\t" \
  "34:\n\t" \
  "s_or_b32 s59, s59, 128\n\t" \
  "s_and_b32 s76, s40, 3\n\t" \
  "s_lshl_b32 s76, s76, 18\n\t" \
  "35:\n\t" \
  "buffer_load_dwordx4 a[0:3], v211, %[rh], s76 offen offset:0 sc1\n\t" \
  "buffer_load_dwordx4 a[4:7], v211, %[rh], s76 offen offset:1024 sc1\n\t" \
  "buffer_load_dwordx4 a[8:11], v212, %[rh], s76 offen offset:0 sc1\n\t" \
  "buffer_load_dwordx4 a[12:15], v212, %[rh], s76 offen offset:1024 sc1\n\t" \
  "buffer_load_dwordx4 a[16:19], v213, %[rh], s76 offen offset:0 sc1\n\t" \
  "buffer_load_dwordx4 a[20:23], v213, %[rh], s76 offen offset:1024 sc1\n\t" \
  "buffer_load_dwordx4 a[24:27], v214, %[rh], s76 offen offset:0 sc1\n\t" \
  "buffer_load_dwordx4 a[28:31], v214, %[rh], s76 offen offset:1024 sc1\n\t" \
  "s_waitcnt vmcnt(0)\n\t" \
  "s_bitcmp1_b32 s58, 2\n\t" \
  "s_cbranch_scc1 36f\n\t" \
  "v_accvgpr_read_b32 v0, a0\n\t" \
  "v_accvgpr_read_b32 v1, a1\n\t" \
  "v_accvgpr_read_b32 v2, a2\n\t" \
  "v_accvgpr_read_b32 v3, a3\n\t" \
  "v_accvgpr_read_b32 v4, a4\n\t" \
  "v_accvgpr_read_b32 v5, a5\n\t" \
  "v_accvgpr_read_b32 v6, a6\n\t" \
  "v_accvgpr_read_b32 v7, a7\n\t" \
  "v_accvgpr_read_b32 v8, a8\n\t" \
  "v_accvgpr_read_b32 v9, a9\n\t" \
  "v_accvgpr_read_b32 v10, a10\n\t" \
  "v_accvgpr_read_b32 v11, a11\n\t" \
  "v_accvgpr_read_b32 v12, a12\n\t" \
  "v_accvgpr_read_b32 v13, a13\n\t" \
  "v_accvgpr_read_b32 v14, a14\n\t" \
  "v_accvgpr_read_b32 v15, a15\n\t" \
  "v_accvgpr_read_b32 v16, a16\n\t" \
  "v_accvgpr_read_b32 v17, a17\n\t" \
  "v_accvgpr_read_b32 v18, a18\n\t" \
  "v_accvgpr_read_b32 v19, a19\n\t" \
  "v_accvgpr_read_b32 v20, a20\n\t" \
  "v_accvgpr_read_b32 v21, a21\n\t" \
  "v_accvgpr_read_b32 v22, a22\n\t" \
  "v_accvgpr_read_b32 v23, a23\n\t" \
  "v_accvgpr_read_b32 v24, a24\n\t" \
  "v_accvgpr_read_b32 v25, a25\n\t" \
  "v_accvgpr_read_b32 v26, a26\n\t" \
  "v_accvgpr_read_b32 v27, a27\n\t" \
  "v_accvgpr_read_b32 v28, a28\n\t" \
  "v_accvgpr_read_b32 v29, a29\n\t" \
  "v_accvgpr_read_b32 v30, a30\n\t" \
  "v_accvgpr_read_b32 v31, a31\n\t" \
  "v_max_u32 v148, v0, v1\n\t" \
  "v_max3_u32 v148, v148, v2, v3\n\t" \
  "v_max3_u32 v148, v148, v4, v5\n\t" \
  "v_max3_u32 v148, v148, v6, v7\n\t" \
  "v_max3_u32 v148, v148, v8, v9\n\t" \
  "v_max3_u32 v148, v148, v10, v11\n\t" \
  "v_max3_u32 v148, v148, v12, v13\n\t" \
  "v_max3_u32 v148, v148, v14, v15\n\t" \
  "v_max3_u32 v148, v148, v16, v17\n\t" \
  "v_max3_u32 v148, v148, v18, v19\n\t" \
  "v_max3_u32 v148, v148, v20, v21\n\t" \
  "v_max3_u32 v148, v148, v22, v23\n\t" \
  "v_max3_u32 v148, v148, v24, v25\n\t" \
  "v_max3_u32 v148, v148, v26, v27\n\t" \
  "v_max3_u32 v148, v148, v28, v29\n\t" \
  "v_max3_u32 v148, v148, v30, v31\n\t" \
  "v_cmp_eq_u32 vcc, -1, v148\n\t" \
  "s_cbranch_vccz 36f\n\t" \
  "s_add_u32 s59, s59, 0x1000000\n\t" \
  "s_cmp_lt_u32 s59, 0x10000000\n\t" \
  "s_cbranch_scc1 35b\n\t" \
  "s_or_b32 s58, s58, 9\n\t" \
  "s_branch 7f\n\t" \
  "36:\n\t" \
  "s_and_b32 s58, s58, 4\n\t" \
  "s_or_b32 s58, s58, 32\n\t" \
  "s_branch 31b\n\t" \
  "18:\n\t" \
  "v_mov_b32 v186, -1\n\t" \
  "s_and_b32 s80, s40, 3\n\t" \
  "s_mul_i32 s77, s80, 0xc000\n\t" \
  "s_add_i32 s77, s77, 0x40000\n\t" \
  "s_lshl_b32 s78, s80, 14\n\t" \
  "s_add_i32 s78, s78, 0x70000\n\t" \
  "s_mov_b32 s57, 0\n\t" \
  "s_add_i32 s96, s77, 0x1000\n\t" \
  "s_lshl_b32 s88, s80, 20\n\t" \
  "s_add_i32 s88, s88, s48\n\t" \
  "s_lshr_b32 s80, s88, 21\n\t" \
  "s_bitcmp1_b64 %[sel], s80\n\t" \
  "s_cselect_b32 s80, 0x2000, 0\n\t" \
  "s_add_i32 s88, s88, s80\n\t" \
  "s_xor_b32 s89, s88, 0x800\n\t" \
  "s_xor_b32 s90, s88, 0x1000\n\t" \
  "s_xor_b32 s91, s88, 0x1800\n\t" \
  "s_add_i32 s92, s77, s50\n\t" \
  "s_xor_b32 s93, s92, 0x100\n\t" \
  "s_xor_b32 s94, s92, 0x200\n\t" \
  "s_xor_b32 s95, s92, 0x300\n\t" \
  "s_add_i32 s97, s40, 1\n\t" \
  "s_and_b32 s97, s97, 7\n\t" \
  "s_lshl_b32 s97, s97, 15\n\t" \
  "s_add_i32 s97, s97, s49\n\t" \
  "12:\n\t" \
  "s_or_b32 s58, s58, 3\n\t" \
  "s_mov_b32 s54, 0x70000000\n\t" \
  "11:\n\t" \
  "buffer_load_dwordx4 v[72:75], v192, %[ru], s88 offen offset:0 sc1\n\t" \
  "buffer_load_dwordx4 v[76:79], v192, %[ru], s88 offen offset:1024 sc1\n\t" \
  "buffer_load_dwordx4 v[80:83], v192, %[ru], s89 offen offset:0 sc1\n\t" \
  "buffer_load_dwordx4 v[84:87], v192, %[ru], s89 offen offset:1024 sc1\n\t" \
  "buffer_load_dwordx4 v[88:91], v192, %[ru], s90 offen offset:0 sc1\n\t" \
  "buffer_load_dwordx4 v[92:95], v192, %[ru], s90 offen offset:1024 sc1\n\t" \
  "buffer_load_dwordx4 v[96:99], v192, %[ru], s91 offen offset:0 sc1\n\t" \
  "buffer_load_dwordx4 v[100:103], v192, %[ru], s91 offen offset:1024 sc1\n\t" \
  "buffer_load_dwordx4 v[104:107], v194, %[rh], s92 offen sc1\n\t" \
  "buffer_load_dwordx4 v[108:111], v194, %[rh], s93 offen sc1\n\t" \
  "buffer_load_dwordx4 v[112:115], v194, %[rh], s94 offen sc1\n\t" \
  "buffer_load_dwordx4 v[116:119], v194, %[rh], s95 offen sc1\n\t" \
  "s_waitcnt vmcnt(0)\n\t" \
  "s_add_u32 s59, s59, 1\n\t" \
  "s_bitcmp1_b32 s58, 2\n\t" \
  "s_cbranch_scc1 10b\n\t" \
  "v_max_u32 v148, v72, v73\n\t" \
  "v_max3_u32 v148, v148, v74, v75\n\t" \
  "v_max3_u32 v148, v148, v76, v77\n\t" \
  "v_max3_u32 v148, v148, v78, v79\n\t" \
  "v_max3_u32 v148, v148, v80, v81\n\t" \
  "v_max3_u32 v148, v148, v82, v83\n\t" \
  "v_max3_u32 v148, v148, v84, v85\n\t" \
  "v_max3_u32 v148, v148, v86, v87\n\t" \
  "v_max3_u32 v148, v148, v88, v89\n\t" \
  "v_max3_u32 v148, v148, v90, v91\n\t" \
  "v_max3_u32 v148, v148, v92, v93\n\t" \
  "v_max3_u32 v148, v148, v94, v95\n\t" \
  "v_max3_u32 v148, v148, v96, v97\n\t" \
  "v_max3_u32 v148, v148, v98, v99\n\t" \
  "v_max3_u32 v148, v148, v100, v101\n\t" \
  "v_max3_u32 v148, v148, v102, v103\n\t" \
  "v_max3_u32 v148, v148, v104, v105\n\t" \
  "v_max3_u32 v148, v148, v106, v107\n\t" \
  "v_max3_u32 v148, v148, v108, v109\n\t" \
  "v_max3_u32 v148, v148, v110, v111\n\t" \
  "v_max3_u32 v148, v148, v112, v113\n\t" \
  "v_max3_u32 v148, v148, v114, v115\n\t" \
  "v_max3_u32 v148, v148, v116, v117\n\t" \
  "v_max3_u32 v148, v148, v118, v119\n\t" \
  "v_cmp_eq_u32 vcc, -1, v148\n\t" \
  "s_cbranch_vccz 10b\n\t" \
  "s_bitcmp1_b32 s59, 6\n\t" \
  "s_cbranch_scc0 11b\n\t" \
  "7:\n\t" \
  "s_branch 92f\n\t" \
  "91:\n\t" \
  "s_mov_b32 s58, s42\n\t" \
  "s_mov_b32 s59, 0\n\t" \
  "s_bitcmp1_b32 s58, 4\n\t" \
  "s_cbranch_scc1 37f\n\t" \
  "s_bitcmp1_b32 s58, 3\n\t" \
  "s_cbranch_scc1 34f\n\t" \
  "s_bitcmp1_b32 s58, 1\n\t" \
  "s_cbranch_scc1 18f\n\t" \
  "17:\n\t" \
  "v_mov_b64 v[40:41], 0\n\t" \
  "v_mov_b64 v[42:43], 0\n\t" \
  "v_mov_b64 v[44:45], 0\n\t" \
  "v_mov_b64 v[46:47], 0\n\t" \
  "v_mov_b64 v[48:49], 0\n\t" \
  "v_mov_b64 v[50:51], 0\n\t" \
  "v_mov_b64 v[52:53], 0\n\t" \
  "v_mov_b64 v[54:55], 0\n\t" \
  "v_mov_b64 v[56:57], 0\n\t" \
  "v_mov_b64 v[58:59], 0\n\t" \
  "v_mov_b64 v[60:61], 0\n\t" \
  "v_mov_b64 v[62:63], 0\n\t" \
  "v_mov_b64 v[64:65], 0\n\t" \
  "v_mov_b64 v[66:67], 0\n\t" \
  "v_mov_b64 v[68:69], 0\n\t" \
  "v_mov_b64 v[70:71], 0\n\t" \
  "v_mov_b64 v[72:73], 0\n\t" \
  "v_mov_b64 v[74:75], 0\n\t" \
  "v_mov_b64 v[76:77], 0\n\t" \
  "v_mov_b64 v[78:79], 0\n\t" \
  "v_mov_b64 v[80:81], 0\n\t" \
  "v_mov_b64 v[82:83], 0\n\t" \
  "v_mov_b64 v[84:85], 0\n\t" \
  "v_mov_b64 v[86:87], 0\n\t" \
  "v_mov_b64 v[88:89], 0\n\t" \
  "v_mov_b64 v[90:91], 0\n\t" \
  "v_mov_b64 v[92:93], 0\n\t" \
  "v_mov_b64 v[94:95], 0\n\t" \
  "v_mov_b64 v[96:97], 0\n\t" \
  "v_mov_b64 v[98:99], 0\n\t" \
  "v_mov_b64 v[100:101], 0\n\t" \
  "v_mov_b64 v[102:103], 0\n\t" \
  "v_mov_b64 v[136:137], 0\n\t" \
  "v_mov_b64 v[138:139], 0\n\t" \
  "v_mov_b64 v[140:141], 0\n\t" \
  "v_mov_b64 v[142:143], 0\n\t" \
  "19:\n\t" \
  "s_add_i32 s85, s40, -1\n\t" \
  "s_and_b32 s82, s85, 2\n\t" \
  "s_lshl_b32 s82, s82, 13\n\t" \
  "s_and_b32 s80, s85, 1\n\t" \
  "s_lshl_b32 s80, s80, 12\n\t" \
  "s_add_i32 s82, s82, s80\n\t" \
  "s_add_i32 s82, s82, 0x84000\n\t" \
  "s_mov_b32 exec_hi, 0\n\t" \
  "buffer_load_dword v184, v200, %[rh], s82 offen sc1\n\t" \
  "buffer_load_dword v185, v201, %[rh], s52 offen sc1\n\t" \
  "s_mov_b64 exec, -1\n\t" \
  "20:\n\t" \
  "s_and_b32 s80, s40, 3\n\t" \
  "s_mul_i32 s77, s80, 0xc000\n\t" \
  "s_add_i32 s77, s77, 0x40000\n\t" \
  "s_lshl_b32 s78, s80, 14\n\t" \
  "s_add_i32 s78, s78, 0x70000\n\t" \
  "s_mov_b32 s57, 0\n\t" \
  "s_add_i32 s96, s77, 0x1000\n\t" \
  "s_and_b32 s98, s40, 2\n\t" \
  "s_lshl_b32 s98, s98, 13\n\t" \
  "s_and_b32 s80, s40, 1\n\t" \
  "s_lshl_b32 s80, s80, 12\n\t" \
  "s_add_i32 s98, s98, s80\n\t" \
  "s_add_i32 s98, s98, 0x84000\n\t" \
  "s_memtime vcc\n\t" \
  "s_memrealtime s[86:87]\n\t" \
  "s_nop 4\n\t" \
  "s_memrealtime s[60:61]\n\t" \
  "s_nop 4\n\t" \
  "s_memrealtime s[62:63]\n\t" \
  "s_nop 4\n\t" \
  "s_memrealtime s[64:65]\n\t" \
  "s_nop 4\n\t" \
  "s_memrealtime s[66:67]\n\t" \
  "s_nop 4\n\t" \
  "s_memrealtime s[68:69]\n\t" \
  "s_mov_b32 s79, 0\n\t" \
  "s_waitcnt lgkmcnt(0)\n\t" \
  "s_cmp_eq_u32 s86, s60\n\t" \
  "s_addc_u32 s79, s79, 0\n\t" \
  "s_cmp_eq_u32 s86, s62\n\t" \
  "s_addc_u32 s79, s79, 0\n\t" \
  "s_cmp_eq_u32 s86, s64\n\t" \
  "s_addc_u32 s79, s79, 0\n\t" \
  "s_cmp_eq_u32 s86, s66\n\t" \
  "s_addc_u32 s79, s79, 0\n\t" \
  "s_cmp_eq_u32 s86, s68\n\t" \
  "s_addc_u32 s79, s79, 0\n\t" \
  "s_mul_i32 s79, s79, 24\n\t" \
  "s_add_i32 s79, s79, vcc_lo\n\t" \
  "s_lshl4_add_u32 s80, s86, 64\n\t" \
  "s_sub_i32 s86, s79, s41\n\t" \
  "s_sub_i32 s87, s80, s43\n\t" \
  "s_cmp_lt_u32 s86, 4000\n\t" \
  "s_cbranch_scc1 21f\n\t" \
  "v_cvt_f32_u32 v165, s86\n\t" \
  "v_cvt_f32_u32 v164, s87\n\t" \
  "v_rcp_f32 v165, v165\n\t" \
  "s_nop 0\n\t" \
  "v_mul_f32 v165, v165, v164\n\t" \
  "v_max_f32 v165, 0x3f028f5c, v165\n\t" \
  "v_min_f32 v165, 0x3ffeb852, v165\n\t" \
  "v_rcp_f32 v164, v165\n\t" \
  "v_mul_f32 v165, 0x4f000000, v165\n\t" \
  "v_mul_f32 v164, 0x47800000, v164\n\t" \
  "v_cvt_u32_f32 v165, v165\n\t" \
  "v_cvt_u32_f32 v164, v164\n\t" \
  "s_nop 0\n\t" \
  "v_readfirstlane_b32 s45, v165\n\t" \
  "v_readfirstlane_b32 s44, v164\n\t" \
  "21:\n\t" \
  "s_mov_b32 s41, s79\n\t" \
  "s_mov_b32 s43, s80\n\t" \
  "s_lshl_b32 s83, s43, 8\n\t" \
  "s_mov_b32 s84, 570\n\t" \
  "s_mov_b32 exec_hi, 0\n\t" \
  "s_waitcnt vmcnt(0)\n\t" \
  "v_cmp_ne_u32_sdwa vcc, v184, s85 src0_sel:BYTE_0 src1_sel:DWORD\n\t" \
  "v_subrev_u32 v184, s83, v184\n\t" \
  "v_ashrrev_i32 v184, 8, v184\n\t" \
  "s_cbranch_vccnz 22f\n\t" \
  "s_nop 0\n\t" \
  "v_max_i32_dpp v184, v184, v184 quad_perm:[1,0,3,2] row_mask:0xf bank_mask:0xf\n\t" \
  "s_nop 1\n\t" \
  "v_max_i32_dpp v184, v184, v184 quad_perm:[2,3,0,1] row_mask:0xf bank_mask:0xf\n\t" \
  "s_nop 1\n\t" \
  "v_max_i32_dpp v184, v184, v184 row_half_mirror row_mask:0xf bank_mask:0xf\n\t" \
  "s_nop 1\n\t" \
  "v_max_i32_dpp v184, v184, v184 row_mirror row_mask:0xf bank_mask:0xf\n\t" \
  "s_nop 0\n\t" \
  "v_readlane_b32 s81, v184, 0\n\t" \
  "v_readlane_b32 s82, v184, 16\n\t" \
  "s_max_i32 s81, s81, s82\n\t" \
  "s_add_i32 s76, s81, s43\n\t" \
  "s_add_i32 s81, s81, s53\n\t" \
  "s_max_i32 s81, s81, 0\n\t" \
  "s_min_i32 s81, s81, 20000\n\t" \
  "s_mul_i32 s81, s81, s44\n\t" \
  "s_lshr_b32 s81, s81, 16\n\t" \
  "s_add_i32 s81, s81, -1020\n\t" \
  "24:\n\t" \
  "s_add_i32 s81, s81, s41\n\t" \
  "v_min_u32 v185, 1760, v185\n\t" \
  "s_nop 1\n\t" \
  "v_max_i32_dpp v185, v185, v185 quad_perm:[1,0,3,2] row_mask:0xf bank_mask:0xf\n\t" \
  "s_nop 1\n\t" \
  "v_max_i32_dpp v185, v185, v185 quad_perm:[2,3,0,1] row_mask:0xf bank_mask:0xf\n\t" \
  "s_nop 1\n\t" \
  "v_max_i32_dpp v185, v185, v185 row_half_mirror row_mask:0xf bank_mask:0xf\n\t" \
  "s_nop 1\n\t" \
  "v_max_i32_dpp v185, v185, v185 row_mirror row_mask:0xf bank_mask:0xf\n\t" \
  "s_nop 0\n\t" \
  "v_readlane_b32 s54, v185, 0\n\t" \
  "v_readlane_b32 s82, v185, 16\n\t" \
  "s_max_i32 s54, s54, s82\n\t" \
  "s_add_i32 s54, s54, 225\n\t" \
  ".p2align 6\n\t" \
  "s_nop 0\n\t" \
  "s_nop 0\n\t" \
  "s_nop 0\n\t" \
  "s_nop 0\n\t" \
  "s_nop 0\n\t" \
  "s_nop 0\n\t" \
  "s_nop 0\n\t" \
  "s_nop 0\n\t" \
  "1:\n\t" \
  "s_memtime vcc\n\t" \
  "s_waitcnt lgkmcnt(0)\n\t" \
  "s_sub_i32 s79, s81, vcc_lo\n\t" \
  "s_cmp_gt_i32 s79, 128\n\t" \
  "s_cbranch_scc1 1b\n\t" \
  ".p2align 4\n\t" \
  "2:\n\t" \
  "s_sub_i32 s79, s79, 33\n\t" \
  "s_cmp_gt_i32 s79, 56\n\t" \
  "s_cbranch_scc1 2b\n\t" \
  "buffer_load_dword v187, v200, %[rh], s78 offen sc1\n\t" \
  "s_memtime vcc\n\t" \
  "s_memrealtime s[86:87]\n\t" \
  "s_nop 4\n\t" \
  "s_memrealtime s[60:61]\n\t" \
  "s_nop 4\n\t" \
  "s_memrealtime s[62:63]\n\t" \
  "s_nop 4\n\t" \
  "s_memrealtime s[64:65]\n\t" \
  "s_nop 4\n\t" \
  "s_memrealtime s[66:67]\n\t" \
  "s_nop 4\n\t" \
  "s_memrealtime s[68:69]\n\t" \
  "s_mov_b32 s79, 0\n\t" \
  "s_waitcnt lgkmcnt(0)\n\t" \
  "s_mov_b64 exec, -1\n\t" \
  "ds_read_b128 a[0:3], v194 offset:0\n\t" \
  "ds_read_b128 a[4:7], v194 offset:1024\n\t" \
  "ds_read_b128 a[8:11], v194 offset:2048\n\t" \
  "ds_read_b128 a[12:15], v194 offset:3072\n\t" \
  "ds_read_b128 a[16:19], v194 offset:4096\n\t" \
  "ds_read_b128 a[20:23], v194 offset:5120\n\t" \
  "ds_read_b128 a[24:27], v194 offset:6144\n\t" \
  "ds_read_b128 a[28:31], v194 offset:7168\n\t" \
  "ds_read_b128 a[32:35], v194 offset:8192\n\t" \
  "ds_read_b128 a[36:39], v194 offset:9216\n\t" \
  "ds_read_b128 a[40:43], v194 offset:10240\n\t" \
  "ds_read_b128 a[44:47], v194 offset:11264\n\t" \
  "ds_read_b128 a[48:51], v194 offset:12288\n\t" \
  "ds_read_b128 a[52:55], v194 offset:13312\n\t" \
  "ds_read_b128 a[56:59], v194 offset:14336\n\t" \
  "ds_read_b128 a[60:63], v194 offset:15360\n\t" \
  "ds_read_b128 a[64:67], v194 offset:16384\n\t" \
  "ds_read_b128 a[68:71], v194 offset:17408\n\t" \
  "ds_read_b128 a[72:75], v194 offset:18432\n\t" \
  "ds_read_b128 a[76:79], v194 offset:19456\n\t" \
  "ds_read_b128 a[80:83], v194 offset:20480\n\t" \
  "ds_read_b128 a[84:87], v194 offset:21504\n\t" \
  "ds_read_b128 a[88:91], v194 offset:22528\n\t" \
  "ds_read_b128 a[92:95], v194 offset:23552\n\t" \
  "ds_read_b128 a[96:99], v194 offset:24576\n\t" \
  "ds_read_b128 a[100:103], v194 offset:25600\n\t" \
  "ds_read_b128 a[104:107], v194 offset:26624\n\t" \
  "ds_read_b128 a[108:111], v194 offset:27648\n\t" \
  "ds_read_b128 a[112:115], v194 offset:28672\n\t" \
  "ds_read_b128 a[116:119], v194 offset:29696\n\t" \
  "ds_read_b128 a[120:123], v194 offset:30720\n\t" \
  "ds_read_b128 a[124:127], v194 offset:31744\n\t" \
  "s_mov_b32 exec_hi, 0\n\t" \
  "s_cmp_eq_u32 s86, s60\n\t" \
  "s_addc_u32 s79, s79, 0\n\t" \
  "s_cmp_eq_u32 s86, s62\n\t" \
  "s_addc_u32 s79, s79, 0\n\t" \
  "s_cmp_eq_u32 s86, s64\n\t" \
  "s_addc_u32 s79, s79, 0\n\t" \
  "s_cmp_eq_u32 s86, s66\n\t" \
  "s_addc_u32 s79, s79, 0\n\t" \
  "s_cmp_eq_u32 s86, s68\n\t" \
  "s_addc_u32 s79, s79, 0\n\t" \
  "s_mul_i32 s79, s79, 24\n\t" \
  "s_add_i32 s79, s79, vcc_lo\n\t" \
  "s_lshl4_add_u32 s80, s86, 64\n\t" \
  "s_mov_b32 s41, s79\n\t" \
  "s_mov_b32 s43, s80\n\t" \
  "s_lshl_b32 s83, s43, 8\n\t" \
  "s_add_i32 s54, s54, s43\n\t" \
  "13:\n\t" \
  "s_waitcnt vmcnt(0)\n\t" \
  "s_memtime s[86:87]\n\t" \
  "v_cmp_ne_u32_sdwa vcc, v187, s40 src0_sel:BYTE_0 src1_sel:DWORD\n\t" \
  "v_subrev_u32 v184, s83, v187\n\t" \
  "v_ashrrev_i32 v184, 8, v184\n\t" \
  "s_nop 1\n\t" \
  "v_max_i32_dpp v184, v184, v184 quad_perm:[1,0,3,2] row_mask:0xf bank_mask:0xf\n\t" \
  "s_nop 1\n\t" \
  "v_max_i32_dpp v184, v184, v184 quad_perm:[2,3,0,1] row_mask:0xf bank_mask:0xf\n\t" \
  "s_cbranch_vccnz 14f\n\t" \
  "15:\n\t" \
  "s_nop 0\n\t" \
  "v_max_i32_dpp v184, v184, v184 row_half_mirror row_mask:0xf bank_mask:0xf\n\t" \
  "s_nop 1\n\t" \
  "v_max_i32_dpp v184, v184, v184 row_mirror row_mask:0xf bank_mask:0xf\n\t" \
  "s_mov_b64 exec, -1\n\t" \
  "v_readlane_b32 s57, v184, 0\n\t" \
  "v_readlane_b32 s82, v184, 16\n\t" \
  "s_waitcnt lgkmcnt(0)\n\t" \
  "s_sub_i32 s79, s41, s86\n\t" \
  "s_max_i32 s57, s57, s82\n\t" \
  "s_add_i32 s81, s57, 212\n\t" \
  "s_max_i32 s81, s81, 0\n\t" \
  "s_min_i32 s81, s81, 20000\n\t" \
  "s_mul_i32 s81, s81, s44\n\t" \
  "s_lshr_b32 s81, s81, 16\n\t" \
  "s_add_i32 s79, s79, s81\n\t" \
  "s_sub_i32 s85, s84, s79\n\t" \
  "s_max_i32 s85, s85, 0\n\t" \
  "s_mul_hi_u32 s85, s85, s45\n\t" \
  "s_lshl1_add_u32 s85, s85, s57\n\t" \
  "s_add_i32 s85, s85, s54\n\t" \
  "s_lshl_b32 s85, s85, 8\n\t" \
  "s_or_b32 s85, s85, s40\n\t" \
  "v_mov_b32 v184, s85\n\t" \
  "buffer_store_dword v184, v202, %[rh], s98 offen sc1\n\t" \
  "s_add_i32 s79, s79, -585\n\t" \
  ".p2align 4\n\t" \
  "3:\n\t" \
  "s_sub_i32 s79, s79, 33\n\t" \
  "s_cmp_gt_i32 s79, 56\n\t" \
  "s_cbranch_scc1 3b\n\t" \
  "buffer_load_dwordx4 v[0:3], v193, %[rh], s77 offen offset:0 sc1\n\t" \
  "s_nop 4\n\t" \
  "buffer_load_dwordx4 v[4:7], v193, %[rh], s77 offen offset:1024 sc1\n\t" \
  "s_nop 4\n\t" \
  "buffer_load_dwordx4 v[8:11], v193, %[rh], s77 offen offset:2048 sc1\n\t" \
  "s_nop 4\n\t" \
  "buffer_load_dwordx4 v[12:15], v193, %[rh], s77 offen offset:3072 sc1\n\t" \
  "s_nop 4\n\t" \
  "buffer_load_dwordx4 v[16:19], v193, %[rh], s96 offen offset:0 sc1\n\t" \
  "s_nop 4\n\t" \
  "buffer_load_dwordx4 v[20:23], v193, %[rh], s96 offen offset:1024 sc1\n\t" \
  "s_nop 4\n\t" \
  "buffer_load_dwordx4 v[24:27], v193, %[rh], s96 offen offset:2048 sc1\n\t" \
  "s_nop 4\n\t" \
  "buffer_load_dwordx4 v[28:31], v193, %[rh], s96 offen offset:3072 sc1\n\t" \
  "s_add_i32 s79, s79, 232\n\t" \
  ".p2align 4\n\t" \
  "4:\n\t" \
  "s_sub_i32 s79, s79, 33\n\t" \
  "s_cmp_gt_i32 s79, 56\n\t" \
  "s_cbranch_scc1 4b\n\t" \
  "buffer_load_dwordx4 v[32:35], v195, %[rh], s77 offen offset:0 sc1\n\t" \
  "buffer_load_dwordx4 v[36:39], v195, %[rh], s77 offen offset:2048 sc1\n\t" \
  "10:\n\t" \
  "s_waitcnt vmcnt(9)\n\t" \
  "v_smfmac_f32_16x16x64_bf16 v[40:43], v[0:3], a[0:7], v203\n\t" \
  "v_smfmac_f32_16x16x64_bf16 v[56:59], v[0:3], a[8:15], v203\n\t" \
  "s_waitcnt vmcnt(8)\n\t" \
  "v_smfmac_f32_16x16x64_bf16 v[44:47], v[4:7], a[16:23], v203\n\t" \
  "v_cvt_pk_bf16_f32 v40, v40, v41\n\t" \
  "v_cvt_pk_bf16_f32 v41, v42, v43\n\t" \
  "v_smfmac_f32_16x16x64_bf16 v[60:63], v[4:7], a[24:31], v203\n\t" \
  "v_cvt_pk_bf16_f32 v56, v56, v57\n\t" \
  "v_cvt_pk_bf16_f32 v57, v58, v59\n\t" \
  "s_waitcnt vmcnt(7)\n\t" \
  "v_smfmac_f32_16x16x64_bf16 v[48:51], v[8:11], a[32:39], v203\n\t" \
  "v_cvt_pk_bf16_f32 v42, v44, v45\n\t" \
  "v_cvt_pk_bf16_f32 v43, v46, v47\n\t" \
  "v_smfmac_f32_16x16x64_bf16 v[64:67], v[8:11], a[40:47], v203\n\t" \
  "v_cvt_pk_bf16_f32 v58, v60, v61\n\t" \
  "v_cvt_pk_bf16_f32 v59, v62, v63\n\t" \
  "s_waitcnt vmcnt(6)\n\t" \
  "v_smfmac_f32_16x16x64_bf16 v[52:55], v[12:15], a[48:55], v203\n\t" \
  "v_cvt_pk_bf16_f32 v44, v48, v49\n\t" \
  "v_cvt_pk_bf16_f32 v45, v50, v51\n\t" \
  "v_smfmac_f32_16x16x64_bf16 v[68:71], v[12:15], a[56:63], v203\n\t" \
  "v_cvt_pk_bf16_f32 v60, v64, v65\n\t" \
  "v_cvt_pk_bf16_f32 v61, v66, v67\n\t" \
  "s_waitcnt vmcnt(5)\n\t" \
  "v_smfmac_f32_16x16x64_bf16 v[72:75], v[16:19], a[64:71], v203\n\t" \
  "v_cvt_pk_bf16_f32 v46, v52, v53\n\t" \
  "v_cvt_pk_bf16_f32 v47, v54, v55\n\t" \
  "v_smfmac_f32_16x16x64_bf16 v[88:91], v[16:19], a[72:79], v203\n\t" \
  "v_cvt_pk_bf16_f32 v62, v68, v69\n\t" \
  "v_cvt_pk_bf16_f32 v63, v70, v71\n\t" \
  "s_waitcnt vmcnt(4)\n\t" \
  "v_smfmac_f32_16x16x64_bf16 v[76:79], v[20:23], a[80:87], v203\n\t" \
  "v_cvt_pk_bf16_f32 v72, v72, v73\n\t" \
  "v_cvt_pk_bf16_f32 v73, v74, v75\n\t" \
  "v_smfmac_f32_16x16x64_bf16 v[92:95], v[20:23], a[88:95], v203\n\t" \
  "v_cvt_pk_bf16_f32 v88, v88, v89\n\t" \
  "v_cvt_pk_bf16_f32 v89, v90, v91\n\t" \
  "s_waitcnt vmcnt(3)\n\t" \
  "v_smfmac_f32_16x16x64_bf16 v[80:83], v[24:27], a[96:103], v203\n\t" \
  "v_cvt_pk_bf16_f32 v74, v76, v77\n\t" \
  "v_cvt_pk_bf16_f32 v75, v78, v79\n\t" \
  "v_smfmac_f32_16x16x64_bf16 v[96:99], v[24:27], a[104:111], v203\n\t" \
  "v_cvt_pk_bf16_f32 v90, v92, v93\n\t" \
  "v_cvt_pk_bf16_f32 v91, v94, v95\n\t" \
  "s_waitcnt vmcnt(2)\n\t" \
  "v_smfmac_f32_16x16x64_bf16 v[84:87], v[28:31], a[112:119], v203\n\t" \
  "v_cvt_pk_bf16_f32 v76, v80, v81\n\t" \
  "v_cvt_pk_bf16_f32 v77, v82, v83\n\t" \
  "v_smfmac_f32_16x16x64_bf16 v[100:103], v[28:31], a[120:127], v203\n\t" \
  "v_cvt_pk_bf16_f32 v92, v96, v97\n\t" \
  "v_cvt_pk_bf16_f32 v93, v98, v99\n\t" \
  "s_nop 1\n\t" \
  "v_cvt_pk_bf16_f32 v78, v84, v85\n\t" \
  "v_cvt_pk_bf16_f32 v79, v86, v87\n\t" \
  "s_nop 1\n\t" \
  "v_cvt_pk_bf16_f32 v94, v100, v101\n\t" \
  "v_cvt_pk_bf16_f32 v95, v102, v103\n\t" \
  "s_waitcnt vmcnt(1)\n\t" \
  "v_smfmac_f32_16x16x64_bf16 v[136:139], v[32:35], v[40:47], v204\n\t" \
  "v_smfmac_f32_16x16x64_bf16 v[140:143], v[32:35], v[56:63], v204\n\t" \
  "s_waitcnt vmcnt(0)\n\t" \
  "v_smfmac_f32_16x16x64_bf16 v[136:139], v[36:39], v[72:79], v204\n\t" \
  "v_smfmac_f32_16x16x64_bf16 v[140:143], v[36:39], v[88:95], v204\n\t" \
  "s_nop 3\n\t" \
  "v_permlane32_swap_b32 v136, v138\n\t" \
  "v_permlane32_swap_b32 v137, v139\n\t" \
  "v_pk_add_f32 v[136:137], v[136:137], v[138:139]\n\t" \
  "s_nop 0\n\t" \
  "v_permlane32_swap_b32 v140, v142\n\t" \
  "v_permlane32_swap_b32 v141, v143\n\t" \
  "v_pk_add_f32 v[138:139], v[140:141], v[142:143]\n\t" \
  "ds_write2st64_b64 v197, v[136:137], v[138:139] offset1:2\n\t" \
  "s_waitcnt lgkmcnt(0)\n\t" \
  "s_barrier\n\t" \
  "s_barrier\n\t" \
  "ds_read2_b32 v[148:149], v207 offset1:8\n\t" \
  "s_waitcnt lgkmcnt(0)\n\t" \
  "v_or_b32 v148, v148, v149\n\t" \
  "v_cmp_ne_u32 vcc, 0, v148\n\t" \
  "s_cbranch_vccnz 12f\n\t" \
  "s_and_b32 s58, s58, 4\n\t" \
  "s_add_i32 s80, s57, s43\n\t" \
  "s_sub_i32 s80, s80, s76\n\t" \
  "s_add_i32 s53, s53, -12\n\t" \
  "s_max_i32 s80, s80, s53\n\t" \
  "s_add_i32 s53, s53, 24\n\t" \
  "s_min_i32 s53, s53, s80\n\t" \
  "s_and_b32 s76, s40, 3\n\t" \
  "s_lshl_b32 s76, s76, 18\n\t" \
  "s_add_i32 s77, s76, 0x10000\n\t" \
  "s_add_i32 s78, s76, 0x1000\n\t" \
  "s_add_i32 s79, s76, 0x11000\n\t" \
  "s_mov_b32 s97, 1\n\t" \
  "s_cmp_ge_i32 s40, 62\n\t" \
  "s_cbranch_scc1 31f\n\t" \
  "s_and_b32 s82, s40, 3\n\t" \
  "s_lshl_b32 s82, s82, 20\n\t" \
  "s_add_i32 s82, s82, s48\n\t" \
  "s_andn2_b32 s82, s82, 0x1800\n\t" \
  "s_lshr_b32 s80, s82, 21\n\t" \
  "s_bitcmp1_b64 %[sel], s80\n\t" \
  "s_cselect_b32 s80, 0x2000, 0\n\t" \
  "s_add_i32 s82, s82, s80\n\t" \
  "s_add_i32 s83, s82, 0x1000\n\t" \
  "v_mov_b64 v[188:189], -1\n\t" \
  "v_mov_b64 v[190:191], -1\n\t" \
  "s_sleep 0\n\t" \
  "buffer_store_dwordx4 v[188:191], v192, %[ru], s82 offen offset:0 sc1\n\t" \
  "buffer_store_dwordx4 v[188:191], v192, %[ru], s82 offen offset:1024 sc1\n\t" \
  "buffer_store_dwordx4 v[188:191], v192, %[ru], s82 offen offset:2048 sc1\n\t" \
  "buffer_store_dwordx4 v[188:191], v192, %[ru], s82 offen offset:3072 sc1\n\t" \
  "buffer_store_dwordx4 v[188:191], v192, %[ru], s83 offen offset:0 sc1\n\t" \
  "buffer_store_dwordx4 v[188:191], v192, %[ru], s83 offen offset:1024 sc1\n\t" \
  "buffer_store_dwordx4 v[188:191], v192, %[ru], s83 offen offset:2048 sc1\n\t" \
  "buffer_store_dwordx4 v[188:191], v192, %[ru], s83 offen offset:3072 sc1\n\t" \
  "31:\n\t" \
  "buffer_load_dwordx4 v[0:3], v210, %[rh], s76 offen offset:0 sc1\n\t" \
  "s_nop 10\n\t" \
  "buffer_load_dwordx4 v[4:7], v210, %[rh], s76 offen offset:1024 sc1\n\t" \
  "s_nop 10\n\t" \
  "buffer_load_dwordx4 v[8:11], v210, %[rh], s77 offen offset:0 sc1\n\t" \
  "s_nop 10\n\t" \
  "buffer_load_dwordx4 v[12:15], v210, %[rh], s77 offen offset:1024 sc1\n\t" \
  "s_cmp_eq_u32 s97, 0\n\t" \
  "s_cbranch_scc1 26f\n\t" \
  "s_barrier\n\t" \
  "s_mov_b32 s97, 0\n\t" \
  "26:\n\t" \
  "buffer_load_dwordx4 v[16:19], v210, %[rh], s76 offen offset:2048 sc1\n\t" \
  "s_nop 10\n\t" \
  "buffer_load_dwordx4 v[20:23], v210, %[rh], s76 offen offset:3072 sc1\n\t" \
  "s_nop 10\n\t" \
  "buffer_load_dwordx4 v[24:27], v210, %[rh], s77 offen offset:2048 sc1\n\t" \
  "s_nop 10\n\t" \
  "buffer_load_dwordx4 v[28:31], v210, %[rh], s77 offen offset:3072 sc1\n\t" \
  "s_nop 10\n\t" \
  "buffer_load_dwordx4 v[32:35], v210, %[rh], s78 offen offset:0 sc1\n\t" \
  "s_nop 10\n\t" \
  "buffer_load_dwordx4 v[36:39], v210, %[rh], s78 offen offset:1024 sc1\n\t" \
  "s_nop 10\n\t" \
  "buffer_load_dwordx4 v[40:43], v210, %[rh], s79 offen offset:0 sc1\n\t" \
  "s_nop 10\n\t" \
  "buffer_load_dwordx4 v[44:47], v210, %[rh], s79 offen offset:1024 sc1\n\t" \
  "s_nop 10\n\t" \
  "buffer_load_dwordx4 v[48:51], v210, %[rh], s78 offen offset:2048 sc1\n\t" \
  "s_nop 10\n\t" \
  "buffer_load_dwordx4 v[52:55], v210, %[rh], s78 offen offset:3072 sc1\n\t" \
  "s_nop 10\n\t" \
  "buffer_load_dwordx4 v[56:59], v210, %[rh], s79 offen offset:2048 sc1\n\t" \
  "s_nop 10\n\t" \
  "buffer_load_dwordx4 v[60:63], v210, %[rh], s79 offen offset:3072 sc1\n\t" \
  "s_nop 10\n\t" \
  "buffer_load_dwordx4 v[64:67], v211, %[rh], s76 offen offset:0 sc1\n\t" \
  "s_nop 10\n\t" \
  "buffer_load_dwordx4 v[68:71], v211, %[rh], s76 offen offset:1024 sc1\n\t" \
  "s_nop 10\n\t" \
  "buffer_load_dwordx4 v[72:75], v211, %[rh], s77 offen offset:0 sc1\n\t" \
  "s_nop 10\n\t" \
  "buffer_load_dwordx4 v[76:79], v211, %[rh], s77 offen offset:1024 sc1\n\t" \
  "s_nop 10\n\t" \
  "buffer_load_dwordx4 v[80:83], v211, %[rh], s76 offen offset:2048 sc1\n\t" \
  "s_nop 10\n\t" \
  "buffer_load_dwordx4 v[84:87], v211, %[rh], s76 offen offset:3072 sc1\n\t" \
  "s_nop 10\n\t" \
  "v_mov_b64 v[136:137], 0\n\t" \
  "v_mov_b64 v[138:139], 0\n\t" \
  "v_mov_b64 v[140:141], 0\n\t" \
  "v_mov_b64 v[142:143], 0\n\t" \
  "s_waitcnt vmcnt(20)\n\t" \
  "v_swap_b32 v1, v4\n\t" \
  "v_swap_b32 v3, v6\n\t" \
  "s_nop 1\n\t" \
  "v_mfma_f32_16x16x32_bf16 v[168:171], a[0:3], v[0:3], 0\n\t" \
  "buffer_load_dwordx4 v[88:91], v211, %[rh], s77 offen offset:2048 sc1\n\t" \
  "v_mfma_f32_16x16x32_bf16 v[172:175], a[8:11], v[0:3], 0\n\t" \
  "s_waitcnt vmcnt(19)\n\t" \
  "v_swap_b32 v9, v12\n\t" \
  "v_mfma_f32_16x16x32_bf16 v[168:171], a[4:7], v[4:7], v[168:171]\n\t" \
  "v_swap_b32 v11, v14\n\t" \
  "v_mfma_f32_16x16x32_bf16 v[172:175], a[12:15], v[4:7], v[172:175]\n\t" \
  "buffer_load_dwordx4 v[92:95], v211, %[rh], s77 offen offset:3072 sc1\n\t" \
  "v_mfma_f32_16x16x32_bf16 v[176:179], a[0:3], v[8:11], 0\n\t" \
  "buffer_load_dwordx4 v[96:99], v211, %[rh], s78 offen offset:0 sc1\n\t" \
  "v_mfma_f32_16x16x32_bf16 v[180:183], a[8:11], v[8:11], 0\n\t" \
  "s_waitcnt vmcnt(19)\n\t" \
  "v_swap_b32 v17, v20\n\t" \
  "v_mfma_f32_16x16x32_bf16 v[176:179], a[4:7], v[12:15], v[176:179]\n\t" \
  "v_swap_b32 v19, v22\n\t" \
  "v_mfma_f32_16x16x32_bf16 v[180:183], a[12:15], v[12:15], v[180:183]\n\t" \
  "buffer_load_dwordx4 v[100:103], v211, %[rh], s78 offen offset:1024 sc1\n\t" \
  "v_mfma_f32_16x16x32_bf16 v[168:171], a[16:19], v[16:19], v[168:171]\n\t" \
  "buffer_load_dwordx4 v[104:107], v211, %[rh], s79 offen offset:0 sc1\n\t" \
  "v_mfma_f32_16x16x32_bf16 v[172:175], a[24:27], v[16:19], v[172:175]\n\t" \
  "s_waitcnt vmcnt(19)\n\t" \
  "v_swap_b32 v25, v28\n\t" \
  "v_mfma_f32_16x16x32_bf16 v[168:171], a[20:23], v[20:23], v[168:171]\n\t" \
  "v_swap_b32 v27, v30\n\t" \
  "v_mfma_f32_16x16x32_bf16 v[172:175], a[28:31], v[20:23], v[172:175]\n\t" \
  "buffer_load_dwordx4 v[108:111], v211, %[rh], s79 offen offset:1024 sc1\n\t" \
  "v_mfma_f32_16x16x32_bf16 v[176:179], a[16:19], v[24:27], v[176:179]\n\t" \
  "buffer_load_dwordx4 v[112:115], v211, %[rh], s78 offen offset:2048 sc1\n\t" \
  "v_mfma_f32_16x16x32_bf16 v[180:183], a[24:27], v[24:27], v[180:183]\n\t" \
  "s_waitcnt vmcnt(19)\n\t" \
  "v_swap_b32 v33, v36\n\t" \
  "v_mfma_f32_16x16x32_bf16 v[176:179], a[20:23], v[28:31], v[176:179]\n\t" \
  "v_swap_b32 v35, v38\n\t" \
  "v_mfma_f32_16x16x32_bf16 v[180:183], a[28:31], v[28:31], v[180:183]\n\t" \
  "buffer_load_dwordx4 v[116:119], v211, %[rh], s78 offen offset:3072 sc1\n\t" \
  "v_mfma_f32_16x16x32_bf16 v[168:171], a[32:35], v[32:35], v[168:171]\n\t" \
  "buffer_load_dwordx4 v[120:123], v211, %[rh], s79 offen offset:2048 sc1\n\t" \
  "v_mfma_f32_16x16x32_bf16 v[172:175], a[40:43], v[32:35], v[172:175]\n\t" \
  "s_waitcnt vmcnt(19)\n\t" \
  "v_swap_b32 v41, v44\n\t" \
  "v_mfma_f32_16x16x32_bf16 v[168:171], a[36:39], v[36:39], v[168:171]\n\t" \
  "v_swap_b32 v43, v46\n\t" \
  "v_mfma_f32_16x16x32_bf16 v[172:175], a[44:47], v[36:39], v[172:175]\n\t" \
  "buffer_load_dwordx4 v[124:127], v211, %[rh], s79 offen offset:3072 sc1\n\t" \
  "v_mfma_f32_16x16x32_bf16 v[176:179], a[32:35], v[40:43], v[176:179]\n\t" \
  "v_mfma_f32_16x16x32_bf16 v[180:183], a[40:43], v[40:43], v[180:183]\n\t" \
  "s_waitcnt vmcnt(18)\n\t" \
  "v_swap_b32 v49, v52\n\t" \
  "v_mfma_f32_16x16x32_bf16 v[176:179], a[36:39], v[44:47], v[176:179]\n\t" \
  "v_swap_b32 v51, v54\n\t" \
  "v_mfma_f32_16x16x32_bf16 v[180:183], a[44:47], v[44:47], v[180:183]\n\t" \
  "v_mov_b64 v[40:41], 0\n\t" \
  "v_mov_b64 v[42:43], 0\n\t" \
  "v_mfma_f32_16x16x32_bf16 v[168:171], a[48:51], v[48:51], v[168:171]\n\t" \
  "v_mov_b64 v[44:45], 0\n\t" \
  "v_mov_b64 v[46:47], 0\n\t" \
  "v_mfma_f32_16x16x32_bf16 v[172:175], a[56:59], v[48:51], v[172:175]\n\t" \
  "s_waitcnt vmcnt(16)\n\t" \
  "v_swap_b32 v57, v60\n\t" \
  "v_mfma_f32_16x16x32_bf16 v[168:171], a[52:55], v[52:55], v[168:171]\n\t" \
  "v_swap_b32 v59, v62\n\t" \
  "v_mfma_f32_16x16x32_bf16 v[172:175], a[60:63], v[52:55], v[172:175]\n\t" \
  "v_mov_b64 v[48:49], 0\n\t" \
  "v_mov_b64 v[50:51], 0\n\t" \
  "v_mfma_f32_16x16x32_bf16 v[176:179], a[48:51], v[56:59], v[176:179]\n\t" \
  "v_mov_b64 v[52:53], 0\n\t" \
  "v_mov_b64 v[54:55], 0\n\t" \
  "v_mfma_f32_16x16x32_bf16 v[180:183], a[56:59], v[56:59], v[180:183]\n\t" \
  "s_waitcnt vmcnt(14)\n\t" \
  "v_swap_b32 v65, v68\n\t" \
  "v_mfma_f32_16x16x32_bf16 v[176:179], a[52:55], v[60:63], v[176:179]\n\t" \
  "v_swap_b32 v67, v70\n\t" \
  "v_mfma_f32_16x16x32_bf16 v[180:183], a[60:63], v[60:63], v[180:183]\n\t" \
  "v_mov_b64 v[56:57], 0\n\t" \
  "v_mov_b64 v[58:59], 0\n\t" \
  "v_mfma_f32_16x16x32_bf16 v[168:171], a[64:67], v[64:67], v[168:171]\n\t" \
  "v_mov_b64 v[60:61], 0\n\t" \
  "v_mov_b64 v[62:63], 0\n\t" \
  "v_mfma_f32_16x16x32_bf16 v[172:175], a[72:75], v[64:67], v[172:175]\n\t" \
  "s_waitcnt vmcnt(12)\n\t" \
  "v_swap_b32 v73, v76\n\t" \
  "v_mfma_f32_16x16x32_bf16 v[168:171], a[68:71], v[68:71], v[168:171]\n\t" \
  "v_swap_b32 v75, v78\n\t" \
  "v_mfma_f32_16x16x32_bf16 v[172:175], a[76:79], v[68:71], v[172:175]\n\t" \
  "v_mov_b64 v[64:65], 0\n\t" \
  "v_mov_b64 v[66:67], 0\n\t" \
  "v_mfma_f32_16x16x32_bf16 v[176:179], a[64:67], v[72:75], v[176:179]\n\t" \
  "v_mov_b64 v[68:69], 0\n\t" \
  "v_mov_b64 v[70:71], 0\n\t" \
  "v_mfma_f32_16x16x32_bf16 v[180:183], a[72:75], v[72:75], v[180:183]\n\t" \
  "s_waitcnt vmcnt(10)\n\t" \
  "v_swap_b32 v81, v84\n\t" \
  "v_mfma_f32_16x16x32_bf16 v[176:179], a[68:71], v[76:79], v[176:179]\n\t" \
  "v_swap_b32 v83, v86\n\t" \
  "v_mfma_f32_16x16x32_bf16 v[180:183], a[76:79], v[76:79], v[180:183]\n\t" \
  "v_mov_b64 v[72:73], 0\n\t" \
  "v_mov_b64 v[74:75], 0\n\t" \
  "v_mfma_f32_16x16x32_bf16 v[168:171], a[80:83], v[80:83], v[168:171]\n\t" \
  "v_mov_b64 v[76:77], 0\n\t" \
  "v_mov_b64 v[78:79], 0\n\t" \
  "v_mfma_f32_16x16x32_bf16 v[172:175], a[88:91], v[80:83], v[172:175]\n\t" \
  "s_waitcnt vmcnt(8)\n\t" \
  "v_swap_b32 v89, v92\n\t" \
  "v_mfma_f32_16x16x32_bf16 v[168:171], a[84:87], v[84:87], v[168:171]\n\t" \
  "v_swap_b32 v91, v94\n\t" \
  "v_mfma_f32_16x16x32_bf16 v[172:175], a[92:95], v[84:87], v[172:175]\n\t" \
  "v_mov_b64 v[80:81], 0\n\t" \
  "v_mov_b64 v[82:83], 0\n\t" \
  "v_mfma_f32_16x16x32_bf16 v[176:179], a[80:83], v[88:91], v[176:179]\n\t" \
  "v_mov_b64 v[84:85], 0\n\t" \
  "v_mov_b64 v[86:87], 0\n\t" \
  "v_mfma_f32_16x16x32_bf16 v[180:183], a[88:91], v[88:91], v[180:183]\n\t" \
  "s_waitcnt vmcnt(6)\n\t" \
  "v_swap_b32 v97, v100\n\t" \
  "v_mfma_f32_16x16x32_bf16 v[176:179], a[84:87], v[92:95], v[176:179]\n\t" \
  "v_swap_b32 v99, v102\n\t" \
  "v_mfma_f32_16x16x32_bf16 v[180:183], a[92:95], v[92:95], v[180:183]\n\t" \
  "v_mov_b64 v[88:89], 0\n\t" \
  "v_mov_b64 v[90:91], 0\n\t" \
  "v_mfma_f32_16x16x32_bf16 v[168:171], a[96:99], v[96:99], v[168:171]\n\t" \
  "v_mov_b64 v[92:93], 0\n\t" \
  "v_mov_b64 v[94:95], 0\n\t" \
  "v_mfma_f32_16x16x32_bf16 v[172:175], a[104:107], v[96:99], v[172:175]\n\t" \
  "s_waitcnt vmcnt(4)\n\t" \
  "v_swap_b32 v105, v108\n\t" \
  "v_mfma_f32_16x16x32_bf16 v[168:171], a[100:103], v[100:103], v[168:171]\n\t" \
  "v_swap_b32 v107, v110\n\t" \
  "v_mfma_f32_16x16x32_bf16 v[172:175], a[108:111], v[100:103], v[172:175]\n\t" \
  "v_mov_b64 v[96:97], 0\n\t" \
  "v_mov_b64 v[98:99], 0\n\t" \
  "v_mfma_f32_16x16x32_bf16 v[176:179], a[96:99], v[104:107], v[176:179]\n\t" \
  "v_mov_b64 v[100:101], 0\n\t" \
  "v_mov_b64 v[102:103], 0\n\t" \
  "v_mfma_f32_16x16x32_bf16 v[180:183], a[104:107], v[104:107], v[180:183]\n\t" \
  "s_waitcnt vmcnt(2)\n\t" \
  "v_swap_b32 v113, v116\n\t" \
  "v_mfma_f32_16x16x32_bf16 v[176:179], a[100:103], v[108:111], v[176:179]\n\t" \
  "v_swap_b32 v115, v118\n\t" \
  "v_mfma_f32_16x16x32_bf16 v[180:183], a[108:111], v[108:111], v[180:183]\n\t" \
  "s_nop 0\n\t" \
  "v_mfma_f32_16x16x32_bf16 v[168:171], a[112:115], v[112:115], v[168:171]\n\t" \
  "v_mfma_f32_16x16x32_bf16 v[172:175], a[120:123], v[112:115], v[172:175]\n\t" \
  "s_waitcnt vmcnt(0)\n\t" \
  "v_swap_b32 v121, v124\n\t" \
  "v_mfma_f32_16x16x32_bf16 v[168:171], a[116:119], v[116:119], v[168:171]\n\t" \
  "v_swap_b32 v123, v126\n\t" \
  "v_mfma_f32_16x16x32_bf16 v[172:175], a[124:127], v[116:119], v[172:175]\n\t" \
  "s_nop 0\n\t" \
  "v_mfma_f32_16x16x32_bf16 v[176:179], a[112:115], v[120:123], v[176:179]\n\t" \
  "v_mfma_f32_16x16x32_bf16 v[180:183], a[120:123], v[120:123], v[180:183]\n\t" \
  "v_mfma_f32_16x16x32_bf16 v[176:179], a[116:119], v[124:127], v[176:179]\n\t" \
  "v_mfma_f32_16x16x32_bf16 v[180:183], a[124:127], v[124:127], v[180:183]\n\t" \
  "s_nop 6\n\t" \
  "39:\n\t" \
  "s_add_i32 s40, s40, 2\n\t" \
  "v_cmp_u_f32 vcc, v168, v176\n\t" \
  "s_cbranch_vccnz 32f\n\t" \
  "33:\n\t" \
  "s_barrier\n\t" \
  "s_add_i32 s85, s40, -1\n\t" \
  "s_cmp_ge_i32 s40, 64\n\t" \
  "s_cbranch_scc1 47f\n\t" \
  "s_and_b32 s82, s85, 2\n\t" \
  "s_lshl_b32 s82, s82, 13\n\t" \
  "s_and_b32 s80, s85, 1\n\t" \
  "s_lshl_b32 s80, s80, 12\n\t" \
  "s_add_i32 s82, s82, s80\n\t" \
  "s_add_i32 s82, s82, 0x84000\n\t" \
  "s_mov_b32 exec_hi, 0\n\t" \
  "buffer_load_dword v184, v200, %[rh], s82 offen sc1\n\t" \
  "buffer_load_dword v185, v201, %[rh], s52 offen sc1\n\t" \
  "s_mov_b64 exec, -1\n\t" \
  "47:\n\t" \
  "ds_write_b128 v199, v[168:171] offset:0\n\t" \
  "ds_write_b128 v199, v[172:175] offset:1024\n\t" \
  "ds_write_b128 v199, v[176:179] offset:8192\n\t" \
  "ds_write_b128 v199, v[180:183] offset:9216\n\t" \
  "s_mov_b32 s59, 0\n\t" \
  "s_and_b32 s58, s58, 4\n\t" \
  "s_cmp_lt_i32 s40, 64\n\t" \
  "s_cbranch_scc1 20b\n\t" \
  "s_waitcnt lgkmcnt(0)\n\t" \
  "s_barrier\n\t" \
  "s_mov_b32 s58, 64\n\t" \
  "s_branch 7f\n\t" \
  "22:\n\t" \
  "s_mov_b32 s81, 0\n\t" \
  "s_cmp_eq_u32 s40, 0\n\t" \
  "s_cbranch_scc1 24b\n\t" \
  "s_bitcmp1_b32 s58, 2\n\t" \
  "s_cbranch_scc1 24b\n\t" \
  "s_mov_b64 exec, -1\n\t" \
  "s_sleep 10\n\t" \
  "s_add_u32 s59, s59, 0x10000\n\t" \
  "s_cmp_lt_u32 s59, 0x100000\n\t" \
  "s_cbranch_scc1 19b\n\t" \
  "s_or_b32 s58, s58, 1\n\t" \
  "s_branch 7f\n\t" \
  "14:\n\t" \
  "v_mov_b32 v184, 0\n\t" \
  "s_bitcmp1_b32 s58, 2\n\t" \
  "s_nop 0\n\t" \
  "s_cbranch_scc1 15b\n\t" \
  "s_mov_b32 s84, 870\n\t" \
  "s_add_u32 s59, s59, 0x10000\n\t" \
  "s_cmp_lt_u32 s59, 0x100000\n\t" \
  "s_mov_b64 exec, vcc\n\t" \
  "buffer_load_dword v187, v200, %[rh], s78 offen sc1\n\t" \
  "s_mov_b32 exec_lo, -1\n\t" \
  "s_cbranch_scc1 13b\n\t" \
  "s_mov_b64 exec, -1\n\t" \
  "s_or_b32 s58, s58, 1\n\t" \
  "s_branch 7f\n\t" \
  "37:\n\t" \
  "s_barrier\n\t" \
  "v_mov_b64 v[40:41], 0\n\t" \
  "v_mov_b64 v[42:43], 0\n\t" \
  "v_mov_b64 v[44:45], 0\n\t" \
  "v_mov_b64 v[46:47], 0\n\t" \
  "v_mov_b64 v[48:49], 0\n\t" \
  "v_mov_b64 v[50:51], 0\n\t" \
  "v_mov_b64 v[52:53], 0\n\t" \
  "v_mov_b64 v[54:55], 0\n\t" \
  "v_mov_b64 v[56:57], 0\n\t" \
  "v_mov_b64 v[58:59], 0\n\t" \
  "v_mov_b64 v[60:61], 0\n\t" \
  "v_mov_b64 v[62:63], 0\n\t" \
  "v_mov_b64 v[64:65], 0\n\t" \
  "v_mov_b64 v[66:67], 0\n\t" \
  "v_mov_b64 v[68:69], 0\n\t" \
  "v_mov_b64 v[70:71], 0\n\t" \
  "v_mov_b64 v[72:73], 0\n\t" \
  "v_mov_b64 v[74:75], 0\n\t" \
  "v_mov_b64 v[76:77], 0\n\t" \
  "v_mov_b64 v[78:79], 0\n\t" \
  "v_mov_b64 v[80:81], 0\n\t" \
  "v_mov_b64 v[82:83], 0\n\t" \
  "v_mov_b64 v[84:85], 0\n\t" \
  "v_mov_b64 v[86:87], 0\n\t" \
  "v_mov_b64 v[88:89], 0\n\t" \
  "v_mov_b64 v[90:91], 0\n\t" \
  "v_mov_b64 v[92:93], 0\n\t" \
  "v_mov_b64 v[94:95], 0\n\t" \
  "v_mov_b64 v[96:97], 0\n\t" \
  "v_mov_b64 v[98:99], 0\n\t" \
  "v_mov_b64 v[100:101], 0\n\t" \
  "v_mov_b64 v[102:103], 0\n\t" \
  "v_mov_b64 v[136:137], 0\n\t" \
  "v_mov_b64 v[138:139], 0\n\t" \
  "v_mov_b64 v[140:141], 0\n\t" \
  "v_mov_b64 v[142:143], 0\n\t" \
  "s_add_i32 s40, s40, 2\n\t" \
  "s_branch 33b\n\t" \
  "32:\n\t" \
  "s_and_b32 s80, s58, 36\n\t" \
  "s_cbranch_scc1 33b\n\t" \
  "s_add_i32 s40, s40, -2\n\t" \
  "34:\n\t" \
  "s_or_b32 s59, s59, 128\n\t" \
  "s_and_b32 s76, s40, 3\n\t" \
  "s_lshl_b32 s76, s76, 18\n\t" \
  "s_add_i32 s77, s76, 0x10000\n\t" \
  "s_add_i32 s78, s76, 0x1000\n\t" \
  "s_add_i32 s79, s76, 0x11000\n\t" \
  "35:\n\t" \
  "buffer_load_dwordx4 v[0:3], v210, %[rh], s76 offen offset:0 sc1\n\t" \
  "buffer_load_dwordx4 v[4:7], v210, %[rh], s76 offen offset:1024 sc1\n\t" \
  "buffer_load_dwordx4 v[8:11], v210, %[rh], s77 offen offset:0 sc1\n\t" \
  "buffer_load_dwordx4 v[12:15], v210, %[rh], s77 offen offset:1024 sc1\n\t" \
  "buffer_load_dwordx4 v[16:19], v210, %[rh], s76 offen offset:2048 sc1\n\t" \
  "buffer_load_dwordx4 v[20:23], v210, %[rh], s76 offen offset:3072 sc1\n\t" \
  "buffer_load_dwordx4 v[24:27], v210, %[rh], s77 offen offset:2048 sc1\n\t" \
  "buffer_load_dwordx4 v[28:31], v210, %[rh], s77 offen offset:3072 sc1\n\t" \
  "buffer_load_dwordx4 v[32:35], v210, %[rh], s78 offen offset:0 sc1\n\t" \
  "buffer_load_dwordx4 v[36:39], v210, %[rh], s78 offen offset:1024 sc1\n\t" \
  "buffer_load_dwordx4 v[40:43], v210, %[rh], s79 offen offset:0 sc1\n\t" \
  "buffer_load_dwordx4 v[44:47], v210, %[rh], s79 offen offset:1024 sc1\n\t" \
  "buffer_load_dwordx4 v[48:51], v210, %[rh], s78 offen offset:2048 sc1\n\t" \
  "buffer_load_dwordx4 v[52:55], v210, %[rh], s78 offen offset:3072 sc1\n\t" \
  "buffer_load_dwordx4 v[56:59], v210, %[rh], s79 offen offset:2048 sc1\n\t" \
  "buffer_load_dwordx4 v[60:63], v210, %[rh], s79 offen offset:3072 sc1\n\t" \
  "buffer_load_dwordx4 v[64:67], v211, %[rh], s76 offen offset:0 sc1\n\t" \
  "buffer_load_dwordx4 v[68:71], v211, %[rh], s76 offen offset:1024 sc1\n\t" \
  "buffer_load_dwordx4 v[72:75], v211, %[rh], s77 offen offset:0 sc1\n\t" \
  "buffer_load_dwordx4 v[76:79], v211, %[rh], s77 offen offset:1024 sc1\n\t" \
  "buffer_load_dwordx4 v[80:83], v211, %[rh], s76 offen offset:2048 sc1\n\t" \
  "buffer_load_dwordx4 v[84:87], v211, %[rh], s76 offen offset:3072 sc1\n\t" \
  "buffer_load_dwordx4 v[88:91], v211, %[rh], s77 offen offset:2048 sc1\n\t" \
  "buffer_load_dwordx4 v[92:95], v211, %[rh], s77 offen offset:3072 sc1\n\t" \
  "buffer_load_dwordx4 v[96:99], v211, %[rh], s78 offen offset:0 sc1\n\t" \
  "buffer_load_dwordx4 v[100:103], v211, %[rh], s78 offen offset:1024 sc1\n\t" \
  "buffer_load_dwordx4 v[104:107], v211, %[rh], s79 offen offset:0 sc1\n\t" \
  "buffer_load_dwordx4 v[108:111], v211, %[rh], s79 offen offset:1024 sc1\n\t" \
  "buffer_load_dwordx4 v[112:115], v211, %[rh], s78 offen offset:2048 sc1\n\t" \
  "buffer_load_dwordx4 v[116:119], v211, %[rh], s78 offen offset:3072 sc1\n\t" \
  "buffer_load_dwordx4 v[120:123], v211, %[rh], s79 offen offset:2048 sc1\n\t" \
  "buffer_load_dwordx4 v[124:127], v211, %[rh], s79 offen offset:3072 sc1\n\t" \
  "s_waitcnt vmcnt(0)\n\t" \
  "s_bitcmp1_b32 s58, 2\n\t" \
  "s_cbranch_scc1 36f\n\t" \
  "v_max_u32 v148, v0, v1\n\t" \
  "v_max3_u32 v148, v148, v2, v3\n\t" \
  "v_max3_u32 v148, v148, v4, v5\n\t" \
  "v_max3_u32 v148, v148, v6, v7\n\t" \
  "v_max3_u32 v148, v148, v8, v9\n\t" \
  "v_max3_u32 v148, v148, v10, v11\n\t" \
  "v_max3_u32 v148, v148, v12, v13\n\t" \
  "v_max3_u32 v148, v148, v14, v15\n\t" \
  "v_max3_u32 v148, v148, v16, v17\n\t" \
  "v_max3_u32 v148, v148, v18, v19\n\t" \
  "v_max3_u32 v148, v148, v20, v21\n\t" \
  "v_max3_u32 v148, v148, v22, v23\n\t" \
  "v_max3_u32 v148, v148, v24, v25\n\t" \
  "v_max3_u32 v148, v148, v26, v27\n\t" \
  "v_max3_u32 v148, v148, v28, v29\n\t" \
  "v_max3_u32 v148, v148, v30, v31\n\t" \
  "v_max3_u32 v148, v148, v32, v33\n\t" \
  "v_max3_u32 v148, v148, v34, v35\n\t" \
  "v_max3_u32 v148, v148, v36, v37\n\t" \
  "v_max3_u32 v148, v148, v38, v39\n\t" \
  "v_max3_u32 v148, v148, v40, v41\n\t" \
  "v_max3_u32 v148, v148, v42, v43\n\t" \
  "v_max3_u32 v148, v148, v44, v45\n\t" \
  "v_max3_u32 v148, v148, v46, v47\n\t" \
  "v_max3_u32 v148, v148, v48, v49\n\t" \
  "v_max3_u32 v148, v148, v50, v51\n\t" \
  "v_max3_u32 v148, v148, v52, v53\n\t" \
  "v_max3_u32 v148, v148, v54, v55\n\t" \
  "v_max3_u32 v148, v148, v56, v57\n\t" \
  "v_max3_u32 v148, v148, v58, v59\n\t" \
  "v_max3_u32 v148, v148, v60, v61\n\t" \
  "v_max3_u32 v148, v148, v62, v63\n\t" \
  "v_max3_u32 v148, v148, v64, v65\n\t" \
  "v_max3_u32 v148, v148, v66, v67\n\t" \
  "v_max3_u32 v148, v148, v68, v69\n\t" \
  "v_max3_u32 v148, v148, v70, v71\n\t" \
  "v_max3_u32 v148, v148, v72, v73\n\t" \
  "v_max3_u32 v148, v148, v74, v75\n\t" \
  "v_max3_u32 v148, v148, v76, v77\n\t" \
  "v_max3_u32 v148, v148, v78, v79\n\t" \
  "v_max3_u32 v148, v148, v80, v81\n\t" \
  "v_max3_u32 v148, v148, v82, v83\n\t" \
  "v_max3_u32 v148, v148, v84, v85\n\t" \
  "v_max3_u32 v148, v148, v86, v87\n\t" \
  "v_max3_u32 v148, v148, v88, v89\n\t" \
  "v_max3_u32 v148, v148, v90, v91\n\t" \
  "v_max3_u32 v148, v148, v92, v93\n\t" \
  "v_max3_u32 v148, v148, v94, v95\n\t" \
  "v_max3_u32 v148, v148, v96, v97\n\t" \
  "v_max3_u32 v148, v148, v98, v99\n\t" \
  "v_max3_u32 v148, v148, v100, v101\n\t" \
  "v_max3_u32 v148, v148, v102, v103\n\t" \
  "v_max3_u32 v148, v148, v104, v105\n\t" \
  "v_max3_u32 v148, v148, v106, v107\n\t" \
  "v_max3_u32 v148, v148, v108, v109\n\t" \
  "v_max3_u32 v148, v148, v110, v111\n\t" \
  "v_max3_u32 v148, v148, v112, v113\n\t" \
  "v_max3_u32 v148, v148, v114, v115\n\t" \
  "v_max3_u32 v148, v148, v116, v117\n\t" \
  "v_max3_u32 v148, v148, v118, v119\n\t" \
  "v_max3_u32 v148, v148, v120, v121\n\t" \
  "v_max3_u32 v148, v148, v122, v123\n\t" \
  "v_max3_u32 v148, v148, v124, v125\n\t" \
  "v_max3_u32 v148, v148, v126, v127\n\t" \
  "v_cmp_eq_u32 vcc, -1, v148\n\t" \
  "s_cbranch_vccz 36f\n\t" \
  "s_add_u32 s59, s59, 0x1000000\n\t" \
  "s_cmp_lt_u32 s59, 0x10000000\n\t" \
  "s_cbranch_scc1 35b\n\t" \
  "s_or_b32 s58, s58, 9\n\t" \
  "s_branch 7f\n\t" \
  "36:\n\t" \
  "s_mov_b32 s97, 0\n\t" \
  "s_and_b32 s58, s58, 4\n\t" \
  "s_or_b32 s58, s58, 32\n\t" \
  "s_branch 31b\n\t" \
  "18:\n\t" \
  "v_mov_b32 v186, -1\n\t" \
  "s_and_b32 s80, s40, 3\n\t" \
  "s_mul_i32 s77, s80, 0xc000\n\t" \
  "s_add_i32 s77, s77, 0x40000\n\t" \
  "s_lshl_b32 s78, s80, 14\n\t" \
  "s_add_i32 s78, s78, 0x70000\n\t" \
  "s_mov_b32 s57, 0\n\t" \
  "s_add_i32 s96, s77, 0x1000\n\t" \
  "s_and_b32 s98, s40, 2\n\t" \
  "s_lshl_b32 s98, s98, 13\n\t" \
  "s_and_b32 s80, s40, 1\n\t" \
  "s_lshl_b32 s80, s80, 12\n\t" \
  "s_add_i32 s98, s98, s80\n\t" \
  "s_add_i32 s98, s98, 0x84000\n\t" \
  "12:\n\t" \
  "s_or_b32 s58, s58, 3\n\t" \
  "11:\n\t" \
  "buffer_load_dwordx4 v[0:3], v193, %[rh], s77 offen offset:0 sc1\n\t" \
  "buffer_load_dwordx4 v[4:7], v193, %[rh], s77 offen offset:1024 sc1\n\t" \
  "buffer_load_dwordx4 v[8:11], v193, %[rh], s77 offen offset:2048 sc1\n\t" \
  "buffer_load_dwordx4 v[12:15], v193, %[rh], s77 offen offset:3072 sc1\n\t" \
  "buffer_load_dwordx4 v[16:19], v193, %[rh], s96 offen offset:0 sc1\n\t" \
  "buffer_load_dwordx4 v[20:23], v193, %[rh], s96 offen offset:1024 sc1\n\t" \
  "buffer_load_dwordx4 v[24:27], v193, %[rh], s96 offen offset:2048 sc1\n\t" \
  "buffer_load_dwordx4 v[28:31], v193, %[rh], s96 offen offset:3072 sc1\n\t" \
  "buffer_load_dwordx4 v[32:35], v195, %[rh], s77 offen offset:0 sc1\n\t" \
  "buffer_load_dwordx4 v[36:39], v195, %[rh], s77 offen offset:2048 sc1\n\t" \
  "v_mov_b64 v[40:41], 0\n\t" \
  "v_mov_b64 v[42:43], 0\n\t" \
  "v_mov_b64 v[44:45], 0\n\t" \
  "v_mov_b64 v[46:47], 0\n\t" \
  "v_mov_b64 v[48:49], 0\n\t" \
  "v_mov_b64 v[50:51], 0\n\t" \
  "v_mov_b64 v[52:53], 0\n\t" \
  "v_mov_b64 v[54:55], 0\n\t" \
  "v_mov_b64 v[56:57], 0\n\t" \
  "v_mov_b64 v[58:59], 0\n\t" \
  "v_mov_b64 v[60:61], 0\n\t" \
  "v_mov_b64 v[62:63], 0\n\t" \
  "v_mov_b64 v[64:65], 0\n\t" \
  "v_mov_b64 v[66:67], 0\n\t" \
  "v_mov_b64 v[68:69], 0\n\t" \
  "v_mov_b64 v[70:71], 0\n\t" \
  "v_mov_b64 v[72:73], 0\n\t" \
  "v_mov_b64 v[74:75], 0\n\t" \
  "v_mov_b64 v[76:77], 0\n\t" \
  "v_mov_b64 v[78:79], 0\n\t" \
  "v_mov_b64 v[80:81], 0\n\t" \
  "v_mov_b64 v[82:83], 0\n\t" \
  "v_mov_b64 v[84:85], 0\n\t" \
  "v_mov_b64 v[86:87], 0\n\t" \
  "v_mov_b64 v[88:89], 0\n\t" \
  "v_mov_b64 v[90:91], 0\n\t" \
  "v_mov_b64 v[92:93], 0\n\t" \
  "v_mov_b64 v[94:95], 0\n\t" \
  "v_mov_b64 v[96:97], 0\n\t" \
  "v_mov_b64 v[98:99], 0\n\t" \
  "v_mov_b64 v[100:101], 0\n\t" \
  "v_mov_b64 v[102:103], 0\n\t" \
  "v_mov_b64 v[136:137], 0\n\t" \
  "v_mov_b64 v[138:139], 0\n\t" \
  "v_mov_b64 v[140:141], 0\n\t" \
  "v_mov_b64 v[142:143], 0\n\t" \
  "s_waitcnt vmcnt(0)\n\t" \
  "s_add_u32 s59, s59, 1\n\t" \
  "s_bitcmp1_b32 s58, 2\n\t" \
  "s_cbranch_scc1 10b\n\t" \
  "v_max_u32 v148, v0, v1\n\t" \
  "v_max3_u32 v148, v148, v2, v3\n\t" \
  "v_max3_u32 v148, v148, v4, v5\n\t" \
  "v_max3_u32 v148, v148, v6, v7\n\t" \
  "v_max3_u32 v148, v148, v8, v9\n\t" \
  "v_max3_u32 v148, v148, v10, v11\n\t" \
  "v_max3_u32 v148, v148, v12, v13\n\t" \
  "v_max3_u32 v148, v148, v14, v15\n\t" \
  "v_max3_u32 v148, v148, v16, v17\n\t" \
  "v_max3_u32 v148, v148, v18, v19\n\t" \
  "v_max3_u32 v148, v148, v20, v21\n\t" \
  "v_max3_u32 v148, v148, v22, v23\n\t" \
  "v_max3_u32 v148, v148, v24, v25\n\t" \
  "v_max3_u32 v148, v148, v26, v27\n\t" \
  "v_max3_u32 v148, v148, v28, v29\n\t" \
  "v_max3_u32 v148, v148, v30, v31\n\t" \
  "v_max3_u32 v148, v148, v32, v33\n\t" \
  "v_max3_u32 v148, v148, v34, v35\n\t" \
  "v_max3_u32 v148, v148, v36, v37\n\t" \
  "v_max3_u32 v148, v148, v38, v39\n\t" \
  "v_cmp_eq_u32 vcc, -1, v148\n\t" \
  "s_cbranch_vccz 10b\n\t" \
  "s_bitcmp1_b32 s59, 6\n\t" \
  "s_cbranch_scc0 11b\n\t" \
  "7:\n\t" \
  "92:\n\t" \
  "s_mov_b32 s42, s58\n\t" \
  "s_waitcnt vmcnt(0) lgkmcnt(0)\n\t" \
  ""

#define V53 "v0", "v1", "v2", "v3", "v4", "v5", "v6", "v7", "v8", "v9", "v10", "v11", "v12", "v13", "v14", "v15", "v16", "v17", \
  "v18", "v19", "v20", "v21", "v22", "v23", "v24", "v25", "v26", "v27", "v28", "v29", "v30", "v31", "v32", "v33", "v34", "v35", \
  "v36", "v37", "v38", "v39", "v40", "v41", "v42", "v43", "v44", "v45", "v46", "v47", "v48", "v49", "v50", "v51", "v52"
#define V100 V53, "v53", "v54", "v55", "v56", "v57", "v58", "v59", "v60", "v61", "v62", "v63", "v64", "v65", "v66", "v67", "v68", "v69", "v70", \
  "v71", "v72", "v73", "v74", "v75", "v76", "v77", "v78", "v79", "v80", "v81", "v82", "v83", "v84", "v85", "v86", "v87", "v88", \
  "v89", "v90", "v91", "v116", "v117", "v118", "v119"
#define STEP_PV
#define CB_CL "s60", "s61", "s62", "s63", "s64", "s65", "s66", "s67", "s68", "s69",
#define STEPS(BODY, ...) asm volatile(BODY : "=&{v[92:99]}"(X), "+{s[40:55]}"(P) STEP_PV \
  : "{v[120:151]}"(W.v[0]), "{v[152:183]}"(W.v[1]), "{v[184:215]}"(W.v[2]), "{v[216:247]}"(W.v[3]), "{v[100:115]}"(C), [rh] "s"(rh), [wv] "s"(w), [ru] "s"(rum), [sel] "s"(sel) __VA_ARGS__ \
  : "memory", "vcc", "scc", "s56", "s57", "s58", "s59", "s76", "s77", "s78", "s79", "s80", "s81", "s82", "s83", "s84", "s85", "s86", "s87", "s88", "s89", \
    "s90", "s91", "s92", "s93", "s94", "s95", "s96", "s97", V100)
__device__ __forceinline__ void keep_a(const Wts& W) {
#pragma unroll
  for (int i = 0; i < 32; ++i) asm volatile("" :: "a"(W.a[i]));
}
__device__ __forceinline__ char* lds_base(char* smem, int off) { asm volatile("" : "+v"(off)); return smem + off; }
__device__ __forceinline__ u32x2 tr4(unsigned p) { return __builtin_bit_cast(u32x2, __builtin_amdgcn_ds_read_tr16_b64_v4i16((LAS bf16x4*)(uintptr_t)p)); }
__device__ __forceinline__ float rb1(float x) { return __uint_as_float(pk2(0.f, x)); }

__device__ __forceinline__ void steps_init(u32x16& P, int h, int w, unsigned place) {
#pragma unroll
  for (int i = 0; i < 16; ++i) P[i] = 0;
  P[2] = 1; P[3] = CDUR; P[14] = H_MEM + (w >> 1) * CHK + (w & 1) * 4096 + (h & RHO) * 1024; P[15] = place;
}
__device__ __forceinline__ void steps_enter(u32x16& P, Clk& ck, bool dead) {
  ck.sync();
  P[2] = (P[2] & 3u) | (dead ? 4u : 0u); P[6] = ck.c2t << 15; P[11] = ck.ce; P[12] = ck.t2c; P[13] = ck.te;
#pragma unroll
  for (int i = 0; i < 16; ++i) P[i] = __builtin_amdgcn_readfirstlane(P[i]);
}

__device__ __forceinline__ void role_qk(const ChainArgs& a, int h, char* smem, const int w, Gate& G) {
  int lane = opaque_tid(0);
  const int b = h; (void)b;
  u64* prof = a.prof; (void)prof;
  PDECL(L_PRF, 4);
  const float CS = 0.08838834764831845f * 1.4426950408889634f;
  GAS unsigned* abortw = (GAS unsigned*)(a.sync + SY_ABORT);
  bool dead = false;
  Wts W;
  Clk ck; ck.init();
  u32x4 gv;
  if (QK_D) while (__builtin_amdgcn_s_memrealtime() - G.entry < QK_D) __builtin_amdgcn_s_sleep(8);
  load_weights<0>(a, h, w, lane, smem, W, G, gv);
  TP(1);
  if (!G.look(a.sync, gv)) return;
  PZERO;
  const Sel sel = G.sel;
  const __amdgpu_buffer_rsrc_t rh = rsrc_hot(a.sync, sel), rum = rsrc_u(a.sync);
  auto pm_o = [&](int l) { return 2 * CHK + h * 256 + (l & 1) * 128 + (l >> 3) * 16 + (l & 6) * 2; };
  auto fac = [&](int l, int hf) { return ((l & 7) >> 1) == 2 * (l >> 5) + ((h >> 1) & 1) && ((l >> 3) & 1) == hf ? 1.f : 0.f; };
  u32x16 C, P;
  {
    const int r = lane & 15, g = lane >> 4;
    const unsigned l0 = (unsigned)(uintptr_t)(LAS char*)smem;
    C[0] = lane * 16; C[1] = l0 + L_MEM + lane * 16; C[2] = l0 + L_WTS + w * 4096 + lane * 16; C[3] = C[2] + 65536;
    C[4] = (lane & 2) ? 0xEEEE : 0x4444;
    C[5] = l0 + L_XCH + (16 * w + 2 * (r >> 2) + (r & 1)) * 32 + g * 8 + ((r >> 1) & 1) * 4;
    C[6] = l0 + L_XK + (32 * w + r) * 16 + g * 4;
    C[7] = w == 3 ? H_PX + (h >> 3) * CHK + (h & 7) * 1024 + lane * 16 : w ? (2 * h + 1) * CHK + (w - 1) * 4096 + lane * 16 : l0 + L_XCH + (8 * g + (r >> 2)) * 32 + (r & 3) * 8;
    C[8] = l0 + L_XK + (8 * g + (r >> 2)) * 16 + (r & 1) * 8;
    C[9] = w ? (lane & 1) * 4096 : (lane == 0 ? H_ANN + h * 128 : 0x7fffffffu);
    C[10] = w == 0 ? (h >> 4) * CHK + (h & 15) * 512 + ((lane >> 1) & 1) * 256 + (lane >> 5) * 128 + (lane & 1) * 64 + ((lane >> 3) & 3) * 16 + ((lane >> 2) & 1) * 8 :
            w < 3 ? H_PX + ((w - 1) * 4 + (h >> 3)) * CHK + (h & 7) * 1024 + lane * 16 :
            lane < 32 ? (h >> 4) * CHK + (h & 15) * 512 + lane * 16 : (lane < 48 ? 2 * CHK + h * 256 + (lane - 32) * 16 : 0x7fffffffu);
    C[11] = w == 0 ? pm_o(lane) : (lane < (w == 3 ? 32 : 16) ? H_PX + (w == 3 ? 8 + g : 7 + w) * CHK + hpos(h) * 256 + r * 16 : 0x7fffffffu);
    C[12] = H_TR + (TRW ? 8 * w + (lane & 7) : lane & 31) * 128; C[13] = l0 + L_PRF + w * 1024 + (lane >> 6);
    unsigned mk = 0;
#pragma unroll
    for (int i = 0; i < 8; ++i) mk |= (16 * ((i >> 1) & 1) + 8 * (g & 1) + 4 * (g >> 1) + 2 * (i & 1) + (i >> 2) > 16 * (w - 1) + r ? 1u : 0u) << i;
    C[14] = w ? mk : __float_as_uint(fac(lane, 0)); C[15] = __float_as_uint(fac(lane, 1));
    asm volatile("" : "+v"(C));
  }
  steps_init(P, h, w, 2 * h * CHK + max(3 * w - 4, 0) * 1024);
  u32x4 k0[2];
  start_wait(a.sync, abortw, lane, dead, CT_Q + ct_near(sel) + h, 1u, [&]() { for (int i = 0; i < 2; ++i) k0[i] = ld16(rum, x_slot(1, 0, h, sel) + (2 * w + i) * 1024 + lane * 16); });
  G.arrive(a.sync);
#pragma unroll
  for (int i = 0; i < 2; ++i) *(u32x4*)(smem + L_KX + (2 * w + i) * 1024 + lane * 16) = k0[i];
  SSTAMP(96 + h);
  TP(2);
  GAS unsigned* kcp = ctr(a, CT_Q + ct_near(sel) + h);
  unsigned spins = 0;
  while (true) {
    u32x8 X; u32x16 pv; (void)pv;
    steps_enter(P, ck, dead);
    STEPS(QK_BODY, , [cs] "s"(CS), [kc] "s"(kcp));
    keep_a(W);
    const unsigned res = __builtin_amdgcn_readfirstlane(P[2]), s = __builtin_amdgcn_readfirstlane(P[4]);
    lane = opaque_tid(0);
    if (res & 8u) {
      auto sw = [](float v, int k) { return __int_as_float(k == 0 ? __builtin_amdgcn_mov_dpp(__float_as_int(v), 0xB1, 0xf, 0xf, false) :
                                                           k == 1 ? __builtin_amdgcn_mov_dpp(__float_as_int(v), 0x4E, 0xf, 0xf, false) :
                                                                    __builtin_amdgcn_mov_dpp(__float_as_int(v), 0x141, 0xf, 0xf, false)); };
      float x[5], d = fmaxf(__uint_as_float(X[4]), 0.f);
      d = fmaxf(d, sw(d, 0)); d = fmaxf(d, sw(d, 1)); d = fmaxf(d, sw(d, 2));
#pragma unroll
      for (int i = 0; i < 5; ++i) x[i] = __builtin_amdgcn_exp2f(__uint_as_float(X[i]) - d);
      float z = ((x[0] + x[1]) + (x[2] + x[3])) + x[4];
      z += sw(z, 0); z += sw(z, 1); z += sw(z, 2);
      z = __builtin_amdgcn_rcpf(z);
      const float y = sw(x[4], 0), f = __builtin_amdgcn_exp2f(-d) * z;
      const unsigned pm = (lane & 1) ? pk2(fac(lane, 0) * f, fac(lane, 1) * f) : pk2(x[4] * z, y * z);
      if (!(__uint_as_float(X[5]) < 1.0e18f))
        __builtin_amdgcn_raw_buffer_store_b32(nosent(pm), rh, pm_o(lane), H_P + ((s - 1) & 3) * 3 * CHK, ST_AUX);
    }
    if (res & 64u) break;
    if (res & 32u) {
      u32x4 k[3];
      start_wait(a.sync, abortw, lane, dead, CT_Q + ct_near(sel) + h, s + 1, [&]() { for (int i = 0; i < 3; ++i) k[i] = ld16(rum, x_slot(1, s, h, sel) + max(3 * w - 4 + i, 0) * 1024 + lane * 16); });
#pragma unroll
      for (int i = 0; i < 3; ++i) *(u32x4*)(smem + L_KX + max(3 * w - 4 + i, 0) * 1024 + lane * 16) = k[i];
    }
    if (res & 17u) {
      spins |= 63u;
      if (dead || poll_fail(spins, abortw, lane)) dead = true;
    }
  }
  PDUMP(0);
}

__device__ __forceinline__ void role_vo(const ChainArgs& a, int h, char* smem, const int w, Gate& G) {
  int lane = opaque_tid(0);
  const int b = 32 + h; (void)b;
  u64* prof = a.prof; (void)prof;
  PDECL(L_PRF, 4);
  GAS unsigned* abortw = (GAS unsigned*)(a.sync + SY_ABORT);
  bool dead = false;
  Wts W;
  Clk ck; ck.init();
  u32x4 gv;
  load_weights<1>(a, h, w, lane, smem, W, G, gv);
  if (!G.look(a.sync, gv)) return;
  PZERO;
  const Sel sel = G.sel;
  const __amdgpu_buffer_rsrc_t rh = rsrc_hot(a.sync, sel), rum = rsrc_u(a.sync);
  u32x16 C, P;
  {
    const int r = lane & 15, g = lane >> 4;
    const unsigned l0 = (unsigned)(uintptr_t)(LAS char*)smem;
#pragma unroll
    for (int i = 0; i < 16; ++i) C[i] = H_TR + (TRW ? 8 * w + (lane & 7) : lane & 31) * 128;
    C[0] = lane * 16; C[1] = l0 + L_MEM + lane * 16; C[2] = l0 + L_WTS + w * 4096 + lane * 16; C[3] = C[2] + 65536;
    C[4] = (lane & 2) ? 0xEEEE : 0x4444;
    C[5] = l0 + L_XCH + (32 * w + r) * 16 + g * 4;
    C[6] = (lane & 8) ? 0xEEEE : 0x4444;
    C[7] = l0 + L_XCH + (32 * (g >> 1) + 8 * (g & 1) + (r >> 2)) * 16 + (r & 1) * 8;
    C[8] = ((g & 1) * 8 + (g >> 1)) * CHK + r * 16;
    C[13] = l0 + L_PRF + w * 1024 + (lane >> 6);
    asm volatile("" : "+v"(C));
  }
  start_wait(a.sync, abortw, lane, dead, -1, 0u, [] {});
  G.arrive(a.sync);
  SSTAMP(128 + h);
  steps_init(P, h, w, SY_UM + (16 * (h & 3) + 2 * w) * CHK + hpos(h) * 256);
  unsigned spins = 0;
  while (true) {
    u32x8 X; u32x16 pv; (void)pv;
    steps_enter(P, ck, dead);
    STEPS(VO_BODY);
    keep_a(W);
    lane = opaque_tid(0);
    if (__builtin_amdgcn_readfirstlane(P[2]) & 64u) break;
    spins |= 63u;
    if (dead || poll_fail(spins, abortw, lane)) dead = true;
  }
  PDUMP(0);
}

__device__ __forceinline__ void calibrate(const ChainArgs& a, int cal, int slot, u64 entry) {
  const u32x4 rs = own_rsrc(a.sync, 64 * PAGE);
  const unsigned so = cal * PAGE + (slot >> 1) * CHK + (slot & 1) * 4096;
  unsigned best[2] = {1u << 30, 1u << 30};
  bool sure;
  {
    unsigned z = 0;
    asm volatile("s_mov_b64 exec, 0xffff\n\tbuffer_atomic_or %0, %1, %2, %3 offen sc0 sc1\n\ts_mov_b64 exec, -1\n\ts_waitcnt vmcnt(0)"
                 : "+v"(z) : "v"((opaque_tid(0) & 8) * 1024 + (opaque_tid(0) & 7) * 256), "s"(rs), "s"(so) : "memory");
  }
  for (int i = 0;; ++i) {
    sure = min(best[0], best[1]) < CALT && max(best[0], best[1]) - min(best[0], best[1]) > 110u;
    if (i >= CALN && (sure || __builtin_amdgcn_s_memrealtime() - entry > CALM)) break;
#pragma unroll
    for (int hf = 0; hf < 2; ++hf) {
      u64 c0, c1;
      unsigned z = 0;
      asm volatile("s_mov_b64 exec, 1\n\ts_memtime %0\n\ts_waitcnt lgkmcnt(0)\n\tbuffer_atomic_or %2, off, %3, %4 sc0 sc1\n\ts_waitcnt vmcnt(0)\n\ts_memtime %1\n\ts_mov_b64 exec, -1\n\ts_waitcnt lgkmcnt(0)"
                   : "=&s"(c0), "=&s"(c1), "+v"(z) : "s"(rs), "s"(so + hf * 8192 + (i & 7) * 256) : "memory");
      best[hf] = min(best[hf], (unsigned)(c1 - c0));
    }
  }
  const u64 hs = (__builtin_amdgcn_s_memrealtime() & ~3ull) | (sure ? 2u : 0u) | ((best[1] < best[0] ? 1u : 0u) ^ die_half());
  if (opaque_tid(0) == 0) __hip_atomic_store((GAS u64*)ctr(a, CT_CAL + (slot >> 1)) + (slot & 1), hs, __ATOMIC_RELAXED, __HIP_MEMORY_SCOPE_AGENT);
}
__device__ __forceinline__ u32x4 pk8(f32x4 a, f32x4 b) { return (u32x4){pk2(a[0], a[1]), pk2(a[2], a[3]), pk2(b[0], b[1]), pk2(b[2], b[3])}; }
__device__ __forceinline__ unsigned pk2v(float a, float b) { unsigned r; asm volatile("v_cvt_pk_bf16_f32 %0, %1, %2" : "=v"(r) : "v"(a), "v"(b)); return r; }
__device__ __forceinline__ u32x4 pk8v(f32x4 a, f32x4 b) { asm volatile("s_nop 7"); return (u32x4){pk2v(a[0], a[1]), pk2v(a[2], a[3]), pk2v(b[0], b[1]), pk2v(b[2], b[3])}; }
__device__ __forceinline__ void st16g(u16* p, u32x4 v) { asm volatile("global_store_dwordx4 %0, %1, off " OUTP "\n\ts_nop 1" :: "v"(p), "v"(v) : "memory"); }
__device__ __forceinline__ void role_cb(const ChainArgs& a, int cbi, char* smem, const int w, Gate& G) {
  int lane = opaque_tid(0);
  const int b = 64 + cbi; (void)b;
  const int par = cbi >> 5, ci = cbi & 31, J = ci >> 3;
  u64* prof = a.prof; (void)prof;
  PDECL(65536, 8); PZERO;
  GAS unsigned* abortw = (GAS unsigned*)(a.sync + SY_ABORT);
  bool dead = false;
  const int tw = 2 * ci + (w & 1);
  const unsigned mo = (tw >> 5) * CHK + (tw & 31) * 256;
  Sel sel = 0;
  auto start = [&]() {
    lane = opaque_tid(0);
    SSTAMP(0 + ci);
    unsigned m0 = 0;
    if (par == 0 && w < 2) {
      const u16* m = a.mem0 + (2 * (lane >> 4) + ((lane >> 2) & 1)) * DM + 16 * tw + (lane & 8);
      m0 = m[lane & 3] | (unsigned)m[7 - (lane & 3)] << 16;
    }
    if (!G.look(a.sync, (u32x4){0u, 0u, 0u, 0u})) return false;
    sel = G.sel;
    SSTAMP(64 + ci);
    const __amdgpu_buffer_rsrc_t rh = rsrc_hot(a.sync, sel), rum = rsrc_u(a.sync);
    const unsigned s1v = ~(unsigned)(lane >> 6);
    const u32x4 sv = {s1v, s1v, s1v, s1v};
    auto um = [&](int k) {
#pragma unroll
      for (int i = 0; i < 4; ++i) st16(rum, near_half(SY_UM + (64 * k + tw) * CHK, sel) + (w >> 1) * 4096 + i * 1024 + lane * 16, sv);
    };
    um(par);
    const unsigned dv = cbi == H_DLT / CHK ? K_DEF * TU : s1v;
#pragma unroll
    for (int i = 0; i < 2; ++i) st16(rh, cbi >= 2 && cbi < 45 ? (cbi < 35 ? cbi * CHK : H_PX + (cbi - 35) * CHK) + w * 2048 + i * 1024 + lane * 16 : 0x7fffffffu, (u32x4){dv, s1v, s1v, s1v});
    if (par == 0 && w < 2) st4(rh, H_MEM + mo + lane * 4, nosent(m0));
    asm volatile("s_waitcnt vmcnt(0)" ::: "memory");
    lds_barrier();
    if (w == 0 && lane == 0) ctr_add(a, CT_INIT + J, 1u);
    SSTAMP(256 + ci);
    TP(6);
    G.arrive(a.sync);
    um(par + 2);
    {
      const __amdgpu_buffer_rsrc_t rx = mk_rsrc((void*)a.X, 4u << 20);
      u32x4 v[16];
#pragma unroll
      for (int i = 0; i < 16; ++i) v[i] = __builtin_amdgcn_raw_buffer_load_b128(rx, (unsigned)(cbi * 4096 + w * 1024 + lane * 16), (unsigned)(i * 262144), 0);
#pragma unroll
      for (int i = 0; i < 16; ++i) asm volatile("" :: "v"(v[i]));
    }
    SSTAMP(224 + ci);
    start_wait(a.sync, abortw, lane, dead, -1, 0u, [] {});
    return true;
  };
  if (!start()) return;
  Wts W;
#pragma unroll
  for (int i = 0; i < 32; ++i) asm volatile("" : "=a"(W.a[i]));
  u32x16 C, P; u32x8 C2;
  {
    const unsigned l0 = (unsigned)(uintptr_t)(LAS char*)smem, oor = 0x7fffffffu;
    const int r = lane & 7, t = (lane >> 3) & 1, g = lane >> 4, e = w & 1;
    const bool L = w < 2;
    C[0] = lane * 16;
    C[1] = L ? l0 + 32768 + e * 8192 + (lane & 2) * 512 + ((lane & 1) * 32 + (lane >> 2)) * 16 : e * CHK + t * 512 + (g & 1) * 256 + (r >> 2) * 128 + (g >> 1) * 64 + (r & 3) * 16;
    C[2] = L ? (4 * t + g) * 1024 + r * 16 : l0 + 81920 + e * 32768 + lane * 16;
    C[3] = L ? l0 + e * 1024 + lane * 8 : 2 * CHK + e * 4096 + (4 * (g >> 1) + 2 * t + (g & 1)) * 256 + 128 + r * 16;
    C[4] = ((16 * e + (lane >> 2)) * OUTW + DM + 32 * ci + 8 * (lane & 3)) * 2;
    C[5] = l0 + (L ? e * 8192 + lane * 16 : e * 512 + lane * 8); C[6] = l0 + 16384 + lane * 16; C[7] = l0 + 32768 + (L ? e * 8192 + 4096 : e * 2048) + lane * 16;
    C[8] = (lane & 31) * 128;
    C[9] = L ? (8 * (g & 1) + 4 * (g >> 1) + 2 * t + (r >> 2)) * 16 + 4 * (r < 4 ? r : 7 - r) : (TRW == 2 ? ci : TRW ? 8 * J + r : lane & 31) * 128;
    C[10] = (lane | e) ? oor : ci * 128;
    C[11] = L ? SY_CT + (CT_U + ct_near(sel) + 16 * e + (lane & 15)) * 128 : (t ? 0xEEEE : 0x4444); C[12] = L ? l0 + 81920 + e * 32768 + lane * 16 : ((lane & 2) ? 0xEEEE : 0x4444); C[13] = r < 4 ? 0x01000504u : 0x07060302u;
    C[14] = l0 + 49152 + 32 * e + (lane >> 6); C[15] = l0 + 49152 + (lane >> 6);
    C2[0] = L ? (lane < 16 ? lane * 16 : oor) : ux_tile(2 * ci) + lane * 16;
    C2[1] = lane == 0 && e && ci < 8 ? SY_CT + (CT_DONE + ct_near(~sel) + 8 * par + ci) * 128 : oor;
    C2[2] = (L ? ux_tile(2 * ci) : H_PX + 2 * e * CHK) + lane * 16;
#pragma unroll
    for (int n = 0; n < 4; ++n) C2[3 + n] = L ? H_PX + (8 + e) * CHK + (J ^ n) * 2048 + lane * 16 : C2[2] + CHK;
    C2[7] = l0 + 65536 + w * 2048 - 64 + (lane >> 6);
    asm volatile("" : "+v"(C), "+v"(C2));
#pragma unroll
    for (int i = 0; i < 16; ++i) P[i] = par - 2;
    P[2] = 48; P[6] = P[7] = K_DEF * TU; P[8] = SY_UM + tw * CHK + J * 2048; P[9] = H_MEM + mo; P[10] = 2 * CHK + J * 256; P[11] = e * 16 * UXH; P[13] = 1780; P[12] = H_DLT + par * 4096;
  }
  Clk ck; ck.init();
  unsigned spins = 0;
  while (true) {
    u32x16 pv; (void)pv;
    unsigned res = __builtin_amdgcn_readfirstlane(P[2]);
    if (res & 64u) break;
    const u32x4 rh2 = own_rsrc(a.sync + near_half(SY_HOT, sel), H_BYTES - 8192), ra = own_rsrc(a.sync, SY_BYTES), ro = own_rsrc(a.out, S_LEN * OUTW * 2);
    if (res & 1u) {
      spins |= 63u;
      if (dead || poll_fail(spins, abortw, opaque_tid(0))) dead = true;
    }
    ck.sync();
    P[2] = (res & 58u) | (dead ? 4u : 0u); P[1] = ck.ce; P[3] = ck.te; P[4] = ck.t2c; P[5] = ck.c2t << 15;
#pragma unroll
    for (int i = 0; i < 16; ++i) P[i] = __builtin_amdgcn_readfirstlane(P[i]);
    asm volatile(CB_BODY : "+{s[40:55]}"(P) STEP_PV
                 : "{v[192:207]}"(C), "{v[208:215]}"(C2), [rh] "s"(rh2), [ru] "s"(ra), [ro] "s"(ro), [wv] "s"(w), [sel] "s"(sel)
                 : "memory", "vcc", "scc", CB_CL "s56", "s57", "s58", "s59", "s76", "s77", "s78", "s79", "s80", "s81", "s82", "s83", "s84", "s85", "s86", "s87", "s88", "s89", "s90", "s91", "s92", "s93", "s94", "s95", "s96", "s97", "s98", "s99", "s100", "s101", V53, "v53", "v54", "v55", "v56", "v57", "v58", "v59", "v60", "v61", "v62", "v63", "v64", "v65", "v66", "v67", "v68", "v69", "v70", "v71", "v72", "v73", "v74", "v75", "v76", "v77", "v78", "v79", "v80", "v81", "v82", "v83", "v84", "v85", "v86", "v87", "v88", "v89", "v90", "v91", "v92", "v93", "v94", "v95", "v96", "v97", "v98", "v99", "v100", "v101", "v102", "v103", "v104", "v105", "v106", "v107", "v108", "v109", "v110", "v111", "v112", "v113", "v114", "v115", "v116", "v117", "v118", "v119", "v120", "v121", "v122", "v123", "v124", "v125", "v126", "v127", "v128", "v129", "v130", "v131", "v132", "v133", "v134", "v135", "v136", "v137", "v138", "v139", "v140", "v141", "v142", "v143", "v144", "v145", "v146", "v147", "v148", "v149", "v150", "v151", "v152", "v153", "v154", "v155", "v156", "v157", "v158", "v159", "v160", "v161", "v162", "v163", "v164", "v165", "v166", "v167", "v168", "v169", "v170", "v171", "v172", "v173", "v174", "v175", "v176", "v177", "v178", "v179", "v180", "v181", "v182", "v183", "v184", "v185", "v186", "v187", "v188", "v189", "v190", "v191");
    keep_a(W);
  }
  PDUMP(2);
}

__device__ __forceinline__ void bg_drain() {
  asm volatile("s_waitcnt vmcnt(0)" ::: "memory");
  __builtin_amdgcn_s_barrier();
  asm volatile("" ::: "memory");
}
#define BG_SLP 16
#define A3_SLP 48
__device__ __forceinline__ float blo(unsigned d) { return __uint_as_float(d << 16); }
__device__ __forceinline__ float bhi(unsigned d) { return __uint_as_float(d & 0xffff0000u); }
struct A1 {
  u32x4 kk[11], vv[8], sh[4];
  __device__ __forceinline__ void load(const ChainArgs& a, int x, int n) {
    const int lane = opaque_tid(0);
    const int d = (4 * x + (n & 3)) * 64 + 8 * (lane & 7), tb = (n >> 2) * 64 + 8 * (lane >> 3);
    const __amdgpu_buffer_rsrc_t rk = mk_rsrc(a.Ak, 4u << 20), rv = mk_rsrc(a.Av, 4u << 20), rs = mk_rsrc(a.Ashift, 8192);
#pragma unroll
    for (int i = 0; i < 4; ++i) sh[i] = __builtin_amdgcn_raw_buffer_load_b128(rs, d * 8 + 16 * i, 0, 0);
#pragma unroll
    for (int i = 0; i < 11; ++i) kk[i] = __builtin_amdgcn_raw_buffer_load_b128(rk, tb - 3 + i < 0 ? 0x7fffffffu : ((tb - 3 + i) * D_A + d) * 2, 0, 2);
#pragma unroll
    for (int i = 0; i < 8; ++i) vv[i] = __builtin_amdgcn_raw_buffer_load_b128(rv, ((tb + i) * D_A + d) * 2, 0, 2);
  }
  __device__ __forceinline__ void finish(const ChainArgs& a, int x, int n, char* T, Sel sel) {
    const int lane = opaque_tid(0);
#pragma unroll
    for (int c = 0; c < 8; ++c) {
      const unsigned t01 = sh[c >> 1][2 * (c & 1)], t23 = sh[c >> 1][2 * (c & 1) + 1];
      const float s0 = blo(t01), s1 = bhi(t01), s2 = blo(t23), s3 = bhi(t23);
      float kf[11];
#pragma unroll
      for (int i = 0; i < 11; ++i) kf[i] = (c & 1) ? bhi(kk[i][c >> 1]) : blo(kk[i][c >> 1]);
      u32x4 o;
#pragma unroll
      for (int i = 0; i < 8; i += 2) {
        float r[2];
#pragma unroll
        for (int j = 0; j < 2; ++j)
          r[j] = fmaf(kf[i + j + 3], s0, fmaf(kf[i + j + 2], s1, fmaf(kf[i + j + 1], s2, kf[i + j] * s3))) * ((c & 1) ? bhi(vv[i + j][c >> 1]) : blo(vv[i + j][c >> 1]));
        o[i >> 1] = pk2(r[0], r[1]);
      }
      *(u32x4*)(T + (8 * (lane & 7) + c) * 128 + (((lane >> 3) ^ (lane & 7)) * 16)) = o;
    }
    const __amdgpu_buffer_rsrc_t rkv = rsrc_u(a.sync);
#pragma unroll
    for (int i = 0; i < 8; ++i)
      st16(rkv, kv_row((4 * x + (n & 3)) * 64, sel) + (4 * i + (lane >> 4)) * CHK + ((lane >> 3) & 1) * 4096 + (n >> 2) * 128 + (lane & 7) * 16,
           *(const u32x4*)(T + (8 * i + (lane >> 3)) * 128 + (((lane & 7) ^ i) * 16)));
  }
};
#define WAH 4
#define DSP 4
#define SIGQ 2
__device__ __forceinline__ void task_w(const ChainArgs& a, int kind, int h, char* smem, int wv, Gate& G) {
  const int lane = opaque_tid(0), w = wv, r = lane & 15, g = lane >> 4;
  const __amdgpu_buffer_rsrc_t rw = mk_rsrc(kind == 0 ? a.Wq : (kind == 1 ? a.Wk : a.Wv), 8u << 20), rx = mk_rsrc(a.X, 4u << 20);
  const unsigned lds0 = (unsigned)(uintptr_t)(LAS char*)smem + w * 4096;
  unsigned vx[2], vw[4];
#pragma unroll
  for (int j = 0; j < 2; ++j) {
    const int rr = 8 * j + (lane >> 3);
    const int tb = kind == 0 ? rr : (kind == 1 ? 16 * ((rr >> 1) & 1) + 8 * ((rr >> 2) & 1) + 4 * (rr >> 3) + 2 * (rr & 1) : 8 * (rr >> 2) + (rr & 3));
    vx[j] = tb * DM * 2 + ((lane & 7) ^ (rr >> 1)) * 16;
  }
#pragma unroll
  for (int j = 0; j < 4; ++j) { const int row = (w * 4 + j) * 4 + g; vw[j] = row * HD * 2 + (r ^ swz_b(row)) * 16; }
  const u64 wo = kind == 2 ? bg_order(2) : (kind ? bg_order(1) : bg_order(0));
  auto xo = [&](int q) { return ((int)((wo >> (16 * q + 4 * w)) & 15) ^ (h & RHO)) * 128; };
  unsigned dT = (kind == 0 ? 16 : (kind == 1 ? 1 : 4)) * DM * 2;
  asm volatile("s_nop 4" : "+s"(dT));
  const unsigned vx0 = ((w & 1) ? vx[1] : vx[0]) + (w >> 1) * dT;
  auto st_w = [&](int n) {
#pragma unroll
    for (int j = 0; j < 4; ++j) dma16(rw, vw[j], (64 * ((int)((wo >> (4 * n)) & 15) ^ (h & RHO)) * HD + 128 * h) * 2, lds0 + (n & 7) * 16384 + j * 1024, W_NT & 16);
    dma16(rx, vx0, ((int)((wo >> (4 * n)) & 15) ^ (h & RHO)) * 128, lds0 - 3072 * w + 131072 + (n & 7) * 4096);
  };
  auto st_x = [&](int s, int b, int q, int j, bool pad) { dma16(rx, vx[j & 1], s * 65536 + (j >> 1) * dT + xo(q), lds0 + (b * 4 + q) * 16384 + j * 1024, false, pad); };
  if (KQ_D && kind < 2) while (__builtin_amdgcn_s_memrealtime() - G.entry < KQ_D) __builtin_amdgcn_s_sleep(8);
  __builtin_amdgcn_s_waitcnt(0x0F70);
  u32x4 W[32][2], gv;
  char* xb[3][2];
#pragma unroll
  for (int i = 0; i < 6; ++i) xb[i >> 1][i & 1] = lds_base(smem, (i >> 1) * 65536 + (r >> 3) * 1024 + (r & 7) * 128 + ((4 * (i & 1) + g) ^ (r >> 1)) * 16);
  f32x4 acc[2][4];
#pragma unroll
  for (int i = 0; i < 4; ++i) acc[1][i] = vzero();
#pragma unroll
  for (int n = 0; n < WAH; ++n) st_w(n);
#pragma unroll
  for (int n = 0; n < 16; ++n) {
    asm volatile("s_waitcnt vmcnt(%0)" :: "n"(4 * WAH - 4 + (n < 16 - WAH ? WAH - 1 : 15 - n)) : "memory");
    lds_barrier();
    u32x4 f[4], y[4];
#pragma unroll
    for (int i = 0; i < 4; ++i) {
#pragma unroll
      for (int q = 0; q < 2; ++q) {
        const int row = (i & 1) * 32 + 8 * g + 4 * q + (r >> 2);
        const u32x2 u = __builtin_bit_cast(u32x2, __builtin_amdgcn_ds_read_tr16_b64_v4i16((LAS bf16x4*)(smem + (n & 7) * 16384 + row * 256 + (((4 * w + (r & 3)) ^ swz_b(row)) * 16) + 8 * (i >> 1))));
        f[i][2 * q] = u[0]; f[i][2 * q + 1] = u[1];
      }
      y[i] = *(const u32x4*)(xb[2][i & 1] + (n & 7) * 4096 + (i >> 1) * 2048);
    }
    SB0;
    if (n == 16 - WAH) G.ask(a.sync, gv);
    if (n == 15) asm volatile("" : "+v"(gv));
    if (n + WAH < 16) st_w(n + WAH);
    else {
#pragma unroll
      for (int j = 0; j < 4; ++j) st_x(1 + ((n + WAH - 16) >> 2), (n + WAH - 16) >> 2, (n + WAH) & 3, j, true);
    }
#pragma unroll
    for (int i = 0; i < 8; ++i)
      asm volatile("s_nop 1\n\tv_mfma_f32_16x16x32_bf16 %0, %1, %2, %0" : "+v"(acc[1][i & 3]) : "v"(f[2 * (i & 1) + (i >> 2)]), "v"(y[(i & 2) + (i >> 2)]));
#pragma unroll
    for (int i = 0; i < 4; ++i) { asm volatile("" : "+a"(f[i])); W[2 * n + (i & 1)][i >> 1] = f[i]; }
  }
  if (!G.look(a.sync, gv)) { bg_drain(); return; }
  TP(4);
  SSTAMP(160 + h);
  const Sel sel = G.sel;
  const __amdgpu_buffer_rsrc_t ru = mk_rsrc(a.sync, SY_BYTES);
  const unsigned sgo = (w | lane) ? 0x7fffffffu : SY_CT + (kind == 2 ? CT_V + ct_near(~sel) : CT_Q + 32 * kind + ct_near(sel)) * 128 + h * 128;
#pragma unroll
  for (int T = 0; T < 2; ++T) st16s(pk8v(acc[1][2 * T], acc[1][2 * T + 1]), ru, lane * 16 + T * 4096, x_slot(kind, 0, h, sel) + w * 1024);
  SB0;
  const u32x4 rx2 = own_rsrc(a.X, 4u << 20);
  unsigned vq[4][4];
#pragma unroll
  for (int i = 0; i < 16; ++i) { vq[i >> 2][i & 3] = vx[i & 1] + ((i >> 1) & 1) * dT + xo(i >> 2); asm volatile("" : "+v"(vq[i >> 2][i & 3])); }
  u32x4 xf[4][2], o;
  auto rd = [&](int b, int k, int T) { xf[k & 3][T] = *(const u32x4*)(xb[b][k & 1] + (k >> 1) * 4096 + T * 2048); };
  auto signal = [&](int s) {
    st4(ru, sgo, s);
    if (w == 0 && lane == 0 && s >= 0 && (s < PSEG || (s & 7) == 0 || ((s & 15) == 2 && PSEG > 2) || s > 61)) {
    }
  };
  if (kind < KQ_NOW) { bg_drain(); signal(0); }
  G.arrive(a.sync);
#pragma unroll
  for (int i = 4 * WAH; i < 28; ++i) st_x(1 + (i >> 4), i >> 4, (i >> 2) & 3, i & 3, true);
  wait_vm<26>();
  lds_barrier();
#pragma unroll
  for (int i = 0; i < 6; ++i) rd(0, i >> 1, i & 1);
  WST(kind == 2, 18);
  auto seg = [&](int s, int b) {
    const unsigned so = x_slot(kind, s - 1, h, sel) + w * 1024;
    WST(kind == 2 && s > 0 && s < 4, 18 + s);
#pragma unroll
    for (int k = 0; k < 32; ++k)
#pragma unroll
      for (int i = 0; i < 4; ++i) {
        if (k == 0) asm volatile("v_mfma_f32_16x16x32_bf16 %0, %1, %2, 0" : "=&v"(acc[b][i]) : "a"(W[k][i & 1]), "v"(xf[k & 3][i >> 1]));
        else asm volatile("v_mfma_f32_16x16x32_bf16 %0, %1, %2, %0" : "+v"(acc[b][i]) : "a"(W[k][i & 1]), "v"(xf[k & 3][i >> 1]));
        SB0;
        const int q = k >> 3, n = 4 * k + i;
        if ((k & 7) == 5 && i == 0) {
          if (q == SIGQ) wait_vm<4 * SIGQ>(); else wait_vm<20>();
          if (q == 1 && __builtin_expect(s - 2u < W_NOW, 0)) { wait_vm<4>(); __builtin_amdgcn_s_barrier(); signal(s - 1); }
          else if (q == 0 && b == 0 && __builtin_expect(s == 1, 0)) { wait_vm<32 - 4 * WAH>(); __builtin_amdgcn_s_barrier(); signal(0); }
          else __builtin_amdgcn_s_barrier();
          asm volatile("" ::: "memory");
        }
        if (i == 1 || i == 2) rd(k + 3 < 32 ? b : b ^ 1, (k + 3) & 31, i - 1);
        {
          const int m = (n + 128 - 23) & 127, j = (m & 31) / DSP, q0 = m >> 5;
          if (j < 4 && (m & 31) % DSP == 0) asm volatile("s_add_i32 m0, %0, %1" :: "s"(lds0), "n"(((b ^ (q0 == 0) ^ (n < 23)) * 4 + ((q0 + 3) & 3)) * 16384 + j * 1024) : "m0", "scc");
          if (j < 4 && (m & 31) % DSP == 1)
            asm volatile("buffer_load_dwordx4 %0, %1, %2 offen lds" :: "v"(vq[(q0 + 3) & 3][j & 3]), "s"(rx2), "s"((s + 2 - (q0 == 0) - (n < 23)) * 65536) : "memory");
        }
        if (k == 8 * SIGQ + 5 && i == 2) signal(s - 1);
        if (k >= 1 && k < 5 && (i == 0 || i == 3)) { const f32x4 x = acc[b ^ 1][k - 1]; o[2 * (~k & 1) + (i & 1)] = pk2v(x[2 * (i & 1)], x[2 * (i & 1) + 1]); }
        if ((k == 2 || k == 4) && i == 3) { __builtin_amdgcn_raw_buffer_store_b128(o, ru, (unsigned)(lane * 16 + (k >> 2) * 4096), so, ST_AUX); asm volatile("s_nop 0" :: "v"(o)); }
        SB0;
      }
  };
  A1 a1;
#pragma unroll
  for (int i = 0; i < 11; ++i) asm volatile("" : "=v"(a1.kk[i]), "=v"(a1.vv[i & 7]), "=v"(a1.sh[i & 3]));
  const int wi = 32 * (2 - kind) + 4 * (h & 7) + w;
  for (int s = 1, ph = 0;; ++ph) {
    const int n = ph == 1 ? wi : (ph == 2 && wi >= 64 ? 32 + wi : 256);
    for (const int e = s + (ph ? A1_D : A1_0); s < e || ph == 4; s += 2) {
      seg(s, 0);
      if (s == NSEG - 1) break;
      seg(s + 1, 1);
    }
    if (ph == 4) break;
    if (ph == 3 && lane == 0) ctr_add(a, CT_A1 + ct_near(~sel) + (h >> 3), 1u);
    if (n < 128) a1.finish(a, h >> 3, n, smem + 131072 + w * 8192, sel);
    a1.load(a, h >> 3, ph == 0 ? wi : (ph == 1 && wi >= 64 ? 32 + wi : 256));
    SB0;
  }
#pragma unroll
  for (int T = 0; T < 2; ++T) st16s(pk8v(acc[0][2 * T], acc[0][2 * T + 1]), ru, lane * 16 + T * 4096, x_slot(kind, NSEG - 1, h, sel) + w * 1024);
  bg_drain();
  signal(NSEG - 1);
}
constexpr int A2_CSTR = 4160;
constexpr int A2_UOFF = 4 * A2_CSTR + 16 * 80;
constexpr int A2_USTR = 80 * 80;
constexpr int A2_WLDS = 4 * A2_CSTR + 2 * A2_USTR;
__device__ __forceinline__ void a2_wave(const ChainArgs& a, int d0, int d1, char* smem, int w, Sel sel) {
  const int lane = opaque_tid(0);
  char* Wb = smem + 1024 + w * A2_WLDS;
  char* U = Wb + A2_UOFF;
  const __amdgpu_buffer_rsrc_t rf = mk_rsrc((void*)a.Assm, 4u << 20), rd_ = mk_rsrc((void*)a.AD, 2048);
  const __amdgpu_buffer_rsrc_t rkv = rsrc_u(a.sync);
  u32x4 hi[4], uv[2][4];
  u32x2 lo[4];
  unsigned dn;
  auto ld_h = [&](int d) {
#pragma unroll
    for (int i = 0; i < 4; ++i) {
      hi[i] = __builtin_amdgcn_raw_buffer_load_b128(rf, (unsigned)((255 - 64 * i - lane) * 16), (unsigned)(d * 4096), 2);
      lo[i] = __builtin_amdgcn_raw_buffer_load_b64(rf, i == 3 && lane == 63 ? 0x7fffffffu : (255 - 64 * i - lane) * 16 - 8, (unsigned)(d * 4096), 2);
    }
    dn = __builtin_amdgcn_raw_buffer_load_b16(rd_, 0, 2 * d, 0);
  };
  auto ld_u = [&](int d) {
#pragma unroll
    for (int i = 0; i < 8; ++i) uv[i & 1][i >> 1] = __builtin_amdgcn_raw_buffer_load_b128(rkv, (unsigned)max(((i >> 1) * 64 + lane) * 16 - 8 * (i & 1), 0), kv_row(d, sel), LD_AUX);
  };
  ld_h(d0);
  {
    bool dead = false;
    ctr_wait<BG_SLP>(ctr(a, CT_A1 + ct_near(~sel) + (d0 >> 8)), 96u, (GAS unsigned*)(a.sync + SY_ABORT), lane, dead);
  }
  ld_u(d0);
  if (lane < 12) *(f32x4*)(Wb + (lane / 3) * A2_CSTR + (256 + lane % 3) * 16) = vzero();
#pragma unroll
  for (int i = 0; i < 4; ++i) if (64 * (i & 1) + lane < 80) *(f32x4*)(U + (i >> 1) * A2_USTR - 16 * 80 + (64 * (i & 1) + lane) * 16) = vzero();
  const int r = lane & 15, g = lane >> 4;
#pragma unroll 1
  for (int d = d0; d < d1; d += 96) {
    u64 at[6]; (void)at;
    AST(0);
#pragma unroll
    for (int i = 0; i < 4; ++i) {
      const unsigned src[6] = {lo[i][0], lo[i][1], hi[i][0], hi[i][1], hi[i][2], hi[i][3]};
      unsigned f[10];
#pragma unroll
      for (int x = 0; x < 10; ++x) {
        if ((x & 1) == 0) f[x] = __builtin_amdgcn_alignbit(src[5 - x / 2], src[5 - x / 2], 16);
        else f[x] = __builtin_amdgcn_perm(src[5 - (x + 1) / 2], src[5 - (x - 1) / 2], 0x07060100u);
      }
#pragma unroll
      for (int m = 0; m < 4; ++m) *(u32x4*)(Wb + m * A2_CSTR + (64 * i + lane) * 16) = (u32x4){f[3 - m], f[5 - m], f[7 - m], f[9 - m]};
    }
    if (lane == 0) uv[1][0] = (u32x4){0u, 0u, uv[1][0][0], uv[1][0][1]};
#pragma unroll
    for (int i = 0; i < 8; ++i) {
      const int ch = (i >> 1) * 64 + lane;
      *(u32x4*)(U + (i & 1) * A2_USTR + (ch >> 2) * 80 + (ch & 3) * 16) = uv[i & 1][i >> 1];
    }
    const float Dd = blo(dn);
    asm volatile("" :: "v"(Dd) : "memory");
    AST(1);
    ld_h(d + 96 < d1 ? d + 96 : D_A); ld_u(d + 96 < d1 ? d + 96 : D_A);
    f32x4 acc[4][2];
#pragma unroll
    for (int i = 0; i < 8; ++i) acc[i >> 1][i & 1] = vzero();
    const char* pa = Wb + (r & 3) * A2_CSTR + 16 * (255 - (r >> 2) + g) - 3 * 1024 - 64;
    const char* pb = U + (r - 1) * 80 + 16 * g;
    bf16x8 fa[2][4], fb[2][8];
    auto rd = [&](int b, int n, int k) {
      if (n == 0 || n > 8) fa[b][n ? n - 8 : 0] = *(const bf16x8*)(pa + 1024 * (3 - (n ? n - 8 : 0)) + 64 * (1 - k));
      else fb[b][n - 1] = *(const bf16x8*)(pb + ((n - 1) & 1) * A2_USTR + 80 * (16 * ((n - 1) >> 1) + 1 - k));
    };
#pragma unroll
    for (int n = 0; n < 12; ++n) rd(0, n, 0);
    AST(2);
#pragma unroll 1
    for (int k = 0; k < 16; k += 2) {
#pragma unroll
      for (int b = 0; b < 2; ++b) {
        int q = 0;
#pragma unroll
        for (int j = 0; j < 4; ++j)
#pragma unroll
          for (int i = j; i < 4; ++i)
#pragma unroll
            for (int mt = 0; mt < 2; ++mt) {
              asm volatile("v_mfma_f32_16x16x32_bf16 %0, %1, %2, %0" : "+v"(acc[i][mt]) : "v"(fa[b][j]), "v"(fb[b][2 * (i - j) + mt]));
              SB0;
              if (q < 12) rd(b ^ 1, q, 1);
              if (q == 12) { pa -= 64; pb -= 80; }
              ++q;
              SB0;
            }
      }
    }
    AST(3);
    u32x4 kv[4];
#pragma unroll
    for (int i = 0; i < 4; ++i) kv[i] = *(const u32x4*)(U + (16 * i + r) * 80 + 16 * g);
    asm volatile("s_nop 7\n\ts_waitcnt vmcnt(0)" ::: "memory");
#pragma unroll
    for (int i = 0; i < 8; ++i) asm volatile("" : "+v"(acc[i >> 1][i & 1]));
    if (lane == 0 && d > d0) ctr_add(a, CT_A2 + ct_near(~sel) + ((d - 96) >> 6), 1u);
#pragma unroll
    for (int i = 0; i < 4; ++i) {
      const f32x4 x = acc[i][1], y = acc[i][0];
      st16s((u32x4){pk2(fmaf(blo(kv[i][0]), Dd, x[0]), fmaf(bhi(kv[i][0]), Dd, x[1])), pk2(fmaf(blo(kv[i][1]), Dd, x[2]), fmaf(bhi(kv[i][1]), Dd, x[3])),
                    pk2(fmaf(blo(kv[i][2]), Dd, y[0]), fmaf(bhi(kv[i][2]), Dd, y[1])), pk2(fmaf(blo(kv[i][3]), Dd, y[2]), fmaf(bhi(kv[i][3]), Dd, y[3]))},
            rkv, (unsigned)(i * 1024 + r * 64 + g * 16), kv_row(d, sel));
    }
    AST(4);
  }
  wait_vm<0>();
  if (lane == 0) ctr_add(a, CT_A2 + ct_near(~sel) + ((d0 + (d1 - 1 - d0) / 96 * 96) >> 6), 1u);
}
__device__ __forceinline__ void task_u(const ChainArgs& a, int h, char* smem, int wv, Gate& G) {
  const int lane = opaque_tid(0);
  const int w = wv;
  GAS unsigned* abortw = (GAS unsigned*)(a.sync + SY_ABORT);
  bool dead = false;
  char* sb = smem + w * 32768;
  const __amdgpu_buffer_rsrc_t rwo = mk_rsrc(a.Wo, 8u << 20);
  const unsigned lds0 = (unsigned)(uintptr_t)(LAS char*)sb;
  unsigned wo4[4];
#pragma unroll
  for (int i = 0; i < 4; ++i) { const int row = i * 4 + (lane >> 4); wo4[i] = row * DM * 2 + (((lane & 15) ^ swz_b(row)) * 16); }
  asm volatile("s_nop 4");
  auto stage = [&](int n) {
    const int p = (U_ORD >> (4 * n)) & 3;
#pragma unroll
    for (int i = 0; i < 16; ++i)
      dma16(rwo, wo4[i & 3], (unsigned)(((128 * h + 64 * (p & 1) + 16 * (i >> 2)) * DM + 256 * w + 128 * (p >> 1)) * 2), lds0 + (n & 1) * 16384 + i * 1024, W_NT & 2);
  };
  __builtin_amdgcn_s_waitcnt(0x0F70);
  u32x4 W[16][4];
  stage(0); stage(1);
#pragma unroll
  for (int n = 0; n < 4; ++n) {
    const int p = (U_ORD >> (4 * n)) & 3;
    if (n < 3) wait_vm<16>(); else wait_vm<0>();
#pragma unroll
    for (int t = 0; t < 8; ++t) {
      const u32x8 f = tr_frag(sb + (n & 1) * 16384, t, lane);
      u32x4 f0 = {f[0], f[1], f[2], f[3]}, f1 = {f[4], f[5], f[6], f[7]};
      asm volatile("" : "+a"(f0), "+a"(f1));
      W[8 * (p >> 1) + t][2 * (p & 1)] = f0; W[8 * (p >> 1) + t][2 * (p & 1) + 1] = f1;
    }
    asm volatile("s_waitcnt lgkmcnt(0)" ::: "memory");
    if (n + 2 < 4) stage(n + 2);
  }
  lds_barrier();
  if (!G.pass(a.sync)) return;
  SSTAMP(192 + h);
  const Sel sel = G.sel;
  const u32x4 ru = own_rsrc(a.sync, SY_BYTES);
  u32x4 V[2][8], pc[2], o[2];
  f32x4 c[2][2];
  unsigned cv, cw, vo[8], lk[2], sgo = (w | lane) ? 0x7fffffffu : SY_CT + (CT_U + ct_near(sel) + h) * 128, nd = (lane & 1) ? 3 - URING : 2;
#pragma unroll
  for (int i = 0; i < 8; ++i) { vo[i] = lane * 16 + ux_tile(16 * w + 2 * i); asm volatile("" : "+v"(vo[i])); }
#pragma unroll
  for (int i = 0; i < 2; ++i) lk[i] = SY_CT + (ct_near(~sel) + ((lane & 1) ? CT_DONE + 8 * i + (h & 7) : CT_V + h)) * 128;
  char* vl = lds_base(smem, lane * 16);
  const unsigned vl2 = lane * 16 + w * 2048;
  auto ld_v = [&](int s, bool pad = false) {
    if (pad) asm volatile("s_nop 4\n\tbuffer_load_dwordx4 %0, %2, %3, %4 offen sc1\n\tbuffer_load_dwordx4 %1, %2, %3, %4 offen offset:1024 sc1" : "=&v"(pc[0]), "=&v"(pc[1]) : "v"(vl2), "s"(ru), "s"(x_slot(2, s, h, sel)) : "memory");
    else asm volatile("buffer_load_dwordx4 %0, %2, %3, %4 offen sc1\n\tbuffer_load_dwordx4 %1, %2, %3, %4 offen offset:1024 sc1" : "=&v"(pc[0]), "=&v"(pc[1]) : "v"(vl2), "s"(ru), "s"(x_slot(2, s, h, sel)) : "memory");
  };
  auto signal = [&](int s) {
    asm volatile("buffer_store_dword %0, %1, %2, 0 offen sc1" :: "v"(s), "v"(sgo), "s"(ru) : "memory");
  };
  auto st_u = [&](int n, unsigned ub) { asm volatile("buffer_store_dwordx4 %0, %1, %2, %3 offen offset:%4 sc1" :: "v"(o[n & 1]), "v"(vo[n >> 1]), "s"(ru), "s"(ub), "n"((n & 1) * 1024) : "memory"); };
  auto fetch = [&](int s) {
    for (unsigned spins = 0;;) {
      asm volatile("buffer_load_dword %0, %1, %2, 0 offen sc1" : "=&v"(cw) : "v"(lk[0]), "s"(ru) : "memory");
      ld_v(s, true);
      asm volatile("s_waitcnt vmcnt(0)" : "+v"(pc[0]), "+v"(pc[1]), "+v"(cw) :: "memory");
      if (s && spins == 0) { __builtin_amdgcn_s_barrier(); signal(0); }
      if ((int)__builtin_amdgcn_readfirstlane(cw) >= s) break;
      __builtin_amdgcn_s_sleep(2);
      if (poll_fail(spins, abortw, lane)) { dead = true; break; }
    }
    WST(s == 0, 24);
    if (s == 0) TP(3);
#pragma unroll
    for (int i = 0; i < 2; ++i) *(u32x4*)(vl + 16384 + w * 2048 + i * 1024) = pc[i];
    lds_barrier();
#pragma unroll
    for (int i = 0; i < 8; ++i) V[s][i] = *(const u32x4*)(vl + 16384 + i * 1024);
  };
#pragma unroll
  for (int i = 0; i < 8; ++i) asm volatile("" : "=v"(V[0][i]));
  u64 bad = 0;
  unsigned up = 0x7f000000u;
  c[1][0] = c[1][1] = vzero();
  auto seg = [&](int s, int b) {
    const unsigned ub = s < 0 ? 0x7f000000u : ux_base(s, h, sel);
    WST(s >= 0 && s < 4, 25 + s);
#pragma unroll
    for (int n = 0; n < 16; ++n)
#pragma unroll
      for (int q = 0; q < 8; ++q) {
        const int kp = q >> 1, m = q & 1;
        if (kp == 0) asm volatile("v_mfma_f32_16x16x32_bf16 %0, %1, %2, 0" : "=&v"(c[n & 1][m]) : "v"(V[b][4 * m + kp]), "a"(W[n][kp]));
        else asm volatile("v_mfma_f32_16x16x32_bf16 %0, %1, %2, %0" : "+v"(c[n & 1][m]) : "v"(V[b][4 * m + kp]), "a"(W[n][kp]));
        SB0;
        if (q >= 2 && q < 6) { const f32x4 x = c[~n & 1][(q - 2) >> 1]; o[~n & 1][q - 2] = pk2v(x[2 * (q & 1)], x[2 * (q & 1) + 1]); }
        if (q == 6) st_u((n - 1) & 15, n ? ub : up);
        if (q == 5) asm volatile("" :: "v"(o[n & 1]));
        if (n == U_LD && q == 0) {
          if (__builtin_expect(bad != 0, 0)) {
            if (s <= U_NOW) {
              asm volatile("s_waitcnt vmcnt(%1)" : "+v"(cw) : "n"(U_LD - 2) : "memory");
              __builtin_amdgcn_s_barrier();
              signal(s - 1);
              bad = (int)__builtin_amdgcn_readfirstlane(cw) <= s;
            }
            if (bad & 1) ctr_wait<2>(ctr(a, CT_V + ct_near(~sel) + h), min(s + 2, NSEG), abortw, lane, dead);
            if (bad & 2) {
              ctr_wait<U_SLP>(ctr(a, CT_DONE + ct_near(~sel) + 8 * b + (h & 7)), max(s + 3 - URING, 0), abortw, lane, dead);
              for (int i = 0; i < U_STG * (h & 7) + U_ST0; ++i) __builtin_amdgcn_s_sleep(1);
            }
          }
          ld_v(min(s + 1u, NSEG - 1u));
        }
        if (n == 1 && q == 7) asm volatile("buffer_load_dword %0, %1, %2, 0 offen sc1" : "=&v"(cw) : "v"(s <= U_NOW ? lk[0] : 0x7fffffffu), "s"(ru) : "memory");
        if (n == U_LD && q == 7) asm volatile("buffer_load_dword %0, %1, %2, 0 offen sc1" : "=&v"(cv) : "v"(lk[b ^ 1]), "s"(ru) : "memory");
        if (n == U_SIG2 && q == 0) asm volatile("s_waitcnt vmcnt(%4)" : "+v"(pc[0]), "+v"(pc[1]), "+v"(cv), "+v"(cw) : "n"(U_SIG2 - U_LD - 1) : "memory");
        if (n == U_SIG2 && (q == 1 || q == 7)) *(u32x4*)(vl + (b ^ 1) * 8192 + w * 2048 + (q >> 2) * 1024) = pc[q >> 2];
        if (n == U_SIG2 + 1 && q == 0) { bad = __builtin_amdgcn_ballot_w64(s >= 0 && (int)cv < (s < U_NOW && lane > 1 ? NSEG : min(s + (int)nd, NSEG - 1))); asm volatile("" : "+s"(bad)); }
        if (n == U_SIG2 + 1 && q == 7) lds_barrier();
        if (n == U_SIG2 + 2 && q == 0) signal(s - 1);
        if ((n == U_SIG2 + 2 || n == U_SIG2 + 3) && (q & 1)) V[b ^ 1][4 * (n - U_SIG2 - 2) + (q >> 1)] = *(const u32x4*)(vl + (b ^ 1) * 8192 + (4 * (n - U_SIG2 - 2) + (q >> 1)) * 1024);
        SB0;
      }
    up = ub;
  };
  for (int s = -U_DRY; s < NSEG; s += 2) {
    if (__builtin_expect(s == 0, 0)) fetch(0);
    seg(s, 0);
    if (__builtin_expect(s == 0, 0)) {
      o[1] = pk8v(c[1][0], c[1][1]);
      st_u(15, up);
      fetch(1);
    }
    seg(s + 1, 1);
  }
  o[1] = pk8v(c[1][0], c[1][1]);
  st_u(15, up);
  bg_drain();
  signal(NSEG - 1);
}

__device__ __forceinline__ void task_a3(const ChainArgs& a, int dg, int t0, char* T, Sel sel) {
  const int lane = opaque_tid(0);
  const int d = dg * 64 + 8 * (lane & 7);
  const int tb = t0 + 8 * (lane >> 3);
  const __amdgpu_buffer_rsrc_t rz = rsrc_u(a.sync);
  u32x4 qq[16], zz[8][2];
#pragma unroll
  for (int i = 0; i < 16; ++i) qq[i] = LDNT((const u32x4*)(a.Aq + (long)(tb + 64 * (i >> 3) + (i & 7)) * D_A + d));
  {
    u32x4 zr[16];
    bool dead = false;
    ctr_wait<A3_SLP>(ctr(a, CT_A2 + ct_near(~sel) + dg), 64u, (GAS unsigned*)(a.sync + SY_ABORT), lane, dead);
  #pragma unroll
    for (int i = 0; i < 16; ++i) zr[i] = ld16(rz, kv_row(dg * 64, sel) + (2 * i + (lane >> 5)) * CHK + ((lane >> 4) & 1) * 4096 + (t0 + (lane & 15) * 8) * 2);
#pragma unroll
    for (int i = 0; i < 16; ++i) {
      const int row = 4 * i + (lane >> 4);
      *(u32x4*)(T + row * 256 + (((lane & 15) ^ (row >> 3)) * 16)) = zr[i];
    }
  }
#pragma unroll
  for (int c = 0; c < 8; ++c)
#pragma unroll
    for (int gq = 0; gq < 2; ++gq) zz[c][gq] = *(const u32x4*)(T + (8 * (lane & 7) + c) * 256 + (((gq * 8 + (lane >> 3)) ^ (lane & 7)) * 16));
#pragma unroll
  for (int i = 0; i < 16; ++i) {
    u32x4 o;
#pragma unroll
    for (int c = 0; c < 8; c += 2) {
      const unsigned z0 = zz[c][i >> 3][(i >> 1) & 3], z1 = zz[c + 1][i >> 3][(i >> 1) & 3];
      o[c >> 1] = pk2(blo(qq[i][c >> 1]) * ((i & 1) ? bhi(z0) : blo(z0)), bhi(qq[i][c >> 1]) * ((i & 1) ? bhi(z1) : blo(z1)));
    }
    st16g(a.out + (long)(tb + 64 * (i >> 3) + (i & 7)) * OUTW + d, o);
  }
}

__device__ __forceinline__ void part_a(const ChainArgs& a, char* smem, int x, int wi, int w, Sel sel) {
  char* T = smem + 1024 + w * A2_WLDS;
  a2_wave(a, 256 * x + wi, 256 * x + 256, smem, w, sel);
  while (true) {
    unsigned t = 0;
    if (opaque_tid(0) == 0) t = __hip_atomic_fetch_add(ctr(a, CT_TK + ct_near(~sel) + x), 1u, __ATOMIC_RELAXED, __HIP_MEMORY_SCOPE_AGENT) + 1u;
    t = __builtin_amdgcn_readfirstlane(t);
    if (t >= 64) break;
    task_a3(a, 4 * x + (t >> 4), (t & 15) * 128, T, sel);
  }
}

__global__ __launch_bounds__(256, 1) void chain_kernel(ChainArgs a) {
  extern __shared__ __attribute__((aligned(16))) char smem[];
  const int b = blockIdx.x;
  {
    u32x4 rc;
    asm volatile("s_getpc_b64 s[60:61]\n\t.Lchain_here:\n\ts_sub_u32 s60, s60, .Lchain_here-_Z12chain_kernel9ChainArgs\n\ts_subb_u32 s61, s61, 0\n\ts_and_b32 s61, s61, 0xffff\n\t"
                 "s_mov_b32 s62, .Lchain_end-_Z12chain_kernel9ChainArgs\n\ts_mov_b32 s63, 0x20000\n\t"
                 "buffer_load_dwordx4 a[252:255], %1, s[60:63], 0 offen\n\tbuffer_load_dwordx4 a[252:255], %1, s[60:63], s63 offen"
                 : "={s[60:63]}"(rc) : "v"((b >> 3) * 4096 + threadIdx.x * 16) : "scc", "a252", "a253", "a254", "a255");
  }
  int w = __builtin_amdgcn_readfirstlane(threadIdx.x >> 6);
  Gate G;
  G.entry = __builtin_amdgcn_s_memrealtime(); G.open = false; G.arr = w == 0 ? b & 7 : -1;
  G.rot = ((unsigned)__builtin_amdgcn_s_getreg(20 | (3 << 11)) - b + WRONG) & 7u;
  {
    volatile LAS unsigned* t = (volatile LAS unsigned*)smem;
    if (w == 0) { t[0] = (unsigned)G.entry; t[1] = (unsigned)(G.entry >> 32); }
    __syncthreads();
    G.entry = (u64)(unsigned)__builtin_amdgcn_readfirstlane(t[0]) | ((u64)(unsigned)__builtin_amdgcn_readfirstlane(t[1]) << 32);
    __syncthreads();
  }
  G.tx = G.entry;
  if (((b + G.rot) & 4) == 0 && b < 128) {
    const int c = (b >> 6) * 32 + 8 * ((b + G.rot) & 3) + ((b >> 3) & 7);
    if (w < 2) calibrate(a, w ? 32 + (0x3200 >> (4 * (c & 3)) & 3) : c, 2 * c + w, G.entry);
    __builtin_amdgcn_s_barrier();
  }
  if (b == 0) {
    const int lane = opaque_tid(0);
    const __amdgpu_buffer_rsrc_t rs = mk_rsrc(a.sync + SY_SET, 3 * CHK);
    st16(rs, CHK + w * 8192 + lane * 128, (u32x4){SENT, SENT, SENT, SENT});
    if (w == 0 && lane >= 2) st16(rs, lane * 128, (u32x4){SENT, SENT, SENT, SENT});
    if (w == 3) {
      u32x4 v;
      bool ok;
      do {
        v = ld16(mk_rsrc(a.sync + SY_CT + CT_CAL * 128, 8192), lane * 128);
        ok = ((u64)v[1] << 32 | v[0]) - G.entry + 2000u < (1ull << 31) && ((u64)v[3] << 32 | v[2]) - G.entry + 2000u < (1ull << 31);
      } while (!__all(ok) && __builtin_amdgcn_s_memrealtime() - G.entry < 2 * CALM);
      const u64 k = lane == 32 ? 0x3333333333333333ull : (lane == 34 || lane == 35 ? 0x1111111111111111ull << (lane & 3) : 0ull);
      const int n1 = __builtin_popcountll(k & __builtin_amdgcn_ballot_w64(ok && (v[2] & 3u) == 3u)), n0 = __builtin_popcountll(k & __builtin_amdgcn_ballot_w64(ok && (v[2] & 3u) == 2u));
      if (!ok) v[0] = 0;
      if (n0 != n1) v[0] = n1 > n0 ? 3u : 2u;
      const unsigned t = (v[0] ^ lane) & 1u;
      const bool most = __builtin_popcountll(__builtin_amdgcn_ballot_w64((v[0] & 2u) && t)) > __builtin_popcountll(__builtin_amdgcn_ballot_w64((v[0] & 2u) && !t));
      const u64 m = __builtin_amdgcn_ballot_w64((v[0] & 2u) ? v[0] & 1u : (lane ^ most) & 1u);
      if (lane == 0) __hip_atomic_store((GAS u64*)(a.sync + SY_SEL), m, __ATOMIC_RELAXED, __HIP_MEMORY_SCOPE_AGENT);
    }
    bg_drain();
    if (w == 0 && lane == 0) __hip_atomic_store((GAS u64*)(a.sync + SY_EPOCH), (__builtin_amdgcn_s_memrealtime() & ~7ull) | G.rot, __ATOMIC_RELAXED, __HIP_MEMORY_SCOPE_AGENT);
  }
  while (true) {
    const unsigned rot = G.rot;
    asm volatile("" : "+s"(w));
    const int x = (b + rot) & 7, j = b >> 3, h = 8 * (x & 3) + (j & 7), role = (x & 4) + (j >> 3);
    if ((role == 0 || role == 7) && w >= 2) {
      const int n = opaque_tid(w - 2);
      const unsigned x0 = __builtin_amdgcn_raw_buffer_load_b32(mk_rsrc(a.sync, 106u << 20), n * PAGE + 4096 + (j & 31) * 128, 0, 2);
      const unsigned x1 = __builtin_amdgcn_raw_buffer_load_b32(mk_rsrc(a.out, 8u << 20), n * PAGE + (j & 31) * 128, 0, 2);
      asm volatile("" :: "v"(x0), "v"(x1));
    }
    if (role < 2) role_cb(a, 32 * role + h, smem, w, G);
    else if (role == 2) role_qk(a, h, smem, w, G);
    else if (role == 3) role_vo(a, h, smem, w, G);
    else if (role == 4) task_u(a, h, smem, w, G);
    else task_w(a, 7 - role, h, smem, w, G);
    if (G.rot == rot && role > 4 && !NOA) part_a(a, smem, x & 3, 4 * (j - 8) + w, w, G.sel);
    if (G.rot == rot) break;
    __syncthreads();
  }
  asm volatile(".Lchain_end:");
}

extern "C" void kernel_launch(void* const* d_in, const int* in_sizes, int n_in,
                              void* d_out, int out_size, void* d_ws, size_t ws_size,
                              hipStream_t stream) {
  const u16* A_q = (const u16*)d_in[0];
  const u16* A_k = (const u16*)d_in[1];
  const u16* A_v = (const u16*)d_in[2];
  const u16* A_shift = (const u16*)d_in[3];
  const u16* A_ssmk = (const u16*)d_in[4];
  const u16* A_D = (const u16*)d_in[5];
  const u16* B_x = (const u16*)d_in[6];
  const u16* B_mem0 = (const u16*)d_in[7];
  const u16* B_Wq = (const u16*)d_in[8];
  const u16* B_Wk = (const u16*)d_in[9];
  const u16* B_Wv = (const u16*)d_in[10];
  const u16* B_Wo = (const u16*)d_in[11];
  u16* out = (u16*)d_out;
  char* ws = (char*)(((uintptr_t)d_ws + (2u << 20) - 1) & ~(uintptr_t)((2u << 20) - 1)) + WSOFF * MB;
  hipFuncSetAttribute((const void*)chain_kernel, hipFuncAttributeMaxDynamicSharedMemorySize, CHAIN_LDS);
  ChainArgs ca;
  ca.prof = (u64*)((char*)d_ws + PROF_AT);
  ca.mem0 = B_mem0; ca.Wq = B_Wq; ca.Wk = B_Wk; ca.Wv = B_Wv; ca.Wo = B_Wo; ca.X = B_x;
  ca.out = out; ca.sync = ws;
  ca.Aq = A_q; ca.Ak = A_k; ca.Av = A_v; ca.Ashift = A_shift; ca.Assm = A_ssmk; ca.AD = A_D;
  chain_kernel<<<256, 256, CHAIN_LDS, stream>>>(ca);
}
```

```cpp
#include <hip/hip_runtime.h>
#include <hip/hip_bf16.h>
#include <stdint.h>
#include <type_traits>

constexpr int S_LEN = 2048;
constexpr int D_A   = 1024;
constexpr int DM    = 1024;
constexpr int NH    = 32;
constexpr int DH    = 128;
constexpr int HD    = NH * DH;
constexpr int MEMR  = 8;
constexpr int SEG   = 32;
constexpr int NSEG  = S_LEN / SEG;
constexpr int OUTW  = 2048;

typedef unsigned short u16;
typedef unsigned long long u64;
typedef short bf16x8 __attribute__((ext_vector_type(8)));
typedef short bf16x4 __attribute__((ext_vector_type(4)));
typedef float f32x4 __attribute__((ext_vector_type(4)));
typedef float f32x16 __attribute__((ext_vector_type(16)));
typedef unsigned u32x4 __attribute__((ext_vector_type(4)));
typedef unsigned u32x2 __attribute__((ext_vector_type(2)));
#define LAS __attribute__((address_space(3)))
#define GAS __attribute__((address_space(1)))

constexpr size_t MB = 1u << 20;
constexpr size_t PROF_AT = 0;

__device__ __forceinline__ float bf2f(u16 b) { return __uint_as_float(((unsigned)b) << 16); }
__device__ __forceinline__ u16 f2bf(float f) { __bf16 b = (__bf16)f; return __builtin_bit_cast(u16, b); }
__device__ __forceinline__ float rbf(float f) { return bf2f(f2bf(f)); }
__device__ __forceinline__ unsigned pk2(float a, float b) {
  typedef float f2 __attribute__((ext_vector_type(2)));
  typedef __bf16 b2 __attribute__((ext_vector_type(2)));
  f2 v = {a, b}; b2 r = __builtin_convertvector(v, b2); return __builtin_bit_cast(unsigned, r);
}

__device__ __forceinline__ int opaque_tid(int wv) {
  int l;
  asm volatile("v_mbcnt_lo_u32_b32 %0, -1, 0\n\tv_mbcnt_hi_u32_b32 %0, -1, %0" : "=v"(l));
  return wv * 64 + l;
}
__device__ __forceinline__ int swz_b(int row) { return ((row & 3) << 2) | ((row >> 2) & 3); }

#define KNOB(name, v) constexpr int name = v
#define KDEF 70
KNOB(K_DEF, KDEF);
KNOB(W_STEP, 33); KNOB(W_OFF, 56);
#define RHOV 3
KNOB(RHO, RHOV);
#define CDUR 1510
#define U_SLP 8
#define U_STG 0
#define U_ST0 0
#define WSOFF 122
#define START_SLP 3
#define QK_D 0
#define U_DRY 0
#define A1_0 20
#define A1_D 4
#define PSEG 1
#define KQ_NOW 2
#define NOA 0
#define V_PF 4
#define V_PF0 3
#define CALN 3
#define CALM 500u
#define CALT 680u
#define KQ_D 0
#define W_NOW 4
#define U_SIG2 11
#define U_LD 5
#define U_NOW 6
#define WRONG 0
#define OUTP "sc1"
#define LDNT(p) __builtin_nontemporal_load(p)
constexpr int CHK      = 16384;
constexpr int PAGE     = 2 << 20;
constexpr int SY_UM    = 34 * PAGE;
constexpr int SY_HOT   = 32 * PAGE;
constexpr int H_MEM    = 0;
constexpr int H_P      = 16 * CHK;
constexpr int H_ANN    = 28 * CHK;
constexpr int H_DLT    = 32 * CHK;
constexpr int H_TR     = 33 * CHK;
constexpr int H_PX     = 64 * CHK;
constexpr int H_BYTES  = 128 * CHK;
constexpr int URING    = 16;
constexpr int SY_SET   = 52 * PAGE;
constexpr int SY_EPOCH = SY_SET + 128;
constexpr int SY_ABORT = SY_SET + 256;
constexpr int SY_SEL   = SY_EPOCH + 8;
constexpr int SY_CT    = SY_SET + CHK;
constexpr int CT_Q = 0, CT_K = 32, CT_U = 128;
constexpr int CT_V = 0, CT_DONE = 32, CT_A2 = 48, CT_A1 = 128, CT_TK = 132;
constexpr int CT_ARR = -124, CT_INIT = -116, CT_CAL = 256;
constexpr int SY_KV    = 48 * PAGE;
constexpr int SY_BYTES = SY_CT + (CT_CAL + 64) * 128;
constexpr unsigned SENT = 0xFFFFFFFFu;
constexpr unsigned SPIN_CAP = 1u << 19;
constexpr int NLF = 16;
constexpr int L_WTS = 0;
constexpr int L_MEM = L_WTS + 4 * NLF * 2048;
constexpr int L_XCH = L_MEM + 16384;
constexpr int L_XK  = L_XCH + 2048;
constexpr int L_KX  = L_XCH + 4096;
constexpr int L_PRF = L_KX + 8192;
constexpr int XRS = 40;
constexpr int CHAIN_LDS = 160 * 1024;
static_assert(L_XCH + 128 * XRS <= L_PRF && L_PRF + 4096 <= CHAIN_LDS, "lds");

typedef __bf16 bfv8 __attribute__((ext_vector_type(8)));
typedef __bf16 bfv16 __attribute__((ext_vector_type(16)));
typedef unsigned u32x8 __attribute__((ext_vector_type(8)));
typedef unsigned u32x12 __attribute__((ext_vector_type(12)));
typedef unsigned u32x16 __attribute__((ext_vector_type(16)));
typedef unsigned u32x32 __attribute__((ext_vector_type(32)));
struct Wts { u32x8 a[32]; u32x32 v[4]; };

#define STAMPL(i) do {} while (0)
#define PVAL(i, v) do {} while (0)
#define PDECL(base, str) do {} while (0)
#define PZERO do {} while (0)
#define PDUMP(mask) do {} while (0)
#define TA(x) do {} while (0)
#define TB(x) TS(x)
#define TS(x) do {} while (0)
#define SSTAMP(i) do {} while (0)
#define WST(c, i) do {} while (0)
#define AST(i) do {} while (0)
struct ChainArgs {
  u64* prof;
  const u16* mem0; const u16* Wq; const u16* Wk; const u16* Wv; const u16* Wo; const u16* X;
  u16* out; char* sync;
  const u16* Aq; const u16* Ak; const u16* Av; const u16* Ashift; const u16* Assm; const u16* AD;
};
__device__ __forceinline__ GAS unsigned* ctr(const ChainArgs& a, int i) { return (GAS unsigned*)(a.sync + SY_CT + i * 128); }
__device__ __forceinline__ void ctr_add(const ChainArgs& a, int i, unsigned v) {
  __hip_atomic_fetch_add(ctr(a, i), v, __ATOMIC_RELAXED, __HIP_MEMORY_SCOPE_AGENT);
}

__device__ __forceinline__ __amdgpu_buffer_rsrc_t mk_rsrc(const void* p, unsigned bytes) {
  const u64 v = (u64)p;
  const unsigned lo = (unsigned)__builtin_amdgcn_readfirstlane((unsigned)v), hi = (unsigned)__builtin_amdgcn_readfirstlane((unsigned)(v >> 32));
  const u64 u = (u64)lo | ((u64)hi << 32);
  return __builtin_amdgcn_make_buffer_rsrc((void*)u, 0, bytes, 0x00020000);
}
__device__ __forceinline__ u32x4 own_rsrc(const void* p, unsigned bytes) {
  u32x4 r = {(unsigned)__builtin_amdgcn_readfirstlane((unsigned)(u64)p), (unsigned)__builtin_amdgcn_readfirstlane((unsigned)((u64)p >> 32)) & 0xffffu, bytes, 0x00020000u};
  asm volatile("s_nop 4" : "+s"(r));
  return r;
}
constexpr int LD_AUX = 16, ST_AUX = 16;
__device__ __forceinline__ u32x4 ld16(__amdgpu_buffer_rsrc_t rs, unsigned off) {
  return __builtin_amdgcn_raw_buffer_load_b128(rs, off, 0, LD_AUX);
}
__device__ __forceinline__ void st16(__amdgpu_buffer_rsrc_t rs, unsigned off, u32x4 v) {
  __builtin_amdgcn_raw_buffer_store_b128(v, rs, off, 0, ST_AUX);
}
__device__ __forceinline__ void st16s(u32x4 v, __amdgpu_buffer_rsrc_t rs, unsigned voff, unsigned soff) {
  __builtin_amdgcn_raw_buffer_store_b128(v, rs, voff, soff, ST_AUX);
  asm volatile("s_nop 1" :: "v"(v) : "memory");
}
__device__ __forceinline__ void st8(__amdgpu_buffer_rsrc_t rs, unsigned off, u32x2 v) {
  __builtin_amdgcn_raw_buffer_store_b64(v, rs, off, 0, ST_AUX);
}
__device__ __forceinline__ void st4(__amdgpu_buffer_rsrc_t rs, unsigned off, unsigned v) {
  __builtin_amdgcn_raw_buffer_store_b32(v, rs, off, 0, ST_AUX);
}
__device__ __forceinline__ unsigned ld4(__amdgpu_buffer_rsrc_t rs, unsigned off) {
  return __builtin_amdgcn_raw_buffer_load_b32(rs, off, 0, LD_AUX);
}
constexpr int TU = 16;
__device__ __forceinline__ unsigned cyc32() { return (unsigned)__builtin_amdgcn_s_memtime(); }
__device__ __forceinline__ unsigned to_sgpr(unsigned x) {
  unsigned r;
  asm volatile("s_nop 0\n\tv_readfirstlane_b32 %0, %1\n\ts_nop 4" : "=s"(r) : "v"(x));
  return r;
}
struct Clk {
  unsigned ce, te;
  unsigned c2t, t2c;
  __device__ __forceinline__ void edge(unsigned& c, unsigned& t) {
    u64 c0, r0, r1, r2, r3, r4, r5;
    asm volatile("s_memtime %0\n\ts_memrealtime %1\n\ts_nop 4\n\ts_memrealtime %2\n\ts_nop 4\n\ts_memrealtime %3\n\ts_nop 4\n\t"
                 "s_memrealtime %4\n\ts_nop 4\n\ts_memrealtime %5\n\ts_nop 4\n\ts_memrealtime %6\n\ts_waitcnt lgkmcnt(0)"
                 : "=&s"(c0), "=&s"(r0), "=&s"(r1), "=&s"(r2), "=&s"(r3), "=&s"(r4), "=&s"(r5) :: "memory");
    const unsigned x = (unsigned)r0;
    const unsigned n = ((unsigned)r1 == x) + ((unsigned)r2 == x) + ((unsigned)r3 == x) + ((unsigned)r4 == x) + ((unsigned)r5 == x);
    c = to_sgpr((unsigned)c0 + 24u * n); t = (x + 4u) * TU;
  }
  __device__ __forceinline__ void init() { edge(ce, te); c2t = 44000u; t2c = 97600u; }
  __device__ __forceinline__ void sync() {
    unsigned c, t;
    edge(c, t);
    set(c, t);
  }
  __device__ __forceinline__ void set(unsigned c, unsigned t) {
    if (c == ce) return;
    const float q = (float)(t - te) * __builtin_amdgcn_rcpf((float)(c - ce));
    const float qc = fminf(fmaxf(q, 0.51f), 1.99f);
    c2t = to_sgpr((unsigned)(qc * 65536.f));
    t2c = to_sgpr((unsigned)(__builtin_amdgcn_rcpf(qc) * 65536.f));
    ce = c; te = t;
  }
  __device__ __forceinline__ unsigned time(unsigned c) const { return te + ((min(c - ce, 30000u) * c2t) >> 16); }
  __device__ __forceinline__ unsigned now() const { return time(cyc32()); }
  __device__ __forceinline__ unsigned target(unsigned t) const {
    return to_sgpr(ce + (((unsigned)min(max((int)(t - te), 0), 20000) * t2c) >> 16));
  }
  __device__ __forceinline__ void wait(unsigned t) const { wait_c(target(t)); }
  __device__ __forceinline__ void wait_c(unsigned c) const {
    unsigned y;
    asm volatile("1:\n\ts_memtime vcc\n\ts_waitcnt lgkmcnt(0)\n\ts_sub_i32 %0, %1, vcc_lo\n\ts_cmp_gt_i32 %0, 128\n\ts_cbranch_scc1 1b\n\t"
                 "2:\n\ts_sub_i32 %0, %0, %2\n\ts_cmp_gt_i32 %0, %3\n\ts_cbranch_scc1 2b"
                 : "=&s"(y) : "s"(c), "n"(W_STEP), "n"(W_OFF) : "memory", "scc", "vcc");
  }
};
__device__ __forceinline__ unsigned nosent(unsigned d) { return min(d, SENT - 1u); }
__device__ __forceinline__ int arow(int row, int half) { return 4 * (row >> 1) + 2 * half + (row & 1); }
__device__ __forceinline__ unsigned umax4(u32x4 v) { return max(max(v[0], v[1]), max(v[2], v[3])); }
__device__ __forceinline__ void stamp_again(GAS u64* p) {
  __hip_atomic_store(p, (__builtin_amdgcn_s_memrealtime() & ~7ull) | (__hip_atomic_load(p, __ATOMIC_RELAXED, __HIP_MEMORY_SCOPE_AGENT) & 7ull),
                     __ATOMIC_RELAXED, __HIP_MEMORY_SCOPE_AGENT);
}
__device__ __forceinline__ bool poll_fail(unsigned& spins, GAS unsigned* abortw, int lane) {
  ++spins;
  if (spins > SPIN_CAP) {
    if (lane == 0) __hip_atomic_store(abortw, 1u, __ATOMIC_RELAXED, __HIP_MEMORY_SCOPE_AGENT);
    return true;
  }
  if ((spins & 511u) == 0u && lane == 0) {
    unsigned n = 0;
    for (int i = 0; i < 8; ++i) n += __hip_atomic_load(abortw + (SY_CT - SY_ABORT + (CT_ARR + i) * 128) / 4, __ATOMIC_RELAXED, __HIP_MEMORY_SCOPE_AGENT) + 1u;
    if (n < 256u) stamp_again((GAS u64*)(abortw - 32));
  }
  if ((spins & 63u) == 0u)
    return __builtin_amdgcn_readfirstlane(__hip_atomic_load(abortw, __ATOMIC_RELAXED, __HIP_MEMORY_SCOPE_AGENT)) != SENT;
  return false;
}
#define TP(k) do {} while (0)
struct Gate {
  u64 tx; u64 entry, sel; int arr; unsigned rot; bool open;
  __device__ __forceinline__ bool look(char* sync, u32x4 v) {
    if (open) return true;
    for (unsigned spins = 1;; ++spins) {
#pragma unroll
      for (int i = 0; i < 4; ++i) v[i] = __builtin_amdgcn_readfirstlane(v[i]);
      if (((u64)v[1] << 32 | v[0]) - entry + 2000u < (1ull << 31) || spins > 4 * SPIN_CAP) break;
      if (spins > 1) __builtin_amdgcn_s_sleep(8);
      v = ld16(mk_rsrc(sync + SY_EPOCH, 16), 0);
    }
    asm volatile("" ::: "memory");
    sel = (u64)v[3] << 32 | v[2];
    const bool same = rot == (v[0] & 7u);
    rot = v[0] & 7u; open = true;
    return same;
  }
  __device__ __forceinline__ void ask(char* sync, u32x4& v) { asm volatile("s_nop 4\n\tbuffer_load_dwordx4 %0, off, %1, 0 sc1" : "=v"(v) : "s"(own_rsrc(sync + SY_EPOCH, 16)) : "memory"); }
  __device__ __forceinline__ void arrive(char* sync) {
    if (arr >= 0 && opaque_tid(0) == 0) __hip_atomic_fetch_add((GAS unsigned*)(sync + SY_CT + (CT_ARR + arr) * 128), 1u, __ATOMIC_RELAXED, __HIP_MEMORY_SCOPE_AGENT);
    arr = -1;
  }
  __device__ __forceinline__ bool pass(char* sync, u32x4 v = {0u, 0u, 0u, 0u}) { const bool r = look(sync, v); arrive(sync); return r; }
};
__device__ __forceinline__ unsigned ann_val(unsigned t, int s) { return (t << 8) | (unsigned)s; }
__device__ __forceinline__ unsigned die_half() { return ((unsigned)__builtin_amdgcn_s_getreg(20 | (3 << 11)) >> 2) & 1u; }
typedef u64 Sel;
template <class F>
__device__ __forceinline__ void start_wait(char* sync, GAS unsigned* abortw, int lane, bool& dead, int ci, unsigned n, F f) {
  unsigned spins = 0, v;
  const bool m = lane < 4 || ci < 0;
  while (true) {
    v = ld4(mk_rsrc(sync + SY_SET, SY_BYTES - SY_SET), lane < 6 ? CHK + (m ? CT_INIT + (lane & 3) : ci + 32 * (lane & 1)) * 128 : 0x7fffffffu) + 1u;
    asm volatile("" ::: "memory");
    f();
    if (__all(lane > 5 || v >= (m ? 16u : n))) break;
    __builtin_amdgcn_s_sleep(START_SLP);
    if (dead || poll_fail(spins, abortw, lane)) { dead = true; break; }
  }
}
__device__ __forceinline__ __amdgpu_buffer_rsrc_t rsrc_u(char* sync) { return mk_rsrc((void*)sync, SY_SET); }
__device__ __forceinline__ unsigned near_half(unsigned off, u64 sel) { return off + ((unsigned)(sel >> ((off >> 21) & 63)) & 1u) * 8192; }
__device__ __forceinline__ unsigned x_slot(int kind, int s, int h, Sel sel) {
  return kind == 2 ? near_half(32 * PAGE + (s * 32 + h) * CHK, ~sel) : near_half((s * 64 + 2 * h + 1 - kind) * CHK, sel);
}
__device__ __forceinline__ int ct_near(Sel sel) { return 64 * ((unsigned)(sel >> (SY_SET / PAGE)) & 1u); }
__device__ __forceinline__ unsigned kv_row(int d, Sel sel) { return near_half(SY_KV + (d >> 1) * CHK, ~sel) + (d & 1) * 4096; }
__device__ __forceinline__ int hpos(int h) { return (h & 3) * 8 + (h >> 2); }
constexpr unsigned UXH = 8 * CHK;
__device__ __forceinline__ unsigned ux_base(int s, int h, Sel sel) { return near_half(((s & (URING - 1)) * 32 + h) * UXH, ~sel); }
__device__ __forceinline__ constexpr unsigned ux_tile(int t) { return (t >> 3) * CHK + (t & 7) * 1024; }
__device__ __forceinline__ __amdgpu_buffer_rsrc_t rsrc_hot(char* sync, Sel sel) {
  return mk_rsrc((void*)(sync + near_half(SY_HOT, sel)), H_BYTES - 8192);
}
template <int SLP = 4>
__device__ __forceinline__ void ctr_wait(GAS unsigned* c, unsigned need, GAS unsigned* abortw, int lane, bool& dead) {
  unsigned spins = 0;
  while (true) {
    unsigned v = __builtin_amdgcn_readfirstlane(__hip_atomic_load(c, __ATOMIC_RELAXED, __HIP_MEMORY_SCOPE_AGENT)) + 1u;
    if (v >= need) break;
    __builtin_amdgcn_s_sleep(SLP);
    if (dead || poll_fail(spins, abortw, lane)) { dead = true; break; }
  }
}
template <int N> __device__ __forceinline__ void wait_vm() { asm volatile("s_waitcnt vmcnt(%0)" :: "n"(N) : "memory"); }
__device__ __forceinline__ void lds_barrier() {
  asm volatile("s_waitcnt lgkmcnt(0)" ::: "memory");
  __builtin_amdgcn_s_barrier();
  asm volatile("" ::: "memory");
}
__device__ __forceinline__ f32x4 vzero() { float z; asm volatile("v_mov_b32 %0, 0" : "=v"(z)); f32x4 r = {z, z, z, z}; asm volatile("" : "+v"(r)); return r; }
__device__ __forceinline__ f32x4 smf(u32x4 a, u32x8 b, f32x4 c, int idx) {
  return __builtin_amdgcn_smfmac_f32_16x16x64_bf16(__builtin_bit_cast(bfv8, a), __builtin_bit_cast(bfv16, b), c, idx, 0, 0);
}
__device__ __forceinline__ float red16max(float x) { auto r = __builtin_amdgcn_permlane16_swap(__float_as_uint(x), __float_as_uint(x), false, false); return fmaxf(__uint_as_float(r[0]), __uint_as_float(r[1])); }
__device__ __forceinline__ float red32max(float x) { auto r = __builtin_amdgcn_permlane32_swap(__float_as_uint(x), __float_as_uint(x), false, false); return fmaxf(__uint_as_float(r[0]), __uint_as_float(r[1])); }
__device__ __forceinline__ float red16add(float x) { auto r = __builtin_amdgcn_permlane16_swap(__float_as_uint(x), __float_as_uint(x), false, false); return __uint_as_float(r[0]) + __uint_as_float(r[1]); }
__device__ __forceinline__ float red32add(float x) { auto r = __builtin_amdgcn_permlane32_swap(__float_as_uint(x), __float_as_uint(x), false, false); return __uint_as_float(r[0]) + __uint_as_float(r[1]); }
constexpr bool blk_in_lds(int R, int b) {
  return R == 0 ? ((b & 7) == 1 || (b & 7) == 4) : (b < 16 ? (b & 3) == 0 : (b & 3) == 3);
}
constexpr int lds_index(int R, int b) { int n = 0; for (int i = 0; i < b; ++i) if (blk_in_lds(R, i)) ++n; return n; }
constexpr bool frag_in_lds(int R, int f) { return blk_in_lds(R, f >> 1); }
constexpr int frag_reg_slot(int R, int f) { int n = 0; for (int i = 0; i < f; ++i) if (!frag_in_lds(R, i)) ++n; return n; }

constexpr int m_block(int R, int m, int i) {
  return R == 0 ? 2 * (i < 12 ? (m ? i + i / 3 + 1 : i + (i + 1) / 3) : 4 * (i - 12) + 2 - 2 * m) + m : m ? 16 + 4 * (i & 3) + (i >> 2) : (i < 12 ? i + i / 3 + 1 : 4 * (i - 12));
}
#define QK_REV 0
#define W_BV 0xba9876543210fedcull
#define VO_ORD 3
constexpr int w_block(int R, int i) {
  if (R && VO_ORD == 2) return i < 12 ? m_block(1, 0, 11 - i) : m_block(1, 1, i - 12);
  if (R && VO_ORD == 3) return i & 1 ? m_block(1, 1, i >> 1) : m_block(1, 0, 11 - (i >> 1));
  if (!R && QK_REV) return m_block(0, i & 1, 11 - (i >> 1));
  return R && VO_ORD ? m_block(R, i < 12, i % 12) : m_block(R, i & 1, i >> 1);
}
#define W_BG 0xfedcba9876543210ull
#define W_NT 0
#define U_ORD 0x3210
constexpr u64 bg_order(int m) { u64 r = 0; for (int n = 0; n < 16; ++n) r |= (u64)(m_block(0, m > 0, ((m == 2 ? W_BV : W_BG) >> (4 * n)) & 15) >> 1) << (4 * n); return r; }
struct WSched { int ord[32]; int slot[32]; int issue[32]; int out[32]; int reg[32]; };
constexpr WSched make_wsched(int R) {
  WSched S{};
  int n = 0;
  for (int i = 0; i < 24; ++i) S.ord[n++] = w_block(R, i);
  for (int i = 0; i < n; ++i) S.reg[i] = frag_reg_slot(R, 2 * S.ord[i]);
  const int npure = n;
  for (int b = 0; b < 32; ++b) if (blk_in_lds(R, b)) S.ord[n++] = b;
  int seq[32] = {};
  int nissued = 0, next = 0;
  for (int i = 0; i < 10; ++i) { S.slot[i] = (i + 10 - npure % 10) % 10; S.issue[i] = -1; seq[i] = nissued++; }
  next = 10;
  S.out[0] = nissued - 1;
  for (int c = 0; c <= npure; ++c) {
    if (c >= 1) {
      const int k = S.slot[c - 1];
      if (next < npure) { S.slot[next] = k; S.issue[next] = c; seq[next] = nissued++; ++next; }
      else if (k < NLF / 2) { S.slot[npure + k] = k; S.issue[npure + k] = c; seq[npure + k] = nissued++; }
    }
    if (c + 1 < npure) S.out[c + 1] = nissued - 1 - seq[c + 1];
  }
  return S;
}
__device__ __forceinline__ int kcol(int k) { return 8 * ((k >> 1) & 1) + ((k & 1) ? 7 - (k >> 2) : k >> 2); }
template <bool PERM = false>
__device__ __forceinline__ u32x8 tr_frag(const char* sb, int tile, int lane) {
  u32x8 f;
#pragma unroll
  for (int q = 0; q < 4; ++q) {
    int row = (q >> 1) * 32 + 8 * (lane >> 4) + 4 * (q & 1) + ((lane & 15) >> 2);
    if (PERM) row = (row & ~15) + kcol(row & 15);
    int p = lane & 3;
    int lc = 2 * tile + (p >> 1);
    int addr = row * 256 + ((lc ^ swz_b(row)) * 16) + 8 * (p & 1);
    bf16x4 v = __builtin_amdgcn_ds_read_tr16_b64_v4i16((LAS bf16x4*)(sb + addr));
    u32x2 u = __builtin_bit_cast(u32x2, v);
    f[2 * q] = u[0]; f[2 * q + 1] = u[1];
  }
  return f;
}
__device__ __forceinline__ void dma16(__amdgpu_buffer_rsrc_t rs, unsigned voff, unsigned soff, unsigned lds, bool nt = false, bool pad = true) {
  if (nt) asm volatile("s_mov_b32 m0, %0\n\ts_nop 3\n\tbuffer_load_dwordx4 %1, %2, %3 offen nt lds" :: "s"(lds), "v"(voff), "s"(rs), "s"(soff) : "m0", "memory");
  else if (pad) asm volatile("s_mov_b32 m0, %0\n\ts_nop 3\n\tbuffer_load_dwordx4 %1, %2, %3 offen lds" :: "s"(lds), "v"(voff), "s"(rs), "s"(soff) : "m0", "memory");
  else asm volatile("s_mov_b32 m0, %0\n\ts_nop 0\n\tbuffer_load_dwordx4 %1, %2, %3 offen lds" :: "s"(lds), "v"(voff), "s"(rs), "s"(soff) : "m0", "memory");
}
__device__ __forceinline__ u32x8 tr_frag2(const char* sb, int tile, int f, int lane) {
  u32x8 r;
#pragma unroll
  for (int c = 0; c < 2; ++c)
#pragma unroll
    for (int t = 0; t < 2; ++t) {
      const int row = 32 * c + 16 * f + 4 * (lane >> 4) + ((lane & 15) >> 2);
      const int p = lane & 3, lc = 2 * (tile + t) + (p >> 1);
      const u32x2 u = __builtin_bit_cast(u32x2, __builtin_amdgcn_ds_read_tr16_b64_v4i16((LAS bf16x4*)(sb + row * 256 + ((lc ^ swz_b(row)) * 16) + 8 * (p & 1))));
      r[4 * c + t] = u[0]; r[4 * c + 2 + t] = u[1];
    }
  return r;
}
template <int ROLE>
__device__ __forceinline__ void load_weights(const ChainArgs& a, int h, int w, int lane, char* smem, Wts& W, Gate& G, u32x4& gv) {
#pragma unroll
  for (int i = 0; i < 4; ++i) asm volatile("" : "=v"(W.v[i]));
#pragma unroll
  for (int i = 0; i < 32; ++i) asm volatile("" : "=a"(W.a[i]));
  constexpr WSched S = make_wsched(ROLE);
  constexpr int NP = 32 - NLF / 2;
  const __amdgpu_buffer_rsrc_t r0 = mk_rsrc(ROLE == 0 ? a.Wq : a.Wv, 8u << 20), r1 = mk_rsrc(ROLE == 0 ? a.Wk : a.Wo, 8u << 20);
  const unsigned lds0 = (unsigned)(uintptr_t)(LAS char*)smem;
  unsigned vo[2][4];
#pragma unroll
  for (int j = 0; j < 4; ++j) {
    const int row = (w * 4 + j) * 4 + (lane >> 4), lc = (lane & 15) ^ swz_b(row);
    vo[0][j] = row * HD * 2 + lc * 16; vo[1][j] = row * DM * 2 + lc * 16;
  }
  asm volatile("s_nop 4");
  auto stage = [&](int i) {
    const int blk = S.ord[i];
    const bool second = ROLE == 0 ? (blk & 1) : blk >= 16, wo = ROLE == 1 && second;
    const unsigned so = wo ? (unsigned)(((128 * h + 64 * (blk & 1)) * DM + 128 * (((blk - 16) >> 1) ^ (2 * (h & 3)))) * 2)
                           : (unsigned)((64 * ((ROLE == 0 ? blk >> 1 : blk) ^ (h & RHO)) * HD + 128 * h) * 2);
#pragma unroll
    for (int j = 0; j < 4; ++j) dma16(second ? r1 : r0, vo[wo][j], so, lds0 + 16384 * S.slot[i] + (w * 4 + j) * 1024, W_NT & (ROLE ? 8 : 1));
  };
#pragma unroll
  for (int i = 0; i < 32; ++i) if (S.issue[i] == -1) stage(i);
#pragma unroll
  for (int c = 0; c < NP; ++c) {
    asm volatile("s_waitcnt vmcnt(%0)" :: "n"(4 * S.out[c]) : "memory");
    lds_barrier();
#pragma unroll
    for (int i = 0; i < 32; ++i) if (S.issue[i] == c) stage(i);
    const char* sb = smem + 16384 * S.slot[c];
#pragma unroll
    for (int t = 0; t < 2; ++t) {
      u32x8 f = (ROLE == 1 && S.ord[c] >= 16) ? tr_frag2(sb, 2 * w, t, lane) : tr_frag<true>(sb, 2 * w + t, lane);
      if (S.reg[c] < 32) {
#pragma unroll
        for (int i = 0; i < 8; ++i) asm volatile("v_accvgpr_write_b32 a[%1], %0" :: "v"(f[i]), "n"(8 * ((S.reg[c] + t) & 31) + i));
      }
      else {
#pragma unroll
        for (int i = 0; i < 8; ++i) W.v[(S.reg[c] >> 2) & 3][8 * ((S.reg[c] + t) & 3) + i] = f[i];
      }
    }
    if (S.reg[c] >= 32) asm volatile("" : "+v"(W.v[(S.reg[c] >> 2) & 3]));
  }
  static_assert(S.issue[31] < NP && S.issue[NP] < S.issue[31], "resident blocks are issued in slot order inside the loop");
  G.ask(a.sync, gv);
#pragma unroll
  for (int j = 0; j < NLF / 2; ++j) {
    asm volatile("s_waitcnt vmcnt(%0)" :: "n"(4 * (NLF / 2 - 1 - j) + 1) : "memory");
    lds_barrier();
    const bool two = ROLE == 1 && S.ord[NP + j] >= 16;
    u32x8 f0 = two ? tr_frag2(smem + 16384 * j, 2 * w, 0, lane) : tr_frag<true>(smem + 16384 * j, 2 * w, lane);
    u32x8 f1 = two ? tr_frag2(smem + 16384 * j, 2 * w, 1, lane) : tr_frag<true>(smem + 16384 * j, 2 * w + 1, lane);
    lds_barrier();
    char* d = smem + 16384 * j + w * 4096 + lane * 16;
    *(u32x4*)d = (u32x4){f0[0], f0[1], f0[2], f0[3]};
    *(u32x4*)(d + 1024) = (u32x4){f0[4], f0[5], f0[6], f0[7]};
    *(u32x4*)(d + 2048) = (u32x4){f1[0], f1[1], f1[2], f1[3]};
    *(u32x4*)(d + 3072) = (u32x4){f1[4], f1[5], f1[6], f1[7]};
  }
  asm volatile("s_waitcnt vmcnt(0)" : "+v"(gv) :: "memory");
  lds_barrier();
}
template <int... Is> struct iseq {};
template <int N, int... Is> struct mkseq : mkseq<N - 1, N - 1, Is...> {};
template <int... Is> struct mkseq<0, Is...> { typedef iseq<Is...> type; };

#define SB0 __builtin_amdgcn_sched_barrier(0)
#define CAT8(x, y) ((u32x8){x[0], x[1], x[2], x[3], y[0], y[1], y[2], y[3]})
template <class F, int... I>
__device__ __forceinline__ void for_slots(iseq<I...>, F f) { (f(std::integral_constant<int, I>{}), ...); }
#define QK_BODY \
  "s_cmp_eq_u32 %[wv], 1\n\t" \
  "s_cbranch_scc1 91f\n\t" \
  "s_cmp_eq_u32 %[wv], 2\n\t" \
  "s_cbranch_scc1 92f\n\t" \
  "s_cmp_eq_u32 %[wv], 3\n\t" \
  "s_cbranch_scc1 93f\n\t" \
  "90:\n\t" \
  "s_mov_b32 s58, s42\n\t" \
  "20:\n\t" \
  "s_and_b32 s76, s44, 7\n\t" \
  "s_lshl_b32 s76, s76, 15\n\t" \
  "s_add_i32 s40, s54, s76\n\t" \
  "s_and_b32 s76, s44, 3\n\t" \
  "s_lshl_b32 s45, s76, 14\n\t" \
  "s_mul_i32 s47, s76, 0xc000\n\t" \
  "s_add_i32 s47, s47, 0x40000\n\t" \
  "s_add_i32 s49, s44, -1\n\t" \
  "s_and_b32 s48, s49, 2\n\t" \
  "s_lshl_b32 s48, s48, 13\n\t" \
  "s_and_b32 s76, s49, 1\n\t" \
  "s_lshl_b32 s76, s76, 12\n\t" \
  "s_add_i32 s48, s48, s76\n\t" \
  "s_lshl_b32 s50, s53, 8\n\t" \
  "s_mov_b32 s59, 0\n\t" \
  "s_mov_b32 s92, s51\n\t" \
  "s_mov_b32 s93, s53\n\t" \
  "s_xor_b32 s76, s40, 0x400\n\t" \
  "s_xor_b32 s77, s40, 0x800\n\t" \
  "s_xor_b32 s78, s40, 0xc00\n\t" \
  "s_cmp_lg_u32 s58, 0\n\t" \
  "s_cbranch_scc1 11f\n\t" \
  "10:\n\t" \
  "ds_read_b128 v[48:51], v102 offset:0\n\t" \
  "ds_read_b128 v[52:55], v102 offset:1024\n\t" \
  "ds_read_b128 v[56:59], v102 offset:2048\n\t" \
  "ds_read_b128 v[60:63], v102 offset:3072\n\t" \
  "ds_read_b128 v[16:19], v102 offset:16384\n\t" \
  "ds_read_b128 v[20:23], v102 offset:17408\n\t" \
  "ds_read_b128 v[24:27], v102 offset:18432\n\t" \
  "ds_read_b128 v[28:31], v102 offset:19456\n\t" \
  "ds_read_b128 v[84:87], v102 offset:49152\n\t" \
  "ds_read_b128 v[88:91], v102 offset:50176\n\t" \
  "ds_read_b128 v[92:95], v102 offset:51200\n\t" \
  "ds_read_b128 v[96:99], v102 offset:52224\n\t" \
  "s_waitcnt lgkmcnt(4)\n\t" \
  "ds_read_b128 v[0:3], v103 offset:16384\n\t" \
  "ds_read_b128 v[4:7], v103 offset:17408\n\t" \
  "ds_read_b128 v[8:11], v103 offset:18432\n\t" \
  "ds_read_b128 v[12:15], v103 offset:19456\n\t" \
  "46:\n\t" \
  "s_bitcmp1_b32 s58, 1\n\t" \
  "s_cselect_b64 s[80:81], -1, 0\n\t" \
  "s_cmp_lg_u32 s58, 0\n\t" \
  "s_cbranch_scc1 17f\n\t" \
  "s_mov_b64 exec, 0xff\n\t" \
  "16:\n\t" \
  "s_memtime vcc\n\t" \
  "s_waitcnt lgkmcnt(0)\n\t" \
  "s_sub_i32 s79, s41, vcc_lo\n\t" \
  "s_cmp_gt_i32 s79, 64\n\t" \
  "s_cbranch_scc1 16b\n\t" \
  "13:\n\t" \
  "buffer_load_dword v116, v112, %[rh], s48 offen sc1\n\t" \
  "s_waitcnt vmcnt(0)\n\t" \
  "15:\n\t" \
  "v_mov_b32 v117, 20000\n\t" \
  "v_cmp_ne_u32_sdwa vcc, v116, s49 src0_sel:BYTE_0 src1_sel:DWORD\n\t" \
  "v_subrev_u32 v116, s50, v116\n\t" \
  "v_ashrrev_i32 v116, 8, v116\n\t" \
  "s_nop 1\n\t" \
  "v_max_i32_dpp v116, v116, v116 quad_perm:[1,0,3,2] row_mask:0xf bank_mask:0xf\n\t" \
  "s_cbranch_vccnz 14f\n\t" \
  "s_nop 0\n\t" \
  "v_max_i32_dpp v116, v116, v116 quad_perm:[2,3,0,1] row_mask:0xf bank_mask:0xf\n\t" \
  "s_nop 1\n\t" \
  "v_max_i32_dpp v116, v116, v116 row_half_mirror row_mask:0xf bank_mask:0xf\n\t" \
  "v_med3_i32 v116, v116, 0, v117\n\t" \
  "v_mul_u32_u24 v116, s52, v116\n\t" \
  "v_lshrrev_b32 v116, 16, v116\n\t" \
  "s_mov_b64 exec, -1\n\t" \
  "v_readfirstlane_b32 s84, v116\n\t" \
  "s_add_i32 s84, s84, s51\n\t" \
  ".p2align 6\n\t" \
  "s_nop 0\n\t" \
  "s_nop 0\n\t" \
  "s_nop 0\n\t" \
  "s_nop 0\n\t" \
  "s_nop 0\n\t" \
  "s_nop 0\n\t" \
  "s_nop 0\n\t" \
  "s_nop 0\n\t" \
  "1:\n\t" \
  "s_memtime vcc\n\t" \
  "s_waitcnt lgkmcnt(0)\n\t" \
  "s_sub_i32 s79, s84, vcc_lo\n\t" \
  "s_cmp_gt_i32 s79, 128\n\t" \
  "s_cbranch_scc1 1b\n\t" \
  ".p2align 4\n\t" \
  "2:\n\t" \
  "s_sub_i32 s79, s79, 33\n\t" \
  "s_cmp_gt_i32 s79, 56\n\t" \
  "s_cbranch_scc1 2b\n\t" \
  "buffer_load_dwordx4 v[32:35], v100, %[rh], s40 offen sc1\n\t" \
  "s_nop 12\n\t" \
  "buffer_load_dwordx4 v[36:39], v100, %[rh], s76 offen sc1\n\t" \
  "s_nop 12\n\t" \
  "buffer_load_dwordx4 v[40:43], v100, %[rh], s77 offen sc1\n\t" \
  "s_nop 12\n\t" \
  "buffer_load_dwordx4 v[44:47], v100, %[rh], s78 offen sc1\n\t" \
  "s_memtime vcc\n\t" \
  "s_memrealtime s[82:83]\n\t" \
  "s_nop 4\n\t" \
  "s_memrealtime s[84:85]\n\t" \
  "s_nop 4\n\t" \
  "s_memrealtime s[56:57]\n\t" \
  "s_nop 4\n\t" \
  "s_memrealtime s[86:87]\n\t" \
  "s_nop 4\n\t" \
  "s_memrealtime s[88:89]\n\t" \
  "s_nop 4\n\t" \
  "s_memrealtime s[90:91]\n\t" \
  "s_mov_b32 s92, 0\n\t" \
  "s_waitcnt lgkmcnt(0)\n\t" \
  "s_cmp_eq_u32 s82, s84\n\t" \
  "s_addc_u32 s92, s92, 0\n\t" \
  "s_cmp_eq_u32 s82, s56\n\t" \
  "s_addc_u32 s92, s92, 0\n\t" \
  "s_cmp_eq_u32 s82, s86\n\t" \
  "s_addc_u32 s92, s92, 0\n\t" \
  "s_cmp_eq_u32 s82, s88\n\t" \
  "s_addc_u32 s92, s92, 0\n\t" \
  "s_cmp_eq_u32 s82, s90\n\t" \
  "s_addc_u32 s92, s92, 0\n\t" \
  "s_mul_i32 s92, s92, 24\n\t" \
  "s_add_i32 s92, s92, vcc_lo\n\t" \
  "s_lshl4_add_u32 s93, s82, 64\n\t" \
  "3:\n\t" \
  "s_sub_i32 s84, s92, s43\n\t" \
  "s_lshl_b32 s85, s93, 8\n\t" \
  "s_or_b32 s85, s85, s44\n\t" \
  "v_mov_b32 v118, s85\n\t" \
  "v_mov_b64 v[68:69], 0\n\t" \
  "v_mov_b64 v[70:71], 0\n\t" \
  "v_mov_b64 v[72:73], 0\n\t" \
  "v_mov_b64 v[74:75], 0\n\t" \
  "v_mov_b64 v[76:77], 0\n\t" \
  "v_mov_b64 v[78:79], 0\n\t" \
  "v_mov_b64 v[80:81], 0\n\t" \
  "v_mov_b64 v[82:83], 0\n\t" \
  "s_waitcnt lgkmcnt(0)\n\t" \
  "s_waitcnt vmcnt(3)\n\t" \
  "v_smfmac_f32_16x16x64_bf16 v[68:71], v[32:35], a[0:7], v104\n\t" \
  "ds_write_b128 v101, v[32:35] offset:0\n\t" \
  "v_smfmac_f32_16x16x64_bf16 v[72:75], v[32:35], a[8:15], v104\n\t" \
  "v_smfmac_f32_16x16x64_bf16 v[76:79], v[32:35], v[48:55], v104\n\t" \
  "v_smfmac_f32_16x16x64_bf16 v[80:83], v[32:35], v[56:63], v104\n\t" \
  "s_waitcnt vmcnt(2)\n\t" \
  "v_smfmac_f32_16x16x64_bf16 v[68:71], v[36:39], a[16:23], v104\n\t" \
  "ds_write_b128 v101, v[36:39] offset:1024\n\t" \
  "v_smfmac_f32_16x16x64_bf16 v[72:75], v[36:39], a[24:31], v104\n\t" \
  "v_smfmac_f32_16x16x64_bf16 v[76:79], v[36:39], a[32:39], v104\n\t" \
  "v_cmp_u_f32 vcc, v68, v69\n\t" \
  "v_smfmac_f32_16x16x64_bf16 v[80:83], v[36:39], a[40:47], v104\n\t" \
  "v_cmp_u_f32 s[82:83], v70, v71\n\t" \
  "s_waitcnt vmcnt(1)\n\t" \
  "v_smfmac_f32_16x16x64_bf16 v[68:71], v[40:43], v[16:23], v104\n\t" \
  "ds_write_b128 v101, v[40:43] offset:2048\n\t" \
  "v_smfmac_f32_16x16x64_bf16 v[72:75], v[40:43], v[24:31], v104\n\t" \
  "s_or_b64 vcc, vcc, s[82:83]\n\t" \
  "s_or_b64 vcc, vcc, s[80:81]\n\t" \
  "s_waitcnt lgkmcnt(1)\n\t" \
  "v_smfmac_f32_16x16x64_bf16 v[76:79], v[40:43], a[48:55], v104\n\t" \
  "s_cbranch_scc1 8f\n\t" \
  "5:\n\t" \
  "s_barrier\n\t" \
  "4:\n\t" \
  "s_memtime s[56:57]\n\t" \
  "ds_read_b128 v[48:51], v101 offset:4096\n\t" \
  "v_smfmac_f32_16x16x64_bf16 v[80:83], v[40:43], a[56:63], v104\n\t" \
  "ds_read_b128 v[52:55], v101 offset:5120\n\t" \
  "ds_read_b128 v[56:59], v101 offset:8192\n\t" \
  "s_waitcnt vmcnt(0)\n\t" \
  "v_smfmac_f32_16x16x64_bf16 v[68:71], v[44:47], a[64:71], v104\n\t" \
  "ds_write_b128 v101, v[44:47] offset:3072\n\t" \
  "v_smfmac_f32_16x16x64_bf16 v[72:75], v[44:47], a[72:79], v104\n\t" \
  "v_smfmac_f32_16x16x64_bf16 v[76:79], v[44:47], a[80:87], v104\n\t" \
  "v_cmp_u_f32 vcc, v68, v69\n\t" \
  "v_smfmac_f32_16x16x64_bf16 v[80:83], v[44:47], a[88:95], v104\n\t" \
  "v_cmp_u_f32 s[82:83], v70, v71\n\t" \
  "s_waitcnt lgkmcnt(3)\n\t" \
  "v_smfmac_f32_16x16x64_bf16 v[68:71], v[48:51], a[96:103], v104\n\t" \
  "v_smfmac_f32_16x16x64_bf16 v[72:75], v[48:51], a[104:111], v104\n\t" \
  "s_or_b64 vcc, vcc, s[82:83]\n\t" \
  "s_waitcnt lgkmcnt(0)\n\t" \
  "v_smfmac_f32_16x16x64_bf16 v[68:71], v[52:55], a[112:119], v104\n\t" \
  "s_cbranch_scc1 9f\n\t" \
  "6:\n\t" \
  "s_barrier\n\t" \
  "ds_read_b128 v[60:63], v101 offset:9216\n\t" \
  "v_smfmac_f32_16x16x64_bf16 v[72:75], v[52:55], a[120:127], v104\n\t" \
  "ds_read_b128 v[64:67], v101 offset:12288\n\t" \
  "s_sub_i32 s79, s56, s84\n\t" \
  "s_mul_hi_u32 s79, s79, s46\n\t" \
  "v_smfmac_f32_16x16x64_bf16 v[68:71], v[56:59], a[192:199], v104\n\t" \
  "v_lshl_add_u32 v116, s79, 9, v118\n\t" \
  "buffer_store_dword v116, v109, %[rh], s45 offen sc1\n\t" \
  "v_smfmac_f32_16x16x64_bf16 v[72:75], v[56:59], a[200:207], v104\n\t" \
  "ds_read_b128 v[48:51], v101 offset:13312\n\t" \
  "s_waitcnt lgkmcnt(2)\n\t" \
  "v_smfmac_f32_16x16x64_bf16 v[68:71], v[60:63], a[208:215], v104\n\t" \
  "ds_read_b128 v[52:55], v101 offset:6144\n\t" \
  "v_smfmac_f32_16x16x64_bf16 v[72:75], v[60:63], a[216:223], v104\n\t" \
  "ds_read_b128 v[56:59], v101 offset:7168\n\t" \
  "s_waitcnt lgkmcnt(3)\n\t" \
  "v_smfmac_f32_16x16x64_bf16 v[68:71], v[64:67], v[152:159], v104\n\t" \
  "ds_read_b128 v[60:63], v101 offset:10240\n\t" \
  "v_smfmac_f32_16x16x64_bf16 v[72:75], v[64:67], v[160:167], v104\n\t" \
  "ds_read_b128 v[32:35], v101 offset:20480\n\t" \
  "s_waitcnt lgkmcnt(4)\n\t" \
  "v_smfmac_f32_16x16x64_bf16 v[68:71], v[48:51], v[168:175], v104\n\t" \
  "ds_read_b128 v[64:67], v101 offset:11264\n\t" \
  "ds_read_b128 v[16:19], v103 offset:49152\n\t" \
  "v_smfmac_f32_16x16x64_bf16 v[72:75], v[48:51], v[176:183], v104\n\t" \
  "ds_read_b128 v[36:39], v101 offset:21504\n\t" \
  "s_waitcnt lgkmcnt(6)\n\t" \
  "v_smfmac_f32_16x16x64_bf16 v[68:71], v[52:55], v[84:91], v104\n\t" \
  "ds_read_b128 v[48:51], v101 offset:15360\n\t" \
  "ds_read_b128 v[20:23], v103 offset:50176\n\t" \
  "v_smfmac_f32_16x16x64_bf16 v[72:75], v[52:55], v[92:99], v104\n\t" \
  "ds_read_b128 v[40:43], v101 offset:22528\n\t" \
  "s_waitcnt lgkmcnt(8)\n\t" \
  "v_smfmac_f32_16x16x64_bf16 v[68:71], v[56:59], a[160:167], v104\n\t" \
  "ds_read_b128 v[52:55], v101 offset:14336\n\t" \
  "ds_read_b128 v[24:27], v103 offset:51200\n\t" \
  "v_smfmac_f32_16x16x64_bf16 v[72:75], v[56:59], a[168:175], v104\n\t" \
  "ds_read_b128 v[28:31], v103 offset:52224\n\t" \
  "s_waitcnt lgkmcnt(10)\n\t" \
  "v_smfmac_f32_16x16x64_bf16 v[68:71], v[60:63], v[0:7], v104\n\t" \
  "ds_read_b128 v[44:47], v101 offset:23552\n\t" \
  "v_smfmac_f32_16x16x64_bf16 v[72:75], v[60:63], v[8:15], v104\n\t" \
  "v_mov_b64 v[92:93], 0\n\t" \
  "s_waitcnt lgkmcnt(9)\n\t" \
  "v_smfmac_f32_16x16x64_bf16 v[68:71], v[64:67], v[120:127], v104\n\t" \
  "ds_read_b128 v[0:3], v101 offset:24576\n\t" \
  "v_smfmac_f32_16x16x64_bf16 v[72:75], v[64:67], v[128:135], v104\n\t" \
  "ds_read_b128 v[4:7], v101 offset:25600\n\t" \
  "s_waitcnt lgkmcnt(8)\n\t" \
  "v_smfmac_f32_16x16x64_bf16 v[68:71], v[48:51], v[216:223], v104\n\t" \
  "v_mov_b64 v[94:95], 0\n\t" \
  "s_waitcnt lgkmcnt(5)\n\t" \
  "v_smfmac_f32_16x16x64_bf16 v[68:71], v[52:55], v[16:23], v104\n\t" \
  "ds_read_b128 v[8:11], v101 offset:26624\n\t" \
  "v_smfmac_f32_16x16x64_bf16 v[72:75], v[48:51], v[224:231], v104\n\t" \
  "ds_read_b128 v[12:15], v101 offset:27648\n\t" \
  "s_waitcnt lgkmcnt(5)\n\t" \
  "v_smfmac_f32_16x16x64_bf16 v[72:75], v[52:55], v[24:31], v104\n\t" \
  "v_add_f32 v96, v68, v70\n\t" \
  "v_smfmac_f32_16x16x64_bf16 v[76:79], v[56:59], a[176:183], v104\n\t" \
  "v_add_f32 v97, v69, v71\n\t" \
  "v_cvt_pk_bf16_f32 v96, v96, v97\n\t" \
  "v_smfmac_f32_16x16x64_bf16 v[80:83], v[56:59], a[184:191], v104\n\t" \
  "v_add_f32 v98, v72, v74\n\t" \
  "v_add_f32 v99, v73, v75\n\t" \
  "v_smfmac_f32_16x16x64_bf16 v[76:79], v[60:63], a[240:247], v104\n\t" \
  "v_cvt_pk_bf16_f32 v98, v98, v99\n\t" \
  "ds_write2st64_b32 v105, v96, v98 offset1:1\n\t" \
  "v_smfmac_f32_16x16x64_bf16 v[80:83], v[60:63], a[248:255], v104\n\t" \
  "ds_read_b128 v[56:59], v101 offset:9216\n\t" \
  "v_mov_b64 v[68:69], 0\n\t" \
  "v_smfmac_f32_16x16x64_bf16 v[76:79], v[64:67], v[136:143], v104\n\t" \
  "ds_read_b128 v[60:63], v101 offset:13312\n\t" \
  "v_mov_b64 v[70:71], 0\n\t" \
  "v_smfmac_f32_16x16x64_bf16 v[80:83], v[64:67], v[144:151], v104\n\t" \
  "v_smfmac_f32_16x16x64_bf16 v[76:79], v[48:51], v[232:239], v104\n\t" \
  "s_waitcnt lgkmcnt(2)\n\t" \
  "s_barrier\n\t" \
  "ds_read_b64_tr_b16 v[84:85], v107 offset:0\n\t" \
  "v_smfmac_f32_16x16x64_bf16 v[80:83], v[48:51], v[240:247], v104\n\t" \
  "ds_read_b64_tr_b16 v[86:87], v107 offset:128\n\t" \
  "ds_read_b64_tr_b16 v[88:89], v107 offset:1024\n\t" \
  "v_smfmac_f32_16x16x64_bf16 v[76:79], v[52:55], v[200:207], v104\n\t" \
  "ds_read_b64_tr_b16 v[90:91], v107 offset:1152\n\t" \
  "v_smfmac_f32_16x16x64_bf16 v[80:83], v[52:55], v[208:215], v104\n\t" \
  "ds_read_b128 v[64:67], v101 offset:6144\n\t" \
  "s_waitcnt lgkmcnt(5)\n\t" \
  "v_smfmac_f32_16x16x64_bf16 v[76:79], v[60:63], v[184:191], v104\n\t" \
  "ds_read_b128 v[52:55], v101 offset:5120\n\t" \
  "s_waitcnt lgkmcnt(4)\n\t" \
  "v_smfmac_f32_16x16x64_bf16 v[92:95], v[84:87], v[32:39], v104\n\t" \
  "s_waitcnt lgkmcnt(2)\n\t" \
  "v_smfmac_f32_16x16x64_bf16 v[92:95], v[88:91], v[40:47], v104\n\t" \
  "v_smfmac_f32_16x16x64_bf16 v[68:71], v[84:87], v[0:7], v104\n\t" \
  "v_smfmac_f32_16x16x64_bf16 v[68:71], v[88:91], v[8:15], v104\n\t" \
  "v_add_f32 v92, v92, v94\n\t" \
  "v_add_f32 v94, v93, v95\n\t" \
  "v_smfmac_f32_16x16x64_bf16 v[80:83], v[60:63], v[192:199], v104\n\t" \
  "v_mov_b32 v97, v94\n\t" \
  "v_mov_b32_dpp v94, v92 row_ror:8 row_mask:0xf bank_mask:0x3\n\t" \
  "s_nop 0\n\t" \
  "v_mov_b32_dpp v92, v97 row_ror:8 row_mask:0xf bank_mask:0xc\n\t" \
  "s_waitcnt lgkmcnt(1)\n\t" \
  "v_smfmac_f32_16x16x64_bf16 v[76:79], v[64:67], a[144:151], v104\n\t" \
  "v_add_f32 v93, v68, v70\n\t" \
  "v_add_f32 v95, v69, v71\n\t" \
  "v_max_f32 v98, v92, v94\n\t" \
  "v_mov_b32 v97, v95\n\t" \
  "v_mov_b32_dpp v95, v93 row_ror:8 row_mask:0xf bank_mask:0x3\n\t" \
  "s_nop 0\n\t" \
  "v_mov_b32_dpp v93, v97 row_ror:8 row_mask:0xf bank_mask:0xc\n\t" \
  "v_max3_f32 v98, v98, v93, v95\n\t" \
  "v_smfmac_f32_16x16x64_bf16 v[80:83], v[64:67], a[152:159], v104\n\t" \
  "s_nop 0\n\t" \
  "v_max_f32_dpp v98, v98, v98 quad_perm:[1,0,3,2] row_mask:0xf bank_mask:0xf\n\t" \
  "s_waitcnt lgkmcnt(0)\n\t" \
  "v_smfmac_f32_16x16x64_bf16 v[76:79], v[52:55], a[128:135], v104\n\t" \
  "v_max_f32_dpp v98, v98, v98 quad_perm:[2,3,0,1] row_mask:0xf bank_mask:0xf\n\t" \
  "v_smfmac_f32_16x16x64_bf16 v[76:79], v[56:59], a[224:231], v104\n\t" \
  "s_nop 0\n\t" \
  "v_max_f32_dpp v98, v98, v98 row_half_mirror row_mask:0xf bank_mask:0xf\n\t" \
  "v_mul_f32 v98, %[cs], v98\n\t" \
  "v_fma_f32 v92, v92, %[cs], -v98\n\t" \
  "v_fma_f32 v93, v93, %[cs], -v98\n\t" \
  "v_fma_f32 v94, v94, %[cs], -v98\n\t" \
  "v_fma_f32 v95, v95, %[cs], -v98\n\t" \
  "v_exp_f32 v72, v92\n\t" \
  "v_exp_f32 v73, v93\n\t" \
  "v_exp_f32 v74, v94\n\t" \
  "v_exp_f32 v75, v95\n\t" \
  "v_cvt_pk_bf16_f32 v68, v72, v73\n\t" \
  "v_cvt_pk_bf16_f32 v69, v74, v75\n\t" \
  "buffer_store_dwordx2 v[68:69], v110, %[rh], s47 offen sc1\n\t" \
  "v_smfmac_f32_16x16x64_bf16 v[80:83], v[52:55], a[136:143], v104\n\t" \
  "v_smfmac_f32_16x16x64_bf16 v[80:83], v[56:59], a[232:239], v104\n\t" \
  "v_add_f32 v20, v76, v78\n\t" \
  "v_add_f32 v21, v77, v79\n\t" \
  "v_cvt_pk_bf16_f32 v20, v20, v21\n\t" \
  "ds_write_b32 v106, v20\n\t" \
  "s_nop 0\n\t" \
  "s_nop 0\n\t" \
  "s_nop 0\n\t" \
  "s_nop 0\n\t" \
  "v_add_f32 v23, v80, v82\n\t" \
  "v_add_f32 v21, v81, v83\n\t" \
  "v_cvt_pk_bf16_f32 v21, v23, v21\n\t" \
  "ds_write_b32 v106, v21 offset:256\n\t" \
  "v_mov_b64 v[0:1], 0\n\t" \
  "v_mov_b64 v[2:3], 0\n\t" \
  "v_mov_b64 v[36:37], 0\n\t" \
  "v_mov_b64 v[38:39], 0\n\t" \
  "s_waitcnt lgkmcnt(0)\n\t" \
  "s_barrier\n\t" \
  "ds_read_b64_tr_b16 v[32:33], v108 offset:12288\n\t" \
  "ds_read_b64_tr_b16 v[34:35], v108 offset:12352\n\t" \
  "ds_read_b64_tr_b16 v[4:5], v108\n\t" \
  "ds_read_b64_tr_b16 v[6:7], v108 offset:64\n\t" \
  "ds_read_b64_tr_b16 v[8:9], v108 offset:512\n\t" \
  "ds_read_b64_tr_b16 v[10:11], v108 offset:576\n\t" \
  "ds_read_b64_tr_b16 v[12:13], v108 offset:1024\n\t" \
  "ds_read_b64_tr_b16 v[14:15], v108 offset:1088\n\t" \
  "ds_read_b64_tr_b16 v[16:17], v108 offset:1536\n\t" \
  "ds_read_b64_tr_b16 v[18:19], v108 offset:1600\n\t" \
  "v_add_f32 v29, v72, v73\n\t" \
  "v_add_f32 v29, v29, v74\n\t" \
  "s_waitcnt lgkmcnt(8)\n\t" \
  "v_smfmac_f32_16x16x64_bf16 v[0:3], v[84:87], v[32:39], v104\n\t" \
  "s_waitcnt lgkmcnt(4)\n\t" \
  "v_smfmac_f32_16x16x64_bf16 v[0:3], v[84:87], v[4:11], v104\n\t" \
  "s_waitcnt lgkmcnt(0)\n\t" \
  "v_smfmac_f32_16x16x64_bf16 v[0:3], v[88:91], v[12:19], v104\n\t" \
  "v_add_f32 v29, v29, v75\n\t" \
  "s_mov_b32 s76, 0xaaaaaaaa\n\t" \
  "s_mov_b32 s77, s76\n\t" \
  "s_nop 4\n\t" \
  "v_add_f32 v96, v0, v2\n\t" \
  "v_add_f32_dpp v96, v1, v3 quad_perm:[0,1,2,3] row_mask:0xf bank_mask:0xc\n\t" \
  "v_fma_f32 v96, v96, %[cs], -v98\n\t" \
  "v_exp_f32 v24, v96\n\t" \
  "s_nop 0\n\t" \
  "v_add_f32 v29, v29, v24\n\t" \
  "s_nop 0\n\t" \
  "v_mov_b32_dpp v25, v24 quad_perm:[1,0,3,2] row_mask:0xf bank_mask:0xf\n\t" \
  "v_add_f32_dpp v29, v29, v29 quad_perm:[1,0,3,2] row_mask:0xf bank_mask:0xf\n\t" \
  "v_cndmask_b32_e64 v24, v24, v114, s[76:77]\n\t" \
  "v_cndmask_b32_e64 v25, v25, v115, s[76:77]\n\t" \
  "v_add_f32_dpp v29, v29, v29 quad_perm:[2,3,0,1] row_mask:0xf bank_mask:0xf\n\t" \
  "s_nop 1\n\t" \
  "v_add_f32_dpp v29, v29, v29 row_half_mirror row_mask:0xf bank_mask:0xf\n\t" \
  "v_rcp_f32 v26, v29\n\t" \
  "v_cmpx_gt_f32 vcc, 0x5d5e0b6b, v29\n\t" \
  "v_mul_f32 v24, v24, v26\n\t" \
  "v_mul_f32 v25, v25, v26\n\t" \
  "v_cvt_pk_bf16_f32 v24, v24, v25\n\t" \
  "buffer_store_dword v24, v111, %[rh], s47 offen sc1\n\t" \
  "s_memtime s[76:77]\n\t" \
  "s_mov_b64 exec, -1\n\t" \
  "v_mov_b32 v97, v29\n\t" \
  "s_cmp_eq_u64 vcc, -1\n\t" \
  "s_cselect_b32 s42, 0, 8\n\t" \
  "s_waitcnt lgkmcnt(0)\n\t" \
  "s_mov_b32 s57, s76\n\t" \
  "s_barrier\n\t" \
  "s_add_i32 s41, s56, 1950\n\t" \
  "s_sub_i32 s76, s57, s56\n\t" \
  "s_add_i32 s77, s43, 8\n\t" \
  "s_min_u32 s76, s76, s77\n\t" \
  "s_sub_i32 s43, s43, 4\n\t" \
  "s_max_u32 s43, s43, s76\n\t" \
  "s_add_i32 s76, s45, 0x4000\n\t" \
  "s_and_b32 s76, s76, 0xc000\n\t" \
  "v_mov_b32 v116, -1\n\t" \
  "buffer_store_dword v116, v109, %[rh], s76 offen sc1\n\t" \
  "s_sub_i32 s76, s92, s51\n\t" \
  "s_sub_i32 s77, s93, s53\n\t" \
  "s_cmp_eq_u32 s76, 0\n\t" \
  "s_cbranch_scc1 21f\n\t" \
  "v_cvt_f32_u32 v117, s76\n\t" \
  "v_cvt_f32_u32 v116, s77\n\t" \
  "v_rcp_f32 v117, v117\n\t" \
  "s_mov_b32 s51, s92\n\t" \
  "v_mul_f32 v117, v117, v116\n\t" \
  "v_max_f32 v117, 0x3f028f5c, v117\n\t" \
  "v_min_f32 v117, 0x3ffeb852, v117\n\t" \
  "v_rcp_f32 v116, v117\n\t" \
  "v_mul_f32 v117, 0x4f000000, v117\n\t" \
  "v_mul_f32 v116, 0x47800000, v116\n\t" \
  "v_cvt_u32_f32 v117, v117\n\t" \
  "v_cvt_u32_f32 v116, v116\n\t" \
  "s_mov_b32 s53, s93\n\t" \
  "v_readfirstlane_b32 s46, v117\n\t" \
  "v_readfirstlane_b32 s52, v116\n\t" \
  "21:\n\t" \
  "s_add_i32 s44, s44, 1\n\t" \
  "s_and_b32 s58, s58, 4\n\t" \
  "s_and_b32 s76, s44, 64\n\t" \
  "s_or_b32 s42, s42, s76\n\t" \
  "s_cbranch_scc0 20b\n\t" \
  "s_branch 99f\n\t" \
  "14:\n\t" \
  "s_sleep 3\n\t" \
  "s_add_u32 s59, s59, 0x10000\n\t" \
  "s_cmp_lt_u32 s59, 0x400000\n\t" \
  "s_cbranch_scc1 13b\n\t" \
  "s_mov_b64 exec, -1\n\t" \
  "s_mov_b32 s42, 16\n\t" \
  "s_branch 99f\n\t" \
  "8:\n\t" \
  "s_bitcmp1_b32 s58, 1\n\t" \
  "s_cbranch_scc1 4b\n\t" \
  "s_bitcmp1_b32 s58, 2\n\t" \
  "s_cbranch_scc1 5b\n\t" \
  "v_max3_u32 v116, v32, v33, v34\n\t" \
  "v_max3_u32 v117, v35, v36, v37\n\t" \
  "v_max3_u32 v116, v116, v38, v39\n\t" \
  "v_max_u32 v116, v116, v117\n\t" \
  "v_cmp_eq_u32 vcc, -1, v116\n\t" \
  "s_cbranch_vccz 5b\n\t" \
  "s_branch 0f\n\t" \
  "9:\n\t" \
  "s_bitcmp1_b32 s58, 2\n\t" \
  "s_cbranch_scc1 6b\n\t" \
  "v_max3_u32 v116, v40, v41, v42\n\t" \
  "v_max3_u32 v117, v43, v44, v45\n\t" \
  "v_max3_u32 v116, v116, v46, v47\n\t" \
  "v_max_u32 v116, v116, v117\n\t" \
  "v_cmp_eq_u32 vcc, -1, v116\n\t" \
  "s_cbranch_vccz 6b\n\t" \
  "s_or_b32 s58, s58, 2\n\t" \
  "0:\n\t" \
  "s_or_b32 s58, s58, 1\n\t" \
  "11:\n\t" \
  "buffer_load_dwordx4 v[32:35], v100, %[rh], s40 offen sc1\n\t" \
  "buffer_load_dwordx4 v[36:39], v100, %[rh], s76 offen sc1\n\t" \
  "buffer_load_dwordx4 v[40:43], v100, %[rh], s77 offen sc1\n\t" \
  "buffer_load_dwordx4 v[44:47], v100, %[rh], s78 offen sc1\n\t" \
  "s_waitcnt lgkmcnt(0)\n\t" \
  "s_branch 10b\n\t" \
  "17:\n\t" \
  "s_add_u32 s59, s59, 1\n\t" \
  "s_bitcmp1_b32 s58, 2\n\t" \
  "s_cbranch_scc1 3b\n\t" \
  "s_waitcnt vmcnt(0)\n\t" \
  "v_max_u32 v116, v32, v33\n\t" \
  "v_max3_u32 v116, v116, v34, v35\n\t" \
  "v_max3_u32 v116, v116, v36, v37\n\t" \
  "v_max3_u32 v116, v116, v38, v39\n\t" \
  "v_max3_u32 v116, v116, v40, v41\n\t" \
  "v_max3_u32 v116, v116, v42, v43\n\t" \
  "v_max3_u32 v116, v116, v44, v45\n\t" \
  "v_max3_u32 v116, v116, v46, v47\n\t" \
  "v_cmp_eq_u32 vcc, -1, v116\n\t" \
  "s_cbranch_vccz 3b\n\t" \
  "s_bitcmp1_b32 s59, 6\n\t" \
  "s_cbranch_scc1 12f\n\t" \
  "buffer_load_dwordx4 v[32:35], v100, %[rh], s40 offen sc1\n\t" \
  "buffer_load_dwordx4 v[36:39], v100, %[rh], s76 offen sc1\n\t" \
  "buffer_load_dwordx4 v[40:43], v100, %[rh], s77 offen sc1\n\t" \
  "buffer_load_dwordx4 v[44:47], v100, %[rh], s78 offen sc1\n\t" \
  "s_branch 17b\n\t" \
  "12:\n\t" \
  "s_mov_b32 s42, s58\n\t" \
  "s_branch 99f\n\t" \
  "91:\n\t" \
  "s_mov_b32 s58, s42\n\t" \
  "s_mov_b32 s95, 0\n\t" \
  "20:\n\t" \
  "s_and_b32 s76, s44, 7\n\t" \
  "s_lshl_b32 s76, s76, 15\n\t" \
  "s_add_i32 s40, s54, s76\n\t" \
  "s_and_b32 s76, s44, 3\n\t" \
  "s_lshl_b32 s45, s76, 14\n\t" \
  "s_add_i32 s47, s44, 1\n\t" \
  "s_min_u32 s47, s47, 63\n\t" \
  "s_lshr_b32 s76, s47, 1\n\t" \
  "s_lshl_b32 s47, s47, 20\n\t" \
  "s_add_i32 s47, s47, s55\n\t" \
  "s_bitcmp1_b64 %[sel], s76\n\t" \
  "s_cselect_b32 s76, 0x2000, 0\n\t" \
  "s_add_i32 s47, s47, s76\n\t" \
  "s_lshr_b32 s76, s44, 1\n\t" \
  "s_lshl_b32 s94, s44, 20\n\t" \
  "s_bitcmp1_b64 %[sel], s76\n\t" \
  "s_cselect_b32 s76, 0x2000, 0\n\t" \
  "s_add_i32 s94, s94, s76\n\t" \
  "s_add_i32 s49, s44, -1\n\t" \
  "s_and_b32 s48, s49, 2\n\t" \
  "s_lshl_b32 s48, s48, 13\n\t" \
  "s_and_b32 s76, s49, 1\n\t" \
  "s_lshl_b32 s76, s76, 12\n\t" \
  "s_add_i32 s48, s48, s76\n\t" \
  "s_lshl_b32 s50, s53, 8\n\t" \
  "s_mov_b32 s59, 0\n\t" \
  "s_mov_b32 s92, s51\n\t" \
  "s_mov_b32 s93, s53\n\t" \
  "s_xor_b32 s76, s40, 0x400\n\t" \
  "s_xor_b32 s77, s40, 0x800\n\t" \
  "s_xor_b32 s78, s40, 0xc00\n\t" \
  "s_cmp_lg_u32 s58, 0\n\t" \
  "s_cbranch_scc1 11f\n\t" \
  "s_cmp_lg_u32 s95, 0\n\t" \
  "s_cbranch_scc1 46f\n\t" \
  "10:\n\t" \
  "ds_read_b128 v[48:51], v102 offset:32768\n\t" \
  "ds_read_b128 v[52:55], v102 offset:33792\n\t" \
  "ds_read_b128 v[56:59], v102 offset:34816\n\t" \
  "ds_read_b128 v[60:63], v102 offset:35840\n\t" \
  "ds_read_b128 v[16:19], v102 offset:49152\n\t" \
  "ds_read_b128 v[20:23], v102 offset:50176\n\t" \
  "ds_read_b128 v[24:27], v102 offset:51200\n\t" \
  "ds_read_b128 v[28:31], v102 offset:52224\n\t" \
  "ds_read_b128 v[84:87], v103 offset:16384\n\t" \
  "ds_read_b128 v[88:91], v103 offset:17408\n\t" \
  "ds_read_b128 v[92:95], v103 offset:18432\n\t" \
  "ds_read_b128 v[96:99], v103 offset:19456\n\t" \
  "s_waitcnt lgkmcnt(4)\n\t" \
  "ds_read_b128 v[0:3], v103 offset:49152\n\t" \
  "ds_read_b128 v[4:7], v103 offset:50176\n\t" \
  "ds_read_b128 v[8:11], v103 offset:51200\n\t" \
  "ds_read_b128 v[12:15], v103 offset:52224\n\t" \
  "46:\n\t" \
  "s_bitcmp1_b32 s58, 1\n\t" \
  "s_cselect_b64 s[80:81], -1, 0\n\t" \
  "s_cmp_lg_u32 s58, 0\n\t" \
  "s_cbranch_scc1 17f\n\t" \
  "s_mov_b64 exec, 0xff\n\t" \
  "s_cmp_eq_u32 s95, 0\n\t" \
  "s_cbranch_scc1 16f\n\t" \
  "s_waitcnt vmcnt(2)\n\t" \
  "v_mov_b32 v116, v118\n\t" \
  "s_branch 15f\n\t" \
  "16:\n\t" \
  "s_memtime vcc\n\t" \
  "s_waitcnt lgkmcnt(0)\n\t" \
  "s_sub_i32 s79, s41, vcc_lo\n\t" \
  "s_cmp_gt_i32 s79, 64\n\t" \
  "s_cbranch_scc1 16b\n\t" \
  "13:\n\t" \
  "buffer_load_dword v116, v112, %[rh], s48 offen sc1\n\t" \
  "s_waitcnt vmcnt(0)\n\t" \
  "15:\n\t" \
  "v_mov_b32 v117, 20000\n\t" \
  "v_cmp_ne_u32_sdwa vcc, v116, s49 src0_sel:BYTE_0 src1_sel:DWORD\n\t" \
  "v_subrev_u32 v116, s50, v116\n\t" \
  "v_ashrrev_i32 v116, 8, v116\n\t" \
  "s_nop 1\n\t" \
  "v_max_i32_dpp v116, v116, v116 quad_perm:[1,0,3,2] row_mask:0xf bank_mask:0xf\n\t" \
  "s_cbranch_vccnz 14f\n\t" \
  "s_nop 0\n\t" \
  "v_max_i32_dpp v116, v116, v116 quad_perm:[2,3,0,1] row_mask:0xf bank_mask:0xf\n\t" \
  "s_nop 1\n\t" \
  "v_max_i32_dpp v116, v116, v116 row_half_mirror row_mask:0xf bank_mask:0xf\n\t" \
  "v_med3_i32 v116, v116, 0, v117\n\t" \
  "v_mul_u32_u24 v116, s52, v116\n\t" \
  "v_lshrrev_b32 v116, 16, v116\n\t" \
  "s_mov_b64 exec, -1\n\t" \
  "v_readfirstlane_b32 s84, v116\n\t" \
  "s_add_i32 s84, s84, s51\n\t" \
  ".p2align 6\n\t" \
  "s_nop 0\n\t" \
  "s_nop 0\n\t" \
  "s_nop 0\n\t" \
  "s_nop 0\n\t" \
  "s_nop 0\n\t" \
  "s_nop 0\n\t" \
  "s_nop 0\n\t" \
  "s_nop 0\n\t" \
  "1:\n\t" \
  "s_memtime vcc\n\t" \
  "s_waitcnt lgkmcnt(0)\n\t" \
  "s_sub_i32 s79, s84, vcc_lo\n\t" \
  "s_cmp_gt_i32 s79, 128\n\t" \
  "s_cbranch_scc1 1b\n\t" \
  ".p2align 4\n\t" \
  "2:\n\t" \
  "s_sub_i32 s79, s79, 33\n\t" \
  "s_cmp_gt_i32 s79, 56\n\t" \
  "s_cbranch_scc1 2b\n\t" \
  "buffer_load_dwordx4 v[32:35], v100, %[rh], s40 offen sc1\n\t" \
  "s_nop 12\n\t" \
  "buffer_load_dwordx4 v[36:39], v100, %[rh], s76 offen sc1\n\t" \
  "s_nop 12\n\t" \
  "buffer_load_dwordx4 v[40:43], v100, %[rh], s77 offen sc1\n\t" \
  "s_nop 12\n\t" \
  "buffer_load_dwordx4 v[44:47], v100, %[rh], s78 offen sc1\n\t" \
  "s_memtime vcc\n\t" \
  "s_memrealtime s[82:83]\n\t" \
  "s_nop 4\n\t" \
  "s_memrealtime s[84:85]\n\t" \
  "s_nop 4\n\t" \
  "s_memrealtime s[56:57]\n\t" \
  "s_nop 4\n\t" \
  "s_memrealtime s[86:87]\n\t" \
  "s_nop 4\n\t" \
  "s_memrealtime s[88:89]\n\t" \
  "s_nop 4\n\t" \
  "s_memrealtime s[90:91]\n\t" \
  "s_mov_b32 s92, 0\n\t" \
  "s_waitcnt lgkmcnt(0)\n\t" \
  "s_cmp_eq_u32 s82, s84\n\t" \
  "s_addc_u32 s92, s92, 0\n\t" \
  "s_cmp_eq_u32 s82, s56\n\t" \
  "s_addc_u32 s92, s92, 0\n\t" \
  "s_cmp_eq_u32 s82, s86\n\t" \
  "s_addc_u32 s92, s92, 0\n\t" \
  "s_cmp_eq_u32 s82, s88\n\t" \
  "s_addc_u32 s92, s92, 0\n\t" \
  "s_cmp_eq_u32 s82, s90\n\t" \
  "s_addc_u32 s92, s92, 0\n\t" \
  "s_mul_i32 s92, s92, 24\n\t" \
  "s_add_i32 s92, s92, vcc_lo\n\t" \
  "s_lshl4_add_u32 s93, s82, 64\n\t" \
  "3:\n\t" \
  "v_mov_b64 v[68:69], 0\n\t" \
  "v_mov_b64 v[70:71], 0\n\t" \
  "v_mov_b64 v[72:73], 0\n\t" \
  "v_mov_b64 v[74:75], 0\n\t" \
  "v_mov_b64 v[76:77], 0\n\t" \
  "v_mov_b64 v[78:79], 0\n\t" \
  "v_mov_b64 v[80:81], 0\n\t" \
  "v_mov_b64 v[82:83], 0\n\t" \
  "s_waitcnt lgkmcnt(0)\n\t" \
  "s_waitcnt vmcnt(3)\n\t" \
  "v_smfmac_f32_16x16x64_bf16 v[68:71], v[32:35], a[96:103], v104\n\t" \
  "ds_write_b128 v101, v[32:35] offset:4096\n\t" \
  "v_smfmac_f32_16x16x64_bf16 v[72:75], v[32:35], a[104:111], v104\n\t" \
  "v_smfmac_f32_16x16x64_bf16 v[76:79], v[32:35], v[48:55], v104\n\t" \
  "v_smfmac_f32_16x16x64_bf16 v[80:83], v[32:35], v[56:63], v104\n\t" \
  "s_waitcnt vmcnt(2)\n\t" \
  "v_smfmac_f32_16x16x64_bf16 v[68:71], v[36:39], a[112:119], v104\n\t" \
  "ds_write_b128 v101, v[36:39] offset:5120\n\t" \
  "v_smfmac_f32_16x16x64_bf16 v[72:75], v[36:39], a[120:127], v104\n\t" \
  "v_smfmac_f32_16x16x64_bf16 v[76:79], v[36:39], a[128:135], v104\n\t" \
  "v_cmp_u_f32 vcc, v68, v69\n\t" \
  "v_smfmac_f32_16x16x64_bf16 v[80:83], v[36:39], a[136:143], v104\n\t" \
  "v_cmp_u_f32 s[82:83], v70, v71\n\t" \
  "s_waitcnt vmcnt(1)\n\t" \
  "v_smfmac_f32_16x16x64_bf16 v[68:71], v[40:43], v[16:23], v104\n\t" \
  "ds_write_b128 v101, v[40:43] offset:6144\n\t" \
  "v_smfmac_f32_16x16x64_bf16 v[72:75], v[40:43], v[24:31], v104\n\t" \
  "s_or_b64 vcc, vcc, s[82:83]\n\t" \
  "s_or_b64 vcc, vcc, s[80:81]\n\t" \
  "s_waitcnt lgkmcnt(1)\n\t" \
  "v_smfmac_f32_16x16x64_bf16 v[76:79], v[40:43], a[144:151], v104\n\t" \
  "s_cbranch_scc1 8f\n\t" \
  "5:\n\t" \
  "s_barrier\n\t" \
  "4:\n\t" \
  "s_memtime s[56:57]\n\t" \
  "ds_read_b128 v[48:51], v101 offset:8192\n\t" \
  "v_smfmac_f32_16x16x64_bf16 v[80:83], v[40:43], a[152:159], v104\n\t" \
  "ds_read_b128 v[52:55], v101 offset:9216\n\t" \
  "ds_read_b128 v[56:59], v101 offset:12288\n\t" \
  "s_waitcnt vmcnt(0)\n\t" \
  "v_smfmac_f32_16x16x64_bf16 v[68:71], v[44:47], a[160:167], v104\n\t" \
  "ds_write_b128 v101, v[44:47] offset:7168\n\t" \
  "v_smfmac_f32_16x16x64_bf16 v[72:75], v[44:47], a[168:175], v104\n\t" \
  "v_smfmac_f32_16x16x64_bf16 v[76:79], v[44:47], a[176:183], v104\n\t" \
  "v_cmp_u_f32 vcc, v68, v69\n\t" \
  "v_smfmac_f32_16x16x64_bf16 v[80:83], v[44:47], a[184:191], v104\n\t" \
  "v_cmp_u_f32 s[82:83], v70, v71\n\t" \
  "s_waitcnt lgkmcnt(3)\n\t" \
  "v_smfmac_f32_16x16x64_bf16 v[68:71], v[48:51], a[192:199], v104\n\t" \
  "v_smfmac_f32_16x16x64_bf16 v[72:75], v[48:51], a[200:207], v104\n\t" \
  "s_or_b64 vcc, vcc, s[82:83]\n\t" \
  "s_waitcnt lgkmcnt(0)\n\t" \
  "v_smfmac_f32_16x16x64_bf16 v[68:71], v[52:55], a[208:215], v104\n\t" \
  "s_cbranch_scc1 9f\n\t" \
  "6:\n\t" \
  "s_barrier\n\t" \
  "ds_read_b128 v[60:63], v101 offset:13312\n\t" \
  "v_smfmac_f32_16x16x64_bf16 v[72:75], v[52:55], a[216:223], v104\n\t" \
  "ds_read_b128 v[64:67], v101 offset:0\n\t" \
  "v_smfmac_f32_16x16x64_bf16 v[68:71], v[56:59], v[152:159], v104\n\t" \
  "v_smfmac_f32_16x16x64_bf16 v[72:75], v[56:59], v[160:167], v104\n\t" \
  "ds_read_b128 v[48:51], v101 offset:1024\n\t" \
  "s_waitcnt lgkmcnt(2)\n\t" \
  "v_smfmac_f32_16x16x64_bf16 v[68:71], v[60:63], v[168:175], v104\n\t" \
  "ds_read_b128 v[52:55], v101 offset:10240\n\t" \
  "v_smfmac_f32_16x16x64_bf16 v[72:75], v[60:63], v[176:183], v104\n\t" \
  "ds_read_b128 v[56:59], v101 offset:11264\n\t" \
  "s_waitcnt lgkmcnt(3)\n\t" \
  "v_smfmac_f32_16x16x64_bf16 v[68:71], v[64:67], a[0:7], v104\n\t" \
  "ds_read_b128 v[60:63], v101 offset:14336\n\t" \
  "v_smfmac_f32_16x16x64_bf16 v[72:75], v[64:67], a[8:15], v104\n\t" \
  "s_waitcnt lgkmcnt(3)\n\t" \
  "v_smfmac_f32_16x16x64_bf16 v[68:71], v[48:51], a[16:23], v104\n\t" \
  "ds_read_b128 v[64:67], v101 offset:15360\n\t" \
  "ds_read_b128 v[16:19], v102 offset:16384\n\t" \
  "v_smfmac_f32_16x16x64_bf16 v[72:75], v[48:51], a[24:31], v104\n\t" \
  "s_waitcnt lgkmcnt(4)\n\t" \
  "v_smfmac_f32_16x16x64_bf16 v[68:71], v[52:55], v[84:91], v104\n\t" \
  "ds_read_b128 v[48:51], v101 offset:3072\n\t" \
  "ds_read_b128 v[20:23], v102 offset:17408\n\t" \
  "v_smfmac_f32_16x16x64_bf16 v[72:75], v[52:55], v[92:99], v104\n\t" \
  "s_waitcnt lgkmcnt(5)\n\t" \
  "v_smfmac_f32_16x16x64_bf16 v[68:71], v[56:59], v[120:127], v104\n\t" \
  "ds_read_b128 v[52:55], v101 offset:2048\n\t" \
  "ds_read_b128 v[24:27], v102 offset:18432\n\t" \
  "v_smfmac_f32_16x16x64_bf16 v[72:75], v[56:59], v[128:135], v104\n\t" \
  "ds_read_b128 v[28:31], v102 offset:19456\n\t" \
  "s_waitcnt lgkmcnt(7)\n\t" \
  "v_smfmac_f32_16x16x64_bf16 v[68:71], v[60:63], v[0:7], v104\n\t" \
  "ds_read_b128 v[36:39], v102 offset:28672\n\t" \
  "v_smfmac_f32_16x16x64_bf16 v[72:75], v[60:63], v[8:15], v104\n\t" \
  "buffer_load_dwordx4 v[84:87], v107, %[ru], s94 offen offset:0 sc1\n\t" \
  "s_waitcnt lgkmcnt(7)\n\t" \
  "v_smfmac_f32_16x16x64_bf16 v[68:71], v[64:67], v[216:223], v104\n\t" \
  "ds_read_b128 v[0:3], v103 offset:0\n\t" \
  "v_smfmac_f32_16x16x64_bf16 v[72:75], v[64:67], v[224:231], v104\n\t" \
  "ds_read_b128 v[4:7], v103 offset:1024\n\t" \
  "s_waitcnt lgkmcnt(7)\n\t" \
  "v_smfmac_f32_16x16x64_bf16 v[68:71], v[48:51], a[64:71], v104\n\t" \
  "buffer_load_dwordx4 v[88:91], v107, %[ru], s94 offen offset:1024 sc1\n\t" \
  "s_waitcnt lgkmcnt(5)\n\t" \
  "v_smfmac_f32_16x16x64_bf16 v[68:71], v[52:55], v[16:23], v104\n\t" \
  "ds_read_b128 v[8:11], v103 offset:2048\n\t" \
  "v_smfmac_f32_16x16x64_bf16 v[72:75], v[48:51], a[72:79], v104\n\t" \
  "ds_read_b128 v[12:15], v103 offset:3072\n\t" \
  "s_waitcnt lgkmcnt(5)\n\t" \
  "v_smfmac_f32_16x16x64_bf16 v[72:75], v[52:55], v[24:31], v104\n\t" \
  "v_add_f32 v44, v68, v70\n\t" \
  "ds_read_b128 v[40:43], v102 offset:29696\n\t" \
  "v_smfmac_f32_16x16x64_bf16 v[76:79], v[56:59], v[136:143], v104\n\t" \
  "v_add_f32 v45, v69, v71\n\t" \
  "v_cvt_pk_bf16_f32 v44, v44, v45\n\t" \
  "v_smfmac_f32_16x16x64_bf16 v[80:83], v[56:59], v[144:151], v104\n\t" \
  "v_add_f32 v46, v72, v74\n\t" \
  "v_add_f32 v47, v73, v75\n\t" \
  "v_smfmac_f32_16x16x64_bf16 v[76:79], v[60:63], v[200:207], v104\n\t" \
  "v_cvt_pk_bf16_f32 v46, v46, v47\n\t" \
  "ds_write2st64_b32 v105, v44, v46 offset1:1\n\t" \
  "v_smfmac_f32_16x16x64_bf16 v[80:83], v[60:63], v[208:215], v104\n\t" \
  "ds_read_b128 v[56:59], v101 offset:8192\n\t" \
  "v_mov_b64 v[68:69], 0\n\t" \
  "v_smfmac_f32_16x16x64_bf16 v[76:79], v[64:67], v[232:239], v104\n\t" \
  "ds_read_b128 v[16:19], v103 offset:32768\n\t" \
  "v_mov_b64 v[70:71], 0\n\t" \
  "v_smfmac_f32_16x16x64_bf16 v[80:83], v[64:67], v[240:247], v104\n\t" \
  "ds_read_b128 v[20:23], v103 offset:33792\n\t" \
  "v_smfmac_f32_16x16x64_bf16 v[76:79], v[48:51], a[80:87], v104\n\t" \
  "s_waitcnt lgkmcnt(3)\n\t" \
  "s_barrier\n\t" \
  "v_smfmac_f32_16x16x64_bf16 v[80:83], v[48:51], a[88:95], v104\n\t" \
  "buffer_load_dwordx4 v[92:95], v107, %[ru], s94 offen offset:2048 sc1\n\t" \
  "v_smfmac_f32_16x16x64_bf16 v[68:71], v[32:35], v[36:43], v104\n\t" \
  "ds_read_b128 v[36:39], v102 offset:61440\n\t" \
  "ds_read_b128 v[40:43], v102 offset:62464\n\t" \
  "v_smfmac_f32_16x16x64_bf16 v[76:79], v[52:55], a[48:55], v104\n\t" \
  "ds_read_b128 v[60:63], v101 offset:1024\n\t" \
  "ds_read_b128 v[24:27], v103 offset:34816\n\t" \
  "v_smfmac_f32_16x16x64_bf16 v[80:83], v[52:55], a[56:63], v104\n\t" \
  "ds_read_b128 v[64:67], v101 offset:10240\n\t" \
  "buffer_load_dwordx4 v[96:99], v107, %[ru], s94 offen offset:3072 sc1\n\t" \
  "s_waitcnt lgkmcnt(7)\n\t" \
  "v_smfmac_f32_16x16x64_bf16 v[76:79], v[56:59], v[0:7], v104\n\t" \
  "ds_read_b128 v[48:51], v101 offset:12288\n\t" \
  "ds_read_b128 v[28:31], v103 offset:35840\n\t" \
  "global_load_dword v119, v109, %[kc] sc1\n\t" \
  "v_smfmac_f32_16x16x64_bf16 v[80:83], v[56:59], v[8:15], v104\n\t" \
  "s_waitcnt lgkmcnt(4)\n\t" \
  "v_smfmac_f32_16x16x64_bf16 v[76:79], v[60:63], a[32:39], v104\n\t" \
  "ds_read_b128 v[52:55], v101 offset:9216\n\t" \
  "buffer_load_dwordx4 v[32:35], v100, %[ru], s47 offen offset:0 sc1\n\t" \
  "v_smfmac_f32_16x16x64_bf16 v[80:83], v[60:63], a[40:47], v104\n\t" \
  "ds_read_b128 v[0:3], v102 offset:0\n\t" \
  "v_smfmac_f32_16x16x64_bf16 v[68:71], v[56:59], v[36:43], v104\n\t" \
  "ds_read_b128 v[36:39], v103 offset:28672\n\t" \
  "ds_read_b128 v[40:43], v103 offset:29696\n\t" \
  "s_waitcnt lgkmcnt(6)\n\t" \
  "v_smfmac_f32_16x16x64_bf16 v[76:79], v[64:67], a[240:247], v104\n\t" \
  "ds_read_b128 v[56:59], v101 offset:13312\n\t" \
  "v_smfmac_f32_16x16x64_bf16 v[80:83], v[64:67], a[248:255], v104\n\t" \
  "ds_read_b128 v[60:63], v101 offset:0\n\t" \
  "ds_read_b128 v[4:7], v102 offset:1024\n\t" \
  "buffer_load_dwordx4 v[44:47], v100, %[ru], s47 offen offset:1024 sc1\n\t" \
  "s_waitcnt lgkmcnt(8)\n\t" \
  "v_smfmac_f32_16x16x64_bf16 v[76:79], v[48:51], v[16:23], v104\n\t" \
  "ds_read_b128 v[8:11], v102 offset:2048\n\t" \
  "s_waitcnt lgkmcnt(8)\n\t" \
  "v_smfmac_f32_16x16x64_bf16 v[80:83], v[48:51], v[24:31], v104\n\t" \
  "ds_read_b128 v[12:15], v102 offset:3072\n\t" \
  "s_waitcnt lgkmcnt(8)\n\t" \
  "v_smfmac_f32_16x16x64_bf16 v[76:79], v[52:55], a[224:231], v104\n\t" \
  "s_waitcnt lgkmcnt(5)\n\t" \
  "v_smfmac_f32_16x16x64_bf16 v[68:71], v[48:51], v[36:43], v104\n\t" \
  "v_smfmac_f32_16x16x64_bf16 v[80:83], v[52:55], a[232:239], v104\n\t" \
  "s_waitcnt lgkmcnt(4)\n\t" \
  "v_smfmac_f32_16x16x64_bf16 v[76:79], v[56:59], v[184:191], v104\n\t" \
  "v_add_f32 v72, v68, v70\n\t" \
  "s_waitcnt lgkmcnt(2)\n\t" \
  "v_smfmac_f32_16x16x64_bf16 v[76:79], v[60:63], v[0:7], v104\n\t" \
  "v_add_f32 v73, v69, v71\n\t" \
  "v_smfmac_f32_16x16x64_bf16 v[80:83], v[56:59], v[192:199], v104\n\t" \
  "v_cvt_pk_bf16_f32 v72, v72, v73\n\t" \
  "s_waitcnt lgkmcnt(0)\n\t" \
  "v_smfmac_f32_16x16x64_bf16 v[80:83], v[60:63], v[8:15], v104\n\t" \
  "ds_write_b32 v106, v72 offset:11776\n\t" \
  "v_add_f32 v20, v76, v78\n\t" \
  "v_add_f32 v21, v77, v79\n\t" \
  "v_cvt_pk_bf16_f32 v20, v20, v21\n\t" \
  "ds_write_b32 v106, v20\n\t" \
  "s_nop 3\n\t" \
  "v_add_f32 v23, v80, v82\n\t" \
  "v_add_f32 v21, v81, v83\n\t" \
  "v_cvt_pk_bf16_f32 v21, v23, v21\n\t" \
  "ds_write_b32 v106, v21 offset:256\n\t" \
  "s_waitcnt lgkmcnt(0)\n\t" \
  "s_barrier\n\t" \
  "v_bfe_i32 v64, v114, 0, 1\n\t" \
  "v_and_b32 v64, 0xff800000, v64\n\t" \
  "v_bfe_i32 v65, v114, 1, 1\n\t" \
  "v_and_b32 v65, 0xff800000, v65\n\t" \
  "v_bfe_i32 v66, v114, 2, 1\n\t" \
  "v_and_b32 v66, 0xff800000, v66\n\t" \
  "v_bfe_i32 v67, v114, 3, 1\n\t" \
  "v_and_b32 v67, 0xff800000, v67\n\t" \
  "v_bfe_i32 v68, v114, 4, 1\n\t" \
  "v_and_b32 v68, 0xff800000, v68\n\t" \
  "v_bfe_i32 v69, v114, 5, 1\n\t" \
  "v_and_b32 v69, 0xff800000, v69\n\t" \
  "v_bfe_i32 v70, v114, 6, 1\n\t" \
  "v_and_b32 v70, 0xff800000, v70\n\t" \
  "v_bfe_i32 v71, v114, 7, 1\n\t" \
  "v_and_b32 v71, 0xff800000, v71\n\t" \
  "s_lshl_b32 s94, s45, 4\n\t" \
  "s_and_b32 s96, s44, 2\n\t" \
  "s_lshl_b32 s96, s96, 13\n\t" \
  "s_and_b32 s76, s44, 1\n\t" \
  "s_lshl_b32 s76, s76, 12\n\t" \
  "s_add_i32 s96, s96, s76\n\t" \
  "s_mov_b32 s95, 1\n\t" \
  "s_add_i32 s41, s56, 1950\n\t" \
  "s_sub_i32 s76, s92, s51\n\t" \
  "s_sub_i32 s77, s93, s53\n\t" \
  "s_cmp_eq_u32 s76, 0\n\t" \
  "s_cbranch_scc1 21f\n\t" \
  "v_cvt_f32_u32 v117, s76\n\t" \
  "v_cvt_f32_u32 v116, s77\n\t" \
  "v_rcp_f32 v117, v117\n\t" \
  "s_mov_b32 s51, s92\n\t" \
  "v_mul_f32 v117, v117, v116\n\t" \
  "v_max_f32 v117, 0x3f028f5c, v117\n\t" \
  "v_min_f32 v117, 0x3ffeb852, v117\n\t" \
  "v_rcp_f32 v116, v117\n\t" \
  "v_mul_f32 v117, 0x4f000000, v117\n\t" \
  "v_mul_f32 v116, 0x47800000, v116\n\t" \
  "v_cvt_u32_f32 v117, v117\n\t" \
  "v_cvt_u32_f32 v116, v116\n\t" \
  "s_mov_b32 s53, s93\n\t" \
  "v_readfirstlane_b32 s46, v117\n\t" \
  "v_readfirstlane_b32 s52, v116\n\t" \
  "21:\n\t" \
  "ds_read_b128 v[0:3], v101 offset:20480\n\t" \
  "ds_read_b128 v[4:7], v101 offset:21504\n\t" \
  "ds_read_b128 v[8:11], v101 offset:22528\n\t" \
  "ds_read_b128 v[12:15], v101 offset:23552\n\t" \
  "ds_read_b128 v[16:19], v101 offset:24576\n\t" \
  "ds_read_b128 v[20:23], v101 offset:25600\n\t" \
  "ds_read_b128 v[24:27], v101 offset:26624\n\t" \
  "ds_read_b128 v[28:31], v101 offset:27648\n\t" \
  "ds_read_b64_tr_b16 v[48:49], v108 offset:0\n\t" \
  "ds_read_b64_tr_b16 v[50:51], v108 offset:64\n\t" \
  "ds_read_b64_tr_b16 v[52:53], v108 offset:512\n\t" \
  "ds_read_b64_tr_b16 v[54:55], v108 offset:576\n\t" \
  "ds_read_b64_tr_b16 v[56:57], v108 offset:1024\n\t" \
  "ds_read_b64_tr_b16 v[58:59], v108 offset:1088\n\t" \
  "ds_read_b64_tr_b16 v[60:61], v108 offset:1536\n\t" \
  "ds_read_b64_tr_b16 v[62:63], v108 offset:1600\n\t" \
  "ds_read_b64_tr_b16 v[76:77], v108 offset:12288\n\t" \
  "ds_read_b64_tr_b16 v[78:79], v108 offset:12352\n\t" \
  "s_waitcnt vmcnt(3) lgkmcnt(10)\n\t" \
  "v_mfma_f32_16x16x32_bf16 v[64:67], v[0:3], v[84:87], v[64:67]\n\t" \
  "v_mfma_f32_16x16x32_bf16 v[68:71], v[16:19], v[84:87], v[68:71]\n\t" \
  "v_mfma_f32_16x16x32_bf16 v[64:67], v[4:7], v[88:91], v[64:67]\n\t" \
  "v_mfma_f32_16x16x32_bf16 v[68:71], v[20:23], v[88:91], v[68:71]\n\t" \
  "v_mfma_f32_16x16x32_bf16 v[64:67], v[8:11], v[92:95], v[64:67]\n\t" \
  "v_mfma_f32_16x16x32_bf16 v[68:71], v[24:27], v[92:95], v[68:71]\n\t" \
  "v_mfma_f32_16x16x32_bf16 v[64:67], v[12:15], v[96:99], v[64:67]\n\t" \
  "v_mfma_f32_16x16x32_bf16 v[68:71], v[28:31], v[96:99], v[68:71]\n\t" \
  "s_waitcnt lgkmcnt(0)\n\t" \
  "s_barrier\n\t" \
  "v_mfma_f32_16x16x32_bf16 v[72:75], v[48:51], v[84:87], 0\n\t" \
  "v_mfma_f32_16x16x32_bf16 v[72:75], v[52:55], v[88:91], v[72:75]\n\t" \
  "v_mfma_f32_16x16x32_bf16 v[72:75], v[56:59], v[92:95], v[72:75]\n\t" \
  "v_mfma_f32_16x16x32_bf16 v[72:75], v[60:63], v[96:99], v[72:75]\n\t" \
  "v_mfma_f32_16x16x32_bf16 v[72:75], v[76:79], v[84:87], v[72:75]\n\t" \
  "s_waitcnt vmcnt(0)\n\t" \
  "ds_write_b128 v101, v[32:35] offset:20480\n\t" \
  "ds_write_b128 v101, v[44:47] offset:21504\n\t" \
  "ds_read_b128 v[48:51], v102 offset:32768\n\t" \
  "ds_read_b128 v[52:55], v102 offset:33792\n\t" \
  "ds_read_b128 v[56:59], v102 offset:34816\n\t" \
  "ds_read_b128 v[60:63], v102 offset:35840\n\t" \
  "ds_read_b128 v[16:19], v102 offset:49152\n\t" \
  "ds_read_b128 v[20:23], v102 offset:50176\n\t" \
  "ds_read_b128 v[24:27], v102 offset:51200\n\t" \
  "ds_read_b128 v[28:31], v102 offset:52224\n\t" \
  "ds_read_b128 v[84:87], v103 offset:16384\n\t" \
  "ds_read_b128 v[88:91], v103 offset:17408\n\t" \
  "ds_read_b128 v[92:95], v103 offset:18432\n\t" \
  "ds_read_b128 v[96:99], v103 offset:19456\n\t" \
  "ds_read_b128 v[0:3], v103 offset:49152\n\t" \
  "ds_read_b128 v[4:7], v103 offset:50176\n\t" \
  "ds_read_b128 v[8:11], v103 offset:51200\n\t" \
  "ds_read_b128 v[12:15], v103 offset:52224\n\t" \
  "v_max3_f32 v76, v64, v65, v66\n\t" \
  "v_max3_f32 v77, v67, v68, v69\n\t" \
  "v_max3_f32 v76, v76, v70, v71\n\t" \
  "v_max3_f32 v77, v77, v72, v73\n\t" \
  "v_max3_f32 v76, v76, v74, v75\n\t" \
  "v_max_f32 v76, v76, v77\n\t" \
  "v_mov_b32 v77, v76\n\t" \
  "s_nop 1\n\t" \
  "v_permlane16_swap_b32 v76, v77\n\t" \
  "v_max_f32 v76, v76, v77\n\t" \
  "v_mov_b32 v77, v76\n\t" \
  "s_nop 1\n\t" \
  "v_permlane32_swap_b32 v76, v77\n\t" \
  "v_max_f32 v76, v76, v77\n\t" \
  "v_mul_f32 v76, %[cs], v76\n\t" \
  "v_fma_f32 v64, v64, %[cs], -v76\n\t" \
  "v_fma_f32 v65, v65, %[cs], -v76\n\t" \
  "v_fma_f32 v66, v66, %[cs], -v76\n\t" \
  "v_fma_f32 v67, v67, %[cs], -v76\n\t" \
  "v_fma_f32 v68, v68, %[cs], -v76\n\t" \
  "v_fma_f32 v69, v69, %[cs], -v76\n\t" \
  "v_fma_f32 v70, v70, %[cs], -v76\n\t" \
  "v_fma_f32 v71, v71, %[cs], -v76\n\t" \
  "v_fma_f32 v72, v72, %[cs], -v76\n\t" \
  "v_fma_f32 v73, v73, %[cs], -v76\n\t" \
  "v_fma_f32 v74, v74, %[cs], -v76\n\t" \
  "v_fma_f32 v75, v75, %[cs], -v76\n\t" \
  "v_exp_f32 v64, v64\n\t" \
  "v_exp_f32 v65, v65\n\t" \
  "v_exp_f32 v66, v66\n\t" \
  "v_exp_f32 v67, v67\n\t" \
  "v_exp_f32 v68, v68\n\t" \
  "v_exp_f32 v69, v69\n\t" \
  "v_exp_f32 v70, v70\n\t" \
  "v_exp_f32 v71, v71\n\t" \
  "v_exp_f32 v72, v72\n\t" \
  "v_exp_f32 v73, v73\n\t" \
  "v_exp_f32 v74, v74\n\t" \
  "v_exp_f32 v75, v75\n\t" \
  "v_add_f32 v76, v72, v73\n\t" \
  "v_add_f32 v77, v74, v75\n\t" \
  "v_add_f32 v78, v64, v65\n\t" \
  "v_add_f32 v79, v66, v67\n\t" \
  "v_add_f32 v76, v76, v77\n\t" \
  "v_add_f32 v78, v78, v68\n\t" \
  "v_add_f32 v79, v79, v69\n\t" \
  "v_add_f32 v78, v78, v70\n\t" \
  "v_add_f32 v79, v79, v71\n\t" \
  "v_fma_f32 v76, v76, 0.5, v78\n\t" \
  "v_add_f32 v76, v76, v79\n\t" \
  "v_mov_b32 v77, v76\n\t" \
  "s_nop 1\n\t" \
  "v_permlane16_swap_b32 v76, v77\n\t" \
  "v_add_f32 v76, v76, v77\n\t" \
  "v_mov_b32 v77, v76\n\t" \
  "s_nop 1\n\t" \
  "v_permlane32_swap_b32 v76, v77\n\t" \
  "v_add_f32 v76, v76, v77\n\t" \
  "v_rcp_f32 v76, v76\n\t" \
  "s_mov_b64 exec, 0xff\n\t" \
  "buffer_load_dword v118, v112, %[rh], s96 offen sc1\n\t" \
  "s_mov_b64 exec, -1\n\t" \
  "v_mul_f32 v64, v64, v76\n\t" \
  "v_mul_f32 v65, v65, v76\n\t" \
  "v_mul_f32 v66, v66, v76\n\t" \
  "v_mul_f32 v67, v67, v76\n\t" \
  "v_mul_f32 v68, v68, v76\n\t" \
  "v_mul_f32 v69, v69, v76\n\t" \
  "v_mul_f32 v70, v70, v76\n\t" \
  "v_mul_f32 v71, v71, v76\n\t" \
  "v_mul_f32 v72, v72, v76\n\t" \
  "v_mul_f32 v73, v73, v76\n\t" \
  "v_mul_f32 v74, v74, v76\n\t" \
  "v_mul_f32 v75, v75, v76\n\t" \
  "v_cvt_pk_bf16_f32 v76, v64, v68\n\t" \
  "v_cvt_pk_bf16_f32 v77, v65, v69\n\t" \
  "v_cvt_pk_bf16_f32 v78, v66, v70\n\t" \
  "v_cvt_pk_bf16_f32 v79, v67, v71\n\t" \
  "v_cvt_pk_bf16_f32 v80, v72, v73\n\t" \
  "v_cvt_pk_bf16_f32 v81, v74, v75\n\t" \
  "v_permlane32_swap_b32 v76, v78\n\t" \
  "v_permlane32_swap_b32 v77, v79\n\t" \
  "v_mov_b64 v[82:83], v[80:81]\n\t" \
  "buffer_store_dwordx4 v[76:79], v110, %[rh], s94 offen sc1\n\t" \
  "s_nop 0\n\t" \
  "v_permlane16_swap_b32 v80, v82\n\t" \
  "v_permlane16_swap_b32 v81, v83\n\t" \
  "buffer_store_dwordx4 v[80:83], v111, %[rh], s94 offen sc1\n\t" \
  "s_add_i32 s77, s44, 1\n\t" \
  "s_min_u32 s77, s77, 63\n\t" \
  "v_cmp_gt_i32 vcc, s77, v119\n\t" \
  "s_cmp_lg_u64 vcc, 0\n\t" \
  "s_cselect_b32 s42, 32, 0\n\t" \
  "s_add_i32 s44, s44, 1\n\t" \
  "s_and_b32 s58, s58, 4\n\t" \
  "s_and_b32 s76, s44, 64\n\t" \
  "s_or_b32 s42, s42, s76\n\t" \
  "s_cbranch_scc0 20b\n\t" \
  "s_branch 99f\n\t" \
  "14:\n\t" \
  "s_sleep 3\n\t" \
  "s_add_u32 s59, s59, 0x10000\n\t" \
  "s_cmp_lt_u32 s59, 0x400000\n\t" \
  "s_cbranch_scc1 13b\n\t" \
  "s_mov_b64 exec, -1\n\t" \
  "s_mov_b32 s42, 16\n\t" \
  "s_branch 99f\n\t" \
  "8:\n\t" \
  "s_bitcmp1_b32 s58, 1\n\t" \
  "s_cbranch_scc1 4b\n\t" \
  "s_bitcmp1_b32 s58, 2\n\t" \
  "s_cbranch_scc1 5b\n\t" \
  "v_max3_u32 v116, v32, v33, v34\n\t" \
  "v_max3_u32 v117, v35, v36, v37\n\t" \
  "v_max3_u32 v116, v116, v38, v39\n\t" \
  "v_max_u32 v116, v116, v117\n\t" \
  "v_cmp_eq_u32 vcc, -1, v116\n\t" \
  "s_cbranch_vccz 5b\n\t" \
  "s_branch 0f\n\t" \
  "9:\n\t" \
  "s_bitcmp1_b32 s58, 2\n\t" \
  "s_cbranch_scc1 6b\n\t" \
  "v_max3_u32 v116, v40, v41, v42\n\t" \
  "v_max3_u32 v117, v43, v44, v45\n\t" \
  "v_max3_u32 v116, v116, v46, v47\n\t" \
  "v_max_u32 v116, v116, v117\n\t" \
  "v_cmp_eq_u32 vcc, -1, v116\n\t" \
  "s_cbranch_vccz 6b\n\t" \
  "s_or_b32 s58, s58, 2\n\t" \
  "0:\n\t" \
  "s_or_b32 s58, s58, 1\n\t" \
  "11:\n\t" \
  "buffer_load_dwordx4 v[32:35], v100, %[rh], s40 offen sc1\n\t" \
  "buffer_load_dwordx4 v[36:39], v100, %[rh], s76 offen sc1\n\t" \
  "buffer_load_dwordx4 v[40:43], v100, %[rh], s77 offen sc1\n\t" \
  "buffer_load_dwordx4 v[44:47], v100, %[rh], s78 offen sc1\n\t" \
  "s_waitcnt lgkmcnt(0)\n\t" \
  "s_branch 10b\n\t" \
  "17:\n\t" \
  "s_add_u32 s59, s59, 1\n\t" \
  "s_bitcmp1_b32 s58, 2\n\t" \
  "s_cbranch_scc1 3b\n\t" \
  "s_waitcnt vmcnt(0)\n\t" \
  "v_max_u32 v116, v32, v33\n\t" \
  "v_max3_u32 v116, v116, v34, v35\n\t" \
  "v_max3_u32 v116, v116, v36, v37\n\t" \
  "v_max3_u32 v116, v116, v38, v39\n\t" \
  "v_max3_u32 v116, v116, v40, v41\n\t" \
  "v_max3_u32 v116, v116, v42, v43\n\t" \
  "v_max3_u32 v116, v116, v44, v45\n\t" \
  "v_max3_u32 v116, v116, v46, v47\n\t" \
  "v_cmp_eq_u32 vcc, -1, v116\n\t" \
  "s_cbranch_vccz 3b\n\t" \
  "s_bitcmp1_b32 s59, 6\n\t" \
  "s_cbranch_scc1 12f\n\t" \
  "buffer_load_dwordx4 v[32:35], v100, %[rh], s40 offen sc1\n\t" \
  "buffer_load_dwordx4 v[36:39], v100, %[rh], s76 offen sc1\n\t" \
  "buffer_load_dwordx4 v[40:43], v100, %[rh], s77 offen sc1\n\t" \
  "buffer_load_dwordx4 v[44:47], v100, %[rh], s78 offen sc1\n\t" \
  "s_branch 17b\n\t" \
  "12:\n\t" \
  "s_mov_b32 s42, s58\n\t" \
  "s_branch 99f\n\t" \
  "92:\n\t" \
  "s_mov_b32 s58, s42\n\t" \
  "s_mov_b32 s95, 0\n\t" \
  "20:\n\t" \
  "s_and_b32 s76, s44, 7\n\t" \
  "s_lshl_b32 s76, s76, 15\n\t" \
  "s_add_i32 s40, s54, s76\n\t" \
  "s_and_b32 s76, s44, 3\n\t" \
  "s_lshl_b32 s45, s76, 14\n\t" \
  "s_add_i32 s47, s44, 1\n\t" \
  "s_min_u32 s47, s47, 63\n\t" \
  "s_lshr_b32 s76, s47, 1\n\t" \
  "s_lshl_b32 s47, s47, 20\n\t" \
  "s_add_i32 s47, s47, s55\n\t" \
  "s_bitcmp1_b64 %[sel], s76\n\t" \
  "s_cselect_b32 s76, 0x2000, 0\n\t" \
  "s_add_i32 s47, s47, s76\n\t" \
  "s_lshr_b32 s76, s44, 1\n\t" \
  "s_lshl_b32 s94, s44, 20\n\t" \
  "s_bitcmp1_b64 %[sel], s76\n\t" \
  "s_cselect_b32 s76, 0x2000, 0\n\t" \
  "s_add_i32 s94, s94, s76\n\t" \
  "s_add_i32 s49, s44, -1\n\t" \
  "s_and_b32 s48, s49, 2\n\t" \
  "s_lshl_b32 s48, s48, 13\n\t" \
  "s_and_b32 s76, s49, 1\n\t" \
  "s_lshl_b32 s76, s76, 12\n\t" \
  "s_add_i32 s48, s48, s76\n\t" \
  "s_lshl_b32 s50, s53, 8\n\t" \
  "s_mov_b32 s59, 0\n\t" \
  "s_mov_b32 s92, s51\n\t" \
  "s_mov_b32 s93, s53\n\t" \
  "s_xor_b32 s76, s40, 0x400\n\t" \
  "s_xor_b32 s77, s40, 0x800\n\t" \
  "s_xor_b32 s78, s40, 0xc00\n\t" \
  "s_cmp_lg_u32 s58, 0\n\t" \
  "s_cbranch_scc1 11f\n\t" \
  "s_cmp_lg_u32 s95, 0\n\t" \
  "s_cbranch_scc1 46f\n\t" \
  "10:\n\t" \
  "ds_read_b128 v[48:51], v103 offset:0\n\t" \
  "ds_read_b128 v[52:55], v103 offset:1024\n\t" \
  "ds_read_b128 v[56:59], v103 offset:2048\n\t" \
  "ds_read_b128 v[60:63], v103 offset:3072\n\t" \
  "ds_read_b128 v[16:19], v103 offset:16384\n\t" \
  "ds_read_b128 v[20:23], v103 offset:17408\n\t" \
  "ds_read_b128 v[24:27], v103 offset:18432\n\t" \
  "ds_read_b128 v[28:31], v103 offset:19456\n\t" \
  "ds_read_b128 v[84:87], v103 offset:49152\n\t" \
  "ds_read_b128 v[88:91], v103 offset:50176\n\t" \
  "ds_read_b128 v[92:95], v103 offset:51200\n\t" \
  "ds_read_b128 v[96:99], v103 offset:52224\n\t" \
  "s_waitcnt lgkmcnt(4)\n\t" \
  "ds_read_b128 v[0:3], v102 offset:16384\n\t" \
  "ds_read_b128 v[4:7], v102 offset:17408\n\t" \
  "ds_read_b128 v[8:11], v102 offset:18432\n\t" \
  "ds_read_b128 v[12:15], v102 offset:19456\n\t" \
  "46:\n\t" \
  "s_bitcmp1_b32 s58, 1\n\t" \
  "s_cselect_b64 s[80:81], -1, 0\n\t" \
  "s_cmp_lg_u32 s58, 0\n\t" \
  "s_cbranch_scc1 17f\n\t" \
  "s_mov_b64 exec, 0xff\n\t" \
  "s_cmp_eq_u32 s95, 0\n\t" \
  "s_cbranch_scc1 16f\n\t" \
  "s_waitcnt vmcnt(2)\n\t" \
  "v_mov_b32 v116, v118\n\t" \
  "s_branch 15f\n\t" \
  "16:\n\t" \
  "s_memtime vcc\n\t" \
  "s_waitcnt lgkmcnt(0)\n\t" \
  "s_sub_i32 s79, s41, vcc_lo\n\t" \
  "s_cmp_gt_i32 s79, 64\n\t" \
  "s_cbranch_scc1 16b\n\t" \
  "13:\n\t" \
  "buffer_load_dword v116, v112, %[rh], s48 offen sc1\n\t" \
  "s_waitcnt vmcnt(0)\n\t" \
  "15:\n\t" \
  "v_mov_b32 v117, 20000\n\t" \
  "v_cmp_ne_u32_sdwa vcc, v116, s49 src0_sel:BYTE_0 src1_sel:DWORD\n\t" \
  "v_subrev_u32 v116, s50, v116\n\t" \
  "v_ashrrev_i32 v116, 8, v116\n\t" \
  "s_nop 1\n\t" \
  "v_max_i32_dpp v116, v116, v116 quad_perm:[1,0,3,2] row_mask:0xf bank_mask:0xf\n\t" \
  "s_cbranch_vccnz 14f\n\t" \
  "s_nop 0\n\t" \
  "v_max_i32_dpp v116, v116, v116 quad_perm:[2,3,0,1] row_mask:0xf bank_mask:0xf\n\t" \
  "s_nop 1\n\t" \
  "v_max_i32_dpp v116, v116, v116 row_half_mirror row_mask:0xf bank_mask:0xf\n\t" \
  "v_med3_i32 v116, v116, 0, v117\n\t" \
  "v_mul_u32_u24 v116, s52, v116\n\t" \
  "v_lshrrev_b32 v116, 16, v116\n\t" \
  "s_mov_b64 exec, -1\n\t" \
  "v_readfirstlane_b32 s84, v116\n\t" \
  "s_add_i32 s84, s84, s51\n\t" \
  ".p2align 6\n\t" \
  "s_nop 0\n\t" \
  "s_nop 0\n\t" \
  "s_nop 0\n\t" \
  "s_nop 0\n\t" \
  "s_nop 0\n\t" \
  "s_nop 0\n\t" \
  "s_nop 0\n\t" \
  "s_nop 0\n\t" \
  "1:\n\t" \
  "s_memtime vcc\n\t" \
  "s_waitcnt lgkmcnt(0)\n\t" \
  "s_sub_i32 s79, s84, vcc_lo\n\t" \
  "s_cmp_gt_i32 s79, 128\n\t" \
  "s_cbranch_scc1 1b\n\t" \
  ".p2align 4\n\t" \
  "2:\n\t" \
  "s_sub_i32 s79, s79, 33\n\t" \
  "s_cmp_gt_i32 s79, 56\n\t" \
  "s_cbranch_scc1 2b\n\t" \
  "buffer_load_dwordx4 v[32:35], v100, %[rh], s40 offen sc1\n\t" \
  "s_nop 12\n\t" \
  "buffer_load_dwordx4 v[36:39], v100, %[rh], s76 offen sc1\n\t" \
  "s_nop 12\n\t" \
  "buffer_load_dwordx4 v[40:43], v100, %[rh], s77 offen sc1\n\t" \
  "s_nop 12\n\t" \
  "buffer_load_dwordx4 v[44:47], v100, %[rh], s78 offen sc1\n\t" \
  "s_memtime vcc\n\t" \
  "s_memrealtime s[82:83]\n\t" \
  "s_nop 4\n\t" \
  "s_memrealtime s[84:85]\n\t" \
  "s_nop 4\n\t" \
  "s_memrealtime s[56:57]\n\t" \
  "s_nop 4\n\t" \
  "s_memrealtime s[86:87]\n\t" \
  "s_nop 4\n\t" \
  "s_memrealtime s[88:89]\n\t" \
  "s_nop 4\n\t" \
  "s_memrealtime s[90:91]\n\t" \
  "s_mov_b32 s92, 0\n\t" \
  "s_waitcnt lgkmcnt(0)\n\t" \
  "s_cmp_eq_u32 s82, s84\n\t" \
  "s_addc_u32 s92, s92, 0\n\t" \
  "s_cmp_eq_u32 s82, s56\n\t" \
  "s_addc_u32 s92, s92, 0\n\t" \
  "s_cmp_eq_u32 s82, s86\n\t" \
  "s_addc_u32 s92, s92, 0\n\t" \
  "s_cmp_eq_u32 s82, s88\n\t" \
  "s_addc_u32 s92, s92, 0\n\t" \
  "s_cmp_eq_u32 s82, s90\n\t" \
  "s_addc_u32 s92, s92, 0\n\t" \
  "s_mul_i32 s92, s92, 24\n\t" \
  "s_add_i32 s92, s92, vcc_lo\n\t" \
  "s_lshl4_add_u32 s93, s82, 64\n\t" \
  "3:\n\t" \
  "v_mov_b64 v[68:69], 0\n\t" \
  "v_mov_b64 v[70:71], 0\n\t" \
  "v_mov_b64 v[72:73], 0\n\t" \
  "v_mov_b64 v[74:75], 0\n\t" \
  "v_mov_b64 v[76:77], 0\n\t" \
  "v_mov_b64 v[78:79], 0\n\t" \
  "v_mov_b64 v[80:81], 0\n\t" \
  "v_mov_b64 v[82:83], 0\n\t" \
  "s_waitcnt lgkmcnt(0)\n\t" \
  "s_waitcnt vmcnt(3)\n\t" \
  "v_smfmac_f32_16x16x64_bf16 v[68:71], v[32:35], a[192:199], v104\n\t" \
  "ds_write_b128 v101, v[32:35] offset:8192\n\t" \
  "v_smfmac_f32_16x16x64_bf16 v[72:75], v[32:35], a[200:207], v104\n\t" \
  "v_smfmac_f32_16x16x64_bf16 v[76:79], v[32:35], v[48:55], v104\n\t" \
  "v_smfmac_f32_16x16x64_bf16 v[80:83], v[32:35], v[56:63], v104\n\t" \
  "s_waitcnt vmcnt(2)\n\t" \
  "v_smfmac_f32_16x16x64_bf16 v[68:71], v[36:39], a[208:215], v104\n\t" \
  "ds_write_b128 v101, v[36:39] offset:9216\n\t" \
  "v_smfmac_f32_16x16x64_bf16 v[72:75], v[36:39], a[216:223], v104\n\t" \
  "v_smfmac_f32_16x16x64_bf16 v[76:79], v[36:39], a[224:231], v104\n\t" \
  "v_cmp_u_f32 vcc, v68, v69\n\t" \
  "v_smfmac_f32_16x16x64_bf16 v[80:83], v[36:39], a[232:239], v104\n\t" \
  "v_cmp_u_f32 s[82:83], v70, v71\n\t" \
  "s_waitcnt vmcnt(1)\n\t" \
  "v_smfmac_f32_16x16x64_bf16 v[68:71], v[40:43], v[16:23], v104\n\t" \
  "ds_write_b128 v101, v[40:43] offset:10240\n\t" \
  "v_smfmac_f32_16x16x64_bf16 v[72:75], v[40:43], v[24:31], v104\n\t" \
  "s_or_b64 vcc, vcc, s[82:83]\n\t" \
  "s_or_b64 vcc, vcc, s[80:81]\n\t" \
  "s_waitcnt lgkmcnt(1)\n\t" \
  "v_smfmac_f32_16x16x64_bf16 v[76:79], v[40:43], a[240:247], v104\n\t" \
  "s_cbranch_scc1 8f\n\t" \
  "5:\n\t" \
  "s_barrier\n\t" \
  "4:\n\t" \
  "s_memtime s[56:57]\n\t" \
  "ds_read_b128 v[48:51], v101 offset:12288\n\t" \
  "v_smfmac_f32_16x16x64_bf16 v[80:83], v[40:43], a[248:255], v104\n\t" \
  "ds_read_b128 v[52:55], v101 offset:13312\n\t" \
  "ds_read_b128 v[56:59], v101 offset:0\n\t" \
  "s_waitcnt vmcnt(0)\n\t" \
  "v_smfmac_f32_16x16x64_bf16 v[68:71], v[44:47], v[120:127], v104\n\t" \
  "v_smfmac_f32_16x16x64_bf16 v[72:75], v[44:47], v[128:135], v104\n\t" \
  "ds_write_b128 v101, v[44:47] offset:11264\n\t" \
  "v_smfmac_f32_16x16x64_bf16 v[76:79], v[44:47], v[136:143], v104\n\t" \
  "v_cmp_u_f32 vcc, v68, v69\n\t" \
  "v_smfmac_f32_16x16x64_bf16 v[80:83], v[44:47], v[144:151], v104\n\t" \
  "v_cmp_u_f32 s[82:83], v70, v71\n\t" \
  "s_waitcnt lgkmcnt(3)\n\t" \
  "v_smfmac_f32_16x16x64_bf16 v[68:71], v[48:51], v[152:159], v104\n\t" \
  "v_smfmac_f32_16x16x64_bf16 v[72:75], v[48:51], v[160:167], v104\n\t" \
  "s_or_b64 vcc, vcc, s[82:83]\n\t" \
  "s_waitcnt lgkmcnt(0)\n\t" \
  "v_smfmac_f32_16x16x64_bf16 v[68:71], v[52:55], v[168:175], v104\n\t" \
  "s_cbranch_scc1 9f\n\t" \
  "6:\n\t" \
  "s_barrier\n\t" \
  "ds_read_b128 v[60:63], v101 offset:1024\n\t" \
  "v_smfmac_f32_16x16x64_bf16 v[72:75], v[52:55], v[176:183], v104\n\t" \
  "ds_read_b128 v[64:67], v101 offset:4096\n\t" \
  "v_smfmac_f32_16x16x64_bf16 v[68:71], v[56:59], a[0:7], v104\n\t" \
  "v_smfmac_f32_16x16x64_bf16 v[72:75], v[56:59], a[8:15], v104\n\t" \
  "ds_read_b128 v[48:51], v101 offset:5120\n\t" \
  "s_waitcnt lgkmcnt(2)\n\t" \
  "v_smfmac_f32_16x16x64_bf16 v[68:71], v[60:63], a[16:23], v104\n\t" \
  "ds_read_b128 v[52:55], v101 offset:14336\n\t" \
  "v_smfmac_f32_16x16x64_bf16 v[72:75], v[60:63], a[24:31], v104\n\t" \
  "ds_read_b128 v[56:59], v101 offset:15360\n\t" \
  "s_waitcnt lgkmcnt(3)\n\t" \
  "v_smfmac_f32_16x16x64_bf16 v[68:71], v[64:67], a[96:103], v104\n\t" \
  "ds_read_b128 v[60:63], v101 offset:2048\n\t" \
  "v_smfmac_f32_16x16x64_bf16 v[72:75], v[64:67], a[104:111], v104\n\t" \
  "s_waitcnt lgkmcnt(3)\n\t" \
  "v_smfmac_f32_16x16x64_bf16 v[68:71], v[48:51], a[112:119], v104\n\t" \
  "ds_read_b128 v[64:67], v101 offset:3072\n\t" \
  "ds_read_b128 v[16:19], v102 offset:49152\n\t" \
  "v_smfmac_f32_16x16x64_bf16 v[72:75], v[48:51], a[120:127], v104\n\t" \
  "s_waitcnt lgkmcnt(4)\n\t" \
  "v_smfmac_f32_16x16x64_bf16 v[68:71], v[52:55], v[84:91], v104\n\t" \
  "ds_read_b128 v[48:51], v101 offset:7168\n\t" \
  "ds_read_b128 v[20:23], v102 offset:50176\n\t" \
  "v_smfmac_f32_16x16x64_bf16 v[72:75], v[52:55], v[92:99], v104\n\t" \
  "s_waitcnt lgkmcnt(5)\n\t" \
  "v_smfmac_f32_16x16x64_bf16 v[68:71], v[56:59], v[216:223], v104\n\t" \
  "ds_read_b128 v[52:55], v101 offset:6144\n\t" \
  "ds_read_b128 v[24:27], v102 offset:51200\n\t" \
  "v_smfmac_f32_16x16x64_bf16 v[72:75], v[56:59], v[224:231], v104\n\t" \
  "ds_read_b128 v[28:31], v102 offset:52224\n\t" \
  "s_waitcnt lgkmcnt(7)\n\t" \
  "v_smfmac_f32_16x16x64_bf16 v[68:71], v[60:63], v[0:7], v104\n\t" \
  "v_smfmac_f32_16x16x64_bf16 v[72:75], v[60:63], v[8:15], v104\n\t" \
  "buffer_load_dwordx4 v[84:87], v107, %[ru], s94 offen offset:0 sc1\n\t" \
  "s_waitcnt lgkmcnt(6)\n\t" \
  "v_smfmac_f32_16x16x64_bf16 v[68:71], v[64:67], a[64:71], v104\n\t" \
  "ds_read_b128 v[0:3], v103 offset:32768\n\t" \
  "v_smfmac_f32_16x16x64_bf16 v[72:75], v[64:67], a[72:79], v104\n\t" \
  "ds_read_b128 v[4:7], v103 offset:33792\n\t" \
  "s_waitcnt lgkmcnt(6)\n\t" \
  "v_smfmac_f32_16x16x64_bf16 v[68:71], v[48:51], a[160:167], v104\n\t" \
  "buffer_load_dwordx4 v[88:91], v107, %[ru], s94 offen offset:1024 sc1\n\t" \
  "s_waitcnt lgkmcnt(4)\n\t" \
  "v_smfmac_f32_16x16x64_bf16 v[68:71], v[52:55], v[16:23], v104\n\t" \
  "ds_read_b128 v[8:11], v103 offset:34816\n\t" \
  "v_smfmac_f32_16x16x64_bf16 v[72:75], v[48:51], a[168:175], v104\n\t" \
  "ds_read_b128 v[12:15], v103 offset:35840\n\t" \
  "s_waitcnt lgkmcnt(4)\n\t" \
  "v_smfmac_f32_16x16x64_bf16 v[72:75], v[52:55], v[24:31], v104\n\t" \
  "v_add_f32 v44, v68, v70\n\t" \
  "v_smfmac_f32_16x16x64_bf16 v[76:79], v[56:59], v[232:239], v104\n\t" \
  "v_add_f32 v45, v69, v71\n\t" \
  "v_cvt_pk_bf16_f32 v44, v44, v45\n\t" \
  "v_smfmac_f32_16x16x64_bf16 v[80:83], v[56:59], v[240:247], v104\n\t" \
  "v_add_f32 v46, v72, v74\n\t" \
  "v_add_f32 v47, v73, v75\n\t" \
  "v_smfmac_f32_16x16x64_bf16 v[76:79], v[60:63], a[48:55], v104\n\t" \
  "v_cvt_pk_bf16_f32 v46, v46, v47\n\t" \
  "ds_write2st64_b32 v105, v44, v46 offset1:1\n\t" \
  "v_smfmac_f32_16x16x64_bf16 v[80:83], v[60:63], a[56:63], v104\n\t" \
  "ds_read_b128 v[56:59], v101 offset:12288\n\t" \
  "v_smfmac_f32_16x16x64_bf16 v[76:79], v[64:67], a[80:87], v104\n\t" \
  "ds_read_b128 v[16:19], v102 offset:0\n\t" \
  "v_smfmac_f32_16x16x64_bf16 v[80:83], v[64:67], a[88:95], v104\n\t" \
  "ds_read_b128 v[20:23], v102 offset:1024\n\t" \
  "v_smfmac_f32_16x16x64_bf16 v[76:79], v[48:51], a[176:183], v104\n\t" \
  "s_waitcnt lgkmcnt(3)\n\t" \
  "s_barrier\n\t" \
  "v_smfmac_f32_16x16x64_bf16 v[80:83], v[48:51], a[184:191], v104\n\t" \
  "buffer_load_dwordx4 v[92:95], v107, %[ru], s94 offen offset:2048 sc1\n\t" \
  "v_smfmac_f32_16x16x64_bf16 v[76:79], v[52:55], a[144:151], v104\n\t" \
  "ds_read_b128 v[60:63], v101 offset:5120\n\t" \
  "ds_read_b128 v[24:27], v102 offset:2048\n\t" \
  "v_smfmac_f32_16x16x64_bf16 v[80:83], v[52:55], a[152:159], v104\n\t" \
  "ds_read_b128 v[64:67], v101 offset:14336\n\t" \
  "buffer_load_dwordx4 v[96:99], v107, %[ru], s94 offen offset:3072 sc1\n\t" \
  "s_waitcnt lgkmcnt(5)\n\t" \
  "v_smfmac_f32_16x16x64_bf16 v[76:79], v[56:59], v[0:7], v104\n\t" \
  "ds_read_b128 v[48:51], v101 offset:0\n\t" \
  "ds_read_b128 v[28:31], v102 offset:3072\n\t" \
  "v_smfmac_f32_16x16x64_bf16 v[80:83], v[56:59], v[8:15], v104\n\t" \
  "global_load_dword v119, v109, %[kc] sc1\n\t" \
  "s_waitcnt lgkmcnt(4)\n\t" \
  "v_smfmac_f32_16x16x64_bf16 v[76:79], v[60:63], a[128:135], v104\n\t" \
  "ds_read_b128 v[52:55], v101 offset:13312\n\t" \
  "v_smfmac_f32_16x16x64_bf16 v[80:83], v[60:63], a[136:143], v104\n\t" \
  "ds_read_b128 v[0:3], v102 offset:32768\n\t" \
  "buffer_load_dwordx4 v[32:35], v100, %[ru], s47 offen offset:0 sc1\n\t" \
  "s_waitcnt lgkmcnt(4)\n\t" \
  "v_smfmac_f32_16x16x64_bf16 v[76:79], v[64:67], v[200:207], v104\n\t" \
  "ds_read_b128 v[56:59], v101 offset:1024\n\t" \
  "v_smfmac_f32_16x16x64_bf16 v[80:83], v[64:67], v[208:215], v104\n\t" \
  "ds_read_b128 v[60:63], v101 offset:4096\n\t" \
  "ds_read_b128 v[4:7], v102 offset:33792\n\t" \
  "s_waitcnt lgkmcnt(6)\n\t" \
  "v_smfmac_f32_16x16x64_bf16 v[76:79], v[48:51], v[16:23], v104\n\t" \
  "ds_read_b128 v[8:11], v102 offset:34816\n\t" \
  "buffer_load_dwordx4 v[36:39], v100, %[ru], s47 offen offset:1024 sc1\n\t" \
  "s_waitcnt lgkmcnt(6)\n\t" \
  "v_smfmac_f32_16x16x64_bf16 v[80:83], v[48:51], v[24:31], v104\n\t" \
  "ds_read_b128 v[12:15], v102 offset:35840\n\t" \
  "s_waitcnt lgkmcnt(6)\n\t" \
  "v_smfmac_f32_16x16x64_bf16 v[76:79], v[52:55], v[184:191], v104\n\t" \
  "v_smfmac_f32_16x16x64_bf16 v[80:83], v[52:55], v[192:199], v104\n\t" \
  "buffer_load_dwordx4 v[40:43], v100, %[ru], s47 offen offset:2048 sc1\n\t" \
  "s_waitcnt lgkmcnt(4)\n\t" \
  "v_smfmac_f32_16x16x64_bf16 v[76:79], v[56:59], a[32:39], v104\n\t" \
  "s_waitcnt lgkmcnt(2)\n\t" \
  "v_smfmac_f32_16x16x64_bf16 v[76:79], v[60:63], v[0:7], v104\n\t" \
  "v_smfmac_f32_16x16x64_bf16 v[80:83], v[56:59], a[40:47], v104\n\t" \
  "s_waitcnt lgkmcnt(0)\n\t" \
  "v_smfmac_f32_16x16x64_bf16 v[80:83], v[60:63], v[8:15], v104\n\t" \
  "v_add_f32 v20, v76, v78\n\t" \
  "v_add_f32 v21, v77, v79\n\t" \
  "v_cvt_pk_bf16_f32 v20, v20, v21\n\t" \
  "ds_write_b32 v106, v20\n\t" \
  "s_nop 3\n\t" \
  "v_add_f32 v23, v80, v82\n\t" \
  "v_add_f32 v21, v81, v83\n\t" \
  "v_cvt_pk_bf16_f32 v21, v23, v21\n\t" \
  "ds_write_b32 v106, v21 offset:256\n\t" \
  "s_waitcnt lgkmcnt(0)\n\t" \
  "s_barrier\n\t" \
  "v_bfe_i32 v64, v114, 0, 1\n\t" \
  "v_and_b32 v64, 0xff800000, v64\n\t" \
  "v_bfe_i32 v65, v114, 1, 1\n\t" \
  "v_and_b32 v65, 0xff800000, v65\n\t" \
  "v_bfe_i32 v66, v114, 2, 1\n\t" \
  "v_and_b32 v66, 0xff800000, v66\n\t" \
  "v_bfe_i32 v67, v114, 3, 1\n\t" \
  "v_and_b32 v67, 0xff800000, v67\n\t" \
  "v_bfe_i32 v68, v114, 4, 1\n\t" \
  "v_and_b32 v68, 0xff800000, v68\n\t" \
  "v_bfe_i32 v69, v114, 5, 1\n\t" \
  "v_and_b32 v69, 0xff800000, v69\n\t" \
  "v_bfe_i32 v70, v114, 6, 1\n\t" \
  "v_and_b32 v70, 0xff800000, v70\n\t" \
  "v_bfe_i32 v71, v114, 7, 1\n\t" \
  "v_and_b32 v71, 0xff800000, v71\n\t" \
  "s_lshl_b32 s94, s45, 4\n\t" \
  "s_and_b32 s96, s44, 2\n\t" \
  "s_lshl_b32 s96, s96, 13\n\t" \
  "s_and_b32 s76, s44, 1\n\t" \
  "s_lshl_b32 s76, s76, 12\n\t" \
  "s_add_i32 s96, s96, s76\n\t" \
  "s_mov_b32 s95, 1\n\t" \
  "s_add_i32 s41, s56, 1950\n\t" \
  "s_sub_i32 s76, s92, s51\n\t" \
  "s_sub_i32 s77, s93, s53\n\t" \
  "s_cmp_eq_u32 s76, 0\n\t" \
  "s_cbranch_scc1 21f\n\t" \
  "v_cvt_f32_u32 v117, s76\n\t" \
  "v_cvt_f32_u32 v116, s77\n\t" \
  "v_rcp_f32 v117, v117\n\t" \
  "s_mov_b32 s51, s92\n\t" \
  "v_mul_f32 v117, v117, v116\n\t" \
  "v_max_f32 v117, 0x3f028f5c, v117\n\t" \
  "v_min_f32 v117, 0x3ffeb852, v117\n\t" \
  "v_rcp_f32 v116, v117\n\t" \
  "v_mul_f32 v117, 0x4f000000, v117\n\t" \
  "v_mul_f32 v116, 0x47800000, v116\n\t" \
  "v_cvt_u32_f32 v117, v117\n\t" \
  "v_cvt_u32_f32 v116, v116\n\t" \
  "s_mov_b32 s53, s93\n\t" \
  "v_readfirstlane_b32 s46, v117\n\t" \
  "v_readfirstlane_b32 s52, v116\n\t" \
  "21:\n\t" \
  "ds_read_b128 v[0:3], v101 offset:20480\n\t" \
  "ds_read_b128 v[4:7], v101 offset:21504\n\t" \
  "ds_read_b128 v[8:11], v101 offset:22528\n\t" \
  "ds_read_b128 v[12:15], v101 offset:23552\n\t" \
  "ds_read_b128 v[16:19], v101 offset:24576\n\t" \
  "ds_read_b128 v[20:23], v101 offset:25600\n\t" \
  "ds_read_b128 v[24:27], v101 offset:26624\n\t" \
  "ds_read_b128 v[28:31], v101 offset:27648\n\t" \
  "ds_read_b64_tr_b16 v[48:49], v108 offset:0\n\t" \
  "ds_read_b64_tr_b16 v[50:51], v108 offset:64\n\t" \
  "ds_read_b64_tr_b16 v[52:53], v108 offset:512\n\t" \
  "ds_read_b64_tr_b16 v[54:55], v108 offset:576\n\t" \
  "ds_read_b64_tr_b16 v[56:57], v108 offset:1024\n\t" \
  "ds_read_b64_tr_b16 v[58:59], v108 offset:1088\n\t" \
  "ds_read_b64_tr_b16 v[60:61], v108 offset:1536\n\t" \
  "ds_read_b64_tr_b16 v[62:63], v108 offset:1600\n\t" \
  "ds_read_b64_tr_b16 v[76:77], v108 offset:12288\n\t" \
  "ds_read_b64_tr_b16 v[78:79], v108 offset:12352\n\t" \
  "s_waitcnt vmcnt(4) lgkmcnt(10)\n\t" \
  "v_mfma_f32_16x16x32_bf16 v[64:67], v[0:3], v[84:87], v[64:67]\n\t" \
  "v_mfma_f32_16x16x32_bf16 v[68:71], v[16:19], v[84:87], v[68:71]\n\t" \
  "v_mfma_f32_16x16x32_bf16 v[64:67], v[4:7], v[88:91], v[64:67]\n\t" \
  "v_mfma_f32_16x16x32_bf16 v[68:71], v[20:23], v[88:91], v[68:71]\n\t" \
  "v_mfma_f32_16x16x32_bf16 v[64:67], v[8:11], v[92:95], v[64:67]\n\t" \
  "v_mfma_f32_16x16x32_bf16 v[68:71], v[24:27], v[92:95], v[68:71]\n\t" \
  "v_mfma_f32_16x16x32_bf16 v[64:67], v[12:15], v[96:99], v[64:67]\n\t" \
  "v_mfma_f32_16x16x32_bf16 v[68:71], v[28:31], v[96:99], v[68:71]\n\t" \
  "s_waitcnt lgkmcnt(0)\n\t" \
  "s_barrier\n\t" \
  "v_mfma_f32_16x16x32_bf16 v[72:75], v[48:51], v[84:87], 0\n\t" \
  "v_mfma_f32_16x16x32_bf16 v[72:75], v[52:55], v[88:91], v[72:75]\n\t" \
  "v_mfma_f32_16x16x32_bf16 v[72:75], v[56:59], v[92:95], v[72:75]\n\t" \
  "v_mfma_f32_16x16x32_bf16 v[72:75], v[60:63], v[96:99], v[72:75]\n\t" \
  "v_mfma_f32_16x16x32_bf16 v[72:75], v[76:79], v[84:87], v[72:75]\n\t" \
  "s_waitcnt vmcnt(0)\n\t" \
  "ds_write_b128 v101, v[32:35] offset:22528\n\t" \
  "ds_write_b128 v101, v[36:39] offset:23552\n\t" \
  "ds_write_b128 v101, v[40:43] offset:24576\n\t" \
  "ds_read_b128 v[48:51], v103 offset:0\n\t" \
  "ds_read_b128 v[52:55], v103 offset:1024\n\t" \
  "ds_read_b128 v[56:59], v103 offset:2048\n\t" \
  "ds_read_b128 v[60:63], v103 offset:3072\n\t" \
  "ds_read_b128 v[16:19], v103 offset:16384\n\t" \
  "ds_read_b128 v[20:23], v103 offset:17408\n\t" \
  "ds_read_b128 v[24:27], v103 offset:18432\n\t" \
  "ds_read_b128 v[28:31], v103 offset:19456\n\t" \
  "ds_read_b128 v[84:87], v103 offset:49152\n\t" \
  "ds_read_b128 v[88:91], v103 offset:50176\n\t" \
  "ds_read_b128 v[92:95], v103 offset:51200\n\t" \
  "ds_read_b128 v[96:99], v103 offset:52224\n\t" \
  "ds_read_b128 v[0:3], v102 offset:16384\n\t" \
  "ds_read_b128 v[4:7], v102 offset:17408\n\t" \
  "ds_read_b128 v[8:11], v102 offset:18432\n\t" \
  "ds_read_b128 v[12:15], v102 offset:19456\n\t" \
  "v_max3_f32 v76, v64, v65, v66\n\t" \
  "v_max3_f32 v77, v67, v68, v69\n\t" \
  "v_max3_f32 v76, v76, v70, v71\n\t" \
  "v_max3_f32 v77, v77, v72, v73\n\t" \
  "v_max3_f32 v76, v76, v74, v75\n\t" \
  "v_max_f32 v76, v76, v77\n\t" \
  "v_mov_b32 v77, v76\n\t" \
  "s_nop 1\n\t" \
  "v_permlane16_swap_b32 v76, v77\n\t" \
  "v_max_f32 v76, v76, v77\n\t" \
  "v_mov_b32 v77, v76\n\t" \
  "s_nop 1\n\t" \
  "v_permlane32_swap_b32 v76, v77\n\t" \
  "v_max_f32 v76, v76, v77\n\t" \
  "v_mul_f32 v76, %[cs], v76\n\t" \
  "v_fma_f32 v64, v64, %[cs], -v76\n\t" \
  "v_fma_f32 v65, v65, %[cs], -v76\n\t" \
  "v_fma_f32 v66, v66, %[cs], -v76\n\t" \
  "v_fma_f32 v67, v67, %[cs], -v76\n\t" \
  "v_fma_f32 v68, v68, %[cs], -v76\n\t" \
  "v_fma_f32 v69, v69, %[cs], -v76\n\t" \
  "v_fma_f32 v70, v70, %[cs], -v76\n\t" \
  "v_fma_f32 v71, v71, %[cs], -v76\n\t" \
  "v_fma_f32 v72, v72, %[cs], -v76\n\t" \
  "v_fma_f32 v73, v73, %[cs], -v76\n\t" \
  "v_fma_f32 v74, v74, %[cs], -v76\n\t" \
  "v_fma_f32 v75, v75, %[cs], -v76\n\t" \
  "v_exp_f32 v64, v64\n\t" \
  "v_exp_f32 v65, v65\n\t" \
  "v_exp_f32 v66, v66\n\t" \
  "v_exp_f32 v67, v67\n\t" \
  "v_exp_f32 v68, v68\n\t" \
  "v_exp_f32 v69, v69\n\t" \
  "v_exp_f32 v70, v70\n\t" \
  "v_exp_f32 v71, v71\n\t" \
  "v_exp_f32 v72, v72\n\t" \
  "v_exp_f32 v73, v73\n\t" \
  "v_exp_f32 v74, v74\n\t" \
  "v_exp_f32 v75, v75\n\t" \
  "v_add_f32 v76, v72, v73\n\t" \
  "v_add_f32 v77, v74, v75\n\t" \
  "v_add_f32 v78, v64, v65\n\t" \
  "v_add_f32 v79, v66, v67\n\t" \
  "v_add_f32 v76, v76, v77\n\t" \
  "v_add_f32 v78, v78, v68\n\t" \
  "v_add_f32 v79, v79, v69\n\t" \
  "v_add_f32 v78, v78, v70\n\t" \
  "v_add_f32 v79, v79, v71\n\t" \
  "v_fma_f32 v76, v76, 0.5, v78\n\t" \
  "v_add_f32 v76, v76, v79\n\t" \
  "v_mov_b32 v77, v76\n\t" \
  "s_nop 1\n\t" \
  "v_permlane16_swap_b32 v76, v77\n\t" \
  "v_add_f32 v76, v76, v77\n\t" \
  "v_mov_b32 v77, v76\n\t" \
  "s_nop 1\n\t" \
  "v_permlane32_swap_b32 v76, v77\n\t" \
  "v_add_f32 v76, v76, v77\n\t" \
  "v_rcp_f32 v76, v76\n\t" \
  "s_mov_b64 exec, 0xff\n\t" \
  "buffer_load_dword v118, v112, %[rh], s96 offen sc1\n\t" \
  "s_mov_b64 exec, -1\n\t" \
  "v_mul_f32 v64, v64, v76\n\t" \
  "v_mul_f32 v65, v65, v76\n\t" \
  "v_mul_f32 v66, v66, v76\n\t" \
  "v_mul_f32 v67, v67, v76\n\t" \
  "v_mul_f32 v68, v68, v76\n\t" \
  "v_mul_f32 v69, v69, v76\n\t" \
  "v_mul_f32 v70, v70, v76\n\t" \
  "v_mul_f32 v71, v71, v76\n\t" \
  "v_mul_f32 v72, v72, v76\n\t" \
  "v_mul_f32 v73, v73, v76\n\t" \
  "v_mul_f32 v74, v74, v76\n\t" \
  "v_mul_f32 v75, v75, v76\n\t" \
  "v_cvt_pk_bf16_f32 v76, v64, v68\n\t" \
  "v_cvt_pk_bf16_f32 v77, v65, v69\n\t" \
  "v_cvt_pk_bf16_f32 v78, v66, v70\n\t" \
  "v_cvt_pk_bf16_f32 v79, v67, v71\n\t" \
  "v_cvt_pk_bf16_f32 v80, v72, v73\n\t" \
  "v_cvt_pk_bf16_f32 v81, v74, v75\n\t" \
  "v_permlane32_swap_b32 v76, v78\n\t" \
  "v_permlane32_swap_b32 v77, v79\n\t" \
  "v_mov_b64 v[82:83], v[80:81]\n\t" \
  "buffer_store_dwordx4 v[76:79], v110, %[rh], s94 offen sc1\n\t" \
  "s_nop 0\n\t" \
  "v_permlane16_swap_b32 v80, v82\n\t" \
  "v_permlane16_swap_b32 v81, v83\n\t" \
  "buffer_store_dwordx4 v[80:83], v111, %[rh], s94 offen sc1\n\t" \
  "s_add_i32 s77, s44, 1\n\t" \
  "s_min_u32 s77, s77, 63\n\t" \
  "v_cmp_gt_i32 vcc, s77, v119\n\t" \
  "s_cmp_lg_u64 vcc, 0\n\t" \
  "s_cselect_b32 s42, 32, 0\n\t" \
  "s_add_i32 s44, s44, 1\n\t" \
  "s_and_b32 s58, s58, 4\n\t" \
  "s_and_b32 s76, s44, 64\n\t" \
  "s_or_b32 s42, s42, s76\n\t" \
  "s_cbranch_scc0 20b\n\t" \
  "s_branch 99f\n\t" \
  "14:\n\t" \
  "s_sleep 3\n\t" \
  "s_add_u32 s59, s59, 0x10000\n\t" \
  "s_cmp_lt_u32 s59, 0x400000\n\t" \
  "s_cbranch_scc1 13b\n\t" \
  "s_mov_b64 exec, -1\n\t" \
  "s_mov_b32 s42, 16\n\t" \
  "s_branch 99f\n\t" \
  "8:\n\t" \
  "s_bitcmp1_b32 s58, 1\n\t" \
  "s_cbranch_scc1 4b\n\t" \
  "s_bitcmp1_b32 s58, 2\n\t" \
  "s_cbranch_scc1 5b\n\t" \
  "v_max3_u32 v116, v32, v33, v34\n\t" \
  "v_max3_u32 v117, v35, v36, v37\n\t" \
  "v_max3_u32 v116, v116, v38, v39\n\t" \
  "v_max_u32 v116, v116, v117\n\t" \
  "v_cmp_eq_u32 vcc, -1, v116\n\t" \
  "s_cbranch_vccz 5b\n\t" \
  "s_branch 0f\n\t" \
  "9:\n\t" \
  "s_bitcmp1_b32 s58, 2\n\t" \
  "s_cbranch_scc1 6b\n\t" \
  "v_max3_u32 v116, v40, v41, v42\n\t" \
  "v_max3_u32 v117, v43, v44, v45\n\t" \
  "v_max3_u32 v116, v116, v46, v47\n\t" \
  "v_max_u32 v116, v116, v117\n\t" \
  "v_cmp_eq_u32 vcc, -1, v116\n\t" \
  "s_cbranch_vccz 6b\n\t" \
  "s_or_b32 s58, s58, 2\n\t" \
  "0:\n\t" \
  "s_or_b32 s58, s58, 1\n\t" \
  "11:\n\t" \
  "buffer_load_dwordx4 v[32:35], v100, %[rh], s40 offen sc1\n\t" \
  "buffer_load_dwordx4 v[36:39], v100, %[rh], s76 offen sc1\n\t" \
  "buffer_load_dwordx4 v[40:43], v100, %[rh], s77 offen sc1\n\t" \
  "buffer_load_dwordx4 v[44:47], v100, %[rh], s78 offen sc1\n\t" \
  "s_waitcnt lgkmcnt(0)\n\t" \
  "s_branch 10b\n\t" \
  "17:\n\t" \
  "s_add_u32 s59, s59, 1\n\t" \
  "s_bitcmp1_b32 s58, 2\n\t" \
  "s_cbranch_scc1 3b\n\t" \
  "s_waitcnt vmcnt(0)\n\t" \
  "v_max_u32 v116, v32, v33\n\t" \
  "v_max3_u32 v116, v116, v34, v35\n\t" \
  "v_max3_u32 v116, v116, v36, v37\n\t" \
  "v_max3_u32 v116, v116, v38, v39\n\t" \
  "v_max3_u32 v116, v116, v40, v41\n\t" \
  "v_max3_u32 v116, v116, v42, v43\n\t" \
  "v_max3_u32 v116, v116, v44, v45\n\t" \
  "v_max3_u32 v116, v116, v46, v47\n\t" \
  "v_cmp_eq_u32 vcc, -1, v116\n\t" \
  "s_cbranch_vccz 3b\n\t" \
  "s_bitcmp1_b32 s59, 6\n\t" \
  "s_cbranch_scc1 12f\n\t" \
  "buffer_load_dwordx4 v[32:35], v100, %[rh], s40 offen sc1\n\t" \
  "buffer_load_dwordx4 v[36:39], v100, %[rh], s76 offen sc1\n\t" \
  "buffer_load_dwordx4 v[40:43], v100, %[rh], s77 offen sc1\n\t" \
  "buffer_load_dwordx4 v[44:47], v100, %[rh], s78 offen sc1\n\t" \
  "s_branch 17b\n\t" \
  "12:\n\t" \
  "s_mov_b32 s42, s58\n\t" \
  "s_branch 99f\n\t" \
  "93:\n\t" \
  "s_mov_b32 s58, s42\n\t" \
  "20:\n\t" \
  "s_and_b32 s76, s44, 7\n\t" \
  "s_lshl_b32 s76, s76, 15\n\t" \
  "s_add_i32 s40, s54, s76\n\t" \
  "s_and_b32 s76, s44, 3\n\t" \
  "s_lshl_b32 s45, s76, 14\n\t" \
  "s_add_i32 s47, s44, 1\n\t" \
  "s_min_u32 s47, s47, 63\n\t" \
  "s_lshr_b32 s76, s47, 1\n\t" \
  "s_lshl_b32 s47, s47, 20\n\t" \
  "s_add_i32 s47, s47, s55\n\t" \
  "s_bitcmp1_b64 %[sel], s76\n\t" \
  "s_cselect_b32 s76, 0x2000, 0\n\t" \
  "s_add_i32 s47, s47, s76\n\t" \
  "s_add_i32 s49, s44, -1\n\t" \
  "s_and_b32 s48, s49, 2\n\t" \
  "s_lshl_b32 s48, s48, 13\n\t" \
  "s_and_b32 s76, s49, 1\n\t" \
  "s_lshl_b32 s76, s76, 12\n\t" \
  "s_add_i32 s48, s48, s76\n\t" \
  "s_lshl_b32 s50, s53, 8\n\t" \
  "s_mov_b32 s59, 0\n\t" \
  "s_mov_b32 s92, s51\n\t" \
  "s_mov_b32 s93, s53\n\t" \
  "s_xor_b32 s76, s40, 0x400\n\t" \
  "s_xor_b32 s77, s40, 0x800\n\t" \
  "s_xor_b32 s78, s40, 0xc00\n\t" \
  "s_cmp_lg_u32 s58, 0\n\t" \
  "s_cbranch_scc1 11f\n\t" \
  "10:\n\t" \
  "ds_read_b128 v[48:51], v103 offset:32768\n\t" \
  "ds_read_b128 v[52:55], v103 offset:33792\n\t" \
  "ds_read_b128 v[56:59], v103 offset:34816\n\t" \
  "ds_read_b128 v[60:63], v103 offset:35840\n\t" \
  "ds_read_b128 v[16:19], v103 offset:49152\n\t" \
  "ds_read_b128 v[20:23], v103 offset:50176\n\t" \
  "ds_read_b128 v[24:27], v103 offset:51200\n\t" \
  "ds_read_b128 v[28:31], v103 offset:52224\n\t" \
  "ds_read_b128 v[84:87], v102 offset:16384\n\t" \
  "ds_read_b128 v[88:91], v102 offset:17408\n\t" \
  "ds_read_b128 v[92:95], v102 offset:18432\n\t" \
  "ds_read_b128 v[96:99], v102 offset:19456\n\t" \
  "s_waitcnt lgkmcnt(4)\n\t" \
  "ds_read_b128 v[0:3], v102 offset:49152\n\t" \
  "ds_read_b128 v[4:7], v102 offset:50176\n\t" \
  "ds_read_b128 v[8:11], v102 offset:51200\n\t" \
  "ds_read_b128 v[12:15], v102 offset:52224\n\t" \
  "46:\n\t" \
  "s_bitcmp1_b32 s58, 1\n\t" \
  "s_cselect_b64 s[80:81], -1, 0\n\t" \
  "s_cmp_lg_u32 s58, 0\n\t" \
  "s_cbranch_scc1 17f\n\t" \
  "s_mov_b64 exec, 0xff\n\t" \
  "16:\n\t" \
  "s_memtime vcc\n\t" \
  "s_waitcnt lgkmcnt(0)\n\t" \
  "s_sub_i32 s79, s41, vcc_lo\n\t" \
  "s_cmp_gt_i32 s79, 64\n\t" \
  "s_cbranch_scc1 16b\n\t" \
  "13:\n\t" \
  "buffer_load_dword v116, v112, %[rh], s48 offen sc1\n\t" \
  "s_waitcnt vmcnt(0)\n\t" \
  "15:\n\t" \
  "v_mov_b32 v117, 20000\n\t" \
  "v_cmp_ne_u32_sdwa vcc, v116, s49 src0_sel:BYTE_0 src1_sel:DWORD\n\t" \
  "v_subrev_u32 v116, s50, v116\n\t" \
  "v_ashrrev_i32 v116, 8, v116\n\t" \
  "s_nop 1\n\t" \
  "v_max_i32_dpp v116, v116, v116 quad_perm:[1,0,3,2] row_mask:0xf bank_mask:0xf\n\t" \
  "s_cbranch_vccnz 14f\n\t" \
  "s_nop 0\n\t" \
  "v_max_i32_dpp v116, v116, v116 quad_perm:[2,3,0,1] row_mask:0xf bank_mask:0xf\n\t" \
  "s_nop 1\n\t" \
  "v_max_i32_dpp v116, v116, v116 row_half_mirror row_mask:0xf bank_mask:0xf\n\t" \
  "v_med3_i32 v116, v116, 0, v117\n\t" \
  "v_mul_u32_u24 v116, s52, v116\n\t" \
  "v_lshrrev_b32 v116, 16, v116\n\t" \
  "s_mov_b64 exec, -1\n\t" \
  "v_readfirstlane_b32 s84, v116\n\t" \
  "s_add_i32 s84, s84, s51\n\t" \
  ".p2align 6\n\t" \
  "s_nop 0\n\t" \
  "s_nop 0\n\t" \
  "s_nop 0\n\t" \
  "s_nop 0\n\t" \
  "s_nop 0\n\t" \
  "s_nop 0\n\t" \
  "s_nop 0\n\t" \
  "s_nop 0\n\t" \
  "1:\n\t" \
  "s_memtime vcc\n\t" \
  "s_waitcnt lgkmcnt(0)\n\t" \
  "s_sub_i32 s79, s84, vcc_lo\n\t" \
  "s_cmp_gt_i32 s79, 128\n\t" \
  "s_cbranch_scc1 1b\n\t" \
  ".p2align 4\n\t" \
  "2:\n\t" \
  "s_sub_i32 s79, s79, 33\n\t" \
  "s_cmp_gt_i32 s79, 56\n\t" \
  "s_cbranch_scc1 2b\n\t" \
  "buffer_load_dwordx4 v[32:35], v100, %[rh], s40 offen sc1\n\t" \
  "s_nop 12\n\t" \
  "buffer_load_dwordx4 v[36:39], v100, %[rh], s76 offen sc1\n\t" \
  "s_nop 12\n\t" \
  "buffer_load_dwordx4 v[40:43], v100, %[rh], s77 offen sc1\n\t" \
  "s_nop 12\n\t" \
  "buffer_load_dwordx4 v[44:47], v100, %[rh], s78 offen sc1\n\t" \
  "s_memtime vcc\n\t" \
  "s_memrealtime s[82:83]\n\t" \
  "s_nop 4\n\t" \
  "s_memrealtime s[84:85]\n\t" \
  "s_nop 4\n\t" \
  "s_memrealtime s[56:57]\n\t" \
  "s_nop 4\n\t" \
  "s_memrealtime s[86:87]\n\t" \
  "s_nop 4\n\t" \
  "s_memrealtime s[88:89]\n\t" \
  "s_nop 4\n\t" \
  "s_memrealtime s[90:91]\n\t" \
  "s_mov_b32 s92, 0\n\t" \
  "s_waitcnt lgkmcnt(0)\n\t" \
  "s_cmp_eq_u32 s82, s84\n\t" \
  "s_addc_u32 s92, s92, 0\n\t" \
  "s_cmp_eq_u32 s82, s56\n\t" \
  "s_addc_u32 s92, s92, 0\n\t" \
  "s_cmp_eq_u32 s82, s86\n\t" \
  "s_addc_u32 s92, s92, 0\n\t" \
  "s_cmp_eq_u32 s82, s88\n\t" \
  "s_addc_u32 s92, s92, 0\n\t" \
  "s_cmp_eq_u32 s82, s90\n\t" \
  "s_addc_u32 s92, s92, 0\n\t" \
  "s_mul_i32 s92, s92, 24\n\t" \
  "s_add_i32 s92, s92, vcc_lo\n\t" \
  "s_lshl4_add_u32 s93, s82, 64\n\t" \
  "3:\n\t" \
  "v_mov_b64 v[68:69], 0\n\t" \
  "v_mov_b64 v[70:71], 0\n\t" \
  "v_mov_b64 v[72:73], 0\n\t" \
  "v_mov_b64 v[74:75], 0\n\t" \
  "v_mov_b64 v[76:77], 0\n\t" \
  "v_mov_b64 v[78:79], 0\n\t" \
  "v_mov_b64 v[80:81], 0\n\t" \
  "v_mov_b64 v[82:83], 0\n\t" \
  "s_waitcnt lgkmcnt(0)\n\t" \
  "s_waitcnt vmcnt(3)\n\t" \
  "v_smfmac_f32_16x16x64_bf16 v[68:71], v[32:35], v[152:159], v104\n\t" \
  "ds_write_b128 v101, v[32:35] offset:12288\n\t" \
  "v_smfmac_f32_16x16x64_bf16 v[72:75], v[32:35], v[160:167], v104\n\t" \
  "v_smfmac_f32_16x16x64_bf16 v[76:79], v[32:35], v[48:55], v104\n\t" \
  "v_smfmac_f32_16x16x64_bf16 v[80:83], v[32:35], v[56:63], v104\n\t" \
  "s_waitcnt vmcnt(2)\n\t" \
  "v_smfmac_f32_16x16x64_bf16 v[68:71], v[36:39], v[168:175], v104\n\t" \
  "ds_write_b128 v101, v[36:39] offset:13312\n\t" \
  "v_smfmac_f32_16x16x64_bf16 v[72:75], v[36:39], v[176:183], v104\n\t" \
  "v_smfmac_f32_16x16x64_bf16 v[76:79], v[36:39], v[184:191], v104\n\t" \
  "v_cmp_u_f32 vcc, v68, v69\n\t" \
  "v_smfmac_f32_16x16x64_bf16 v[80:83], v[36:39], v[192:199], v104\n\t" \
  "v_cmp_u_f32 s[82:83], v70, v71\n\t" \
  "s_waitcnt vmcnt(1)\n\t" \
  "v_smfmac_f32_16x16x64_bf16 v[68:71], v[40:43], v[16:23], v104\n\t" \
  "ds_write_b128 v101, v[40:43] offset:14336\n\t" \
  "v_smfmac_f32_16x16x64_bf16 v[72:75], v[40:43], v[24:31], v104\n\t" \
  "s_or_b64 vcc, vcc, s[82:83]\n\t" \
  "s_or_b64 vcc, vcc, s[80:81]\n\t" \
  "s_waitcnt lgkmcnt(1)\n\t" \
  "v_smfmac_f32_16x16x64_bf16 v[76:79], v[40:43], v[200:207], v104\n\t" \
  "s_cbranch_scc1 8f\n\t" \
  "5:\n\t" \
  "s_barrier\n\t" \
  "4:\n\t" \
  "s_memtime s[56:57]\n\t" \
  "ds_read_b128 v[48:51], v101 offset:0\n\t" \
  "v_smfmac_f32_16x16x64_bf16 v[80:83], v[40:43], v[208:215], v104\n\t" \
  "ds_read_b128 v[52:55], v101 offset:1024\n\t" \
  "ds_read_b128 v[56:59], v101 offset:4096\n\t" \
  "s_waitcnt vmcnt(0)\n\t" \
  "v_smfmac_f32_16x16x64_bf16 v[68:71], v[44:47], v[216:223], v104\n\t" \
  "v_smfmac_f32_16x16x64_bf16 v[72:75], v[44:47], v[224:231], v104\n\t" \
  "ds_write_b128 v101, v[44:47] offset:15360\n\t" \
  "v_smfmac_f32_16x16x64_bf16 v[76:79], v[44:47], v[232:239], v104\n\t" \
  "v_cmp_u_f32 vcc, v68, v69\n\t" \
  "v_smfmac_f32_16x16x64_bf16 v[80:83], v[44:47], v[240:247], v104\n\t" \
  "v_cmp_u_f32 s[82:83], v70, v71\n\t" \
  "s_waitcnt lgkmcnt(3)\n\t" \
  "v_smfmac_f32_16x16x64_bf16 v[68:71], v[48:51], a[0:7], v104\n\t" \
  "v_smfmac_f32_16x16x64_bf16 v[72:75], v[48:51], a[8:15], v104\n\t" \
  "s_or_b64 vcc, vcc, s[82:83]\n\t" \
  "s_waitcnt lgkmcnt(0)\n\t" \
  "v_smfmac_f32_16x16x64_bf16 v[68:71], v[52:55], a[16:23], v104\n\t" \
  "s_cbranch_scc1 9f\n\t" \
  "6:\n\t" \
  "s_barrier\n\t" \
  "ds_read_b128 v[60:63], v101 offset:5120\n\t" \
  "v_smfmac_f32_16x16x64_bf16 v[72:75], v[52:55], a[24:31], v104\n\t" \
  "ds_read_b128 v[64:67], v101 offset:8192\n\t" \
  "v_smfmac_f32_16x16x64_bf16 v[68:71], v[56:59], a[96:103], v104\n\t" \
  "v_smfmac_f32_16x16x64_bf16 v[72:75], v[56:59], a[104:111], v104\n\t" \
  "ds_read_b128 v[48:51], v101 offset:9216\n\t" \
  "s_waitcnt lgkmcnt(2)\n\t" \
  "v_smfmac_f32_16x16x64_bf16 v[68:71], v[60:63], a[112:119], v104\n\t" \
  "ds_read_b128 v[52:55], v101 offset:2048\n\t" \
  "v_smfmac_f32_16x16x64_bf16 v[72:75], v[60:63], a[120:127], v104\n\t" \
  "ds_read_b128 v[56:59], v101 offset:3072\n\t" \
  "s_waitcnt lgkmcnt(3)\n\t" \
  "v_smfmac_f32_16x16x64_bf16 v[68:71], v[64:67], a[192:199], v104\n\t" \
  "ds_read_b128 v[60:63], v101 offset:6144\n\t" \
  "v_smfmac_f32_16x16x64_bf16 v[72:75], v[64:67], a[200:207], v104\n\t" \
  "s_waitcnt lgkmcnt(3)\n\t" \
  "v_smfmac_f32_16x16x64_bf16 v[68:71], v[48:51], a[208:215], v104\n\t" \
  "ds_read_b128 v[64:67], v101 offset:7168\n\t" \
  "ds_read_b128 v[16:19], v103 offset:16384\n\t" \
  "v_smfmac_f32_16x16x64_bf16 v[72:75], v[48:51], a[216:223], v104\n\t" \
  "s_waitcnt lgkmcnt(4)\n\t" \
  "v_smfmac_f32_16x16x64_bf16 v[68:71], v[52:55], v[84:91], v104\n\t" \
  "ds_read_b128 v[48:51], v101 offset:11264\n\t" \
  "ds_read_b128 v[20:23], v103 offset:17408\n\t" \
  "v_smfmac_f32_16x16x64_bf16 v[72:75], v[52:55], v[92:99], v104\n\t" \
  "s_waitcnt lgkmcnt(5)\n\t" \
  "v_smfmac_f32_16x16x64_bf16 v[68:71], v[56:59], a[64:71], v104\n\t" \
  "ds_read_b128 v[52:55], v101 offset:10240\n\t" \
  "ds_read_b128 v[24:27], v103 offset:18432\n\t" \
  "v_smfmac_f32_16x16x64_bf16 v[72:75], v[56:59], a[72:79], v104\n\t" \
  "ds_read_b128 v[28:31], v103 offset:19456\n\t" \
  "s_waitcnt lgkmcnt(7)\n\t" \
  "v_smfmac_f32_16x16x64_bf16 v[68:71], v[60:63], v[0:7], v104\n\t" \
  "ds_read_b128 v[84:87], v103 offset:22528\n\t" \
  "v_smfmac_f32_16x16x64_bf16 v[72:75], v[60:63], v[8:15], v104\n\t" \
  "ds_read_b128 v[88:91], v103 offset:23552\n\t" \
  "s_waitcnt lgkmcnt(8)\n\t" \
  "v_smfmac_f32_16x16x64_bf16 v[68:71], v[64:67], a[160:167], v104\n\t" \
  "ds_read_b128 v[0:3], v102 offset:0\n\t" \
  "v_smfmac_f32_16x16x64_bf16 v[72:75], v[64:67], a[168:175], v104\n\t" \
  "ds_read_b128 v[4:7], v102 offset:1024\n\t" \
  "s_waitcnt lgkmcnt(8)\n\t" \
  "v_smfmac_f32_16x16x64_bf16 v[68:71], v[48:51], v[120:127], v104\n\t" \
  "ds_read_b128 v[92:95], v102 offset:22528\n\t" \
  "s_waitcnt lgkmcnt(7)\n\t" \
  "v_smfmac_f32_16x16x64_bf16 v[68:71], v[52:55], v[16:23], v104\n\t" \
  "ds_read_b128 v[8:11], v102 offset:2048\n\t" \
  "v_smfmac_f32_16x16x64_bf16 v[72:75], v[48:51], v[128:135], v104\n\t" \
  "ds_read_b128 v[12:15], v102 offset:3072\n\t" \
  "s_waitcnt lgkmcnt(7)\n\t" \
  "v_smfmac_f32_16x16x64_bf16 v[72:75], v[52:55], v[24:31], v104\n\t" \
  "v_add_f32 v44, v68, v70\n\t" \
  "ds_read_b128 v[96:99], v102 offset:23552\n\t" \
  "v_smfmac_f32_16x16x64_bf16 v[76:79], v[56:59], a[80:87], v104\n\t" \
  "v_add_f32 v45, v69, v71\n\t" \
  "v_cvt_pk_bf16_f32 v44, v44, v45\n\t" \
  "v_smfmac_f32_16x16x64_bf16 v[80:83], v[56:59], a[88:95], v104\n\t" \
  "v_add_f32 v46, v72, v74\n\t" \
  "v_add_f32 v47, v73, v75\n\t" \
  "v_smfmac_f32_16x16x64_bf16 v[76:79], v[60:63], a[144:151], v104\n\t" \
  "v_cvt_pk_bf16_f32 v46, v46, v47\n\t" \
  "ds_write2st64_b32 v105, v44, v46 offset1:1\n\t" \
  "v_smfmac_f32_16x16x64_bf16 v[80:83], v[60:63], a[152:159], v104\n\t" \
  "ds_read_b128 v[56:59], v101 offset:0\n\t" \
  "v_mov_b64 v[68:69], 0\n\t" \
  "v_smfmac_f32_16x16x64_bf16 v[76:79], v[64:67], a[176:183], v104\n\t" \
  "ds_read_b128 v[16:19], v102 offset:32768\n\t" \
  "v_mov_b64 v[70:71], 0\n\t" \
  "v_smfmac_f32_16x16x64_bf16 v[80:83], v[64:67], a[184:191], v104\n\t" \
  "ds_read_b128 v[20:23], v102 offset:33792\n\t" \
  "v_smfmac_f32_16x16x64_bf16 v[76:79], v[48:51], v[136:143], v104\n\t" \
  "s_waitcnt lgkmcnt(3)\n\t" \
  "s_barrier\n\t" \
  "v_smfmac_f32_16x16x64_bf16 v[80:83], v[48:51], v[144:151], v104\n\t" \
  "v_smfmac_f32_16x16x64_bf16 v[68:71], v[32:35], v[84:91], v104\n\t" \
  "v_smfmac_f32_16x16x64_bf16 v[76:79], v[52:55], a[240:247], v104\n\t" \
  "ds_read_b128 v[60:63], v101 offset:9216\n\t" \
  "ds_read_b128 v[24:27], v102 offset:34816\n\t" \
  "v_smfmac_f32_16x16x64_bf16 v[80:83], v[52:55], a[248:255], v104\n\t" \
  "ds_read_b128 v[64:67], v101 offset:2048\n\t" \
  "s_waitcnt lgkmcnt(5)\n\t" \
  "v_smfmac_f32_16x16x64_bf16 v[76:79], v[56:59], v[0:7], v104\n\t" \
  "ds_read_b128 v[48:51], v101 offset:4096\n\t" \
  "ds_read_b128 v[28:31], v102 offset:35840\n\t" \
  "v_smfmac_f32_16x16x64_bf16 v[80:83], v[56:59], v[8:15], v104\n\t" \
  "ds_read_b128 v[84:87], v102 offset:55296\n\t" \
  "global_load_dword v119, v109, %[kc] sc1\n\t" \
  "s_waitcnt lgkmcnt(5)\n\t" \
  "v_smfmac_f32_16x16x64_bf16 v[76:79], v[60:63], a[224:231], v104\n\t" \
  "ds_read_b128 v[52:55], v101 offset:1024\n\t" \
  "ds_read_b128 v[88:91], v102 offset:56320\n\t" \
  "v_smfmac_f32_16x16x64_bf16 v[80:83], v[60:63], a[232:239], v104\n\t" \
  "ds_read_b128 v[0:3], v103 offset:0\n\t" \
  "s_waitcnt lgkmcnt(6)\n\t" \
  "v_smfmac_f32_16x16x64_bf16 v[76:79], v[64:67], a[48:55], v104\n\t" \
  "ds_read_b128 v[56:59], v101 offset:5120\n\t" \
  "buffer_load_dwordx4 v[32:35], v100, %[ru], s47 offen offset:0 sc1\n\t" \
  "v_smfmac_f32_16x16x64_bf16 v[80:83], v[64:67], a[56:63], v104\n\t" \
  "ds_read_b128 v[60:63], v101 offset:8192\n\t" \
  "ds_read_b128 v[4:7], v103 offset:1024\n\t" \
  "s_waitcnt lgkmcnt(8)\n\t" \
  "v_smfmac_f32_16x16x64_bf16 v[76:79], v[48:51], v[16:23], v104\n\t" \
  "ds_read_b128 v[8:11], v103 offset:2048\n\t" \
  "s_waitcnt lgkmcnt(8)\n\t" \
  "v_smfmac_f32_16x16x64_bf16 v[80:83], v[48:51], v[24:31], v104\n\t" \
  "ds_read_b128 v[12:15], v103 offset:3072\n\t" \
  "buffer_load_dwordx4 v[36:39], v100, %[ru], s47 offen offset:1024 sc1\n\t" \
  "v_smfmac_f32_16x16x64_bf16 v[68:71], v[48:51], v[92:99], v104\n\t" \
  "s_waitcnt lgkmcnt(7)\n\t" \
  "v_smfmac_f32_16x16x64_bf16 v[76:79], v[52:55], a[32:39], v104\n\t" \
  "v_smfmac_f32_16x16x64_bf16 v[80:83], v[52:55], a[40:47], v104\n\t" \
  "s_waitcnt lgkmcnt(4)\n\t" \
  "v_smfmac_f32_16x16x64_bf16 v[76:79], v[56:59], a[128:135], v104\n\t" \
  "buffer_load_dwordx4 v[40:43], v100, %[ru], s47 offen offset:2048 sc1\n\t" \
  "s_waitcnt lgkmcnt(2)\n\t" \
  "v_smfmac_f32_16x16x64_bf16 v[76:79], v[60:63], v[0:7], v104\n\t" \
  "v_smfmac_f32_16x16x64_bf16 v[68:71], v[60:63], v[84:91], v104\n\t" \
  "v_smfmac_f32_16x16x64_bf16 v[80:83], v[56:59], a[136:143], v104\n\t" \
  "s_waitcnt lgkmcnt(0)\n\t" \
  "v_smfmac_f32_16x16x64_bf16 v[80:83], v[60:63], v[8:15], v104\n\t" \
  "v_add_f32 v20, v76, v78\n\t" \
  "v_add_f32 v21, v77, v79\n\t" \
  "v_cvt_pk_bf16_f32 v20, v20, v21\n\t" \
  "ds_write_b32 v106, v20\n\t" \
  "v_add_f32 v72, v68, v70\n\t" \
  "v_add_f32 v73, v69, v71\n\t" \
  "v_cvt_pk_bf16_f32 v72, v72, v73\n\t" \
  "ds_write_b32 v106, v72 offset:11008\n\t" \
  "v_add_f32 v23, v80, v82\n\t" \
  "v_add_f32 v21, v81, v83\n\t" \
  "v_cvt_pk_bf16_f32 v21, v23, v21\n\t" \
  "ds_write_b32 v106, v21 offset:256\n\t" \
  "s_waitcnt lgkmcnt(0)\n\t" \
  "s_barrier\n\t" \
  "s_waitcnt vmcnt(0)\n\t" \
  "s_barrier\n\t" \
  "ds_write_b128 v101, v[32:35] offset:25600\n\t" \
  "ds_write_b128 v101, v[36:39] offset:26624\n\t" \
  "ds_write_b128 v101, v[40:43] offset:27648\n\t" \
  "s_add_i32 s77, s44, 1\n\t" \
  "s_min_u32 s77, s77, 63\n\t" \
  "v_cmp_gt_i32 vcc, s77, v119\n\t" \
  "s_cmp_lg_u64 vcc, 0\n\t" \
  "s_cselect_b32 s42, 32, 0\n\t" \
  "s_add_i32 s41, s56, 1950\n\t" \
  "s_sub_i32 s76, s92, s51\n\t" \
  "s_sub_i32 s77, s93, s53\n\t" \
  "s_cmp_eq_u32 s76, 0\n\t" \
  "s_cbranch_scc1 21f\n\t" \
  "v_cvt_f32_u32 v117, s76\n\t" \
  "v_cvt_f32_u32 v116, s77\n\t" \
  "v_rcp_f32 v117, v117\n\t" \
  "s_mov_b32 s51, s92\n\t" \
  "v_mul_f32 v117, v117, v116\n\t" \
  "v_max_f32 v117, 0x3f028f5c, v117\n\t" \
  "v_min_f32 v117, 0x3ffeb852, v117\n\t" \
  "v_rcp_f32 v116, v117\n\t" \
  "v_mul_f32 v117, 0x4f000000, v117\n\t" \
  "v_mul_f32 v116, 0x47800000, v116\n\t" \
  "v_cvt_u32_f32 v117, v117\n\t" \
  "v_cvt_u32_f32 v116, v116\n\t" \
  "s_mov_b32 s53, s93\n\t" \
  "v_readfirstlane_b32 s46, v117\n\t" \
  "v_readfirstlane_b32 s52, v116\n\t" \
  "21:\n\t" \
  "v_mov_b32 v0, -1\n\t" \
  "v_mov_b32 v1, -1\n\t" \
  "v_mov_b32 v2, -1\n\t" \
  "v_mov_b32 v3, -1\n\t" \
  "s_and_b32 s76, s49, 3\n\t" \
  "s_mul_i32 s76, s76, 0xc000\n\t" \
  "s_add_i32 s76, s76, 0x40000\n\t" \
  "buffer_store_dwordx4 v[0:3], v110, %[rh], s76 offen sc1\n\t" \
  "s_add_i32 s76, s44, 1\n\t" \
  "s_and_b32 s76, s76, 3\n\t" \
  "s_lshl_b32 s76, s76, 18\n\t" \
  "s_add_i32 s77, s76, 0x10000\n\t" \
  "buffer_store_dwordx4 v[0:3], v107, %[rh], s76 offen sc1\n\t" \
  "buffer_store_dwordx4 v[0:3], v107, %[rh], s77 offen sc1\n\t" \
  "buffer_store_dwordx4 v[0:3], v111, %[rh], s76 offen sc1\n\t" \
  "s_add_i32 s44, s44, 1\n\t" \
  "s_and_b32 s58, s58, 4\n\t" \
  "s_and_b32 s76, s44, 64\n\t" \
  "s_or_b32 s42, s42, s76\n\t" \
  "s_cbranch_scc0 20b\n\t" \
  "s_branch 99f\n\t" \
  "14:\n\t" \
  "s_sleep 3\n\t" \
  "s_add_u32 s59, s59, 0x10000\n\t" \
  "s_cmp_lt_u32 s59, 0x400000\n\t" \
  "s_cbranch_scc1 13b\n\t" \
  "s_mov_b64 exec, -1\n\t" \
  "s_mov_b32 s42, 16\n\t" \
  "s_branch 99f\n\t" \
  "8:\n\t" \
  "s_bitcmp1_b32 s58, 1\n\t" \
  "s_cbranch_scc1 4b\n\t" \
  "s_bitcmp1_b32 s58, 2\n\t" \
  "s_cbranch_scc1 5b\n\t" \
  "v_max3_u32 v116, v32, v33, v34\n\t" \
  "v_max3_u32 v117, v35, v36, v37\n\t" \
  "v_max3_u32 v116, v116, v38, v39\n\t" \
  "v_max_u32 v116, v116, v117\n\t" \
  "v_cmp_eq_u32 vcc, -1, v116\n\t" \
  "s_cbranch_vccz 5b\n\t" \
  "s_branch 0f\n\t" \
  "9:\n\t" \
  "s_bitcmp1_b32 s58, 2\n\t" \
  "s_cbranch_scc1 6b\n\t" \
  "v_max3_u32 v116, v40, v41, v42\n\t" \
  "v_max3_u32 v117, v43, v44, v45\n\t" \
  "v_max3_u32 v116, v116, v46, v47\n\t" \
  "v_max_u32 v116, v116, v117\n\t" \
  "v_cmp_eq_u32 vcc, -1, v116\n\t" \
  "s_cbranch_vccz 6b\n\t" \
  "s_or_b32 s58, s58, 2\n\t" \
  "0:\n\t" \
  "s_or_b32 s58, s58, 1\n\t" \
  "11:\n\t" \
  "buffer_load_dwordx4 v[32:35], v100, %[rh], s40 offen sc1\n\t" \
  "buffer_load_dwordx4 v[36:39], v100, %[rh], s76 offen sc1\n\t" \
  "buffer_load_dwordx4 v[40:43], v100, %[rh], s77 offen sc1\n\t" \
  "buffer_load_dwordx4 v[44:47], v100, %[rh], s78 offen sc1\n\t" \
  "s_waitcnt lgkmcnt(0)\n\t" \
  "s_branch 10b\n\t" \
  "17:\n\t" \
  "s_add_u32 s59, s59, 1\n\t" \
  "s_bitcmp1_b32 s58, 2\n\t" \
  "s_cbranch_scc1 3b\n\t" \
  "s_waitcnt vmcnt(0)\n\t" \
  "v_max_u32 v116, v32, v33\n\t" \
  "v_max3_u32 v116, v116, v34, v35\n\t" \
  "v_max3_u32 v116, v116, v36, v37\n\t" \
  "v_max3_u32 v116, v116, v38, v39\n\t" \
  "v_max3_u32 v116, v116, v40, v41\n\t" \
  "v_max3_u32 v116, v116, v42, v43\n\t" \
  "v_max3_u32 v116, v116, v44, v45\n\t" \
  "v_max3_u32 v116, v116, v46, v47\n\t" \
  "v_cmp_eq_u32 vcc, -1, v116\n\t" \
  "s_cbranch_vccz 3b\n\t" \
  "s_bitcmp1_b32 s59, 6\n\t" \
  "s_cbranch_scc1 12f\n\t" \
  "buffer_load_dwordx4 v[32:35], v100, %[rh], s40 offen sc1\n\t" \
  "buffer_load_dwordx4 v[36:39], v100, %[rh], s76 offen sc1\n\t" \
  "buffer_load_dwordx4 v[40:43], v100, %[rh], s77 offen sc1\n\t" \
  "buffer_load_dwordx4 v[44:47], v100, %[rh], s78 offen sc1\n\t" \
  "s_branch 17b\n\t" \
  "12:\n\t" \
  "s_mov_b32 s42, s58\n\t" \
  "s_branch 99f\n\t" \
  "99:\n\t" \
  "s_waitcnt vmcnt(0) lgkmcnt(0)\n\t" \
  ""
#define VO_BODY \
  "s_cmp_eq_u32 %[wv], 1\n\t" \
  "s_cbranch_scc1 91f\n\t" \
  "s_cmp_eq_u32 %[wv], 2\n\t" \
  "s_cbranch_scc1 92f\n\t" \
  "s_cmp_eq_u32 %[wv], 3\n\t" \
  "s_cbranch_scc1 93f\n\t" \
  "90:\n\t" \
  "s_mov_b32 s58, s42\n\t" \
  "20:\n\t" \
  "s_and_b32 s76, s44, 7\n\t" \
  "s_lshl_b32 s76, s76, 15\n\t" \
  "s_add_i32 s40, s54, s76\n\t" \
  "s_and_b32 s76, s44, 3\n\t" \
  "s_lshl_b32 s45, s76, 14\n\t" \
  "s_lshl_b32 s47, s76, 20\n\t" \
  "s_add_i32 s47, s47, s55\n\t" \
  "s_lshr_b32 s76, s47, 21\n\t" \
  "s_bitcmp1_b64 %[sel], s76\n\t" \
  "s_cselect_b32 s76, 0x2000, 0\n\t" \
  "s_add_i32 s47, s47, s76\n\t" \
  "s_add_i32 s49, s44, -1\n\t" \
  "s_and_b32 s48, s49, 2\n\t" \
  "s_lshl_b32 s48, s48, 13\n\t" \
  "s_and_b32 s76, s49, 1\n\t" \
  "s_lshl_b32 s76, s76, 12\n\t" \
  "s_add_i32 s48, s48, s76\n\t" \
  "s_lshl_b32 s50, s53, 8\n\t" \
  "s_mov_b32 s59, 0\n\t" \
  "s_mov_b32 s92, s51\n\t" \
  "s_mov_b32 s93, s53\n\t" \
  "s_xor_b32 s76, s40, 0x400\n\t" \
  "s_xor_b32 s77, s40, 0x800\n\t" \
  "s_xor_b32 s78, s40, 0xc00\n\t" \
  "s_cmp_lg_u32 s58, 0\n\t" \
  "s_cbranch_scc1 11f\n\t" \
  "10:\n\t" \
  "ds_read_b128 v[48:51], v102 offset:0\n\t" \
  "ds_read_b128 v[52:55], v102 offset:1024\n\t" \
  "ds_read_b128 v[56:59], v102 offset:2048\n\t" \
  "ds_read_b128 v[60:63], v102 offset:3072\n\t" \
  "ds_read_b128 v[0:3], v102 offset:16384\n\t" \
  "ds_read_b128 v[4:7], v102 offset:17408\n\t" \
  "ds_read_b128 v[8:11], v102 offset:18432\n\t" \
  "ds_read_b128 v[12:15], v102 offset:19456\n\t" \
  "ds_read_b128 v[16:19], v102 offset:32768\n\t" \
  "ds_read_b128 v[20:23], v102 offset:33792\n\t" \
  "ds_read_b128 v[24:27], v102 offset:34816\n\t" \
  "ds_read_b128 v[28:31], v102 offset:35840\n\t" \
  "s_waitcnt lgkmcnt(4)\n\t" \
  "ds_read_b128 v[80:83], v102 offset:49152\n\t" \
  "ds_read_b128 v[84:87], v102 offset:50176\n\t" \
  "ds_read_b128 v[88:91], v102 offset:51200\n\t" \
  "ds_read_b128 v[92:95], v102 offset:52224\n\t" \
  "46:\n\t" \
  "s_bitcmp1_b32 s58, 1\n\t" \
  "s_cselect_b64 s[80:81], -1, 0\n\t" \
  "s_cmp_lg_u32 s58, 0\n\t" \
  "s_cbranch_scc1 17f\n\t" \
  "s_mov_b64 exec, 0xff\n\t" \
  "16:\n\t" \
  "s_memtime vcc\n\t" \
  "s_waitcnt lgkmcnt(0)\n\t" \
  "s_sub_i32 s79, s41, vcc_lo\n\t" \
  "s_cmp_gt_i32 s79, 64\n\t" \
  "s_cbranch_scc1 16b\n\t" \
  "13:\n\t" \
  "buffer_load_dword v116, v112, %[rh], s48 offen sc1\n\t" \
  "s_waitcnt vmcnt(0)\n\t" \
  "15:\n\t" \
  "v_mov_b32 v117, 20000\n\t" \
  "v_cmp_ne_u32_sdwa vcc, v116, s49 src0_sel:BYTE_0 src1_sel:DWORD\n\t" \
  "v_subrev_u32 v116, s50, v116\n\t" \
  "v_ashrrev_i32 v116, 8, v116\n\t" \
  "s_nop 1\n\t" \
  "v_max_i32_dpp v116, v116, v116 quad_perm:[1,0,3,2] row_mask:0xf bank_mask:0xf\n\t" \
  "s_cbranch_vccnz 14f\n\t" \
  "s_nop 0\n\t" \
  "v_max_i32_dpp v116, v116, v116 quad_perm:[2,3,0,1] row_mask:0xf bank_mask:0xf\n\t" \
  "s_nop 1\n\t" \
  "v_max_i32_dpp v116, v116, v116 row_half_mirror row_mask:0xf bank_mask:0xf\n\t" \
  "v_med3_i32 v116, v116, 0, v117\n\t" \
  "v_mul_u32_u24 v116, s52, v116\n\t" \
  "v_lshrrev_b32 v116, 16, v116\n\t" \
  "s_mov_b64 exec, -1\n\t" \
  "v_readfirstlane_b32 s84, v116\n\t" \
  "s_add_i32 s84, s84, s51\n\t" \
  ".p2align 6\n\t" \
  "s_nop 0\n\t" \
  "s_nop 0\n\t" \
  "s_nop 0\n\t" \
  "s_nop 0\n\t" \
  "s_nop 0\n\t" \
  "s_nop 0\n\t" \
  "s_nop 0\n\t" \
  "s_nop 0\n\t" \
  "1:\n\t" \
  "s_memtime vcc\n\t" \
  "s_waitcnt lgkmcnt(0)\n\t" \
  "s_sub_i32 s79, s84, vcc_lo\n\t" \
  "s_cmp_gt_i32 s79, 128\n\t" \
  "s_cbranch_scc1 1b\n\t" \
  ".p2align 4\n\t" \
  "2:\n\t" \
  "s_sub_i32 s79, s79, 33\n\t" \
  "s_cmp_gt_i32 s79, 56\n\t" \
  "s_cbranch_scc1 2b\n\t" \
  "buffer_load_dwordx4 v[32:35], v100, %[rh], s40 offen sc1\n\t" \
  "s_nop 12\n\t" \
  "buffer_load_dwordx4 v[36:39], v100, %[rh], s76 offen sc1\n\t" \
  "s_nop 12\n\t" \
  "buffer_load_dwordx4 v[40:43], v100, %[rh], s77 offen sc1\n\t" \
  "s_nop 12\n\t" \
  "buffer_load_dwordx4 v[44:47], v100, %[rh], s78 offen sc1\n\t" \
  "s_memtime vcc\n\t" \
  "s_memrealtime s[82:83]\n\t" \
  "s_nop 4\n\t" \
  "s_memrealtime s[84:85]\n\t" \
  "s_nop 4\n\t" \
  "s_memrealtime s[56:57]\n\t" \
  "s_nop 4\n\t" \
  "s_memrealtime s[86:87]\n\t" \
  "s_nop 4\n\t" \
  "s_memrealtime s[88:89]\n\t" \
  "s_nop 4\n\t" \
  "s_memrealtime s[90:91]\n\t" \
  "s_mov_b32 s92, 0\n\t" \
  "s_waitcnt lgkmcnt(0)\n\t" \
  "s_cmp_eq_u32 s82, s84\n\t" \
  "s_addc_u32 s92, s92, 0\n\t" \
  "s_cmp_eq_u32 s82, s56\n\t" \
  "s_addc_u32 s92, s92, 0\n\t" \
  "s_cmp_eq_u32 s82, s86\n\t" \
  "s_addc_u32 s92, s92, 0\n\t" \
  "s_cmp_eq_u32 s82, s88\n\t" \
  "s_addc_u32 s92, s92, 0\n\t" \
  "s_cmp_eq_u32 s82, s90\n\t" \
  "s_addc_u32 s92, s92, 0\n\t" \
  "s_mul_i32 s92, s92, 24\n\t" \
  "s_add_i32 s92, s92, vcc_lo\n\t" \
  "s_lshl4_add_u32 s93, s82, 64\n\t" \
  "3:\n\t" \
  "v_mov_b64 v[72:73], 0\n\t" \
  "v_mov_b64 v[74:75], 0\n\t" \
  "v_mov_b64 v[76:77], 0\n\t" \
  "v_mov_b64 v[78:79], 0\n\t" \
  "s_waitcnt lgkmcnt(0)\n\t" \
  "s_waitcnt vmcnt(3)\n\t" \
  "v_smfmac_f32_16x16x64_bf16 v[72:75], v[32:35], v[48:55], v104\n\t" \
  "ds_write_b128 v101, v[32:35] offset:0\n\t" \
  "v_smfmac_f32_16x16x64_bf16 v[76:79], v[32:35], v[56:63], v104\n\t" \
  "s_waitcnt vmcnt(2)\n\t" \
  "ds_write_b128 v101, v[36:39] offset:1024\n\t" \
  "v_smfmac_f32_16x16x64_bf16 v[72:75], v[36:39], a[0:7], v104\n\t" \
  "v_smfmac_f32_16x16x64_bf16 v[76:79], v[36:39], a[8:15], v104\n\t" \
  "s_nop 3\n\t" \
  "v_cmp_u_f32 vcc, v72, v73\n\t" \
  "v_cmp_u_f32 s[82:83], v74, v75\n\t" \
  "s_waitcnt vmcnt(1)\n\t" \
  "v_smfmac_f32_16x16x64_bf16 v[72:75], v[40:43], a[16:23], v104\n\t" \
  "s_or_b64 vcc, vcc, s[82:83]\n\t" \
  "s_or_b64 vcc, vcc, s[80:81]\n\t" \
  "v_smfmac_f32_16x16x64_bf16 v[76:79], v[40:43], a[24:31], v104\n\t" \
  "s_waitcnt lgkmcnt(0)\n\t" \
  "s_cbranch_scc1 8f\n\t" \
  "5:\n\t" \
  "s_barrier\n\t" \
  "4:\n\t" \
  "s_memtime s[56:57]\n\t" \
  "ds_read_b128 v[48:51], v101 offset:4096\n\t" \
  "ds_read_b128 v[52:55], v101 offset:5120\n\t" \
  "ds_read_b128 v[56:59], v101 offset:9216\n\t" \
  "s_waitcnt vmcnt(0)\n\t" \
  "v_smfmac_f32_16x16x64_bf16 v[72:75], v[44:47], a[32:39], v104\n\t" \
  "ds_read_b128 v[60:63], v101 offset:8192\n\t" \
  "ds_read_b128 v[64:67], v101 offset:13312\n\t" \
  "v_smfmac_f32_16x16x64_bf16 v[76:79], v[44:47], a[40:47], v104\n\t" \
  "ds_read_b128 v[68:71], v101 offset:12288\n\t" \
  "ds_write_b128 v101, v[40:43] offset:2048\n\t" \
  "s_nop 1\n\t" \
  "v_cmp_u_f32 vcc, v72, v73\n\t" \
  "v_cmp_u_f32 s[82:83], v74, v75\n\t" \
  "s_waitcnt lgkmcnt(6)\n\t" \
  "v_smfmac_f32_16x16x64_bf16 v[72:75], v[48:51], v[0:7], v104\n\t" \
  "s_or_b64 vcc, vcc, s[82:83]\n\t" \
  "v_smfmac_f32_16x16x64_bf16 v[76:79], v[48:51], v[8:15], v104\n\t" \
  "s_cbranch_scc1 9f\n\t" \
  "6:\n\t" \
  "s_waitcnt lgkmcnt(5)\n\t" \
  "v_smfmac_f32_16x16x64_bf16 v[72:75], v[52:55], a[48:55], v104\n\t" \
  "ds_write_b128 v101, v[44:47] offset:3072\n\t" \
  "v_smfmac_f32_16x16x64_bf16 v[76:79], v[52:55], a[56:63], v104\n\t" \
  "v_mov_b64 v[40:41], 0\n\t" \
  "s_waitcnt lgkmcnt(5)\n\t" \
  "v_smfmac_f32_16x16x64_bf16 v[72:75], v[56:59], a[96:103], v104\n\t" \
  "v_mov_b64 v[42:43], 0\n\t" \
  "v_smfmac_f32_16x16x64_bf16 v[76:79], v[56:59], a[104:111], v104\n\t" \
  "s_waitcnt lgkmcnt(4)\n\t" \
  "v_smfmac_f32_16x16x64_bf16 v[72:75], v[60:63], v[16:23], v104\n\t" \
  "s_waitcnt lgkmcnt(0)\n\t" \
  "s_barrier\n\t" \
  "v_smfmac_f32_16x16x64_bf16 v[76:79], v[60:63], v[24:31], v104\n\t" \
  "ds_read_b128 v[48:51], v101 offset:6144\n\t" \
  "ds_read_b128 v[52:55], v101 offset:10240\n\t" \
  "v_smfmac_f32_16x16x64_bf16 v[72:75], v[64:67], a[144:151], v104\n\t" \
  "ds_read_b128 v[56:59], v101 offset:14336\n\t" \
  "ds_read_b128 v[60:63], v101 offset:7168\n\t" \
  "v_smfmac_f32_16x16x64_bf16 v[76:79], v[64:67], a[152:159], v104\n\t" \
  "ds_read_b128 v[32:35], v101 offset:11264\n\t" \
  "ds_read_b128 v[36:39], v101 offset:15360\n\t" \
  "v_smfmac_f32_16x16x64_bf16 v[72:75], v[68:71], v[80:87], v104\n\t" \
  "ds_read_b128 v[0:3], v103 offset:0\n\t" \
  "v_mov_b64 v[44:45], 0\n\t" \
  "v_smfmac_f32_16x16x64_bf16 v[76:79], v[68:71], v[88:95], v104\n\t" \
  "v_mov_b64 v[46:47], 0\n\t" \
  "s_waitcnt lgkmcnt(5)\n\t" \
  "v_smfmac_f32_16x16x64_bf16 v[72:75], v[48:51], a[64:71], v104\n\t" \
  "ds_read_b128 v[4:7], v103 offset:1024\n\t" \
  "v_mov_b64 v[80:81], 0\n\t" \
  "v_smfmac_f32_16x16x64_bf16 v[72:75], v[52:55], a[112:119], v104\n\t" \
  "v_mov_b64 v[82:83], 0\n\t" \
  "s_waitcnt lgkmcnt(2)\n\t" \
  "v_smfmac_f32_16x16x64_bf16 v[72:75], v[56:59], a[160:167], v104\n\t" \
  "ds_read_b128 v[8:11], v103 offset:2048\n\t" \
  "v_mov_b64 v[84:85], 0\n\t" \
  "v_smfmac_f32_16x16x64_bf16 v[72:75], v[60:63], a[80:87], v104\n\t" \
  "ds_read_b128 v[12:15], v103 offset:3072\n\t" \
  "v_mov_b64 v[86:87], 0\n\t" \
  "v_smfmac_f32_16x16x64_bf16 v[72:75], v[32:35], a[128:135], v104\n\t" \
  "ds_read_b128 v[16:19], v103 offset:16384\n\t" \
  "v_mov_b64 v[88:89], 0\n\t" \
  "v_smfmac_f32_16x16x64_bf16 v[72:75], v[36:39], a[176:183], v104\n\t" \
  "ds_read_b128 v[20:23], v103 offset:17408\n\t" \
  "v_mov_b64 v[90:91], 0\n\t" \
  "v_smfmac_f32_16x16x64_bf16 v[76:79], v[48:51], a[72:79], v104\n\t" \
  "ds_read_b128 v[24:27], v103 offset:18432\n\t" \
  "v_mov_b64 v[92:93], 0\n\t" \
  "v_smfmac_f32_16x16x64_bf16 v[76:79], v[52:55], a[120:127], v104\n\t" \
  "v_add_f32 v96, v72, v74\n\t" \
  "v_add_f32 v97, v73, v75\n\t" \
  "v_cvt_pk_bf16_f32 v96, v96, v97\n\t" \
  "ds_write_b32 v105, v96\n\t" \
  "v_smfmac_f32_16x16x64_bf16 v[76:79], v[56:59], a[168:175], v104\n\t" \
  "ds_read_b128 v[28:31], v103 offset:19456\n\t" \
  "v_mov_b64 v[94:95], 0\n\t" \
  "v_smfmac_f32_16x16x64_bf16 v[76:79], v[60:63], a[88:95], v104\n\t" \
  "v_smfmac_f32_16x16x64_bf16 v[76:79], v[32:35], a[136:143], v104\n\t" \
  "v_smfmac_f32_16x16x64_bf16 v[76:79], v[36:39], a[184:191], v104\n\t" \
  "s_waitcnt lgkmcnt(1)\n\t" \
  "s_barrier\n\t" \
  "ds_read_b64_tr_b16 v[96:97], v107 offset:0\n\t" \
  "ds_read_b64_tr_b16 v[98:99], v107 offset:64\n\t" \
  "ds_read_b64_tr_b16 v[56:57], v107 offset:1024\n\t" \
  "ds_read_b64_tr_b16 v[58:59], v107 offset:1088\n\t" \
  "s_nop 1\n\t" \
  "v_add_f32 v116, v76, v78\n\t" \
  "v_add_f32 v117, v77, v79\n\t" \
  "v_cvt_pk_bf16_f32 v116, v116, v117\n\t" \
  "ds_write_b32 v105, v116 offset:256\n\t" \
  "s_waitcnt lgkmcnt(3)\n\t" \
  "v_smfmac_f32_16x16x64_bf16 v[40:43], v[96:99], a[192:199], v106\n\t" \
  "v_smfmac_f32_16x16x64_bf16 v[44:47], v[96:99], a[224:231], v106\n\t" \
  "s_waitcnt lgkmcnt(0)\n\t" \
  "s_barrier\n\t" \
  "ds_read_b64_tr_b16 v[52:53], v107 offset:256\n\t" \
  "ds_read_b64_tr_b16 v[54:55], v107 offset:320\n\t" \
  "v_smfmac_f32_16x16x64_bf16 v[40:43], v[56:59], a[208:215], v106\n\t" \
  "ds_read_b64_tr_b16 v[60:61], v107 offset:1280\n\t" \
  "ds_read_b64_tr_b16 v[62:63], v107 offset:1344\n\t" \
  "v_smfmac_f32_16x16x64_bf16 v[44:47], v[56:59], v[0:7], v106\n\t" \
  "v_smfmac_f32_16x16x64_bf16 v[80:83], v[96:99], a[240:247], v106\n\t" \
  "v_mov_b64 v[32:33], 0\n\t" \
  "v_smfmac_f32_16x16x64_bf16 v[84:87], v[96:99], v[136:143], v106\n\t" \
  "v_mov_b64 v[34:35], 0\n\t" \
  "v_smfmac_f32_16x16x64_bf16 v[80:83], v[56:59], v[120:127], v106\n\t" \
  "v_mov_b64 v[36:37], 0\n\t" \
  "v_smfmac_f32_16x16x64_bf16 v[84:87], v[56:59], v[16:23], v106\n\t" \
  "s_waitcnt lgkmcnt(2)\n\t" \
  "v_smfmac_f32_16x16x64_bf16 v[40:43], v[52:55], a[200:207], v106\n\t" \
  "v_smfmac_f32_16x16x64_bf16 v[44:47], v[52:55], a[232:239], v106\n\t" \
  "s_xor_b32 s76, s47, 0x40000\n\t" \
  "s_waitcnt lgkmcnt(0)\n\t" \
  "v_smfmac_f32_16x16x64_bf16 v[40:43], v[60:63], a[216:223], v106\n\t" \
  "v_mov_b64 v[38:39], 0\n\t" \
  "s_xor_b32 s77, s47, 0x80000\n\t" \
  "v_smfmac_f32_16x16x64_bf16 v[44:47], v[60:63], v[8:15], v106\n\t" \
  "s_xor_b32 s78, s47, 0xc0000\n\t" \
  "v_smfmac_f32_16x16x64_bf16 v[80:83], v[52:55], a[248:255], v106\n\t" \
  "ds_read_b128 v[0:3], v103 offset:32768\n\t" \
  "v_cvt_pk_bf16_f32 v64, v40, v41\n\t" \
  "v_smfmac_f32_16x16x64_bf16 v[84:87], v[52:55], v[144:151], v106\n\t" \
  "ds_read_b128 v[4:7], v103 offset:33792\n\t" \
  "v_cvt_pk_bf16_f32 v65, v42, v43\n\t" \
  "v_smfmac_f32_16x16x64_bf16 v[80:83], v[60:63], v[128:135], v106\n\t" \
  "ds_read_b128 v[8:11], v103 offset:34816\n\t" \
  "v_cvt_pk_bf16_f32 v66, v44, v45\n\t" \
  "v_smfmac_f32_16x16x64_bf16 v[84:87], v[60:63], v[24:31], v106\n\t" \
  "ds_read_b128 v[12:15], v103 offset:35840\n\t" \
  "v_cvt_pk_bf16_f32 v67, v46, v47\n\t" \
  "v_smfmac_f32_16x16x64_bf16 v[88:91], v[96:99], v[152:159], v106\n\t" \
  "v_permlane16_swap_b32 v64, v66\n\t" \
  "v_cvt_pk_bf16_f32 v68, v80, v81\n\t" \
  "v_smfmac_f32_16x16x64_bf16 v[92:95], v[96:99], v[184:191], v106\n\t" \
  "v_permlane16_swap_b32 v65, v67\n\t" \
  "buffer_store_dwordx4 v[64:67], v108, %[ru], s47 offen sc1\n\t" \
  "v_cvt_pk_bf16_f32 v69, v82, v83\n\t" \
  "v_smfmac_f32_16x16x64_bf16 v[88:91], v[52:55], v[160:167], v106\n\t" \
  "v_cvt_pk_bf16_f32 v70, v84, v85\n\t" \
  "v_smfmac_f32_16x16x64_bf16 v[92:95], v[52:55], v[192:199], v106\n\t" \
  "v_cvt_pk_bf16_f32 v71, v86, v87\n\t" \
  "v_smfmac_f32_16x16x64_bf16 v[88:91], v[56:59], v[168:175], v106\n\t" \
  "ds_read_b128 v[16:19], v103 offset:49152\n\t" \
  "v_permlane16_swap_b32 v68, v70\n\t" \
  "s_waitcnt lgkmcnt(3)\n\t" \
  "v_smfmac_f32_16x16x64_bf16 v[92:95], v[56:59], v[0:7], v106\n\t" \
  "ds_read_b128 v[20:23], v103 offset:50176\n\t" \
  "v_permlane16_swap_b32 v69, v71\n\t" \
  "buffer_store_dwordx4 v[68:71], v108, %[ru], s76 offen sc1\n\t" \
  "v_smfmac_f32_16x16x64_bf16 v[88:91], v[60:63], v[176:183], v106\n\t" \
  "ds_read_b128 v[24:27], v103 offset:51200\n\t" \
  "s_waitcnt lgkmcnt(3)\n\t" \
  "v_smfmac_f32_16x16x64_bf16 v[92:95], v[60:63], v[8:15], v106\n\t" \
  "ds_read_b128 v[28:31], v103 offset:52224\n\t" \
  "v_smfmac_f32_16x16x64_bf16 v[32:35], v[96:99], v[200:207], v106\n\t" \
  "v_cvt_pk_bf16_f32 v72, v88, v89\n\t" \
  "v_smfmac_f32_16x16x64_bf16 v[36:39], v[96:99], v[232:239], v106\n\t" \
  "v_cvt_pk_bf16_f32 v73, v90, v91\n\t" \
  "v_smfmac_f32_16x16x64_bf16 v[32:35], v[52:55], v[208:215], v106\n\t" \
  "v_cvt_pk_bf16_f32 v74, v92, v93\n\t" \
  "v_smfmac_f32_16x16x64_bf16 v[36:39], v[52:55], v[240:247], v106\n\t" \
  "v_cvt_pk_bf16_f32 v75, v94, v95\n\t" \
  "v_smfmac_f32_16x16x64_bf16 v[32:35], v[56:59], v[216:223], v106\n\t" \
  "v_permlane16_swap_b32 v72, v74\n\t" \
  "v_smfmac_f32_16x16x64_bf16 v[32:35], v[60:63], v[224:231], v106\n\t" \
  "v_permlane16_swap_b32 v73, v75\n\t" \
  "buffer_store_dwordx4 v[72:75], v108, %[ru], s77 offen sc1\n\t" \
  "s_waitcnt lgkmcnt(2)\n\t" \
  "v_smfmac_f32_16x16x64_bf16 v[36:39], v[56:59], v[16:23], v106\n\t" \
  "s_waitcnt lgkmcnt(0)\n\t" \
  "v_smfmac_f32_16x16x64_bf16 v[36:39], v[60:63], v[24:31], v106\n\t" \
  "v_cvt_pk_bf16_f32 v76, v32, v33\n\t" \
  "v_cvt_pk_bf16_f32 v77, v34, v35\n\t" \
  "s_nop 5\n\t" \
  "v_cvt_pk_bf16_f32 v78, v36, v37\n\t" \
  "v_cvt_pk_bf16_f32 v79, v38, v39\n\t" \
  "s_nop 0\n\t" \
  "v_permlane16_swap_b32 v76, v78\n\t" \
  "v_permlane16_swap_b32 v77, v79\n\t" \
  "buffer_store_dwordx4 v[76:79], v108, %[ru], s78 offen sc1\n\t" \
  "s_memtime s[76:77]\n\t" \
  "s_mov_b32 s42, 0\n\t" \
  "s_waitcnt lgkmcnt(0)\n\t" \
  "s_mov_b32 s57, s76\n\t" \
  "s_add_i32 s41, s56, 1950\n\t" \
  "s_sub_i32 s76, s92, s51\n\t" \
  "s_sub_i32 s77, s93, s53\n\t" \
  "s_cmp_eq_u32 s76, 0\n\t" \
  "s_cbranch_scc1 21f\n\t" \
  "v_cvt_f32_u32 v117, s76\n\t" \
  "v_cvt_f32_u32 v116, s77\n\t" \
  "v_rcp_f32 v117, v117\n\t" \
  "s_mov_b32 s51, s92\n\t" \
  "v_mul_f32 v117, v117, v116\n\t" \
  "v_max_f32 v117, 0x3f028f5c, v117\n\t" \
  "v_min_f32 v117, 0x3ffeb852, v117\n\t" \
  "v_rcp_f32 v116, v117\n\t" \
  "v_mul_f32 v117, 0x4f000000, v117\n\t" \
  "v_mul_f32 v116, 0x47800000, v116\n\t" \
  "v_cvt_u32_f32 v117, v117\n\t" \
  "v_cvt_u32_f32 v116, v116\n\t" \
  "s_mov_b32 s53, s93\n\t" \
  "v_readfirstlane_b32 s46, v117\n\t" \
  "v_readfirstlane_b32 s52, v116\n\t" \
  "21:\n\t" \
  "s_add_i32 s44, s44, 1\n\t" \
  "s_and_b32 s58, s58, 4\n\t" \
  "s_and_b32 s76, s44, 64\n\t" \
  "s_or_b32 s42, s42, s76\n\t" \
  "s_cbranch_scc0 20b\n\t" \
  "s_branch 99f\n\t" \
  "14:\n\t" \
  "s_sleep 3\n\t" \
  "s_add_u32 s59, s59, 0x10000\n\t" \
  "s_cmp_lt_u32 s59, 0x400000\n\t" \
  "s_cbranch_scc1 13b\n\t" \
  "s_mov_b64 exec, -1\n\t" \
  "s_mov_b32 s42, 16\n\t" \
  "s_branch 99f\n\t" \
  "8:\n\t" \
  "s_bitcmp1_b32 s58, 1\n\t" \
  "s_cbranch_scc1 4b\n\t" \
  "s_bitcmp1_b32 s58, 2\n\t" \
  "s_cbranch_scc1 5b\n\t" \
  "v_max3_u32 v116, v32, v33, v34\n\t" \
  "v_max3_u32 v117, v35, v36, v37\n\t" \
  "v_max3_u32 v116, v116, v38, v39\n\t" \
  "v_max_u32 v116, v116, v117\n\t" \
  "v_cmp_eq_u32 vcc, -1, v116\n\t" \
  "s_cbranch_vccz 5b\n\t" \
  "s_branch 0f\n\t" \
  "9:\n\t" \
  "s_bitcmp1_b32 s58, 2\n\t" \
  "s_cbranch_scc1 6b\n\t" \
  "v_max3_u32 v116, v40, v41, v42\n\t" \
  "v_max3_u32 v117, v43, v44, v45\n\t" \
  "v_max3_u32 v116, v116, v46, v47\n\t" \
  "v_max_u32 v116, v116, v117\n\t" \
  "v_cmp_eq_u32 vcc, -1, v116\n\t" \
  "s_cbranch_vccz 6b\n\t" \
  "s_or_b32 s58, s58, 2\n\t" \
  "0:\n\t" \
  "s_or_b32 s58, s58, 1\n\t" \
  "11:\n\t" \
  "buffer_load_dwordx4 v[32:35], v100, %[rh], s40 offen sc1\n\t" \
  "buffer_load_dwordx4 v[36:39], v100, %[rh], s76 offen sc1\n\t" \
  "buffer_load_dwordx4 v[40:43], v100, %[rh], s77 offen sc1\n\t" \
  "buffer_load_dwordx4 v[44:47], v100, %[rh], s78 offen sc1\n\t" \
  "s_waitcnt lgkmcnt(0)\n\t" \
  "s_branch 10b\n\t" \
  "17:\n\t" \
  "s_add_u32 s59, s59, 1\n\t" \
  "s_bitcmp1_b32 s58, 2\n\t" \
  "s_cbranch_scc1 3b\n\t" \
  "s_waitcnt vmcnt(0)\n\t" \
  "v_max_u32 v116, v32, v33\n\t" \
  "v_max3_u32 v116, v116, v34, v35\n\t" \
  "v_max3_u32 v116, v116, v36, v37\n\t" \
  "v_max3_u32 v116, v116, v38, v39\n\t" \
  "v_max3_u32 v116, v116, v40, v41\n\t" \
  "v_max3_u32 v116, v116, v42, v43\n\t" \
  "v_max3_u32 v116, v116, v44, v45\n\t" \
  "v_max3_u32 v116, v116, v46, v47\n\t" \
  "v_cmp_eq_u32 vcc, -1, v116\n\t" \
  "s_cbranch_vccz 3b\n\t" \
  "s_bitcmp1_b32 s59, 6\n\t" \
  "s_cbranch_scc1 12f\n\t" \
  "buffer_load_dwordx4 v[32:35], v100, %[rh], s40 offen sc1\n\t" \
  "buffer_load_dwordx4 v[36:39], v100, %[rh], s76 offen sc1\n\t" \
  "buffer_load_dwordx4 v[40:43], v100, %[rh], s77 offen sc1\n\t" \
  "buffer_load_dwordx4 v[44:47], v100, %[rh], s78 offen sc1\n\t" \
  "s_branch 17b\n\t" \
  "12:\n\t" \
  "s_mov_b32 s42, s58\n\t" \
  "s_branch 99f\n\t" \
  "91:\n\t" \
  "s_mov_b32 s58, s42\n\t" \
  "20:\n\t" \
  "s_and_b32 s76, s44, 7\n\t" \
  "s_lshl_b32 s76, s76, 15\n\t" \
  "s_add_i32 s40, s54, s76\n\t" \
  "s_and_b32 s76, s44, 3\n\t" \
  "s_lshl_b32 s45, s76, 14\n\t" \
  "s_lshl_b32 s47, s76, 20\n\t" \
  "s_add_i32 s47, s47, s55\n\t" \
  "s_lshr_b32 s76, s47, 21\n\t" \
  "s_bitcmp1_b64 %[sel], s76\n\t" \
  "s_cselect_b32 s76, 0x2000, 0\n\t" \
  "s_add_i32 s47, s47, s76\n\t" \
  "s_add_i32 s49, s44, -1\n\t" \
  "s_and_b32 s48, s49, 2\n\t" \
  "s_lshl_b32 s48, s48, 13\n\t" \
  "s_and_b32 s76, s49, 1\n\t" \
  "s_lshl_b32 s76, s76, 12\n\t" \
  "s_add_i32 s48, s48, s76\n\t" \
  "s_lshl_b32 s50, s53, 8\n\t" \
  "s_mov_b32 s59, 0\n\t" \
  "s_mov_b32 s92, s51\n\t" \
  "s_mov_b32 s93, s53\n\t" \
  "s_xor_b32 s76, s40, 0x400\n\t" \
  "s_xor_b32 s77, s40, 0x800\n\t" \
  "s_xor_b32 s78, s40, 0xc00\n\t" \
  "s_cmp_lg_u32 s58, 0\n\t" \
  "s_cbranch_scc1 11f\n\t" \
  "10:\n\t" \
  "ds_read_b128 v[48:51], v102 offset:16384\n\t" \
  "ds_read_b128 v[52:55], v102 offset:17408\n\t" \
  "ds_read_b128 v[56:59], v102 offset:18432\n\t" \
  "ds_read_b128 v[60:63], v102 offset:19456\n\t" \
  "ds_read_b128 v[0:3], v102 offset:32768\n\t" \
  "ds_read_b128 v[4:7], v102 offset:33792\n\t" \
  "ds_read_b128 v[8:11], v102 offset:34816\n\t" \
  "ds_read_b128 v[12:15], v102 offset:35840\n\t" \
  "ds_read_b128 v[16:19], v102 offset:49152\n\t" \
  "ds_read_b128 v[20:23], v102 offset:50176\n\t" \
  "ds_read_b128 v[24:27], v102 offset:51200\n\t" \
  "ds_read_b128 v[28:31], v102 offset:52224\n\t" \
  "s_waitcnt lgkmcnt(4)\n\t" \
  "ds_read_b128 v[80:83], v102 offset:0\n\t" \
  "ds_read_b128 v[84:87], v102 offset:1024\n\t" \
  "ds_read_b128 v[88:91], v102 offset:2048\n\t" \
  "ds_read_b128 v[92:95], v102 offset:3072\n\t" \
  "46:\n\t" \
  "s_bitcmp1_b32 s58, 1\n\t" \
  "s_cselect_b64 s[80:81], -1, 0\n\t" \
  "s_cmp_lg_u32 s58, 0\n\t" \
  "s_cbranch_scc1 17f\n\t" \
  "s_mov_b64 exec, 0xff\n\t" \
  "16:\n\t" \
  "s_memtime vcc\n\t" \
  "s_waitcnt lgkmcnt(0)\n\t" \
  "s_sub_i32 s79, s41, vcc_lo\n\t" \
  "s_cmp_gt_i32 s79, 64\n\t" \
  "s_cbranch_scc1 16b\n\t" \
  "13:\n\t" \
  "buffer_load_dword v116, v112, %[rh], s48 offen sc1\n\t" \
  "s_waitcnt vmcnt(0)\n\t" \
  "15:\n\t" \
  "v_mov_b32 v117, 20000\n\t" \
  "v_cmp_ne_u32_sdwa vcc, v116, s49 src0_sel:BYTE_0 src1_sel:DWORD\n\t" \
  "v_subrev_u32 v116, s50, v116\n\t" \
  "v_ashrrev_i32 v116, 8, v116\n\t" \
  "s_nop 1\n\t" \
  "v_max_i32_dpp v116, v116, v116 quad_perm:[1,0,3,2] row_mask:0xf bank_mask:0xf\n\t" \
  "s_cbranch_vccnz 14f\n\t" \
  "s_nop 0\n\t" \
  "v_max_i32_dpp v116, v116, v116 quad_perm:[2,3,0,1] row_mask:0xf bank_mask:0xf\n\t" \
  "s_nop 1\n\t" \
  "v_max_i32_dpp v116, v116, v116 row_half_mirror row_mask:0xf bank_mask:0xf\n\t" \
  "v_med3_i32 v116, v116, 0, v117\n\t" \
  "v_mul_u32_u24 v116, s52, v116\n\t" \
  "v_lshrrev_b32 v116, 16, v116\n\t" \
  "s_mov_b64 exec, -1\n\t" \
  "v_readfirstlane_b32 s84, v116\n\t" \
  "s_add_i32 s84, s84, s51\n\t" \
  ".p2align 6\n\t" \
  "s_nop 0\n\t" \
  "s_nop 0\n\t" \
  "s_nop 0\n\t" \
  "s_nop 0\n\t" \
  "s_nop 0\n\t" \
  "s_nop 0\n\t" \
  "s_nop 0\n\t" \
  "s_nop 0\n\t" \
  "1:\n\t" \
  "s_memtime vcc\n\t" \
  "s_waitcnt lgkmcnt(0)\n\t" \
  "s_sub_i32 s79, s84, vcc_lo\n\t" \
  "s_cmp_gt_i32 s79, 128\n\t" \
  "s_cbranch_scc1 1b\n\t" \
  ".p2align 4\n\t" \
  "2:\n\t" \
  "s_sub_i32 s79, s79, 33\n\t" \
  "s_cmp_gt_i32 s79, 56\n\t" \
  "s_cbranch_scc1 2b\n\t" \
  "buffer_load_dwordx4 v[32:35], v100, %[rh], s40 offen sc1\n\t" \
  "s_nop 12\n\t" \
  "buffer_load_dwordx4 v[36:39], v100, %[rh], s76 offen sc1\n\t" \
  "s_nop 12\n\t" \
  "buffer_load_dwordx4 v[40:43], v100, %[rh], s77 offen sc1\n\t" \
  "s_nop 12\n\t" \
  "buffer_load_dwordx4 v[44:47], v100, %[rh], s78 offen sc1\n\t" \
  "s_memtime vcc\n\t" \
  "s_memrealtime s[82:83]\n\t" \
  "s_nop 4\n\t" \
  "s_memrealtime s[84:85]\n\t" \
  "s_nop 4\n\t" \
  "s_memrealtime s[56:57]\n\t" \
  "s_nop 4\n\t" \
  "s_memrealtime s[86:87]\n\t" \
  "s_nop 4\n\t" \
  "s_memrealtime s[88:89]\n\t" \
  "s_nop 4\n\t" \
  "s_memrealtime s[90:91]\n\t" \
  "s_mov_b32 s92, 0\n\t" \
  "s_waitcnt lgkmcnt(0)\n\t" \
  "s_cmp_eq_u32 s82, s84\n\t" \
  "s_addc_u32 s92, s92, 0\n\t" \
  "s_cmp_eq_u32 s82, s56\n\t" \
  "s_addc_u32 s92, s92, 0\n\t" \
  "s_cmp_eq_u32 s82, s86\n\t" \
  "s_addc_u32 s92, s92, 0\n\t" \
  "s_cmp_eq_u32 s82, s88\n\t" \
  "s_addc_u32 s92, s92, 0\n\t" \
  "s_cmp_eq_u32 s82, s90\n\t" \
  "s_addc_u32 s92, s92, 0\n\t" \
  "s_mul_i32 s92, s92, 24\n\t" \
  "s_add_i32 s92, s92, vcc_lo\n\t" \
  "s_lshl4_add_u32 s93, s82, 64\n\t" \
  "3:\n\t" \
  "v_mov_b64 v[72:73], 0\n\t" \
  "v_mov_b64 v[74:75], 0\n\t" \
  "v_mov_b64 v[76:77], 0\n\t" \
  "v_mov_b64 v[78:79], 0\n\t" \
  "s_waitcnt lgkmcnt(0)\n\t" \
  "s_waitcnt vmcnt(3)\n\t" \
  "v_smfmac_f32_16x16x64_bf16 v[72:75], v[32:35], v[48:55], v104\n\t" \
  "ds_write_b128 v101, v[32:35] offset:4096\n\t" \
  "v_smfmac_f32_16x16x64_bf16 v[76:79], v[32:35], v[56:63], v104\n\t" \
  "s_waitcnt vmcnt(2)\n\t" \
  "ds_write_b128 v101, v[36:39] offset:5120\n\t" \
  "v_smfmac_f32_16x16x64_bf16 v[72:75], v[36:39], a[48:55], v104\n\t" \
  "v_smfmac_f32_16x16x64_bf16 v[76:79], v[36:39], a[56:63], v104\n\t" \
  "s_nop 3\n\t" \
  "v_cmp_u_f32 vcc, v72, v73\n\t" \
  "v_cmp_u_f32 s[82:83], v74, v75\n\t" \
  "s_waitcnt vmcnt(1)\n\t" \
  "v_smfmac_f32_16x16x64_bf16 v[72:75], v[40:43], a[64:71], v104\n\t" \
  "s_or_b64 vcc, vcc, s[82:83]\n\t" \
  "s_or_b64 vcc, vcc, s[80:81]\n\t" \
  "v_smfmac_f32_16x16x64_bf16 v[76:79], v[40:43], a[72:79], v104\n\t" \
  "s_waitcnt lgkmcnt(0)\n\t" \
  "s_cbranch_scc1 8f\n\t" \
  "5:\n\t" \
  "s_barrier\n\t" \
  "4:\n\t" \
  "s_memtime s[56:57]\n\t" \
  "ds_read_b128 v[48:51], v101 offset:8192\n\t" \
  "ds_read_b128 v[52:55], v101 offset:9216\n\t" \
  "ds_read_b128 v[56:59], v101 offset:13312\n\t" \
  "s_waitcnt vmcnt(0)\n\t" \
  "v_smfmac_f32_16x16x64_bf16 v[72:75], v[44:47], a[80:87], v104\n\t" \
  "ds_read_b128 v[60:63], v101 offset:12288\n\t" \
  "ds_read_b128 v[64:67], v101 offset:1024\n\t" \
  "v_smfmac_f32_16x16x64_bf16 v[76:79], v[44:47], a[88:95], v104\n\t" \
  "ds_read_b128 v[68:71], v101 offset:0\n\t" \
  "ds_write_b128 v101, v[40:43] offset:6144\n\t" \
  "s_nop 1\n\t" \
  "v_cmp_u_f32 vcc, v72, v73\n\t" \
  "v_cmp_u_f32 s[82:83], v74, v75\n\t" \
  "s_waitcnt lgkmcnt(6)\n\t" \
  "v_smfmac_f32_16x16x64_bf16 v[72:75], v[48:51], v[0:7], v104\n\t" \
  "s_or_b64 vcc, vcc, s[82:83]\n\t" \
  "v_smfmac_f32_16x16x64_bf16 v[76:79], v[48:51], v[8:15], v104\n\t" \
  "s_cbranch_scc1 9f\n\t" \
  "6:\n\t" \
  "s_waitcnt lgkmcnt(5)\n\t" \
  "v_smfmac_f32_16x16x64_bf16 v[72:75], v[52:55], a[96:103], v104\n\t" \
  "ds_write_b128 v101, v[44:47] offset:7168\n\t" \
  "v_smfmac_f32_16x16x64_bf16 v[76:79], v[52:55], a[104:111], v104\n\t" \
  "v_mov_b64 v[40:41], 0\n\t" \
  "s_waitcnt lgkmcnt(5)\n\t" \
  "v_smfmac_f32_16x16x64_bf16 v[72:75], v[56:59], a[144:151], v104\n\t" \
  "v_mov_b64 v[42:43], 0\n\t" \
  "v_smfmac_f32_16x16x64_bf16 v[76:79], v[56:59], a[152:159], v104\n\t" \
  "s_waitcnt lgkmcnt(4)\n\t" \
  "v_smfmac_f32_16x16x64_bf16 v[72:75], v[60:63], v[16:23], v104\n\t" \
  "s_waitcnt lgkmcnt(0)\n\t" \
  "s_barrier\n\t" \
  "v_smfmac_f32_16x16x64_bf16 v[76:79], v[60:63], v[24:31], v104\n\t" \
  "ds_read_b128 v[48:51], v101 offset:10240\n\t" \
  "ds_read_b128 v[52:55], v101 offset:14336\n\t" \
  "v_smfmac_f32_16x16x64_bf16 v[72:75], v[64:67], a[0:7], v104\n\t" \
  "ds_read_b128 v[56:59], v101 offset:2048\n\t" \
  "ds_read_b128 v[60:63], v101 offset:11264\n\t" \
  "v_smfmac_f32_16x16x64_bf16 v[76:79], v[64:67], a[8:15], v104\n\t" \
  "ds_read_b128 v[32:35], v101 offset:15360\n\t" \
  "ds_read_b128 v[36:39], v101 offset:3072\n\t" \
  "v_smfmac_f32_16x16x64_bf16 v[72:75], v[68:71], v[80:87], v104\n\t" \
  "ds_read_b128 v[0:3], v103 offset:0\n\t" \
  "v_mov_b64 v[44:45], 0\n\t" \
  "v_smfmac_f32_16x16x64_bf16 v[76:79], v[68:71], v[88:95], v104\n\t" \
  "v_mov_b64 v[46:47], 0\n\t" \
  "s_waitcnt lgkmcnt(5)\n\t" \
  "v_smfmac_f32_16x16x64_bf16 v[72:75], v[48:51], a[112:119], v104\n\t" \
  "ds_read_b128 v[4:7], v103 offset:1024\n\t" \
  "v_mov_b64 v[80:81], 0\n\t" \
  "v_smfmac_f32_16x16x64_bf16 v[72:75], v[52:55], a[160:167], v104\n\t" \
  "v_mov_b64 v[82:83], 0\n\t" \
  "s_waitcnt lgkmcnt(2)\n\t" \
  "v_smfmac_f32_16x16x64_bf16 v[72:75], v[56:59], a[16:23], v104\n\t" \
  "ds_read_b128 v[8:11], v103 offset:2048\n\t" \
  "v_mov_b64 v[84:85], 0\n\t" \
  "v_smfmac_f32_16x16x64_bf16 v[72:75], v[60:63], a[128:135], v104\n\t" \
  "ds_read_b128 v[12:15], v103 offset:3072\n\t" \
  "v_mov_b64 v[86:87], 0\n\t" \
  "v_smfmac_f32_16x16x64_bf16 v[72:75], v[32:35], a[176:183], v104\n\t" \
  "ds_read_b128 v[16:19], v103 offset:16384\n\t" \
  "v_mov_b64 v[88:89], 0\n\t" \
  "v_smfmac_f32_16x16x64_bf16 v[72:75], v[36:39], a[32:39], v104\n\t" \
  "ds_read_b128 v[20:23], v103 offset:17408\n\t" \
  "v_mov_b64 v[90:91], 0\n\t" \
  "v_smfmac_f32_16x16x64_bf16 v[76:79], v[48:51], a[120:127], v104\n\t" \
  "ds_read_b128 v[24:27], v103 offset:18432\n\t" \
  "v_mov_b64 v[92:93], 0\n\t" \
  "v_smfmac_f32_16x16x64_bf16 v[76:79], v[52:55], a[168:175], v104\n\t" \
  "v_add_f32 v96, v72, v74\n\t" \
  "v_add_f32 v97, v73, v75\n\t" \
  "v_cvt_pk_bf16_f32 v96, v96, v97\n\t" \
  "ds_write_b32 v105, v96\n\t" \
  "v_smfmac_f32_16x16x64_bf16 v[76:79], v[56:59], a[24:31], v104\n\t" \
  "ds_read_b128 v[28:31], v103 offset:19456\n\t" \
  "v_mov_b64 v[94:95], 0\n\t" \
  "v_smfmac_f32_16x16x64_bf16 v[76:79], v[60:63], a[136:143], v104\n\t" \
  "v_smfmac_f32_16x16x64_bf16 v[76:79], v[32:35], a[184:191], v104\n\t" \
  "v_smfmac_f32_16x16x64_bf16 v[76:79], v[36:39], a[40:47], v104\n\t" \
  "s_waitcnt lgkmcnt(1)\n\t" \
  "s_barrier\n\t" \
  "ds_read_b64_tr_b16 v[96:97], v107 offset:0\n\t" \
  "ds_read_b64_tr_b16 v[98:99], v107 offset:64\n\t" \
  "ds_read_b64_tr_b16 v[56:57], v107 offset:1024\n\t" \
  "ds_read_b64_tr_b16 v[58:59], v107 offset:1088\n\t" \
  "s_nop 1\n\t" \
  "v_add_f32 v116, v76, v78\n\t" \
  "v_add_f32 v117, v77, v79\n\t" \
  "v_cvt_pk_bf16_f32 v116, v116, v117\n\t" \
  "ds_write_b32 v105, v116 offset:256\n\t" \
  "s_waitcnt lgkmcnt(3)\n\t" \
  "v_smfmac_f32_16x16x64_bf16 v[40:43], v[96:99], a[192:199], v106\n\t" \
  "v_smfmac_f32_16x16x64_bf16 v[44:47], v[96:99], a[224:231], v106\n\t" \
  "s_waitcnt lgkmcnt(0)\n\t" \
  "s_barrier\n\t" \
  "ds_read_b64_tr_b16 v[52:53], v107 offset:256\n\t" \
  "ds_read_b64_tr_b16 v[54:55], v107 offset:320\n\t" \
  "v_smfmac_f32_16x16x64_bf16 v[40:43], v[56:59], a[208:215], v106\n\t" \
  "ds_read_b64_tr_b16 v[60:61], v107 offset:1280\n\t" \
  "ds_read_b64_tr_b16 v[62:63], v107 offset:1344\n\t" \
  "v_smfmac_f32_16x16x64_bf16 v[44:47], v[56:59], v[0:7], v106\n\t" \
  "v_smfmac_f32_16x16x64_bf16 v[80:83], v[96:99], a[240:247], v106\n\t" \
  "v_mov_b64 v[32:33], 0\n\t" \
  "v_smfmac_f32_16x16x64_bf16 v[84:87], v[96:99], v[136:143], v106\n\t" \
  "v_mov_b64 v[34:35], 0\n\t" \
  "v_smfmac_f32_16x16x64_bf16 v[80:83], v[56:59], v[120:127], v106\n\t" \
  "v_mov_b64 v[36:37], 0\n\t" \
  "v_smfmac_f32_16x16x64_bf16 v[84:87], v[56:59], v[16:23], v106\n\t" \
  "s_waitcnt lgkmcnt(2)\n\t" \
  "v_smfmac_f32_16x16x64_bf16 v[40:43], v[52:55], a[200:207], v106\n\t" \
  "v_smfmac_f32_16x16x64_bf16 v[44:47], v[52:55], a[232:239], v106\n\t" \
  "s_xor_b32 s76, s47, 0x40000\n\t" \
  "s_waitcnt lgkmcnt(0)\n\t" \
  "v_smfmac_f32_16x16x64_bf16 v[40:43], v[60:63], a[216:223], v106\n\t" \
  "v_mov_b64 v[38:39], 0\n\t" \
  "s_xor_b32 s77, s47, 0x80000\n\t" \
  "v_smfmac_f32_16x16x64_bf16 v[44:47], v[60:63], v[8:15], v106\n\t" \
  "s_xor_b32 s78, s47, 0xc0000\n\t" \
  "v_smfmac_f32_16x16x64_bf16 v[80:83], v[52:55], a[248:255], v106\n\t" \
  "ds_read_b128 v[0:3], v103 offset:32768\n\t" \
  "v_cvt_pk_bf16_f32 v64, v40, v41\n\t" \
  "v_smfmac_f32_16x16x64_bf16 v[84:87], v[52:55], v[144:151], v106\n\t" \
  "ds_read_b128 v[4:7], v103 offset:33792\n\t" \
  "v_cvt_pk_bf16_f32 v65, v42, v43\n\t" \
  "v_smfmac_f32_16x16x64_bf16 v[80:83], v[60:63], v[128:135], v106\n\t" \
  "ds_read_b128 v[8:11], v103 offset:34816\n\t" \
  "v_cvt_pk_bf16_f32 v66, v44, v45\n\t" \
  "v_smfmac_f32_16x16x64_bf16 v[84:87], v[60:63], v[24:31], v106\n\t" \
  "ds_read_b128 v[12:15], v103 offset:35840\n\t" \
  "v_cvt_pk_bf16_f32 v67, v46, v47\n\t" \
  "v_smfmac_f32_16x16x64_bf16 v[88:91], v[96:99], v[152:159], v106\n\t" \
  "v_permlane16_swap_b32 v64, v66\n\t" \
  "v_cvt_pk_bf16_f32 v68, v80, v81\n\t" \
  "v_smfmac_f32_16x16x64_bf16 v[92:95], v[96:99], v[184:191], v106\n\t" \
  "v_permlane16_swap_b32 v65, v67\n\t" \
  "v_cvt_pk_bf16_f32 v69, v82, v83\n\t" \
  "v_smfmac_f32_16x16x64_bf16 v[88:91], v[52:55], v[160:167], v106\n\t" \
  "buffer_store_dwordx4 v[64:67], v108, %[ru], s47 offen sc1\n\t" \
  "v_cvt_pk_bf16_f32 v70, v84, v85\n\t" \
  "v_smfmac_f32_16x16x64_bf16 v[92:95], v[52:55], v[192:199], v106\n\t" \
  "v_cvt_pk_bf16_f32 v71, v86, v87\n\t" \
  "v_smfmac_f32_16x16x64_bf16 v[88:91], v[56:59], v[168:175], v106\n\t" \
  "ds_read_b128 v[16:19], v103 offset:49152\n\t" \
  "v_permlane16_swap_b32 v68, v70\n\t" \
  "s_waitcnt lgkmcnt(3)\n\t" \
  "v_smfmac_f32_16x16x64_bf16 v[92:95], v[56:59], v[0:7], v106\n\t" \
  "ds_read_b128 v[20:23], v103 offset:50176\n\t" \
  "v_permlane16_swap_b32 v69, v71\n\t" \
  "v_smfmac_f32_16x16x64_bf16 v[88:91], v[60:63], v[176:183], v106\n\t" \
  "ds_read_b128 v[24:27], v103 offset:51200\n\t" \
  "buffer_store_dwordx4 v[68:71], v108, %[ru], s76 offen sc1\n\t" \
  "s_waitcnt lgkmcnt(3)\n\t" \
  "v_smfmac_f32_16x16x64_bf16 v[92:95], v[60:63], v[8:15], v106\n\t" \
  "ds_read_b128 v[28:31], v103 offset:52224\n\t" \
  "v_smfmac_f32_16x16x64_bf16 v[32:35], v[96:99], v[200:207], v106\n\t" \
  "v_cvt_pk_bf16_f32 v72, v88, v89\n\t" \
  "v_smfmac_f32_16x16x64_bf16 v[36:39], v[96:99], v[232:239], v106\n\t" \
  "v_cvt_pk_bf16_f32 v73, v90, v91\n\t" \
  "v_smfmac_f32_16x16x64_bf16 v[32:35], v[52:55], v[208:215], v106\n\t" \
  "v_cvt_pk_bf16_f32 v74, v92, v93\n\t" \
  "v_smfmac_f32_16x16x64_bf16 v[36:39], v[52:55], v[240:247], v106\n\t" \
  "v_cvt_pk_bf16_f32 v75, v94, v95\n\t" \
  "v_smfmac_f32_16x16x64_bf16 v[32:35], v[56:59], v[216:223], v106\n\t" \
  "v_permlane16_swap_b32 v72, v74\n\t" \
  "v_smfmac_f32_16x16x64_bf16 v[32:35], v[60:63], v[224:231], v106\n\t" \
  "v_permlane16_swap_b32 v73, v75\n\t" \
  "s_waitcnt lgkmcnt(2)\n\t" \
  "v_smfmac_f32_16x16x64_bf16 v[36:39], v[56:59], v[16:23], v106\n\t" \
  "buffer_store_dwordx4 v[72:75], v108, %[ru], s77 offen sc1\n\t" \
  "s_waitcnt lgkmcnt(0)\n\t" \
  "v_smfmac_f32_16x16x64_bf16 v[36:39], v[60:63], v[24:31], v106\n\t" \
  "v_cvt_pk_bf16_f32 v76, v32, v33\n\t" \
  "v_cvt_pk_bf16_f32 v77, v34, v35\n\t" \
  "s_nop 5\n\t" \
  "v_cvt_pk_bf16_f32 v78, v36, v37\n\t" \
  "v_cvt_pk_bf16_f32 v79, v38, v39\n\t" \
  "s_nop 0\n\t" \
  "v_permlane16_swap_b32 v76, v78\n\t" \
  "v_permlane16_swap_b32 v77, v79\n\t" \
  "buffer_store_dwordx4 v[76:79], v108, %[ru], s78 offen sc1\n\t" \
  "s_memtime s[76:77]\n\t" \
  "s_mov_b32 s42, 0\n\t" \
  "s_waitcnt lgkmcnt(0)\n\t" \
  "s_mov_b32 s57, s76\n\t" \
  "s_add_i32 s41, s56, 1950\n\t" \
  "s_sub_i32 s76, s92, s51\n\t" \
  "s_sub_i32 s77, s93, s53\n\t" \
  "s_cmp_eq_u32 s76, 0\n\t" \
  "s_cbranch_scc1 21f\n\t" \
  "v_cvt_f32_u32 v117, s76\n\t" \
  "v_cvt_f32_u32 v116, s77\n\t" \
  "v_rcp_f32 v117, v117\n\t" \
  "s_mov_b32 s51, s92\n\t" \
  "v_mul_f32 v117, v117, v116\n\t" \
  "v_max_f32 v117, 0x3f028f5c, v117\n\t" \
  "v_min_f32 v117, 0x3ffeb852, v117\n\t" \
  "v_rcp_f32 v116, v117\n\t" \
  "v_mul_f32 v117, 0x4f000000, v117\n\t" \
  "v_mul_f32 v116, 0x47800000, v116\n\t" \
  "v_cvt_u32_f32 v117, v117\n\t" \
  "v_cvt_u32_f32 v116, v116\n\t" \
  "s_mov_b32 s53, s93\n\t" \
  "v_readfirstlane_b32 s46, v117\n\t" \
  "v_readfirstlane_b32 s52, v116\n\t" \
  "21:\n\t" \
  "s_add_i32 s44, s44, 1\n\t" \
  "s_and_b32 s58, s58, 4\n\t" \
  "s_and_b32 s76, s44, 64\n\t" \
  "s_or_b32 s42, s42, s76\n\t" \
  "s_cbranch_scc0 20b\n\t" \
  "s_branch 99f\n\t" \
  "14:\n\t" \
  "s_sleep 3\n\t" \
  "s_add_u32 s59, s59, 0x10000\n\t" \
  "s_cmp_lt_u32 s59, 0x400000\n\t" \
  "s_cbranch_scc1 13b\n\t" \
  "s_mov_b64 exec, -1\n\t" \
  "s_mov_b32 s42, 16\n\t" \
  "s_branch 99f\n\t" \
  "8:\n\t" \
  "s_bitcmp1_b32 s58, 1\n\t" \
  "s_cbranch_scc1 4b\n\t" \
  "s_bitcmp1_b32 s58, 2\n\t" \
  "s_cbranch_scc1 5b\n\t" \
  "v_max3_u32 v116, v32, v33, v34\n\t" \
  "v_max3_u32 v117, v35, v36, v37\n\t" \
  "v_max3_u32 v116, v116, v38, v39\n\t" \
  "v_max_u32 v116, v116, v117\n\t" \
  "v_cmp_eq_u32 vcc, -1, v116\n\t" \
  "s_cbranch_vccz 5b\n\t" \
  "s_branch 0f\n\t" \
  "9:\n\t" \
  "s_bitcmp1_b32 s58, 2\n\t" \
  "s_cbranch_scc1 6b\n\t" \
  "v_max3_u32 v116, v40, v41, v42\n\t" \
  "v_max3_u32 v117, v43, v44, v45\n\t" \
  "v_max3_u32 v116, v116, v46, v47\n\t" \
  "v_max_u32 v116, v116, v117\n\t" \
  "v_cmp_eq_u32 vcc, -1, v116\n\t" \
  "s_cbranch_vccz 6b\n\t" \
  "s_or_b32 s58, s58, 2\n\t" \
  "0:\n\t" \
  "s_or_b32 s58, s58, 1\n\t" \
  "11:\n\t" \
  "buffer_load_dwordx4 v[32:35], v100, %[rh], s40 offen sc1\n\t" \
  "buffer_load_dwordx4 v[36:39], v100, %[rh], s76 offen sc1\n\t" \
  "buffer_load_dwordx4 v[40:43], v100, %[rh], s77 offen sc1\n\t" \
  "buffer_load_dwordx4 v[44:47], v100, %[rh], s78 offen sc1\n\t" \
  "s_waitcnt lgkmcnt(0)\n\t" \
  "s_branch 10b\n\t" \
  "17:\n\t" \
  "s_add_u32 s59, s59, 1\n\t" \
  "s_bitcmp1_b32 s58, 2\n\t" \
  "s_cbranch_scc1 3b\n\t" \
  "s_waitcnt vmcnt(0)\n\t" \
  "v_max_u32 v116, v32, v33\n\t" \
  "v_max3_u32 v116, v116, v34, v35\n\t" \
  "v_max3_u32 v116, v116, v36, v37\n\t" \
  "v_max3_u32 v116, v116, v38, v39\n\t" \
  "v_max3_u32 v116, v116, v40, v41\n\t" \
  "v_max3_u32 v116, v116, v42, v43\n\t" \
  "v_max3_u32 v116, v116, v44, v45\n\t" \
  "v_max3_u32 v116, v116, v46, v47\n\t" \
  "v_cmp_eq_u32 vcc, -1, v116\n\t" \
  "s_cbranch_vccz 3b\n\t" \
  "s_bitcmp1_b32 s59, 6\n\t" \
  "s_cbranch_scc1 12f\n\t" \
  "buffer_load_dwordx4 v[32:35], v100, %[rh], s40 offen sc1\n\t" \
  "buffer_load_dwordx4 v[36:39], v100, %[rh], s76 offen sc1\n\t" \
  "buffer_load_dwordx4 v[40:43], v100, %[rh], s77 offen sc1\n\t" \
  "buffer_load_dwordx4 v[44:47], v100, %[rh], s78 offen sc1\n\t" \
  "s_branch 17b\n\t" \
  "12:\n\t" \
  "s_mov_b32 s42, s58\n\t" \
  "s_branch 99f\n\t" \
  "92:\n\t" \
  "s_mov_b32 s58, s42\n\t" \
  "20:\n\t" \
  "s_and_b32 s76, s44, 7\n\t" \
  "s_lshl_b32 s76, s76, 15\n\t" \
  "s_add_i32 s40, s54, s76\n\t" \
  "s_and_b32 s76, s44, 3\n\t" \
  "s_lshl_b32 s45, s76, 14\n\t" \
  "s_lshl_b32 s47, s76, 20\n\t" \
  "s_add_i32 s47, s47, s55\n\t" \
  "s_lshr_b32 s76, s47, 21\n\t" \
  "s_bitcmp1_b64 %[sel], s76\n\t" \
  "s_cselect_b32 s76, 0x2000, 0\n\t" \
  "s_add_i32 s47, s47, s76\n\t" \
  "s_add_i32 s49, s44, -1\n\t" \
  "s_and_b32 s48, s49, 2\n\t" \
  "s_lshl_b32 s48, s48, 13\n\t" \
  "s_and_b32 s76, s49, 1\n\t" \
  "s_lshl_b32 s76, s76, 12\n\t" \
  "s_add_i32 s48, s48, s76\n\t" \
  "s_lshl_b32 s50, s53, 8\n\t" \
  "s_mov_b32 s59, 0\n\t" \
  "s_mov_b32 s92, s51\n\t" \
  "s_mov_b32 s93, s53\n\t" \
  "s_xor_b32 s76, s40, 0x400\n\t" \
  "s_xor_b32 s77, s40, 0x800\n\t" \
  "s_xor_b32 s78, s40, 0xc00\n\t" \
  "s_cmp_lg_u32 s58, 0\n\t" \
  "s_cbranch_scc1 11f\n\t" \
  "10:\n\t" \
  "ds_read_b128 v[48:51], v102 offset:32768\n\t" \
  "ds_read_b128 v[52:55], v102 offset:33792\n\t" \
  "ds_read_b128 v[56:59], v102 offset:34816\n\t" \
  "ds_read_b128 v[60:63], v102 offset:35840\n\t" \
  "ds_read_b128 v[0:3], v102 offset:49152\n\t" \
  "ds_read_b128 v[4:7], v102 offset:50176\n\t" \
  "ds_read_b128 v[8:11], v102 offset:51200\n\t" \
  "ds_read_b128 v[12:15], v102 offset:52224\n\t" \
  "ds_read_b128 v[16:19], v102 offset:0\n\t" \
  "ds_read_b128 v[20:23], v102 offset:1024\n\t" \
  "ds_read_b128 v[24:27], v102 offset:2048\n\t" \
  "ds_read_b128 v[28:31], v102 offset:3072\n\t" \
  "s_waitcnt lgkmcnt(4)\n\t" \
  "ds_read_b128 v[80:83], v102 offset:16384\n\t" \
  "ds_read_b128 v[84:87], v102 offset:17408\n\t" \
  "ds_read_b128 v[88:91], v102 offset:18432\n\t" \
  "ds_read_b128 v[92:95], v102 offset:19456\n\t" \
  "46:\n\t" \
  "s_bitcmp1_b32 s58, 1\n\t" \
  "s_cselect_b64 s[80:81], -1, 0\n\t" \
  "s_cmp_lg_u32 s58, 0\n\t" \
  "s_cbranch_scc1 17f\n\t" \
  "s_mov_b64 exec, 0xff\n\t" \
  "16:\n\t" \
  "s_memtime vcc\n\t" \
  "s_waitcnt lgkmcnt(0)\n\t" \
  "s_sub_i32 s79, s41, vcc_lo\n\t" \
  "s_cmp_gt_i32 s79, 64\n\t" \
  "s_cbranch_scc1 16b\n\t" \
  "13:\n\t" \
  "buffer_load_dword v116, v112, %[rh], s48 offen sc1\n\t" \
  "s_waitcnt vmcnt(0)\n\t" \
  "15:\n\t" \
  "v_mov_b32 v117, 20000\n\t" \
  "v_cmp_ne_u32_sdwa vcc, v116, s49 src0_sel:BYTE_0 src1_sel:DWORD\n\t" \
  "v_subrev_u32 v116, s50, v116\n\t" \
  "v_ashrrev_i32 v116, 8, v116\n\t" \
  "s_nop 1\n\t" \
  "v_max_i32_dpp v116, v116, v116 quad_perm:[1,0,3,2] row_mask:0xf bank_mask:0xf\n\t" \
  "s_cbranch_vccnz 14f\n\t" \
  "s_nop 0\n\t" \
  "v_max_i32_dpp v116, v116, v116 quad_perm:[2,3,0,1] row_mask:0xf bank_mask:0xf\n\t" \
  "s_nop 1\n\t" \
  "v_max_i32_dpp v116, v116, v116 row_half_mirror row_mask:0xf bank_mask:0xf\n\t" \
  "v_med3_i32 v116, v116, 0, v117\n\t" \
  "v_mul_u32_u24 v116, s52, v116\n\t" \
  "v_lshrrev_b32 v116, 16, v116\n\t" \
  "s_mov_b64 exec, -1\n\t" \
  "v_readfirstlane_b32 s84, v116\n\t" \
  "s_add_i32 s84, s84, s51\n\t" \
  ".p2align 6\n\t" \
  "s_nop 0\n\t" \
  "s_nop 0\n\t" \
  "s_nop 0\n\t" \
  "s_nop 0\n\t" \
  "s_nop 0\n\t" \
  "s_nop 0\n\t" \
  "s_nop 0\n\t" \
  "s_nop 0\n\t" \
  "1:\n\t" \
  "s_memtime vcc\n\t" \
  "s_waitcnt lgkmcnt(0)\n\t" \
  "s_sub_i32 s79, s84, vcc_lo\n\t" \
  "s_cmp_gt_i32 s79, 128\n\t" \
  "s_cbranch_scc1 1b\n\t" \
  ".p2align 4\n\t" \
  "2:\n\t" \
  "s_sub_i32 s79, s79, 33\n\t" \
  "s_cmp_gt_i32 s79, 56\n\t" \
  "s_cbranch_scc1 2b\n\t" \
  "buffer_load_dwordx4 v[32:35], v100, %[rh], s40 offen sc1\n\t" \
  "s_nop 12\n\t" \
  "buffer_load_dwordx4 v[36:39], v100, %[rh], s76 offen sc1\n\t" \
  "s_nop 12\n\t" \
  "buffer_load_dwordx4 v[40:43], v100, %[rh], s77 offen sc1\n\t" \
  "s_nop 12\n\t" \
  "buffer_load_dwordx4 v[44:47], v100, %[rh], s78 offen sc1\n\t" \
  "s_memtime vcc\n\t" \
  "s_memrealtime s[82:83]\n\t" \
  "s_nop 4\n\t" \
  "s_memrealtime s[84:85]\n\t" \
  "s_nop 4\n\t" \
  "s_memrealtime s[56:57]\n\t" \
  "s_nop 4\n\t" \
  "s_memrealtime s[86:87]\n\t" \
  "s_nop 4\n\t" \
  "s_memrealtime s[88:89]\n\t" \
  "s_nop 4\n\t" \
  "s_memrealtime s[90:91]\n\t" \
  "s_mov_b32 s92, 0\n\t" \
  "s_waitcnt lgkmcnt(0)\n\t" \
  "s_cmp_eq_u32 s82, s84\n\t" \
  "s_addc_u32 s92, s92, 0\n\t" \
  "s_cmp_eq_u32 s82, s56\n\t" \
  "s_addc_u32 s92, s92, 0\n\t" \
  "s_cmp_eq_u32 s82, s86\n\t" \
  "s_addc_u32 s92, s92, 0\n\t" \
  "s_cmp_eq_u32 s82, s88\n\t" \
  "s_addc_u32 s92, s92, 0\n\t" \
  "s_cmp_eq_u32 s82, s90\n\t" \
  "s_addc_u32 s92, s92, 0\n\t" \
  "s_mul_i32 s92, s92, 24\n\t" \
  "s_add_i32 s92, s92, vcc_lo\n\t" \
  "s_lshl4_add_u32 s93, s82, 64\n\t" \
  "3:\n\t" \
  "v_mov_b64 v[72:73], 0\n\t" \
  "v_mov_b64 v[74:75], 0\n\t" \
  "v_mov_b64 v[76:77], 0\n\t" \
  "v_mov_b64 v[78:79], 0\n\t" \
  "s_waitcnt lgkmcnt(0)\n\t" \
  "s_waitcnt vmcnt(3)\n\t" \
  "v_smfmac_f32_16x16x64_bf16 v[72:75], v[32:35], v[48:55], v104\n\t" \
  "ds_write_b128 v101, v[32:35] offset:8192\n\t" \
  "v_smfmac_f32_16x16x64_bf16 v[76:79], v[32:35], v[56:63], v104\n\t" \
  "s_waitcnt vmcnt(2)\n\t" \
  "ds_write_b128 v101, v[36:39] offset:9216\n\t" \
  "v_smfmac_f32_16x16x64_bf16 v[72:75], v[36:39], a[96:103], v104\n\t" \
  "v_smfmac_f32_16x16x64_bf16 v[76:79], v[36:39], a[104:111], v104\n\t" \
  "s_nop 3\n\t" \
  "v_cmp_u_f32 vcc, v72, v73\n\t" \
  "v_cmp_u_f32 s[82:83], v74, v75\n\t" \
  "s_waitcnt vmcnt(1)\n\t" \
  "v_smfmac_f32_16x16x64_bf16 v[72:75], v[40:43], a[112:119], v104\n\t" \
  "s_or_b64 vcc, vcc, s[82:83]\n\t" \
  "s_or_b64 vcc, vcc, s[80:81]\n\t" \
  "v_smfmac_f32_16x16x64_bf16 v[76:79], v[40:43], a[120:127], v104\n\t" \
  "s_waitcnt lgkmcnt(0)\n\t" \
  "s_cbranch_scc1 8f\n\t" \
  "5:\n\t" \
  "s_barrier\n\t" \
  "4:\n\t" \
  "s_memtime s[56:57]\n\t" \
  "ds_read_b128 v[48:51], v101 offset:12288\n\t" \
  "ds_read_b128 v[52:55], v101 offset:13312\n\t" \
  "ds_read_b128 v[56:59], v101 offset:1024\n\t" \
  "s_waitcnt vmcnt(0)\n\t" \
  "v_smfmac_f32_16x16x64_bf16 v[72:75], v[44:47], a[128:135], v104\n\t" \
  "ds_read_b128 v[60:63], v101 offset:0\n\t" \
  "ds_read_b128 v[64:67], v101 offset:5120\n\t" \
  "v_smfmac_f32_16x16x64_bf16 v[76:79], v[44:47], a[136:143], v104\n\t" \
  "ds_read_b128 v[68:71], v101 offset:4096\n\t" \
  "ds_write_b128 v101, v[40:43] offset:10240\n\t" \
  "s_nop 1\n\t" \
  "v_cmp_u_f32 vcc, v72, v73\n\t" \
  "v_cmp_u_f32 s[82:83], v74, v75\n\t" \
  "s_waitcnt lgkmcnt(6)\n\t" \
  "v_smfmac_f32_16x16x64_bf16 v[72:75], v[48:51], v[0:7], v104\n\t" \
  "s_or_b64 vcc, vcc, s[82:83]\n\t" \
  "v_smfmac_f32_16x16x64_bf16 v[76:79], v[48:51], v[8:15], v104\n\t" \
  "s_cbranch_scc1 9f\n\t" \
  "6:\n\t" \
  "s_waitcnt lgkmcnt(5)\n\t" \
  "v_smfmac_f32_16x16x64_bf16 v[72:75], v[52:55], a[144:151], v104\n\t" \
  "ds_write_b128 v101, v[44:47] offset:11264\n\t" \
  "v_smfmac_f32_16x16x64_bf16 v[76:79], v[52:55], a[152:159], v104\n\t" \
  "v_mov_b64 v[40:41], 0\n\t" \
  "s_waitcnt lgkmcnt(5)\n\t" \
  "v_smfmac_f32_16x16x64_bf16 v[72:75], v[56:59], a[0:7], v104\n\t" \
  "v_mov_b64 v[42:43], 0\n\t" \
  "v_smfmac_f32_16x16x64_bf16 v[76:79], v[56:59], a[8:15], v104\n\t" \
  "s_waitcnt lgkmcnt(4)\n\t" \
  "v_smfmac_f32_16x16x64_bf16 v[72:75], v[60:63], v[16:23], v104\n\t" \
  "s_waitcnt lgkmcnt(0)\n\t" \
  "s_barrier\n\t" \
  "v_smfmac_f32_16x16x64_bf16 v[76:79], v[60:63], v[24:31], v104\n\t" \
  "ds_read_b128 v[48:51], v101 offset:14336\n\t" \
  "ds_read_b128 v[52:55], v101 offset:2048\n\t" \
  "v_smfmac_f32_16x16x64_bf16 v[72:75], v[64:67], a[48:55], v104\n\t" \
  "ds_read_b128 v[56:59], v101 offset:6144\n\t" \
  "ds_read_b128 v[60:63], v101 offset:15360\n\t" \
  "v_smfmac_f32_16x16x64_bf16 v[76:79], v[64:67], a[56:63], v104\n\t" \
  "ds_read_b128 v[32:35], v101 offset:3072\n\t" \
  "ds_read_b128 v[36:39], v101 offset:7168\n\t" \
  "v_smfmac_f32_16x16x64_bf16 v[72:75], v[68:71], v[80:87], v104\n\t" \
  "ds_read_b128 v[0:3], v103 offset:0\n\t" \
  "v_mov_b64 v[44:45], 0\n\t" \
  "v_smfmac_f32_16x16x64_bf16 v[76:79], v[68:71], v[88:95], v104\n\t" \
  "v_mov_b64 v[46:47], 0\n\t" \
  "s_waitcnt lgkmcnt(5)\n\t" \
  "v_smfmac_f32_16x16x64_bf16 v[72:75], v[48:51], a[160:167], v104\n\t" \
  "ds_read_b128 v[4:7], v103 offset:1024\n\t" \
  "v_mov_b64 v[80:81], 0\n\t" \
  "v_smfmac_f32_16x16x64_bf16 v[72:75], v[52:55], a[16:23], v104\n\t" \
  "v_mov_b64 v[82:83], 0\n\t" \
  "s_waitcnt lgkmcnt(2)\n\t" \
  "v_smfmac_f32_16x16x64_bf16 v[72:75], v[56:59], a[64:71], v104\n\t" \
  "ds_read_b128 v[8:11], v103 offset:2048\n\t" \
  "v_mov_b64 v[84:85], 0\n\t" \
  "v_smfmac_f32_16x16x64_bf16 v[72:75], v[60:63], a[176:183], v104\n\t" \
  "ds_read_b128 v[12:15], v103 offset:3072\n\t" \
  "v_mov_b64 v[86:87], 0\n\t" \
  "v_smfmac_f32_16x16x64_bf16 v[72:75], v[32:35], a[32:39], v104\n\t" \
  "ds_read_b128 v[16:19], v103 offset:16384\n\t" \
  "v_mov_b64 v[88:89], 0\n\t" \
  "v_smfmac_f32_16x16x64_bf16 v[72:75], v[36:39], a[80:87], v104\n\t" \
  "ds_read_b128 v[20:23], v103 offset:17408\n\t" \
  "v_mov_b64 v[90:91], 0\n\t" \
  "v_smfmac_f32_16x16x64_bf16 v[76:79], v[48:51], a[168:175], v104\n\t" \
  "ds_read_b128 v[24:27], v103 offset:18432\n\t" \
  "v_mov_b64 v[92:93], 0\n\t" \
  "v_smfmac_f32_16x16x64_bf16 v[76:79], v[52:55], a[24:31], v104\n\t" \
  "v_add_f32 v96, v72, v74\n\t" \
  "v_add_f32 v97, v73, v75\n\t" \
  "v_cvt_pk_bf16_f32 v96, v96, v97\n\t" \
  "ds_write_b32 v105, v96\n\t" \
  "v_smfmac_f32_16x16x64_bf16 v[76:79], v[56:59], a[72:79], v104\n\t" \
  "ds_read_b128 v[28:31], v103 offset:19456\n\t" \
  "v_mov_b64 v[94:95], 0\n\t" \
  "v_smfmac_f32_16x16x64_bf16 v[76:79], v[60:63], a[184:191], v104\n\t" \
  "v_smfmac_f32_16x16x64_bf16 v[76:79], v[32:35], a[40:47], v104\n\t" \
  "v_smfmac_f32_16x16x64_bf16 v[76:79], v[36:39], a[88:95], v104\n\t" \
  "s_waitcnt lgkmcnt(1)\n\t" \
  "s_barrier\n\t" \
  "ds_read_b64_tr_b16 v[96:97], v107 offset:0\n\t" \
  "ds_read_b64_tr_b16 v[98:99], v107 offset:64\n\t" \
  "ds_read_b64_tr_b16 v[56:57], v107 offset:1024\n\t" \
  "ds_read_b64_tr_b16 v[58:59], v107 offset:1088\n\t" \
  "s_nop 1\n\t" \
  "v_add_f32 v116, v76, v78\n\t" \
  "v_add_f32 v117, v77, v79\n\t" \
  "v_cvt_pk_bf16_f32 v116, v116, v117\n\t" \
  "ds_write_b32 v105, v116 offset:256\n\t" \
  "s_waitcnt lgkmcnt(3)\n\t" \
  "v_smfmac_f32_16x16x64_bf16 v[40:43], v[96:99], a[192:199], v106\n\t" \
  "v_smfmac_f32_16x16x64_bf16 v[44:47], v[96:99], a[224:231], v106\n\t" \
  "s_waitcnt lgkmcnt(0)\n\t" \
  "s_barrier\n\t" \
  "ds_read_b64_tr_b16 v[52:53], v107 offset:256\n\t" \
  "ds_read_b64_tr_b16 v[54:55], v107 offset:320\n\t" \
  "v_smfmac_f32_16x16x64_bf16 v[40:43], v[56:59], a[208:215], v106\n\t" \
  "ds_read_b64_tr_b16 v[60:61], v107 offset:1280\n\t" \
  "ds_read_b64_tr_b16 v[62:63], v107 offset:1344\n\t" \
  "v_smfmac_f32_16x16x64_bf16 v[44:47], v[56:59], v[0:7], v106\n\t" \
  "v_smfmac_f32_16x16x64_bf16 v[80:83], v[96:99], a[240:247], v106\n\t" \
  "v_mov_b64 v[32:33], 0\n\t" \
  "v_smfmac_f32_16x16x64_bf16 v[84:87], v[96:99], v[136:143], v106\n\t" \
  "v_mov_b64 v[34:35], 0\n\t" \
  "v_smfmac_f32_16x16x64_bf16 v[80:83], v[56:59], v[120:127], v106\n\t" \
  "v_mov_b64 v[36:37], 0\n\t" \
  "v_smfmac_f32_16x16x64_bf16 v[84:87], v[56:59], v[16:23], v106\n\t" \
  "s_waitcnt lgkmcnt(2)\n\t" \
  "v_smfmac_f32_16x16x64_bf16 v[40:43], v[52:55], a[200:207], v106\n\t" \
  "v_smfmac_f32_16x16x64_bf16 v[44:47], v[52:55], a[232:239], v106\n\t" \
  "s_xor_b32 s76, s47, 0x40000\n\t" \
  "s_waitcnt lgkmcnt(0)\n\t" \
  "v_smfmac_f32_16x16x64_bf16 v[40:43], v[60:63], a[216:223], v106\n\t" \
  "v_mov_b64 v[38:39], 0\n\t" \
  "s_xor_b32 s77, s47, 0x80000\n\t" \
  "v_smfmac_f32_16x16x64_bf16 v[44:47], v[60:63], v[8:15], v106\n\t" \
  "s_xor_b32 s78, s47, 0xc0000\n\t" \
  "v_smfmac_f32_16x16x64_bf16 v[80:83], v[52:55], a[248:255], v106\n\t" \
  "ds_read_b128 v[0:3], v103 offset:32768\n\t" \
  "v_cvt_pk_bf16_f32 v64, v40, v41\n\t" \
  "v_smfmac_f32_16x16x64_bf16 v[84:87], v[52:55], v[144:151], v106\n\t" \
  "ds_read_b128 v[4:7], v103 offset:33792\n\t" \
  "v_cvt_pk_bf16_f32 v65, v42, v43\n\t" \
  "v_smfmac_f32_16x16x64_bf16 v[80:83], v[60:63], v[128:135], v106\n\t" \
  "ds_read_b128 v[8:11], v103 offset:34816\n\t" \
  "v_cvt_pk_bf16_f32 v66, v44, v45\n\t" \
  "v_smfmac_f32_16x16x64_bf16 v[84:87], v[60:63], v[24:31], v106\n\t" \
  "ds_read_b128 v[12:15], v103 offset:35840\n\t" \
  "v_cvt_pk_bf16_f32 v67, v46, v47\n\t" \
  "v_smfmac_f32_16x16x64_bf16 v[88:91], v[96:99], v[152:159], v106\n\t" \
  "v_permlane16_swap_b32 v64, v66\n\t" \
  "v_cvt_pk_bf16_f32 v68, v80, v81\n\t" \
  "v_smfmac_f32_16x16x64_bf16 v[92:95], v[96:99], v[184:191], v106\n\t" \
  "v_permlane16_swap_b32 v65, v67\n\t" \
  "v_cvt_pk_bf16_f32 v69, v82, v83\n\t" \
  "v_smfmac_f32_16x16x64_bf16 v[88:91], v[52:55], v[160:167], v106\n\t" \
  "v_cvt_pk_bf16_f32 v70, v84, v85\n\t" \
  "v_smfmac_f32_16x16x64_bf16 v[92:95], v[52:55], v[192:199], v106\n\t" \
  "buffer_store_dwordx4 v[64:67], v108, %[ru], s47 offen sc1\n\t" \
  "v_cvt_pk_bf16_f32 v71, v86, v87\n\t" \
  "v_smfmac_f32_16x16x64_bf16 v[88:91], v[56:59], v[168:175], v106\n\t" \
  "ds_read_b128 v[16:19], v103 offset:49152\n\t" \
  "v_permlane16_swap_b32 v68, v70\n\t" \
  "s_waitcnt lgkmcnt(3)\n\t" \
  "v_smfmac_f32_16x16x64_bf16 v[92:95], v[56:59], v[0:7], v106\n\t" \
  "ds_read_b128 v[20:23], v103 offset:50176\n\t" \
  "v_permlane16_swap_b32 v69, v71\n\t" \
  "v_smfmac_f32_16x16x64_bf16 v[88:91], v[60:63], v[176:183], v106\n\t" \
  "ds_read_b128 v[24:27], v103 offset:51200\n\t" \
  "s_waitcnt lgkmcnt(3)\n\t" \
  "v_smfmac_f32_16x16x64_bf16 v[92:95], v[60:63], v[8:15], v106\n\t" \
  "ds_read_b128 v[28:31], v103 offset:52224\n\t" \
  "buffer_store_dwordx4 v[68:71], v108, %[ru], s76 offen sc1\n\t" \
  "v_smfmac_f32_16x16x64_bf16 v[32:35], v[96:99], v[200:207], v106\n\t" \
  "v_cvt_pk_bf16_f32 v72, v88, v89\n\t" \
  "v_smfmac_f32_16x16x64_bf16 v[36:39], v[96:99], v[232:239], v106\n\t" \
  "v_cvt_pk_bf16_f32 v73, v90, v91\n\t" \
  "v_smfmac_f32_16x16x64_bf16 v[32:35], v[52:55], v[208:215], v106\n\t" \
  "v_cvt_pk_bf16_f32 v74, v92, v93\n\t" \
  "v_smfmac_f32_16x16x64_bf16 v[36:39], v[52:55], v[240:247], v106\n\t" \
  "v_cvt_pk_bf16_f32 v75, v94, v95\n\t" \
  "v_smfmac_f32_16x16x64_bf16 v[32:35], v[56:59], v[216:223], v106\n\t" \
  "v_permlane16_swap_b32 v72, v74\n\t" \
  "v_smfmac_f32_16x16x64_bf16 v[32:35], v[60:63], v[224:231], v106\n\t" \
  "v_permlane16_swap_b32 v73, v75\n\t" \
  "s_waitcnt lgkmcnt(2)\n\t" \
  "v_smfmac_f32_16x16x64_bf16 v[36:39], v[56:59], v[16:23], v106\n\t" \
  "s_waitcnt lgkmcnt(0)\n\t" \
  "v_smfmac_f32_16x16x64_bf16 v[36:39], v[60:63], v[24:31], v106\n\t" \
  "buffer_store_dwordx4 v[72:75], v108, %[ru], s77 offen sc1\n\t" \
  "v_cvt_pk_bf16_f32 v76, v32, v33\n\t" \
  "v_cvt_pk_bf16_f32 v77, v34, v35\n\t" \
  "s_nop 5\n\t" \
  "v_cvt_pk_bf16_f32 v78, v36, v37\n\t" \
  "v_cvt_pk_bf16_f32 v79, v38, v39\n\t" \
  "s_nop 0\n\t" \
  "v_permlane16_swap_b32 v76, v78\n\t" \
  "v_permlane16_swap_b32 v77, v79\n\t" \
  "buffer_store_dwordx4 v[76:79], v108, %[ru], s78 offen sc1\n\t" \
  "s_memtime s[76:77]\n\t" \
  "s_mov_b32 s42, 0\n\t" \
  "s_waitcnt lgkmcnt(0)\n\t" \
  "s_mov_b32 s57, s76\n\t" \
  "s_add_i32 s41, s56, 1950\n\t" \
  "s_sub_i32 s76, s92, s51\n\t" \
  "s_sub_i32 s77, s93, s53\n\t" \
  "s_cmp_eq_u32 s76, 0\n\t" \
  "s_cbranch_scc1 21f\n\t" \
  "v_cvt_f32_u32 v117, s76\n\t" \
  "v_cvt_f32_u32 v116, s77\n\t" \
  "v_rcp_f32 v117, v117\n\t" \
  "s_mov_b32 s51, s92\n\t" \
  "v_mul_f32 v117, v117, v116\n\t" \
  "v_max_f32 v117, 0x3f028f5c, v117\n\t" \
  "v_min_f32 v117, 0x3ffeb852, v117\n\t" \
  "v_rcp_f32 v116, v117\n\t" \
  "v_mul_f32 v117, 0x4f000000, v117\n\t" \
  "v_mul_f32 v116, 0x47800000, v116\n\t" \
  "v_cvt_u32_f32 v117, v117\n\t" \
  "v_cvt_u32_f32 v116, v116\n\t" \
  "s_mov_b32 s53, s93\n\t" \
  "v_readfirstlane_b32 s46, v117\n\t" \
  "v_readfirstlane_b32 s52, v116\n\t" \
  "21:\n\t" \
  "s_add_i32 s44, s44, 1\n\t" \
  "s_and_b32 s58, s58, 4\n\t" \
  "s_and_b32 s76, s44, 64\n\t" \
  "s_or_b32 s42, s42, s76\n\t" \
  "s_cbranch_scc0 20b\n\t" \
  "s_branch 99f\n\t" \
  "14:\n\t" \
  "s_sleep 3\n\t" \
  "s_add_u32 s59, s59, 0x10000\n\t" \
  "s_cmp_lt_u32 s59, 0x400000\n\t" \
  "s_cbranch_scc1 13b\n\t" \
  "s_mov_b64 exec, -1\n\t" \
  "s_mov_b32 s42, 16\n\t" \
  "s_branch 99f\n\t" \
  "8:\n\t" \
  "s_bitcmp1_b32 s58, 1\n\t" \
  "s_cbranch_scc1 4b\n\t" \
  "s_bitcmp1_b32 s58, 2\n\t" \
  "s_cbranch_scc1 5b\n\t" \
  "v_max3_u32 v116, v32, v33, v34\n\t" \
  "v_max3_u32 v117, v35, v36, v37\n\t" \
  "v_max3_u32 v116, v116, v38, v39\n\t" \
  "v_max_u32 v116, v116, v117\n\t" \
  "v_cmp_eq_u32 vcc, -1, v116\n\t" \
  "s_cbranch_vccz 5b\n\t" \
  "s_branch 0f\n\t" \
  "9:\n\t" \
  "s_bitcmp1_b32 s58, 2\n\t" \
  "s_cbranch_scc1 6b\n\t" \
  "v_max3_u32 v116, v40, v41, v42\n\t" \
  "v_max3_u32 v117, v43, v44, v45\n\t" \
  "v_max3_u32 v116, v116, v46, v47\n\t" \
  "v_max_u32 v116, v116, v117\n\t" \
  "v_cmp_eq_u32 vcc, -1, v116\n\t" \
  "s_cbranch_vccz 6b\n\t" \
  "s_or_b32 s58, s58, 2\n\t" \
  "0:\n\t" \
  "s_or_b32 s58, s58, 1\n\t" \
  "11:\n\t" \
  "buffer_load_dwordx4 v[32:35], v100, %[rh], s40 offen sc1\n\t" \
  "buffer_load_dwordx4 v[36:39], v100, %[rh], s76 offen sc1\n\t" \
  "buffer_load_dwordx4 v[40:43], v100, %[rh], s77 offen sc1\n\t" \
  "buffer_load_dwordx4 v[44:47], v100, %[rh], s78 offen sc1\n\t" \
  "s_waitcnt lgkmcnt(0)\n\t" \
  "s_branch 10b\n\t" \
  "17:\n\t" \
  "s_add_u32 s59, s59, 1\n\t" \
  "s_bitcmp1_b32 s58, 2\n\t" \
  "s_cbranch_scc1 3b\n\t" \
  "s_waitcnt vmcnt(0)\n\t" \
  "v_max_u32 v116, v32, v33\n\t" \
  "v_max3_u32 v116, v116, v34, v35\n\t" \
  "v_max3_u32 v116, v116, v36, v37\n\t" \
  "v_max3_u32 v116, v116, v38, v39\n\t" \
  "v_max3_u32 v116, v116, v40, v41\n\t" \
  "v_max3_u32 v116, v116, v42, v43\n\t" \
  "v_max3_u32 v116, v116, v44, v45\n\t" \
  "v_max3_u32 v116, v116, v46, v47\n\t" \
  "v_cmp_eq_u32 vcc, -1, v116\n\t" \
  "s_cbranch_vccz 3b\n\t" \
  "s_bitcmp1_b32 s59, 6\n\t" \
  "s_cbranch_scc1 12f\n\t" \
  "buffer_load_dwordx4 v[32:35], v100, %[rh], s40 offen sc1\n\t" \
  "buffer_load_dwordx4 v[36:39], v100, %[rh], s76 offen sc1\n\t" \
  "buffer_load_dwordx4 v[40:43], v100, %[rh], s77 offen sc1\n\t" \
  "buffer_load_dwordx4 v[44:47], v100, %[rh], s78 offen sc1\n\t" \
  "s_branch 17b\n\t" \
  "12:\n\t" \
  "s_mov_b32 s42, s58\n\t" \
  "s_branch 99f\n\t" \
  "93:\n\t" \
  "s_mov_b32 s58, s42\n\t" \
  "20:\n\t" \
  "s_and_b32 s76, s44, 7\n\t" \
  "s_lshl_b32 s76, s76, 15\n\t" \
  "s_add_i32 s40, s54, s76\n\t" \
  "s_and_b32 s76, s44, 3\n\t" \
  "s_lshl_b32 s45, s76, 14\n\t" \
  "s_lshl_b32 s47, s76, 20\n\t" \
  "s_add_i32 s47, s47, s55\n\t" \
  "s_lshr_b32 s76, s47, 21\n\t" \
  "s_bitcmp1_b64 %[sel], s76\n\t" \
  "s_cselect_b32 s76, 0x2000, 0\n\t" \
  "s_add_i32 s47, s47, s76\n\t" \
  "s_add_i32 s49, s44, -1\n\t" \
  "s_and_b32 s48, s49, 2\n\t" \
  "s_lshl_b32 s48, s48, 13\n\t" \
  "s_and_b32 s76, s49, 1\n\t" \
  "s_lshl_b32 s76, s76, 12\n\t" \
  "s_add_i32 s48, s48, s76\n\t" \
  "s_lshl_b32 s50, s53, 8\n\t" \
  "s_mov_b32 s59, 0\n\t" \
  "s_mov_b32 s92, s51\n\t" \
  "s_mov_b32 s93, s53\n\t" \
  "s_xor_b32 s76, s40, 0x400\n\t" \
  "s_xor_b32 s77, s40, 0x800\n\t" \
  "s_xor_b32 s78, s40, 0xc00\n\t" \
  "s_cmp_lg_u32 s58, 0\n\t" \
  "s_cbranch_scc1 11f\n\t" \
  "10:\n\t" \
  "ds_read_b128 v[48:51], v102 offset:49152\n\t" \
  "ds_read_b128 v[52:55], v102 offset:50176\n\t" \
  "ds_read_b128 v[56:59], v102 offset:51200\n\t" \
  "ds_read_b128 v[60:63], v102 offset:52224\n\t" \
  "ds_read_b128 v[0:3], v102 offset:0\n\t" \
  "ds_read_b128 v[4:7], v102 offset:1024\n\t" \
  "ds_read_b128 v[8:11], v102 offset:2048\n\t" \
  "ds_read_b128 v[12:15], v102 offset:3072\n\t" \
  "ds_read_b128 v[16:19], v102 offset:16384\n\t" \
  "ds_read_b128 v[20:23], v102 offset:17408\n\t" \
  "ds_read_b128 v[24:27], v102 offset:18432\n\t" \
  "ds_read_b128 v[28:31], v102 offset:19456\n\t" \
  "s_waitcnt lgkmcnt(4)\n\t" \
  "ds_read_b128 v[80:83], v102 offset:32768\n\t" \
  "ds_read_b128 v[84:87], v102 offset:33792\n\t" \
  "ds_read_b128 v[88:91], v102 offset:34816\n\t" \
  "ds_read_b128 v[92:95], v102 offset:35840\n\t" \
  "46:\n\t" \
  "s_bitcmp1_b32 s58, 1\n\t" \
  "s_cselect_b64 s[80:81], -1, 0\n\t" \
  "s_cmp_lg_u32 s58, 0\n\t" \
  "s_cbranch_scc1 17f\n\t" \
  "s_mov_b64 exec, 0xff\n\t" \
  "16:\n\t" \
  "s_memtime vcc\n\t" \
  "s_waitcnt lgkmcnt(0)\n\t" \
  "s_sub_i32 s79, s41, vcc_lo\n\t" \
  "s_cmp_gt_i32 s79, 64\n\t" \
  "s_cbranch_scc1 16b\n\t" \
  "13:\n\t" \
  "buffer_load_dword v116, v112, %[rh], s48 offen sc1\n\t" \
  "s_waitcnt vmcnt(0)\n\t" \
  "15:\n\t" \
  "v_mov_b32 v117, 20000\n\t" \
  "v_cmp_ne_u32_sdwa vcc, v116, s49 src0_sel:BYTE_0 src1_sel:DWORD\n\t" \
  "v_subrev_u32 v116, s50, v116\n\t" \
  "v_ashrrev_i32 v116, 8, v116\n\t" \
  "s_nop 1\n\t" \
  "v_max_i32_dpp v116, v116, v116 quad_perm:[1,0,3,2] row_mask:0xf bank_mask:0xf\n\t" \
  "s_cbranch_vccnz 14f\n\t" \
  "s_nop 0\n\t" \
  "v_max_i32_dpp v116, v116, v116 quad_perm:[2,3,0,1] row_mask:0xf bank_mask:0xf\n\t" \
  "s_nop 1\n\t" \
  "v_max_i32_dpp v116, v116, v116 row_half_mirror row_mask:0xf bank_mask:0xf\n\t" \
  "v_med3_i32 v116, v116, 0, v117\n\t" \
  "v_mul_u32_u24 v116, s52, v116\n\t" \
  "v_lshrrev_b32 v116, 16, v116\n\t" \
  "s_mov_b64 exec, -1\n\t" \
  "v_readfirstlane_b32 s84, v116\n\t" \
  "s_add_i32 s84, s84, s51\n\t" \
  ".p2align 6\n\t" \
  "s_nop 0\n\t" \
  "s_nop 0\n\t" \
  "s_nop 0\n\t" \
  "s_nop 0\n\t" \
  "s_nop 0\n\t" \
  "s_nop 0\n\t" \
  "s_nop 0\n\t" \
  "s_nop 0\n\t" \
  "1:\n\t" \
  "s_memtime vcc\n\t" \
  "s_waitcnt lgkmcnt(0)\n\t" \
  "s_sub_i32 s79, s84, vcc_lo\n\t" \
  "s_cmp_gt_i32 s79, 128\n\t" \
  "s_cbranch_scc1 1b\n\t" \
  ".p2align 4\n\t" \
  "2:\n\t" \
  "s_sub_i32 s79, s79, 33\n\t" \
  "s_cmp_gt_i32 s79, 56\n\t" \
  "s_cbranch_scc1 2b\n\t" \
  "buffer_load_dwordx4 v[32:35], v100, %[rh], s40 offen sc1\n\t" \
  "s_nop 12\n\t" \
  "buffer_load_dwordx4 v[36:39], v100, %[rh], s76 offen sc1\n\t" \
  "s_nop 12\n\t" \
  "buffer_load_dwordx4 v[40:43], v100, %[rh], s77 offen sc1\n\t" \
  "s_nop 12\n\t" \
  "buffer_load_dwordx4 v[44:47], v100, %[rh], s78 offen sc1\n\t" \
  "s_memtime vcc\n\t" \
  "s_memrealtime s[82:83]\n\t" \
  "s_nop 4\n\t" \
  "s_memrealtime s[84:85]\n\t" \
  "s_nop 4\n\t" \
  "s_memrealtime s[56:57]\n\t" \
  "s_nop 4\n\t" \
  "s_memrealtime s[86:87]\n\t" \
  "s_nop 4\n\t" \
  "s_memrealtime s[88:89]\n\t" \
  "s_nop 4\n\t" \
  "s_memrealtime s[90:91]\n\t" \
  "s_mov_b32 s92, 0\n\t" \
  "s_waitcnt lgkmcnt(0)\n\t" \
  "s_cmp_eq_u32 s82, s84\n\t" \
  "s_addc_u32 s92, s92, 0\n\t" \
  "s_cmp_eq_u32 s82, s56\n\t" \
  "s_addc_u32 s92, s92, 0\n\t" \
  "s_cmp_eq_u32 s82, s86\n\t" \
  "s_addc_u32 s92, s92, 0\n\t" \
  "s_cmp_eq_u32 s82, s88\n\t" \
  "s_addc_u32 s92, s92, 0\n\t" \
  "s_cmp_eq_u32 s82, s90\n\t" \
  "s_addc_u32 s92, s92, 0\n\t" \
  "s_mul_i32 s92, s92, 24\n\t" \
  "s_add_i32 s92, s92, vcc_lo\n\t" \
  "s_lshl4_add_u32 s93, s82, 64\n\t" \
  "3:\n\t" \
  "v_mov_b64 v[72:73], 0\n\t" \
  "v_mov_b64 v[74:75], 0\n\t" \
  "v_mov_b64 v[76:77], 0\n\t" \
  "v_mov_b64 v[78:79], 0\n\t" \
  "s_waitcnt lgkmcnt(0)\n\t" \
  "s_waitcnt vmcnt(3)\n\t" \
  "v_smfmac_f32_16x16x64_bf16 v[72:75], v[32:35], v[48:55], v104\n\t" \
  "ds_write_b128 v101, v[32:35] offset:12288\n\t" \
  "v_smfmac_f32_16x16x64_bf16 v[76:79], v[32:35], v[56:63], v104\n\t" \
  "s_waitcnt vmcnt(2)\n\t" \
  "ds_write_b128 v101, v[36:39] offset:13312\n\t" \
  "v_smfmac_f32_16x16x64_bf16 v[72:75], v[36:39], a[144:151], v104\n\t" \
  "v_smfmac_f32_16x16x64_bf16 v[76:79], v[36:39], a[152:159], v104\n\t" \
  "s_nop 3\n\t" \
  "v_cmp_u_f32 vcc, v72, v73\n\t" \
  "v_cmp_u_f32 s[82:83], v74, v75\n\t" \
  "s_waitcnt vmcnt(1)\n\t" \
  "v_smfmac_f32_16x16x64_bf16 v[72:75], v[40:43], a[160:167], v104\n\t" \
  "s_or_b64 vcc, vcc, s[82:83]\n\t" \
  "s_or_b64 vcc, vcc, s[80:81]\n\t" \
  "v_smfmac_f32_16x16x64_bf16 v[76:79], v[40:43], a[168:175], v104\n\t" \
  "s_waitcnt lgkmcnt(0)\n\t" \
  "s_cbranch_scc1 8f\n\t" \
  "5:\n\t" \
  "s_barrier\n\t" \
  "4:\n\t" \
  "s_memtime s[56:57]\n\t" \
  "ds_read_b128 v[48:51], v101 offset:0\n\t" \
  "ds_read_b128 v[52:55], v101 offset:1024\n\t" \
  "ds_read_b128 v[56:59], v101 offset:5120\n\t" \
  "s_waitcnt vmcnt(0)\n\t" \
  "v_smfmac_f32_16x16x64_bf16 v[72:75], v[44:47], a[176:183], v104\n\t" \
  "ds_read_b128 v[60:63], v101 offset:4096\n\t" \
  "ds_read_b128 v[64:67], v101 offset:9216\n\t" \
  "v_smfmac_f32_16x16x64_bf16 v[76:79], v[44:47], a[184:191], v104\n\t" \
  "ds_read_b128 v[68:71], v101 offset:8192\n\t" \
  "ds_write_b128 v101, v[40:43] offset:14336\n\t" \
  "s_nop 1\n\t" \
  "v_cmp_u_f32 vcc, v72, v73\n\t" \
  "v_cmp_u_f32 s[82:83], v74, v75\n\t" \
  "s_waitcnt lgkmcnt(6)\n\t" \
  "v_smfmac_f32_16x16x64_bf16 v[72:75], v[48:51], v[0:7], v104\n\t" \
  "s_or_b64 vcc, vcc, s[82:83]\n\t" \
  "v_smfmac_f32_16x16x64_bf16 v[76:79], v[48:51], v[8:15], v104\n\t" \
  "s_cbranch_scc1 9f\n\t" \
  "6:\n\t" \
  "s_waitcnt lgkmcnt(5)\n\t" \
  "v_smfmac_f32_16x16x64_bf16 v[72:75], v[52:55], a[0:7], v104\n\t" \
  "ds_write_b128 v101, v[44:47] offset:15360\n\t" \
  "v_smfmac_f32_16x16x64_bf16 v[76:79], v[52:55], a[8:15], v104\n\t" \
  "v_mov_b64 v[40:41], 0\n\t" \
  "s_waitcnt lgkmcnt(5)\n\t" \
  "v_smfmac_f32_16x16x64_bf16 v[72:75], v[56:59], a[48:55], v104\n\t" \
  "v_mov_b64 v[42:43], 0\n\t" \
  "v_smfmac_f32_16x16x64_bf16 v[76:79], v[56:59], a[56:63], v104\n\t" \
  "s_waitcnt lgkmcnt(4)\n\t" \
  "v_smfmac_f32_16x16x64_bf16 v[72:75], v[60:63], v[16:23], v104\n\t" \
  "s_waitcnt lgkmcnt(0)\n\t" \
  "s_barrier\n\t" \
  "v_smfmac_f32_16x16x64_bf16 v[76:79], v[60:63], v[24:31], v104\n\t" \
  "ds_read_b128 v[48:51], v101 offset:2048\n\t" \
  "ds_read_b128 v[52:55], v101 offset:6144\n\t" \
  "v_smfmac_f32_16x16x64_bf16 v[72:75], v[64:67], a[96:103], v104\n\t" \
  "ds_read_b128 v[56:59], v101 offset:10240\n\t" \
  "ds_read_b128 v[60:63], v101 offset:3072\n\t" \
  "v_smfmac_f32_16x16x64_bf16 v[76:79], v[64:67], a[104:111], v104\n\t" \
  "ds_read_b128 v[32:35], v101 offset:7168\n\t" \
  "ds_read_b128 v[36:39], v101 offset:11264\n\t" \
  "v_smfmac_f32_16x16x64_bf16 v[72:75], v[68:71], v[80:87], v104\n\t" \
  "ds_read_b128 v[0:3], v103 offset:0\n\t" \
  "v_mov_b64 v[44:45], 0\n\t" \
  "v_smfmac_f32_16x16x64_bf16 v[76:79], v[68:71], v[88:95], v104\n\t" \
  "v_mov_b64 v[46:47], 0\n\t" \
  "s_waitcnt lgkmcnt(5)\n\t" \
  "v_smfmac_f32_16x16x64_bf16 v[72:75], v[48:51], a[16:23], v104\n\t" \
  "ds_read_b128 v[4:7], v103 offset:1024\n\t" \
  "v_mov_b64 v[80:81], 0\n\t" \
  "v_smfmac_f32_16x16x64_bf16 v[72:75], v[52:55], a[64:71], v104\n\t" \
  "v_mov_b64 v[82:83], 0\n\t" \
  "s_waitcnt lgkmcnt(2)\n\t" \
  "v_smfmac_f32_16x16x64_bf16 v[72:75], v[56:59], a[112:119], v104\n\t" \
  "ds_read_b128 v[8:11], v103 offset:2048\n\t" \
  "v_mov_b64 v[84:85], 0\n\t" \
  "v_smfmac_f32_16x16x64_bf16 v[72:75], v[60:63], a[32:39], v104\n\t" \
  "ds_read_b128 v[12:15], v103 offset:3072\n\t" \
  "v_mov_b64 v[86:87], 0\n\t" \
  "v_smfmac_f32_16x16x64_bf16 v[72:75], v[32:35], a[80:87], v104\n\t" \
  "ds_read_b128 v[16:19], v103 offset:16384\n\t" \
  "v_mov_b64 v[88:89], 0\n\t" \
  "v_smfmac_f32_16x16x64_bf16 v[72:75], v[36:39], a[128:135], v104\n\t" \
  "ds_read_b128 v[20:23], v103 offset:17408\n\t" \
  "v_mov_b64 v[90:91], 0\n\t" \
  "v_smfmac_f32_16x16x64_bf16 v[76:79], v[48:51], a[24:31], v104\n\t" \
  "ds_read_b128 v[24:27], v103 offset:18432\n\t" \
  "v_mov_b64 v[92:93], 0\n\t" \
  "v_smfmac_f32_16x16x64_bf16 v[76:79], v[52:55], a[72:79], v104\n\t" \
  "v_add_f32 v96, v72, v74\n\t" \
  "v_add_f32 v97, v73, v75\n\t" \
  "v_cvt_pk_bf16_f32 v96, v96, v97\n\t" \
  "ds_write_b32 v105, v96\n\t" \
  "v_smfmac_f32_16x16x64_bf16 v[76:79], v[56:59], a[120:127], v104\n\t" \
  "ds_read_b128 v[28:31], v103 offset:19456\n\t" \
  "v_mov_b64 v[94:95], 0\n\t" \
  "v_smfmac_f32_16x16x64_bf16 v[76:79], v[60:63], a[40:47], v104\n\t" \
  "v_smfmac_f32_16x16x64_bf16 v[76:79], v[32:35], a[88:95], v104\n\t" \
  "v_smfmac_f32_16x16x64_bf16 v[76:79], v[36:39], a[136:143], v104\n\t" \
  "s_waitcnt lgkmcnt(1)\n\t" \
  "s_barrier\n\t" \
  "ds_read_b64_tr_b16 v[96:97], v107 offset:0\n\t" \
  "ds_read_b64_tr_b16 v[98:99], v107 offset:64\n\t" \
  "ds_read_b64_tr_b16 v[56:57], v107 offset:1024\n\t" \
  "ds_read_b64_tr_b16 v[58:59], v107 offset:1088\n\t" \
  "s_nop 1\n\t" \
  "v_add_f32 v116, v76, v78\n\t" \
  "v_add_f32 v117, v77, v79\n\t" \
  "v_cvt_pk_bf16_f32 v116, v116, v117\n\t" \
  "ds_write_b32 v105, v116 offset:256\n\t" \
  "s_waitcnt lgkmcnt(3)\n\t" \
  "v_smfmac_f32_16x16x64_bf16 v[40:43], v[96:99], a[192:199], v106\n\t" \
  "v_smfmac_f32_16x16x64_bf16 v[44:47], v[96:99], a[224:231], v106\n\t" \
  "s_waitcnt lgkmcnt(0)\n\t" \
  "s_barrier\n\t" \
  "ds_read_b64_tr_b16 v[52:53], v107 offset:256\n\t" \
  "ds_read_b64_tr_b16 v[54:55], v107 offset:320\n\t" \
  "v_smfmac_f32_16x16x64_bf16 v[40:43], v[56:59], a[208:215], v106\n\t" \
  "ds_read_b64_tr_b16 v[60:61], v107 offset:1280\n\t" \
  "ds_read_b64_tr_b16 v[62:63], v107 offset:1344\n\t" \
  "v_smfmac_f32_16x16x64_bf16 v[44:47], v[56:59], v[0:7], v106\n\t" \
  "v_smfmac_f32_16x16x64_bf16 v[80:83], v[96:99], a[240:247], v106\n\t" \
  "v_mov_b64 v[32:33], 0\n\t" \
  "v_smfmac_f32_16x16x64_bf16 v[84:87], v[96:99], v[136:143], v106\n\t" \
  "v_mov_b64 v[34:35], 0\n\t" \
  "v_smfmac_f32_16x16x64_bf16 v[80:83], v[56:59], v[120:127], v106\n\t" \
  "v_mov_b64 v[36:37], 0\n\t" \
  "v_smfmac_f32_16x16x64_bf16 v[84:87], v[56:59], v[16:23], v106\n\t" \
  "s_waitcnt lgkmcnt(2)\n\t" \
  "v_smfmac_f32_16x16x64_bf16 v[40:43], v[52:55], a[200:207], v106\n\t" \
  "v_smfmac_f32_16x16x64_bf16 v[44:47], v[52:55], a[232:239], v106\n\t" \
  "s_xor_b32 s76, s47, 0x40000\n\t" \
  "s_waitcnt lgkmcnt(0)\n\t" \
  "v_smfmac_f32_16x16x64_bf16 v[40:43], v[60:63], a[216:223], v106\n\t" \
  "v_mov_b64 v[38:39], 0\n\t" \
  "s_xor_b32 s77, s47, 0x80000\n\t" \
  "v_smfmac_f32_16x16x64_bf16 v[44:47], v[60:63], v[8:15], v106\n\t" \
  "s_xor_b32 s78, s47, 0xc0000\n\t" \
  "v_smfmac_f32_16x16x64_bf16 v[80:83], v[52:55], a[248:255], v106\n\t" \
  "ds_read_b128 v[0:3], v103 offset:32768\n\t" \
  "v_cvt_pk_bf16_f32 v64, v40, v41\n\t" \
  "v_smfmac_f32_16x16x64_bf16 v[84:87], v[52:55], v[144:151], v106\n\t" \
  "ds_read_b128 v[4:7], v103 offset:33792\n\t" \
  "v_cvt_pk_bf16_f32 v65, v42, v43\n\t" \
  "v_smfmac_f32_16x16x64_bf16 v[80:83], v[60:63], v[128:135], v106\n\t" \
  "ds_read_b128 v[8:11], v103 offset:34816\n\t" \
  "v_cvt_pk_bf16_f32 v66, v44, v45\n\t" \
  "v_smfmac_f32_16x16x64_bf16 v[84:87], v[60:63], v[24:31], v106\n\t" \
  "ds_read_b128 v[12:15], v103 offset:35840\n\t" \
  "v_cvt_pk_bf16_f32 v67, v46, v47\n\t" \
  "v_smfmac_f32_16x16x64_bf16 v[88:91], v[96:99], v[152:159], v106\n\t" \
  "v_permlane16_swap_b32 v64, v66\n\t" \
  "v_cvt_pk_bf16_f32 v68, v80, v81\n\t" \
  "v_smfmac_f32_16x16x64_bf16 v[92:95], v[96:99], v[184:191], v106\n\t" \
  "v_permlane16_swap_b32 v65, v67\n\t" \
  "v_cvt_pk_bf16_f32 v69, v82, v83\n\t" \
  "v_smfmac_f32_16x16x64_bf16 v[88:91], v[52:55], v[160:167], v106\n\t" \
  "v_cvt_pk_bf16_f32 v70, v84, v85\n\t" \
  "v_smfmac_f32_16x16x64_bf16 v[92:95], v[52:55], v[192:199], v106\n\t" \
  "v_cvt_pk_bf16_f32 v71, v86, v87\n\t" \
  "v_smfmac_f32_16x16x64_bf16 v[88:91], v[56:59], v[168:175], v106\n\t" \
  "ds_read_b128 v[16:19], v103 offset:49152\n\t" \
  "buffer_store_dwordx4 v[64:67], v108, %[ru], s47 offen sc1\n\t" \
  "v_permlane16_swap_b32 v68, v70\n\t" \
  "s_waitcnt lgkmcnt(3)\n\t" \
  "v_smfmac_f32_16x16x64_bf16 v[92:95], v[56:59], v[0:7], v106\n\t" \
  "ds_read_b128 v[20:23], v103 offset:50176\n\t" \
  "v_permlane16_swap_b32 v69, v71\n\t" \
  "v_smfmac_f32_16x16x64_bf16 v[88:91], v[60:63], v[176:183], v106\n\t" \
  "ds_read_b128 v[24:27], v103 offset:51200\n\t" \
  "s_waitcnt lgkmcnt(3)\n\t" \
  "v_smfmac_f32_16x16x64_bf16 v[92:95], v[60:63], v[8:15], v106\n\t" \
  "ds_read_b128 v[28:31], v103 offset:52224\n\t" \
  "v_smfmac_f32_16x16x64_bf16 v[32:35], v[96:99], v[200:207], v106\n\t" \
  "buffer_store_dwordx4 v[68:71], v108, %[ru], s76 offen sc1\n\t" \
  "v_cvt_pk_bf16_f32 v72, v88, v89\n\t" \
  "v_smfmac_f32_16x16x64_bf16 v[36:39], v[96:99], v[232:239], v106\n\t" \
  "v_cvt_pk_bf16_f32 v73, v90, v91\n\t" \
  "v_smfmac_f32_16x16x64_bf16 v[32:35], v[52:55], v[208:215], v106\n\t" \
  "v_cvt_pk_bf16_f32 v74, v92, v93\n\t" \
  "v_smfmac_f32_16x16x64_bf16 v[36:39], v[52:55], v[240:247], v106\n\t" \
  "v_cvt_pk_bf16_f32 v75, v94, v95\n\t" \
  "v_smfmac_f32_16x16x64_bf16 v[32:35], v[56:59], v[216:223], v106\n\t" \
  "v_permlane16_swap_b32 v72, v74\n\t" \
  "v_smfmac_f32_16x16x64_bf16 v[32:35], v[60:63], v[224:231], v106\n\t" \
  "v_permlane16_swap_b32 v73, v75\n\t" \
  "s_waitcnt lgkmcnt(2)\n\t" \
  "v_smfmac_f32_16x16x64_bf16 v[36:39], v[56:59], v[16:23], v106\n\t" \
  "s_waitcnt lgkmcnt(0)\n\t" \
  "v_smfmac_f32_16x16x64_bf16 v[36:39], v[60:63], v[24:31], v106\n\t" \
  "buffer_store_dwordx4 v[72:75], v108, %[ru], s77 offen sc1\n\t" \
  "v_cvt_pk_bf16_f32 v76, v32, v33\n\t" \
  "v_cvt_pk_bf16_f32 v77, v34, v35\n\t" \
  "s_nop 5\n\t" \
  "v_cvt_pk_bf16_f32 v78, v36, v37\n\t" \
  "v_cvt_pk_bf16_f32 v79, v38, v39\n\t" \
  "s_nop 0\n\t" \
  "v_permlane16_swap_b32 v76, v78\n\t" \
  "v_permlane16_swap_b32 v77, v79\n\t" \
  "buffer_store_dwordx4 v[76:79], v108, %[ru], s78 offen sc1\n\t" \
  "s_memtime s[76:77]\n\t" \
  "s_mov_b32 s42, 0\n\t" \
  "s_waitcnt lgkmcnt(0)\n\t" \
  "s_mov_b32 s57, s76\n\t" \
  "s_add_i32 s41, s56, 1950\n\t" \
  "s_sub_i32 s76, s92, s51\n\t" \
  "s_sub_i32 s77, s93, s53\n\t" \
  "s_cmp_eq_u32 s76, 0\n\t" \
  "s_cbranch_scc1 21f\n\t" \
  "v_cvt_f32_u32 v117, s76\n\t" \
  "v_cvt_f32_u32 v116, s77\n\t" \
  "v_rcp_f32 v117, v117\n\t" \
  "s_mov_b32 s51, s92\n\t" \
  "v_mul_f32 v117, v117, v116\n\t" \
  "v_max_f32 v117, 0x3f028f5c, v117\n\t" \
  "v_min_f32 v117, 0x3ffeb852, v117\n\t" \
  "v_rcp_f32 v116, v117\n\t" \
  "v_mul_f32 v117, 0x4f000000, v117\n\t" \
  "v_mul_f32 v116, 0x47800000, v116\n\t" \
  "v_cvt_u32_f32 v117, v117\n\t" \
  "v_cvt_u32_f32 v116, v116\n\t" \
  "s_mov_b32 s53, s93\n\t" \
  "v_readfirstlane_b32 s46, v117\n\t" \
  "v_readfirstlane_b32 s52, v116\n\t" \
  "21:\n\t" \
  "s_add_i32 s44, s44, 1\n\t" \
  "s_and_b32 s58, s58, 4\n\t" \
  "s_and_b32 s76, s44, 64\n\t" \
  "s_or_b32 s42, s42, s76\n\t" \
  "s_cbranch_scc0 20b\n\t" \
  "s_branch 99f\n\t" \
  "14:\n\t" \
  "s_sleep 3\n\t" \
  "s_add_u32 s59, s59, 0x10000\n\t" \
  "s_cmp_lt_u32 s59, 0x400000\n\t" \
  "s_cbranch_scc1 13b\n\t" \
  "s_mov_b64 exec, -1\n\t" \
  "s_mov_b32 s42, 16\n\t" \
  "s_branch 99f\n\t" \
  "8:\n\t" \
  "s_bitcmp1_b32 s58, 1\n\t" \
  "s_cbranch_scc1 4b\n\t" \
  "s_bitcmp1_b32 s58, 2\n\t" \
  "s_cbranch_scc1 5b\n\t" \
  "v_max3_u32 v116, v32, v33, v34\n\t" \
  "v_max3_u32 v117, v35, v36, v37\n\t" \
  "v_max3_u32 v116, v116, v38, v39\n\t" \
  "v_max_u32 v116, v116, v117\n\t" \
  "v_cmp_eq_u32 vcc, -1, v116\n\t" \
  "s_cbranch_vccz 5b\n\t" \
  "s_branch 0f\n\t" \
  "9:\n\t" \
  "s_bitcmp1_b32 s58, 2\n\t" \
  "s_cbranch_scc1 6b\n\t" \
  "v_max3_u32 v116, v40, v41, v42\n\t" \
  "v_max3_u32 v117, v43, v44, v45\n\t" \
  "v_max3_u32 v116, v116, v46, v47\n\t" \
  "v_max_u32 v116, v116, v117\n\t" \
  "v_cmp_eq_u32 vcc, -1, v116\n\t" \
  "s_cbranch_vccz 6b\n\t" \
  "s_or_b32 s58, s58, 2\n\t" \
  "0:\n\t" \
  "s_or_b32 s58, s58, 1\n\t" \
  "11:\n\t" \
  "buffer_load_dwordx4 v[32:35], v100, %[rh], s40 offen sc1\n\t" \
  "buffer_load_dwordx4 v[36:39], v100, %[rh], s76 offen sc1\n\t" \
  "buffer_load_dwordx4 v[40:43], v100, %[rh], s77 offen sc1\n\t" \
  "buffer_load_dwordx4 v[44:47], v100, %[rh], s78 offen sc1\n\t" \
  "s_waitcnt lgkmcnt(0)\n\t" \
  "s_branch 10b\n\t" \
  "17:\n\t" \
  "s_add_u32 s59, s59, 1\n\t" \
  "s_bitcmp1_b32 s58, 2\n\t" \
  "s_cbranch_scc1 3b\n\t" \
  "s_waitcnt vmcnt(0)\n\t" \
  "v_max_u32 v116, v32, v33\n\t" \
  "v_max3_u32 v116, v116, v34, v35\n\t" \
  "v_max3_u32 v116, v116, v36, v37\n\t" \
  "v_max3_u32 v116, v116, v38, v39\n\t" \
  "v_max3_u32 v116, v116, v40, v41\n\t" \
  "v_max3_u32 v116, v116, v42, v43\n\t" \
  "v_max3_u32 v116, v116, v44, v45\n\t" \
  "v_max3_u32 v116, v116, v46, v47\n\t" \
  "v_cmp_eq_u32 vcc, -1, v116\n\t" \
  "s_cbranch_vccz 3b\n\t" \
  "s_bitcmp1_b32 s59, 6\n\t" \
  "s_cbranch_scc1 12f\n\t" \
  "buffer_load_dwordx4 v[32:35], v100, %[rh], s40 offen sc1\n\t" \
  "buffer_load_dwordx4 v[36:39], v100, %[rh], s76 offen sc1\n\t" \
  "buffer_load_dwordx4 v[40:43], v100, %[rh], s77 offen sc1\n\t" \
  "buffer_load_dwordx4 v[44:47], v100, %[rh], s78 offen sc1\n\t" \
  "s_branch 17b\n\t" \
  "12:\n\t" \
  "s_mov_b32 s42, s58\n\t" \
  "s_branch 99f\n\t" \
  "99:\n\t" \
  "s_waitcnt vmcnt(0) lgkmcnt(0)\n\t" \
  ".Lchain_last:\n\t" \
  ""
#define CB_BODY \
  "s_cmp_gt_u32 %[wv], 1\n\t" \
  "s_cbranch_scc1 91f\n\t" \
  "s_mov_b32 s58, s42\n\t" \
  "s_mov_b32 s59, 0\n\t" \
  "s_bitcmp1_b32 s58, 4\n\t" \
  "s_cbranch_scc1 26f\n\t" \
  "s_bitcmp1_b32 s58, 3\n\t" \
  "s_cbranch_scc1 34f\n\t" \
  "s_bitcmp1_b32 s58, 1\n\t" \
  "s_cbranch_scc1 18f\n\t" \
  "17:\n\t" \
  "19:\n\t" \
  "s_add_i32 s85, s40, -1\n\t" \
  "s_and_b32 s82, s85, 2\n\t" \
  "s_lshl_b32 s82, s82, 13\n\t" \
  "s_and_b32 s80, s85, 1\n\t" \
  "s_lshl_b32 s80, s80, 12\n\t" \
  "s_add_i32 s82, s82, s80\n\t" \
  "s_add_i32 s82, s82, 0x84000\n\t" \
  "s_mov_b32 exec_hi, 0\n\t" \
  "buffer_load_dword v184, v200, %[rh], s82 offen sc1\n\t" \
  "buffer_load_dword v186, v203, %[ru], 0 offen sc1\n\t" \
  "s_mov_b64 exec, -1\n\t" \
  "20:\n\t" \
  "s_and_b32 s80, s40, 3\n\t" \
  "s_mul_i32 s77, s80, 0xc000\n\t" \
  "s_add_i32 s77, s77, 0x40000\n\t" \
  "s_lshl_b32 s78, s80, 14\n\t" \
  "s_add_i32 s78, s78, 0x70000\n\t" \
  "s_mov_b32 s57, 0\n\t" \
  "s_add_i32 s96, s77, 0x1000\n\t" \
  "s_lshl_b32 s88, s80, 20\n\t" \
  "s_add_i32 s88, s88, s48\n\t" \
  "s_lshr_b32 s80, s88, 21\n\t" \
  "s_bitcmp1_b64 %[sel], s80\n\t" \
  "s_cselect_b32 s80, 0x2000, 0\n\t" \
  "s_add_i32 s88, s88, s80\n\t" \
  "s_xor_b32 s89, s88, 0x800\n\t" \
  "s_xor_b32 s90, s88, 0x1000\n\t" \
  "s_xor_b32 s91, s88, 0x1800\n\t" \
  "s_add_i32 s92, s77, s50\n\t" \
  "s_xor_b32 s93, s92, 0x100\n\t" \
  "s_xor_b32 s94, s92, 0x200\n\t" \
  "s_xor_b32 s95, s92, 0x300\n\t" \
  "s_add_i32 s97, s40, 1\n\t" \
  "s_and_b32 s97, s97, 7\n\t" \
  "s_lshl_b32 s97, s97, 15\n\t" \
  "s_add_i32 s97, s97, s49\n\t" \
  "s_memtime vcc\n\t" \
  "s_memrealtime s[86:87]\n\t" \
  "s_nop 4\n\t" \
  "s_memrealtime s[60:61]\n\t" \
  "s_nop 4\n\t" \
  "s_memrealtime s[62:63]\n\t" \
  "s_nop 4\n\t" \
  "s_memrealtime s[64:65]\n\t" \
  "s_nop 4\n\t" \
  "s_memrealtime s[66:67]\n\t" \
  "s_nop 4\n\t" \
  "s_memrealtime s[68:69]\n\t" \
  "s_mov_b32 s79, 0\n\t" \
  "s_waitcnt lgkmcnt(0)\n\t" \
  "s_cmp_eq_u32 s86, s60\n\t" \
  "s_addc_u32 s79, s79, 0\n\t" \
  "s_cmp_eq_u32 s86, s62\n\t" \
  "s_addc_u32 s79, s79, 0\n\t" \
  "s_cmp_eq_u32 s86, s64\n\t" \
  "s_addc_u32 s79, s79, 0\n\t" \
  "s_cmp_eq_u32 s86, s66\n\t" \
  "s_addc_u32 s79, s79, 0\n\t" \
  "s_cmp_eq_u32 s86, s68\n\t" \
  "s_addc_u32 s79, s79, 0\n\t" \
  "s_mul_i32 s79, s79, 24\n\t" \
  "s_add_i32 s79, s79, vcc_lo\n\t" \
  "s_lshl4_add_u32 s80, s86, 64\n\t" \
  "s_sub_i32 s86, s79, s41\n\t" \
  "s_sub_i32 s87, s80, s43\n\t" \
  "s_cmp_lt_u32 s86, 4000\n\t" \
  "s_cbranch_scc1 21f\n\t" \
  "v_cvt_f32_u32 v165, s86\n\t" \
  "v_cvt_f32_u32 v164, s87\n\t" \
  "v_rcp_f32 v165, v165\n\t" \
  "s_nop 0\n\t" \
  "v_mul_f32 v165, v165, v164\n\t" \
  "v_max_f32 v165, 0x3f028f5c, v165\n\t" \
  "v_min_f32 v165, 0x3ffeb852, v165\n\t" \
  "v_rcp_f32 v164, v165\n\t" \
  "v_mul_f32 v165, 0x4f000000, v165\n\t" \
  "v_mul_f32 v164, 0x47800000, v164\n\t" \
  "v_cvt_u32_f32 v165, v165\n\t" \
  "v_cvt_u32_f32 v164, v164\n\t" \
  "s_nop 0\n\t" \
  "v_readfirstlane_b32 s45, v165\n\t" \
  "v_readfirstlane_b32 s44, v164\n\t" \
  "21:\n\t" \
  "s_mov_b32 s41, s79\n\t" \
  "s_mov_b32 s43, s80\n\t" \
  "s_lshl_b32 s83, s43, 8\n\t" \
  "s_mov_b32 s84, 610\n\t" \
  "s_mov_b32 exec_hi, 0\n\t" \
  "s_waitcnt vmcnt(0)\n\t" \
  "v_cmp_ne_u32_sdwa vcc, v184, s85 src0_sel:BYTE_0 src1_sel:DWORD\n\t" \
  "v_subrev_u32 v184, s83, v184\n\t" \
  "v_ashrrev_i32 v184, 8, v184\n\t" \
  "s_cbranch_vccnz 22f\n\t" \
  "s_nop 0\n\t" \
  "v_max_i32_dpp v184, v184, v184 quad_perm:[1,0,3,2] row_mask:0xf bank_mask:0xf\n\t" \
  "s_nop 1\n\t" \
  "v_max_i32_dpp v184, v184, v184 quad_perm:[2,3,0,1] row_mask:0xf bank_mask:0xf\n\t" \
  "s_nop 1\n\t" \
  "v_max_i32_dpp v184, v184, v184 row_half_mirror row_mask:0xf bank_mask:0xf\n\t" \
  "s_nop 1\n\t" \
  "v_max_i32_dpp v184, v184, v184 row_mirror row_mask:0xf bank_mask:0xf\n\t" \
  "s_nop 0\n\t" \
  "v_readlane_b32 s81, v184, 0\n\t" \
  "v_readlane_b32 s82, v184, 16\n\t" \
  "s_max_i32 s81, s81, s82\n\t" \
  "s_add_i32 s76, s81, s43\n\t" \
  "s_add_i32 s81, s81, s53\n\t" \
  "s_max_i32 s81, s81, 0\n\t" \
  "s_min_i32 s81, s81, 20000\n\t" \
  "s_mul_i32 s81, s81, s44\n\t" \
  "s_lshr_b32 s81, s81, 16\n\t" \
  "s_add_i32 s81, s81, -900\n\t" \
  "24:\n\t" \
  "s_add_i32 s81, s81, s41\n\t" \
  ".p2align 6\n\t" \
  "s_nop 0\n\t" \
  "s_nop 0\n\t" \
  "s_nop 0\n\t" \
  "s_nop 0\n\t" \
  "s_nop 0\n\t" \
  "s_nop 0\n\t" \
  "s_nop 0\n\t" \
  "s_nop 0\n\t" \
  "1:\n\t" \
  "s_memtime vcc\n\t" \
  "s_waitcnt lgkmcnt(0)\n\t" \
  "s_sub_i32 s79, s81, vcc_lo\n\t" \
  "s_cmp_gt_i32 s79, 128\n\t" \
  "s_cbranch_scc1 1b\n\t" \
  ".p2align 4\n\t" \
  "2:\n\t" \
  "s_sub_i32 s79, s79, 33\n\t" \
  "s_cmp_gt_i32 s79, 56\n\t" \
  "s_cbranch_scc1 2b\n\t" \
  "buffer_load_dword v187, v200, %[rh], s78 offen sc1\n\t" \
  "s_memtime vcc\n\t" \
  "s_memrealtime s[86:87]\n\t" \
  "s_nop 4\n\t" \
  "s_memrealtime s[60:61]\n\t" \
  "s_nop 4\n\t" \
  "s_memrealtime s[62:63]\n\t" \
  "s_nop 4\n\t" \
  "s_memrealtime s[64:65]\n\t" \
  "s_nop 4\n\t" \
  "s_memrealtime s[66:67]\n\t" \
  "s_nop 4\n\t" \
  "s_memrealtime s[68:69]\n\t" \
  "s_mov_b32 s79, 0\n\t" \
  "s_waitcnt lgkmcnt(0)\n\t" \
  "s_cmp_eq_u32 s86, s60\n\t" \
  "s_addc_u32 s79, s79, 0\n\t" \
  "s_cmp_eq_u32 s86, s62\n\t" \
  "s_addc_u32 s79, s79, 0\n\t" \
  "s_cmp_eq_u32 s86, s64\n\t" \
  "s_addc_u32 s79, s79, 0\n\t" \
  "s_cmp_eq_u32 s86, s66\n\t" \
  "s_addc_u32 s79, s79, 0\n\t" \
  "s_cmp_eq_u32 s86, s68\n\t" \
  "s_addc_u32 s79, s79, 0\n\t" \
  "s_mul_i32 s79, s79, 24\n\t" \
  "s_add_i32 s79, s79, vcc_lo\n\t" \
  "s_lshl4_add_u32 s80, s86, 64\n\t" \
  "s_mov_b32 s41, s79\n\t" \
  "s_mov_b32 s43, s80\n\t" \
  "s_lshl_b32 s83, s43, 8\n\t" \
  "s_add_i32 s98, s40, 2\n\t" \
  "v_cmp_gt_i32 vcc, s98, v186\n\t" \
  "s_and_b32 s54, s98, 15\n\t" \
  "s_lshl_b32 s54, s54, 22\n\t" \
  "s_add_i32 s54, s54, s51\n\t" \
  "s_lshr_b32 s80, s54, 21\n\t" \
  "s_bitcmp0_b64 %[sel], s80\n\t" \
  "s_cselect_b32 s80, 0x2000, 0\n\t" \
  "s_add_i32 s54, s54, s80\n\t" \
  "s_cmp_eq_u64 vcc, 0\n\t" \
  "s_cselect_b32 s96, 0x70000000, 0\n\t" \
  "s_cselect_b32 s98, 0, s98\n\t" \
  "13:\n\t" \
  "s_waitcnt vmcnt(0)\n\t" \
  "s_memtime s[86:87]\n\t" \
  "v_cmp_ne_u32_sdwa vcc, v187, s40 src0_sel:BYTE_0 src1_sel:DWORD\n\t" \
  "v_subrev_u32 v184, s83, v187\n\t" \
  "v_ashrrev_i32 v184, 8, v184\n\t" \
  "s_nop 1\n\t" \
  "v_max_i32_dpp v184, v184, v184 quad_perm:[1,0,3,2] row_mask:0xf bank_mask:0xf\n\t" \
  "s_nop 1\n\t" \
  "v_max_i32_dpp v184, v184, v184 quad_perm:[2,3,0,1] row_mask:0xf bank_mask:0xf\n\t" \
  "s_cbranch_vccnz 14f\n\t" \
  "15:\n\t" \
  "s_nop 0\n\t" \
  "v_max_i32_dpp v184, v184, v184 row_half_mirror row_mask:0xf bank_mask:0xf\n\t" \
  "s_nop 1\n\t" \
  "v_max_i32_dpp v184, v184, v184 row_mirror row_mask:0xf bank_mask:0xf\n\t" \
  "s_mov_b64 exec, -1\n\t" \
  "v_readlane_b32 s57, v184, 0\n\t" \
  "v_readlane_b32 s82, v184, 16\n\t" \
  "s_waitcnt lgkmcnt(0)\n\t" \
  "s_sub_i32 s79, s41, s86\n\t" \
  "s_max_i32 s57, s57, s82\n\t" \
  "s_add_i32 s81, s57, 212\n\t" \
  "s_max_i32 s81, s81, 0\n\t" \
  "s_min_i32 s81, s81, 20000\n\t" \
  "s_mul_i32 s81, s81, s44\n\t" \
  "s_lshr_b32 s81, s81, 16\n\t" \
  "s_add_i32 s79, s79, s81\n\t" \
  "s_add_i32 s79, s79, -330\n\t" \
  ".p2align 4\n\t" \
  "3:\n\t" \
  "s_sub_i32 s79, s79, 33\n\t" \
  "s_cmp_gt_i32 s79, 56\n\t" \
  "s_cbranch_scc1 3b\n\t" \
  "buffer_load_dwordx4 v[72:75], v192, %[ru], s88 offen offset:0 sc1\n\t" \
  "s_nop 4\n\t" \
  "buffer_load_dwordx4 v[76:79], v192, %[ru], s88 offen offset:1024 sc1\n\t" \
  "s_nop 4\n\t" \
  "s_add_i32 s79, s79, 12\n\t" \
  ".p2align 4\n\t" \
  "4:\n\t" \
  "s_sub_i32 s79, s79, 33\n\t" \
  "s_cmp_gt_i32 s79, 56\n\t" \
  "s_cbranch_scc1 4b\n\t" \
  "buffer_load_dwordx4 v[80:83], v192, %[ru], s89 offen offset:0 sc1\n\t" \
  "s_nop 4\n\t" \
  "buffer_load_dwordx4 v[84:87], v192, %[ru], s89 offen offset:1024 sc1\n\t" \
  "s_nop 4\n\t" \
  "s_add_i32 s79, s79, 22\n\t" \
  ".p2align 4\n\t" \
  "5:\n\t" \
  "s_sub_i32 s79, s79, 33\n\t" \
  "s_cmp_gt_i32 s79, 56\n\t" \
  "s_cbranch_scc1 5b\n\t" \
  "buffer_load_dwordx4 v[88:91], v192, %[ru], s90 offen offset:0 sc1\n\t" \
  "buffer_load_dwordx4 v[92:95], v192, %[ru], s90 offen offset:1024 sc1\n\t" \
  "s_add_i32 s79, s79, 122\n\t" \
  ".p2align 4\n\t" \
  "9:\n\t" \
  "s_sub_i32 s79, s79, 33\n\t" \
  "s_cmp_gt_i32 s79, 56\n\t" \
  "s_cbranch_scc1 9b\n\t" \
  "buffer_load_dwordx4 v[104:107], v194, %[rh], s92 offen sc1\n\t" \
  "s_nop 4\n\t" \
  "buffer_load_dwordx4 v[108:111], v194, %[rh], s93 offen sc1\n\t" \
  "s_nop 4\n\t" \
  "buffer_load_dwordx4 v[112:115], v194, %[rh], s94 offen sc1\n\t" \
  "s_nop 4\n\t" \
  "buffer_load_dwordx4 v[116:119], v194, %[rh], s95 offen sc1\n\t" \
  "s_nop 4\n\t" \
  "buffer_load_dwordx4 v[96:99], v192, %[ru], s91 offen offset:0 sc1\n\t" \
  "s_nop 4\n\t" \
  "buffer_load_dwordx4 v[100:103], v192, %[ru], s91 offen offset:1024 sc1\n\t" \
  "s_nop 4\n\t" \
  "s_mov_b64 exec, 0xffff\n\t" \
  "buffer_load_dword v185, v203, %[ru], s96 offen sc1\n\t" \
  "s_mov_b64 exec, -1\n\t" \
  "10:\n\t" \
  "s_waitcnt vmcnt(6)\n\t" \
  "v_mfma_f32_16x16x32_bf16 v[136:139], v[104:107], v[72:75], 0\n\t" \
  "v_mfma_f32_16x16x32_bf16 v[140:143], v[104:107], v[76:79], 0\n\t" \
  "s_waitcnt vmcnt(5)\n\t" \
  "v_mfma_f32_16x16x32_bf16 v[136:139], v[108:111], v[80:83], v[136:139]\n\t" \
  "v_mfma_f32_16x16x32_bf16 v[140:143], v[108:111], v[84:87], v[140:143]\n\t" \
  "s_waitcnt vmcnt(4)\n\t" \
  "v_mfma_f32_16x16x32_bf16 v[136:139], v[112:115], v[88:91], v[136:139]\n\t" \
  "v_mfma_f32_16x16x32_bf16 v[140:143], v[112:115], v[92:95], v[140:143]\n\t" \
  "s_waitcnt vmcnt(2)\n\t" \
  "v_mfma_f32_16x16x32_bf16 v[136:139], v[116:119], v[96:99], v[136:139]\n\t" \
  "s_waitcnt vmcnt(1)\n\t" \
  "v_mfma_f32_16x16x32_bf16 v[140:143], v[116:119], v[100:103], v[140:143]\n\t" \
  "s_barrier\n\t" \
  "ds_read2st64_b64 v[144:147], v195 offset1:1\n\t" \
  "s_nop 1\n\t" \
  "v_permlane32_swap_b32 v136, v138\n\t" \
  "v_permlane32_swap_b32 v137, v139\n\t" \
  "s_nop 1\n\t" \
  "v_permlane32_swap_b32 v140, v142\n\t" \
  "v_permlane32_swap_b32 v141, v143\n\t" \
  "v_pk_add_f32 v[136:137], v[136:137], v[142:143]\n\t" \
  "s_waitcnt lgkmcnt(0)\n\t" \
  "v_pk_add_f32 v[136:137], v[136:137], v[144:145]\n\t" \
  "v_pk_add_f32 v[136:137], v[136:137], v[146:147]\n\t" \
  "v_cvt_pk_bf16_f32 v148, v136, v137\n\t" \
  "s_nop 1\n\t" \
  "v_mov_b32_dpp v149, v148 row_half_mirror row_mask:0xf bank_mask:0xf\n\t" \
  "v_perm_b32 v150, v148, v149, v205\n\t" \
  "v_cmpx_o_f16_sdwa vcc, v150, v150 src0_sel:WORD_0 src1_sel:WORD_1\n\t" \
  "6:\n\t" \
  "buffer_store_dword v150, v201, %[rh], s97 offen sc1\n\t" \
  "s_memtime s[82:83]\n\t" \
  "s_mov_b64 exec, -1\n\t" \
  "s_cmp_eq_u64 vcc, -1\n\t" \
  "s_cbranch_scc0 8f\n\t" \
  "v_mov_b32 v151, 0\n\t" \
  "5:\n\t" \
  "ds_write_b32 v206, v151\n\t" \
  "s_mov_b64 exec, 0xffff\n\t" \
  "s_waitcnt vmcnt(1)\n\t" \
  "v_cmp_gt_i32 vcc, s98, v185\n\t" \
  "s_mov_b64 exec, -1\n\t" \
  "s_cmp_eq_u64 vcc, 0\n\t" \
  "s_cselect_b32 s54, s54, 0x70000000\n\t" \
  "buffer_load_dwordx4 v[0:3], v210, %[ru], s54 offen offset:0 sc1\n\t" \
  "buffer_load_dwordx4 v[4:7], v210, %[ru], s54 offen offset:1024 sc1\n\t" \
  "s_add_i32 s54, s54, 0x20000\n\t" \
  "buffer_load_dwordx4 v[8:11], v210, %[ru], s54 offen offset:0 sc1\n\t" \
  "buffer_load_dwordx4 v[12:15], v210, %[ru], s54 offen offset:1024 sc1\n\t" \
  "s_add_i32 s54, s54, 0x20000\n\t" \
  "s_waitcnt lgkmcnt(0)\n\t" \
  "s_mov_b32 s56, s82\n\t" \
  "s_barrier\n\t" \
  "ds_read2_b32 v[148:149], v207 offset1:8\n\t" \
  "s_waitcnt lgkmcnt(0)\n\t" \
  "v_or_b32 v148, v148, v149\n\t" \
  "v_cmp_ne_u32 vcc, 0, v148\n\t" \
  "s_cbranch_vccnz 12f\n\t" \
  "s_and_b32 s58, s58, 4\n\t" \
  "s_add_i32 s80, s57, s43\n\t" \
  "s_sub_i32 s80, s80, s76\n\t" \
  "s_add_i32 s53, s53, -12\n\t" \
  "s_max_i32 s80, s80, s53\n\t" \
  "s_add_i32 s53, s53, 24\n\t" \
  "s_min_i32 s53, s53, s80\n\t" \
  "s_cmp_lt_u32 s54, 0x70000000\n\t" \
  "s_cbranch_scc1 48f\n\t" \
  "25:\n\t" \
  "s_add_i32 s82, s40, 2\n\t" \
  "s_min_i32 s83, s82, 63\n\t" \
  "s_mov_b64 exec, 0xffff\n\t" \
  "v_cmp_gt_i32 vcc, s83, v186\n\t" \
  "s_mov_b64 exec, -1\n\t" \
  "s_and_b32 s54, s82, 15\n\t" \
  "s_lshl_b32 s54, s54, 22\n\t" \
  "s_add_i32 s54, s54, s51\n\t" \
  "s_lshr_b32 s80, s54, 21\n\t" \
  "s_bitcmp0_b64 %[sel], s80\n\t" \
  "s_cselect_b32 s80, 0x2000, 0\n\t" \
  "s_add_i32 s54, s54, s80\n\t" \
  "s_cbranch_vccnz 27f\n\t" \
  "28:\n\t" \
  "s_cmp_ge_i32 s40, 62\n\t" \
  "s_cbranch_scc1 37f\n\t" \
  "buffer_load_dwordx4 v[0:3], v210, %[ru], s54 offen offset:0 sc1\n\t" \
  "buffer_load_dwordx4 v[4:7], v210, %[ru], s54 offen offset:1024 sc1\n\t" \
  "s_add_i32 s54, s54, 0x20000\n\t" \
  "buffer_load_dwordx4 v[8:11], v210, %[ru], s54 offen offset:0 sc1\n\t" \
  "buffer_load_dwordx4 v[12:15], v210, %[ru], s54 offen offset:1024 sc1\n\t" \
  "s_add_i32 s54, s54, 0x20000\n\t" \
  "48:\n\t" \
  "s_and_b32 s76, s40, 3\n\t" \
  "s_lshl_b32 s76, s76, 18\n\t" \
  "s_bitcmp1_b32 s58, 4\n\t" \
  "s_cbranch_scc1 29f\n\t" \
  "s_sub_i32 s80, s56, s41\n\t" \
  "s_mul_hi_u32 s80, s80, s45\n\t" \
  "s_lshl_b32 s80, s80, 1\n\t" \
  "s_sub_i32 s80, s80, s57\n\t" \
  "s_min_u32 s47, s47, s46\n\t" \
  "s_min_u32 s47, s47, s80\n\t" \
  "v_mov_b32 v188, s47\n\t" \
  "s_mov_b32 s47, s46\n\t" \
  "s_mov_b32 s46, s80\n\t" \
  "buffer_store_dword v188, v202, %[rh], s52 offen sc1\n\t" \
  "v_mov_b64 v[188:189], -1\n\t" \
  "v_mov_b64 v[190:191], -1\n\t" \
  "v_mov_b32 v187, s40\n\t" \
  "s_add_i32 s80, s40, -1\n\t" \
  "s_and_b32 s80, s80, 7\n\t" \
  "s_lshl_b32 s80, s80, 15\n\t" \
  "s_add_i32 s80, s80, s49\n\t" \
  "buffer_store_dwordx4 v[188:191], v208, %[rh], s80 offen sc1\n\t" \
  "buffer_store_dword v187, v209, %[ru], 0 offen sc1\n\t" \
  "ds_write_b128 v197, v[72:75] offset:16384\n\t" \
  "ds_write_b128 v197, v[76:79] offset:17408\n\t" \
  "ds_write_b128 v197, v[80:83] offset:18432\n\t" \
  "ds_write_b128 v197, v[84:87] offset:19456\n\t" \
  "ds_write_b128 v197, v[88:91] offset:20480\n\t" \
  "ds_write_b128 v197, v[92:95] offset:21504\n\t" \
  "ds_write_b128 v197, v[96:99] offset:22528\n\t" \
  "ds_write_b128 v197, v[100:103] offset:23552\n\t" \
  "29:\n\t" \
  "s_and_b32 s58, s58, 36\n\t" \
  "buffer_load_dwordx4 v[16:19], v210, %[ru], s54 offen offset:0 sc1\n\t" \
  "buffer_load_dwordx4 v[20:23], v210, %[ru], s54 offen offset:1024 sc1\n\t" \
  "s_add_i32 s54, s54, 0x20000\n\t" \
  "buffer_load_dwordx4 v[24:27], v210, %[ru], s54 offen offset:0 sc1\n\t" \
  "buffer_load_dwordx4 v[28:31], v210, %[ru], s54 offen offset:1024 sc1\n\t" \
  "s_add_i32 s54, s54, 0x20000\n\t" \
  "buffer_load_dwordx4 v[32:35], v210, %[ru], s54 offen offset:0 sc1\n\t" \
  "buffer_load_dwordx4 v[36:39], v210, %[ru], s54 offen offset:1024 sc1\n\t" \
  "s_add_i32 s54, s54, 0x20000\n\t" \
  "buffer_load_dwordx4 v[40:43], v210, %[ru], s54 offen offset:0 sc1\n\t" \
  "buffer_load_dwordx4 v[44:47], v210, %[ru], s54 offen offset:1024 sc1\n\t" \
  "s_add_i32 s54, s54, 0x20000\n\t" \
  "buffer_load_dwordx4 a[0:3], v211, %[rh], s76 offen offset:0 sc1\n\t" \
  "buffer_load_dwordx4 a[4:7], v211, %[rh], s76 offen offset:1024 sc1\n\t" \
  "buffer_load_dwordx4 v[48:51], v210, %[ru], s54 offen offset:0 sc1\n\t" \
  "buffer_load_dwordx4 v[52:55], v210, %[ru], s54 offen offset:1024 sc1\n\t" \
  "s_add_i32 s54, s54, 0x20000\n\t" \
  "buffer_load_dwordx4 v[56:59], v210, %[ru], s54 offen offset:0 sc1\n\t" \
  "buffer_load_dwordx4 v[60:63], v210, %[ru], s54 offen offset:1024 sc1\n\t" \
  "s_add_i32 s54, s54, 0x20000\n\t" \
  "buffer_load_dwordx4 a[8:11], v212, %[rh], s76 offen offset:0 sc1\n\t" \
  "buffer_load_dwordx4 a[12:15], v212, %[rh], s76 offen offset:1024 sc1\n\t" \
  "s_waitcnt lgkmcnt(0)\n\t" \
  "s_barrier\n\t" \
  "buffer_load_dwordx4 v[64:67], v210, %[ru], s54 offen offset:0 sc1\n\t" \
  "buffer_load_dwordx4 v[68:71], v210, %[ru], s54 offen offset:1024 sc1\n\t" \
  "s_add_i32 s54, s54, 0x20000\n\t" \
  "buffer_load_dwordx4 v[104:107], v210, %[ru], s54 offen offset:0 sc1\n\t" \
  "buffer_load_dwordx4 v[108:111], v210, %[ru], s54 offen offset:1024 sc1\n\t" \
  "s_add_i32 s54, s54, 0x20000\n\t" \
  "buffer_load_dwordx4 a[16:19], v213, %[rh], s76 offen offset:0 sc1\n\t" \
  "buffer_load_dwordx4 a[20:23], v213, %[rh], s76 offen offset:1024 sc1\n\t" \
  "buffer_load_dwordx4 v[112:115], v210, %[ru], s54 offen offset:0 sc1\n\t" \
  "buffer_load_dwordx4 v[116:119], v210, %[ru], s54 offen offset:1024 sc1\n\t" \
  "s_add_i32 s54, s54, 0x20000\n\t" \
  "buffer_load_dwordx4 v[120:123], v210, %[ru], s54 offen offset:0 sc1\n\t" \
  "buffer_load_dwordx4 v[124:127], v210, %[ru], s54 offen offset:1024 sc1\n\t" \
  "s_add_i32 s54, s54, 0x20000\n\t" \
  "buffer_load_dwordx4 a[24:27], v214, %[rh], s76 offen offset:0 sc1\n\t" \
  "buffer_load_dwordx4 a[28:31], v214, %[rh], s76 offen offset:1024 sc1\n\t" \
  "ds_read_b128 v[160:163], v193 offset:0\n\t" \
  "ds_read_b128 v[164:167], v193 offset:256\n\t" \
  "ds_read_b128 v[168:171], v193 offset:2048\n\t" \
  "ds_read_b128 v[172:175], v193 offset:2304\n\t" \
  "ds_read_b128 v[176:179], v193 offset:4096\n\t" \
  "ds_read_b128 v[180:183], v193 offset:4352\n\t" \
  "s_waitcnt lgkmcnt(2)\n\t" \
  "v_pk_add_f32 v[160:161], v[160:161], v[168:169]\n\t" \
  "v_pk_add_f32 v[162:163], v[162:163], v[170:171]\n\t" \
  "v_pk_add_f32 v[164:165], v[164:165], v[172:173]\n\t" \
  "v_pk_add_f32 v[166:167], v[166:167], v[174:175]\n\t" \
  "s_waitcnt lgkmcnt(0)\n\t" \
  "v_pk_add_f32 v[160:161], v[160:161], v[176:177]\n\t" \
  "v_pk_add_f32 v[162:163], v[162:163], v[178:179]\n\t" \
  "v_pk_add_f32 v[164:165], v[164:165], v[180:181]\n\t" \
  "v_pk_add_f32 v[166:167], v[166:167], v[182:183]\n\t" \
  "v_cvt_pk_bf16_f32 v160, v160, v161\n\t" \
  "v_cvt_pk_bf16_f32 v161, v162, v163\n\t" \
  "v_cvt_pk_bf16_f32 v162, v164, v165\n\t" \
  "v_cvt_pk_bf16_f32 v163, v166, v167\n\t" \
  "buffer_load_dwordx4 v[128:131], v210, %[ru], s54 offen offset:0 sc1\n\t" \
  "buffer_load_dwordx4 v[132:135], v210, %[ru], s54 offen offset:1024 sc1\n\t" \
  "s_add_i32 s54, s54, 0x20000\n\t" \
  "buffer_load_dwordx4 v[136:139], v210, %[ru], s54 offen offset:0 sc1\n\t" \
  "buffer_load_dwordx4 v[140:143], v210, %[ru], s54 offen offset:1024 sc1\n\t" \
  "s_add_i32 s54, s54, 0x20000\n\t" \
  "s_waitcnt vmcnt(32)\n\t" \
  "v_swap_b32 v1, v8\n\t" \
  "v_swap_b32 v3, v10\n\t" \
  "v_swap_b32 v5, v12\n\t" \
  "v_swap_b32 v7, v14\n\t" \
  "ds_write_b128 v204, v[0:3] offset:0\n\t" \
  "ds_write_b128 v204, v[8:11] offset:1024\n\t" \
  "ds_write_b128 v204, v[4:7] offset:2048\n\t" \
  "ds_write_b128 v204, v[12:15] offset:3072\n\t" \
  "buffer_load_dwordx4 v[144:147], v210, %[ru], s54 offen offset:0 sc1\n\t" \
  "buffer_load_dwordx4 v[148:151], v210, %[ru], s54 offen offset:1024 sc1\n\t" \
  "s_add_i32 s54, s54, 0x20000\n\t" \
  "buffer_load_dwordx4 v[152:155], v210, %[ru], s54 offen offset:0 sc1\n\t" \
  "buffer_load_dwordx4 v[156:159], v210, %[ru], s54 offen offset:1024 sc1\n\t" \
  "s_add_i32 s54, s54, 0x20000\n\t" \
  "s_waitcnt vmcnt(32)\n\t" \
  "v_swap_b32 v17, v24\n\t" \
  "v_swap_b32 v19, v26\n\t" \
  "v_swap_b32 v21, v28\n\t" \
  "v_swap_b32 v23, v30\n\t" \
  "ds_write_b128 v204, v[16:19] offset:4096\n\t" \
  "ds_write_b128 v204, v[24:27] offset:5120\n\t" \
  "ds_write_b128 v204, v[20:23] offset:6144\n\t" \
  "ds_write_b128 v204, v[28:31] offset:7168\n\t" \
  "s_waitcnt vmcnt(28)\n\t" \
  "v_swap_b32 v33, v40\n\t" \
  "v_swap_b32 v35, v42\n\t" \
  "v_swap_b32 v37, v44\n\t" \
  "v_swap_b32 v39, v46\n\t" \
  "ds_write_b128 v204, v[32:35] offset:8192\n\t" \
  "ds_write_b128 v204, v[40:43] offset:9216\n\t" \
  "ds_write_b128 v204, v[36:39] offset:10240\n\t" \
  "ds_write_b128 v204, v[44:47] offset:11264\n\t" \
  "s_waitcnt vmcnt(22)\n\t" \
  "v_swap_b32 v49, v56\n\t" \
  "v_swap_b32 v51, v58\n\t" \
  "v_swap_b32 v53, v60\n\t" \
  "v_swap_b32 v55, v62\n\t" \
  "ds_write_b128 v204, v[48:51] offset:12288\n\t" \
  "ds_write_b128 v204, v[56:59] offset:13312\n\t" \
  "ds_write_b128 v204, v[52:55] offset:14336\n\t" \
  "ds_write_b128 v204, v[60:63] offset:15360\n\t" \
  "s_waitcnt vmcnt(16)\n\t" \
  "v_swap_b32 v65, v104\n\t" \
  "v_swap_b32 v67, v106\n\t" \
  "v_swap_b32 v69, v108\n\t" \
  "v_swap_b32 v71, v110\n\t" \
  "ds_write_b128 v204, v[64:67] offset:16384\n\t" \
  "ds_write_b128 v204, v[104:107] offset:17408\n\t" \
  "ds_write_b128 v204, v[68:71] offset:18432\n\t" \
  "ds_write_b128 v204, v[108:111] offset:19456\n\t" \
  "s_waitcnt vmcnt(10)\n\t" \
  "v_swap_b32 v113, v120\n\t" \
  "v_swap_b32 v115, v122\n\t" \
  "v_swap_b32 v117, v124\n\t" \
  "v_swap_b32 v119, v126\n\t" \
  "ds_write_b128 v204, v[112:115] offset:20480\n\t" \
  "ds_write_b128 v204, v[120:123] offset:21504\n\t" \
  "ds_write_b128 v204, v[116:119] offset:22528\n\t" \
  "ds_write_b128 v204, v[124:127] offset:23552\n\t" \
  "s_waitcnt vmcnt(4)\n\t" \
  "v_swap_b32 v129, v136\n\t" \
  "v_swap_b32 v131, v138\n\t" \
  "v_swap_b32 v133, v140\n\t" \
  "v_swap_b32 v135, v142\n\t" \
  "ds_write_b128 v204, v[128:131] offset:24576\n\t" \
  "ds_write_b128 v204, v[136:139] offset:25600\n\t" \
  "ds_write_b128 v204, v[132:135] offset:26624\n\t" \
  "ds_write_b128 v204, v[140:143] offset:27648\n\t" \
  "s_waitcnt vmcnt(0)\n\t" \
  "v_swap_b32 v145, v152\n\t" \
  "v_swap_b32 v147, v154\n\t" \
  "v_swap_b32 v149, v156\n\t" \
  "v_swap_b32 v151, v158\n\t" \
  "ds_write_b128 v204, v[144:147] offset:28672\n\t" \
  "ds_write_b128 v204, v[152:155] offset:29696\n\t" \
  "ds_write_b128 v204, v[148:151] offset:30720\n\t" \
  "ds_write_b128 v204, v[156:159] offset:31744\n\t" \
  "s_waitcnt lgkmcnt(0)\n\t" \
  "s_cmp_lt_i32 s40, 0\n\t" \
  "s_cbranch_scc1 39f\n\t" \
  "38:\n\t" \
  "s_add_i32 s80, s40, -2\n\t" \
  "s_lshl_b32 s80, s80, 17\n\t" \
  "s_min_u32 s80, s80, 0x70000000\n\t" \
  "buffer_store_dwordx4 v[160:163], v196, %[ro], s80 offen sc1\n\t" \
  "ds_read_b128 a[32:35], v198 offset:0\n\t" \
  "ds_read_b128 a[36:39], v198 offset:1024\n\t" \
  "ds_read_b128 a[40:43], v198 offset:2048\n\t" \
  "ds_read_b128 a[44:47], v198 offset:3072\n\t" \
  "ds_read_b128 a[48:51], v198 offset:4096\n\t" \
  "ds_read_b128 a[52:55], v198 offset:5120\n\t" \
  "ds_read_b128 a[56:59], v198 offset:6144\n\t" \
  "ds_read_b128 a[60:63], v198 offset:7168\n\t" \
  "ds_read_b128 a[64:67], v198 offset:8192\n\t" \
  "ds_read_b128 a[68:71], v198 offset:9216\n\t" \
  "ds_read_b128 a[72:75], v198 offset:10240\n\t" \
  "ds_read_b128 a[76:79], v198 offset:11264\n\t" \
  "ds_read_b128 a[80:83], v198 offset:12288\n\t" \
  "ds_read_b128 a[84:87], v198 offset:13312\n\t" \
  "ds_read_b128 a[88:91], v198 offset:14336\n\t" \
  "ds_read_b128 a[92:95], v198 offset:15360\n\t" \
  "s_waitcnt vmcnt(1) lgkmcnt(0)\n\t" \
  "31:\n\t" \
  "v_mfma_f32_16x16x32_bf16 v[168:171], a[32:35], a[0:3], 0\n\t" \
  "v_mfma_f32_16x16x32_bf16 v[172:175], a[64:67], a[0:3], 0\n\t" \
  "v_mfma_f32_16x16x32_bf16 v[168:171], a[36:39], a[4:7], v[168:171]\n\t" \
  "v_mfma_f32_16x16x32_bf16 v[172:175], a[68:71], a[4:7], v[172:175]\n\t" \
  "v_mfma_f32_16x16x32_bf16 v[168:171], a[40:43], a[8:11], v[168:171]\n\t" \
  "v_mfma_f32_16x16x32_bf16 v[172:175], a[72:75], a[8:11], v[172:175]\n\t" \
  "v_mfma_f32_16x16x32_bf16 v[168:171], a[44:47], a[12:15], v[168:171]\n\t" \
  "v_mfma_f32_16x16x32_bf16 v[172:175], a[76:79], a[12:15], v[172:175]\n\t" \
  "v_mfma_f32_16x16x32_bf16 v[168:171], a[48:51], a[16:19], v[168:171]\n\t" \
  "v_mfma_f32_16x16x32_bf16 v[172:175], a[80:83], a[16:19], v[172:175]\n\t" \
  "v_mfma_f32_16x16x32_bf16 v[168:171], a[52:55], a[20:23], v[168:171]\n\t" \
  "v_mfma_f32_16x16x32_bf16 v[172:175], a[84:87], a[20:23], v[172:175]\n\t" \
  "v_mfma_f32_16x16x32_bf16 v[168:171], a[56:59], a[24:27], v[168:171]\n\t" \
  "v_mfma_f32_16x16x32_bf16 v[172:175], a[88:91], a[24:27], v[172:175]\n\t" \
  "v_mfma_f32_16x16x32_bf16 v[168:171], a[60:63], a[28:31], v[168:171]\n\t" \
  "v_mfma_f32_16x16x32_bf16 v[172:175], a[92:95], a[28:31], v[172:175]\n\t" \
  "s_nop 7\n\t" \
  "39:\n\t" \
  "s_add_i32 s40, s40, 2\n\t" \
  "v_cmp_u_f32 vcc, v168, v168\n\t" \
  "s_cbranch_vccnz 32f\n\t" \
  "33:\n\t" \
  "s_barrier\n\t" \
  "s_add_i32 s85, s40, -1\n\t" \
  "s_cmp_ge_i32 s40, 64\n\t" \
  "s_cbranch_scc1 47f\n\t" \
  "s_and_b32 s82, s85, 2\n\t" \
  "s_lshl_b32 s82, s82, 13\n\t" \
  "s_and_b32 s80, s85, 1\n\t" \
  "s_lshl_b32 s80, s80, 12\n\t" \
  "s_add_i32 s82, s82, s80\n\t" \
  "s_add_i32 s82, s82, 0x84000\n\t" \
  "s_mov_b32 exec_hi, 0\n\t" \
  "buffer_load_dword v184, v200, %[rh], s82 offen sc1\n\t" \
  "buffer_load_dword v186, v203, %[ru], 0 offen sc1\n\t" \
  "s_mov_b64 exec, -1\n\t" \
  "47:\n\t" \
  "ds_write_b128 v199, v[168:171] offset:0\n\t" \
  "ds_write_b128 v199, v[172:175] offset:1024\n\t" \
  "s_mov_b32 s59, 0\n\t" \
  "s_and_b32 s58, s58, 4\n\t" \
  "s_cmp_lt_i32 s40, 64\n\t" \
  "s_cbranch_scc1 20b\n\t" \
  "s_waitcnt lgkmcnt(0)\n\t" \
  "s_barrier\n\t" \
  "ds_read_b128 v[160:163], v193 offset:0\n\t" \
  "ds_read_b128 v[164:167], v193 offset:256\n\t" \
  "ds_read_b128 v[168:171], v193 offset:2048\n\t" \
  "ds_read_b128 v[172:175], v193 offset:2304\n\t" \
  "ds_read_b128 v[176:179], v193 offset:4096\n\t" \
  "ds_read_b128 v[180:183], v193 offset:4352\n\t" \
  "s_waitcnt lgkmcnt(2)\n\t" \
  "v_pk_add_f32 v[160:161], v[160:161], v[168:169]\n\t" \
  "v_pk_add_f32 v[162:163], v[162:163], v[170:171]\n\t" \
  "v_pk_add_f32 v[164:165], v[164:165], v[172:173]\n\t" \
  "v_pk_add_f32 v[166:167], v[166:167], v[174:175]\n\t" \
  "s_waitcnt lgkmcnt(0)\n\t" \
  "v_pk_add_f32 v[160:161], v[160:161], v[176:177]\n\t" \
  "v_pk_add_f32 v[162:163], v[162:163], v[178:179]\n\t" \
  "v_pk_add_f32 v[164:165], v[164:165], v[180:181]\n\t" \
  "v_pk_add_f32 v[166:167], v[166:167], v[182:183]\n\t" \
  "v_cvt_pk_bf16_f32 v160, v160, v161\n\t" \
  "v_cvt_pk_bf16_f32 v161, v162, v163\n\t" \
  "v_cvt_pk_bf16_f32 v162, v164, v165\n\t" \
  "v_cvt_pk_bf16_f32 v163, v166, v167\n\t" \
  "s_add_i32 s80, s40, -2\n\t" \
  "s_lshl_b32 s80, s80, 17\n\t" \
  "s_min_u32 s80, s80, 0x70000000\n\t" \
  "buffer_store_dwordx4 v[160:163], v196, %[ro], s80 offen sc1\n\t" \
  "s_mov_b32 s58, 64\n\t" \
  "s_branch 7f\n\t" \
  "22:\n\t" \
  "s_mov_b32 s81, 0\n\t" \
  "s_cmp_eq_u32 s40, 0\n\t" \
  "s_cbranch_scc1 24b\n\t" \
  "s_bitcmp1_b32 s58, 2\n\t" \
  "s_cbranch_scc1 24b\n\t" \
  "s_mov_b64 exec, -1\n\t" \
  "s_sleep 10\n\t" \
  "s_add_u32 s59, s59, 0x10000\n\t" \
  "s_cmp_lt_u32 s59, 0x400000\n\t" \
  "s_cbranch_scc1 19b\n\t" \
  "s_or_b32 s58, s58, 1\n\t" \
  "s_branch 7f\n\t" \
  "14:\n\t" \
  "v_mov_b32 v184, 0\n\t" \
  "s_bitcmp1_b32 s58, 2\n\t" \
  "s_nop 0\n\t" \
  "s_cbranch_scc1 15b\n\t" \
  "s_mov_b32 s84, 910\n\t" \
  "s_add_u32 s59, s59, 0x10000\n\t" \
  "s_cmp_lt_u32 s59, 0x400000\n\t" \
  "s_mov_b64 exec, vcc\n\t" \
  "buffer_load_dword v187, v200, %[rh], s78 offen sc1\n\t" \
  "s_mov_b32 exec_lo, -1\n\t" \
  "s_cbranch_scc1 13b\n\t" \
  "s_mov_b64 exec, -1\n\t" \
  "s_or_b32 s58, s58, 1\n\t" \
  "s_branch 7f\n\t" \
  "27:\n\t" \
  "s_bitcmp1_b32 s58, 2\n\t" \
  "s_cbranch_scc1 28b\n\t" \
  "s_bitcmp1_b32 s58, 4\n\t" \
  "s_cbranch_scc1 23f\n\t" \
  "s_sub_i32 s80, s56, s41\n\t" \
  "s_mul_hi_u32 s80, s80, s45\n\t" \
  "s_lshl_b32 s80, s80, 1\n\t" \
  "s_sub_i32 s80, s80, s57\n\t" \
  "s_min_u32 s47, s47, s46\n\t" \
  "s_min_u32 s47, s47, s80\n\t" \
  "v_mov_b32 v188, s47\n\t" \
  "s_mov_b32 s47, s46\n\t" \
  "s_mov_b32 s46, s80\n\t" \
  "buffer_store_dword v188, v202, %[rh], s52 offen sc1\n\t" \
  "v_mov_b64 v[188:189], -1\n\t" \
  "v_mov_b64 v[190:191], -1\n\t" \
  "v_mov_b32 v187, s40\n\t" \
  "s_add_i32 s80, s40, -1\n\t" \
  "s_and_b32 s80, s80, 7\n\t" \
  "s_lshl_b32 s80, s80, 15\n\t" \
  "s_add_i32 s80, s80, s49\n\t" \
  "buffer_store_dwordx4 v[188:191], v208, %[rh], s80 offen sc1\n\t" \
  "buffer_store_dword v187, v209, %[ru], 0 offen sc1\n\t" \
  "ds_write_b128 v197, v[72:75] offset:16384\n\t" \
  "ds_write_b128 v197, v[76:79] offset:17408\n\t" \
  "ds_write_b128 v197, v[80:83] offset:18432\n\t" \
  "ds_write_b128 v197, v[84:87] offset:19456\n\t" \
  "ds_write_b128 v197, v[88:91] offset:20480\n\t" \
  "ds_write_b128 v197, v[92:95] offset:21504\n\t" \
  "ds_write_b128 v197, v[96:99] offset:22528\n\t" \
  "ds_write_b128 v197, v[100:103] offset:23552\n\t" \
  "s_or_b32 s58, s58, 16\n\t" \
  "23:\n\t" \
  "s_add_u32 s59, s59, 0x1000000\n\t" \
  "s_cmp_lt_u32 s59, 0x40000000\n\t" \
  "s_cbranch_scc1 26f\n\t" \
  "s_or_b32 s58, s58, 1\n\t" \
  "s_branch 7f\n\t" \
  "26:\n\t" \
  "buffer_load_dword v186, v203, %[ru], 0 offen sc1\n\t" \
  "s_waitcnt vmcnt(0)\n\t" \
  "s_branch 25b\n\t" \
  "37:\n\t" \
  "s_and_b32 s76, s40, 3\n\t" \
  "s_lshl_b32 s76, s76, 18\n\t" \
  "buffer_load_dwordx4 a[0:3], v211, %[rh], s76 offen offset:0 sc1\n\t" \
  "buffer_load_dwordx4 a[4:7], v211, %[rh], s76 offen offset:1024 sc1\n\t" \
  "buffer_load_dwordx4 a[8:11], v212, %[rh], s76 offen offset:0 sc1\n\t" \
  "buffer_load_dwordx4 a[12:15], v212, %[rh], s76 offen offset:1024 sc1\n\t" \
  "buffer_load_dwordx4 a[16:19], v213, %[rh], s76 offen offset:0 sc1\n\t" \
  "buffer_load_dwordx4 a[20:23], v213, %[rh], s76 offen offset:1024 sc1\n\t" \
  "buffer_load_dwordx4 a[24:27], v214, %[rh], s76 offen offset:0 sc1\n\t" \
  "buffer_load_dwordx4 a[28:31], v214, %[rh], s76 offen offset:1024 sc1\n\t" \
  "s_bitcmp1_b32 s58, 4\n\t" \
  "s_cbranch_scc1 29f\n\t" \
  "s_sub_i32 s80, s56, s41\n\t" \
  "s_mul_hi_u32 s80, s80, s45\n\t" \
  "s_lshl_b32 s80, s80, 1\n\t" \
  "s_sub_i32 s80, s80, s57\n\t" \
  "s_min_u32 s47, s47, s46\n\t" \
  "s_min_u32 s47, s47, s80\n\t" \
  "v_mov_b32 v188, s47\n\t" \
  "s_mov_b32 s47, s46\n\t" \
  "s_mov_b32 s46, s80\n\t" \
  "buffer_store_dword v188, v202, %[rh], s52 offen sc1\n\t" \
  "v_mov_b64 v[188:189], -1\n\t" \
  "v_mov_b64 v[190:191], -1\n\t" \
  "v_mov_b32 v187, s40\n\t" \
  "s_add_i32 s80, s40, -1\n\t" \
  "s_and_b32 s80, s80, 7\n\t" \
  "s_lshl_b32 s80, s80, 15\n\t" \
  "s_add_i32 s80, s80, s49\n\t" \
  "buffer_store_dwordx4 v[188:191], v208, %[rh], s80 offen sc1\n\t" \
  "buffer_store_dword v187, v209, %[ru], 0 offen sc1\n\t" \
  "ds_write_b128 v197, v[72:75] offset:16384\n\t" \
  "ds_write_b128 v197, v[76:79] offset:17408\n\t" \
  "ds_write_b128 v197, v[80:83] offset:18432\n\t" \
  "ds_write_b128 v197, v[84:87] offset:19456\n\t" \
  "ds_write_b128 v197, v[88:91] offset:20480\n\t" \
  "ds_write_b128 v197, v[92:95] offset:21504\n\t" \
  "ds_write_b128 v197, v[96:99] offset:22528\n\t" \
  "ds_write_b128 v197, v[100:103] offset:23552\n\t" \
  "29:\n\t" \
  "s_and_b32 s58, s58, 36\n\t" \
  "s_waitcnt lgkmcnt(0)\n\t" \
  "s_barrier\n\t" \
  "ds_read_b128 v[160:163], v193 offset:0\n\t" \
  "ds_read_b128 v[164:167], v193 offset:256\n\t" \
  "ds_read_b128 v[168:171], v193 offset:2048\n\t" \
  "ds_read_b128 v[172:175], v193 offset:2304\n\t" \
  "ds_read_b128 v[176:179], v193 offset:4096\n\t" \
  "ds_read_b128 v[180:183], v193 offset:4352\n\t" \
  "s_waitcnt lgkmcnt(2)\n\t" \
  "v_pk_add_f32 v[160:161], v[160:161], v[168:169]\n\t" \
  "v_pk_add_f32 v[162:163], v[162:163], v[170:171]\n\t" \
  "v_pk_add_f32 v[164:165], v[164:165], v[172:173]\n\t" \
  "v_pk_add_f32 v[166:167], v[166:167], v[174:175]\n\t" \
  "s_waitcnt lgkmcnt(0)\n\t" \
  "v_pk_add_f32 v[160:161], v[160:161], v[176:177]\n\t" \
  "v_pk_add_f32 v[162:163], v[162:163], v[178:179]\n\t" \
  "v_pk_add_f32 v[164:165], v[164:165], v[180:181]\n\t" \
  "v_pk_add_f32 v[166:167], v[166:167], v[182:183]\n\t" \
  "v_cvt_pk_bf16_f32 v160, v160, v161\n\t" \
  "v_cvt_pk_bf16_f32 v161, v162, v163\n\t" \
  "v_cvt_pk_bf16_f32 v162, v164, v165\n\t" \
  "v_cvt_pk_bf16_f32 v163, v166, v167\n\t" \
  "s_branch 38b\n\t" \
  "8:\n\t" \
  "s_mov_b64 vcc, -1\n\t" \
  "s_and_b32 s80, s58, 6\n\t" \
  "s_cbranch_scc1 6b\n\t" \
  "v_mov_b32 v151, 1\n\t" \
  "s_branch 5b\n\t" \
  "32:\n\t" \
  "s_and_b32 s80, s58, 36\n\t" \
  "s_cbranch_scc1 33b\n\t" \
  "s_add_i32 s40, s40, -2\n\t" \
  "34:\n\t" \
  "s_or_b32 s59, s59, 128\n\t" \
  "s_and_b32 s76, s40, 3\n\t" \
  "s_lshl_b32 s76, s76, 18\n\t" \
  "35:\n\t" \
  "buffer_load_dwordx4 a[0:3], v211, %[rh], s76 offen offset:0 sc1\n\t" \
  "buffer_load_dwordx4 a[4:7], v211, %[rh], s76 offen offset:1024 sc1\n\t" \
  "buffer_load_dwordx4 a[8:11], v212, %[rh], s76 offen offset:0 sc1\n\t" \
  "buffer_load_dwordx4 a[12:15], v212, %[rh], s76 offen offset:1024 sc1\n\t" \
  "buffer_load_dwordx4 a[16:19], v213, %[rh], s76 offen offset:0 sc1\n\t" \
  "buffer_load_dwordx4 a[20:23], v213, %[rh], s76 offen offset:1024 sc1\n\t" \
  "buffer_load_dwordx4 a[24:27], v214, %[rh], s76 offen offset:0 sc1\n\t" \
  "buffer_load_dwordx4 a[28:31], v214, %[rh], s76 offen offset:1024 sc1\n\t" \
  "s_waitcnt vmcnt(0)\n\t" \
  "s_bitcmp1_b32 s58, 2\n\t" \
  "s_cbranch_scc1 36f\n\t" \
  "v_accvgpr_read_b32 v0, a0\n\t" \
  "v_accvgpr_read_b32 v1, a1\n\t" \
  "v_accvgpr_read_b32 v2, a2\n\t" \
  "v_accvgpr_read_b32 v3, a3\n\t" \
  "v_accvgpr_read_b32 v4, a4\n\t" \
  "v_accvgpr_read_b32 v5, a5\n\t" \
  "v_accvgpr_read_b32 v6, a6\n\t" \
  "v_accvgpr_read_b32 v7, a7\n\t" \
  "v_accvgpr_read_b32 v8, a8\n\t" \
  "v_accvgpr_read_b32 v9, a9\n\t" \
  "v_accvgpr_read_b32 v10, a10\n\t" \
  "v_accvgpr_read_b32 v11, a11\n\t" \
  "v_accvgpr_read_b32 v12, a12\n\t" \
  "v_accvgpr_read_b32 v13, a13\n\t" \
  "v_accvgpr_read_b32 v14, a14\n\t" \
  "v_accvgpr_read_b32 v15, a15\n\t" \
  "v_accvgpr_read_b32 v16, a16\n\t" \
  "v_accvgpr_read_b32 v17, a17\n\t" \
  "v_accvgpr_read_b32 v18, a18\n\t" \
  "v_accvgpr_read_b32 v19, a19\n\t" \
  "v_accvgpr_read_b32 v20, a20\n\t" \
  "v_accvgpr_read_b32 v21, a21\n\t" \
  "v_accvgpr_read_b32 v22, a22\n\t" \
  "v_accvgpr_read_b32 v23, a23\n\t" \
  "v_accvgpr_read_b32 v24, a24\n\t" \
  "v_accvgpr_read_b32 v25, a25\n\t" \
  "v_accvgpr_read_b32 v26, a26\n\t" \
  "v_accvgpr_read_b32 v27, a27\n\t" \
  "v_accvgpr_read_b32 v28, a28\n\t" \
  "v_accvgpr_read_b32 v29, a29\n\t" \
  "v_accvgpr_read_b32 v30, a30\n\t" \
  "v_accvgpr_read_b32 v31, a31\n\t" \
  "v_max_u32 v148, v0, v1\n\t" \
  "v_max3_u32 v148, v148, v2, v3\n\t" \
  "v_max3_u32 v148, v148, v4, v5\n\t" \
  "v_max3_u32 v148, v148, v6, v7\n\t" \
  "v_max3_u32 v148, v148, v8, v9\n\t" \
  "v_max3_u32 v148, v148, v10, v11\n\t" \
  "v_max3_u32 v148, v148, v12, v13\n\t" \
  "v_max3_u32 v148, v148, v14, v15\n\t" \
  "v_max3_u32 v148, v148, v16, v17\n\t" \
  "v_max3_u32 v148, v148, v18, v19\n\t" \
  "v_max3_u32 v148, v148, v20, v21\n\t" \
  "v_max3_u32 v148, v148, v22, v23\n\t" \
  "v_max3_u32 v148, v148, v24, v25\n\t" \
  "v_max3_u32 v148, v148, v26, v27\n\t" \
  "v_max3_u32 v148, v148, v28, v29\n\t" \
  "v_max3_u32 v148, v148, v30, v31\n\t" \
  "v_cmp_eq_u32 vcc, -1, v148\n\t" \
  "s_cbranch_vccz 36f\n\t" \
  "s_add_u32 s59, s59, 0x1000000\n\t" \
  "s_cmp_lt_u32 s59, 0x10000000\n\t" \
  "s_cbranch_scc1 35b\n\t" \
  "s_or_b32 s58, s58, 9\n\t" \
  "s_branch 7f\n\t" \
  "36:\n\t" \
  "s_and_b32 s58, s58, 4\n\t" \
  "s_or_b32 s58, s58, 32\n\t" \
  "s_branch 31b\n\t" \
  "18:\n\t" \
  "v_mov_b32 v186, -1\n\t" \
  "s_and_b32 s80, s40, 3\n\t" \
  "s_mul_i32 s77, s80, 0xc000\n\t" \
  "s_add_i32 s77, s77, 0x40000\n\t" \
  "s_lshl_b32 s78, s80, 14\n\t" \
  "s_add_i32 s78, s78, 0x70000\n\t" \
  "s_mov_b32 s57, 0\n\t" \
  "s_add_i32 s96, s77, 0x1000\n\t" \
  "s_lshl_b32 s88, s80, 20\n\t" \
  "s_add_i32 s88, s88, s48\n\t" \
  "s_lshr_b32 s80, s88, 21\n\t" \
  "s_bitcmp1_b64 %[sel], s80\n\t" \
  "s_cselect_b32 s80, 0x2000, 0\n\t" \
  "s_add_i32 s88, s88, s80\n\t" \
  "s_xor_b32 s89, s88, 0x800\n\t" \
  "s_xor_b32 s90, s88, 0x1000\n\t" \
  "s_xor_b32 s91, s88, 0x1800\n\t" \
  "s_add_i32 s92, s77, s50\n\t" \
  "s_xor_b32 s93, s92, 0x100\n\t" \
  "s_xor_b32 s94, s92, 0x200\n\t" \
  "s_xor_b32 s95, s92, 0x300\n\t" \
  "s_add_i32 s97, s40, 1\n\t" \
  "s_and_b32 s97, s97, 7\n\t" \
  "s_lshl_b32 s97, s97, 15\n\t" \
  "s_add_i32 s97, s97, s49\n\t" \
  "12:\n\t" \
  "s_or_b32 s58, s58, 3\n\t" \
  "s_mov_b32 s54, 0x70000000\n\t" \
  "11:\n\t" \
  "buffer_load_dwordx4 v[72:75], v192, %[ru], s88 offen offset:0 sc1\n\t" \
  "buffer_load_dwordx4 v[76:79], v192, %[ru], s88 offen offset:1024 sc1\n\t" \
  "buffer_load_dwordx4 v[80:83], v192, %[ru], s89 offen offset:0 sc1\n\t" \
  "buffer_load_dwordx4 v[84:87], v192, %[ru], s89 offen offset:1024 sc1\n\t" \
  "buffer_load_dwordx4 v[88:91], v192, %[ru], s90 offen offset:0 sc1\n\t" \
  "buffer_load_dwordx4 v[92:95], v192, %[ru], s90 offen offset:1024 sc1\n\t" \
  "buffer_load_dwordx4 v[96:99], v192, %[ru], s91 offen offset:0 sc1\n\t" \
  "buffer_load_dwordx4 v[100:103], v192, %[ru], s91 offen offset:1024 sc1\n\t" \
  "buffer_load_dwordx4 v[104:107], v194, %[rh], s92 offen sc1\n\t" \
  "buffer_load_dwordx4 v[108:111], v194, %[rh], s93 offen sc1\n\t" \
  "buffer_load_dwordx4 v[112:115], v194, %[rh], s94 offen sc1\n\t" \
  "buffer_load_dwordx4 v[116:119], v194, %[rh], s95 offen sc1\n\t" \
  "s_waitcnt vmcnt(0)\n\t" \
  "s_add_u32 s59, s59, 1\n\t" \
  "s_bitcmp1_b32 s58, 2\n\t" \
  "s_cbranch_scc1 10b\n\t" \
  "v_max_u32 v148, v72, v73\n\t" \
  "v_max3_u32 v148, v148, v74, v75\n\t" \
  "v_max3_u32 v148, v148, v76, v77\n\t" \
  "v_max3_u32 v148, v148, v78, v79\n\t" \
  "v_max3_u32 v148, v148, v80, v81\n\t" \
  "v_max3_u32 v148, v148, v82, v83\n\t" \
  "v_max3_u32 v148, v148, v84, v85\n\t" \
  "v_max3_u32 v148, v148, v86, v87\n\t" \
  "v_max3_u32 v148, v148, v88, v89\n\t" \
  "v_max3_u32 v148, v148, v90, v91\n\t" \
  "v_max3_u32 v148, v148, v92, v93\n\t" \
  "v_max3_u32 v148, v148, v94, v95\n\t" \
  "v_max3_u32 v148, v148, v96, v97\n\t" \
  "v_max3_u32 v148, v148, v98, v99\n\t" \
  "v_max3_u32 v148, v148, v100, v101\n\t" \
  "v_max3_u32 v148, v148, v102, v103\n\t" \
  "v_max3_u32 v148, v148, v104, v105\n\t" \
  "v_max3_u32 v148, v148, v106, v107\n\t" \
  "v_max3_u32 v148, v148, v108, v109\n\t" \
  "v_max3_u32 v148, v148, v110, v111\n\t" \
  "v_max3_u32 v148, v148, v112, v113\n\t" \
  "v_max3_u32 v148, v148, v114, v115\n\t" \
  "v_max3_u32 v148, v148, v116, v117\n\t" \
  "v_max3_u32 v148, v148, v118, v119\n\t" \
  "v_cmp_eq_u32 vcc, -1, v148\n\t" \
  "s_cbranch_vccz 10b\n\t" \
  "s_bitcmp1_b32 s59, 6\n\t" \
  "s_cbranch_scc0 11b\n\t" \
  "7:\n\t" \
  "s_branch 92f\n\t" \
  "91:\n\t" \
  "s_mov_b32 s90, s41\n\t" \
  "s_mov_b32 s91, s43\n\t" \
  "s_mov_b32 s58, s42\n\t" \
  "s_mov_b32 s59, 0\n\t" \
  "s_bitcmp1_b32 s58, 4\n\t" \
  "s_cbranch_scc1 37f\n\t" \
  "s_bitcmp1_b32 s58, 3\n\t" \
  "s_cbranch_scc1 34f\n\t" \
  "s_bitcmp1_b32 s58, 1\n\t" \
  "s_cbranch_scc1 18f\n\t" \
  "17:\n\t" \
  "v_mov_b64 v[40:41], 0\n\t" \
  "v_mov_b64 v[42:43], 0\n\t" \
  "v_mov_b64 v[44:45], 0\n\t" \
  "v_mov_b64 v[46:47], 0\n\t" \
  "v_mov_b64 v[48:49], 0\n\t" \
  "v_mov_b64 v[50:51], 0\n\t" \
  "v_mov_b64 v[52:53], 0\n\t" \
  "v_mov_b64 v[54:55], 0\n\t" \
  "v_mov_b64 v[56:57], 0\n\t" \
  "v_mov_b64 v[58:59], 0\n\t" \
  "v_mov_b64 v[60:61], 0\n\t" \
  "v_mov_b64 v[62:63], 0\n\t" \
  "v_mov_b64 v[64:65], 0\n\t" \
  "v_mov_b64 v[66:67], 0\n\t" \
  "v_mov_b64 v[68:69], 0\n\t" \
  "v_mov_b64 v[70:71], 0\n\t" \
  "v_mov_b64 v[72:73], 0\n\t" \
  "v_mov_b64 v[74:75], 0\n\t" \
  "v_mov_b64 v[76:77], 0\n\t" \
  "v_mov_b64 v[78:79], 0\n\t" \
  "v_mov_b64 v[80:81], 0\n\t" \
  "v_mov_b64 v[82:83], 0\n\t" \
  "v_mov_b64 v[84:85], 0\n\t" \
  "v_mov_b64 v[86:87], 0\n\t" \
  "v_mov_b64 v[88:89], 0\n\t" \
  "v_mov_b64 v[90:91], 0\n\t" \
  "v_mov_b64 v[92:93], 0\n\t" \
  "v_mov_b64 v[94:95], 0\n\t" \
  "v_mov_b64 v[96:97], 0\n\t" \
  "v_mov_b64 v[98:99], 0\n\t" \
  "v_mov_b64 v[100:101], 0\n\t" \
  "v_mov_b64 v[102:103], 0\n\t" \
  "v_mov_b64 v[136:137], 0\n\t" \
  "v_mov_b64 v[138:139], 0\n\t" \
  "v_mov_b64 v[140:141], 0\n\t" \
  "v_mov_b64 v[142:143], 0\n\t" \
  "19:\n\t" \
  "s_add_i32 s85, s40, -1\n\t" \
  "s_and_b32 s82, s85, 2\n\t" \
  "s_lshl_b32 s82, s82, 13\n\t" \
  "s_and_b32 s80, s85, 1\n\t" \
  "s_lshl_b32 s80, s80, 12\n\t" \
  "s_add_i32 s82, s82, s80\n\t" \
  "s_add_i32 s82, s82, 0x84000\n\t" \
  "s_mov_b32 exec_hi, 0\n\t" \
  "buffer_load_dword v184, v200, %[rh], s82 offen sc1\n\t" \
  "buffer_load_dword v185, v201, %[rh], s52 offen sc1\n\t" \
  "s_mov_b64 exec, -1\n\t" \
  "20:\n\t" \
  "s_and_b32 s80, s40, 3\n\t" \
  "s_mul_i32 s77, s80, 0xc000\n\t" \
  "s_add_i32 s77, s77, 0x40000\n\t" \
  "s_lshl_b32 s78, s80, 14\n\t" \
  "s_add_i32 s78, s78, 0x70000\n\t" \
  "s_mov_b32 s57, 0\n\t" \
  "s_add_i32 s96, s77, 0x1000\n\t" \
  "s_and_b32 s98, s40, 2\n\t" \
  "s_lshl_b32 s98, s98, 13\n\t" \
  "s_and_b32 s80, s40, 1\n\t" \
  "s_lshl_b32 s80, s80, 12\n\t" \
  "s_add_i32 s98, s98, s80\n\t" \
  "s_add_i32 s98, s98, 0x84000\n\t" \
  "s_memtime vcc\n\t" \
  "s_memrealtime s[86:87]\n\t" \
  "s_nop 4\n\t" \
  "s_memrealtime s[60:61]\n\t" \
  "s_nop 4\n\t" \
  "s_memrealtime s[62:63]\n\t" \
  "s_nop 4\n\t" \
  "s_memrealtime s[64:65]\n\t" \
  "s_nop 4\n\t" \
  "s_memrealtime s[66:67]\n\t" \
  "s_nop 4\n\t" \
  "s_memrealtime s[68:69]\n\t" \
  "s_mov_b32 s79, 0\n\t" \
  "s_waitcnt lgkmcnt(0)\n\t" \
  "s_cmp_eq_u32 s86, s60\n\t" \
  "s_addc_u32 s79, s79, 0\n\t" \
  "s_cmp_eq_u32 s86, s62\n\t" \
  "s_addc_u32 s79, s79, 0\n\t" \
  "s_cmp_eq_u32 s86, s64\n\t" \
  "s_addc_u32 s79, s79, 0\n\t" \
  "s_cmp_eq_u32 s86, s66\n\t" \
  "s_addc_u32 s79, s79, 0\n\t" \
  "s_cmp_eq_u32 s86, s68\n\t" \
  "s_addc_u32 s79, s79, 0\n\t" \
  "s_mul_i32 s79, s79, 24\n\t" \
  "s_add_i32 s79, s79, vcc_lo\n\t" \
  "s_lshl4_add_u32 s80, s86, 64\n\t" \
  "s_mov_b32 s41, s79\n\t" \
  "s_mov_b32 s43, s80\n\t" \
  "s_lshl_b32 s83, s43, 8\n\t" \
  "s_mov_b32 s84, 610\n\t" \
  "s_mov_b32 exec_hi, 0\n\t" \
  "s_waitcnt vmcnt(0)\n\t" \
  "v_cmp_ne_u32_sdwa vcc, v184, s85 src0_sel:BYTE_0 src1_sel:DWORD\n\t" \
  "v_subrev_u32 v184, s83, v184\n\t" \
  "v_ashrrev_i32 v184, 8, v184\n\t" \
  "s_cbranch_vccnz 22f\n\t" \
  "v_max_i32_dpp v185, v185, v185 quad_perm:[1,0,3,2] row_mask:0xf bank_mask:0xf\n\t" \
  "v_max_i32_dpp v184, v184, v184 quad_perm:[1,0,3,2] row_mask:0xf bank_mask:0xf\n\t" \
  "s_nop 0\n\t" \
  "v_max_i32_dpp v185, v185, v185 quad_perm:[2,3,0,1] row_mask:0xf bank_mask:0xf\n\t" \
  "v_max_i32_dpp v184, v184, v184 quad_perm:[2,3,0,1] row_mask:0xf bank_mask:0xf\n\t" \
  "s_nop 0\n\t" \
  "v_max_i32_dpp v185, v185, v185 row_half_mirror row_mask:0xf bank_mask:0xf\n\t" \
  "v_max_i32_dpp v184, v184, v184 row_half_mirror row_mask:0xf bank_mask:0xf\n\t" \
  "v_readlane_b32 s54, v185, 0\n\t" \
  "s_nop 0\n\t" \
  "v_max_i32_dpp v184, v184, v184 row_mirror row_mask:0xf bank_mask:0xf\n\t" \
  "s_nop 0\n\t" \
  "v_readlane_b32 s81, v184, 0\n\t" \
  "v_readlane_b32 s82, v184, 16\n\t" \
  "s_max_i32 s81, s81, s82\n\t" \
  "s_add_i32 s76, s81, s43\n\t" \
  "s_add_i32 s81, s81, s53\n\t" \
  "s_max_i32 s81, s81, 0\n\t" \
  "s_min_i32 s81, s81, 20000\n\t" \
  "s_mul_i32 s81, s81, s44\n\t" \
  "s_lshr_b32 s81, s81, 16\n\t" \
  "s_add_i32 s81, s81, -1020\n\t" \
  "24:\n\t" \
  "s_add_i32 s81, s81, s41\n\t" \
  "s_min_u32 s54, s54, 1760\n\t" \
  "s_add_i32 s54, s54, 225\n\t" \
  ".p2align 6\n\t" \
  "s_nop 0\n\t" \
  "s_nop 0\n\t" \
  "s_nop 0\n\t" \
  "s_nop 0\n\t" \
  "s_nop 0\n\t" \
  "s_nop 0\n\t" \
  "s_nop 0\n\t" \
  "s_nop 0\n\t" \
  "1:\n\t" \
  "s_memtime vcc\n\t" \
  "s_waitcnt lgkmcnt(0)\n\t" \
  "s_sub_i32 s79, s81, vcc_lo\n\t" \
  "s_cmp_gt_i32 s79, 128\n\t" \
  "s_cbranch_scc1 1b\n\t" \
  ".p2align 4\n\t" \
  "2:\n\t" \
  "s_sub_i32 s79, s79, 33\n\t" \
  "s_cmp_gt_i32 s79, 56\n\t" \
  "s_cbranch_scc1 2b\n\t" \
  "buffer_load_dword v187, v200, %[rh], s78 offen sc1\n\t" \
  "s_memtime vcc\n\t" \
  "s_memrealtime s[86:87]\n\t" \
  "s_nop 4\n\t" \
  "s_memrealtime s[60:61]\n\t" \
  "s_nop 4\n\t" \
  "s_memrealtime s[62:63]\n\t" \
  "s_nop 4\n\t" \
  "s_memrealtime s[64:65]\n\t" \
  "s_nop 4\n\t" \
  "s_memrealtime s[66:67]\n\t" \
  "s_nop 4\n\t" \
  "s_memrealtime s[68:69]\n\t" \
  "s_mov_b32 s79, 0\n\t" \
  "s_waitcnt lgkmcnt(0)\n\t" \
  "s_mov_b64 exec, -1\n\t" \
  "ds_read_b128 a[0:3], v194 offset:0\n\t" \
  "ds_read_b128 a[4:7], v194 offset:1024\n\t" \
  "ds_read_b128 a[8:11], v194 offset:2048\n\t" \
  "ds_read_b128 a[12:15], v194 offset:3072\n\t" \
  "ds_read_b128 a[16:19], v194 offset:4096\n\t" \
  "ds_read_b128 a[20:23], v194 offset:5120\n\t" \
  "ds_read_b128 a[24:27], v194 offset:6144\n\t" \
  "ds_read_b128 a[28:31], v194 offset:7168\n\t" \
  "ds_read_b128 a[32:35], v194 offset:8192\n\t" \
  "ds_read_b128 a[36:39], v194 offset:9216\n\t" \
  "ds_read_b128 a[40:43], v194 offset:10240\n\t" \
  "ds_read_b128 a[44:47], v194 offset:11264\n\t" \
  "ds_read_b128 a[48:51], v194 offset:12288\n\t" \
  "ds_read_b128 a[52:55], v194 offset:13312\n\t" \
  "ds_read_b128 a[56:59], v194 offset:14336\n\t" \
  "ds_read_b128 a[60:63], v194 offset:15360\n\t" \
  "ds_read_b128 a[64:67], v194 offset:16384\n\t" \
  "ds_read_b128 a[68:71], v194 offset:17408\n\t" \
  "ds_read_b128 a[72:75], v194 offset:18432\n\t" \
  "ds_read_b128 a[76:79], v194 offset:19456\n\t" \
  "ds_read_b128 a[80:83], v194 offset:20480\n\t" \
  "ds_read_b128 a[84:87], v194 offset:21504\n\t" \
  "ds_read_b128 a[88:91], v194 offset:22528\n\t" \
  "ds_read_b128 a[92:95], v194 offset:23552\n\t" \
  "ds_read_b128 a[96:99], v194 offset:24576\n\t" \
  "ds_read_b128 a[100:103], v194 offset:25600\n\t" \
  "ds_read_b128 a[104:107], v194 offset:26624\n\t" \
  "ds_read_b128 a[108:111], v194 offset:27648\n\t" \
  "ds_read_b128 a[112:115], v194 offset:28672\n\t" \
  "ds_read_b128 a[116:119], v194 offset:29696\n\t" \
  "ds_read_b128 a[120:123], v194 offset:30720\n\t" \
  "ds_read_b128 a[124:127], v194 offset:31744\n\t" \
  "s_mov_b32 exec_hi, 0\n\t" \
  "s_cmp_eq_u32 s86, s60\n\t" \
  "s_addc_u32 s79, s79, 0\n\t" \
  "s_cmp_eq_u32 s86, s62\n\t" \
  "s_addc_u32 s79, s79, 0\n\t" \
  "s_cmp_eq_u32 s86, s64\n\t" \
  "s_addc_u32 s79, s79, 0\n\t" \
  "s_cmp_eq_u32 s86, s66\n\t" \
  "s_addc_u32 s79, s79, 0\n\t" \
  "s_cmp_eq_u32 s86, s68\n\t" \
  "s_addc_u32 s79, s79, 0\n\t" \
  "s_mul_i32 s79, s79, 24\n\t" \
  "s_add_i32 s79, s79, vcc_lo\n\t" \
  "s_lshl4_add_u32 s80, s86, 64\n\t" \
  "s_mov_b32 s41, s79\n\t" \
  "s_mov_b32 s43, s80\n\t" \
  "s_lshl_b32 s83, s43, 8\n\t" \
  "s_add_i32 s54, s54, s43\n\t" \
  "13:\n\t" \
  "s_waitcnt vmcnt(0)\n\t" \
  "s_memtime s[86:87]\n\t" \
  "v_cmp_ne_u32_sdwa vcc, v187, s40 src0_sel:BYTE_0 src1_sel:DWORD\n\t" \
  "v_subrev_u32 v184, s83, v187\n\t" \
  "v_ashrrev_i32 v184, 8, v184\n\t" \
  "s_nop 1\n\t" \
  "v_max_i32_dpp v184, v184, v184 quad_perm:[1,0,3,2] row_mask:0xf bank_mask:0xf\n\t" \
  "s_nop 1\n\t" \
  "v_max_i32_dpp v184, v184, v184 quad_perm:[2,3,0,1] row_mask:0xf bank_mask:0xf\n\t" \
  "s_cbranch_vccnz 14f\n\t" \
  "15:\n\t" \
  "s_nop 0\n\t" \
  "v_max_i32_dpp v184, v184, v184 row_half_mirror row_mask:0xf bank_mask:0xf\n\t" \
  "s_nop 1\n\t" \
  "v_max_i32_dpp v184, v184, v184 row_mirror row_mask:0xf bank_mask:0xf\n\t" \
  "s_mov_b64 exec, -1\n\t" \
  "v_readlane_b32 s57, v184, 0\n\t" \
  "v_readlane_b32 s82, v184, 16\n\t" \
  "s_waitcnt lgkmcnt(0)\n\t" \
  "s_sub_i32 s79, s41, s86\n\t" \
  "s_max_i32 s57, s57, s82\n\t" \
  "s_add_i32 s81, s57, 212\n\t" \
  "s_max_i32 s81, s81, 0\n\t" \
  "s_min_i32 s81, s81, 20000\n\t" \
  "s_mul_i32 s81, s81, s44\n\t" \
  "s_lshr_b32 s81, s81, 16\n\t" \
  "s_add_i32 s79, s79, s81\n\t" \
  "s_sub_i32 s85, s84, s79\n\t" \
  "s_max_i32 s85, s85, 0\n\t" \
  "s_mul_hi_u32 s85, s85, s45\n\t" \
  "s_lshl1_add_u32 s85, s85, s57\n\t" \
  "s_add_i32 s85, s85, s54\n\t" \
  "s_lshl_b32 s85, s85, 8\n\t" \
  "s_or_b32 s85, s85, s40\n\t" \
  "v_mov_b32 v184, s85\n\t" \
  "buffer_store_dword v184, v202, %[rh], s98 offen sc1\n\t" \
  "s_add_i32 s79, s79, -620\n\t" \
  ".p2align 4\n\t" \
  "3:\n\t" \
  "s_sub_i32 s79, s79, 33\n\t" \
  "s_cmp_gt_i32 s79, 56\n\t" \
  "s_cbranch_scc1 3b\n\t" \
  "buffer_load_dwordx4 v[0:3], v193, %[rh], s77 offen offset:0 sc1\n\t" \
  "s_nop 4\n\t" \
  "buffer_load_dwordx4 v[4:7], v193, %[rh], s77 offen offset:1024 sc1\n\t" \
  "s_nop 4\n\t" \
  "buffer_load_dwordx4 v[8:11], v193, %[rh], s77 offen offset:2048 sc1\n\t" \
  "s_nop 4\n\t" \
  "buffer_load_dwordx4 v[12:15], v193, %[rh], s77 offen offset:3072 sc1\n\t" \
  "s_nop 4\n\t" \
  "buffer_load_dwordx4 v[16:19], v193, %[rh], s96 offen offset:0 sc1\n\t" \
  "s_nop 4\n\t" \
  "buffer_load_dwordx4 v[20:23], v193, %[rh], s96 offen offset:1024 sc1\n\t" \
  "s_nop 4\n\t" \
  "buffer_load_dwordx4 v[24:27], v193, %[rh], s96 offen offset:2048 sc1\n\t" \
  "s_nop 4\n\t" \
  "buffer_load_dwordx4 v[28:31], v193, %[rh], s96 offen offset:3072 sc1\n\t" \
  "s_add_i32 s79, s79, 267\n\t" \
  ".p2align 4\n\t" \
  "4:\n\t" \
  "s_sub_i32 s79, s79, 33\n\t" \
  "s_cmp_gt_i32 s79, 56\n\t" \
  "s_cbranch_scc1 4b\n\t" \
  "buffer_load_dwordx4 v[32:35], v195, %[rh], s77 offen offset:0 sc1\n\t" \
  "buffer_load_dwordx4 v[36:39], v195, %[rh], s77 offen offset:2048 sc1\n\t" \
  "10:\n\t" \
  "s_waitcnt vmcnt(9)\n\t" \
  "v_smfmac_f32_16x16x64_bf16 v[40:43], v[0:3], a[0:7], v203\n\t" \
  "v_smfmac_f32_16x16x64_bf16 v[56:59], v[0:3], a[8:15], v203\n\t" \
  "s_waitcnt vmcnt(8)\n\t" \
  "v_smfmac_f32_16x16x64_bf16 v[44:47], v[4:7], a[16:23], v203\n\t" \
  "v_cvt_pk_bf16_f32 v40, v40, v41\n\t" \
  "v_cvt_pk_bf16_f32 v41, v42, v43\n\t" \
  "v_smfmac_f32_16x16x64_bf16 v[60:63], v[4:7], a[24:31], v203\n\t" \
  "v_cvt_pk_bf16_f32 v56, v56, v57\n\t" \
  "v_cvt_pk_bf16_f32 v57, v58, v59\n\t" \
  "s_waitcnt vmcnt(7)\n\t" \
  "v_smfmac_f32_16x16x64_bf16 v[48:51], v[8:11], a[32:39], v203\n\t" \
  "v_cvt_pk_bf16_f32 v42, v44, v45\n\t" \
  "v_cvt_pk_bf16_f32 v43, v46, v47\n\t" \
  "v_smfmac_f32_16x16x64_bf16 v[64:67], v[8:11], a[40:47], v203\n\t" \
  "v_cvt_pk_bf16_f32 v58, v60, v61\n\t" \
  "v_cvt_pk_bf16_f32 v59, v62, v63\n\t" \
  "s_waitcnt vmcnt(6)\n\t" \
  "v_smfmac_f32_16x16x64_bf16 v[52:55], v[12:15], a[48:55], v203\n\t" \
  "v_cvt_pk_bf16_f32 v44, v48, v49\n\t" \
  "v_cvt_pk_bf16_f32 v45, v50, v51\n\t" \
  "v_smfmac_f32_16x16x64_bf16 v[68:71], v[12:15], a[56:63], v203\n\t" \
  "v_cvt_pk_bf16_f32 v60, v64, v65\n\t" \
  "v_cvt_pk_bf16_f32 v61, v66, v67\n\t" \
  "s_waitcnt vmcnt(5)\n\t" \
  "v_smfmac_f32_16x16x64_bf16 v[72:75], v[16:19], a[64:71], v203\n\t" \
  "v_cvt_pk_bf16_f32 v46, v52, v53\n\t" \
  "v_cvt_pk_bf16_f32 v47, v54, v55\n\t" \
  "v_smfmac_f32_16x16x64_bf16 v[88:91], v[16:19], a[72:79], v203\n\t" \
  "v_cvt_pk_bf16_f32 v62, v68, v69\n\t" \
  "v_cvt_pk_bf16_f32 v63, v70, v71\n\t" \
  "s_waitcnt vmcnt(4)\n\t" \
  "v_smfmac_f32_16x16x64_bf16 v[76:79], v[20:23], a[80:87], v203\n\t" \
  "v_cvt_pk_bf16_f32 v72, v72, v73\n\t" \
  "v_cvt_pk_bf16_f32 v73, v74, v75\n\t" \
  "v_smfmac_f32_16x16x64_bf16 v[92:95], v[20:23], a[88:95], v203\n\t" \
  "v_cvt_pk_bf16_f32 v88, v88, v89\n\t" \
  "v_cvt_pk_bf16_f32 v89, v90, v91\n\t" \
  "s_waitcnt vmcnt(3)\n\t" \
  "v_smfmac_f32_16x16x64_bf16 v[80:83], v[24:27], a[96:103], v203\n\t" \
  "v_cvt_pk_bf16_f32 v74, v76, v77\n\t" \
  "v_cvt_pk_bf16_f32 v75, v78, v79\n\t" \
  "v_smfmac_f32_16x16x64_bf16 v[96:99], v[24:27], a[104:111], v203\n\t" \
  "v_cvt_pk_bf16_f32 v90, v92, v93\n\t" \
  "v_cvt_pk_bf16_f32 v91, v94, v95\n\t" \
  "s_waitcnt vmcnt(2)\n\t" \
  "v_smfmac_f32_16x16x64_bf16 v[84:87], v[28:31], a[112:119], v203\n\t" \
  "v_cvt_pk_bf16_f32 v76, v80, v81\n\t" \
  "v_cvt_pk_bf16_f32 v77, v82, v83\n\t" \
  "v_smfmac_f32_16x16x64_bf16 v[100:103], v[28:31], a[120:127], v203\n\t" \
  "v_cvt_pk_bf16_f32 v92, v96, v97\n\t" \
  "v_cvt_pk_bf16_f32 v93, v98, v99\n\t" \
  "s_nop 1\n\t" \
  "v_cvt_pk_bf16_f32 v78, v84, v85\n\t" \
  "v_cvt_pk_bf16_f32 v79, v86, v87\n\t" \
  "s_nop 1\n\t" \
  "v_cvt_pk_bf16_f32 v94, v100, v101\n\t" \
  "v_cvt_pk_bf16_f32 v95, v102, v103\n\t" \
  "s_waitcnt vmcnt(1)\n\t" \
  "v_smfmac_f32_16x16x64_bf16 v[136:139], v[32:35], v[40:47], v204\n\t" \
  "v_smfmac_f32_16x16x64_bf16 v[140:143], v[32:35], v[56:63], v204\n\t" \
  "s_waitcnt vmcnt(0)\n\t" \
  "v_smfmac_f32_16x16x64_bf16 v[136:139], v[36:39], v[72:79], v204\n\t" \
  "v_smfmac_f32_16x16x64_bf16 v[140:143], v[36:39], v[88:95], v204\n\t" \
  "s_nop 3\n\t" \
  "v_permlane32_swap_b32 v136, v138\n\t" \
  "v_permlane32_swap_b32 v137, v139\n\t" \
  "v_pk_add_f32 v[136:137], v[136:137], v[138:139]\n\t" \
  "s_nop 0\n\t" \
  "v_permlane32_swap_b32 v140, v142\n\t" \
  "v_permlane32_swap_b32 v141, v143\n\t" \
  "v_pk_add_f32 v[138:139], v[140:141], v[142:143]\n\t" \
  "ds_write2st64_b64 v197, v[136:137], v[138:139] offset1:2\n\t" \
  "s_waitcnt lgkmcnt(0)\n\t" \
  "s_barrier\n\t" \
  "s_sub_i32 s86, s41, s90\n\t" \
  "s_sub_i32 s87, s43, s91\n\t" \
  "s_mov_b32 s90, s41\n\t" \
  "s_mov_b32 s91, s43\n\t" \
  "s_cmp_lt_u32 s86, 4000\n\t" \
  "s_cbranch_scc1 21f\n\t" \
  "v_cvt_f32_u32 v165, s86\n\t" \
  "v_cvt_f32_u32 v164, s87\n\t" \
  "v_rcp_f32 v165, v165\n\t" \
  "s_nop 0\n\t" \
  "v_mul_f32 v165, v165, v164\n\t" \
  "v_max_f32 v165, 0x3f028f5c, v165\n\t" \
  "v_min_f32 v165, 0x3ffeb852, v165\n\t" \
  "v_rcp_f32 v164, v165\n\t" \
  "v_mul_f32 v165, 0x4f000000, v165\n\t" \
  "v_mul_f32 v164, 0x47800000, v164\n\t" \
  "v_cvt_u32_f32 v165, v165\n\t" \
  "v_cvt_u32_f32 v164, v164\n\t" \
  "s_nop 0\n\t" \
  "v_readfirstlane_b32 s45, v165\n\t" \
  "v_readfirstlane_b32 s44, v164\n\t" \
  "21:\n\t" \
  "s_barrier\n\t" \
  "ds_read2_b32 v[148:149], v207 offset1:8\n\t" \
  "s_waitcnt lgkmcnt(0)\n\t" \
  "v_or_b32 v148, v148, v149\n\t" \
  "v_cmp_ne_u32 vcc, 0, v148\n\t" \
  "s_cbranch_vccnz 12f\n\t" \
  "s_and_b32 s58, s58, 4\n\t" \
  "s_add_i32 s80, s57, s43\n\t" \
  "s_sub_i32 s80, s80, s76\n\t" \
  "s_add_i32 s53, s53, -12\n\t" \
  "s_max_i32 s80, s80, s53\n\t" \
  "s_add_i32 s53, s53, 24\n\t" \
  "s_min_i32 s53, s53, s80\n\t" \
  "s_and_b32 s76, s40, 3\n\t" \
  "s_lshl_b32 s76, s76, 18\n\t" \
  "s_add_i32 s77, s76, 0x10000\n\t" \
  "s_add_i32 s78, s76, 0x1000\n\t" \
  "s_add_i32 s79, s76, 0x11000\n\t" \
  "s_mov_b32 s97, 1\n\t" \
  "s_cmp_ge_i32 s40, 62\n\t" \
  "s_cbranch_scc1 31f\n\t" \
  "s_and_b32 s82, s40, 3\n\t" \
  "s_lshl_b32 s82, s82, 20\n\t" \
  "s_add_i32 s82, s82, s48\n\t" \
  "s_andn2_b32 s82, s82, 0x1800\n\t" \
  "s_lshr_b32 s80, s82, 21\n\t" \
  "s_bitcmp1_b64 %[sel], s80\n\t" \
  "s_cselect_b32 s80, 0x2000, 0\n\t" \
  "s_add_i32 s82, s82, s80\n\t" \
  "s_add_i32 s83, s82, 0x1000\n\t" \
  "v_mov_b64 v[188:189], -1\n\t" \
  "v_mov_b64 v[190:191], -1\n\t" \
  "s_sleep 0\n\t" \
  "buffer_store_dwordx4 v[188:191], v192, %[ru], s82 offen offset:0 sc1\n\t" \
  "buffer_store_dwordx4 v[188:191], v192, %[ru], s82 offen offset:1024 sc1\n\t" \
  "buffer_store_dwordx4 v[188:191], v192, %[ru], s82 offen offset:2048 sc1\n\t" \
  "buffer_store_dwordx4 v[188:191], v192, %[ru], s82 offen offset:3072 sc1\n\t" \
  "buffer_store_dwordx4 v[188:191], v192, %[ru], s83 offen offset:0 sc1\n\t" \
  "buffer_store_dwordx4 v[188:191], v192, %[ru], s83 offen offset:1024 sc1\n\t" \
  "buffer_store_dwordx4 v[188:191], v192, %[ru], s83 offen offset:2048 sc1\n\t" \
  "buffer_store_dwordx4 v[188:191], v192, %[ru], s83 offen offset:3072 sc1\n\t" \
  "31:\n\t" \
  "buffer_load_dwordx4 v[0:3], v210, %[rh], s76 offen offset:0 sc1\n\t" \
  "s_nop 10\n\t" \
  "buffer_load_dwordx4 v[4:7], v210, %[rh], s76 offen offset:1024 sc1\n\t" \
  "s_nop 10\n\t" \
  "buffer_load_dwordx4 v[8:11], v210, %[rh], s77 offen offset:0 sc1\n\t" \
  "s_nop 10\n\t" \
  "buffer_load_dwordx4 v[12:15], v210, %[rh], s77 offen offset:1024 sc1\n\t" \
  "s_cmp_eq_u32 s97, 0\n\t" \
  "s_cbranch_scc1 26f\n\t" \
  "s_barrier\n\t" \
  "s_mov_b32 s97, 0\n\t" \
  "26:\n\t" \
  "buffer_load_dwordx4 v[16:19], v210, %[rh], s76 offen offset:2048 sc1\n\t" \
  "s_nop 10\n\t" \
  "buffer_load_dwordx4 v[20:23], v210, %[rh], s76 offen offset:3072 sc1\n\t" \
  "s_nop 10\n\t" \
  "buffer_load_dwordx4 v[24:27], v210, %[rh], s77 offen offset:2048 sc1\n\t" \
  "s_nop 10\n\t" \
  "buffer_load_dwordx4 v[28:31], v210, %[rh], s77 offen offset:3072 sc1\n\t" \
  "s_nop 10\n\t" \
  "buffer_load_dwordx4 v[32:35], v210, %[rh], s78 offen offset:0 sc1\n\t" \
  "s_nop 10\n\t" \
  "buffer_load_dwordx4 v[36:39], v210, %[rh], s78 offen offset:1024 sc1\n\t" \
  "s_nop 10\n\t" \
  "buffer_load_dwordx4 v[40:43], v210, %[rh], s79 offen offset:0 sc1\n\t" \
  "s_nop 10\n\t" \
  "buffer_load_dwordx4 v[44:47], v210, %[rh], s79 offen offset:1024 sc1\n\t" \
  "s_nop 10\n\t" \
  "buffer_load_dwordx4 v[48:51], v210, %[rh], s78 offen offset:2048 sc1\n\t" \
  "s_nop 10\n\t" \
  "buffer_load_dwordx4 v[52:55], v210, %[rh], s78 offen offset:3072 sc1\n\t" \
  "s_nop 10\n\t" \
  "buffer_load_dwordx4 v[56:59], v210, %[rh], s79 offen offset:2048 sc1\n\t" \
  "s_nop 10\n\t" \
  "buffer_load_dwordx4 v[60:63], v210, %[rh], s79 offen offset:3072 sc1\n\t" \
  "s_nop 10\n\t" \
  "buffer_load_dwordx4 v[64:67], v211, %[rh], s76 offen offset:0 sc1\n\t" \
  "s_nop 10\n\t" \
  "buffer_load_dwordx4 v[68:71], v211, %[rh], s76 offen offset:1024 sc1\n\t" \
  "s_nop 10\n\t" \
  "buffer_load_dwordx4 v[72:75], v211, %[rh], s77 offen offset:0 sc1\n\t" \
  "s_nop 10\n\t" \
  "buffer_load_dwordx4 v[76:79], v211, %[rh], s77 offen offset:1024 sc1\n\t" \
  "s_nop 10\n\t" \
  "buffer_load_dwordx4 v[80:83], v211, %[rh], s76 offen offset:2048 sc1\n\t" \
  "s_nop 10\n\t" \
  "buffer_load_dwordx4 v[84:87], v211, %[rh], s76 offen offset:3072 sc1\n\t" \
  "s_nop 10\n\t" \
  "buffer_load_dwordx4 v[88:91], v211, %[rh], s77 offen offset:2048 sc1\n\t" \
  "s_nop 10\n\t" \
  "buffer_load_dwordx4 v[92:95], v211, %[rh], s77 offen offset:3072 sc1\n\t" \
  "s_nop 10\n\t" \
  "v_mov_b64 v[136:137], 0\n\t" \
  "v_mov_b64 v[138:139], 0\n\t" \
  "v_mov_b64 v[140:141], 0\n\t" \
  "v_mov_b64 v[142:143], 0\n\t" \
  "s_waitcnt vmcnt(22)\n\t" \
  "v_swap_b32 v1, v4\n\t" \
  "v_swap_b32 v3, v6\n\t" \
  "s_nop 1\n\t" \
  "v_mfma_f32_16x16x32_bf16 v[168:171], a[0:3], v[0:3], 0\n\t" \
  "buffer_load_dwordx4 v[96:99], v211, %[rh], s78 offen offset:0 sc1\n\t" \
  "v_mfma_f32_16x16x32_bf16 v[172:175], a[8:11], v[0:3], 0\n\t" \
  "s_waitcnt vmcnt(21)\n\t" \
  "v_swap_b32 v9, v12\n\t" \
  "v_mfma_f32_16x16x32_bf16 v[168:171], a[4:7], v[4:7], v[168:171]\n\t" \
  "v_swap_b32 v11, v14\n\t" \
  "v_mfma_f32_16x16x32_bf16 v[172:175], a[12:15], v[4:7], v[172:175]\n\t" \
  "buffer_load_dwordx4 v[100:103], v211, %[rh], s78 offen offset:1024 sc1\n\t" \
  "v_mfma_f32_16x16x32_bf16 v[176:179], a[0:3], v[8:11], 0\n\t" \
  "buffer_load_dwordx4 v[104:107], v211, %[rh], s79 offen offset:0 sc1\n\t" \
  "v_mfma_f32_16x16x32_bf16 v[180:183], a[8:11], v[8:11], 0\n\t" \
  "s_waitcnt vmcnt(21)\n\t" \
  "v_swap_b32 v17, v20\n\t" \
  "v_mfma_f32_16x16x32_bf16 v[176:179], a[4:7], v[12:15], v[176:179]\n\t" \
  "v_swap_b32 v19, v22\n\t" \
  "v_mfma_f32_16x16x32_bf16 v[180:183], a[12:15], v[12:15], v[180:183]\n\t" \
  "buffer_load_dwordx4 v[108:111], v211, %[rh], s79 offen offset:1024 sc1\n\t" \
  "v_mfma_f32_16x16x32_bf16 v[168:171], a[16:19], v[16:19], v[168:171]\n\t" \
  "buffer_load_dwordx4 v[112:115], v211, %[rh], s78 offen offset:2048 sc1\n\t" \
  "v_mfma_f32_16x16x32_bf16 v[172:175], a[24:27], v[16:19], v[172:175]\n\t" \
  "s_waitcnt vmcnt(21)\n\t" \
  "v_swap_b32 v25, v28\n\t" \
  "v_mfma_f32_16x16x32_bf16 v[168:171], a[20:23], v[20:23], v[168:171]\n\t" \
  "v_swap_b32 v27, v30\n\t" \
  "v_mfma_f32_16x16x32_bf16 v[172:175], a[28:31], v[20:23], v[172:175]\n\t" \
  "buffer_load_dwordx4 v[116:119], v211, %[rh], s78 offen offset:3072 sc1\n\t" \
  "v_mfma_f32_16x16x32_bf16 v[176:179], a[16:19], v[24:27], v[176:179]\n\t" \
  "buffer_load_dwordx4 v[120:123], v211, %[rh], s79 offen offset:2048 sc1\n\t" \
  "v_mfma_f32_16x16x32_bf16 v[180:183], a[24:27], v[24:27], v[180:183]\n\t" \
  "s_waitcnt vmcnt(21)\n\t" \
  "v_swap_b32 v33, v36\n\t" \
  "v_mfma_f32_16x16x32_bf16 v[176:179], a[20:23], v[28:31], v[176:179]\n\t" \
  "v_swap_b32 v35, v38\n\t" \
  "v_mfma_f32_16x16x32_bf16 v[180:183], a[28:31], v[28:31], v[180:183]\n\t" \
  "buffer_load_dwordx4 v[124:127], v211, %[rh], s79 offen offset:3072 sc1\n\t" \
  "v_mfma_f32_16x16x32_bf16 v[168:171], a[32:35], v[32:35], v[168:171]\n\t" \
  "v_mfma_f32_16x16x32_bf16 v[172:175], a[40:43], v[32:35], v[172:175]\n\t" \
  "s_waitcnt vmcnt(20)\n\t" \
  "v_swap_b32 v41, v44\n\t" \
  "v_mfma_f32_16x16x32_bf16 v[168:171], a[36:39], v[36:39], v[168:171]\n\t" \
  "v_swap_b32 v43, v46\n\t" \
  "v_mfma_f32_16x16x32_bf16 v[172:175], a[44:47], v[36:39], v[172:175]\n\t" \
  "s_nop 0\n\t" \
  "v_mfma_f32_16x16x32_bf16 v[176:179], a[32:35], v[40:43], v[176:179]\n\t" \
  "v_mfma_f32_16x16x32_bf16 v[180:183], a[40:43], v[40:43], v[180:183]\n\t" \
  "s_waitcnt vmcnt(18)\n\t" \
  "v_swap_b32 v49, v52\n\t" \
  "v_mfma_f32_16x16x32_bf16 v[176:179], a[36:39], v[44:47], v[176:179]\n\t" \
  "v_swap_b32 v51, v54\n\t" \
  "v_mfma_f32_16x16x32_bf16 v[180:183], a[44:47], v[44:47], v[180:183]\n\t" \
  "v_mov_b64 v[40:41], 0\n\t" \
  "v_mov_b64 v[42:43], 0\n\t" \
  "v_mfma_f32_16x16x32_bf16 v[168:171], a[48:51], v[48:51], v[168:171]\n\t" \
  "v_mov_b64 v[44:45], 0\n\t" \
  "v_mov_b64 v[46:47], 0\n\t" \
  "v_mfma_f32_16x16x32_bf16 v[172:175], a[56:59], v[48:51], v[172:175]\n\t" \
  "s_waitcnt vmcnt(16)\n\t" \
  "v_swap_b32 v57, v60\n\t" \
  "v_mfma_f32_16x16x32_bf16 v[168:171], a[52:55], v[52:55], v[168:171]\n\t" \
  "v_swap_b32 v59, v62\n\t" \
  "v_mfma_f32_16x16x32_bf16 v[172:175], a[60:63], v[52:55], v[172:175]\n\t" \
  "v_mov_b64 v[48:49], 0\n\t" \
  "v_mov_b64 v[50:51], 0\n\t" \
  "v_mfma_f32_16x16x32_bf16 v[176:179], a[48:51], v[56:59], v[176:179]\n\t" \
  "v_mov_b64 v[52:53], 0\n\t" \
  "v_mov_b64 v[54:55], 0\n\t" \
  "v_mfma_f32_16x16x32_bf16 v[180:183], a[56:59], v[56:59], v[180:183]\n\t" \
  "s_waitcnt vmcnt(14)\n\t" \
  "v_swap_b32 v65, v68\n\t" \
  "v_mfma_f32_16x16x32_bf16 v[176:179], a[52:55], v[60:63], v[176:179]\n\t" \
  "v_swap_b32 v67, v70\n\t" \
  "v_mfma_f32_16x16x32_bf16 v[180:183], a[60:63], v[60:63], v[180:183]\n\t" \
  "v_mov_b64 v[56:57], 0\n\t" \
  "v_mov_b64 v[58:59], 0\n\t" \
  "v_mfma_f32_16x16x32_bf16 v[168:171], a[64:67], v[64:67], v[168:171]\n\t" \
  "v_mov_b64 v[60:61], 0\n\t" \
  "v_mov_b64 v[62:63], 0\n\t" \
  "v_mfma_f32_16x16x32_bf16 v[172:175], a[72:75], v[64:67], v[172:175]\n\t" \
  "s_waitcnt vmcnt(12)\n\t" \
  "v_swap_b32 v73, v76\n\t" \
  "v_mfma_f32_16x16x32_bf16 v[168:171], a[68:71], v[68:71], v[168:171]\n\t" \
  "v_swap_b32 v75, v78\n\t" \
  "v_mfma_f32_16x16x32_bf16 v[172:175], a[76:79], v[68:71], v[172:175]\n\t" \
  "v_mov_b64 v[64:65], 0\n\t" \
  "v_mov_b64 v[66:67], 0\n\t" \
  "v_mfma_f32_16x16x32_bf16 v[176:179], a[64:67], v[72:75], v[176:179]\n\t" \
  "v_mov_b64 v[68:69], 0\n\t" \
  "v_mov_b64 v[70:71], 0\n\t" \
  "v_mfma_f32_16x16x32_bf16 v[180:183], a[72:75], v[72:75], v[180:183]\n\t" \
  "s_waitcnt vmcnt(10)\n\t" \
  "v_swap_b32 v81, v84\n\t" \
  "v_mfma_f32_16x16x32_bf16 v[176:179], a[68:71], v[76:79], v[176:179]\n\t" \
  "v_swap_b32 v83, v86\n\t" \
  "v_mfma_f32_16x16x32_bf16 v[180:183], a[76:79], v[76:79], v[180:183]\n\t" \
  "v_mov_b64 v[72:73], 0\n\t" \
  "v_mov_b64 v[74:75], 0\n\t" \
  "v_mfma_f32_16x16x32_bf16 v[168:171], a[80:83], v[80:83], v[168:171]\n\t" \
  "v_mov_b64 v[76:77], 0\n\t" \
  "v_mov_b64 v[78:79], 0\n\t" \
  "v_mfma_f32_16x16x32_bf16 v[172:175], a[88:91], v[80:83], v[172:175]\n\t" \
  "s_waitcnt vmcnt(8)\n\t" \
  "v_swap_b32 v89, v92\n\t" \
  "v_mfma_f32_16x16x32_bf16 v[168:171], a[84:87], v[84:87], v[168:171]\n\t" \
  "v_swap_b32 v91, v94\n\t" \
  "v_mfma_f32_16x16x32_bf16 v[172:175], a[92:95], v[84:87], v[172:175]\n\t" \
  "v_mov_b64 v[80:81], 0\n\t" \
  "v_mov_b64 v[82:83], 0\n\t" \
  "v_mfma_f32_16x16x32_bf16 v[176:179], a[80:83], v[88:91], v[176:179]\n\t" \
  "v_mov_b64 v[84:85], 0\n\t" \
  "v_mov_b64 v[86:87], 0\n\t" \
  "v_mfma_f32_16x16x32_bf16 v[180:183], a[88:91], v[88:91], v[180:183]\n\t" \
  "s_waitcnt vmcnt(6)\n\t" \
  "v_swap_b32 v97, v100\n\t" \
  "v_mfma_f32_16x16x32_bf16 v[176:179], a[84:87], v[92:95], v[176:179]\n\t" \
  "v_swap_b32 v99, v102\n\t" \
  "v_mfma_f32_16x16x32_bf16 v[180:183], a[92:95], v[92:95], v[180:183]\n\t" \
  "v_mov_b64 v[88:89], 0\n\t" \
  "v_mov_b64 v[90:91], 0\n\t" \
  "v_mfma_f32_16x16x32_bf16 v[168:171], a[96:99], v[96:99], v[168:171]\n\t" \
  "v_mov_b64 v[92:93], 0\n\t" \
  "v_mov_b64 v[94:95], 0\n\t" \
  "v_mfma_f32_16x16x32_bf16 v[172:175], a[104:107], v[96:99], v[172:175]\n\t" \
  "s_waitcnt vmcnt(4)\n\t" \
  "v_swap_b32 v105, v108\n\t" \
  "v_mfma_f32_16x16x32_bf16 v[168:171], a[100:103], v[100:103], v[168:171]\n\t" \
  "v_swap_b32 v107, v110\n\t" \
  "v_mfma_f32_16x16x32_bf16 v[172:175], a[108:111], v[100:103], v[172:175]\n\t" \
  "v_mov_b64 v[96:97], 0\n\t" \
  "v_mov_b64 v[98:99], 0\n\t" \
  "v_mfma_f32_16x16x32_bf16 v[176:179], a[96:99], v[104:107], v[176:179]\n\t" \
  "v_mov_b64 v[100:101], 0\n\t" \
  "v_mov_b64 v[102:103], 0\n\t" \
  "v_mfma_f32_16x16x32_bf16 v[180:183], a[104:107], v[104:107], v[180:183]\n\t" \
  "s_waitcnt vmcnt(2)\n\t" \
  "v_swap_b32 v113, v116\n\t" \
  "v_mfma_f32_16x16x32_bf16 v[176:179], a[100:103], v[108:111], v[176:179]\n\t" \
  "v_swap_b32 v115, v118\n\t" \
  "v_mfma_f32_16x16x32_bf16 v[180:183], a[108:111], v[108:111], v[180:183]\n\t" \
  "s_nop 0\n\t" \
  "s_add_i32 s85, s40, 1\n\t" \
  "s_and_b32 s82, s85, 2\n\t" \
  "s_lshl_b32 s82, s82, 13\n\t" \
  "s_and_b32 s80, s85, 1\n\t" \
  "s_lshl_b32 s80, s80, 12\n\t" \
  "s_add_i32 s82, s82, s80\n\t" \
  "s_add_i32 s82, s82, 0x84000\n\t" \
  "s_mov_b32 exec_hi, 0\n\t" \
  "buffer_load_dword v184, v200, %[rh], s82 offen sc1\n\t" \
  "buffer_load_dword v185, v201, %[rh], s52 offen sc1\n\t" \
  "s_mov_b64 exec, -1\n\t" \
  "v_mfma_f32_16x16x32_bf16 v[168:171], a[112:115], v[112:115], v[168:171]\n\t" \
  "v_mfma_f32_16x16x32_bf16 v[172:175], a[120:123], v[112:115], v[172:175]\n\t" \
  "s_waitcnt vmcnt(2)\n\t" \
  "v_swap_b32 v121, v124\n\t" \
  "v_mfma_f32_16x16x32_bf16 v[168:171], a[116:119], v[116:119], v[168:171]\n\t" \
  "v_swap_b32 v123, v126\n\t" \
  "v_mfma_f32_16x16x32_bf16 v[172:175], a[124:127], v[116:119], v[172:175]\n\t" \
  "s_nop 0\n\t" \
  "v_mfma_f32_16x16x32_bf16 v[176:179], a[112:115], v[120:123], v[176:179]\n\t" \
  "v_mfma_f32_16x16x32_bf16 v[180:183], a[120:123], v[120:123], v[180:183]\n\t" \
  "v_mfma_f32_16x16x32_bf16 v[176:179], a[116:119], v[124:127], v[176:179]\n\t" \
  "v_mfma_f32_16x16x32_bf16 v[180:183], a[124:127], v[124:127], v[180:183]\n\t" \
  "s_nop 6\n\t" \
  "39:\n\t" \
  "s_add_i32 s40, s40, 2\n\t" \
  "v_cmp_u_f32 vcc, v168, v176\n\t" \
  "s_cbranch_vccnz 32f\n\t" \
  "33:\n\t" \
  "s_barrier\n\t" \
  "s_add_i32 s85, s40, -1\n\t" \
  "s_cmp_ge_i32 s40, 64\n\t" \
  "s_cbranch_scc1 47f\n\t" \
  "47:\n\t" \
  "ds_write_b128 v199, v[168:171] offset:0\n\t" \
  "ds_write_b128 v199, v[172:175] offset:1024\n\t" \
  "ds_write_b128 v199, v[176:179] offset:8192\n\t" \
  "ds_write_b128 v199, v[180:183] offset:9216\n\t" \
  "s_mov_b32 s59, 0\n\t" \
  "s_and_b32 s58, s58, 4\n\t" \
  "s_cmp_lt_i32 s40, 64\n\t" \
  "s_cbranch_scc1 20b\n\t" \
  "s_waitcnt lgkmcnt(0)\n\t" \
  "s_barrier\n\t" \
  "s_mov_b32 s58, 64\n\t" \
  "s_branch 7f\n\t" \
  "22:\n\t" \
  "s_mov_b32 s81, 0\n\t" \
  "s_mov_b32 s54, s46\n\t" \
  "s_cmp_eq_u32 s40, 0\n\t" \
  "s_cbranch_scc1 24b\n\t" \
  "s_bitcmp1_b32 s58, 2\n\t" \
  "s_cbranch_scc1 24b\n\t" \
  "s_mov_b64 exec, -1\n\t" \
  "s_sleep 10\n\t" \
  "s_add_u32 s59, s59, 0x10000\n\t" \
  "s_cmp_lt_u32 s59, 0x400000\n\t" \
  "s_cbranch_scc1 19b\n\t" \
  "s_or_b32 s58, s58, 1\n\t" \
  "s_branch 7f\n\t" \
  "14:\n\t" \
  "v_mov_b32 v184, 0\n\t" \
  "s_bitcmp1_b32 s58, 2\n\t" \
  "s_nop 0\n\t" \
  "s_cbranch_scc1 15b\n\t" \
  "s_mov_b32 s84, 910\n\t" \
  "s_add_u32 s59, s59, 0x10000\n\t" \
  "s_cmp_lt_u32 s59, 0x400000\n\t" \
  "s_mov_b64 exec, vcc\n\t" \
  "buffer_load_dword v187, v200, %[rh], s78 offen sc1\n\t" \
  "s_mov_b32 exec_lo, -1\n\t" \
  "s_cbranch_scc1 13b\n\t" \
  "s_mov_b64 exec, -1\n\t" \
  "s_or_b32 s58, s58, 1\n\t" \
  "s_branch 7f\n\t" \
  "37:\n\t" \
  "s_barrier\n\t" \
  "s_barrier\n\t" \
  "s_add_i32 s40, s40, 2\n\t" \
  "s_and_b32 s58, s58, 4\n\t" \
  "s_branch 17b\n\t" \
  "32:\n\t" \
  "s_and_b32 s80, s58, 36\n\t" \
  "s_cbranch_scc1 33b\n\t" \
  "s_add_i32 s40, s40, -2\n\t" \
  "34:\n\t" \
  "s_or_b32 s59, s59, 128\n\t" \
  "s_and_b32 s76, s40, 3\n\t" \
  "s_lshl_b32 s76, s76, 18\n\t" \
  "s_add_i32 s77, s76, 0x10000\n\t" \
  "s_add_i32 s78, s76, 0x1000\n\t" \
  "s_add_i32 s79, s76, 0x11000\n\t" \
  "35:\n\t" \
  "buffer_load_dwordx4 v[0:3], v210, %[rh], s76 offen offset:0 sc1\n\t" \
  "buffer_load_dwordx4 v[4:7], v210, %[rh], s76 offen offset:1024 sc1\n\t" \
  "buffer_load_dwordx4 v[8:11], v210, %[rh], s77 offen offset:0 sc1\n\t" \
  "buffer_load_dwordx4 v[12:15], v210, %[rh], s77 offen offset:1024 sc1\n\t" \
  "buffer_load_dwordx4 v[16:19], v210, %[rh], s76 offen offset:2048 sc1\n\t" \
  "buffer_load_dwordx4 v[20:23], v210, %[rh], s76 offen offset:3072 sc1\n\t" \
  "buffer_load_dwordx4 v[24:27], v210, %[rh], s77 offen offset:2048 sc1\n\t" \
  "buffer_load_dwordx4 v[28:31], v210, %[rh], s77 offen offset:3072 sc1\n\t" \
  "buffer_load_dwordx4 v[32:35], v210, %[rh], s78 offen offset:0 sc1\n\t" \
  "buffer_load_dwordx4 v[36:39], v210, %[rh], s78 offen offset:1024 sc1\n\t" \
  "buffer_load_dwordx4 v[40:43], v210, %[rh], s79 offen offset:0 sc1\n\t" \
  "buffer_load_dwordx4 v[44:47], v210, %[rh], s79 offen offset:1024 sc1\n\t" \
  "buffer_load_dwordx4 v[48:51], v210, %[rh], s78 offen offset:2048 sc1\n\t" \
  "buffer_load_dwordx4 v[52:55], v210, %[rh], s78 offen offset:3072 sc1\n\t" \
  "buffer_load_dwordx4 v[56:59], v210, %[rh], s79 offen offset:2048 sc1\n\t" \
  "buffer_load_dwordx4 v[60:63], v210, %[rh], s79 offen offset:3072 sc1\n\t" \
  "buffer_load_dwordx4 v[64:67], v211, %[rh], s76 offen offset:0 sc1\n\t" \
  "buffer_load_dwordx4 v[68:71], v211, %[rh], s76 offen offset:1024 sc1\n\t" \
  "buffer_load_dwordx4 v[72:75], v211, %[rh], s77 offen offset:0 sc1\n\t" \
  "buffer_load_dwordx4 v[76:79], v211, %[rh], s77 offen offset:1024 sc1\n\t" \
  "buffer_load_dwordx4 v[80:83], v211, %[rh], s76 offen offset:2048 sc1\n\t" \
  "buffer_load_dwordx4 v[84:87], v211, %[rh], s76 offen offset:3072 sc1\n\t" \
  "buffer_load_dwordx4 v[88:91], v211, %[rh], s77 offen offset:2048 sc1\n\t" \
  "buffer_load_dwordx4 v[92:95], v211, %[rh], s77 offen offset:3072 sc1\n\t" \
  "buffer_load_dwordx4 v[96:99], v211, %[rh], s78 offen offset:0 sc1\n\t" \
  "buffer_load_dwordx4 v[100:103], v211, %[rh], s78 offen offset:1024 sc1\n\t" \
  "buffer_load_dwordx4 v[104:107], v211, %[rh], s79 offen offset:0 sc1\n\t" \
  "buffer_load_dwordx4 v[108:111], v211, %[rh], s79 offen offset:1024 sc1\n\t" \
  "buffer_load_dwordx4 v[112:115], v211, %[rh], s78 offen offset:2048 sc1\n\t" \
  "buffer_load_dwordx4 v[116:119], v211, %[rh], s78 offen offset:3072 sc1\n\t" \
  "buffer_load_dwordx4 v[120:123], v211, %[rh], s79 offen offset:2048 sc1\n\t" \
  "buffer_load_dwordx4 v[124:127], v211, %[rh], s79 offen offset:3072 sc1\n\t" \
  "s_waitcnt vmcnt(0)\n\t" \
  "s_bitcmp1_b32 s58, 2\n\t" \
  "s_cbranch_scc1 36f\n\t" \
  "v_max_u32 v148, v0, v1\n\t" \
  "v_max3_u32 v148, v148, v2, v3\n\t" \
  "v_max3_u32 v148, v148, v4, v5\n\t" \
  "v_max3_u32 v148, v148, v6, v7\n\t" \
  "v_max3_u32 v148, v148, v8, v9\n\t" \
  "v_max3_u32 v148, v148, v10, v11\n\t" \
  "v_max3_u32 v148, v148, v12, v13\n\t" \
  "v_max3_u32 v148, v148, v14, v15\n\t" \
  "v_max3_u32 v148, v148, v16, v17\n\t" \
  "v_max3_u32 v148, v148, v18, v19\n\t" \
  "v_max3_u32 v148, v148, v20, v21\n\t" \
  "v_max3_u32 v148, v148, v22, v23\n\t" \
  "v_max3_u32 v148, v148, v24, v25\n\t" \
  "v_max3_u32 v148, v148, v26, v27\n\t" \
  "v_max3_u32 v148, v148, v28, v29\n\t" \
  "v_max3_u32 v148, v148, v30, v31\n\t" \
  "v_max3_u32 v148, v148, v32, v33\n\t" \
  "v_max3_u32 v148, v148, v34, v35\n\t" \
  "v_max3_u32 v148, v148, v36, v37\n\t" \
  "v_max3_u32 v148, v148, v38, v39\n\t" \
  "v_max3_u32 v148, v148, v40, v41\n\t" \
  "v_max3_u32 v148, v148, v42, v43\n\t" \
  "v_max3_u32 v148, v148, v44, v45\n\t" \
  "v_max3_u32 v148, v148, v46, v47\n\t" \
  "v_max3_u32 v148, v148, v48, v49\n\t" \
  "v_max3_u32 v148, v148, v50, v51\n\t" \
  "v_max3_u32 v148, v148, v52, v53\n\t" \
  "v_max3_u32 v148, v148, v54, v55\n\t" \
  "v_max3_u32 v148, v148, v56, v57\n\t" \
  "v_max3_u32 v148, v148, v58, v59\n\t" \
  "v_max3_u32 v148, v148, v60, v61\n\t" \
  "v_max3_u32 v148, v148, v62, v63\n\t" \
  "v_max3_u32 v148, v148, v64, v65\n\t" \
  "v_max3_u32 v148, v148, v66, v67\n\t" \
  "v_max3_u32 v148, v148, v68, v69\n\t" \
  "v_max3_u32 v148, v148, v70, v71\n\t" \
  "v_max3_u32 v148, v148, v72, v73\n\t" \
  "v_max3_u32 v148, v148, v74, v75\n\t" \
  "v_max3_u32 v148, v148, v76, v77\n\t" \
  "v_max3_u32 v148, v148, v78, v79\n\t" \
  "v_max3_u32 v148, v148, v80, v81\n\t" \
  "v_max3_u32 v148, v148, v82, v83\n\t" \
  "v_max3_u32 v148, v148, v84, v85\n\t" \
  "v_max3_u32 v148, v148, v86, v87\n\t" \
  "v_max3_u32 v148, v148, v88, v89\n\t" \
  "v_max3_u32 v148, v148, v90, v91\n\t" \
  "v_max3_u32 v148, v148, v92, v93\n\t" \
  "v_max3_u32 v148, v148, v94, v95\n\t" \
  "v_max3_u32 v148, v148, v96, v97\n\t" \
  "v_max3_u32 v148, v148, v98, v99\n\t" \
  "v_max3_u32 v148, v148, v100, v101\n\t" \
  "v_max3_u32 v148, v148, v102, v103\n\t" \
  "v_max3_u32 v148, v148, v104, v105\n\t" \
  "v_max3_u32 v148, v148, v106, v107\n\t" \
  "v_max3_u32 v148, v148, v108, v109\n\t" \
  "v_max3_u32 v148, v148, v110, v111\n\t" \
  "v_max3_u32 v148, v148, v112, v113\n\t" \
  "v_max3_u32 v148, v148, v114, v115\n\t" \
  "v_max3_u32 v148, v148, v116, v117\n\t" \
  "v_max3_u32 v148, v148, v118, v119\n\t" \
  "v_max3_u32 v148, v148, v120, v121\n\t" \
  "v_max3_u32 v148, v148, v122, v123\n\t" \
  "v_max3_u32 v148, v148, v124, v125\n\t" \
  "v_max3_u32 v148, v148, v126, v127\n\t" \
  "v_cmp_eq_u32 vcc, -1, v148\n\t" \
  "s_cbranch_vccz 36f\n\t" \
  "s_add_u32 s59, s59, 0x1000000\n\t" \
  "s_cmp_lt_u32 s59, 0x10000000\n\t" \
  "s_cbranch_scc1 35b\n\t" \
  "s_or_b32 s58, s58, 9\n\t" \
  "s_branch 7f\n\t" \
  "36:\n\t" \
  "s_mov_b32 s97, 0\n\t" \
  "s_and_b32 s58, s58, 4\n\t" \
  "s_or_b32 s58, s58, 32\n\t" \
  "s_branch 31b\n\t" \
  "18:\n\t" \
  "v_mov_b32 v186, -1\n\t" \
  "s_and_b32 s80, s40, 3\n\t" \
  "s_mul_i32 s77, s80, 0xc000\n\t" \
  "s_add_i32 s77, s77, 0x40000\n\t" \
  "s_lshl_b32 s78, s80, 14\n\t" \
  "s_add_i32 s78, s78, 0x70000\n\t" \
  "s_mov_b32 s57, 0\n\t" \
  "s_add_i32 s96, s77, 0x1000\n\t" \
  "s_and_b32 s98, s40, 2\n\t" \
  "s_lshl_b32 s98, s98, 13\n\t" \
  "s_and_b32 s80, s40, 1\n\t" \
  "s_lshl_b32 s80, s80, 12\n\t" \
  "s_add_i32 s98, s98, s80\n\t" \
  "s_add_i32 s98, s98, 0x84000\n\t" \
  "12:\n\t" \
  "s_or_b32 s58, s58, 3\n\t" \
  "11:\n\t" \
  "buffer_load_dwordx4 v[0:3], v193, %[rh], s77 offen offset:0 sc1\n\t" \
  "buffer_load_dwordx4 v[4:7], v193, %[rh], s77 offen offset:1024 sc1\n\t" \
  "buffer_load_dwordx4 v[8:11], v193, %[rh], s77 offen offset:2048 sc1\n\t" \
  "buffer_load_dwordx4 v[12:15], v193, %[rh], s77 offen offset:3072 sc1\n\t" \
  "buffer_load_dwordx4 v[16:19], v193, %[rh], s96 offen offset:0 sc1\n\t" \
  "buffer_load_dwordx4 v[20:23], v193, %[rh], s96 offen offset:1024 sc1\n\t" \
  "buffer_load_dwordx4 v[24:27], v193, %[rh], s96 offen offset:2048 sc1\n\t" \
  "buffer_load_dwordx4 v[28:31], v193, %[rh], s96 offen offset:3072 sc1\n\t" \
  "buffer_load_dwordx4 v[32:35], v195, %[rh], s77 offen offset:0 sc1\n\t" \
  "buffer_load_dwordx4 v[36:39], v195, %[rh], s77 offen offset:2048 sc1\n\t" \
  "v_mov_b64 v[40:41], 0\n\t" \
  "v_mov_b64 v[42:43], 0\n\t" \
  "v_mov_b64 v[44:45], 0\n\t" \
  "v_mov_b64 v[46:47], 0\n\t" \
  "v_mov_b64 v[48:49], 0\n\t" \
  "v_mov_b64 v[50:51], 0\n\t" \
  "v_mov_b64 v[52:53], 0\n\t" \
  "v_mov_b64 v[54:55], 0\n\t" \
  "v_mov_b64 v[56:57], 0\n\t" \
  "v_mov_b64 v[58:59], 0\n\t" \
  "v_mov_b64 v[60:61], 0\n\t" \
  "v_mov_b64 v[62:63], 0\n\t" \
  "v_mov_b64 v[64:65], 0\n\t" \
  "v_mov_b64 v[66:67], 0\n\t" \
  "v_mov_b64 v[68:69], 0\n\t" \
  "v_mov_b64 v[70:71], 0\n\t" \
  "v_mov_b64 v[72:73], 0\n\t" \
  "v_mov_b64 v[74:75], 0\n\t" \
  "v_mov_b64 v[76:77], 0\n\t" \
  "v_mov_b64 v[78:79], 0\n\t" \
  "v_mov_b64 v[80:81], 0\n\t" \
  "v_mov_b64 v[82:83], 0\n\t" \
  "v_mov_b64 v[84:85], 0\n\t" \
  "v_mov_b64 v[86:87], 0\n\t" \
  "v_mov_b64 v[88:89], 0\n\t" \
  "v_mov_b64 v[90:91], 0\n\t" \
  "v_mov_b64 v[92:93], 0\n\t" \
  "v_mov_b64 v[94:95], 0\n\t" \
  "v_mov_b64 v[96:97], 0\n\t" \
  "v_mov_b64 v[98:99], 0\n\t" \
  "v_mov_b64 v[100:101], 0\n\t" \
  "v_mov_b64 v[102:103], 0\n\t" \
  "v_mov_b64 v[136:137], 0\n\t" \
  "v_mov_b64 v[138:139], 0\n\t" \
  "v_mov_b64 v[140:141], 0\n\t" \
  "v_mov_b64 v[142:143], 0\n\t" \
  "s_waitcnt vmcnt(0)\n\t" \
  "s_add_u32 s59, s59, 1\n\t" \
  "s_bitcmp1_b32 s58, 2\n\t" \
  "s_cbranch_scc1 10b\n\t" \
  "v_max_u32 v148, v0, v1\n\t" \
  "v_max3_u32 v148, v148, v2, v3\n\t" \
  "v_max3_u32 v148, v148, v4, v5\n\t" \
  "v_max3_u32 v148, v148, v6, v7\n\t" \
  "v_max3_u32 v148, v148, v8, v9\n\t" \
  "v_max3_u32 v148, v148, v10, v11\n\t" \
  "v_max3_u32 v148, v148, v12, v13\n\t" \
  "v_max3_u32 v148, v148, v14, v15\n\t" \
  "v_max3_u32 v148, v148, v16, v17\n\t" \
  "v_max3_u32 v148, v148, v18, v19\n\t" \
  "v_max3_u32 v148, v148, v20, v21\n\t" \
  "v_max3_u32 v148, v148, v22, v23\n\t" \
  "v_max3_u32 v148, v148, v24, v25\n\t" \
  "v_max3_u32 v148, v148, v26, v27\n\t" \
  "v_max3_u32 v148, v148, v28, v29\n\t" \
  "v_max3_u32 v148, v148, v30, v31\n\t" \
  "v_max3_u32 v148, v148, v32, v33\n\t" \
  "v_max3_u32 v148, v148, v34, v35\n\t" \
  "v_max3_u32 v148, v148, v36, v37\n\t" \
  "v_max3_u32 v148, v148, v38, v39\n\t" \
  "v_cmp_eq_u32 vcc, -1, v148\n\t" \
  "s_cbranch_vccz 10b\n\t" \
  "s_bitcmp1_b32 s59, 6\n\t" \
  "s_cbranch_scc0 11b\n\t" \
  "7:\n\t" \
  "92:\n\t" \
  "s_mov_b32 s42, s58\n\t" \
  "s_waitcnt vmcnt(0) lgkmcnt(0)\n\t" \
  ""

#define V53 "v0", "v1", "v2", "v3", "v4", "v5", "v6", "v7", "v8", "v9", "v10", "v11", "v12", "v13", "v14", "v15", "v16", "v17", \
  "v18", "v19", "v20", "v21", "v22", "v23", "v24", "v25", "v26", "v27", "v28", "v29", "v30", "v31", "v32", "v33", "v34", "v35", \
  "v36", "v37", "v38", "v39", "v40", "v41", "v42", "v43", "v44", "v45", "v46", "v47", "v48", "v49", "v50", "v51", "v52"
#define V100 V53, "v53", "v54", "v55", "v56", "v57", "v58", "v59", "v60", "v61", "v62", "v63", "v64", "v65", "v66", "v67", "v68", "v69", "v70", \
  "v71", "v72", "v73", "v74", "v75", "v76", "v77", "v78", "v79", "v80", "v81", "v82", "v83", "v84", "v85", "v86", "v87", "v88", \
  "v89", "v90", "v91", "v116", "v117", "v118", "v119"
#define STEP_PV
#define CB_CL "s60", "s61", "s62", "s63", "s64", "s65", "s66", "s67", "s68", "s69",
#define STEPS(BODY, ...) asm volatile(BODY : "=&{v[92:99]}"(X), "+{s[40:55]}"(P) STEP_PV \
  : "{v[120:151]}"(W.v[0]), "{v[152:183]}"(W.v[1]), "{v[184:215]}"(W.v[2]), "{v[216:247]}"(W.v[3]), "{v[100:115]}"(C), [rh] "s"(rh), [wv] "s"(w), [ru] "s"(rum), [sel] "s"(sel) __VA_ARGS__ \
  : "memory", "vcc", "scc", "s56", "s57", "s58", "s59", "s76", "s77", "s78", "s79", "s80", "s81", "s82", "s83", "s84", "s85", "s86", "s87", "s88", "s89", \
    "s90", "s91", "s92", "s93", "s94", "s95", "s96", "s97", V100)
__device__ __forceinline__ void keep_a(const Wts& W) {
#pragma unroll
  for (int i = 0; i < 32; ++i) asm volatile("" :: "a"(W.a[i]));
}
__device__ __forceinline__ char* lds_base(char* smem, int off) { asm volatile("" : "+v"(off)); return smem + off; }
__device__ __forceinline__ u32x2 tr4(unsigned p) { return __builtin_bit_cast(u32x2, __builtin_amdgcn_ds_read_tr16_b64_v4i16((LAS bf16x4*)(uintptr_t)p)); }
__device__ __forceinline__ float rb1(float x) { return __uint_as_float(pk2(0.f, x)); }

__device__ __forceinline__ void steps_init(u32x16& P, int h, int w, unsigned place) {
#pragma unroll
  for (int i = 0; i < 16; ++i) P[i] = 0;
  P[2] = 1; P[3] = CDUR; P[14] = H_MEM + (w >> 1) * CHK + (w & 1) * 4096 + (h & RHO) * 1024; P[15] = place;
}
__device__ __forceinline__ void steps_enter(u32x16& P, Clk& ck, bool dead) {
  ck.sync();
  P[2] = (P[2] & 3u) | (dead ? 4u : 0u); P[6] = ck.c2t << 15; P[11] = ck.ce; P[12] = ck.t2c; P[13] = ck.te;
#pragma unroll
  for (int i = 0; i < 16; ++i) P[i] = __builtin_amdgcn_readfirstlane(P[i]);
}

__device__ __forceinline__ void role_qk(const ChainArgs& a, int h, char* smem, const int w, Gate& G) {
  int lane = opaque_tid(0);
  const int b = h; (void)b;
  u64* prof = a.prof; (void)prof;
  PDECL(L_PRF, 4);
  const float CS = 0.08838834764831845f * 1.4426950408889634f;
  GAS unsigned* abortw = (GAS unsigned*)(a.sync + SY_ABORT);
  bool dead = false;
  Wts W;
  Clk ck; ck.init();
  u32x4 gv;
  if (QK_D) while (__builtin_amdgcn_s_memrealtime() - G.entry < QK_D) __builtin_amdgcn_s_sleep(8);
  load_weights<0>(a, h, w, lane, smem, W, G, gv);
  TP(1);
  if (!G.look(a.sync, gv)) return;
  PZERO;
  const Sel sel = G.sel;
  const __amdgpu_buffer_rsrc_t rh = rsrc_hot(a.sync, sel), rum = rsrc_u(a.sync);
  auto pm_o = [&](int l) { return 2 * CHK + h * 256 + (l & 1) * 128 + (l >> 3) * 16 + (l & 6) * 2; };
  auto fac = [&](int l, int hf) { return ((l & 7) >> 1) == 2 * (l >> 5) + ((h >> 1) & 1) && ((l >> 3) & 1) == hf ? 1.f : 0.f; };
  u32x16 C, P;
  {
    const int r = lane & 15, g = lane >> 4;
    const unsigned l0 = (unsigned)(uintptr_t)(LAS char*)smem;
    C[0] = lane * 16; C[1] = l0 + L_MEM + lane * 16; C[2] = l0 + L_WTS + w * 4096 + lane * 16; C[3] = C[2] + 65536;
    C[4] = (lane & 2) ? 0xEEEE : 0x4444;
    C[5] = l0 + L_XCH + (16 * w + 2 * (r >> 2) + (r & 1)) * 32 + g * 8 + ((r >> 1) & 1) * 4;
    C[6] = l0 + L_XK + (32 * w + r) * 16 + g * 4;
    C[7] = w == 3 ? H_PX + (h >> 3) * CHK + (h & 7) * 1024 + lane * 16 : w ? (2 * h + 1) * CHK + (w - 1) * 4096 + lane * 16 : l0 + L_XCH + (8 * g + (r >> 2)) * 32 + (r & 3) * 8;
    C[8] = l0 + L_XK + (8 * g + (r >> 2)) * 16 + (r & 1) * 8;
    C[9] = w ? (lane & 1) * 4096 : (lane == 0 ? H_ANN + h * 128 : 0x7fffffffu);
    C[10] = w == 0 ? (h >> 4) * CHK + (h & 15) * 512 + ((lane >> 1) & 1) * 256 + (lane >> 5) * 128 + (lane & 1) * 64 + ((lane >> 3) & 3) * 16 + ((lane >> 2) & 1) * 8 :
            w < 3 ? H_PX + ((w - 1) * 4 + (h >> 3)) * CHK + (h & 7) * 1024 + lane * 16 :
            lane < 32 ? (h >> 4) * CHK + (h & 15) * 512 + lane * 16 : (lane < 48 ? 2 * CHK + h * 256 + (lane - 32) * 16 : 0x7fffffffu);
    C[11] = w == 0 ? pm_o(lane) : (lane < (w == 3 ? 32 : 16) ? H_PX + (w == 3 ? 8 + g : 7 + w) * CHK + hpos(h) * 256 + r * 16 : 0x7fffffffu);
    C[12] = H_TR + (8 * w + (lane & 7)) * 128; C[13] = l0 + L_PRF + w * 1024 + (lane >> 6);
    unsigned mk = 0;
#pragma unroll
    for (int i = 0; i < 8; ++i) mk |= (16 * ((i >> 1) & 1) + 8 * (g & 1) + 4 * (g >> 1) + 2 * (i & 1) + (i >> 2) > 16 * (w - 1) + r ? 1u : 0u) << i;
    C[14] = w ? mk : __float_as_uint(fac(lane, 0)); C[15] = __float_as_uint(fac(lane, 1));
    asm volatile("" : "+v"(C));
  }
  steps_init(P, h, w, 2 * h * CHK + max(3 * w - 4, 0) * 1024);
  u32x4 k0[2];
  start_wait(a.sync, abortw, lane, dead, CT_Q + ct_near(sel) + h, 1u, [&]() { for (int i = 0; i < 2; ++i) k0[i] = ld16(rum, x_slot(1, 0, h, sel) + (2 * w + i) * 1024 + lane * 16); });
  G.arrive(a.sync);
#pragma unroll
  for (int i = 0; i < 2; ++i) *(u32x4*)(smem + L_KX + (2 * w + i) * 1024 + lane * 16) = k0[i];
  SSTAMP(96 + h);
  TP(2);
  GAS unsigned* kcp = ctr(a, CT_Q + ct_near(sel) + h);
  unsigned spins = 0;
  while (true) {
    u32x8 X; u32x16 pv; (void)pv;
    steps_enter(P, ck, dead);
    STEPS(QK_BODY, , [cs] "s"(CS), [kc] "s"(kcp));
    keep_a(W);
    const unsigned res = __builtin_amdgcn_readfirstlane(P[2]), s = __builtin_amdgcn_readfirstlane(P[4]);
    lane = opaque_tid(0);
    if (res & 8u) {
      auto sw = [](float v, int k) { return __int_as_float(k == 0 ? __builtin_amdgcn_mov_dpp(__float_as_int(v), 0xB1, 0xf, 0xf, false) :
                                                           k == 1 ? __builtin_amdgcn_mov_dpp(__float_as_int(v), 0x4E, 0xf, 0xf, false) :
                                                                    __builtin_amdgcn_mov_dpp(__float_as_int(v), 0x141, 0xf, 0xf, false)); };
      float x[5], d = fmaxf(__uint_as_float(X[4]), 0.f);
      d = fmaxf(d, sw(d, 0)); d = fmaxf(d, sw(d, 1)); d = fmaxf(d, sw(d, 2));
#pragma unroll
      for (int i = 0; i < 5; ++i) x[i] = __builtin_amdgcn_exp2f(__uint_as_float(X[i]) - d);
      float z = ((x[0] + x[1]) + (x[2] + x[3])) + x[4];
      z += sw(z, 0); z += sw(z, 1); z += sw(z, 2);
      z = __builtin_amdgcn_rcpf(z);
      const float y = sw(x[4], 0), f = __builtin_amdgcn_exp2f(-d) * z;
      const unsigned pm = (lane & 1) ? pk2(fac(lane, 0) * f, fac(lane, 1) * f) : pk2(x[4] * z, y * z);
      if (!(__uint_as_float(X[5]) < 1.0e18f))
        __builtin_amdgcn_raw_buffer_store_b32(nosent(pm), rh, pm_o(lane), H_P + ((s - 1) & 3) * 3 * CHK, ST_AUX);
    }
    if (res & 64u) break;
    if (res & 32u) {
      u32x4 k[3];
      start_wait(a.sync, abortw, lane, dead, CT_Q + ct_near(sel) + h, s + 1, [&]() { for (int i = 0; i < 3; ++i) k[i] = ld16(rum, x_slot(1, s, h, sel) + max(3 * w - 4 + i, 0) * 1024 + lane * 16); });
#pragma unroll
      for (int i = 0; i < 3; ++i) *(u32x4*)(smem + L_KX + max(3 * w - 4 + i, 0) * 1024 + lane * 16) = k[i];
    }
    if (res & 17u) {
      spins |= 63u;
      if (dead || poll_fail(spins, abortw, lane)) dead = true;
    }
  }
  PDUMP(0);
}

__device__ __forceinline__ void role_vo(const ChainArgs& a, int h, char* smem, const int w, Gate& G) {
  int lane = opaque_tid(0);
  const int b = 32 + h; (void)b;
  u64* prof = a.prof; (void)prof;
  PDECL(L_PRF, 4);
  GAS unsigned* abortw = (GAS unsigned*)(a.sync + SY_ABORT);
  bool dead = false;
  Wts W;
  Clk ck; ck.init();
  u32x4 gv;
  load_weights<1>(a, h, w, lane, smem, W, G, gv);
  if (!G.look(a.sync, gv)) return;
  PZERO;
  const Sel sel = G.sel;
  const __amdgpu_buffer_rsrc_t rh = rsrc_hot(a.sync, sel), rum = rsrc_u(a.sync);
  u32x16 C, P;
  {
    const int r = lane & 15, g = lane >> 4;
    const unsigned l0 = (unsigned)(uintptr_t)(LAS char*)smem;
#pragma unroll
    for (int i = 0; i < 16; ++i) C[i] = H_TR + (8 * w + (lane & 7)) * 128;
    C[0] = lane * 16; C[1] = l0 + L_MEM + lane * 16; C[2] = l0 + L_WTS + w * 4096 + lane * 16; C[3] = C[2] + 65536;
    C[4] = (lane & 2) ? 0xEEEE : 0x4444;
    C[5] = l0 + L_XCH + (32 * w + r) * 16 + g * 4;
    C[6] = (lane & 8) ? 0xEEEE : 0x4444;
    C[7] = l0 + L_XCH + (32 * (g >> 1) + 8 * (g & 1) + (r >> 2)) * 16 + (r & 1) * 8;
    C[8] = ((g & 1) * 8 + (g >> 1)) * CHK + r * 16;
    C[13] = l0 + L_PRF + w * 1024 + (lane >> 6);
    asm volatile("" : "+v"(C));
  }
  start_wait(a.sync, abortw, lane, dead, -1, 0u, [] {});
  G.arrive(a.sync);
  SSTAMP(128 + h);
  steps_init(P, h, w, SY_UM + (16 * (h & 3) + 2 * w) * CHK + hpos(h) * 256);
  unsigned spins = 0;
  while (true) {
    u32x8 X; u32x16 pv; (void)pv;
    steps_enter(P, ck, dead);
    STEPS(VO_BODY);
    keep_a(W);
    lane = opaque_tid(0);
    if (__builtin_amdgcn_readfirstlane(P[2]) & 64u) break;
    spins |= 63u;
    if (dead || poll_fail(spins, abortw, lane)) dead = true;
  }
  PDUMP(0);
}

__device__ __forceinline__ void calibrate(const ChainArgs& a, int cal, int slot, u64 entry) {
  const u32x4 rs = own_rsrc(a.sync, 64 * PAGE);
  const unsigned so = cal * PAGE + (slot >> 1) * CHK + (slot & 1) * 4096;
  unsigned best[2] = {1u << 30, 1u << 30};
  bool sure;
  {
    unsigned z = 0;
    asm volatile("s_mov_b64 exec, 0xffff\n\tbuffer_atomic_or %0, %1, %2, %3 offen sc0 sc1\n\ts_mov_b64 exec, -1\n\ts_waitcnt vmcnt(0)"
                 : "+v"(z) : "v"((opaque_tid(0) & 8) * 1024 + (opaque_tid(0) & 7) * 256), "s"(rs), "s"(so) : "memory");
  }
  for (int i = 0;; ++i) {
    sure = min(best[0], best[1]) < CALT && max(best[0], best[1]) - min(best[0], best[1]) > 110u;
    if (i >= CALN && (sure || __builtin_amdgcn_s_memrealtime() - entry > CALM)) break;
#pragma unroll
    for (int hf = 0; hf < 2; ++hf) {
      u64 c0, c1;
      unsigned z = 0;
      asm volatile("s_mov_b64 exec, 1\n\ts_memtime %0\n\ts_waitcnt lgkmcnt(0)\n\tbuffer_atomic_or %2, off, %3, %4 sc0 sc1\n\ts_waitcnt vmcnt(0)\n\ts_memtime %1\n\ts_mov_b64 exec, -1\n\ts_waitcnt lgkmcnt(0)"
                   : "=&s"(c0), "=&s"(c1), "+v"(z) : "s"(rs), "s"(so + hf * 8192 + (i & 7) * 256) : "memory");
      best[hf] = min(best[hf], (unsigned)(c1 - c0));
    }
  }
  const u64 hs = (__builtin_amdgcn_s_memrealtime() & ~3ull) | (sure ? 2u : 0u) | ((best[1] < best[0] ? 1u : 0u) ^ die_half());
  if (opaque_tid(0) == 0) __hip_atomic_store((GAS u64*)ctr(a, CT_CAL + (slot >> 1)) + (slot & 1), hs, __ATOMIC_RELAXED, __HIP_MEMORY_SCOPE_AGENT);
}
__device__ __forceinline__ u32x4 pk8(f32x4 a, f32x4 b) { return (u32x4){pk2(a[0], a[1]), pk2(a[2], a[3]), pk2(b[0], b[1]), pk2(b[2], b[3])}; }
__device__ __forceinline__ unsigned pk2v(float a, float b) { unsigned r; asm volatile("v_cvt_pk_bf16_f32 %0, %1, %2" : "=v"(r) : "v"(a), "v"(b)); return r; }
__device__ __forceinline__ u32x4 pk8v(f32x4 a, f32x4 b) { asm volatile("s_nop 7"); return (u32x4){pk2v(a[0], a[1]), pk2v(a[2], a[3]), pk2v(b[0], b[1]), pk2v(b[2], b[3])}; }
__device__ __forceinline__ void st16g(u16* p, u32x4 v) { asm volatile("global_store_dwordx4 %0, %1, off " OUTP "\n\ts_nop 1" :: "v"(p), "v"(v) : "memory"); }
__device__ __forceinline__ void role_cb(const ChainArgs& a, int cbi, char* smem, const int w, Gate& G) {
  int lane = opaque_tid(0);
  const int b = 64 + cbi; (void)b;
  const int par = cbi >> 5, ci = cbi & 31, J = ci >> 3;
  u64* prof = a.prof; (void)prof;
  PDECL(65536, 8); PZERO;
  GAS unsigned* abortw = (GAS unsigned*)(a.sync + SY_ABORT);
  bool dead = false;
  const int tw = 2 * ci + (w & 1);
  const unsigned mo = (tw >> 5) * CHK + (tw & 31) * 256;
  Sel sel = 0;
  auto start = [&]() {
    lane = opaque_tid(0);
    SSTAMP(0 + ci);
    unsigned m0 = 0;
    if (par == 0 && w < 2) {
      const u16* m = a.mem0 + (2 * (lane >> 4) + ((lane >> 2) & 1)) * DM + 16 * tw + (lane & 8);
      m0 = m[lane & 3] | (unsigned)m[7 - (lane & 3)] << 16;
    }
    if (!G.look(a.sync, (u32x4){0u, 0u, 0u, 0u})) return false;
    sel = G.sel;
    SSTAMP(64 + ci);
    const __amdgpu_buffer_rsrc_t rh = rsrc_hot(a.sync, sel), rum = rsrc_u(a.sync);
    const unsigned s1v = ~(unsigned)(lane >> 6);
    const u32x4 sv = {s1v, s1v, s1v, s1v};
    auto um = [&](int k) {
#pragma unroll
      for (int i = 0; i < 4; ++i) st16(rum, near_half(SY_UM + (64 * k + tw) * CHK, sel) + (w >> 1) * 4096 + i * 1024 + lane * 16, sv);
    };
    um(par);
    const unsigned dv = cbi == H_DLT / CHK ? K_DEF * TU : s1v;
#pragma unroll
    for (int i = 0; i < 2; ++i) st16(rh, cbi >= 2 && cbi < 45 ? (cbi < 35 ? cbi * CHK : H_PX + (cbi - 35) * CHK) + w * 2048 + i * 1024 + lane * 16 : 0x7fffffffu, (u32x4){dv, s1v, s1v, s1v});
    if (par == 0 && w < 2) st4(rh, H_MEM + mo + lane * 4, nosent(m0));
    asm volatile("s_waitcnt vmcnt(0)" ::: "memory");
    lds_barrier();
    if (w == 0 && lane == 0) ctr_add(a, CT_INIT + J, 1u);
    SSTAMP(256 + ci);
    TP(6);
    G.arrive(a.sync);
    um(par + 2);
    {
      const __amdgpu_buffer_rsrc_t rx = mk_rsrc((void*)a.X, 4u << 20);
      u32x4 v[16];
#pragma unroll
      for (int i = 0; i < 16; ++i) v[i] = __builtin_amdgcn_raw_buffer_load_b128(rx, (unsigned)(cbi * 4096 + w * 1024 + lane * 16), (unsigned)(i * 262144), 0);
#pragma unroll
      for (int i = 0; i < 16; ++i) asm volatile("" :: "v"(v[i]));
    }
    SSTAMP(224 + ci);
    start_wait(a.sync, abortw, lane, dead, -1, 0u, [] {});
    return true;
  };
  if (!start()) return;
  Wts W;
#pragma unroll
  for (int i = 0; i < 32; ++i) asm volatile("" : "=a"(W.a[i]));
  u32x16 C, P; u32x8 C2;
  {
    const unsigned l0 = (unsigned)(uintptr_t)(LAS char*)smem, oor = 0x7fffffffu;
    const int r = lane & 7, t = (lane >> 3) & 1, g = lane >> 4, e = w & 1;
    const bool L = w < 2;
    C[0] = lane * 16;
    C[1] = L ? l0 + 32768 + e * 8192 + (lane & 2) * 512 + ((lane & 1) * 32 + (lane >> 2)) * 16 : e * CHK + t * 512 + (g & 1) * 256 + (r >> 2) * 128 + (g >> 1) * 64 + (r & 3) * 16;
    C[2] = L ? (4 * t + g) * 1024 + r * 16 : l0 + 81920 + e * 32768 + lane * 16;
    C[3] = L ? l0 + e * 1024 + lane * 8 : 2 * CHK + e * 4096 + (4 * (g >> 1) + 2 * t + (g & 1)) * 256 + 128 + r * 16;
    C[4] = ((16 * e + (lane >> 2)) * OUTW + DM + 32 * ci + 8 * (lane & 3)) * 2;
    C[5] = l0 + (L ? e * 8192 + lane * 16 : e * 512 + lane * 8); C[6] = l0 + 16384 + lane * 16; C[7] = l0 + 32768 + (L ? e * 8192 + 4096 : e * 2048) + lane * 16;
    C[8] = (lane & 31) * 128;
    C[9] = L ? (8 * (g & 1) + 4 * (g >> 1) + 2 * t + (r >> 2)) * 16 + 4 * (r < 4 ? r : 7 - r) : (8 * J + r) * 128;
    C[10] = (lane | e) ? oor : ci * 128;
    C[11] = L ? SY_CT + (CT_U + ct_near(sel) + 16 * e + (lane & 15)) * 128 : (t ? 0xEEEE : 0x4444); C[12] = L ? l0 + 81920 + e * 32768 + lane * 16 : ((lane & 2) ? 0xEEEE : 0x4444); C[13] = r < 4 ? 0x01000504u : 0x07060302u;
    C[14] = l0 + 49152 + 32 * e + (lane >> 6); C[15] = l0 + 49152 + (lane >> 6);
    C2[0] = L ? (lane < 16 ? lane * 16 : oor) : ux_tile(2 * ci) + lane * 16;
    C2[1] = lane == 0 && e && ci < 8 ? SY_CT + (CT_DONE + ct_near(~sel) + 8 * par + ci) * 128 : oor;
    C2[2] = (L ? ux_tile(2 * ci) : H_PX + 2 * e * CHK) + lane * 16;
#pragma unroll
    for (int n = 0; n < 4; ++n) C2[3 + n] = L ? H_PX + (8 + e) * CHK + (J ^ n) * 2048 + lane * 16 : C2[2] + CHK;
    C2[7] = l0 + 65536 + w * 2048 - 64 + (lane >> 6);
    asm volatile("" : "+v"(C), "+v"(C2));
#pragma unroll
    for (int i = 0; i < 16; ++i) P[i] = par - 2;
    P[2] = 48; P[6] = P[7] = K_DEF * TU; P[8] = SY_UM + tw * CHK + J * 2048; P[9] = H_MEM + mo; P[10] = 2 * CHK + J * 256; P[11] = e * 16 * UXH; P[13] = 1780; P[12] = H_DLT + par * 4096;
  }
  Clk ck; ck.init();
  unsigned spins = 0;
  while (true) {
    u32x16 pv; (void)pv;
    unsigned res = __builtin_amdgcn_readfirstlane(P[2]);
    if (res & 64u) break;
    const u32x4 rh2 = own_rsrc(a.sync + near_half(SY_HOT, sel), H_BYTES - 8192), ra = own_rsrc(a.sync, SY_BYTES), ro = own_rsrc(a.out, S_LEN * OUTW * 2);
    if (res & 1u) {
      spins |= 63u;
      if (dead || poll_fail(spins, abortw, opaque_tid(0))) dead = true;
    }
    ck.sync();
    P[2] = (res & 58u) | (dead ? 4u : 0u); P[1] = ck.ce; P[3] = ck.te; P[4] = ck.t2c; P[5] = ck.c2t << 15;
#pragma unroll
    for (int i = 0; i < 16; ++i) P[i] = __builtin_amdgcn_readfirstlane(P[i]);
    asm volatile(CB_BODY : "+{s[40:55]}"(P) STEP_PV
                 : "{v[192:207]}"(C), "{v[208:215]}"(C2), [rh] "s"(rh2), [ru] "s"(ra), [ro] "s"(ro), [wv] "s"(w), [sel] "s"(sel)
                 : "memory", "vcc", "scc", CB_CL "s56", "s57", "s58", "s59", "s76", "s77", "s78", "s79", "s80", "s81", "s82", "s83", "s84", "s85", "s86", "s87", "s88", "s89", "s90", "s91", "s92", "s93", "s94", "s95", "s96", "s97", "s98", "s99", "s100", "s101", V53, "v53", "v54", "v55", "v56", "v57", "v58", "v59", "v60", "v61", "v62", "v63", "v64", "v65", "v66", "v67", "v68", "v69", "v70", "v71", "v72", "v73", "v74", "v75", "v76", "v77", "v78", "v79", "v80", "v81", "v82", "v83", "v84", "v85", "v86", "v87", "v88", "v89", "v90", "v91", "v92", "v93", "v94", "v95", "v96", "v97", "v98", "v99", "v100", "v101", "v102", "v103", "v104", "v105", "v106", "v107", "v108", "v109", "v110", "v111", "v112", "v113", "v114", "v115", "v116", "v117", "v118", "v119", "v120", "v121", "v122", "v123", "v124", "v125", "v126", "v127", "v128", "v129", "v130", "v131", "v132", "v133", "v134", "v135", "v136", "v137", "v138", "v139", "v140", "v141", "v142", "v143", "v144", "v145", "v146", "v147", "v148", "v149", "v150", "v151", "v152", "v153", "v154", "v155", "v156", "v157", "v158", "v159", "v160", "v161", "v162", "v163", "v164", "v165", "v166", "v167", "v168", "v169", "v170", "v171", "v172", "v173", "v174", "v175", "v176", "v177", "v178", "v179", "v180", "v181", "v182", "v183", "v184", "v185", "v186", "v187", "v188", "v189", "v190", "v191");
    keep_a(W);
  }
  PDUMP(2);
}

__device__ __forceinline__ void bg_drain() {
  asm volatile("s_waitcnt vmcnt(0)" ::: "memory");
  __builtin_amdgcn_s_barrier();
  asm volatile("" ::: "memory");
}
#define BG_SLP 16
#define A3_SLP 48
__device__ __forceinline__ float blo(unsigned d) { return __uint_as_float(d << 16); }
__device__ __forceinline__ float bhi(unsigned d) { return __uint_as_float(d & 0xffff0000u); }
struct A1 {
  u32x4 kk[11], vv[8], sh[4];
  __device__ __forceinline__ void load(const ChainArgs& a, int x, int n) {
    const int lane = opaque_tid(0);
    const int d = (4 * x + (n & 3)) * 64 + 8 * (lane & 7), tb = (n >> 2) * 64 + 8 * (lane >> 3);
    const __amdgpu_buffer_rsrc_t rk = mk_rsrc(a.Ak, 4u << 20), rv = mk_rsrc(a.Av, 4u << 20), rs = mk_rsrc(a.Ashift, 8192);
#pragma unroll
    for (int i = 0; i < 4; ++i) sh[i] = __builtin_amdgcn_raw_buffer_load_b128(rs, d * 8 + 16 * i, 0, 0);
#pragma unroll
    for (int i = 0; i < 11; ++i) kk[i] = __builtin_amdgcn_raw_buffer_load_b128(rk, tb - 3 + i < 0 ? 0x7fffffffu : ((tb - 3 + i) * D_A + d) * 2, 0, 2);
#pragma unroll
    for (int i = 0; i < 8; ++i) vv[i] = __builtin_amdgcn_raw_buffer_load_b128(rv, ((tb + i) * D_A + d) * 2, 0, 2);
  }
  __device__ __forceinline__ void finish(const ChainArgs& a, int x, int n, char* T, Sel sel) {
    const int lane = opaque_tid(0);
#pragma unroll
    for (int c = 0; c < 8; ++c) {
      const unsigned t01 = sh[c >> 1][2 * (c & 1)], t23 = sh[c >> 1][2 * (c & 1) + 1];
      const float s0 = blo(t01), s1 = bhi(t01), s2 = blo(t23), s3 = bhi(t23);
      float kf[11];
#pragma unroll
      for (int i = 0; i < 11; ++i) kf[i] = (c & 1) ? bhi(kk[i][c >> 1]) : blo(kk[i][c >> 1]);
      u32x4 o;
#pragma unroll
      for (int i = 0; i < 8; i += 2) {
        float r[2];
#pragma unroll
        for (int j = 0; j < 2; ++j)
          r[j] = fmaf(kf[i + j + 3], s0, fmaf(kf[i + j + 2], s1, fmaf(kf[i + j + 1], s2, kf[i + j] * s3))) * ((c & 1) ? bhi(vv[i + j][c >> 1]) : blo(vv[i + j][c >> 1]));
        o[i >> 1] = pk2(r[0], r[1]);
      }
      *(u32x4*)(T + (8 * (lane & 7) + c) * 128 + (((lane >> 3) ^ (lane & 7)) * 16)) = o;
    }
    const __amdgpu_buffer_rsrc_t rkv = rsrc_u(a.sync);
#pragma unroll
    for (int i = 0; i < 8; ++i)
      st16(rkv, kv_row((4 * x + (n & 3)) * 64, sel) + (4 * i + (lane >> 4)) * CHK + ((lane >> 3) & 1) * 4096 + (n >> 2) * 128 + (lane & 7) * 16,
           *(const u32x4*)(T + (8 * i + (lane >> 3)) * 128 + (((lane & 7) ^ i) * 16)));
  }
};
#define WAH 4
#define DSP 4
#define SIGQ 2
__device__ __forceinline__ void task_w(const ChainArgs& a, int kind, int h, char* smem, int wv, Gate& G) {
  const int lane = opaque_tid(0), w = wv, r = lane & 15, g = lane >> 4;
  const __amdgpu_buffer_rsrc_t rw = mk_rsrc(kind == 0 ? a.Wq : (kind == 1 ? a.Wk : a.Wv), 8u << 20), rx = mk_rsrc(a.X, 4u << 20);
  const unsigned lds0 = (unsigned)(uintptr_t)(LAS char*)smem + w * 4096;
  unsigned vx[2], vw[4];
#pragma unroll
  for (int j = 0; j < 2; ++j) {
    const int rr = 8 * j + (lane >> 3);
    const int tb = kind == 0 ? rr : (kind == 1 ? 16 * ((rr >> 1) & 1) + 8 * ((rr >> 2) & 1) + 4 * (rr >> 3) + 2 * (rr & 1) : 8 * (rr >> 2) + (rr & 3));
    vx[j] = tb * DM * 2 + ((lane & 7) ^ (rr >> 1)) * 16;
  }
#pragma unroll
  for (int j = 0; j < 4; ++j) { const int row = (w * 4 + j) * 4 + g; vw[j] = row * HD * 2 + (r ^ swz_b(row)) * 16; }
  const u64 wo = kind == 2 ? bg_order(2) : (kind ? bg_order(1) : bg_order(0));
  auto xo = [&](int q) { return ((int)((wo >> (16 * q + 4 * w)) & 15) ^ (h & RHO)) * 128; };
  unsigned dT = (kind == 0 ? 16 : (kind == 1 ? 1 : 4)) * DM * 2;
  asm volatile("s_nop 4" : "+s"(dT));
  const unsigned vx0 = ((w & 1) ? vx[1] : vx[0]) + (w >> 1) * dT;
  auto st_w = [&](int n) {
#pragma unroll
    for (int j = 0; j < 4; ++j) dma16(rw, vw[j], (64 * ((int)((wo >> (4 * n)) & 15) ^ (h & RHO)) * HD + 128 * h) * 2, lds0 + (n & 7) * 16384 + j * 1024, W_NT & 16);
    dma16(rx, vx0, ((int)((wo >> (4 * n)) & 15) ^ (h & RHO)) * 128, lds0 - 3072 * w + 131072 + (n & 7) * 4096);
  };
  auto st_x = [&](int s, int b, int q, int j, bool pad) { dma16(rx, vx[j & 1], s * 65536 + (j >> 1) * dT + xo(q), lds0 + (b * 4 + q) * 16384 + j * 1024, false, pad); };
  if (kind < 2) {
    const __amdgpu_buffer_rsrc_t rs = mk_rsrc(a.Wv, 8u << 20);
#pragma unroll
    for (int n = 0; n < 4 * V_PF; ++n) dma16(rs, vw[n & 3], (64 * ((int)((bg_order(2) >> (4 * (V_PF0 + 2 * (n >> 2) + kind))) & 15) ^ (h & RHO)) * HD + 128 * h) * 2, lds0);
  }
  if (KQ_D && kind < 2) while (__builtin_amdgcn_s_memrealtime() - G.entry < KQ_D) __builtin_amdgcn_s_sleep(8);
  __builtin_amdgcn_s_waitcnt(0x0F70);
  u32x4 W[32][2], gv;
  char* xb[3][2];
#pragma unroll
  for (int i = 0; i < 6; ++i) xb[i >> 1][i & 1] = lds_base(smem, (i >> 1) * 65536 + (r >> 3) * 1024 + (r & 7) * 128 + ((4 * (i & 1) + g) ^ (r >> 1)) * 16);
  f32x4 acc[2][4];
#pragma unroll
  for (int i = 0; i < 4; ++i) acc[1][i] = vzero();
#pragma unroll
  for (int n = 0; n < WAH; ++n) st_w(n);
#pragma unroll
  for (int n = 0; n < 16; ++n) {
    asm volatile("s_waitcnt vmcnt(%0)" :: "n"(4 * WAH - 4 + (n < 16 - WAH ? WAH - 1 : 15 - n)) : "memory");
    lds_barrier();
    u32x4 f[4], y[4];
#pragma unroll
    for (int i = 0; i < 4; ++i) {
#pragma unroll
      for (int q = 0; q < 2; ++q) {
        const int row = (i & 1) * 32 + 8 * g + 4 * q + (r >> 2);
        const u32x2 u = __builtin_bit_cast(u32x2, __builtin_amdgcn_ds_read_tr16_b64_v4i16((LAS bf16x4*)(smem + (n & 7) * 16384 + row * 256 + (((4 * w + (r & 3)) ^ swz_b(row)) * 16) + 8 * (i >> 1))));
        f[i][2 * q] = u[0]; f[i][2 * q + 1] = u[1];
      }
      y[i] = *(const u32x4*)(xb[2][i & 1] + (n & 7) * 4096 + (i >> 1) * 2048);
    }
    SB0;
    if (n == 16 - WAH) G.ask(a.sync, gv);
    if (n == 15) asm volatile("" : "+v"(gv));
    if (n + WAH < 16) st_w(n + WAH);
    else {
#pragma unroll
      for (int j = 0; j < 4; ++j) st_x(1 + ((n + WAH - 16) >> 2), (n + WAH - 16) >> 2, (n + WAH) & 3, j, true);
    }
#pragma unroll
    for (int i = 0; i < 8; ++i)
      asm volatile("s_nop 1\n\tv_mfma_f32_16x16x32_bf16 %0, %1, %2, %0" : "+v"(acc[1][i & 3]) : "v"(f[2 * (i & 1) + (i >> 2)]), "v"(y[(i & 2) + (i >> 2)]));
#pragma unroll
    for (int i = 0; i < 4; ++i) { asm volatile("" : "+a"(f[i])); W[2 * n + (i & 1)][i >> 1] = f[i]; }
  }
  if (!G.look(a.sync, gv)) { bg_drain(); return; }
  TP(4);
  SSTAMP(160 + h);
  const Sel sel = G.sel;
  const __amdgpu_buffer_rsrc_t ru = mk_rsrc(a.sync, SY_BYTES);
  const unsigned sgo = (w | lane) ? 0x7fffffffu : SY_CT + (kind == 2 ? CT_V + ct_near(~sel) : CT_Q + 32 * kind + ct_near(sel)) * 128 + h * 128;
#pragma unroll
  for (int T = 0; T < 2; ++T) st16s(pk8v(acc[1][2 * T], acc[1][2 * T + 1]), ru, lane * 16 + T * 4096, x_slot(kind, 0, h, sel) + w * 1024);
  SB0;
  const u32x4 rx2 = own_rsrc(a.X, 4u << 20);
  unsigned vq[4][4];
#pragma unroll
  for (int i = 0; i < 16; ++i) { vq[i >> 2][i & 3] = vx[i & 1] + ((i >> 1) & 1) * dT + xo(i >> 2); asm volatile("" : "+v"(vq[i >> 2][i & 3])); }
  u32x4 xf[4][2], o;
  auto rd = [&](int b, int k, int T) { xf[k & 3][T] = *(const u32x4*)(xb[b][k & 1] + (k >> 1) * 4096 + T * 2048); };
  auto signal = [&](int s) {
    st4(ru, sgo, s);
    if (w == 0 && lane == 0 && s >= 0 && (s < PSEG || (s & 7) == 0 || ((s & 15) == 2 && PSEG > 2) || s > 61)) {
    }
  };
  if (kind < KQ_NOW) { bg_drain(); signal(0); }
  G.arrive(a.sync);
#pragma unroll
  for (int i = 4 * WAH; i < 28; ++i) st_x(1 + (i >> 4), i >> 4, (i >> 2) & 3, i & 3, true);
  wait_vm<26>();
  lds_barrier();
#pragma unroll
  for (int i = 0; i < 6; ++i) rd(0, i >> 1, i & 1);
  WST(kind == 2, 18);
  auto seg = [&](int s, int b) {
    const unsigned so = x_slot(kind, s - 1, h, sel) + w * 1024;
    WST(kind == 2 && s > 0 && s < 4, 18 + s);
#pragma unroll
    for (int k = 0; k < 32; ++k)
#pragma unroll
      for (int i = 0; i < 4; ++i) {
        if (k == 0) asm volatile("v_mfma_f32_16x16x32_bf16 %0, %1, %2, 0" : "=&v"(acc[b][i]) : "a"(W[k][i & 1]), "v"(xf[k & 3][i >> 1]));
        else asm volatile("v_mfma_f32_16x16x32_bf16 %0, %1, %2, %0" : "+v"(acc[b][i]) : "a"(W[k][i & 1]), "v"(xf[k & 3][i >> 1]));
        SB0;
        const int q = k >> 3, n = 4 * k + i;
        if ((k & 7) == 5 && i == 0) {
          if (q == SIGQ) wait_vm<4 * SIGQ>(); else wait_vm<20>();
          if (q == 1 && __builtin_expect(s - 2u < W_NOW, 0)) { wait_vm<4>(); __builtin_amdgcn_s_barrier(); signal(s - 1); }
          else if (q == 0 && b == 0 && __builtin_expect(s == 1, 0)) { wait_vm<32 - 4 * WAH>(); __builtin_amdgcn_s_barrier(); signal(0); }
          else __builtin_amdgcn_s_barrier();
          asm volatile("" ::: "memory");
        }
        if (i == 1 || i == 2) rd(k + 3 < 32 ? b : b ^ 1, (k + 3) & 31, i - 1);
        {
          const int m = (n + 128 - 23) & 127, j = (m & 31) / DSP, q0 = m >> 5;
          if (j < 4 && (n == 0 || ((m & 31) % DSP == 0 && n != 127))) asm volatile("s_add_i32 m0, %0, %1" :: "s"(lds0), "n"(((b ^ (q0 == 0) ^ (n < 23)) * 4 + ((q0 + 3) & 3)) * 16384 + j * 1024) : "m0", "scc");
          if (n == 0) asm volatile("s_nop 0");
          if (j < 4 && (m & 31) % DSP == 1)
            asm volatile("buffer_load_dwordx4 %0, %1, %2 offen lds" :: "v"(vq[(q0 + 3) & 3][j & 3]), "s"(rx2), "s"((s + 2 - (q0 == 0) - (n < 23)) * 65536) : "memory");
        }
        if (k == 8 * SIGQ + 5 && i == 2) signal(s - 1);
        if (k >= 1 && k < 5 && (i == 0 || i == 3)) { const f32x4 x = acc[b ^ 1][k - 1]; o[2 * (~k & 1) + (i & 1)] = pk2v(x[2 * (i & 1)], x[2 * (i & 1) + 1]); }
        if ((k == 2 || k == 4) && i == 3) { __builtin_amdgcn_raw_buffer_store_b128(o, ru, (unsigned)(lane * 16 + (k >> 2) * 4096), so, ST_AUX); asm volatile("s_nop 0" :: "v"(o)); }
        SB0;
      }
  };
  A1 a1;
#pragma unroll
  for (int i = 0; i < 11; ++i) asm volatile("" : "=v"(a1.kk[i]), "=v"(a1.vv[i & 7]), "=v"(a1.sh[i & 3]));
  const int wi = 32 * (2 - kind) + 4 * (h & 7) + w;
  for (int s = 1, ph = 0;; ++ph) {
    const int n = ph == 1 ? wi : (ph == 2 && wi >= 64 ? 32 + wi : 256);
    for (const int e = s + (ph ? A1_D : A1_0); s < e || ph == 4; s += 2) {
      seg(s, 0);
      if (s == NSEG - 1) break;
      seg(s + 1, 1);
    }
    if (ph == 4) break;
    if (ph == 3 && lane == 0) ctr_add(a, CT_A1 + ct_near(~sel) + (h >> 3), 1u);
    if (n < 128) a1.finish(a, h >> 3, n, smem + 131072 + w * 8192, sel);
    a1.load(a, h >> 3, ph == 0 ? wi : (ph == 1 && wi >= 64 ? 32 + wi : 256));
    SB0;
  }
#pragma unroll
  for (int T = 0; T < 2; ++T) st16s(pk8v(acc[0][2 * T], acc[0][2 * T + 1]), ru, lane * 16 + T * 4096, x_slot(kind, NSEG - 1, h, sel) + w * 1024);
  bg_drain();
  signal(NSEG - 1);
}
constexpr int A2_CSTR = 4160;
constexpr int A2_UOFF = 4 * A2_CSTR + 16 * 80;
constexpr int A2_USTR = 80 * 80;
constexpr int A2_WLDS = 4 * A2_CSTR + 2 * A2_USTR;
__device__ __forceinline__ void a2_wave(const ChainArgs& a, int d0, int d1, char* smem, int w, Sel sel) {
  const int lane = opaque_tid(0);
  char* Wb = smem + 1024 + w * A2_WLDS;
  char* U = Wb + A2_UOFF;
  const __amdgpu_buffer_rsrc_t rf = mk_rsrc((void*)a.Assm, 4u << 20), rd_ = mk_rsrc((void*)a.AD, 2048);
  const __amdgpu_buffer_rsrc_t rkv = rsrc_u(a.sync);
  u32x4 hi[4], uv[2][4];
  u32x2 lo[4];
  unsigned dn;
  auto ld_h = [&](int d) {
#pragma unroll
    for (int i = 0; i < 4; ++i) {
      hi[i] = __builtin_amdgcn_raw_buffer_load_b128(rf, (unsigned)((255 - 64 * i - lane) * 16), (unsigned)(d * 4096), 2);
      lo[i] = __builtin_amdgcn_raw_buffer_load_b64(rf, i == 3 && lane == 63 ? 0x7fffffffu : (255 - 64 * i - lane) * 16 - 8, (unsigned)(d * 4096), 2);
    }
    dn = __builtin_amdgcn_raw_buffer_load_b16(rd_, 0, 2 * d, 0);
  };
  auto ld_u = [&](int d) {
#pragma unroll
    for (int i = 0; i < 8; ++i) uv[i & 1][i >> 1] = __builtin_amdgcn_raw_buffer_load_b128(rkv, (unsigned)max(((i >> 1) * 64 + lane) * 16 - 8 * (i & 1), 0), kv_row(d, sel), LD_AUX);
  };
  ld_h(d0);
  {
    bool dead = false;
    ctr_wait<BG_SLP>(ctr(a, CT_A1 + ct_near(~sel) + (d0 >> 8)), 96u, (GAS unsigned*)(a.sync + SY_ABORT), lane, dead);
  }
  ld_u(d0);
  if (lane < 12) *(f32x4*)(Wb + (lane / 3) * A2_CSTR + (256 + lane % 3) * 16) = vzero();
#pragma unroll
  for (int i = 0; i < 4; ++i) if (64 * (i & 1) + lane < 80) *(f32x4*)(U + (i >> 1) * A2_USTR - 16 * 80 + (64 * (i & 1) + lane) * 16) = vzero();
  const int r = lane & 15, g = lane >> 4;
#pragma unroll 1
  for (int d = d0; d < d1; d += 96) {
    u64 at[6]; (void)at;
    AST(0);
#pragma unroll
    for (int i = 0; i < 4; ++i) {
      const unsigned src[6] = {lo[i][0], lo[i][1], hi[i][0], hi[i][1], hi[i][2], hi[i][3]};
      unsigned f[10];
#pragma unroll
      for (int x = 0; x < 10; ++x) {
        if ((x & 1) == 0) f[x] = __builtin_amdgcn_alignbit(src[5 - x / 2], src[5 - x / 2], 16);
        else f[x] = __builtin_amdgcn_perm(src[5 - (x + 1) / 2], src[5 - (x - 1) / 2], 0x07060100u);
      }
#pragma unroll
      for (int m = 0; m < 4; ++m) *(u32x4*)(Wb + m * A2_CSTR + (64 * i + lane) * 16) = (u32x4){f[3 - m], f[5 - m], f[7 - m], f[9 - m]};
    }
    if (lane == 0) uv[1][0] = (u32x4){0u, 0u, uv[1][0][0], uv[1][0][1]};
#pragma unroll
    for (int i = 0; i < 8; ++i) {
      const int ch = (i >> 1) * 64 + lane;
      *(u32x4*)(U + (i & 1) * A2_USTR + (ch >> 2) * 80 + (ch & 3) * 16) = uv[i & 1][i >> 1];
    }
    const float Dd = blo(dn);
    asm volatile("" :: "v"(Dd) : "memory");
    AST(1);
    ld_h(d + 96 < d1 ? d + 96 : D_A); ld_u(d + 96 < d1 ? d + 96 : D_A);
    f32x4 acc[4][2];
#pragma unroll
    for (int i = 0; i < 8; ++i) acc[i >> 1][i & 1] = vzero();
    const char* pa = Wb + (r & 3) * A2_CSTR + 16 * (255 - (r >> 2) + g) - 3 * 1024 - 64;
    const char* pb = U + (r - 1) * 80 + 16 * g;
    bf16x8 fa[2][4], fb[2][8];
    auto rd = [&](int b, int n, int k) {
      if (n == 0 || n > 8) fa[b][n ? n - 8 : 0] = *(const bf16x8*)(pa + 1024 * (3 - (n ? n - 8 : 0)) + 64 * (1 - k));
      else fb[b][n - 1] = *(const bf16x8*)(pb + ((n - 1) & 1) * A2_USTR + 80 * (16 * ((n - 1) >> 1) + 1 - k));
    };
#pragma unroll
    for (int n = 0; n < 12; ++n) rd(0, n, 0);
    AST(2);
#pragma unroll 1
    for (int k = 0; k < 16; k += 2) {
#pragma unroll
      for (int b = 0; b < 2; ++b) {
        int q = 0;
#pragma unroll
        for (int j = 0; j < 4; ++j)
#pragma unroll
          for (int i = j; i < 4; ++i)
#pragma unroll
            for (int mt = 0; mt < 2; ++mt) {
              asm volatile("v_mfma_f32_16x16x32_bf16 %0, %1, %2, %0" : "+v"(acc[i][mt]) : "v"(fa[b][j]), "v"(fb[b][2 * (i - j) + mt]));
              SB0;
              if (q < 12) rd(b ^ 1, q, 1);
              if (q == 12) { pa -= 64; pb -= 80; }
              ++q;
              SB0;
            }
      }
    }
    AST(3);
    u32x4 kv[4];
#pragma unroll
    for (int i = 0; i < 4; ++i) kv[i] = *(const u32x4*)(U + (16 * i + r) * 80 + 16 * g);
    asm volatile("s_nop 7\n\ts_waitcnt vmcnt(0)" ::: "memory");
#pragma unroll
    for (int i = 0; i < 8; ++i) asm volatile("" : "+v"(acc[i >> 1][i & 1]));
    if (lane == 0 && d > d0) ctr_add(a, CT_A2 + ct_near(~sel) + ((d - 96) >> 6), 1u);
#pragma unroll
    for (int i = 0; i < 4; ++i) {
      const f32x4 x = acc[i][1], y = acc[i][0];
      st16s((u32x4){pk2(fmaf(blo(kv[i][0]), Dd, x[0]), fmaf(bhi(kv[i][0]), Dd, x[1])), pk2(fmaf(blo(kv[i][1]), Dd, x[2]), fmaf(bhi(kv[i][1]), Dd, x[3])),
                    pk2(fmaf(blo(kv[i][2]), Dd, y[0]), fmaf(bhi(kv[i][2]), Dd, y[1])), pk2(fmaf(blo(kv[i][3]), Dd, y[2]), fmaf(bhi(kv[i][3]), Dd, y[3]))},
            rkv, (unsigned)(i * 1024 + r * 64 + g * 16), kv_row(d, sel));
    }
    AST(4);
  }
  wait_vm<0>();
  if (lane == 0) ctr_add(a, CT_A2 + ct_near(~sel) + ((d0 + (d1 - 1 - d0) / 96 * 96) >> 6), 1u);
}
__device__ __forceinline__ void task_u(const ChainArgs& a, int h, char* smem, int wv, Gate& G) {
  const int lane = opaque_tid(0);
  const int w = wv;
  GAS unsigned* abortw = (GAS unsigned*)(a.sync + SY_ABORT);
  bool dead = false;
  char* sb = smem + w * 32768;
  const __amdgpu_buffer_rsrc_t rwo = mk_rsrc(a.Wo, 8u << 20);
  const unsigned lds0 = (unsigned)(uintptr_t)(LAS char*)sb;
  unsigned wo4[4];
#pragma unroll
  for (int i = 0; i < 4; ++i) { const int row = i * 4 + (lane >> 4); wo4[i] = row * DM * 2 + (((lane & 15) ^ swz_b(row)) * 16); }
  asm volatile("s_nop 4");
  auto stage = [&](int n) {
    const int p = (U_ORD >> (4 * n)) & 3;
#pragma unroll
    for (int i = 0; i < 16; ++i)
      dma16(rwo, wo4[i & 3], (unsigned)(((128 * h + 64 * (p & 1) + 16 * (i >> 2)) * DM + 256 * w + 128 * (p >> 1)) * 2), lds0 + (n & 1) * 16384 + i * 1024, W_NT & 2);
  };
  __builtin_amdgcn_s_waitcnt(0x0F70);
  u32x4 W[16][4];
  stage(0); stage(1);
#pragma unroll
  for (int n = 0; n < 4; ++n) {
    const int p = (U_ORD >> (4 * n)) & 3;
    if (n < 3) wait_vm<16>(); else wait_vm<0>();
#pragma unroll
    for (int t = 0; t < 8; ++t) {
      const u32x8 f = tr_frag(sb + (n & 1) * 16384, t, lane);
      u32x4 f0 = {f[0], f[1], f[2], f[3]}, f1 = {f[4], f[5], f[6], f[7]};
      asm volatile("" : "+a"(f0), "+a"(f1));
      W[8 * (p >> 1) + t][2 * (p & 1)] = f0; W[8 * (p >> 1) + t][2 * (p & 1) + 1] = f1;
    }
    asm volatile("s_waitcnt lgkmcnt(0)" ::: "memory");
    if (n + 2 < 4) stage(n + 2);
  }
  lds_barrier();
  if (!G.pass(a.sync)) return;
  SSTAMP(192 + h);
  const Sel sel = G.sel;
  const u32x4 ru = own_rsrc(a.sync, SY_BYTES);
  u32x4 V[2][8], pc[2], o[2];
  f32x4 c[2][2];
  unsigned cv, cw, vo[8], lk[2], sgo = (w | lane) ? 0x7fffffffu : SY_CT + (CT_U + ct_near(sel) + h) * 128, nd = (lane & 1) ? 3 - URING : 2;
#pragma unroll
  for (int i = 0; i < 8; ++i) { vo[i] = lane * 16 + ux_tile(16 * w + 2 * i); asm volatile("" : "+v"(vo[i])); }
#pragma unroll
  for (int i = 0; i < 2; ++i) lk[i] = SY_CT + (ct_near(~sel) + ((lane & 1) ? CT_DONE + 8 * i + (h & 7) : CT_V + h)) * 128;
  char* vl = lds_base(smem, lane * 16);
  const unsigned vl2 = lane * 16 + w * 2048;
  auto ld_v = [&](int s, bool pad = false) {
    if (pad) asm volatile("s_nop 4\n\tbuffer_load_dwordx4 %0, %2, %3, %4 offen sc1\n\tbuffer_load_dwordx4 %1, %2, %3, %4 offen offset:1024 sc1" : "=&v"(pc[0]), "=&v"(pc[1]) : "v"(vl2), "s"(ru), "s"(x_slot(2, s, h, sel)) : "memory");
    else asm volatile("buffer_load_dwordx4 %0, %2, %3, %4 offen sc1\n\tbuffer_load_dwordx4 %1, %2, %3, %4 offen offset:1024 sc1" : "=&v"(pc[0]), "=&v"(pc[1]) : "v"(vl2), "s"(ru), "s"(x_slot(2, s, h, sel)) : "memory");
  };
  auto signal = [&](int s) {
    asm volatile("buffer_store_dword %0, %1, %2, 0 offen sc1" :: "v"(s), "v"(sgo), "s"(ru) : "memory");
  };
  auto st_u = [&](int n, unsigned ub) { asm volatile("buffer_store_dwordx4 %0, %1, %2, %3 offen offset:%4 sc1" :: "v"(o[n & 1]), "v"(vo[n >> 1]), "s"(ru), "s"(ub), "n"((n & 1) * 1024) : "memory"); };
  auto fetch = [&](int s) {
    for (unsigned spins = 0;;) {
      asm volatile("buffer_load_dword %0, %1, %2, 0 offen sc1" : "=&v"(cw) : "v"(lk[0]), "s"(ru) : "memory");
      ld_v(s, true);
      asm volatile("s_waitcnt vmcnt(0)" : "+v"(pc[0]), "+v"(pc[1]), "+v"(cw) :: "memory");
      if (s && spins == 0) { __builtin_amdgcn_s_barrier(); signal(0); }
      if ((int)__builtin_amdgcn_readfirstlane(cw) >= s) break;
      __builtin_amdgcn_s_sleep(2);
      if (poll_fail(spins, abortw, lane)) { dead = true; break; }
    }
    WST(s == 0, 24);
    if (s == 0) TP(3);
#pragma unroll
    for (int i = 0; i < 2; ++i) *(u32x4*)(vl + 16384 + w * 2048 + i * 1024) = pc[i];
    lds_barrier();
#pragma unroll
    for (int i = 0; i < 8; ++i) V[s][i] = *(const u32x4*)(vl + 16384 + i * 1024);
  };
#pragma unroll
  for (int i = 0; i < 8; ++i) asm volatile("" : "=v"(V[0][i]));
  u64 bad = 0;
  unsigned up = 0x7f000000u;
  c[1][0] = c[1][1] = vzero();
  auto seg = [&](int s, int b) {
    const unsigned ub = s < 0 ? 0x7f000000u : ux_base(s, h, sel);
    WST(s >= 0 && s < 4, 25 + s);
#pragma unroll
    for (int n = 0; n < 16; ++n)
#pragma unroll
      for (int q = 0; q < 8; ++q) {
        const int kp = q >> 1, m = q & 1;
        if (kp == 0) asm volatile("v_mfma_f32_16x16x32_bf16 %0, %1, %2, 0" : "=&v"(c[n & 1][m]) : "v"(V[b][4 * m + kp]), "a"(W[n][kp]));
        else asm volatile("v_mfma_f32_16x16x32_bf16 %0, %1, %2, %0" : "+v"(c[n & 1][m]) : "v"(V[b][4 * m + kp]), "a"(W[n][kp]));
        SB0;
        if (q >= 2 && q < 6) { const f32x4 x = c[~n & 1][(q - 2) >> 1]; o[~n & 1][q - 2] = pk2v(x[2 * (q & 1)], x[2 * (q & 1) + 1]); }
        if (q == 6) st_u((n - 1) & 15, n ? ub : up);
        if (q == 5) asm volatile("" :: "v"(o[n & 1]));
        if (n == U_LD && q == 0) {
          if (__builtin_expect(bad != 0, 0)) {
            if (s <= U_NOW) {
              asm volatile("s_waitcnt vmcnt(%1)" : "+v"(cw) : "n"(U_LD - 2) : "memory");
              __builtin_amdgcn_s_barrier();
              signal(s - 1);
              bad = (int)__builtin_amdgcn_readfirstlane(cw) <= s;
            }
            if (bad & 1) ctr_wait<2>(ctr(a, CT_V + ct_near(~sel) + h), min(s + 2, NSEG), abortw, lane, dead);
            if (bad & 2) {
              ctr_wait<U_SLP>(ctr(a, CT_DONE + ct_near(~sel) + 8 * b + (h & 7)), max(s + 3 - URING, 0), abortw, lane, dead);
              for (int i = 0; i < U_STG * (h & 7) + U_ST0; ++i) __builtin_amdgcn_s_sleep(1);
            }
          }
          ld_v(min(s + 1u, NSEG - 1u));
        }
        if (n == 1 && q == 7) asm volatile("buffer_load_dword %0, %1, %2, 0 offen sc1" : "=&v"(cw) : "v"(s <= U_NOW ? lk[0] : 0x7fffffffu), "s"(ru) : "memory");
        if (n == U_LD && q == 7) asm volatile("buffer_load_dword %0, %1, %2, 0 offen sc1" : "=&v"(cv) : "v"(lk[b ^ 1]), "s"(ru) : "memory");
        if (n == U_SIG2 && q == 0) asm volatile("s_waitcnt vmcnt(%4)" : "+v"(pc[0]), "+v"(pc[1]), "+v"(cv), "+v"(cw) : "n"(U_SIG2 - U_LD - 1) : "memory");
        if (n == U_SIG2 && (q == 1 || q == 7)) *(u32x4*)(vl + (b ^ 1) * 8192 + w * 2048 + (q >> 2) * 1024) = pc[q >> 2];
        if (n == U_SIG2 + 1 && q == 0) { bad = __builtin_amdgcn_ballot_w64(s >= 0 && (int)cv < (s < U_NOW && lane > 1 ? NSEG : min(s + (int)nd, NSEG - 1))); asm volatile("" : "+s"(bad)); }
        if (n == U_SIG2 + 1 && q == 7) lds_barrier();
        if (n == U_SIG2 + 2 && q == 0) signal(s - 1);
        if ((n == U_SIG2 + 2 || n == U_SIG2 + 3) && (q & 1)) V[b ^ 1][4 * (n - U_SIG2 - 2) + (q >> 1)] = *(const u32x4*)(vl + (b ^ 1) * 8192 + (4 * (n - U_SIG2 - 2) + (q >> 1)) * 1024);
        SB0;
      }
    up = ub;
  };
  for (int s = -U_DRY; s < NSEG; s += 2) {
    if (__builtin_expect(s == 0, 0)) fetch(0);
    seg(s, 0);
    if (__builtin_expect(s == 0, 0)) {
      o[1] = pk8v(c[1][0], c[1][1]);
      st_u(15, up);
      fetch(1);
    }
    seg(s + 1, 1);
  }
  o[1] = pk8v(c[1][0], c[1][1]);
  st_u(15, up);
  bg_drain();
  signal(NSEG - 1);
}

__device__ __forceinline__ void task_a3(const ChainArgs& a, int dg, int t0, char* T, Sel sel) {
  const int lane = opaque_tid(0);
  const int d = dg * 64 + 8 * (lane & 7);
  const int tb = t0 + 8 * (lane >> 3);
  const __amdgpu_buffer_rsrc_t rz = rsrc_u(a.sync);
  u32x4 qq[16], zz[8][2];
#pragma unroll
  for (int i = 0; i < 16; ++i) qq[i] = LDNT((const u32x4*)(a.Aq + (long)(tb + 64 * (i >> 3) + (i & 7)) * D_A + d));
  {
    u32x4 zr[16];
    bool dead = false;
    ctr_wait<A3_SLP>(ctr(a, CT_A2 + ct_near(~sel) + dg), 64u, (GAS unsigned*)(a.sync + SY_ABORT), lane, dead);
  #pragma unroll
    for (int i = 0; i < 16; ++i) zr[i] = ld16(rz, kv_row(dg * 64, sel) + (2 * i + (lane >> 5)) * CHK + ((lane >> 4) & 1) * 4096 + (t0 + (lane & 15) * 8) * 2);
#pragma unroll
    for (int i = 0; i < 16; ++i) {
      const int row = 4 * i + (lane >> 4);
      *(u32x4*)(T + row * 256 + (((lane & 15) ^ (row >> 3)) * 16)) = zr[i];
    }
  }
#pragma unroll
  for (int c = 0; c < 8; ++c)
#pragma unroll
    for (int gq = 0; gq < 2; ++gq) zz[c][gq] = *(const u32x4*)(T + (8 * (lane & 7) + c) * 256 + (((gq * 8 + (lane >> 3)) ^ (lane & 7)) * 16));
#pragma unroll
  for (int i = 0; i < 16; ++i) {
    u32x4 o;
#pragma unroll
    for (int c = 0; c < 8; c += 2) {
      const unsigned z0 = zz[c][i >> 3][(i >> 1) & 3], z1 = zz[c + 1][i >> 3][(i >> 1) & 3];
      o[c >> 1] = pk2(blo(qq[i][c >> 1]) * ((i & 1) ? bhi(z0) : blo(z0)), bhi(qq[i][c >> 1]) * ((i & 1) ? bhi(z1) : blo(z1)));
    }
    st16g(a.out + (long)(tb + 64 * (i >> 3) + (i & 7)) * OUTW + d, o);
  }
}

__device__ __forceinline__ void part_a(const ChainArgs& a, char* smem, int x, int wi, int w, Sel sel) {
  char* T = smem + 1024 + w * A2_WLDS;
  if (wi >= 0) a2_wave(a, 256 * x + wi, 256 * x + 256, smem, w, sel);
  while (true) {
    unsigned t = 0;
    if (opaque_tid(0) == 0) t = __hip_atomic_fetch_add(ctr(a, CT_TK + ct_near(~sel) + x), 1u, __ATOMIC_RELAXED, __HIP_MEMORY_SCOPE_AGENT) + 1u;
    t = __builtin_amdgcn_readfirstlane(t);
    if (t >= 64) break;
    task_a3(a, 4 * x + (t >> 4), (t & 15) * 128, T, sel);
  }
}

__global__ __launch_bounds__(256, 1) void chain_kernel(ChainArgs a) {
  extern __shared__ __attribute__((aligned(16))) char smem[];
  const int b = blockIdx.x;
  {
    u32x4 rc;
    asm volatile("s_getpc_b64 s[60:61]\n\t.Lchain_here:\n\ts_sub_u32 s60, s60, .Lchain_here-_Z12chain_kernel9ChainArgs\n\ts_subb_u32 s61, s61, 0\n\ts_and_b32 s61, s61, 0xffff\n\t"
                 "s_mov_b32 s62, .Lchain_end-_Z12chain_kernel9ChainArgs\n\ts_mov_b32 s63, 0x20000\n\t"
                 "buffer_load_dwordx4 a[252:255], %1, s[60:63], 0 offen\n\tbuffer_load_dwordx4 a[252:255], %1, s[60:63], s63 offen"
                 : "={s[60:63]}"(rc) : "v"((b >> 3) * 4096 + threadIdx.x * 16) : "scc", "a252", "a253", "a254", "a255");
  }
  int w = __builtin_amdgcn_readfirstlane(threadIdx.x >> 6);
  Gate G;
  G.entry = __builtin_amdgcn_s_memrealtime(); G.open = false; G.arr = w == 0 ? b & 7 : -1;
  G.rot = ((unsigned)__builtin_amdgcn_s_getreg(20 | (3 << 11)) - b + WRONG) & 7u;
  {
    volatile LAS unsigned* t = (volatile LAS unsigned*)smem;
    if (w == 0) { t[0] = (unsigned)G.entry; t[1] = (unsigned)(G.entry >> 32); }
    __syncthreads();
    G.entry = (u64)(unsigned)__builtin_amdgcn_readfirstlane(t[0]) | ((u64)(unsigned)__builtin_amdgcn_readfirstlane(t[1]) << 32);
    __syncthreads();
  }
  G.tx = G.entry;
  if (((b + G.rot) & 4) == 0 && b < 128) {
    const int c = (b >> 6) * 32 + 8 * ((b + G.rot) & 3) + ((b >> 3) & 7);
    if (w < 2) calibrate(a, w ? 32 + (0x3200 >> (4 * (c & 3)) & 3) : c, 2 * c + w, G.entry);
    __builtin_amdgcn_s_barrier();
  }
  if (b == 0) {
    const int lane = opaque_tid(0);
    const __amdgpu_buffer_rsrc_t rs = mk_rsrc(a.sync + SY_SET, 3 * CHK);
    st16(rs, CHK + w * 8192 + lane * 128, (u32x4){SENT, SENT, SENT, SENT});
    if (w == 0 && lane >= 2) st16(rs, lane * 128, (u32x4){SENT, SENT, SENT, SENT});
    if (w == 3) {
      u32x4 v;
      bool ok;
      do {
        v = ld16(mk_rsrc(a.sync + SY_CT + CT_CAL * 128, 8192), lane * 128);
        ok = ((u64)v[1] << 32 | v[0]) - G.entry + 2000u < (1ull << 31) && ((u64)v[3] << 32 | v[2]) - G.entry + 2000u < (1ull << 31);
      } while (!__all(ok) && __builtin_amdgcn_s_memrealtime() - G.entry < 2 * CALM);
      const u64 k = lane == 32 ? 0x3333333333333333ull : (lane == 34 || lane == 35 ? 0x1111111111111111ull << (lane & 3) : 0ull);
      const int n1 = __builtin_popcountll(k & __builtin_amdgcn_ballot_w64(ok && (v[2] & 3u) == 3u)), n0 = __builtin_popcountll(k & __builtin_amdgcn_ballot_w64(ok && (v[2] & 3u) == 2u));
      if (!ok) v[0] = 0;
      if (n0 != n1) v[0] = n1 > n0 ? 3u : 2u;
      const unsigned t = (v[0] ^ lane) & 1u;
      const bool most = __builtin_popcountll(__builtin_amdgcn_ballot_w64((v[0] & 2u) && t)) > __builtin_popcountll(__builtin_amdgcn_ballot_w64((v[0] & 2u) && !t));
      const u64 m = __builtin_amdgcn_ballot_w64((v[0] & 2u) ? v[0] & 1u : (lane ^ most) & 1u);
      if (lane == 0) __hip_atomic_store((GAS u64*)(a.sync + SY_SEL), m, __ATOMIC_RELAXED, __HIP_MEMORY_SCOPE_AGENT);
    }
    bg_drain();
    if (w == 0 && lane == 0) __hip_atomic_store((GAS u64*)(a.sync + SY_EPOCH), (__builtin_amdgcn_s_memrealtime() & ~7ull) | G.rot, __ATOMIC_RELAXED, __HIP_MEMORY_SCOPE_AGENT);
  }
  while (true) {
    const unsigned rot = G.rot;
    asm volatile("" : "+s"(w));
    const int x = (b + rot) & 7, j = b >> 3, h = 8 * (x & 3) + (j & 7), role = (x & 4) + (j >> 3);
    if ((role == 0 || role == 7) && w >= 2) {
      const int n = opaque_tid(w - 2);
      const unsigned x0 = __builtin_amdgcn_raw_buffer_load_b32(mk_rsrc(a.sync, 106u << 20), n * PAGE + 4096 + (j & 31) * 128, 0, 2);
      const unsigned x1 = __builtin_amdgcn_raw_buffer_load_b32(mk_rsrc(a.out, 8u << 20), n * PAGE + (j & 31) * 128, 0, 2);
      asm volatile("" :: "v"(x0), "v"(x1));
    }
    if (role < 2) role_cb(a, 32 * role + h, smem, w, G);
    else if (role == 2) role_qk(a, h, smem, w, G);
    else if (role == 3) role_vo(a, h, smem, w, G);
    else if (role == 4) task_u(a, h, smem, w, G);
    else task_w(a, 7 - role, h, smem, w, G);
    if (G.rot == rot && role >= 4 && !NOA) part_a(a, smem, x & 3, 4 * (j - 8) + w, w, G.sel);
    if (G.rot == rot) break;
    __syncthreads();
  }
  asm volatile(".Lchain_end:");
}

extern "C" void kernel_launch(void* const* d_in, const int* in_sizes, int n_in,
                              void* d_out, int out_size, void* d_ws, size_t ws_size,
                              hipStream_t stream) {
  const u16* A_q = (const u16*)d_in[0];
  const u16* A_k = (const u16*)d_in[1];
  const u16* A_v = (const u16*)d_in[2];
  const u16* A_shift = (const u16*)d_in[3];
  const u16* A_ssmk = (const u16*)d_in[4];
  const u16* A_D = (const u16*)d_in[5];
  const u16* B_x = (const u16*)d_in[6];
  const u16* B_mem0 = (const u16*)d_in[7];
  const u16* B_Wq = (const u16*)d_in[8];
  const u16* B_Wk = (const u16*)d_in[9];
  const u16* B_Wv = (const u16*)d_in[10];
  const u16* B_Wo = (const u16*)d_in[11];
  u16* out = (u16*)d_out;
  char* ws = (char*)(((uintptr_t)d_ws + (2u << 20) - 1) & ~(uintptr_t)((2u << 20) - 1)) + WSOFF * MB;
  hipFuncSetAttribute((const void*)chain_kernel, hipFuncAttributeMaxDynamicSharedMemorySize, CHAIN_LDS);
  ChainArgs ca;
  ca.prof = (u64*)((char*)d_ws + PROF_AT);
  ca.mem0 = B_mem0; ca.Wq = B_Wq; ca.Wk = B_Wk; ca.Wv = B_Wv; ca.Wo = B_Wo; ca.X = B_x;
  ca.out = out; ca.sync = ws;
  ca.Aq = A_q; ca.Ak = A_k; ca.Av = A_v; ca.Ashift = A_shift; ca.Assm = A_ssmk; ca.AD = A_D;
  chain_kernel<<<256, 256, CHAIN_LDS, stream>>>(ca);
}
```

```cpp
#include <hip/hip_runtime.h>
#include <hip/hip_bf16.h>
#include <stdint.h>
#include <type_traits>

constexpr int S_LEN = 2048;
constexpr int D_A   = 1024;
constexpr int DM    = 1024;
constexpr int NH    = 32;
constexpr int DH    = 128;
constexpr int HD    = NH * DH;
constexpr int MEMR  = 8;
constexpr int SEG   = 32;
constexpr int NSEG  = S_LEN / SEG;
constexpr int OUTW  = 2048;

typedef unsigned short u16;
typedef unsigned long long u64;
typedef short bf16x8 __attribute__((ext_vector_type(8)));
typedef short bf16x4 __attribute__((ext_vector_type(4)));
typedef float f32x4 __attribute__((ext_vector_type(4)));
typedef float f32x16 __attribute__((ext_vector_type(16)));
typedef unsigned u32x4 __attribute__((ext_vector_type(4)));
typedef unsigned u32x2 __attribute__((ext_vector_type(2)));
#define LAS __attribute__((address_space(3)))
#define GAS __attribute__((address_space(1)))

constexpr size_t MB = 1u << 20;
constexpr size_t PROF_AT = 0;

__device__ __forceinline__ float bf2f(u16 b) { return __uint_as_float(((unsigned)b) << 16); }
__device__ __forceinline__ u16 f2bf(float f) { __bf16 b = (__bf16)f; return __builtin_bit_cast(u16, b); }
__device__ __forceinline__ float rbf(float f) { return bf2f(f2bf(f)); }
__device__ __forceinline__ unsigned pk2(float a, float b) {
  typedef float f2 __attribute__((ext_vector_type(2)));
  typedef __bf16 b2 __attribute__((ext_vector_type(2)));
  f2 v = {a, b}; b2 r = __builtin_convertvector(v, b2); return __builtin_bit_cast(unsigned, r);
}

__device__ __forceinline__ int opaque_tid(int wv) {
  int l;
  asm volatile("v_mbcnt_lo_u32_b32 %0, -1, 0\n\tv_mbcnt_hi_u32_b32 %0, -1, %0" : "=v"(l));
  return wv * 64 + l;
}
__device__ __forceinline__ int swz_b(int row) { return ((row & 3) << 2) | ((row >> 2) & 3); }

#define KNOB(name, v) constexpr int name = v
#define KDEF 70
KNOB(K_DEF, KDEF);
KNOB(W_STEP, 33); KNOB(W_OFF, 56);
#define RHOV 3
KNOB(RHO, RHOV);
#define CDUR 1510
#define U_SLP 8
#define U_STG 0
#define U_ST0 0
#define WSOFF 122
#define START_SLP 3
#define QK_D 0
#define U_DRY 0
#define A1_0 20
#define A1_D 4
#define PSEG 1
#define KQ_NOW 2
#define NOA 0
#define V_PF 4
#define V_PF0 3
#define CALN 3
#define CALM 500u
#define CALT 680u
#define KQ_D 0
#define W_NOW 4
#define U_SIG2 11
#define U_LD 5
#define U_NOW 6
#define WRONG 0
#define OUTP "sc1"
#define LDNT(p) __builtin_nontemporal_load(p)
constexpr int CHK      = 16384;
constexpr int PAGE     = 2 << 20;
constexpr int SY_UM    = 34 * PAGE;
constexpr int SY_HOT   = 32 * PAGE;
constexpr int H_MEM    = 0;
constexpr int H_P      = 16 * CHK;
constexpr int H_ANN    = 28 * CHK;
constexpr int H_DLT    = 32 * CHK;
constexpr int H_TR     = 33 * CHK;
constexpr int H_PX     = 64 * CHK;
constexpr int H_BYTES  = 128 * CHK;
constexpr int URING    = 16;
constexpr int SY_SET   = 52 * PAGE;
constexpr int SY_EPOCH = SY_SET + 128;
constexpr int SY_ABORT = SY_SET + 256;
constexpr int SY_SEL   = SY_EPOCH + 8;
constexpr int SY_CT    = SY_SET + CHK;
constexpr int CT_Q = 0, CT_K = 32, CT_U = 128;
constexpr int CT_V = 0, CT_DONE = 32, CT_A2 = 48, CT_A1 = 128, CT_TK = 132;
constexpr int CT_ARR = -124, CT_INIT = -116, CT_CAL = 256;
constexpr int SY_KV    = 48 * PAGE;
constexpr int SY_BYTES = SY_CT + (CT_CAL + 64) * 128;
constexpr unsigned SENT = 0xFFFFFFFFu;
constexpr unsigned SPIN_CAP = 1u << 19;
constexpr int NLF = 16;
constexpr int L_WTS = 0;
constexpr int L_MEM = L_WTS + 4 * NLF * 2048;
constexpr int L_XCH = L_MEM + 16384;
constexpr int L_XK  = L_XCH + 2048;
constexpr int L_KX  = L_XCH + 4096;
constexpr int L_PRF = L_KX + 8192;
constexpr int XRS = 40;
constexpr int CHAIN_LDS = 160 * 1024;
static_assert(L_XCH + 128 * XRS <= L_PRF && L_PRF + 4096 <= CHAIN_LDS, "lds");

typedef __bf16 bfv8 __attribute__((ext_vector_type(8)));
typedef __bf16 bfv16 __attribute__((ext_vector_type(16)));
typedef unsigned u32x8 __attribute__((ext_vector_type(8)));
typedef unsigned u32x12 __attribute__((ext_vector_type(12)));
typedef unsigned u32x16 __attribute__((ext_vector_type(16)));
typedef unsigned u32x32 __attribute__((ext_vector_type(32)));
struct Wts { u32x8 a[32]; u32x32 v[4]; };

#define STAMPL(i) do {} while (0)
#define PVAL(i, v) do {} while (0)
#define PDECL(base, str) do {} while (0)
#define PZERO do {} while (0)
#define PDUMP(mask) do {} while (0)
#define TA(x) do {} while (0)
#define TB(x) TS(x)
#define TS(x) do {} while (0)
#define SSTAMP(i) do {} while (0)
#define WST(c, i) do {} while (0)
#define AST(i) do {} while (0)
struct ChainArgs {
  u64* prof;
  const u16* mem0; const u16* Wq; const u16* Wk; const u16* Wv; const u16* Wo; const u16* X;
  u16* out; char* sync;
  const u16* Aq; const u16* Ak; const u16* Av; const u16* Ashift; const u16* Assm; const u16* AD;
};
__device__ __forceinline__ GAS unsigned* ctr(const ChainArgs& a, int i) { return (GAS unsigned*)(a.sync + SY_CT + i * 128); }
__device__ __forceinline__ void ctr_add(const ChainArgs& a, int i, unsigned v) {
  __hip_atomic_fetch_add(ctr(a, i), v, __ATOMIC_RELAXED, __HIP_MEMORY_SCOPE_AGENT);
}

__device__ __forceinline__ __amdgpu_buffer_rsrc_t mk_rsrc(const void* p, unsigned bytes) {
  const u64 v = (u64)p;
  const unsigned lo = (unsigned)__builtin_amdgcn_readfirstlane((unsigned)v), hi = (unsigned)__builtin_amdgcn_readfirstlane((unsigned)(v >> 32));
  const u64 u = (u64)lo | ((u64)hi << 32);
  return __builtin_amdgcn_make_buffer_rsrc((void*)u, 0, bytes, 0x00020000);
}
__device__ __forceinline__ u32x4 own_rsrc(const void* p, unsigned bytes) {
  u32x4 r = {(unsigned)__builtin_amdgcn_readfirstlane((unsigned)(u64)p), (unsigned)__builtin_amdgcn_readfirstlane((unsigned)((u64)p >> 32)) & 0xffffu, bytes, 0x00020000u};
  asm volatile("s_nop 4" : "+s"(r));
  return r;
}
constexpr int LD_AUX = 16, ST_AUX = 16;
__device__ __forceinline__ u32x4 ld16(__amdgpu_buffer_rsrc_t rs, unsigned off) {
  return __builtin_amdgcn_raw_buffer_load_b128(rs, off, 0, LD_AUX);
}
__device__ __forceinline__ void st16(__amdgpu_buffer_rsrc_t rs, unsigned off, u32x4 v) {
  __builtin_amdgcn_raw_buffer_store_b128(v, rs, off, 0, ST_AUX);
}
__device__ __forceinline__ void st16s(u32x4 v, __amdgpu_buffer_rsrc_t rs, unsigned voff, unsigned soff) {
  __builtin_amdgcn_raw_buffer_store_b128(v, rs, voff, soff, ST_AUX);
  asm volatile("s_nop 1" :: "v"(v) : "memory");
}
__device__ __forceinline__ void st8(__amdgpu_buffer_rsrc_t rs, unsigned off, u32x2 v) {
  __builtin_amdgcn_raw_buffer_store_b64(v, rs, off, 0, ST_AUX);
}
__device__ __forceinline__ void st4(__amdgpu_buffer_rsrc_t rs, unsigned off, unsigned v) {
  __builtin_amdgcn_raw_buffer_store_b32(v, rs, off, 0, ST_AUX);
}
__device__ __forceinline__ unsigned ld4(__amdgpu_buffer_rsrc_t rs, unsigned off) {
  return __builtin_amdgcn_raw_buffer_load_b32(rs, off, 0, LD_AUX);
}
constexpr int TU = 16;
__device__ __forceinline__ unsigned cyc32() { return (unsigned)__builtin_amdgcn_s_memtime(); }
__device__ __forceinline__ unsigned to_sgpr(unsigned x) {
  unsigned r;
  asm volatile("s_nop 0\n\tv_readfirstlane_b32 %0, %1\n\ts_nop 4" : "=s"(r) : "v"(x));
  return r;
}
struct Clk {
  unsigned ce, te;
  unsigned c2t, t2c;
  __device__ __forceinline__ void edge(unsigned& c, unsigned& t) {
    u64 c0, r0, r1, r2, r3, r4, r5;
    asm volatile("s_memtime %0\n\ts_memrealtime %1\n\ts_nop 4\n\ts_memrealtime %2\n\ts_nop 4\n\ts_memrealtime %3\n\ts_nop 4\n\t"
                 "s_memrealtime %4\n\ts_nop 4\n\ts_memrealtime %5\n\ts_nop 4\n\ts_memrealtime %6\n\ts_waitcnt lgkmcnt(0)"
                 : "=&s"(c0), "=&s"(r0), "=&s"(r1), "=&s"(r2), "=&s"(r3), "=&s"(r4), "=&s"(r5) :: "memory");
    const unsigned x = (unsigned)r0;
    const unsigned n = ((unsigned)r1 == x) + ((unsigned)r2 == x) + ((unsigned)r3 == x) + ((unsigned)r4 == x) + ((unsigned)r5 == x);
    c = to_sgpr((unsigned)c0 + 24u * n); t = (x + 4u) * TU;
  }
  __device__ __forceinline__ void init() { edge(ce, te); c2t = 44000u; t2c = 97600u; }
  __device__ __forceinline__ void sync() {
    unsigned c, t;
    edge(c, t);
    set(c, t);
  }
  __device__ __forceinline__ void set(unsigned c, unsigned t) {
    if (c == ce) return;
    const float q = (float)(t - te) * __builtin_amdgcn_rcpf((float)(c - ce));
    const float qc = fminf(fmaxf(q, 0.51f), 1.99f);
    c2t = to_sgpr((unsigned)(qc * 65536.f));
    t2c = to_sgpr((unsigned)(__builtin_amdgcn_rcpf(qc) * 65536.f));
    ce = c; te = t;
  }
  __device__ __forceinline__ unsigned time(unsigned c) const { return te + ((min(c - ce, 30000u) * c2t) >> 16); }
  __device__ __forceinline__ unsigned now() const { return time(cyc32()); }
  __device__ __forceinline__ unsigned target(unsigned t) const {
    return to_sgpr(ce + (((unsigned)min(max((int)(t - te), 0), 20000) * t2c) >> 16));
  }
  __device__ __forceinline__ void wait(unsigned t) const { wait_c(target(t)); }
  __device__ __forceinline__ void wait_c(unsigned c) const {
    unsigned y;
    asm volatile("1:\n\ts_memtime vcc\n\ts_waitcnt lgkmcnt(0)\n\ts_sub_i32 %0, %1, vcc_lo\n\ts_cmp_gt_i32 %0, 128\n\ts_cbranch_scc1 1b\n\t"
                 "2:\n\ts_sub_i32 %0, %0, %2\n\ts_cmp_gt_i32 %0, %3\n\ts_cbranch_scc1 2b"
                 : "=&s"(y) : "s"(c), "n"(W_STEP), "n"(W_OFF) : "memory", "scc", "vcc");
  }
};
__device__ __forceinline__ unsigned nosent(unsigned d) { return min(d, SENT - 1u); }
__device__ __forceinline__ int arow(int row, int half) { return 4 * (row >> 1) + 2 * half + (row & 1); }
__device__ __forceinline__ unsigned umax4(u32x4 v) { return max(max(v[0], v[1]), max(v[2], v[3])); }
__device__ __forceinline__ void stamp_again(GAS u64* p) {
  __hip_atomic_store(p, (__builtin_amdgcn_s_memrealtime() & ~7ull) | (__hip_atomic_load(p, __ATOMIC_RELAXED, __HIP_MEMORY_SCOPE_AGENT) & 7ull),
                     __ATOMIC_RELAXED, __HIP_MEMORY_SCOPE_AGENT);
}
__device__ __forceinline__ bool poll_fail(unsigned& spins, GAS unsigned* abortw, int lane) {
  ++spins;
  if (spins > SPIN_CAP) {
    if (lane == 0) __hip_atomic_store(abortw, 1u, __ATOMIC_RELAXED, __HIP_MEMORY_SCOPE_AGENT);
    return true;
  }
  if ((spins & 511u) == 0u && lane == 0) {
    unsigned n = 0;
    for (int i = 0; i < 8; ++i) n += __hip_atomic_load(abortw + (SY_CT - SY_ABORT + (CT_ARR + i) * 128) / 4, __ATOMIC_RELAXED, __HIP_MEMORY_SCOPE_AGENT) + 1u;
    if (n < 256u) stamp_again((GAS u64*)(abortw - 32));
  }
  if ((spins & 63u) == 0u)
    return __builtin_amdgcn_readfirstlane(__hip_atomic_load(abortw, __ATOMIC_RELAXED, __HIP_MEMORY_SCOPE_AGENT)) != SENT;
  return false;
}
#define TP(k) do {} while (0)
struct Gate {
  u64 tx; u64 entry, sel; int arr; unsigned rot; bool open;
  __device__ __forceinline__ bool look(char* sync, u32x4 v) {
    if (open) return true;
    for (unsigned spins = 1;; ++spins) {
#pragma unroll
      for (int i = 0; i < 4; ++i) v[i] = __builtin_amdgcn_readfirstlane(v[i]);
      if (((u64)v[1] << 32 | v[0]) - entry + 2000u < (1ull << 31) || spins > 4 * SPIN_CAP) break;
      if (spins > 1) __builtin_amdgcn_s_sleep(8);
      v = ld16(mk_rsrc(sync + SY_EPOCH, 16), 0);
    }
    asm volatile("" ::: "memory");
    sel = (u64)v[3] << 32 | v[2];
    const bool same = rot == (v[0] & 7u);
    rot = v[0] & 7u; open = true;
    return same;
  }
  __device__ __forceinline__ void ask(char* sync, u32x4& v) { asm volatile("s_nop 4\n\tbuffer_load_dwordx4 %0, off, %1, 0 sc1" : "=v"(v) : "s"(own_rsrc(sync + SY_EPOCH, 16)) : "memory"); }
  __device__ __forceinline__ void arrive(char* sync) {
    if (arr >= 0 && opaque_tid(0) == 0) __hip_atomic_fetch_add((GAS unsigned*)(sync + SY_CT + (CT_ARR + arr) * 128), 1u, __ATOMIC_RELAXED, __HIP_MEMORY_SCOPE_AGENT);
    arr = -1;
  }
  __device__ __forceinline__ bool pass(char* sync, u32x4 v = {0u, 0u, 0u, 0u}) { const bool r = look(sync, v); arrive(sync); return r; }
};
__device__ __forceinline__ unsigned ann_val(unsigned t, int s) { return (t << 8) | (unsigned)s; }
__device__ __forceinline__ unsigned die_half() { return ((unsigned)__builtin_amdgcn_s_getreg(20 | (3 << 11)) >> 2) & 1u; }
typedef u64 Sel;
template <class F>
__device__ __forceinline__ void start_wait(char* sync, GAS unsigned* abortw, int lane, bool& dead, int ci, unsigned n, F f) {
  unsigned spins = 0, v;
  const bool m = lane < 4 || ci < 0;
  while (true) {
    v = ld4(mk_rsrc(sync + SY_SET, SY_BYTES - SY_SET), lane < 6 ? CHK + (m ? CT_INIT + (lane & 3) : ci + 32 * (lane & 1)) * 128 : 0x7fffffffu) + 1u;
    asm volatile("" ::: "memory");
    f();
    if (__all(lane > 5 || v >= (m ? 16u : n))) break;
    __builtin_amdgcn_s_sleep(START_SLP);
    if (dead || poll_fail(spins, abortw, lane)) { dead = true; break; }
  }
}
__device__ __forceinline__ __amdgpu_buffer_rsrc_t rsrc_u(char* sync) { return mk_rsrc((void*)sync, SY_SET); }
__device__ __forceinline__ unsigned near_half(unsigned off, u64 sel) { return off + ((unsigned)(sel >> ((off >> 21) & 63)) & 1u) * 8192; }
__device__ __forceinline__ unsigned x_slot(int kind, int s, int h, Sel sel) {
  return kind == 2 ? near_half(32 * PAGE + (s * 32 + h) * CHK, ~sel) : near_half((s * 64 + 2 * h + 1 - kind) * CHK, sel);
}
__device__ __forceinline__ int ct_near(Sel sel) { return 64 * ((unsigned)(sel >> (SY_SET / PAGE)) & 1u); }
__device__ __forceinline__ unsigned kv_row(int d, Sel sel) { return near_half(SY_KV + (d >> 1) * CHK, ~sel) + (d & 1) * 4096; }
__device__ __forceinline__ int hpos(int h) { return (h & 3) * 8 + (h >> 2); }
constexpr unsigned UXH = 8 * CHK;
__device__ __forceinline__ unsigned ux_base(int s, int h, Sel sel) { return near_half(((s & (URING - 1)) * 32 + h) * UXH, ~sel); }
__device__ __forceinline__ constexpr unsigned ux_tile(int t) { return (t >> 3) * CHK + (t & 7) * 1024; }
__device__ __forceinline__ __amdgpu_buffer_rsrc_t rsrc_hot(char* sync, Sel sel) {
  return mk_rsrc((void*)(sync + near_half(SY_HOT, sel)), H_BYTES - 8192);
}
template <int SLP = 4>
__device__ __forceinline__ void ctr_wait(GAS unsigned* c, unsigned need, GAS unsigned* abortw, int lane, bool& dead) {
  unsigned spins = 0;
  while (true) {
    unsigned v = __builtin_amdgcn_readfirstlane(__hip_atomic_load(c, __ATOMIC_RELAXED, __HIP_MEMORY_SCOPE_AGENT)) + 1u;
    if (v >= need) break;
    __builtin_amdgcn_s_sleep(SLP);
    if (dead || poll_fail(spins, abortw, lane)) { dead = true; break; }
  }
}
template <int N> __device__ __forceinline__ void wait_vm() { asm volatile("s_waitcnt vmcnt(%0)" :: "n"(N) : "memory"); }
__device__ __forceinline__ void lds_barrier() {
  asm volatile("s_waitcnt lgkmcnt(0)" ::: "memory");
  __builtin_amdgcn_s_barrier();
  asm volatile("" ::: "memory");
}
__device__ __forceinline__ f32x4 vzero() { float z; asm volatile("v_mov_b32 %0, 0" : "=v"(z)); f32x4 r = {z, z, z, z}; asm volatile("" : "+v"(r)); return r; }
__device__ __forceinline__ f32x4 smf(u32x4 a, u32x8 b, f32x4 c, int idx) {
  return __builtin_amdgcn_smfmac_f32_16x16x64_bf16(__builtin_bit_cast(bfv8, a), __builtin_bit_cast(bfv16, b), c, idx, 0, 0);
}
__device__ __forceinline__ float red16max(float x) { auto r = __builtin_amdgcn_permlane16_swap(__float_as_uint(x), __float_as_uint(x), false, false); return fmaxf(__uint_as_float(r[0]), __uint_as_float(r[1])); }
__device__ __forceinline__ float red32max(float x) { auto r = __builtin_amdgcn_permlane32_swap(__float_as_uint(x), __float_as_uint(x), false, false); return fmaxf(__uint_as_float(r[0]), __uint_as_float(r[1])); }
__device__ __forceinline__ float red16add(float x) { auto r = __builtin_amdgcn_permlane16_swap(__float_as_uint(x), __float_as_uint(x), false, false); return __uint_as_float(r[0]) + __uint_as_float(r[1]); }
__device__ __forceinline__ float red32add(float x) { auto r = __builtin_amdgcn_permlane32_swap(__float_as_uint(x), __float_as_uint(x), false, false); return __uint_as_float(r[0]) + __uint_as_float(r[1]); }
constexpr bool blk_in_lds(int R, int b) {
  return R == 0 ? ((b & 7) == 1 || (b & 7) == 4) : (b < 16 ? (b & 3) == 0 : (b & 3) == 3);
}
constexpr int lds_index(int R, int b) { int n = 0; for (int i = 0; i < b; ++i) if (blk_in_lds(R, i)) ++n; return n; }
constexpr bool frag_in_lds(int R, int f) { return blk_in_lds(R, f >> 1); }
constexpr int frag_reg_slot(int R, int f) { int n = 0; for (int i = 0; i < f; ++i) if (!frag_in_lds(R, i)) ++n; return n; }

constexpr int m_block(int R, int m, int i) {
  return R == 0 ? 2 * (i < 12 ? (m ? i + i / 3 + 1 : i + (i + 1) / 3) : 4 * (i - 12) + 2 - 2 * m) + m : m ? 16 + 4 * (i & 3) + (i >> 2) : (i < 12 ? i + i / 3 + 1 : 4 * (i - 12));
}
#define QK_REV 0
#define W_BV 0xba9876543210fedcull
#define VO_ORD 3
constexpr int w_block(int R, int i) {
  if (R && VO_ORD == 2) return i < 12 ? m_block(1, 0, 11 - i) : m_block(1, 1, i - 12);
  if (R && VO_ORD == 3) return i & 1 ? m_block(1, 1, i >> 1) : m_block(1, 0, 11 - (i >> 1));
  if (!R && QK_REV) return m_block(0, i & 1, 11 - (i >> 1));
  return R && VO_ORD ? m_block(R, i < 12, i % 12) : m_block(R, i & 1, i >> 1);
}
#define W_BG 0xfedcba9876543210ull
#define W_NT 0
#define U_ORD 0x3210
constexpr u64 bg_order(int m) { u64 r = 0; for (int n = 0; n < 16; ++n) r |= (u64)(m_block(0, m > 0, ((m == 2 ? W_BV : W_BG) >> (4 * n)) & 15) >> 1) << (4 * n); return r; }
struct WSched { int ord[32]; int slot[32]; int issue[32]; int out[32]; int reg[32]; };
constexpr WSched make_wsched(int R) {
  WSched S{};
  int n = 0;
  for (int i = 0; i < 24; ++i) S.ord[n++] = w_block(R, i);
  for (int i = 0; i < n; ++i) S.reg[i] = frag_reg_slot(R, 2 * S.ord[i]);
  const int npure = n;
  for (int b = 0; b < 32; ++b) if (blk_in_lds(R, b)) S.ord[n++] = b;
  int seq[32] = {};
  int nissued = 0, next = 0;
  for (int i = 0; i < 10; ++i) { S.slot[i] = (i + 10 - npure % 10) % 10; S.issue[i] = -1; seq[i] = nissued++; }
  next = 10;
  S.out[0] = nissued - 1;
  for (int c = 0; c <= npure; ++c) {
    if (c >= 1) {
      const int k = S.slot[c - 1];
      if (next < npure) { S.slot[next] = k; S.issue[next] = c; seq[next] = nissued++; ++next; }
      else if (k < NLF / 2) { S.slot[npure + k] = k; S.issue[npure + k] = c; seq[npure + k] = nissued++; }
    }
    if (c + 1 < npure) S.out[c + 1] = nissued - 1 - seq[c + 1];
  }
  return S;
}
__device__ __forceinline__ int kcol(int k) { return 8 * ((k >> 1) & 1) + ((k & 1) ? 7 - (k >> 2) : k >> 2); }
template <bool PERM = false>
__device__ __forceinline__ u32x8 tr_frag(const char* sb, int tile, int lane) {
  u32x8 f;
#pragma unroll
  for (int q = 0; q < 4; ++q) {
    int row = (q >> 1) * 32 + 8 * (lane >> 4) + 4 * (q & 1) + ((lane & 15) >> 2);
    if (PERM) row = (row & ~15) + kcol(row & 15);
    int p = lane & 3;
    int lc = 2 * tile + (p >> 1);
    int addr = row * 256 + ((lc ^ swz_b(row)) * 16) + 8 * (p & 1);
    bf16x4 v = __builtin_amdgcn_ds_read_tr16_b64_v4i16((LAS bf16x4*)(sb + addr));
    u32x2 u = __builtin_bit_cast(u32x2, v);
    f[2 * q] = u[0]; f[2 * q + 1] = u[1];
  }
  return f;
}
__device__ __forceinline__ void dma16(__amdgpu_buffer_rsrc_t rs, unsigned voff, unsigned soff, unsigned lds, bool nt = false, bool pad = true) {
  if (nt) asm volatile("s_mov_b32 m0, %0\n\ts_nop 3\n\tbuffer_load_dwordx4 %1, %2, %3 offen nt lds" :: "s"(lds), "v"(voff), "s"(rs), "s"(soff) : "m0", "memory");
  else if (pad) asm volatile("s_mov_b32 m0, %0\n\ts_nop 3\n\tbuffer_load_dwordx4 %1, %2, %3 offen lds" :: "s"(lds), "v"(voff), "s"(rs), "s"(soff) : "m0", "memory");
  else asm volatile("s_mov_b32 m0, %0\n\ts_nop 0\n\tbuffer_load_dwordx4 %1, %2, %3 offen lds" :: "s"(lds), "v"(voff), "s"(rs), "s"(soff) : "m0", "memory");
}
__device__ __forceinline__ u32x8 tr_frag2(const char* sb, int tile, int f, int lane) {
  u32x8 r;
#pragma unroll
  for (int c = 0; c < 2; ++c)
#pragma unroll
    for (int t = 0; t < 2; ++t) {
      const int row = 32 * c + 16 * f + 4 * (lane >> 4) + ((lane & 15) >> 2);
      const int p = lane & 3, lc = 2 * (tile + t) + (p >> 1);
      const u32x2 u = __builtin_bit_cast(u32x2, __builtin_amdgcn_ds_read_tr16_b64_v4i16((LAS bf16x4*)(sb + row * 256 + ((lc ^ swz_b(row)) * 16) + 8 * (p & 1))));
      r[4 * c + t] = u[0]; r[4 * c + 2 + t] = u[1];
    }
  return r;
}
template <int ROLE>
__device__ __forceinline__ void load_weights(const ChainArgs& a, int h, int w, int lane, char* smem, Wts& W, Gate& G, u32x4& gv) {
#pragma unroll
  for (int i = 0; i < 4; ++i) asm volatile("" : "=v"(W.v[i]));
#pragma unroll
  for (int i = 0; i < 32; ++i) asm volatile("" : "=a"(W.a[i]));
  constexpr WSched S = make_wsched(ROLE);
  constexpr int NP = 32 - NLF / 2;
  const __amdgpu_buffer_rsrc_t r0 = mk_rsrc(ROLE == 0 ? a.Wq : a.Wv, 8u << 20), r1 = mk_rsrc(ROLE == 0 ? a.Wk : a.Wo, 8u << 20);
  const unsigned lds0 = (unsigned)(uintptr_t)(LAS char*)smem;
  unsigned vo[2][4];
#pragma unroll
  for (int j = 0; j < 4; ++j) {
    const int row = (w * 4 + j) * 4 + (lane >> 4), lc = (lane & 15) ^ swz_b(row);
    vo[0][j] = row * HD * 2 + lc * 16; vo[1][j] = row * DM * 2 + lc * 16;
  }
  asm volatile("s_nop 4");
  auto stage = [&](int i) {
    const int blk = S.ord[i];
    const bool second = ROLE == 0 ? (blk & 1) : blk >= 16, wo = ROLE == 1 && second;
    const unsigned so = wo ? (unsigned)(((128 * h + 64 * (blk & 1)) * DM + 128 * (((blk - 16) >> 1) ^ (2 * (h & 3)))) * 2)
                           : (unsigned)((64 * ((ROLE == 0 ? blk >> 1 : blk) ^ (h & RHO)) * HD + 128 * h) * 2);
#pragma unroll
    for (int j = 0; j < 4; ++j) dma16(second ? r1 : r0, vo[wo][j], so, lds0 + 16384 * S.slot[i] + (w * 4 + j) * 1024, W_NT & (ROLE ? 8 : 1));
  };
#pragma unroll
  for (int i = 0; i < 32; ++i) if (S.issue[i] == -1) stage(i);
#pragma unroll
  for (int c = 0; c < NP; ++c) {
    asm volatile("s_waitcnt vmcnt(%0)" :: "n"(4 * S.out[c]) : "memory");
    lds_barrier();
#pragma unroll
    for (int i = 0; i < 32; ++i) if (S.issue[i] == c) stage(i);
    const char* sb = smem + 16384 * S.slot[c];
#pragma unroll
    for (int t = 0; t < 2; ++t) {
      u32x8 f = (ROLE == 1 && S.ord[c] >= 16) ? tr_frag2(sb, 2 * w, t, lane) : tr_frag<true>(sb, 2 * w + t, lane);
      if (S.reg[c] < 32) {
#pragma unroll
        for (int i = 0; i < 8; ++i) asm volatile("v_accvgpr_write_b32 a[%1], %0" :: "v"(f[i]), "n"(8 * ((S.reg[c] + t) & 31) + i));
      }
      else {
#pragma unroll
        for (int i = 0; i < 8; ++i) W.v[(S.reg[c] >> 2) & 3][8 * ((S.reg[c] + t) & 3) + i] = f[i];
      }
    }
    if (S.reg[c] >= 32) asm volatile("" : "+v"(W.v[(S.reg[c] >> 2) & 3]));
  }
  static_assert(S.issue[31] < NP && S.issue[NP] < S.issue[31], "resident blocks are issued in slot order inside the loop");
  G.ask(a.sync, gv);
#pragma unroll
  for (int j = 0; j < NLF / 2; ++j) {
    asm volatile("s_waitcnt vmcnt(%0)" :: "n"(4 * (NLF / 2 - 1 - j) + 1) : "memory");
    lds_barrier();
    const bool two = ROLE == 1 && S.ord[NP + j] >= 16;
    u32x8 f0 = two ? tr_frag2(smem + 16384 * j, 2 * w, 0, lane) : tr_frag<true>(smem + 16384 * j, 2 * w, lane);
    u32x8 f1 = two ? tr_frag2(smem + 16384 * j, 2 * w, 1, lane) : tr_frag<true>(smem + 16384 * j, 2 * w + 1, lane);
    lds_barrier();
    char* d = smem + 16384 * j + w * 4096 + lane * 16;
    *(u32x4*)d = (u32x4){f0[0], f0[1], f0[2], f0[3]};
    *(u32x4*)(d + 1024) = (u32x4){f0[4], f0[5], f0[6], f0[7]};
    *(u32x4*)(d + 2048) = (u32x4){f1[0], f1[1], f1[2], f1[3]};
    *(u32x4*)(d + 3072) = (u32x4){f1[4], f1[5], f1[6], f1[7]};
  }
  asm volatile("s_waitcnt vmcnt(0)" : "+v"(gv) :: "memory");
  lds_barrier();
}
template <int... Is> struct iseq {};
template <int N, int... Is> struct mkseq : mkseq<N - 1, N - 1, Is...> {};
template <int... Is> struct mkseq<0, Is...> { typedef iseq<Is...> type; };

#define SB0 __builtin_amdgcn_sched_barrier(0)
#define CAT8(x, y) ((u32x8){x[0], x[1], x[2], x[3], y[0], y[1], y[2], y[3]})
template <class F, int... I>
__device__ __forceinline__ void for_slots(iseq<I...>, F f) { (f(std::integral_constant<int, I>{}), ...); }
#define QK_BODY \
  "s_cmp_eq_u32 %[wv], 1\n\t" \
  "s_cbranch_scc1 91f\n\t" \
  "s_cmp_eq_u32 %[wv], 2\n\t" \
  "s_cbranch_scc1 92f\n\t" \
  "s_cmp_eq_u32 %[wv], 3\n\t" \
  "s_cbranch_scc1 93f\n\t" \
  "90:\n\t" \
  "s_mov_b32 s58, s42\n\t" \
  "20:\n\t" \
  "s_and_b32 s76, s44, 7\n\t" \
  "s_lshl_b32 s76, s76, 15\n\t" \
  "s_add_i32 s40, s54, s76\n\t" \
  "s_and_b32 s76, s44, 3\n\t" \
  "s_lshl_b32 s45, s76, 14\n\t" \
  "s_mul_i32 s47, s76, 0xc000\n\t" \
  "s_add_i32 s47, s47, 0x40000\n\t" \
  "s_add_i32 s49, s44, -1\n\t" \
  "s_and_b32 s48, s49, 2\n\t" \
  "s_lshl_b32 s48, s48, 13\n\t" \
  "s_and_b32 s76, s49, 1\n\t" \
  "s_lshl_b32 s76, s76, 12\n\t" \
  "s_add_i32 s48, s48, s76\n\t" \
  "s_lshl_b32 s50, s53, 8\n\t" \
  "s_mov_b32 s59, 0\n\t" \
  "s_mov_b32 s92, s51\n\t" \
  "s_mov_b32 s93, s53\n\t" \
  "s_xor_b32 s76, s40, 0x400\n\t" \
  "s_xor_b32 s77, s40, 0x800\n\t" \
  "s_xor_b32 s78, s40, 0xc00\n\t" \
  "s_cmp_lg_u32 s58, 0\n\t" \
  "s_cbranch_scc1 11f\n\t" \
  "10:\n\t" \
  "ds_read_b128 v[48:51], v102 offset:0\n\t" \
  "ds_read_b128 v[52:55], v102 offset:1024\n\t" \
  "ds_read_b128 v[56:59], v102 offset:2048\n\t" \
  "ds_read_b128 v[60:63], v102 offset:3072\n\t" \
  "ds_read_b128 v[16:19], v102 offset:16384\n\t" \
  "ds_read_b128 v[20:23], v102 offset:17408\n\t" \
  "ds_read_b128 v[24:27], v102 offset:18432\n\t" \
  "ds_read_b128 v[28:31], v102 offset:19456\n\t" \
  "ds_read_b128 v[84:87], v102 offset:49152\n\t" \
  "ds_read_b128 v[88:91], v102 offset:50176\n\t" \
  "ds_read_b128 v[92:95], v102 offset:51200\n\t" \
  "ds_read_b128 v[96:99], v102 offset:52224\n\t" \
  "s_waitcnt lgkmcnt(4)\n\t" \
  "ds_read_b128 v[0:3], v103 offset:16384\n\t" \
  "ds_read_b128 v[4:7], v103 offset:17408\n\t" \
  "ds_read_b128 v[8:11], v103 offset:18432\n\t" \
  "ds_read_b128 v[12:15], v103 offset:19456\n\t" \
  "46:\n\t" \
  "s_bitcmp1_b32 s58, 1\n\t" \
  "s_cselect_b64 s[80:81], -1, 0\n\t" \
  "s_cmp_lg_u32 s58, 0\n\t" \
  "s_cbranch_scc1 17f\n\t" \
  "s_mov_b64 exec, 0xff\n\t" \
  "16:\n\t" \
  "s_memtime vcc\n\t" \
  "s_waitcnt lgkmcnt(0)\n\t" \
  "s_sub_i32 s79, s41, vcc_lo\n\t" \
  "s_cmp_gt_i32 s79, 64\n\t" \
  "s_cbranch_scc1 16b\n\t" \
  "13:\n\t" \
  "buffer_load_dword v116, v112, %[rh], s48 offen sc1\n\t" \
  "s_memtime vcc\n\t" \
  "s_memrealtime s[82:83]\n\t" \
  "s_nop 4\n\t" \
  "s_memrealtime s[84:85]\n\t" \
  "s_nop 4\n\t" \
  "s_memrealtime s[56:57]\n\t" \
  "s_nop 4\n\t" \
  "s_memrealtime s[86:87]\n\t" \
  "s_nop 4\n\t" \
  "s_memrealtime s[88:89]\n\t" \
  "s_nop 4\n\t" \
  "s_memrealtime s[90:91]\n\t" \
  "s_mov_b32 s92, 0\n\t" \
  "s_waitcnt lgkmcnt(0)\n\t" \
  "s_cmp_eq_u32 s82, s84\n\t" \
  "s_addc_u32 s92, s92, 0\n\t" \
  "s_cmp_eq_u32 s82, s56\n\t" \
  "s_addc_u32 s92, s92, 0\n\t" \
  "s_cmp_eq_u32 s82, s86\n\t" \
  "s_addc_u32 s92, s92, 0\n\t" \
  "s_cmp_eq_u32 s82, s88\n\t" \
  "s_addc_u32 s92, s92, 0\n\t" \
  "s_cmp_eq_u32 s82, s90\n\t" \
  "s_addc_u32 s92, s92, 0\n\t" \
  "s_mul_i32 s92, s92, 24\n\t" \
  "s_add_i32 s92, s92, vcc_lo\n\t" \
  "s_lshl4_add_u32 s93, s82, 64\n\t" \
  "s_lshl_b32 s50, s93, 8\n\t" \
  "s_waitcnt vmcnt(0)\n\t" \
  "15:\n\t" \
  "v_mov_b32 v117, 20000\n\t" \
  "v_cmp_ne_u32_sdwa vcc, v116, s49 src0_sel:BYTE_0 src1_sel:DWORD\n\t" \
  "v_subrev_u32 v116, s50, v116\n\t" \
  "v_ashrrev_i32 v116, 8, v116\n\t" \
  "s_nop 1\n\t" \
  "v_max_i32_dpp v116, v116, v116 quad_perm:[1,0,3,2] row_mask:0xf bank_mask:0xf\n\t" \
  "s_cbranch_vccnz 14f\n\t" \
  "s_nop 0\n\t" \
  "v_max_i32_dpp v116, v116, v116 quad_perm:[2,3,0,1] row_mask:0xf bank_mask:0xf\n\t" \
  "s_nop 1\n\t" \
  "v_max_i32_dpp v116, v116, v116 row_half_mirror row_mask:0xf bank_mask:0xf\n\t" \
  "v_med3_i32 v116, v116, 0, v117\n\t" \
  "v_mul_u32_u24 v116, s52, v116\n\t" \
  "v_lshrrev_b32 v116, 16, v116\n\t" \
  "s_mov_b64 exec, -1\n\t" \
  "v_readfirstlane_b32 s84, v116\n\t" \
  "s_add_i32 s84, s84, s92\n\t" \
  ".p2align 6\n\t" \
  "s_nop 0\n\t" \
  "s_nop 0\n\t" \
  "s_nop 0\n\t" \
  "s_nop 0\n\t" \
  "s_nop 0\n\t" \
  "s_nop 0\n\t" \
  "s_nop 0\n\t" \
  "s_nop 0\n\t" \
  "1:\n\t" \
  "s_memtime vcc\n\t" \
  "s_waitcnt lgkmcnt(0)\n\t" \
  "s_sub_i32 s79, s84, vcc_lo\n\t" \
  "s_cmp_gt_i32 s79, 128\n\t" \
  "s_cbranch_scc1 1b\n\t" \
  ".p2align 4\n\t" \
  "2:\n\t" \
  "s_sub_i32 s79, s79, 33\n\t" \
  "s_cmp_gt_i32 s79, 56\n\t" \
  "s_cbranch_scc1 2b\n\t" \
  "buffer_load_dwordx4 v[32:35], v100, %[rh], s40 offen sc1\n\t" \
  "s_nop 12\n\t" \
  "buffer_load_dwordx4 v[36:39], v100, %[rh], s76 offen sc1\n\t" \
  "s_nop 12\n\t" \
  "buffer_load_dwordx4 v[40:43], v100, %[rh], s77 offen sc1\n\t" \
  "s_nop 12\n\t" \
  "buffer_load_dwordx4 v[44:47], v100, %[rh], s78 offen sc1\n\t" \
  "s_memtime vcc\n\t" \
  "s_memrealtime s[82:83]\n\t" \
  "s_nop 4\n\t" \
  "s_memrealtime s[84:85]\n\t" \
  "s_nop 4\n\t" \
  "s_memrealtime s[56:57]\n\t" \
  "s_nop 4\n\t" \
  "s_memrealtime s[86:87]\n\t" \
  "s_nop 4\n\t" \
  "s_memrealtime s[88:89]\n\t" \
  "s_nop 4\n\t" \
  "s_memrealtime s[90:91]\n\t" \
  "s_mov_b32 s92, 0\n\t" \
  "s_waitcnt lgkmcnt(0)\n\t" \
  "s_cmp_eq_u32 s82, s84\n\t" \
  "s_addc_u32 s92, s92, 0\n\t" \
  "s_cmp_eq_u32 s82, s56\n\t" \
  "s_addc_u32 s92, s92, 0\n\t" \
  "s_cmp_eq_u32 s82, s86\n\t" \
  "s_addc_u32 s92, s92, 0\n\t" \
  "s_cmp_eq_u32 s82, s88\n\t" \
  "s_addc_u32 s92, s92, 0\n\t" \
  "s_cmp_eq_u32 s82, s90\n\t" \
  "s_addc_u32 s92, s92, 0\n\t" \
  "s_mul_i32 s92, s92, 24\n\t" \
  "s_add_i32 s92, s92, vcc_lo\n\t" \
  "s_lshl4_add_u32 s93, s82, 64\n\t" \
  "3:\n\t" \
  "s_sub_i32 s84, s92, s43\n\t" \
  "s_lshl_b32 s85, s93, 8\n\t" \
  "s_or_b32 s85, s85, s44\n\t" \
  "v_mov_b32 v118, s85\n\t" \
  "v_mov_b64 v[68:69], 0\n\t" \
  "v_mov_b64 v[70:71], 0\n\t" \
  "v_mov_b64 v[72:73], 0\n\t" \
  "v_mov_b64 v[74:75], 0\n\t" \
  "v_mov_b64 v[76:77], 0\n\t" \
  "v_mov_b64 v[78:79], 0\n\t" \
  "v_mov_b64 v[80:81], 0\n\t" \
  "v_mov_b64 v[82:83], 0\n\t" \
  "s_waitcnt lgkmcnt(0)\n\t" \
  "s_waitcnt vmcnt(3)\n\t" \
  "v_smfmac_f32_16x16x64_bf16 v[68:71], v[32:35], a[0:7], v104\n\t" \
  "ds_write_b128 v101, v[32:35] offset:0\n\t" \
  "v_smfmac_f32_16x16x64_bf16 v[72:75], v[32:35], a[8:15], v104\n\t" \
  "v_smfmac_f32_16x16x64_bf16 v[76:79], v[32:35], v[48:55], v104\n\t" \
  "v_smfmac_f32_16x16x64_bf16 v[80:83], v[32:35], v[56:63], v104\n\t" \
  "s_waitcnt vmcnt(2)\n\t" \
  "v_smfmac_f32_16x16x64_bf16 v[68:71], v[36:39], a[16:23], v104\n\t" \
  "ds_write_b128 v101, v[36:39] offset:1024\n\t" \
  "v_smfmac_f32_16x16x64_bf16 v[72:75], v[36:39], a[24:31], v104\n\t" \
  "v_smfmac_f32_16x16x64_bf16 v[76:79], v[36:39], a[32:39], v104\n\t" \
  "v_cmp_u_f32 vcc, v68, v69\n\t" \
  "v_smfmac_f32_16x16x64_bf16 v[80:83], v[36:39], a[40:47], v104\n\t" \
  "v_cmp_u_f32 s[82:83], v70, v71\n\t" \
  "s_waitcnt vmcnt(1)\n\t" \
  "v_smfmac_f32_16x16x64_bf16 v[68:71], v[40:43], v[16:23], v104\n\t" \
  "ds_write_b128 v101, v[40:43] offset:2048\n\t" \
  "v_smfmac_f32_16x16x64_bf16 v[72:75], v[40:43], v[24:31], v104\n\t" \
  "s_or_b64 vcc, vcc, s[82:83]\n\t" \
  "s_or_b64 vcc, vcc, s[80:81]\n\t" \
  "s_waitcnt lgkmcnt(1)\n\t" \
  "v_smfmac_f32_16x16x64_bf16 v[76:79], v[40:43], a[48:55], v104\n\t" \
  "s_cbranch_scc1 8f\n\t" \
  "5:\n\t" \
  "s_barrier\n\t" \
  "4:\n\t" \
  "s_memtime s[56:57]\n\t" \
  "ds_read_b128 v[48:51], v101 offset:4096\n\t" \
  "v_smfmac_f32_16x16x64_bf16 v[80:83], v[40:43], a[56:63], v104\n\t" \
  "ds_read_b128 v[52:55], v101 offset:5120\n\t" \
  "ds_read_b128 v[56:59], v101 offset:8192\n\t" \
  "s_waitcnt vmcnt(0)\n\t" \
  "v_smfmac_f32_16x16x64_bf16 v[68:71], v[44:47], a[64:71], v104\n\t" \
  "ds_write_b128 v101, v[44:47] offset:3072\n\t" \
  "v_smfmac_f32_16x16x64_bf16 v[72:75], v[44:47], a[72:79], v104\n\t" \
  "v_smfmac_f32_16x16x64_bf16 v[76:79], v[44:47], a[80:87], v104\n\t" \
  "v_cmp_u_f32 vcc, v68, v69\n\t" \
  "v_smfmac_f32_16x16x64_bf16 v[80:83], v[44:47], a[88:95], v104\n\t" \
  "v_cmp_u_f32 s[82:83], v70, v71\n\t" \
  "s_waitcnt lgkmcnt(3)\n\t" \
  "v_smfmac_f32_16x16x64_bf16 v[68:71], v[48:51], a[96:103], v104\n\t" \
  "v_smfmac_f32_16x16x64_bf16 v[72:75], v[48:51], a[104:111], v104\n\t" \
  "s_or_b64 vcc, vcc, s[82:83]\n\t" \
  "s_waitcnt lgkmcnt(0)\n\t" \
  "v_smfmac_f32_16x16x64_bf16 v[68:71], v[52:55], a[112:119], v104\n\t" \
  "s_cbranch_scc1 9f\n\t" \
  "6:\n\t" \
  "s_barrier\n\t" \
  "ds_read_b128 v[60:63], v101 offset:9216\n\t" \
  "v_smfmac_f32_16x16x64_bf16 v[72:75], v[52:55], a[120:127], v104\n\t" \
  "ds_read_b128 v[64:67], v101 offset:12288\n\t" \
  "s_sub_i32 s79, s56, s84\n\t" \
  "s_mul_hi_u32 s79, s79, s46\n\t" \
  "v_smfmac_f32_16x16x64_bf16 v[68:71], v[56:59], a[192:199], v104\n\t" \
  "v_lshl_add_u32 v116, s79, 9, v118\n\t" \
  "buffer_store_dword v116, v109, %[rh], s45 offen sc1\n\t" \
  "v_smfmac_f32_16x16x64_bf16 v[72:75], v[56:59], a[200:207], v104\n\t" \
  "ds_read_b128 v[48:51], v101 offset:13312\n\t" \
  "s_waitcnt lgkmcnt(2)\n\t" \
  "v_smfmac_f32_16x16x64_bf16 v[68:71], v[60:63], a[208:215], v104\n\t" \
  "ds_read_b128 v[52:55], v101 offset:6144\n\t" \
  "v_smfmac_f32_16x16x64_bf16 v[72:75], v[60:63], a[216:223], v104\n\t" \
  "ds_read_b128 v[56:59], v101 offset:7168\n\t" \
  "s_waitcnt lgkmcnt(3)\n\t" \
  "v_smfmac_f32_16x16x64_bf16 v[68:71], v[64:67], v[152:159], v104\n\t" \
  "ds_read_b128 v[60:63], v101 offset:10240\n\t" \
  "v_smfmac_f32_16x16x64_bf16 v[72:75], v[64:67], v[160:167], v104\n\t" \
  "ds_read_b128 v[32:35], v101 offset:20480\n\t" \
  "s_waitcnt lgkmcnt(4)\n\t" \
  "v_smfmac_f32_16x16x64_bf16 v[68:71], v[48:51], v[168:175], v104\n\t" \
  "ds_read_b128 v[64:67], v101 offset:11264\n\t" \
  "ds_read_b128 v[16:19], v103 offset:49152\n\t" \
  "v_smfmac_f32_16x16x64_bf16 v[72:75], v[48:51], v[176:183], v104\n\t" \
  "ds_read_b128 v[36:39], v101 offset:21504\n\t" \
  "s_waitcnt lgkmcnt(6)\n\t" \
  "v_smfmac_f32_16x16x64_bf16 v[68:71], v[52:55], v[84:91], v104\n\t" \
  "ds_read_b128 v[48:51], v101 offset:15360\n\t" \
  "ds_read_b128 v[20:23], v103 offset:50176\n\t" \
  "v_smfmac_f32_16x16x64_bf16 v[72:75], v[52:55], v[92:99], v104\n\t" \
  "ds_read_b128 v[40:43], v101 offset:22528\n\t" \
  "s_waitcnt lgkmcnt(8)\n\t" \
  "v_smfmac_f32_16x16x64_bf16 v[68:71], v[56:59], a[160:167], v104\n\t" \
  "ds_read_b128 v[52:55], v101 offset:14336\n\t" \
  "ds_read_b128 v[24:27], v103 offset:51200\n\t" \
  "v_smfmac_f32_16x16x64_bf16 v[72:75], v[56:59], a[168:175], v104\n\t" \
  "ds_read_b128 v[28:31], v103 offset:52224\n\t" \
  "s_waitcnt lgkmcnt(10)\n\t" \
  "v_smfmac_f32_16x16x64_bf16 v[68:71], v[60:63], v[0:7], v104\n\t" \
  "ds_read_b128 v[44:47], v101 offset:23552\n\t" \
  "v_smfmac_f32_16x16x64_bf16 v[72:75], v[60:63], v[8:15], v104\n\t" \
  "v_mov_b64 v[92:93], 0\n\t" \
  "s_waitcnt lgkmcnt(9)\n\t" \
  "v_smfmac_f32_16x16x64_bf16 v[68:71], v[64:67], v[120:127], v104\n\t" \
  "ds_read_b128 v[0:3], v101 offset:24576\n\t" \
  "v_smfmac_f32_16x16x64_bf16 v[72:75], v[64:67], v[128:135], v104\n\t" \
  "ds_read_b128 v[4:7], v101 offset:25600\n\t" \
  "s_waitcnt lgkmcnt(8)\n\t" \
  "v_smfmac_f32_16x16x64_bf16 v[68:71], v[48:51], v[216:223], v104\n\t" \
  "v_mov_b64 v[94:95], 0\n\t" \
  "s_waitcnt lgkmcnt(5)\n\t" \
  "v_smfmac_f32_16x16x64_bf16 v[68:71], v[52:55], v[16:23], v104\n\t" \
  "ds_read_b128 v[8:11], v101 offset:26624\n\t" \
  "v_smfmac_f32_16x16x64_bf16 v[72:75], v[48:51], v[224:231], v104\n\t" \
  "ds_read_b128 v[12:15], v101 offset:27648\n\t" \
  "s_waitcnt lgkmcnt(5)\n\t" \
  "v_smfmac_f32_16x16x64_bf16 v[72:75], v[52:55], v[24:31], v104\n\t" \
  "v_add_f32 v96, v68, v70\n\t" \
  "v_smfmac_f32_16x16x64_bf16 v[76:79], v[56:59], a[176:183], v104\n\t" \
  "v_add_f32 v97, v69, v71\n\t" \
  "v_cvt_pk_bf16_f32 v96, v96, v97\n\t" \
  "v_smfmac_f32_16x16x64_bf16 v[80:83], v[56:59], a[184:191], v104\n\t" \
  "v_add_f32 v98, v72, v74\n\t" \
  "v_add_f32 v99, v73, v75\n\t" \
  "v_smfmac_f32_16x16x64_bf16 v[76:79], v[60:63], a[240:247], v104\n\t" \
  "v_cvt_pk_bf16_f32 v98, v98, v99\n\t" \
  "ds_write2st64_b32 v105, v96, v98 offset1:1\n\t" \
  "v_smfmac_f32_16x16x64_bf16 v[80:83], v[60:63], a[248:255], v104\n\t" \
  "ds_read_b128 v[56:59], v101 offset:9216\n\t" \
  "v_mov_b64 v[68:69], 0\n\t" \
  "v_smfmac_f32_16x16x64_bf16 v[76:79], v[64:67], v[136:143], v104\n\t" \
  "ds_read_b128 v[60:63], v101 offset:13312\n\t" \
  "v_mov_b64 v[70:71], 0\n\t" \
  "v_smfmac_f32_16x16x64_bf16 v[80:83], v[64:67], v[144:151], v104\n\t" \
  "v_smfmac_f32_16x16x64_bf16 v[76:79], v[48:51], v[232:239], v104\n\t" \
  "s_waitcnt lgkmcnt(2)\n\t" \
  "s_barrier\n\t" \
  "ds_read_b64_tr_b16 v[84:85], v107 offset:0\n\t" \
  "v_smfmac_f32_16x16x64_bf16 v[80:83], v[48:51], v[240:247], v104\n\t" \
  "ds_read_b64_tr_b16 v[86:87], v107 offset:128\n\t" \
  "ds_read_b64_tr_b16 v[88:89], v107 offset:1024\n\t" \
  "v_smfmac_f32_16x16x64_bf16 v[76:79], v[52:55], v[200:207], v104\n\t" \
  "ds_read_b64_tr_b16 v[90:91], v107 offset:1152\n\t" \
  "v_smfmac_f32_16x16x64_bf16 v[80:83], v[52:55], v[208:215], v104\n\t" \
  "ds_read_b128 v[64:67], v101 offset:6144\n\t" \
  "s_waitcnt lgkmcnt(5)\n\t" \
  "v_smfmac_f32_16x16x64_bf16 v[76:79], v[60:63], v[184:191], v104\n\t" \
  "ds_read_b128 v[52:55], v101 offset:5120\n\t" \
  "s_waitcnt lgkmcnt(4)\n\t" \
  "v_smfmac_f32_16x16x64_bf16 v[92:95], v[84:87], v[32:39], v104\n\t" \
  "s_waitcnt lgkmcnt(2)\n\t" \
  "v_smfmac_f32_16x16x64_bf16 v[92:95], v[88:91], v[40:47], v104\n\t" \
  "v_smfmac_f32_16x16x64_bf16 v[68:71], v[84:87], v[0:7], v104\n\t" \
  "v_smfmac_f32_16x16x64_bf16 v[68:71], v[88:91], v[8:15], v104\n\t" \
  "v_add_f32 v92, v92, v94\n\t" \
  "v_add_f32 v94, v93, v95\n\t" \
  "v_smfmac_f32_16x16x64_bf16 v[80:83], v[60:63], v[192:199], v104\n\t" \
  "v_mov_b32 v97, v94\n\t" \
  "v_mov_b32_dpp v94, v92 row_ror:8 row_mask:0xf bank_mask:0x3\n\t" \
  "s_nop 0\n\t" \
  "v_mov_b32_dpp v92, v97 row_ror:8 row_mask:0xf bank_mask:0xc\n\t" \
  "s_waitcnt lgkmcnt(1)\n\t" \
  "v_smfmac_f32_16x16x64_bf16 v[76:79], v[64:67], a[144:151], v104\n\t" \
  "v_add_f32 v93, v68, v70\n\t" \
  "v_add_f32 v95, v69, v71\n\t" \
  "v_max_f32 v98, v92, v94\n\t" \
  "v_mov_b32 v97, v95\n\t" \
  "v_mov_b32_dpp v95, v93 row_ror:8 row_mask:0xf bank_mask:0x3\n\t" \
  "s_nop 0\n\t" \
  "v_mov_b32_dpp v93, v97 row_ror:8 row_mask:0xf bank_mask:0xc\n\t" \
  "v_max3_f32 v98, v98, v93, v95\n\t" \
  "v_smfmac_f32_16x16x64_bf16 v[80:83], v[64:67], a[152:159], v104\n\t" \
  "s_nop 0\n\t" \
  "v_max_f32_dpp v98, v98, v98 quad_perm:[1,0,3,2] row_mask:0xf bank_mask:0xf\n\t" \
  "s_waitcnt lgkmcnt(0)\n\t" \
  "v_smfmac_f32_16x16x64_bf16 v[76:79], v[52:55], a[128:135], v104\n\t" \
  "v_max_f32_dpp v98, v98, v98 quad_perm:[2,3,0,1] row_mask:0xf bank_mask:0xf\n\t" \
  "v_smfmac_f32_16x16x64_bf16 v[76:79], v[56:59], a[224:231], v104\n\t" \
  "s_nop 0\n\t" \
  "v_max_f32_dpp v98, v98, v98 row_half_mirror row_mask:0xf bank_mask:0xf\n\t" \
  "v_mul_f32 v98, %[cs], v98\n\t" \
  "v_fma_f32 v92, v92, %[cs], -v98\n\t" \
  "v_fma_f32 v93, v93, %[cs], -v98\n\t" \
  "v_fma_f32 v94, v94, %[cs], -v98\n\t" \
  "v_fma_f32 v95, v95, %[cs], -v98\n\t" \
  "v_exp_f32 v72, v92\n\t" \
  "v_exp_f32 v73, v93\n\t" \
  "v_exp_f32 v74, v94\n\t" \
  "v_exp_f32 v75, v95\n\t" \
  "v_cvt_pk_bf16_f32 v68, v72, v73\n\t" \
  "v_cvt_pk_bf16_f32 v69, v74, v75\n\t" \
  "buffer_store_dwordx2 v[68:69], v110, %[rh], s47 offen sc1\n\t" \
  "v_smfmac_f32_16x16x64_bf16 v[80:83], v[52:55], a[136:143], v104\n\t" \
  "v_smfmac_f32_16x16x64_bf16 v[80:83], v[56:59], a[232:239], v104\n\t" \
  "v_add_f32 v20, v76, v78\n\t" \
  "v_add_f32 v21, v77, v79\n\t" \
  "v_cvt_pk_bf16_f32 v20, v20, v21\n\t" \
  "ds_write_b32 v106, v20\n\t" \
  "s_nop 0\n\t" \
  "s_nop 0\n\t" \
  "s_nop 0\n\t" \
  "s_nop 0\n\t" \
  "v_add_f32 v23, v80, v82\n\t" \
  "v_add_f32 v21, v81, v83\n\t" \
  "v_cvt_pk_bf16_f32 v21, v23, v21\n\t" \
  "ds_write_b32 v106, v21 offset:256\n\t" \
  "v_mov_b64 v[0:1], 0\n\t" \
  "v_mov_b64 v[2:3], 0\n\t" \
  "v_mov_b64 v[36:37], 0\n\t" \
  "v_mov_b64 v[38:39], 0\n\t" \
  "s_waitcnt lgkmcnt(0)\n\t" \
  "s_barrier\n\t" \
  "ds_read_b64_tr_b16 v[32:33], v108 offset:12288\n\t" \
  "ds_read_b64_tr_b16 v[34:35], v108 offset:12352\n\t" \
  "ds_read_b64_tr_b16 v[4:5], v108\n\t" \
  "ds_read_b64_tr_b16 v[6:7], v108 offset:64\n\t" \
  "ds_read_b64_tr_b16 v[8:9], v108 offset:512\n\t" \
  "ds_read_b64_tr_b16 v[10:11], v108 offset:576\n\t" \
  "ds_read_b64_tr_b16 v[12:13], v108 offset:1024\n\t" \
  "ds_read_b64_tr_b16 v[14:15], v108 offset:1088\n\t" \
  "ds_read_b64_tr_b16 v[16:17], v108 offset:1536\n\t" \
  "ds_read_b64_tr_b16 v[18:19], v108 offset:1600\n\t" \
  "v_add_f32 v29, v72, v73\n\t" \
  "v_add_f32 v29, v29, v74\n\t" \
  "s_waitcnt lgkmcnt(8)\n\t" \
  "v_smfmac_f32_16x16x64_bf16 v[0:3], v[84:87], v[32:39], v104\n\t" \
  "s_waitcnt lgkmcnt(4)\n\t" \
  "v_smfmac_f32_16x16x64_bf16 v[0:3], v[84:87], v[4:11], v104\n\t" \
  "s_waitcnt lgkmcnt(0)\n\t" \
  "v_smfmac_f32_16x16x64_bf16 v[0:3], v[88:91], v[12:19], v104\n\t" \
  "v_add_f32 v29, v29, v75\n\t" \
  "s_mov_b32 s76, 0xaaaaaaaa\n\t" \
  "s_mov_b32 s77, s76\n\t" \
  "s_nop 4\n\t" \
  "v_add_f32 v96, v0, v2\n\t" \
  "v_add_f32_dpp v96, v1, v3 quad_perm:[0,1,2,3] row_mask:0xf bank_mask:0xc\n\t" \
  "v_fma_f32 v96, v96, %[cs], -v98\n\t" \
  "v_exp_f32 v24, v96\n\t" \
  "s_nop 0\n\t" \
  "v_add_f32 v29, v29, v24\n\t" \
  "s_nop 0\n\t" \
  "v_mov_b32_dpp v25, v24 quad_perm:[1,0,3,2] row_mask:0xf bank_mask:0xf\n\t" \
  "v_add_f32_dpp v29, v29, v29 quad_perm:[1,0,3,2] row_mask:0xf bank_mask:0xf\n\t" \
  "v_cndmask_b32_e64 v24, v24, v114, s[76:77]\n\t" \
  "v_cndmask_b32_e64 v25, v25, v115, s[76:77]\n\t" \
  "v_add_f32_dpp v29, v29, v29 quad_perm:[2,3,0,1] row_mask:0xf bank_mask:0xf\n\t" \
  "s_nop 1\n\t" \
  "v_add_f32_dpp v29, v29, v29 row_half_mirror row_mask:0xf bank_mask:0xf\n\t" \
  "v_rcp_f32 v26, v29\n\t" \
  "v_cmpx_gt_f32 vcc, 0x5d5e0b6b, v29\n\t" \
  "v_mul_f32 v24, v24, v26\n\t" \
  "v_mul_f32 v25, v25, v26\n\t" \
  "v_cvt_pk_bf16_f32 v24, v24, v25\n\t" \
  "buffer_store_dword v24, v111, %[rh], s47 offen sc1\n\t" \
  "s_memtime s[76:77]\n\t" \
  "s_mov_b64 exec, -1\n\t" \
  "v_mov_b32 v97, v29\n\t" \
  "s_cmp_eq_u64 vcc, -1\n\t" \
  "s_cselect_b32 s42, 0, 8\n\t" \
  "s_waitcnt lgkmcnt(0)\n\t" \
  "s_mov_b32 s57, s76\n\t" \
  "s_barrier\n\t" \
  "s_add_i32 s41, s56, 1950\n\t" \
  "s_sub_i32 s76, s57, s56\n\t" \
  "s_add_i32 s77, s43, 8\n\t" \
  "s_min_u32 s76, s76, s77\n\t" \
  "s_sub_i32 s43, s43, 4\n\t" \
  "s_max_u32 s43, s43, s76\n\t" \
  "s_add_i32 s76, s45, 0x4000\n\t" \
  "s_and_b32 s76, s76, 0xc000\n\t" \
  "v_mov_b32 v116, -1\n\t" \
  "buffer_store_dword v116, v109, %[rh], s76 offen sc1\n\t" \
  "s_sub_i32 s76, s92, s51\n\t" \
  "s_sub_i32 s77, s93, s53\n\t" \
  "s_cmp_eq_u32 s76, 0\n\t" \
  "s_cbranch_scc1 21f\n\t" \
  "v_cvt_f32_u32 v117, s76\n\t" \
  "v_cvt_f32_u32 v116, s77\n\t" \
  "v_rcp_f32 v117, v117\n\t" \
  "s_mov_b32 s51, s92\n\t" \
  "v_mul_f32 v117, v117, v116\n\t" \
  "v_max_f32 v117, 0x3f028f5c, v117\n\t" \
  "v_min_f32 v117, 0x3ffeb852, v117\n\t" \
  "v_rcp_f32 v116, v117\n\t" \
  "v_mul_f32 v117, 0x4f000000, v117\n\t" \
  "v_mul_f32 v116, 0x47800000, v116\n\t" \
  "v_cvt_u32_f32 v117, v117\n\t" \
  "v_cvt_u32_f32 v116, v116\n\t" \
  "s_mov_b32 s53, s93\n\t" \
  "v_readfirstlane_b32 s46, v117\n\t" \
  "v_readfirstlane_b32 s52, v116\n\t" \
  "21:\n\t" \
  "s_add_i32 s44, s44, 1\n\t" \
  "s_and_b32 s58, s58, 4\n\t" \
  "s_and_b32 s76, s44, 64\n\t" \
  "s_or_b32 s42, s42, s76\n\t" \
  "s_cbranch_scc0 20b\n\t" \
  "s_branch 99f\n\t" \
  "14:\n\t" \
  "s_sleep 3\n\t" \
  "s_add_u32 s59, s59, 0x10000\n\t" \
  "s_cmp_lt_u32 s59, 0x400000\n\t" \
  "s_cbranch_scc1 13b\n\t" \
  "s_mov_b64 exec, -1\n\t" \
  "s_mov_b32 s42, 16\n\t" \
  "s_branch 99f\n\t" \
  "8:\n\t" \
  "s_bitcmp1_b32 s58, 1\n\t" \
  "s_cbranch_scc1 4b\n\t" \
  "s_bitcmp1_b32 s58, 2\n\t" \
  "s_cbranch_scc1 5b\n\t" \
  "v_max3_u32 v116, v32, v33, v34\n\t" \
  "v_max3_u32 v117, v35, v36, v37\n\t" \
  "v_max3_u32 v116, v116, v38, v39\n\t" \
  "v_max_u32 v116, v116, v117\n\t" \
  "v_cmp_eq_u32 vcc, -1, v116\n\t" \
  "s_cbranch_vccz 5b\n\t" \
  "s_branch 0f\n\t" \
  "9:\n\t" \
  "s_bitcmp1_b32 s58, 2\n\t" \
  "s_cbranch_scc1 6b\n\t" \
  "v_max3_u32 v116, v40, v41, v42\n\t" \
  "v_max3_u32 v117, v43, v44, v45\n\t" \
  "v_max3_u32 v116, v116, v46, v47\n\t" \
  "v_max_u32 v116, v116, v117\n\t" \
  "v_cmp_eq_u32 vcc, -1, v116\n\t" \
  "s_cbranch_vccz 6b\n\t" \
  "s_or_b32 s58, s58, 2\n\t" \
  "0:\n\t" \
  "s_or_b32 s58, s58, 1\n\t" \
  "11:\n\t" \
  "buffer_load_dwordx4 v[32:35], v100, %[rh], s40 offen sc1\n\t" \
  "buffer_load_dwordx4 v[36:39], v100, %[rh], s76 offen sc1\n\t" \
  "buffer_load_dwordx4 v[40:43], v100, %[rh], s77 offen sc1\n\t" \
  "buffer_load_dwordx4 v[44:47], v100, %[rh], s78 offen sc1\n\t" \
  "s_waitcnt lgkmcnt(0)\n\t" \
  "s_branch 10b\n\t" \
  "17:\n\t" \
  "s_add_u32 s59, s59, 1\n\t" \
  "s_bitcmp1_b32 s58, 2\n\t" \
  "s_cbranch_scc1 3b\n\t" \
  "s_waitcnt vmcnt(0)\n\t" \
  "v_max_u32 v116, v32, v33\n\t" \
  "v_max3_u32 v116, v116, v34, v35\n\t" \
  "v_max3_u32 v116, v116, v36, v37\n\t" \
  "v_max3_u32 v116, v116, v38, v39\n\t" \
  "v_max3_u32 v116, v116, v40, v41\n\t" \
  "v_max3_u32 v116, v116, v42, v43\n\t" \
  "v_max3_u32 v116, v116, v44, v45\n\t" \
  "v_max3_u32 v116, v116, v46, v47\n\t" \
  "v_cmp_eq_u32 vcc, -1, v116\n\t" \
  "s_cbranch_vccz 3b\n\t" \
  "s_bitcmp1_b32 s59, 6\n\t" \
  "s_cbranch_scc1 12f\n\t" \
  "buffer_load_dwordx4 v[32:35], v100, %[rh], s40 offen sc1\n\t" \
  "buffer_load_dwordx4 v[36:39], v100, %[rh], s76 offen sc1\n\t" \
  "buffer_load_dwordx4 v[40:43], v100, %[rh], s77 offen sc1\n\t" \
  "buffer_load_dwordx4 v[44:47], v100, %[rh], s78 offen sc1\n\t" \
  "s_branch 17b\n\t" \
  "12:\n\t" \
  "s_mov_b32 s42, s58\n\t" \
  "s_branch 99f\n\t" \
  "91:\n\t" \
  "s_mov_b32 s58, s42\n\t" \
  "s_mov_b32 s95, 0\n\t" \
  "20:\n\t" \
  "s_and_b32 s76, s44, 7\n\t" \
  "s_lshl_b32 s76, s76, 15\n\t" \
  "s_add_i32 s40, s54, s76\n\t" \
  "s_and_b32 s76, s44, 3\n\t" \
  "s_lshl_b32 s45, s76, 14\n\t" \
  "s_add_i32 s47, s44, 1\n\t" \
  "s_min_u32 s47, s47, 63\n\t" \
  "s_lshr_b32 s76, s47, 1\n\t" \
  "s_lshl_b32 s47, s47, 20\n\t" \
  "s_add_i32 s47, s47, s55\n\t" \
  "s_bitcmp1_b64 %[sel], s76\n\t" \
  "s_cselect_b32 s76, 0x2000, 0\n\t" \
  "s_add_i32 s47, s47, s76\n\t" \
  "s_lshr_b32 s76, s44, 1\n\t" \
  "s_lshl_b32 s94, s44, 20\n\t" \
  "s_bitcmp1_b64 %[sel], s76\n\t" \
  "s_cselect_b32 s76, 0x2000, 0\n\t" \
  "s_add_i32 s94, s94, s76\n\t" \
  "s_add_i32 s49, s44, -1\n\t" \
  "s_and_b32 s48, s49, 2\n\t" \
  "s_lshl_b32 s48, s48, 13\n\t" \
  "s_and_b32 s76, s49, 1\n\t" \
  "s_lshl_b32 s76, s76, 12\n\t" \
  "s_add_i32 s48, s48, s76\n\t" \
  "s_lshl_b32 s50, s53, 8\n\t" \
  "s_mov_b32 s59, 0\n\t" \
  "s_mov_b32 s92, s51\n\t" \
  "s_mov_b32 s93, s53\n\t" \
  "s_xor_b32 s76, s40, 0x400\n\t" \
  "s_xor_b32 s77, s40, 0x800\n\t" \
  "s_xor_b32 s78, s40, 0xc00\n\t" \
  "s_cmp_lg_u32 s58, 0\n\t" \
  "s_cbranch_scc1 11f\n\t" \
  "s_cmp_lg_u32 s95, 0\n\t" \
  "s_cbranch_scc1 46f\n\t" \
  "10:\n\t" \
  "ds_read_b128 v[48:51], v102 offset:32768\n\t" \
  "ds_read_b128 v[52:55], v102 offset:33792\n\t" \
  "ds_read_b128 v[56:59], v102 offset:34816\n\t" \
  "ds_read_b128 v[60:63], v102 offset:35840\n\t" \
  "ds_read_b128 v[16:19], v102 offset:49152\n\t" \
  "ds_read_b128 v[20:23], v102 offset:50176\n\t" \
  "ds_read_b128 v[24:27], v102 offset:51200\n\t" \
  "ds_read_b128 v[28:31], v102 offset:52224\n\t" \
  "ds_read_b128 v[84:87], v103 offset:16384\n\t" \
  "ds_read_b128 v[88:91], v103 offset:17408\n\t" \
  "ds_read_b128 v[92:95], v103 offset:18432\n\t" \
  "ds_read_b128 v[96:99], v103 offset:19456\n\t" \
  "s_waitcnt lgkmcnt(4)\n\t" \
  "ds_read_b128 v[0:3], v103 offset:49152\n\t" \
  "ds_read_b128 v[4:7], v103 offset:50176\n\t" \
  "ds_read_b128 v[8:11], v103 offset:51200\n\t" \
  "ds_read_b128 v[12:15], v103 offset:52224\n\t" \
  "46:\n\t" \
  "s_bitcmp1_b32 s58, 1\n\t" \
  "s_cselect_b64 s[80:81], -1, 0\n\t" \
  "s_cmp_lg_u32 s58, 0\n\t" \
  "s_cbranch_scc1 17f\n\t" \
  "s_mov_b64 exec, 0xff\n\t" \
  "s_cmp_eq_u32 s95, 0\n\t" \
  "s_cbranch_scc1 16f\n\t" \
  "s_memtime vcc\n\t" \
  "s_memrealtime s[82:83]\n\t" \
  "s_nop 4\n\t" \
  "s_memrealtime s[84:85]\n\t" \
  "s_nop 4\n\t" \
  "s_memrealtime s[56:57]\n\t" \
  "s_nop 4\n\t" \
  "s_memrealtime s[86:87]\n\t" \
  "s_nop 4\n\t" \
  "s_memrealtime s[88:89]\n\t" \
  "s_nop 4\n\t" \
  "s_memrealtime s[90:91]\n\t" \
  "s_mov_b32 s92, 0\n\t" \
  "s_waitcnt lgkmcnt(0)\n\t" \
  "s_cmp_eq_u32 s82, s84\n\t" \
  "s_addc_u32 s92, s92, 0\n\t" \
  "s_cmp_eq_u32 s82, s56\n\t" \
  "s_addc_u32 s92, s92, 0\n\t" \
  "s_cmp_eq_u32 s82, s86\n\t" \
  "s_addc_u32 s92, s92, 0\n\t" \
  "s_cmp_eq_u32 s82, s88\n\t" \
  "s_addc_u32 s92, s92, 0\n\t" \
  "s_cmp_eq_u32 s82, s90\n\t" \
  "s_addc_u32 s92, s92, 0\n\t" \
  "s_mul_i32 s92, s92, 24\n\t" \
  "s_add_i32 s92, s92, vcc_lo\n\t" \
  "s_lshl4_add_u32 s93, s82, 64\n\t" \
  "s_lshl_b32 s50, s93, 8\n\t" \
  "s_waitcnt vmcnt(2)\n\t" \
  "v_mov_b32 v116, v118\n\t" \
  "s_branch 15f\n\t" \
  "16:\n\t" \
  "s_memtime vcc\n\t" \
  "s_waitcnt lgkmcnt(0)\n\t" \
  "s_sub_i32 s79, s41, vcc_lo\n\t" \
  "s_cmp_gt_i32 s79, 64\n\t" \
  "s_cbranch_scc1 16b\n\t" \
  "13:\n\t" \
  "buffer_load_dword v116, v112, %[rh], s48 offen sc1\n\t" \
  "s_memtime vcc\n\t" \
  "s_memrealtime s[82:83]\n\t" \
  "s_nop 4\n\t" \
  "s_memrealtime s[84:85]\n\t" \
  "s_nop 4\n\t" \
  "s_memrealtime s[56:57]\n\t" \
  "s_nop 4\n\t" \
  "s_memrealtime s[86:87]\n\t" \
  "s_nop 4\n\t" \
  "s_memrealtime s[88:89]\n\t" \
  "s_nop 4\n\t" \
  "s_memrealtime s[90:91]\n\t" \
  "s_mov_b32 s92, 0\n\t" \
  "s_waitcnt lgkmcnt(0)\n\t" \
  "s_cmp_eq_u32 s82, s84\n\t" \
  "s_addc_u32 s92, s92, 0\n\t" \
  "s_cmp_eq_u32 s82, s56\n\t" \
  "s_addc_u32 s92, s92, 0\n\t" \
  "s_cmp_eq_u32 s82, s86\n\t" \
  "s_addc_u32 s92, s92, 0\n\t" \
  "s_cmp_eq_u32 s82, s88\n\t" \
  "s_addc_u32 s92, s92, 0\n\t" \
  "s_cmp_eq_u32 s82, s90\n\t" \
  "s_addc_u32 s92, s92, 0\n\t" \
  "s_mul_i32 s92, s92, 24\n\t" \
  "s_add_i32 s92, s92, vcc_lo\n\t" \
  "s_lshl4_add_u32 s93, s82, 64\n\t" \
  "s_lshl_b32 s50, s93, 8\n\t" \
  "s_waitcnt vmcnt(0)\n\t" \
  "15:\n\t" \
  "v_mov_b32 v117, 20000\n\t" \
  "v_cmp_ne_u32_sdwa vcc, v116, s49 src0_sel:BYTE_0 src1_sel:DWORD\n\t" \
  "v_subrev_u32 v116, s50, v116\n\t" \
  "v_ashrrev_i32 v116, 8, v116\n\t" \
  "s_nop 1\n\t" \
  "v_max_i32_dpp v116, v116, v116 quad_perm:[1,0,3,2] row_mask:0xf bank_mask:0xf\n\t" \
  "s_cbranch_vccnz 14f\n\t" \
  "s_nop 0\n\t" \
  "v_max_i32_dpp v116, v116, v116 quad_perm:[2,3,0,1] row_mask:0xf bank_mask:0xf\n\t" \
  "s_nop 1\n\t" \
  "v_max_i32_dpp v116, v116, v116 row_half_mirror row_mask:0xf bank_mask:0xf\n\t" \
  "v_med3_i32 v116, v116, 0, v117\n\t" \
  "v_mul_u32_u24 v116, s52, v116\n\t" \
  "v_lshrrev_b32 v116, 16, v116\n\t" \
  "s_mov_b64 exec, -1\n\t" \
  "v_readfirstlane_b32 s84, v116\n\t" \
  "s_add_i32 s84, s84, s92\n\t" \
  ".p2align 6\n\t" \
  "s_nop 0\n\t" \
  "s_nop 0\n\t" \
  "s_nop 0\n\t" \
  "s_nop 0\n\t" \
  "s_nop 0\n\t" \
  "s_nop 0\n\t" \
  "s_nop 0\n\t" \
  "s_nop 0\n\t" \
  "1:\n\t" \
  "s_memtime vcc\n\t" \
  "s_waitcnt lgkmcnt(0)\n\t" \
  "s_sub_i32 s79, s84, vcc_lo\n\t" \
  "s_cmp_gt_i32 s79, 128\n\t" \
  "s_cbranch_scc1 1b\n\t" \
  ".p2align 4\n\t" \
  "2:\n\t" \
  "s_sub_i32 s79, s79, 33\n\t" \
  "s_cmp_gt_i32 s79, 56\n\t" \
  "s_cbranch_scc1 2b\n\t" \
  "buffer_load_dwordx4 v[32:35], v100, %[rh], s40 offen sc1\n\t" \
  "s_nop 12\n\t" \
  "buffer_load_dwordx4 v[36:39], v100, %[rh], s76 offen sc1\n\t" \
  "s_nop 12\n\t" \
  "buffer_load_dwordx4 v[40:43], v100, %[rh], s77 offen sc1\n\t" \
  "s_nop 12\n\t" \
  "buffer_load_dwordx4 v[44:47], v100, %[rh], s78 offen sc1\n\t" \
  "3:\n\t" \
  "v_mov_b64 v[68:69], 0\n\t" \
  "v_mov_b64 v[70:71], 0\n\t" \
  "v_mov_b64 v[72:73], 0\n\t" \
  "v_mov_b64 v[74:75], 0\n\t" \
  "v_mov_b64 v[76:77], 0\n\t" \
  "v_mov_b64 v[78:79], 0\n\t" \
  "v_mov_b64 v[80:81], 0\n\t" \
  "v_mov_b64 v[82:83], 0\n\t" \
  "s_waitcnt lgkmcnt(0)\n\t" \
  "s_waitcnt vmcnt(3)\n\t" \
  "v_smfmac_f32_16x16x64_bf16 v[68:71], v[32:35], a[96:103], v104\n\t" \
  "ds_write_b128 v101, v[32:35] offset:4096\n\t" \
  "v_smfmac_f32_16x16x64_bf16 v[72:75], v[32:35], a[104:111], v104\n\t" \
  "v_smfmac_f32_16x16x64_bf16 v[76:79], v[32:35], v[48:55], v104\n\t" \
  "v_smfmac_f32_16x16x64_bf16 v[80:83], v[32:35], v[56:63], v104\n\t" \
  "s_waitcnt vmcnt(2)\n\t" \
  "v_smfmac_f32_16x16x64_bf16 v[68:71], v[36:39], a[112:119], v104\n\t" \
  "ds_write_b128 v101, v[36:39] offset:5120\n\t" \
  "v_smfmac_f32_16x16x64_bf16 v[72:75], v[36:39], a[120:127], v104\n\t" \
  "v_smfmac_f32_16x16x64_bf16 v[76:79], v[36:39], a[128:135], v104\n\t" \
  "v_cmp_u_f32 vcc, v68, v69\n\t" \
  "v_smfmac_f32_16x16x64_bf16 v[80:83], v[36:39], a[136:143], v104\n\t" \
  "v_cmp_u_f32 s[82:83], v70, v71\n\t" \
  "s_waitcnt vmcnt(1)\n\t" \
  "v_smfmac_f32_16x16x64_bf16 v[68:71], v[40:43], v[16:23], v104\n\t" \
  "ds_write_b128 v101, v[40:43] offset:6144\n\t" \
  "v_smfmac_f32_16x16x64_bf16 v[72:75], v[40:43], v[24:31], v104\n\t" \
  "s_or_b64 vcc, vcc, s[82:83]\n\t" \
  "s_or_b64 vcc, vcc, s[80:81]\n\t" \
  "s_waitcnt lgkmcnt(1)\n\t" \
  "v_smfmac_f32_16x16x64_bf16 v[76:79], v[40:43], a[144:151], v104\n\t" \
  "s_cbranch_scc1 8f\n\t" \
  "5:\n\t" \
  "s_barrier\n\t" \
  "4:\n\t" \
  "s_memtime s[56:57]\n\t" \
  "ds_read_b128 v[48:51], v101 offset:8192\n\t" \
  "v_smfmac_f32_16x16x64_bf16 v[80:83], v[40:43], a[152:159], v104\n\t" \
  "ds_read_b128 v[52:55], v101 offset:9216\n\t" \
  "ds_read_b128 v[56:59], v101 offset:12288\n\t" \
  "s_waitcnt vmcnt(0)\n\t" \
  "v_smfmac_f32_16x16x64_bf16 v[68:71], v[44:47], a[160:167], v104\n\t" \
  "ds_write_b128 v101, v[44:47] offset:7168\n\t" \
  "v_smfmac_f32_16x16x64_bf16 v[72:75], v[44:47], a[168:175], v104\n\t" \
  "v_smfmac_f32_16x16x64_bf16 v[76:79], v[44:47], a[176:183], v104\n\t" \
  "v_cmp_u_f32 vcc, v68, v69\n\t" \
  "v_smfmac_f32_16x16x64_bf16 v[80:83], v[44:47], a[184:191], v104\n\t" \
  "v_cmp_u_f32 s[82:83], v70, v71\n\t" \
  "s_waitcnt lgkmcnt(3)\n\t" \
  "v_smfmac_f32_16x16x64_bf16 v[68:71], v[48:51], a[192:199], v104\n\t" \
  "v_smfmac_f32_16x16x64_bf16 v[72:75], v[48:51], a[200:207], v104\n\t" \
  "s_or_b64 vcc, vcc, s[82:83]\n\t" \
  "s_waitcnt lgkmcnt(0)\n\t" \
  "v_smfmac_f32_16x16x64_bf16 v[68:71], v[52:55], a[208:215], v104\n\t" \
  "s_cbranch_scc1 9f\n\t" \
  "6:\n\t" \
  "s_barrier\n\t" \
  "ds_read_b128 v[60:63], v101 offset:13312\n\t" \
  "v_smfmac_f32_16x16x64_bf16 v[72:75], v[52:55], a[216:223], v104\n\t" \
  "ds_read_b128 v[64:67], v101 offset:0\n\t" \
  "v_smfmac_f32_16x16x64_bf16 v[68:71], v[56:59], v[152:159], v104\n\t" \
  "v_smfmac_f32_16x16x64_bf16 v[72:75], v[56:59], v[160:167], v104\n\t" \
  "ds_read_b128 v[48:51], v101 offset:1024\n\t" \
  "s_waitcnt lgkmcnt(2)\n\t" \
  "v_smfmac_f32_16x16x64_bf16 v[68:71], v[60:63], v[168:175], v104\n\t" \
  "ds_read_b128 v[52:55], v101 offset:10240\n\t" \
  "v_smfmac_f32_16x16x64_bf16 v[72:75], v[60:63], v[176:183], v104\n\t" \
  "ds_read_b128 v[56:59], v101 offset:11264\n\t" \
  "s_waitcnt lgkmcnt(3)\n\t" \
  "v_smfmac_f32_16x16x64_bf16 v[68:71], v[64:67], a[0:7], v104\n\t" \
  "ds_read_b128 v[60:63], v101 offset:14336\n\t" \
  "v_smfmac_f32_16x16x64_bf16 v[72:75], v[64:67], a[8:15], v104\n\t" \
  "s_waitcnt lgkmcnt(3)\n\t" \
  "v_smfmac_f32_16x16x64_bf16 v[68:71], v[48:51], a[16:23], v104\n\t" \
  "ds_read_b128 v[64:67], v101 offset:15360\n\t" \
  "ds_read_b128 v[16:19], v102 offset:16384\n\t" \
  "v_smfmac_f32_16x16x64_bf16 v[72:75], v[48:51], a[24:31], v104\n\t" \
  "s_waitcnt lgkmcnt(4)\n\t" \
  "v_smfmac_f32_16x16x64_bf16 v[68:71], v[52:55], v[84:91], v104\n\t" \
  "ds_read_b128 v[48:51], v101 offset:3072\n\t" \
  "ds_read_b128 v[20:23], v102 offset:17408\n\t" \
  "v_smfmac_f32_16x16x64_bf16 v[72:75], v[52:55], v[92:99], v104\n\t" \
  "s_waitcnt lgkmcnt(5)\n\t" \
  "v_smfmac_f32_16x16x64_bf16 v[68:71], v[56:59], v[120:127], v104\n\t" \
  "ds_read_b128 v[52:55], v101 offset:2048\n\t" \
  "ds_read_b128 v[24:27], v102 offset:18432\n\t" \
  "v_smfmac_f32_16x16x64_bf16 v[72:75], v[56:59], v[128:135], v104\n\t" \
  "ds_read_b128 v[28:31], v102 offset:19456\n\t" \
  "s_waitcnt lgkmcnt(7)\n\t" \
  "v_smfmac_f32_16x16x64_bf16 v[68:71], v[60:63], v[0:7], v104\n\t" \
  "ds_read_b128 v[36:39], v102 offset:28672\n\t" \
  "v_smfmac_f32_16x16x64_bf16 v[72:75], v[60:63], v[8:15], v104\n\t" \
  "buffer_load_dwordx4 v[84:87], v107, %[ru], s94 offen offset:0 sc1\n\t" \
  "s_waitcnt lgkmcnt(7)\n\t" \
  "v_smfmac_f32_16x16x64_bf16 v[68:71], v[64:67], v[216:223], v104\n\t" \
  "ds_read_b128 v[0:3], v103 offset:0\n\t" \
  "v_smfmac_f32_16x16x64_bf16 v[72:75], v[64:67], v[224:231], v104\n\t" \
  "ds_read_b128 v[4:7], v103 offset:1024\n\t" \
  "s_waitcnt lgkmcnt(7)\n\t" \
  "v_smfmac_f32_16x16x64_bf16 v[68:71], v[48:51], a[64:71], v104\n\t" \
  "buffer_load_dwordx4 v[88:91], v107, %[ru], s94 offen offset:1024 sc1\n\t" \
  "s_waitcnt lgkmcnt(5)\n\t" \
  "v_smfmac_f32_16x16x64_bf16 v[68:71], v[52:55], v[16:23], v104\n\t" \
  "ds_read_b128 v[8:11], v103 offset:2048\n\t" \
  "v_smfmac_f32_16x16x64_bf16 v[72:75], v[48:51], a[72:79], v104\n\t" \
  "ds_read_b128 v[12:15], v103 offset:3072\n\t" \
  "s_waitcnt lgkmcnt(5)\n\t" \
  "v_smfmac_f32_16x16x64_bf16 v[72:75], v[52:55], v[24:31], v104\n\t" \
  "v_add_f32 v44, v68, v70\n\t" \
  "ds_read_b128 v[40:43], v102 offset:29696\n\t" \
  "v_smfmac_f32_16x16x64_bf16 v[76:79], v[56:59], v[136:143], v104\n\t" \
  "v_add_f32 v45, v69, v71\n\t" \
  "v_cvt_pk_bf16_f32 v44, v44, v45\n\t" \
  "v_smfmac_f32_16x16x64_bf16 v[80:83], v[56:59], v[144:151], v104\n\t" \
  "v_add_f32 v46, v72, v74\n\t" \
  "v_add_f32 v47, v73, v75\n\t" \
  "v_smfmac_f32_16x16x64_bf16 v[76:79], v[60:63], v[200:207], v104\n\t" \
  "v_cvt_pk_bf16_f32 v46, v46, v47\n\t" \
  "ds_write2st64_b32 v105, v44, v46 offset1:1\n\t" \
  "v_smfmac_f32_16x16x64_bf16 v[80:83], v[60:63], v[208:215], v104\n\t" \
  "ds_read_b128 v[56:59], v101 offset:8192\n\t" \
  "v_mov_b64 v[68:69], 0\n\t" \
  "v_smfmac_f32_16x16x64_bf16 v[76:79], v[64:67], v[232:239], v104\n\t" \
  "ds_read_b128 v[16:19], v103 offset:32768\n\t" \
  "v_mov_b64 v[70:71], 0\n\t" \
  "v_smfmac_f32_16x16x64_bf16 v[80:83], v[64:67], v[240:247], v104\n\t" \
  "ds_read_b128 v[20:23], v103 offset:33792\n\t" \
  "v_smfmac_f32_16x16x64_bf16 v[76:79], v[48:51], a[80:87], v104\n\t" \
  "s_waitcnt lgkmcnt(3)\n\t" \
  "s_barrier\n\t" \
  "v_smfmac_f32_16x16x64_bf16 v[80:83], v[48:51], a[88:95], v104\n\t" \
  "buffer_load_dwordx4 v[92:95], v107, %[ru], s94 offen offset:2048 sc1\n\t" \
  "v_smfmac_f32_16x16x64_bf16 v[68:71], v[32:35], v[36:43], v104\n\t" \
  "ds_read_b128 v[36:39], v102 offset:61440\n\t" \
  "ds_read_b128 v[40:43], v102 offset:62464\n\t" \
  "v_smfmac_f32_16x16x64_bf16 v[76:79], v[52:55], a[48:55], v104\n\t" \
  "ds_read_b128 v[60:63], v101 offset:1024\n\t" \
  "ds_read_b128 v[24:27], v103 offset:34816\n\t" \
  "v_smfmac_f32_16x16x64_bf16 v[80:83], v[52:55], a[56:63], v104\n\t" \
  "ds_read_b128 v[64:67], v101 offset:10240\n\t" \
  "buffer_load_dwordx4 v[96:99], v107, %[ru], s94 offen offset:3072 sc1\n\t" \
  "s_waitcnt lgkmcnt(7)\n\t" \
  "v_smfmac_f32_16x16x64_bf16 v[76:79], v[56:59], v[0:7], v104\n\t" \
  "ds_read_b128 v[48:51], v101 offset:12288\n\t" \
  "ds_read_b128 v[28:31], v103 offset:35840\n\t" \
  "global_load_dword v119, v109, %[kc] sc1\n\t" \
  "v_smfmac_f32_16x16x64_bf16 v[80:83], v[56:59], v[8:15], v104\n\t" \
  "s_waitcnt lgkmcnt(4)\n\t" \
  "v_smfmac_f32_16x16x64_bf16 v[76:79], v[60:63], a[32:39], v104\n\t" \
  "ds_read_b128 v[52:55], v101 offset:9216\n\t" \
  "buffer_load_dwordx4 v[32:35], v100, %[ru], s47 offen offset:0 sc1\n\t" \
  "v_smfmac_f32_16x16x64_bf16 v[80:83], v[60:63], a[40:47], v104\n\t" \
  "ds_read_b128 v[0:3], v102 offset:0\n\t" \
  "v_smfmac_f32_16x16x64_bf16 v[68:71], v[56:59], v[36:43], v104\n\t" \
  "ds_read_b128 v[36:39], v103 offset:28672\n\t" \
  "ds_read_b128 v[40:43], v103 offset:29696\n\t" \
  "s_waitcnt lgkmcnt(6)\n\t" \
  "v_smfmac_f32_16x16x64_bf16 v[76:79], v[64:67], a[240:247], v104\n\t" \
  "ds_read_b128 v[56:59], v101 offset:13312\n\t" \
  "v_smfmac_f32_16x16x64_bf16 v[80:83], v[64:67], a[248:255], v104\n\t" \
  "ds_read_b128 v[60:63], v101 offset:0\n\t" \
  "ds_read_b128 v[4:7], v102 offset:1024\n\t" \
  "buffer_load_dwordx4 v[44:47], v100, %[ru], s47 offen offset:1024 sc1\n\t" \
  "s_waitcnt lgkmcnt(8)\n\t" \
  "v_smfmac_f32_16x16x64_bf16 v[76:79], v[48:51], v[16:23], v104\n\t" \
  "ds_read_b128 v[8:11], v102 offset:2048\n\t" \
  "s_waitcnt lgkmcnt(8)\n\t" \
  "v_smfmac_f32_16x16x64_bf16 v[80:83], v[48:51], v[24:31], v104\n\t" \
  "ds_read_b128 v[12:15], v102 offset:3072\n\t" \
  "s_waitcnt lgkmcnt(8)\n\t" \
  "v_smfmac_f32_16x16x64_bf16 v[76:79], v[52:55], a[224:231], v104\n\t" \
  "s_waitcnt lgkmcnt(5)\n\t" \
  "v_smfmac_f32_16x16x64_bf16 v[68:71], v[48:51], v[36:43], v104\n\t" \
  "v_smfmac_f32_16x16x64_bf16 v[80:83], v[52:55], a[232:239], v104\n\t" \
  "s_waitcnt lgkmcnt(4)\n\t" \
  "v_smfmac_f32_16x16x64_bf16 v[76:79], v[56:59], v[184:191], v104\n\t" \
  "v_add_f32 v72, v68, v70\n\t" \
  "s_waitcnt lgkmcnt(2)\n\t" \
  "v_smfmac_f32_16x16x64_bf16 v[76:79], v[60:63], v[0:7], v104\n\t" \
  "v_add_f32 v73, v69, v71\n\t" \
  "v_smfmac_f32_16x16x64_bf16 v[80:83], v[56:59], v[192:199], v104\n\t" \
  "v_cvt_pk_bf16_f32 v72, v72, v73\n\t" \
  "s_waitcnt lgkmcnt(0)\n\t" \
  "v_smfmac_f32_16x16x64_bf16 v[80:83], v[60:63], v[8:15], v104\n\t" \
  "ds_write_b32 v106, v72 offset:11776\n\t" \
  "v_add_f32 v20, v76, v78\n\t" \
  "v_add_f32 v21, v77, v79\n\t" \
  "v_cvt_pk_bf16_f32 v20, v20, v21\n\t" \
  "ds_write_b32 v106, v20\n\t" \
  "s_nop 3\n\t" \
  "v_add_f32 v23, v80, v82\n\t" \
  "v_add_f32 v21, v81, v83\n\t" \
  "v_cvt_pk_bf16_f32 v21, v23, v21\n\t" \
  "ds_write_b32 v106, v21 offset:256\n\t" \
  "s_waitcnt lgkmcnt(0)\n\t" \
  "s_barrier\n\t" \
  "v_bfe_i32 v64, v114, 0, 1\n\t" \
  "v_and_b32 v64, 0xff800000, v64\n\t" \
  "v_bfe_i32 v65, v114, 1, 1\n\t" \
  "v_and_b32 v65, 0xff800000, v65\n\t" \
  "v_bfe_i32 v66, v114, 2, 1\n\t" \
  "v_and_b32 v66, 0xff800000, v66\n\t" \
  "v_bfe_i32 v67, v114, 3, 1\n\t" \
  "v_and_b32 v67, 0xff800000, v67\n\t" \
  "v_bfe_i32 v68, v114, 4, 1\n\t" \
  "v_and_b32 v68, 0xff800000, v68\n\t" \
  "v_bfe_i32 v69, v114, 5, 1\n\t" \
  "v_and_b32 v69, 0xff800000, v69\n\t" \
  "v_bfe_i32 v70, v114, 6, 1\n\t" \
  "v_and_b32 v70, 0xff800000, v70\n\t" \
  "v_bfe_i32 v71, v114, 7, 1\n\t" \
  "v_and_b32 v71, 0xff800000, v71\n\t" \
  "s_lshl_b32 s94, s45, 4\n\t" \
  "s_and_b32 s96, s44, 2\n\t" \
  "s_lshl_b32 s96, s96, 13\n\t" \
  "s_and_b32 s76, s44, 1\n\t" \
  "s_lshl_b32 s76, s76, 12\n\t" \
  "s_add_i32 s96, s96, s76\n\t" \
  "s_mov_b32 s95, 1\n\t" \
  "s_add_i32 s41, s56, 1950\n\t" \
  "s_sub_i32 s76, s92, s51\n\t" \
  "s_sub_i32 s77, s93, s53\n\t" \
  "s_cmp_eq_u32 s76, 0\n\t" \
  "s_cbranch_scc1 21f\n\t" \
  "v_cvt_f32_u32 v117, s76\n\t" \
  "v_cvt_f32_u32 v116, s77\n\t" \
  "v_rcp_f32 v117, v117\n\t" \
  "s_mov_b32 s51, s92\n\t" \
  "v_mul_f32 v117, v117, v116\n\t" \
  "v_max_f32 v117, 0x3f028f5c, v117\n\t" \
  "v_min_f32 v117, 0x3ffeb852, v117\n\t" \
  "v_rcp_f32 v116, v117\n\t" \
  "v_mul_f32 v117, 0x4f000000, v117\n\t" \
  "v_mul_f32 v116, 0x47800000, v116\n\t" \
  "v_cvt_u32_f32 v117, v117\n\t" \
  "v_cvt_u32_f32 v116, v116\n\t" \
  "s_mov_b32 s53, s93\n\t" \
  "v_readfirstlane_b32 s46, v117\n\t" \
  "v_readfirstlane_b32 s52, v116\n\t" \
  "21:\n\t" \
  "ds_read_b128 v[0:3], v101 offset:20480\n\t" \
  "ds_read_b128 v[4:7], v101 offset:21504\n\t" \
  "ds_read_b128 v[8:11], v101 offset:22528\n\t" \
  "ds_read_b128 v[12:15], v101 offset:23552\n\t" \
  "ds_read_b128 v[16:19], v101 offset:24576\n\t" \
  "ds_read_b128 v[20:23], v101 offset:25600\n\t" \
  "ds_read_b128 v[24:27], v101 offset:26624\n\t" \
  "ds_read_b128 v[28:31], v101 offset:27648\n\t" \
  "ds_read_b64_tr_b16 v[48:49], v108 offset:0\n\t" \
  "ds_read_b64_tr_b16 v[50:51], v108 offset:64\n\t" \
  "ds_read_b64_tr_b16 v[52:53], v108 offset:512\n\t" \
  "ds_read_b64_tr_b16 v[54:55], v108 offset:576\n\t" \
  "ds_read_b64_tr_b16 v[56:57], v108 offset:1024\n\t" \
  "ds_read_b64_tr_b16 v[58:59], v108 offset:1088\n\t" \
  "ds_read_b64_tr_b16 v[60:61], v108 offset:1536\n\t" \
  "ds_read_b64_tr_b16 v[62:63], v108 offset:1600\n\t" \
  "ds_read_b64_tr_b16 v[76:77], v108 offset:12288\n\t" \
  "ds_read_b64_tr_b16 v[78:79], v108 offset:12352\n\t" \
  "s_waitcnt vmcnt(3) lgkmcnt(10)\n\t" \
  "v_mfma_f32_16x16x32_bf16 v[64:67], v[0:3], v[84:87], v[64:67]\n\t" \
  "v_mfma_f32_16x16x32_bf16 v[68:71], v[16:19], v[84:87], v[68:71]\n\t" \
  "v_mfma_f32_16x16x32_bf16 v[64:67], v[4:7], v[88:91], v[64:67]\n\t" \
  "v_mfma_f32_16x16x32_bf16 v[68:71], v[20:23], v[88:91], v[68:71]\n\t" \
  "v_mfma_f32_16x16x32_bf16 v[64:67], v[8:11], v[92:95], v[64:67]\n\t" \
  "v_mfma_f32_16x16x32_bf16 v[68:71], v[24:27], v[92:95], v[68:71]\n\t" \
  "v_mfma_f32_16x16x32_bf16 v[64:67], v[12:15], v[96:99], v[64:67]\n\t" \
  "v_mfma_f32_16x16x32_bf16 v[68:71], v[28:31], v[96:99], v[68:71]\n\t" \
  "s_waitcnt lgkmcnt(0)\n\t" \
  "s_barrier\n\t" \
  "v_mfma_f32_16x16x32_bf16 v[72:75], v[48:51], v[84:87], 0\n\t" \
  "v_mfma_f32_16x16x32_bf16 v[72:75], v[52:55], v[88:91], v[72:75]\n\t" \
  "v_mfma_f32_16x16x32_bf16 v[72:75], v[56:59], v[92:95], v[72:75]\n\t" \
  "v_mfma_f32_16x16x32_bf16 v[72:75], v[60:63], v[96:99], v[72:75]\n\t" \
  "v_mfma_f32_16x16x32_bf16 v[72:75], v[76:79], v[84:87], v[72:75]\n\t" \
  "s_waitcnt vmcnt(0)\n\t" \
  "ds_write_b128 v101, v[32:35] offset:20480\n\t" \
  "ds_write_b128 v101, v[44:47] offset:21504\n\t" \
  "ds_read_b128 v[48:51], v102 offset:32768\n\t" \
  "ds_read_b128 v[52:55], v102 offset:33792\n\t" \
  "ds_read_b128 v[56:59], v102 offset:34816\n\t" \
  "ds_read_b128 v[60:63], v102 offset:35840\n\t" \
  "ds_read_b128 v[16:19], v102 offset:49152\n\t" \
  "ds_read_b128 v[20:23], v102 offset:50176\n\t" \
  "ds_read_b128 v[24:27], v102 offset:51200\n\t" \
  "ds_read_b128 v[28:31], v102 offset:52224\n\t" \
  "ds_read_b128 v[84:87], v103 offset:16384\n\t" \
  "ds_read_b128 v[88:91], v103 offset:17408\n\t" \
  "ds_read_b128 v[92:95], v103 offset:18432\n\t" \
  "ds_read_b128 v[96:99], v103 offset:19456\n\t" \
  "ds_read_b128 v[0:3], v103 offset:49152\n\t" \
  "ds_read_b128 v[4:7], v103 offset:50176\n\t" \
  "ds_read_b128 v[8:11], v103 offset:51200\n\t" \
  "ds_read_b128 v[12:15], v103 offset:52224\n\t" \
  "v_max3_f32 v76, v64, v65, v66\n\t" \
  "v_max3_f32 v77, v67, v68, v69\n\t" \
  "v_max3_f32 v76, v76, v70, v71\n\t" \
  "v_max3_f32 v77, v77, v72, v73\n\t" \
  "v_max3_f32 v76, v76, v74, v75\n\t" \
  "v_max_f32 v76, v76, v77\n\t" \
  "v_mov_b32 v77, v76\n\t" \
  "s_nop 1\n\t" \
  "v_permlane16_swap_b32 v76, v77\n\t" \
  "v_max_f32 v76, v76, v77\n\t" \
  "v_mov_b32 v77, v76\n\t" \
  "s_nop 1\n\t" \
  "v_permlane32_swap_b32 v76, v77\n\t" \
  "v_max_f32 v76, v76, v77\n\t" \
  "v_mul_f32 v76, %[cs], v76\n\t" \
  "v_fma_f32 v64, v64, %[cs], -v76\n\t" \
  "v_fma_f32 v65, v65, %[cs], -v76\n\t" \
  "v_fma_f32 v66, v66, %[cs], -v76\n\t" \
  "v_fma_f32 v67, v67, %[cs], -v76\n\t" \
  "v_fma_f32 v68, v68, %[cs], -v76\n\t" \
  "v_fma_f32 v69, v69, %[cs], -v76\n\t" \
  "v_fma_f32 v70, v70, %[cs], -v76\n\t" \
  "v_fma_f32 v71, v71, %[cs], -v76\n\t" \
  "v_fma_f32 v72, v72, %[cs], -v76\n\t" \
  "v_fma_f32 v73, v73, %[cs], -v76\n\t" \
  "v_fma_f32 v74, v74, %[cs], -v76\n\t" \
  "v_fma_f32 v75, v75, %[cs], -v76\n\t" \
  "v_exp_f32 v64, v64\n\t" \
  "v_exp_f32 v65, v65\n\t" \
  "v_exp_f32 v66, v66\n\t" \
  "v_exp_f32 v67, v67\n\t" \
  "v_exp_f32 v68, v68\n\t" \
  "v_exp_f32 v69, v69\n\t" \
  "v_exp_f32 v70, v70\n\t" \
  "v_exp_f32 v71, v71\n\t" \
  "v_exp_f32 v72, v72\n\t" \
  "v_exp_f32 v73, v73\n\t" \
  "v_exp_f32 v74, v74\n\t" \
  "v_exp_f32 v75, v75\n\t" \
  "v_add_f32 v76, v72, v73\n\t" \
  "v_add_f32 v77, v74, v75\n\t" \
  "v_add_f32 v78, v64, v65\n\t" \
  "v_add_f32 v79, v66, v67\n\t" \
  "v_add_f32 v76, v76, v77\n\t" \
  "v_add_f32 v78, v78, v68\n\t" \
  "v_add_f32 v79, v79, v69\n\t" \
  "v_add_f32 v78, v78, v70\n\t" \
  "v_add_f32 v79, v79, v71\n\t" \
  "v_fma_f32 v76, v76, 0.5, v78\n\t" \
  "v_add_f32 v76, v76, v79\n\t" \
  "v_mov_b32 v77, v76\n\t" \
  "s_nop 1\n\t" \
  "v_permlane16_swap_b32 v76, v77\n\t" \
  "v_add_f32 v76, v76, v77\n\t" \
  "v_mov_b32 v77, v76\n\t" \
  "s_nop 1\n\t" \
  "v_permlane32_swap_b32 v76, v77\n\t" \
  "v_add_f32 v76, v76, v77\n\t" \
  "v_rcp_f32 v76, v76\n\t" \
  "s_mov_b64 exec, 0xff\n\t" \
  "buffer_load_dword v118, v112, %[rh], s96 offen sc1\n\t" \
  "s_mov_b64 exec, -1\n\t" \
  "v_mul_f32 v64, v64, v76\n\t" \
  "v_mul_f32 v65, v65, v76\n\t" \
  "v_mul_f32 v66, v66, v76\n\t" \
  "v_mul_f32 v67, v67, v76\n\t" \
  "v_mul_f32 v68, v68, v76\n\t" \
  "v_mul_f32 v69, v69, v76\n\t" \
  "v_mul_f32 v70, v70, v76\n\t" \
  "v_mul_f32 v71, v71, v76\n\t" \
  "v_mul_f32 v72, v72, v76\n\t" \
  "v_mul_f32 v73, v73, v76\n\t" \
  "v_mul_f32 v74, v74, v76\n\t" \
  "v_mul_f32 v75, v75, v76\n\t" \
  "v_cvt_pk_bf16_f32 v76, v64, v68\n\t" \
  "v_cvt_pk_bf16_f32 v77, v65, v69\n\t" \
  "v_cvt_pk_bf16_f32 v78, v66, v70\n\t" \
  "v_cvt_pk_bf16_f32 v79, v67, v71\n\t" \
  "v_cvt_pk_bf16_f32 v80, v72, v73\n\t" \
  "v_cvt_pk_bf16_f32 v81, v74, v75\n\t" \
  "v_permlane32_swap_b32 v76, v78\n\t" \
  "v_permlane32_swap_b32 v77, v79\n\t" \
  "v_mov_b64 v[82:83], v[80:81]\n\t" \
  "buffer_store_dwordx4 v[76:79], v110, %[rh], s94 offen sc1\n\t" \
  "s_nop 0\n\t" \
  "v_permlane16_swap_b32 v80, v82\n\t" \
  "v_permlane16_swap_b32 v81, v83\n\t" \
  "buffer_store_dwordx4 v[80:83], v111, %[rh], s94 offen sc1\n\t" \
  "s_add_i32 s77, s44, 1\n\t" \
  "s_min_u32 s77, s77, 63\n\t" \
  "v_cmp_gt_i32 vcc, s77, v119\n\t" \
  "s_cmp_lg_u64 vcc, 0\n\t" \
  "s_cselect_b32 s42, 32, 0\n\t" \
  "s_add_i32 s44, s44, 1\n\t" \
  "s_and_b32 s58, s58, 4\n\t" \
  "s_and_b32 s76, s44, 64\n\t" \
  "s_or_b32 s42, s42, s76\n\t" \
  "s_cbranch_scc0 20b\n\t" \
  "s_branch 99f\n\t" \
  "14:\n\t" \
  "s_sleep 3\n\t" \
  "s_add_u32 s59, s59, 0x10000\n\t" \
  "s_cmp_lt_u32 s59, 0x400000\n\t" \
  "s_cbranch_scc1 13b\n\t" \
  "s_mov_b64 exec, -1\n\t" \
  "s_mov_b32 s42, 16\n\t" \
  "s_branch 99f\n\t" \
  "8:\n\t" \
  "s_bitcmp1_b32 s58, 1\n\t" \
  "s_cbranch_scc1 4b\n\t" \
  "s_bitcmp1_b32 s58, 2\n\t" \
  "s_cbranch_scc1 5b\n\t" \
  "v_max3_u32 v116, v32, v33, v34\n\t" \
  "v_max3_u32 v117, v35, v36, v37\n\t" \
  "v_max3_u32 v116, v116, v38, v39\n\t" \
  "v_max_u32 v116, v116, v117\n\t" \
  "v_cmp_eq_u32 vcc, -1, v116\n\t" \
  "s_cbranch_vccz 5b\n\t" \
  "s_branch 0f\n\t" \
  "9:\n\t" \
  "s_bitcmp1_b32 s58, 2\n\t" \
  "s_cbranch_scc1 6b\n\t" \
  "v_max3_u32 v116, v40, v41, v42\n\t" \
  "v_max3_u32 v117, v43, v44, v45\n\t" \
  "v_max3_u32 v116, v116, v46, v47\n\t" \
  "v_max_u32 v116, v116, v117\n\t" \
  "v_cmp_eq_u32 vcc, -1, v116\n\t" \
  "s_cbranch_vccz 6b\n\t" \
  "s_or_b32 s58, s58, 2\n\t" \
  "0:\n\t" \
  "s_or_b32 s58, s58, 1\n\t" \
  "11:\n\t" \
  "buffer_load_dwordx4 v[32:35], v100, %[rh], s40 offen sc1\n\t" \
  "buffer_load_dwordx4 v[36:39], v100, %[rh], s76 offen sc1\n\t" \
  "buffer_load_dwordx4 v[40:43], v100, %[rh], s77 offen sc1\n\t" \
  "buffer_load_dwordx4 v[44:47], v100, %[rh], s78 offen sc1\n\t" \
  "s_waitcnt lgkmcnt(0)\n\t" \
  "s_branch 10b\n\t" \
  "17:\n\t" \
  "s_add_u32 s59, s59, 1\n\t" \
  "s_bitcmp1_b32 s58, 2\n\t" \
  "s_cbranch_scc1 3b\n\t" \
  "s_waitcnt vmcnt(0)\n\t" \
  "v_max_u32 v116, v32, v33\n\t" \
  "v_max3_u32 v116, v116, v34, v35\n\t" \
  "v_max3_u32 v116, v116, v36, v37\n\t" \
  "v_max3_u32 v116, v116, v38, v39\n\t" \
  "v_max3_u32 v116, v116, v40, v41\n\t" \
  "v_max3_u32 v116, v116, v42, v43\n\t" \
  "v_max3_u32 v116, v116, v44, v45\n\t" \
  "v_max3_u32 v116, v116, v46, v47\n\t" \
  "v_cmp_eq_u32 vcc, -1, v116\n\t" \
  "s_cbranch_vccz 3b\n\t" \
  "s_bitcmp1_b32 s59, 6\n\t" \
  "s_cbranch_scc1 12f\n\t" \
  "buffer_load_dwordx4 v[32:35], v100, %[rh], s40 offen sc1\n\t" \
  "buffer_load_dwordx4 v[36:39], v100, %[rh], s76 offen sc1\n\t" \
  "buffer_load_dwordx4 v[40:43], v100, %[rh], s77 offen sc1\n\t" \
  "buffer_load_dwordx4 v[44:47], v100, %[rh], s78 offen sc1\n\t" \
  "s_branch 17b\n\t" \
  "12:\n\t" \
  "s_mov_b32 s42, s58\n\t" \
  "s_branch 99f\n\t" \
  "92:\n\t" \
  "s_mov_b32 s58, s42\n\t" \
  "s_mov_b32 s95, 0\n\t" \
  "20:\n\t" \
  "s_and_b32 s76, s44, 7\n\t" \
  "s_lshl_b32 s76, s76, 15\n\t" \
  "s_add_i32 s40, s54, s76\n\t" \
  "s_and_b32 s76, s44, 3\n\t" \
  "s_lshl_b32 s45, s76, 14\n\t" \
  "s_add_i32 s47, s44, 1\n\t" \
  "s_min_u32 s47, s47, 63\n\t" \
  "s_lshr_b32 s76, s47, 1\n\t" \
  "s_lshl_b32 s47, s47, 20\n\t" \
  "s_add_i32 s47, s47, s55\n\t" \
  "s_bitcmp1_b64 %[sel], s76\n\t" \
  "s_cselect_b32 s76, 0x2000, 0\n\t" \
  "s_add_i32 s47, s47, s76\n\t" \
  "s_lshr_b32 s76, s44, 1\n\t" \
  "s_lshl_b32 s94, s44, 20\n\t" \
  "s_bitcmp1_b64 %[sel], s76\n\t" \
  "s_cselect_b32 s76, 0x2000, 0\n\t" \
  "s_add_i32 s94, s94, s76\n\t" \
  "s_add_i32 s49, s44, -1\n\t" \
  "s_and_b32 s48, s49, 2\n\t" \
  "s_lshl_b32 s48, s48, 13\n\t" \
  "s_and_b32 s76, s49, 1\n\t" \
  "s_lshl_b32 s76, s76, 12\n\t" \
  "s_add_i32 s48, s48, s76\n\t" \
  "s_lshl_b32 s50, s53, 8\n\t" \
  "s_mov_b32 s59, 0\n\t" \
  "s_mov_b32 s92, s51\n\t" \
  "s_mov_b32 s93, s53\n\t" \
  "s_xor_b32 s76, s40, 0x400\n\t" \
  "s_xor_b32 s77, s40, 0x800\n\t" \
  "s_xor_b32 s78, s40, 0xc00\n\t" \
  "s_cmp_lg_u32 s58, 0\n\t" \
  "s_cbranch_scc1 11f\n\t" \
  "s_cmp_lg_u32 s95, 0\n\t" \
  "s_cbranch_scc1 46f\n\t" \
  "10:\n\t" \
  "ds_read_b128 v[48:51], v103 offset:0\n\t" \
  "ds_read_b128 v[52:55], v103 offset:1024\n\t" \
  "ds_read_b128 v[56:59], v103 offset:2048\n\t" \
  "ds_read_b128 v[60:63], v103 offset:3072\n\t" \
  "ds_read_b128 v[16:19], v103 offset:16384\n\t" \
  "ds_read_b128 v[20:23], v103 offset:17408\n\t" \
  "ds_read_b128 v[24:27], v103 offset:18432\n\t" \
  "ds_read_b128 v[28:31], v103 offset:19456\n\t" \
  "ds_read_b128 v[84:87], v103 offset:49152\n\t" \
  "ds_read_b128 v[88:91], v103 offset:50176\n\t" \
  "ds_read_b128 v[92:95], v103 offset:51200\n\t" \
  "ds_read_b128 v[96:99], v103 offset:52224\n\t" \
  "s_waitcnt lgkmcnt(4)\n\t" \
  "ds_read_b128 v[0:3], v102 offset:16384\n\t" \
  "ds_read_b128 v[4:7], v102 offset:17408\n\t" \
  "ds_read_b128 v[8:11], v102 offset:18432\n\t" \
  "ds_read_b128 v[12:15], v102 offset:19456\n\t" \
  "46:\n\t" \
  "s_bitcmp1_b32 s58, 1\n\t" \
  "s_cselect_b64 s[80:81], -1, 0\n\t" \
  "s_cmp_lg_u32 s58, 0\n\t" \
  "s_cbranch_scc1 17f\n\t" \
  "s_mov_b64 exec, 0xff\n\t" \
  "s_cmp_eq_u32 s95, 0\n\t" \
  "s_cbranch_scc1 16f\n\t" \
  "s_memtime vcc\n\t" \
  "s_memrealtime s[82:83]\n\t" \
  "s_nop 4\n\t" \
  "s_memrealtime s[84:85]\n\t" \
  "s_nop 4\n\t" \
  "s_memrealtime s[56:57]\n\t" \
  "s_nop 4\n\t" \
  "s_memrealtime s[86:87]\n\t" \
  "s_nop 4\n\t" \
  "s_memrealtime s[88:89]\n\t" \
  "s_nop 4\n\t" \
  "s_memrealtime s[90:91]\n\t" \
  "s_mov_b32 s92, 0\n\t" \
  "s_waitcnt lgkmcnt(0)\n\t" \
  "s_cmp_eq_u32 s82, s84\n\t" \
  "s_addc_u32 s92, s92, 0\n\t" \
  "s_cmp_eq_u32 s82, s56\n\t" \
  "s_addc_u32 s92, s92, 0\n\t" \
  "s_cmp_eq_u32 s82, s86\n\t" \
  "s_addc_u32 s92, s92, 0\n\t" \
  "s_cmp_eq_u32 s82, s88\n\t" \
  "s_addc_u32 s92, s92, 0\n\t" \
  "s_cmp_eq_u32 s82, s90\n\t" \
  "s_addc_u32 s92, s92, 0\n\t" \
  "s_mul_i32 s92, s92, 24\n\t" \
  "s_add_i32 s92, s92, vcc_lo\n\t" \
  "s_lshl4_add_u32 s93, s82, 64\n\t" \
  "s_lshl_b32 s50, s93, 8\n\t" \
  "s_waitcnt vmcnt(2)\n\t" \
  "v_mov_b32 v116, v118\n\t" \
  "s_branch 15f\n\t" \
  "16:\n\t" \
  "s_memtime vcc\n\t" \
  "s_waitcnt lgkmcnt(0)\n\t" \
  "s_sub_i32 s79, s41, vcc_lo\n\t" \
  "s_cmp_gt_i32 s79, 64\n\t" \
  "s_cbranch_scc1 16b\n\t" \
  "13:\n\t" \
  "buffer_load_dword v116, v112, %[rh], s48 offen sc1\n\t" \
  "s_memtime vcc\n\t" \
  "s_memrealtime s[82:83]\n\t" \
  "s_nop 4\n\t" \
  "s_memrealtime s[84:85]\n\t" \
  "s_nop 4\n\t" \
  "s_memrealtime s[56:57]\n\t" \
  "s_nop 4\n\t" \
  "s_memrealtime s[86:87]\n\t" \
  "s_nop 4\n\t" \
  "s_memrealtime s[88:89]\n\t" \
  "s_nop 4\n\t" \
  "s_memrealtime s[90:91]\n\t" \
  "s_mov_b32 s92, 0\n\t" \
  "s_waitcnt lgkmcnt(0)\n\t" \
  "s_cmp_eq_u32 s82, s84\n\t" \
  "s_addc_u32 s92, s92, 0\n\t" \
  "s_cmp_eq_u32 s82, s56\n\t" \
  "s_addc_u32 s92, s92, 0\n\t" \
  "s_cmp_eq_u32 s82, s86\n\t" \
  "s_addc_u32 s92, s92, 0\n\t" \
  "s_cmp_eq_u32 s82, s88\n\t" \
  "s_addc_u32 s92, s92, 0\n\t" \
  "s_cmp_eq_u32 s82, s90\n\t" \
  "s_addc_u32 s92, s92, 0\n\t" \
  "s_mul_i32 s92, s92, 24\n\t" \
  "s_add_i32 s92, s92, vcc_lo\n\t" \
  "s_lshl4_add_u32 s93, s82, 64\n\t" \
  "s_lshl_b32 s50, s93, 8\n\t" \
  "s_waitcnt vmcnt(0)\n\t" \
  "15:\n\t" \
  "v_mov_b32 v117, 20000\n\t" \
  "v_cmp_ne_u32_sdwa vcc, v116, s49 src0_sel:BYTE_0 src1_sel:DWORD\n\t" \
  "v_subrev_u32 v116, s50, v116\n\t" \
  "v_ashrrev_i32 v116, 8, v116\n\t" \
  "s_nop 1\n\t" \
  "v_max_i32_dpp v116, v116, v116 quad_perm:[1,0,3,2] row_mask:0xf bank_mask:0xf\n\t" \
  "s_cbranch_vccnz 14f\n\t" \
  "s_nop 0\n\t" \
  "v_max_i32_dpp v116, v116, v116 quad_perm:[2,3,0,1] row_mask:0xf bank_mask:0xf\n\t" \
  "s_nop 1\n\t" \
  "v_max_i32_dpp v116, v116, v116 row_half_mirror row_mask:0xf bank_mask:0xf\n\t" \
  "v_med3_i32 v116, v116, 0, v117\n\t" \
  "v_mul_u32_u24 v116, s52, v116\n\t" \
  "v_lshrrev_b32 v116, 16, v116\n\t" \
  "s_mov_b64 exec, -1\n\t" \
  "v_readfirstlane_b32 s84, v116\n\t" \
  "s_add_i32 s84, s84, s92\n\t" \
  ".p2align 6\n\t" \
  "s_nop 0\n\t" \
  "s_nop 0\n\t" \
  "s_nop 0\n\t" \
  "s_nop 0\n\t" \
  "s_nop 0\n\t" \
  "s_nop 0\n\t" \
  "s_nop 0\n\t" \
  "s_nop 0\n\t" \
  "1:\n\t" \
  "s_memtime vcc\n\t" \
  "s_waitcnt lgkmcnt(0)\n\t" \
  "s_sub_i32 s79, s84, vcc_lo\n\t" \
  "s_cmp_gt_i32 s79, 128\n\t" \
  "s_cbranch_scc1 1b\n\t" \
  ".p2align 4\n\t" \
  "2:\n\t" \
  "s_sub_i32 s79, s79, 33\n\t" \
  "s_cmp_gt_i32 s79, 56\n\t" \
  "s_cbranch_scc1 2b\n\t" \
  "buffer_load_dwordx4 v[32:35], v100, %[rh], s40 offen sc1\n\t" \
  "s_nop 12\n\t" \
  "buffer_load_dwordx4 v[36:39], v100, %[rh], s76 offen sc1\n\t" \
  "s_nop 12\n\t" \
  "buffer_load_dwordx4 v[40:43], v100, %[rh], s77 offen sc1\n\t" \
  "s_nop 12\n\t" \
  "buffer_load_dwordx4 v[44:47], v100, %[rh], s78 offen sc1\n\t" \
  "3:\n\t" \
  "v_mov_b64 v[68:69], 0\n\t" \
  "v_mov_b64 v[70:71], 0\n\t" \
  "v_mov_b64 v[72:73], 0\n\t" \
  "v_mov_b64 v[74:75], 0\n\t" \
  "v_mov_b64 v[76:77], 0\n\t" \
  "v_mov_b64 v[78:79], 0\n\t" \
  "v_mov_b64 v[80:81], 0\n\t" \
  "v_mov_b64 v[82:83], 0\n\t" \
  "s_waitcnt lgkmcnt(0)\n\t" \
  "s_waitcnt vmcnt(3)\n\t" \
  "v_smfmac_f32_16x16x64_bf16 v[68:71], v[32:35], a[192:199], v104\n\t" \
  "ds_write_b128 v101, v[32:35] offset:8192\n\t" \
  "v_smfmac_f32_16x16x64_bf16 v[72:75], v[32:35], a[200:207], v104\n\t" \
  "v_smfmac_f32_16x16x64_bf16 v[76:79], v[32:35], v[48:55], v104\n\t" \
  "v_smfmac_f32_16x16x64_bf16 v[80:83], v[32:35], v[56:63], v104\n\t" \
  "s_waitcnt vmcnt(2)\n\t" \
  "v_smfmac_f32_16x16x64_bf16 v[68:71], v[36:39], a[208:215], v104\n\t" \
  "ds_write_b128 v101, v[36:39] offset:9216\n\t" \
  "v_smfmac_f32_16x16x64_bf16 v[72:75], v[36:39], a[216:223], v104\n\t" \
  "v_smfmac_f32_16x16x64_bf16 v[76:79], v[36:39], a[224:231], v104\n\t" \
  "v_cmp_u_f32 vcc, v68, v69\n\t" \
  "v_smfmac_f32_16x16x64_bf16 v[80:83], v[36:39], a[232:239], v104\n\t" \
  "v_cmp_u_f32 s[82:83], v70, v71\n\t" \
  "s_waitcnt vmcnt(1)\n\t" \
  "v_smfmac_f32_16x16x64_bf16 v[68:71], v[40:43], v[16:23], v104\n\t" \
  "ds_write_b128 v101, v[40:43] offset:10240\n\t" \
  "v_smfmac_f32_16x16x64_bf16 v[72:75], v[40:43], v[24:31], v104\n\t" \
  "s_or_b64 vcc, vcc, s[82:83]\n\t" \
  "s_or_b64 vcc, vcc, s[80:81]\n\t" \
  "s_waitcnt lgkmcnt(1)\n\t" \
  "v_smfmac_f32_16x16x64_bf16 v[76:79], v[40:43], a[240:247], v104\n\t" \
  "s_cbranch_scc1 8f\n\t" \
  "5:\n\t" \
  "s_barrier\n\t" \
  "4:\n\t" \
  "s_memtime s[56:57]\n\t" \
  "ds_read_b128 v[48:51], v101 offset:12288\n\t" \
  "v_smfmac_f32_16x16x64_bf16 v[80:83], v[40:43], a[248:255], v104\n\t" \
  "ds_read_b128 v[52:55], v101 offset:13312\n\t" \
  "ds_read_b128 v[56:59], v101 offset:0\n\t" \
  "s_waitcnt vmcnt(0)\n\t" \
  "v_smfmac_f32_16x16x64_bf16 v[68:71], v[44:47], v[120:127], v104\n\t" \
  "v_smfmac_f32_16x16x64_bf16 v[72:75], v[44:47], v[128:135], v104\n\t" \
  "ds_write_b128 v101, v[44:47] offset:11264\n\t" \
  "v_smfmac_f32_16x16x64_bf16 v[76:79], v[44:47], v[136:143], v104\n\t" \
  "v_cmp_u_f32 vcc, v68, v69\n\t" \
  "v_smfmac_f32_16x16x64_bf16 v[80:83], v[44:47], v[144:151], v104\n\t" \
  "v_cmp_u_f32 s[82:83], v70, v71\n\t" \
  "s_waitcnt lgkmcnt(3)\n\t" \
  "v_smfmac_f32_16x16x64_bf16 v[68:71], v[48:51], v[152:159], v104\n\t" \
  "v_smfmac_f32_16x16x64_bf16 v[72:75], v[48:51], v[160:167], v104\n\t" \
  "s_or_b64 vcc, vcc, s[82:83]\n\t" \
  "s_waitcnt lgkmcnt(0)\n\t" \
  "v_smfmac_f32_16x16x64_bf16 v[68:71], v[52:55], v[168:175], v104\n\t" \
  "s_cbranch_scc1 9f\n\t" \
  "6:\n\t" \
  "s_barrier\n\t" \
  "ds_read_b128 v[60:63], v101 offset:1024\n\t" \
  "v_smfmac_f32_16x16x64_bf16 v[72:75], v[52:55], v[176:183], v104\n\t" \
  "ds_read_b128 v[64:67], v101 offset:4096\n\t" \
  "v_smfmac_f32_16x16x64_bf16 v[68:71], v[56:59], a[0:7], v104\n\t" \
  "v_smfmac_f32_16x16x64_bf16 v[72:75], v[56:59], a[8:15], v104\n\t" \
  "ds_read_b128 v[48:51], v101 offset:5120\n\t" \
  "s_waitcnt lgkmcnt(2)\n\t" \
  "v_smfmac_f32_16x16x64_bf16 v[68:71], v[60:63], a[16:23], v104\n\t" \
  "ds_read_b128 v[52:55], v101 offset:14336\n\t" \
  "v_smfmac_f32_16x16x64_bf16 v[72:75], v[60:63], a[24:31], v104\n\t" \
  "ds_read_b128 v[56:59], v101 offset:15360\n\t" \
  "s_waitcnt lgkmcnt(3)\n\t" \
  "v_smfmac_f32_16x16x64_bf16 v[68:71], v[64:67], a[96:103], v104\n\t" \
  "ds_read_b128 v[60:63], v101 offset:2048\n\t" \
  "v_smfmac_f32_16x16x64_bf16 v[72:75], v[64:67], a[104:111], v104\n\t" \
  "s_waitcnt lgkmcnt(3)\n\t" \
  "v_smfmac_f32_16x16x64_bf16 v[68:71], v[48:51], a[112:119], v104\n\t" \
  "ds_read_b128 v[64:67], v101 offset:3072\n\t" \
  "ds_read_b128 v[16:19], v102 offset:49152\n\t" \
  "v_smfmac_f32_16x16x64_bf16 v[72:75], v[48:51], a[120:127], v104\n\t" \
  "s_waitcnt lgkmcnt(4)\n\t" \
  "v_smfmac_f32_16x16x64_bf16 v[68:71], v[52:55], v[84:91], v104\n\t" \
  "ds_read_b128 v[48:51], v101 offset:7168\n\t" \
  "ds_read_b128 v[20:23], v102 offset:50176\n\t" \
  "v_smfmac_f32_16x16x64_bf16 v[72:75], v[52:55], v[92:99], v104\n\t" \
  "s_waitcnt lgkmcnt(5)\n\t" \
  "v_smfmac_f32_16x16x64_bf16 v[68:71], v[56:59], v[216:223], v104\n\t" \
  "ds_read_b128 v[52:55], v101 offset:6144\n\t" \
  "ds_read_b128 v[24:27], v102 offset:51200\n\t" \
  "v_smfmac_f32_16x16x64_bf16 v[72:75], v[56:59], v[224:231], v104\n\t" \
  "ds_read_b128 v[28:31], v102 offset:52224\n\t" \
  "s_waitcnt lgkmcnt(7)\n\t" \
  "v_smfmac_f32_16x16x64_bf16 v[68:71], v[60:63], v[0:7], v104\n\t" \
  "v_smfmac_f32_16x16x64_bf16 v[72:75], v[60:63], v[8:15], v104\n\t" \
  "buffer_load_dwordx4 v[84:87], v107, %[ru], s94 offen offset:0 sc1\n\t" \
  "s_waitcnt lgkmcnt(6)\n\t" \
  "v_smfmac_f32_16x16x64_bf16 v[68:71], v[64:67], a[64:71], v104\n\t" \
  "ds_read_b128 v[0:3], v103 offset:32768\n\t" \
  "v_smfmac_f32_16x16x64_bf16 v[72:75], v[64:67], a[72:79], v104\n\t" \
  "ds_read_b128 v[4:7], v103 offset:33792\n\t" \
  "s_waitcnt lgkmcnt(6)\n\t" \
  "v_smfmac_f32_16x16x64_bf16 v[68:71], v[48:51], a[160:167], v104\n\t" \
  "buffer_load_dwordx4 v[88:91], v107, %[ru], s94 offen offset:1024 sc1\n\t" \
  "s_waitcnt lgkmcnt(4)\n\t" \
  "v_smfmac_f32_16x16x64_bf16 v[68:71], v[52:55], v[16:23], v104\n\t" \
  "ds_read_b128 v[8:11], v103 offset:34816\n\t" \
  "v_smfmac_f32_16x16x64_bf16 v[72:75], v[48:51], a[168:175], v104\n\t" \
  "ds_read_b128 v[12:15], v103 offset:35840\n\t" \
  "s_waitcnt lgkmcnt(4)\n\t" \
  "v_smfmac_f32_16x16x64_bf16 v[72:75], v[52:55], v[24:31], v104\n\t" \
  "v_add_f32 v44, v68, v70\n\t" \
  "v_smfmac_f32_16x16x64_bf16 v[76:79], v[56:59], v[232:239], v104\n\t" \
  "v_add_f32 v45, v69, v71\n\t" \
  "v_cvt_pk_bf16_f32 v44, v44, v45\n\t" \
  "v_smfmac_f32_16x16x64_bf16 v[80:83], v[56:59], v[240:247], v104\n\t" \
  "v_add_f32 v46, v72, v74\n\t" \
  "v_add_f32 v47, v73, v75\n\t" \
  "v_smfmac_f32_16x16x64_bf16 v[76:79], v[60:63], a[48:55], v104\n\t" \
  "v_cvt_pk_bf16_f32 v46, v46, v47\n\t" \
  "ds_write2st64_b32 v105, v44, v46 offset1:1\n\t" \
  "v_smfmac_f32_16x16x64_bf16 v[80:83], v[60:63], a[56:63], v104\n\t" \
  "ds_read_b128 v[56:59], v101 offset:12288\n\t" \
  "v_smfmac_f32_16x16x64_bf16 v[76:79], v[64:67], a[80:87], v104\n\t" \
  "ds_read_b128 v[16:19], v102 offset:0\n\t" \
  "v_smfmac_f32_16x16x64_bf16 v[80:83], v[64:67], a[88:95], v104\n\t" \
  "ds_read_b128 v[20:23], v102 offset:1024\n\t" \
  "v_smfmac_f32_16x16x64_bf16 v[76:79], v[48:51], a[176:183], v104\n\t" \
  "s_waitcnt lgkmcnt(3)\n\t" \
  "s_barrier\n\t" \
  "v_smfmac_f32_16x16x64_bf16 v[80:83], v[48:51], a[184:191], v104\n\t" \
  "buffer_load_dwordx4 v[92:95], v107, %[ru], s94 offen offset:2048 sc1\n\t" \
  "v_smfmac_f32_16x16x64_bf16 v[76:79], v[52:55], a[144:151], v104\n\t" \
  "ds_read_b128 v[60:63], v101 offset:5120\n\t" \
  "ds_read_b128 v[24:27], v102 offset:2048\n\t" \
  "v_smfmac_f32_16x16x64_bf16 v[80:83], v[52:55], a[152:159], v104\n\t" \
  "ds_read_b128 v[64:67], v101 offset:14336\n\t" \
  "buffer_load_dwordx4 v[96:99], v107, %[ru], s94 offen offset:3072 sc1\n\t" \
  "s_waitcnt lgkmcnt(5)\n\t" \
  "v_smfmac_f32_16x16x64_bf16 v[76:79], v[56:59], v[0:7], v104\n\t" \
  "ds_read_b128 v[48:51], v101 offset:0\n\t" \
  "ds_read_b128 v[28:31], v102 offset:3072\n\t" \
  "v_smfmac_f32_16x16x64_bf16 v[80:83], v[56:59], v[8:15], v104\n\t" \
  "global_load_dword v119, v109, %[kc] sc1\n\t" \
  "s_waitcnt lgkmcnt(4)\n\t" \
  "v_smfmac_f32_16x16x64_bf16 v[76:79], v[60:63], a[128:135], v104\n\t" \
  "ds_read_b128 v[52:55], v101 offset:13312\n\t" \
  "v_smfmac_f32_16x16x64_bf16 v[80:83], v[60:63], a[136:143], v104\n\t" \
  "ds_read_b128 v[0:3], v102 offset:32768\n\t" \
  "buffer_load_dwordx4 v[32:35], v100, %[ru], s47 offen offset:0 sc1\n\t" \
  "s_waitcnt lgkmcnt(4)\n\t" \
  "v_smfmac_f32_16x16x64_bf16 v[76:79], v[64:67], v[200:207], v104\n\t" \
  "ds_read_b128 v[56:59], v101 offset:1024\n\t" \
  "v_smfmac_f32_16x16x64_bf16 v[80:83], v[64:67], v[208:215], v104\n\t" \
  "ds_read_b128 v[60:63], v101 offset:4096\n\t" \
  "ds_read_b128 v[4:7], v102 offset:33792\n\t" \
  "s_waitcnt lgkmcnt(6)\n\t" \
  "v_smfmac_f32_16x16x64_bf16 v[76:79], v[48:51], v[16:23], v104\n\t" \
  "ds_read_b128 v[8:11], v102 offset:34816\n\t" \
  "buffer_load_dwordx4 v[36:39], v100, %[ru], s47 offen offset:1024 sc1\n\t" \
  "s_waitcnt lgkmcnt(6)\n\t" \
  "v_smfmac_f32_16x16x64_bf16 v[80:83], v[48:51], v[24:31], v104\n\t" \
  "ds_read_b128 v[12:15], v102 offset:35840\n\t" \
  "s_waitcnt lgkmcnt(6)\n\t" \
  "v_smfmac_f32_16x16x64_bf16 v[76:79], v[52:55], v[184:191], v104\n\t" \
  "v_smfmac_f32_16x16x64_bf16 v[80:83], v[52:55], v[192:199], v104\n\t" \
  "buffer_load_dwordx4 v[40:43], v100, %[ru], s47 offen offset:2048 sc1\n\t" \
  "s_waitcnt lgkmcnt(4)\n\t" \
  "v_smfmac_f32_16x16x64_bf16 v[76:79], v[56:59], a[32:39], v104\n\t" \
  "s_waitcnt lgkmcnt(2)\n\t" \
  "v_smfmac_f32_16x16x64_bf16 v[76:79], v[60:63], v[0:7], v104\n\t" \
  "v_smfmac_f32_16x16x64_bf16 v[80:83], v[56:59], a[40:47], v104\n\t" \
  "s_waitcnt lgkmcnt(0)\n\t" \
  "v_smfmac_f32_16x16x64_bf16 v[80:83], v[60:63], v[8:15], v104\n\t" \
  "v_add_f32 v20, v76, v78\n\t" \
  "v_add_f32 v21, v77, v79\n\t" \
  "v_cvt_pk_bf16_f32 v20, v20, v21\n\t" \
  "ds_write_b32 v106, v20\n\t" \
  "s_nop 3\n\t" \
  "v_add_f32 v23, v80, v82\n\t" \
  "v_add_f32 v21, v81, v83\n\t" \
  "v_cvt_pk_bf16_f32 v21, v23, v21\n\t" \
  "ds_write_b32 v106, v21 offset:256\n\t" \
  "s_waitcnt lgkmcnt(0)\n\t" \
  "s_barrier\n\t" \
  "v_bfe_i32 v64, v114, 0, 1\n\t" \
  "v_and_b32 v64, 0xff800000, v64\n\t" \
  "v_bfe_i32 v65, v114, 1, 1\n\t" \
  "v_and_b32 v65, 0xff800000, v65\n\t" \
  "v_bfe_i32 v66, v114, 2, 1\n\t" \
  "v_and_b32 v66, 0xff800000, v66\n\t" \
  "v_bfe_i32 v67, v114, 3, 1\n\t" \
  "v_and_b32 v67, 0xff800000, v67\n\t" \
  "v_bfe_i32 v68, v114, 4, 1\n\t" \
  "v_and_b32 v68, 0xff800000, v68\n\t" \
  "v_bfe_i32 v69, v114, 5, 1\n\t" \
  "v_and_b32 v69, 0xff800000, v69\n\t" \
  "v_bfe_i32 v70, v114, 6, 1\n\t" \
  "v_and_b32 v70, 0xff800000, v70\n\t" \
  "v_bfe_i32 v71, v114, 7, 1\n\t" \
  "v_and_b32 v71, 0xff800000, v71\n\t" \
  "s_lshl_b32 s94, s45, 4\n\t" \
  "s_and_b32 s96, s44, 2\n\t" \
  "s_lshl_b32 s96, s96, 13\n\t" \
  "s_and_b32 s76, s44, 1\n\t" \
  "s_lshl_b32 s76, s76, 12\n\t" \
  "s_add_i32 s96, s96, s76\n\t" \
  "s_mov_b32 s95, 1\n\t" \
  "s_add_i32 s41, s56, 1950\n\t" \
  "s_sub_i32 s76, s92, s51\n\t" \
  "s_sub_i32 s77, s93, s53\n\t" \
  "s_cmp_eq_u32 s76, 0\n\t" \
  "s_cbranch_scc1 21f\n\t" \
  "v_cvt_f32_u32 v117, s76\n\t" \
  "v_cvt_f32_u32 v116, s77\n\t" \
  "v_rcp_f32 v117, v117\n\t" \
  "s_mov_b32 s51, s92\n\t" \
  "v_mul_f32 v117, v117, v116\n\t" \
  "v_max_f32 v117, 0x3f028f5c, v117\n\t" \
  "v_min_f32 v117, 0x3ffeb852, v117\n\t" \
  "v_rcp_f32 v116, v117\n\t" \
  "v_mul_f32 v117, 0x4f000000, v117\n\t" \
  "v_mul_f32 v116, 0x47800000, v116\n\t" \
  "v_cvt_u32_f32 v117, v117\n\t" \
  "v_cvt_u32_f32 v116, v116\n\t" \
  "s_mov_b32 s53, s93\n\t" \
  "v_readfirstlane_b32 s46, v117\n\t" \
  "v_readfirstlane_b32 s52, v116\n\t" \
  "21:\n\t" \
  "ds_read_b128 v[0:3], v101 offset:20480\n\t" \
  "ds_read_b128 v[4:7], v101 offset:21504\n\t" \
  "ds_read_b128 v[8:11], v101 offset:22528\n\t" \
  "ds_read_b128 v[12:15], v101 offset:23552\n\t" \
  "ds_read_b128 v[16:19], v101 offset:24576\n\t" \
  "ds_read_b128 v[20:23], v101 offset:25600\n\t" \
  "ds_read_b128 v[24:27], v101 offset:26624\n\t" \
  "ds_read_b128 v[28:31], v101 offset:27648\n\t" \
  "ds_read_b64_tr_b16 v[48:49], v108 offset:0\n\t" \
  "ds_read_b64_tr_b16 v[50:51], v108 offset:64\n\t" \
  "ds_read_b64_tr_b16 v[52:53], v108 offset:512\n\t" \
  "ds_read_b64_tr_b16 v[54:55], v108 offset:576\n\t" \
  "ds_read_b64_tr_b16 v[56:57], v108 offset:1024\n\t" \
  "ds_read_b64_tr_b16 v[58:59], v108 offset:1088\n\t" \
  "ds_read_b64_tr_b16 v[60:61], v108 offset:1536\n\t" \
  "ds_read_b64_tr_b16 v[62:63], v108 offset:1600\n\t" \
  "ds_read_b64_tr_b16 v[76:77], v108 offset:12288\n\t" \
  "ds_read_b64_tr_b16 v[78:79], v108 offset:12352\n\t" \
  "s_waitcnt vmcnt(4) lgkmcnt(10)\n\t" \
  "v_mfma_f32_16x16x32_bf16 v[64:67], v[0:3], v[84:87], v[64:67]\n\t" \
  "v_mfma_f32_16x16x32_bf16 v[68:71], v[16:19], v[84:87], v[68:71]\n\t" \
  "v_mfma_f32_16x16x32_bf16 v[64:67], v[4:7], v[88:91], v[64:67]\n\t" \
  "v_mfma_f32_16x16x32_bf16 v[68:71], v[20:23], v[88:91], v[68:71]\n\t" \
  "v_mfma_f32_16x16x32_bf16 v[64:67], v[8:11], v[92:95], v[64:67]\n\t" \
  "v_mfma_f32_16x16x32_bf16 v[68:71], v[24:27], v[92:95], v[68:71]\n\t" \
  "v_mfma_f32_16x16x32_bf16 v[64:67], v[12:15], v[96:99], v[64:67]\n\t" \
  "v_mfma_f32_16x16x32_bf16 v[68:71], v[28:31], v[96:99], v[68:71]\n\t" \
  "s_waitcnt lgkmcnt(0)\n\t" \
  "s_barrier\n\t" \
  "v_mfma_f32_16x16x32_bf16 v[72:75], v[48:51], v[84:87], 0\n\t" \
  "v_mfma_f32_16x16x32_bf16 v[72:75], v[52:55], v[88:91], v[72:75]\n\t" \
  "v_mfma_f32_16x16x32_bf16 v[72:75], v[56:59], v[92:95], v[72:75]\n\t" \
  "v_mfma_f32_16x16x32_bf16 v[72:75], v[60:63], v[96:99], v[72:75]\n\t" \
  "v_mfma_f32_16x16x32_bf16 v[72:75], v[76:79], v[84:87], v[72:75]\n\t" \
  "s_waitcnt vmcnt(0)\n\t" \
  "ds_write_b128 v101, v[32:35] offset:22528\n\t" \
  "ds_write_b128 v101, v[36:39] offset:23552\n\t" \
  "ds_write_b128 v101, v[40:43] offset:24576\n\t" \
  "ds_read_b128 v[48:51], v103 offset:0\n\t" \
  "ds_read_b128 v[52:55], v103 offset:1024\n\t" \
  "ds_read_b128 v[56:59], v103 offset:2048\n\t" \
  "ds_read_b128 v[60:63], v103 offset:3072\n\t" \
  "ds_read_b128 v[16:19], v103 offset:16384\n\t" \
  "ds_read_b128 v[20:23], v103 offset:17408\n\t" \
  "ds_read_b128 v[24:27], v103 offset:18432\n\t" \
  "ds_read_b128 v[28:31], v103 offset:19456\n\t" \
  "ds_read_b128 v[84:87], v103 offset:49152\n\t" \
  "ds_read_b128 v[88:91], v103 offset:50176\n\t" \
  "ds_read_b128 v[92:95], v103 offset:51200\n\t" \
  "ds_read_b128 v[96:99], v103 offset:52224\n\t" \
  "ds_read_b128 v[0:3], v102 offset:16384\n\t" \
  "ds_read_b128 v[4:7], v102 offset:17408\n\t" \
  "ds_read_b128 v[8:11], v102 offset:18432\n\t" \
  "ds_read_b128 v[12:15], v102 offset:19456\n\t" \
  "v_max3_f32 v76, v64, v65, v66\n\t" \
  "v_max3_f32 v77, v67, v68, v69\n\t" \
  "v_max3_f32 v76, v76, v70, v71\n\t" \
  "v_max3_f32 v77, v77, v72, v73\n\t" \
  "v_max3_f32 v76, v76, v74, v75\n\t" \
  "v_max_f32 v76, v76, v77\n\t" \
  "v_mov_b32 v77, v76\n\t" \
  "s_nop 1\n\t" \
  "v_permlane16_swap_b32 v76, v77\n\t" \
  "v_max_f32 v76, v76, v77\n\t" \
  "v_mov_b32 v77, v76\n\t" \
  "s_nop 1\n\t" \
  "v_permlane32_swap_b32 v76, v77\n\t" \
  "v_max_f32 v76, v76, v77\n\t" \
  "v_mul_f32 v76, %[cs], v76\n\t" \
  "v_fma_f32 v64, v64, %[cs], -v76\n\t" \
  "v_fma_f32 v65, v65, %[cs], -v76\n\t" \
  "v_fma_f32 v66, v66, %[cs], -v76\n\t" \
  "v_fma_f32 v67, v67, %[cs], -v76\n\t" \
  "v_fma_f32 v68, v68, %[cs], -v76\n\t" \
  "v_fma_f32 v69, v69, %[cs], -v76\n\t" \
  "v_fma_f32 v70, v70, %[cs], -v76\n\t" \
  "v_fma_f32 v71, v71, %[cs], -v76\n\t" \
  "v_fma_f32 v72, v72, %[cs], -v76\n\t" \
  "v_fma_f32 v73, v73, %[cs], -v76\n\t" \
  "v_fma_f32 v74, v74, %[cs], -v76\n\t" \
  "v_fma_f32 v75, v75, %[cs], -v76\n\t" \
  "v_exp_f32 v64, v64\n\t" \
  "v_exp_f32 v65, v65\n\t" \
  "v_exp_f32 v66, v66\n\t" \
  "v_exp_f32 v67, v67\n\t" \
  "v_exp_f32 v68, v68\n\t" \
  "v_exp_f32 v69, v69\n\t" \
  "v_exp_f32 v70, v70\n\t" \
  "v_exp_f32 v71, v71\n\t" \
  "v_exp_f32 v72, v72\n\t" \
  "v_exp_f32 v73, v73\n\t" \
  "v_exp_f32 v74, v74\n\t" \
  "v_exp_f32 v75, v75\n\t" \
  "v_add_f32 v76, v72, v73\n\t" \
  "v_add_f32 v77, v74, v75\n\t" \
  "v_add_f32 v78, v64, v65\n\t" \
  "v_add_f32 v79, v66, v67\n\t" \
  "v_add_f32 v76, v76, v77\n\t" \
  "v_add_f32 v78, v78, v68\n\t" \
  "v_add_f32 v79, v79, v69\n\t" \
  "v_add_f32 v78, v78, v70\n\t" \
  "v_add_f32 v79, v79, v71\n\t" \
  "v_fma_f32 v76, v76, 0.5, v78\n\t" \
  "v_add_f32 v76, v76, v79\n\t" \
  "v_mov_b32 v77, v76\n\t" \
  "s_nop 1\n\t" \
  "v_permlane16_swap_b32 v76, v77\n\t" \
  "v_add_f32 v76, v76, v77\n\t" \
  "v_mov_b32 v77, v76\n\t" \
  "s_nop 1\n\t" \
  "v_permlane32_swap_b32 v76, v77\n\t" \
  "v_add_f32 v76, v76, v77\n\t" \
  "v_rcp_f32 v76, v76\n\t" \
  "s_mov_b64 exec, 0xff\n\t" \
  "buffer_load_dword v118, v112, %[rh], s96 offen sc1\n\t" \
  "s_mov_b64 exec, -1\n\t" \
  "v_mul_f32 v64, v64, v76\n\t" \
  "v_mul_f32 v65, v65, v76\n\t" \
  "v_mul_f32 v66, v66, v76\n\t" \
  "v_mul_f32 v67, v67, v76\n\t" \
  "v_mul_f32 v68, v68, v76\n\t" \
  "v_mul_f32 v69, v69, v76\n\t" \
  "v_mul_f32 v70, v70, v76\n\t" \
  "v_mul_f32 v71, v71, v76\n\t" \
  "v_mul_f32 v72, v72, v76\n\t" \
  "v_mul_f32 v73, v73, v76\n\t" \
  "v_mul_f32 v74, v74, v76\n\t" \
  "v_mul_f32 v75, v75, v76\n\t" \
  "v_cvt_pk_bf16_f32 v76, v64, v68\n\t" \
  "v_cvt_pk_bf16_f32 v77, v65, v69\n\t" \
  "v_cvt_pk_bf16_f32 v78, v66, v70\n\t" \
  "v_cvt_pk_bf16_f32 v79, v67, v71\n\t" \
  "v_cvt_pk_bf16_f32 v80, v72, v73\n\t" \
  "v_cvt_pk_bf16_f32 v81, v74, v75\n\t" \
  "v_permlane32_swap_b32 v76, v78\n\t" \
  "v_permlane32_swap_b32 v77, v79\n\t" \
  "v_mov_b64 v[82:83], v[80:81]\n\t" \
  "buffer_store_dwordx4 v[76:79], v110, %[rh], s94 offen sc1\n\t" \
  "s_nop 0\n\t" \
  "v_permlane16_swap_b32 v80, v82\n\t" \
  "v_permlane16_swap_b32 v81, v83\n\t" \
  "buffer_store_dwordx4 v[80:83], v111, %[rh], s94 offen sc1\n\t" \
  "s_add_i32 s77, s44, 1\n\t" \
  "s_min_u32 s77, s77, 63\n\t" \
  "v_cmp_gt_i32 vcc, s77, v119\n\t" \
  "s_cmp_lg_u64 vcc, 0\n\t" \
  "s_cselect_b32 s42, 32, 0\n\t" \
  "s_add_i32 s44, s44, 1\n\t" \
  "s_and_b32 s58, s58, 4\n\t" \
  "s_and_b32 s76, s44, 64\n\t" \
  "s_or_b32 s42, s42, s76\n\t" \
  "s_cbranch_scc0 20b\n\t" \
  "s_branch 99f\n\t" \
  "14:\n\t" \
  "s_sleep 3\n\t" \
  "s_add_u32 s59, s59, 0x10000\n\t" \
  "s_cmp_lt_u32 s59, 0x400000\n\t" \
  "s_cbranch_scc1 13b\n\t" \
  "s_mov_b64 exec, -1\n\t" \
  "s_mov_b32 s42, 16\n\t" \
  "s_branch 99f\n\t" \
  "8:\n\t" \
  "s_bitcmp1_b32 s58, 1\n\t" \
  "s_cbranch_scc1 4b\n\t" \
  "s_bitcmp1_b32 s58, 2\n\t" \
  "s_cbranch_scc1 5b\n\t" \
  "v_max3_u32 v116, v32, v33, v34\n\t" \
  "v_max3_u32 v117, v35, v36, v37\n\t" \
  "v_max3_u32 v116, v116, v38, v39\n\t" \
  "v_max_u32 v116, v116, v117\n\t" \
  "v_cmp_eq_u32 vcc, -1, v116\n\t" \
  "s_cbranch_vccz 5b\n\t" \
  "s_branch 0f\n\t" \
  "9:\n\t" \
  "s_bitcmp1_b32 s58, 2\n\t" \
  "s_cbranch_scc1 6b\n\t" \
  "v_max3_u32 v116, v40, v41, v42\n\t" \
  "v_max3_u32 v117, v43, v44, v45\n\t" \
  "v_max3_u32 v116, v116, v46, v47\n\t" \
  "v_max_u32 v116, v116, v117\n\t" \
  "v_cmp_eq_u32 vcc, -1, v116\n\t" \
  "s_cbranch_vccz 6b\n\t" \
  "s_or_b32 s58, s58, 2\n\t" \
  "0:\n\t" \
  "s_or_b32 s58, s58, 1\n\t" \
  "11:\n\t" \
  "buffer_load_dwordx4 v[32:35], v100, %[rh], s40 offen sc1\n\t" \
  "buffer_load_dwordx4 v[36:39], v100, %[rh], s76 offen sc1\n\t" \
  "buffer_load_dwordx4 v[40:43], v100, %[rh], s77 offen sc1\n\t" \
  "buffer_load_dwordx4 v[44:47], v100, %[rh], s78 offen sc1\n\t" \
  "s_waitcnt lgkmcnt(0)\n\t" \
  "s_branch 10b\n\t" \
  "17:\n\t" \
  "s_add_u32 s59, s59, 1\n\t" \
  "s_bitcmp1_b32 s58, 2\n\t" \
  "s_cbranch_scc1 3b\n\t" \
  "s_waitcnt vmcnt(0)\n\t" \
  "v_max_u32 v116, v32, v33\n\t" \
  "v_max3_u32 v116, v116, v34, v35\n\t" \
  "v_max3_u32 v116, v116, v36, v37\n\t" \
  "v_max3_u32 v116, v116, v38, v39\n\t" \
  "v_max3_u32 v116, v116, v40, v41\n\t" \
  "v_max3_u32 v116, v116, v42, v43\n\t" \
  "v_max3_u32 v116, v116, v44, v45\n\t" \
  "v_max3_u32 v116, v116, v46, v47\n\t" \
  "v_cmp_eq_u32 vcc, -1, v116\n\t" \
  "s_cbranch_vccz 3b\n\t" \
  "s_bitcmp1_b32 s59, 6\n\t" \
  "s_cbranch_scc1 12f\n\t" \
  "buffer_load_dwordx4 v[32:35], v100, %[rh], s40 offen sc1\n\t" \
  "buffer_load_dwordx4 v[36:39], v100, %[rh], s76 offen sc1\n\t" \
  "buffer_load_dwordx4 v[40:43], v100, %[rh], s77 offen sc1\n\t" \
  "buffer_load_dwordx4 v[44:47], v100, %[rh], s78 offen sc1\n\t" \
  "s_branch 17b\n\t" \
  "12:\n\t" \
  "s_mov_b32 s42, s58\n\t" \
  "s_branch 99f\n\t" \
  "93:\n\t" \
  "s_mov_b32 s58, s42\n\t" \
  "20:\n\t" \
  "s_and_b32 s76, s44, 7\n\t" \
  "s_lshl_b32 s76, s76, 15\n\t" \
  "s_add_i32 s40, s54, s76\n\t" \
  "s_and_b32 s76, s44, 3\n\t" \
  "s_lshl_b32 s45, s76, 14\n\t" \
  "s_add_i32 s47, s44, 1\n\t" \
  "s_min_u32 s47, s47, 63\n\t" \
  "s_lshr_b32 s76, s47, 1\n\t" \
  "s_lshl_b32 s47, s47, 20\n\t" \
  "s_add_i32 s47, s47, s55\n\t" \
  "s_bitcmp1_b64 %[sel], s76\n\t" \
  "s_cselect_b32 s76, 0x2000, 0\n\t" \
  "s_add_i32 s47, s47, s76\n\t" \
  "s_add_i32 s49, s44, -1\n\t" \
  "s_and_b32 s48, s49, 2\n\t" \
  "s_lshl_b32 s48, s48, 13\n\t" \
  "s_and_b32 s76, s49, 1\n\t" \
  "s_lshl_b32 s76, s76, 12\n\t" \
  "s_add_i32 s48, s48, s76\n\t" \
  "s_lshl_b32 s50, s53, 8\n\t" \
  "s_mov_b32 s59, 0\n\t" \
  "s_mov_b32 s92, s51\n\t" \
  "s_mov_b32 s93, s53\n\t" \
  "s_xor_b32 s76, s40, 0x400\n\t" \
  "s_xor_b32 s77, s40, 0x800\n\t" \
  "s_xor_b32 s78, s40, 0xc00\n\t" \
  "s_cmp_lg_u32 s58, 0\n\t" \
  "s_cbranch_scc1 11f\n\t" \
  "10:\n\t" \
  "ds_read_b128 v[48:51], v103 offset:32768\n\t" \
  "ds_read_b128 v[52:55], v103 offset:33792\n\t" \
  "ds_read_b128 v[56:59], v103 offset:34816\n\t" \
  "ds_read_b128 v[60:63], v103 offset:35840\n\t" \
  "ds_read_b128 v[16:19], v103 offset:49152\n\t" \
  "ds_read_b128 v[20:23], v103 offset:50176\n\t" \
  "ds_read_b128 v[24:27], v103 offset:51200\n\t" \
  "ds_read_b128 v[28:31], v103 offset:52224\n\t" \
  "ds_read_b128 v[84:87], v102 offset:16384\n\t" \
  "ds_read_b128 v[88:91], v102 offset:17408\n\t" \
  "ds_read_b128 v[92:95], v102 offset:18432\n\t" \
  "ds_read_b128 v[96:99], v102 offset:19456\n\t" \
  "s_waitcnt lgkmcnt(4)\n\t" \
  "ds_read_b128 v[0:3], v102 offset:49152\n\t" \
  "ds_read_b128 v[4:7], v102 offset:50176\n\t" \
  "ds_read_b128 v[8:11], v102 offset:51200\n\t" \
  "ds_read_b128 v[12:15], v102 offset:52224\n\t" \
  "46:\n\t" \
  "s_bitcmp1_b32 s58, 1\n\t" \
  "s_cselect_b64 s[80:81], -1, 0\n\t" \
  "s_cmp_lg_u32 s58, 0\n\t" \
  "s_cbranch_scc1 17f\n\t" \
  "s_mov_b64 exec, 0xff\n\t" \
  "16:\n\t" \
  "s_memtime vcc\n\t" \
  "s_waitcnt lgkmcnt(0)\n\t" \
  "s_sub_i32 s79, s41, vcc_lo\n\t" \
  "s_cmp_gt_i32 s79, 64\n\t" \
  "s_cbranch_scc1 16b\n\t" \
  "13:\n\t" \
  "buffer_load_dword v116, v112, %[rh], s48 offen sc1\n\t" \
  "s_memtime vcc\n\t" \
  "s_memrealtime s[82:83]\n\t" \
  "s_nop 4\n\t" \
  "s_memrealtime s[84:85]\n\t" \
  "s_nop 4\n\t" \
  "s_memrealtime s[56:57]\n\t" \
  "s_nop 4\n\t" \
  "s_memrealtime s[86:87]\n\t" \
  "s_nop 4\n\t" \
  "s_memrealtime s[88:89]\n\t" \
  "s_nop 4\n\t" \
  "s_memrealtime s[90:91]\n\t" \
  "s_mov_b32 s92, 0\n\t" \
  "s_waitcnt lgkmcnt(0)\n\t" \
  "s_cmp_eq_u32 s82, s84\n\t" \
  "s_addc_u32 s92, s92, 0\n\t" \
  "s_cmp_eq_u32 s82, s56\n\t" \
  "s_addc_u32 s92, s92, 0\n\t" \
  "s_cmp_eq_u32 s82, s86\n\t" \
  "s_addc_u32 s92, s92, 0\n\t" \
  "s_cmp_eq_u32 s82, s88\n\t" \
  "s_addc_u32 s92, s92, 0\n\t" \
  "s_cmp_eq_u32 s82, s90\n\t" \
  "s_addc_u32 s92, s92, 0\n\t" \
  "s_mul_i32 s92, s92, 24\n\t" \
  "s_add_i32 s92, s92, vcc_lo\n\t" \
  "s_lshl4_add_u32 s93, s82, 64\n\t" \
  "s_lshl_b32 s50, s93, 8\n\t" \
  "s_waitcnt vmcnt(0)\n\t" \
  "15:\n\t" \
  "v_mov_b32 v117, 20000\n\t" \
  "v_cmp_ne_u32_sdwa vcc, v116, s49 src0_sel:BYTE_0 src1_sel:DWORD\n\t" \
  "v_subrev_u32 v116, s50, v116\n\t" \
  "v_ashrrev_i32 v116, 8, v116\n\t" \
  "s_nop 1\n\t" \
  "v_max_i32_dpp v116, v116, v116 quad_perm:[1,0,3,2] row_mask:0xf bank_mask:0xf\n\t" \
  "s_cbranch_vccnz 14f\n\t" \
  "s_nop 0\n\t" \
  "v_max_i32_dpp v116, v116, v116 quad_perm:[2,3,0,1] row_mask:0xf bank_mask:0xf\n\t" \
  "s_nop 1\n\t" \
  "v_max_i32_dpp v116, v116, v116 row_half_mirror row_mask:0xf bank_mask:0xf\n\t" \
  "v_med3_i32 v116, v116, 0, v117\n\t" \
  "v_mul_u32_u24 v116, s52, v116\n\t" \
  "v_lshrrev_b32 v116, 16, v116\n\t" \
  "s_mov_b64 exec, -1\n\t" \
  "v_readfirstlane_b32 s84, v116\n\t" \
  "s_add_i32 s84, s84, s92\n\t" \
  ".p2align 6\n\t" \
  "s_nop 0\n\t" \
  "s_nop 0\n\t" \
  "s_nop 0\n\t" \
  "s_nop 0\n\t" \
  "s_nop 0\n\t" \
  "s_nop 0\n\t" \
  "s_nop 0\n\t" \
  "s_nop 0\n\t" \
  "1:\n\t" \
  "s_memtime vcc\n\t" \
  "s_waitcnt lgkmcnt(0)\n\t" \
  "s_sub_i32 s79, s84, vcc_lo\n\t" \
  "s_cmp_gt_i32 s79, 128\n\t" \
  "s_cbranch_scc1 1b\n\t" \
  ".p2align 4\n\t" \
  "2:\n\t" \
  "s_sub_i32 s79, s79, 33\n\t" \
  "s_cmp_gt_i32 s79, 56\n\t" \
  "s_cbranch_scc1 2b\n\t" \
  "buffer_load_dwordx4 v[32:35], v100, %[rh], s40 offen sc1\n\t" \
  "s_nop 12\n\t" \
  "buffer_load_dwordx4 v[36:39], v100, %[rh], s76 offen sc1\n\t" \
  "s_nop 12\n\t" \
  "buffer_load_dwordx4 v[40:43], v100, %[rh], s77 offen sc1\n\t" \
  "s_nop 12\n\t" \
  "buffer_load_dwordx4 v[44:47], v100, %[rh], s78 offen sc1\n\t" \
  "3:\n\t" \
  "v_mov_b64 v[68:69], 0\n\t" \
  "v_mov_b64 v[70:71], 0\n\t" \
  "v_mov_b64 v[72:73], 0\n\t" \
  "v_mov_b64 v[74:75], 0\n\t" \
  "v_mov_b64 v[76:77], 0\n\t" \
  "v_mov_b64 v[78:79], 0\n\t" \
  "v_mov_b64 v[80:81], 0\n\t" \
  "v_mov_b64 v[82:83], 0\n\t" \
  "s_waitcnt lgkmcnt(0)\n\t" \
  "s_waitcnt vmcnt(3)\n\t" \
  "v_smfmac_f32_16x16x64_bf16 v[68:71], v[32:35], v[152:159], v104\n\t" \
  "ds_write_b128 v101, v[32:35] offset:12288\n\t" \
  "v_smfmac_f32_16x16x64_bf16 v[72:75], v[32:35], v[160:167], v104\n\t" \
  "v_smfmac_f32_16x16x64_bf16 v[76:79], v[32:35], v[48:55], v104\n\t" \
  "v_smfmac_f32_16x16x64_bf16 v[80:83], v[32:35], v[56:63], v104\n\t" \
  "s_waitcnt vmcnt(2)\n\t" \
  "v_smfmac_f32_16x16x64_bf16 v[68:71], v[36:39], v[168:175], v104\n\t" \
  "ds_write_b128 v101, v[36:39] offset:13312\n\t" \
  "v_smfmac_f32_16x16x64_bf16 v[72:75], v[36:39], v[176:183], v104\n\t" \
  "v_smfmac_f32_16x16x64_bf16 v[76:79], v[36:39], v[184:191], v104\n\t" \
  "v_cmp_u_f32 vcc, v68, v69\n\t" \
  "v_smfmac_f32_16x16x64_bf16 v[80:83], v[36:39], v[192:199], v104\n\t" \
  "v_cmp_u_f32 s[82:83], v70, v71\n\t" \
  "s_waitcnt vmcnt(1)\n\t" \
  "v_smfmac_f32_16x16x64_bf16 v[68:71], v[40:43], v[16:23], v104\n\t" \
  "ds_write_b128 v101, v[40:43] offset:14336\n\t" \
  "v_smfmac_f32_16x16x64_bf16 v[72:75], v[40:43], v[24:31], v104\n\t" \
  "s_or_b64 vcc, vcc, s[82:83]\n\t" \
  "s_or_b64 vcc, vcc, s[80:81]\n\t" \
  "s_waitcnt lgkmcnt(1)\n\t" \
  "v_smfmac_f32_16x16x64_bf16 v[76:79], v[40:43], v[200:207], v104\n\t" \
  "s_cbranch_scc1 8f\n\t" \
  "5:\n\t" \
  "s_barrier\n\t" \
  "4:\n\t" \
  "s_memtime s[56:57]\n\t" \
  "ds_read_b128 v[48:51], v101 offset:0\n\t" \
  "v_smfmac_f32_16x16x64_bf16 v[80:83], v[40:43], v[208:215], v104\n\t" \
  "ds_read_b128 v[52:55], v101 offset:1024\n\t" \
  "ds_read_b128 v[56:59], v101 offset:4096\n\t" \
  "s_waitcnt vmcnt(0)\n\t" \
  "v_smfmac_f32_16x16x64_bf16 v[68:71], v[44:47], v[216:223], v104\n\t" \
  "v_smfmac_f32_16x16x64_bf16 v[72:75], v[44:47], v[224:231], v104\n\t" \
  "ds_write_b128 v101, v[44:47] offset:15360\n\t" \
  "v_smfmac_f32_16x16x64_bf16 v[76:79], v[44:47], v[232:239], v104\n\t" \
  "v_cmp_u_f32 vcc, v68, v69\n\t" \
  "v_smfmac_f32_16x16x64_bf16 v[80:83], v[44:47], v[240:247], v104\n\t" \
  "v_cmp_u_f32 s[82:83], v70, v71\n\t" \
  "s_waitcnt lgkmcnt(3)\n\t" \
  "v_smfmac_f32_16x16x64_bf16 v[68:71], v[48:51], a[0:7], v104\n\t" \
  "v_smfmac_f32_16x16x64_bf16 v[72:75], v[48:51], a[8:15], v104\n\t" \
  "s_or_b64 vcc, vcc, s[82:83]\n\t" \
  "s_waitcnt lgkmcnt(0)\n\t" \
  "v_smfmac_f32_16x16x64_bf16 v[68:71], v[52:55], a[16:23], v104\n\t" \
  "s_cbranch_scc1 9f\n\t" \
  "6:\n\t" \
  "s_barrier\n\t" \
  "ds_read_b128 v[60:63], v101 offset:5120\n\t" \
  "v_smfmac_f32_16x16x64_bf16 v[72:75], v[52:55], a[24:31], v104\n\t" \
  "ds_read_b128 v[64:67], v101 offset:8192\n\t" \
  "v_smfmac_f32_16x16x64_bf16 v[68:71], v[56:59], a[96:103], v104\n\t" \
  "v_smfmac_f32_16x16x64_bf16 v[72:75], v[56:59], a[104:111], v104\n\t" \
  "ds_read_b128 v[48:51], v101 offset:9216\n\t" \
  "s_waitcnt lgkmcnt(2)\n\t" \
  "v_smfmac_f32_16x16x64_bf16 v[68:71], v[60:63], a[112:119], v104\n\t" \
  "ds_read_b128 v[52:55], v101 offset:2048\n\t" \
  "v_smfmac_f32_16x16x64_bf16 v[72:75], v[60:63], a[120:127], v104\n\t" \
  "ds_read_b128 v[56:59], v101 offset:3072\n\t" \
  "s_waitcnt lgkmcnt(3)\n\t" \
  "v_smfmac_f32_16x16x64_bf16 v[68:71], v[64:67], a[192:199], v104\n\t" \
  "ds_read_b128 v[60:63], v101 offset:6144\n\t" \
  "v_smfmac_f32_16x16x64_bf16 v[72:75], v[64:67], a[200:207], v104\n\t" \
  "s_waitcnt lgkmcnt(3)\n\t" \
  "v_smfmac_f32_16x16x64_bf16 v[68:71], v[48:51], a[208:215], v104\n\t" \
  "ds_read_b128 v[64:67], v101 offset:7168\n\t" \
  "ds_read_b128 v[16:19], v103 offset:16384\n\t" \
  "v_smfmac_f32_16x16x64_bf16 v[72:75], v[48:51], a[216:223], v104\n\t" \
  "s_waitcnt lgkmcnt(4)\n\t" \
  "v_smfmac_f32_16x16x64_bf16 v[68:71], v[52:55], v[84:91], v104\n\t" \
  "ds_read_b128 v[48:51], v101 offset:11264\n\t" \
  "ds_read_b128 v[20:23], v103 offset:17408\n\t" \
  "v_smfmac_f32_16x16x64_bf16 v[72:75], v[52:55], v[92:99], v104\n\t" \
  "s_waitcnt lgkmcnt(5)\n\t" \
  "v_smfmac_f32_16x16x64_bf16 v[68:71], v[56:59], a[64:71], v104\n\t" \
  "ds_read_b128 v[52:55], v101 offset:10240\n\t" \
  "ds_read_b128 v[24:27], v103 offset:18432\n\t" \
  "v_smfmac_f32_16x16x64_bf16 v[72:75], v[56:59], a[72:79], v104\n\t" \
  "ds_read_b128 v[28:31], v103 offset:19456\n\t" \
  "s_waitcnt lgkmcnt(7)\n\t" \
  "v_smfmac_f32_16x16x64_bf16 v[68:71], v[60:63], v[0:7], v104\n\t" \
  "ds_read_b128 v[84:87], v103 offset:22528\n\t" \
  "v_smfmac_f32_16x16x64_bf16 v[72:75], v[60:63], v[8:15], v104\n\t" \
  "ds_read_b128 v[88:91], v103 offset:23552\n\t" \
  "s_waitcnt lgkmcnt(8)\n\t" \
  "v_smfmac_f32_16x16x64_bf16 v[68:71], v[64:67], a[160:167], v104\n\t" \
  "ds_read_b128 v[0:3], v102 offset:0\n\t" \
  "v_smfmac_f32_16x16x64_bf16 v[72:75], v[64:67], a[168:175], v104\n\t" \
  "ds_read_b128 v[4:7], v102 offset:1024\n\t" \
  "s_waitcnt lgkmcnt(8)\n\t" \
  "v_smfmac_f32_16x16x64_bf16 v[68:71], v[48:51], v[120:127], v104\n\t" \
  "ds_read_b128 v[92:95], v102 offset:22528\n\t" \
  "s_waitcnt lgkmcnt(7)\n\t" \
  "v_smfmac_f32_16x16x64_bf16 v[68:71], v[52:55], v[16:23], v104\n\t" \
  "ds_read_b128 v[8:11], v102 offset:2048\n\t" \
  "v_smfmac_f32_16x16x64_bf16 v[72:75], v[48:51], v[128:135], v104\n\t" \
  "ds_read_b128 v[12:15], v102 offset:3072\n\t" \
  "s_waitcnt lgkmcnt(7)\n\t" \
  "v_smfmac_f32_16x16x64_bf16 v[72:75], v[52:55], v[24:31], v104\n\t" \
  "v_add_f32 v44, v68, v70\n\t" \
  "ds_read_b128 v[96:99], v102 offset:23552\n\t" \
  "v_smfmac_f32_16x16x64_bf16 v[76:79], v[56:59], a[80:87], v104\n\t" \
  "v_add_f32 v45, v69, v71\n\t" \
  "v_cvt_pk_bf16_f32 v44, v44, v45\n\t" \
  "v_smfmac_f32_16x16x64_bf16 v[80:83], v[56:59], a[88:95], v104\n\t" \
  "v_add_f32 v46, v72, v74\n\t" \
  "v_add_f32 v47, v73, v75\n\t" \
  "v_smfmac_f32_16x16x64_bf16 v[76:79], v[60:63], a[144:151], v104\n\t" \
  "v_cvt_pk_bf16_f32 v46, v46, v47\n\t" \
  "ds_write2st64_b32 v105, v44, v46 offset1:1\n\t" \
  "v_smfmac_f32_16x16x64_bf16 v[80:83], v[60:63], a[152:159], v104\n\t" \
  "ds_read_b128 v[56:59], v101 offset:0\n\t" \
  "v_mov_b64 v[68:69], 0\n\t" \
  "v_smfmac_f32_16x16x64_bf16 v[76:79], v[64:67], a[176:183], v104\n\t" \
  "ds_read_b128 v[16:19], v102 offset:32768\n\t" \
  "v_mov_b64 v[70:71], 0\n\t" \
  "v_smfmac_f32_16x16x64_bf16 v[80:83], v[64:67], a[184:191], v104\n\t" \
  "ds_read_b128 v[20:23], v102 offset:33792\n\t" \
  "v_smfmac_f32_16x16x64_bf16 v[76:79], v[48:51], v[136:143], v104\n\t" \
  "s_waitcnt lgkmcnt(3)\n\t" \
  "s_barrier\n\t" \
  "v_smfmac_f32_16x16x64_bf16 v[80:83], v[48:51], v[144:151], v104\n\t" \
  "v_smfmac_f32_16x16x64_bf16 v[68:71], v[32:35], v[84:91], v104\n\t" \
  "v_smfmac_f32_16x16x64_bf16 v[76:79], v[52:55], a[240:247], v104\n\t" \
  "ds_read_b128 v[60:63], v101 offset:9216\n\t" \
  "ds_read_b128 v[24:27], v102 offset:34816\n\t" \
  "v_smfmac_f32_16x16x64_bf16 v[80:83], v[52:55], a[248:255], v104\n\t" \
  "ds_read_b128 v[64:67], v101 offset:2048\n\t" \
  "s_waitcnt lgkmcnt(5)\n\t" \
  "v_smfmac_f32_16x16x64_bf16 v[76:79], v[56:59], v[0:7], v104\n\t" \
  "ds_read_b128 v[48:51], v101 offset:4096\n\t" \
  "ds_read_b128 v[28:31], v102 offset:35840\n\t" \
  "v_smfmac_f32_16x16x64_bf16 v[80:83], v[56:59], v[8:15], v104\n\t" \
  "ds_read_b128 v[84:87], v102 offset:55296\n\t" \
  "global_load_dword v119, v109, %[kc] sc1\n\t" \
  "s_waitcnt lgkmcnt(5)\n\t" \
  "v_smfmac_f32_16x16x64_bf16 v[76:79], v[60:63], a[224:231], v104\n\t" \
  "ds_read_b128 v[52:55], v101 offset:1024\n\t" \
  "ds_read_b128 v[88:91], v102 offset:56320\n\t" \
  "v_smfmac_f32_16x16x64_bf16 v[80:83], v[60:63], a[232:239], v104\n\t" \
  "ds_read_b128 v[0:3], v103 offset:0\n\t" \
  "s_waitcnt lgkmcnt(6)\n\t" \
  "v_smfmac_f32_16x16x64_bf16 v[76:79], v[64:67], a[48:55], v104\n\t" \
  "ds_read_b128 v[56:59], v101 offset:5120\n\t" \
  "buffer_load_dwordx4 v[32:35], v100, %[ru], s47 offen offset:0 sc1\n\t" \
  "v_smfmac_f32_16x16x64_bf16 v[80:83], v[64:67], a[56:63], v104\n\t" \
  "ds_read_b128 v[60:63], v101 offset:8192\n\t" \
  "ds_read_b128 v[4:7], v103 offset:1024\n\t" \
  "s_waitcnt lgkmcnt(8)\n\t" \
  "v_smfmac_f32_16x16x64_bf16 v[76:79], v[48:51], v[16:23], v104\n\t" \
  "ds_read_b128 v[8:11], v103 offset:2048\n\t" \
  "s_waitcnt lgkmcnt(8)\n\t" \
  "v_smfmac_f32_16x16x64_bf16 v[80:83], v[48:51], v[24:31], v104\n\t" \
  "ds_read_b128 v[12:15], v103 offset:3072\n\t" \
  "buffer_load_dwordx4 v[36:39], v100, %[ru], s47 offen offset:1024 sc1\n\t" \
  "v_smfmac_f32_16x16x64_bf16 v[68:71], v[48:51], v[92:99], v104\n\t" \
  "s_waitcnt lgkmcnt(7)\n\t" \
  "v_smfmac_f32_16x16x64_bf16 v[76:79], v[52:55], a[32:39], v104\n\t" \
  "v_smfmac_f32_16x16x64_bf16 v[80:83], v[52:55], a[40:47], v104\n\t" \
  "s_waitcnt lgkmcnt(4)\n\t" \
  "v_smfmac_f32_16x16x64_bf16 v[76:79], v[56:59], a[128:135], v104\n\t" \
  "buffer_load_dwordx4 v[40:43], v100, %[ru], s47 offen offset:2048 sc1\n\t" \
  "s_waitcnt lgkmcnt(2)\n\t" \
  "v_smfmac_f32_16x16x64_bf16 v[76:79], v[60:63], v[0:7], v104\n\t" \
  "v_smfmac_f32_16x16x64_bf16 v[68:71], v[60:63], v[84:91], v104\n\t" \
  "v_smfmac_f32_16x16x64_bf16 v[80:83], v[56:59], a[136:143], v104\n\t" \
  "s_waitcnt lgkmcnt(0)\n\t" \
  "v_smfmac_f32_16x16x64_bf16 v[80:83], v[60:63], v[8:15], v104\n\t" \
  "v_add_f32 v20, v76, v78\n\t" \
  "v_add_f32 v21, v77, v79\n\t" \
  "v_cvt_pk_bf16_f32 v20, v20, v21\n\t" \
  "ds_write_b32 v106, v20\n\t" \
  "v_add_f32 v72, v68, v70\n\t" \
  "v_add_f32 v73, v69, v71\n\t" \
  "v_cvt_pk_bf16_f32 v72, v72, v73\n\t" \
  "ds_write_b32 v106, v72 offset:11008\n\t" \
  "v_add_f32 v23, v80, v82\n\t" \
  "v_add_f32 v21, v81, v83\n\t" \
  "v_cvt_pk_bf16_f32 v21, v23, v21\n\t" \
  "ds_write_b32 v106, v21 offset:256\n\t" \
  "s_waitcnt lgkmcnt(0)\n\t" \
  "s_barrier\n\t" \
  "s_waitcnt vmcnt(0)\n\t" \
  "s_barrier\n\t" \
  "ds_write_b128 v101, v[32:35] offset:25600\n\t" \
  "ds_write_b128 v101, v[36:39] offset:26624\n\t" \
  "ds_write_b128 v101, v[40:43] offset:27648\n\t" \
  "s_add_i32 s77, s44, 1\n\t" \
  "s_min_u32 s77, s77, 63\n\t" \
  "v_cmp_gt_i32 vcc, s77, v119\n\t" \
  "s_cmp_lg_u64 vcc, 0\n\t" \
  "s_cselect_b32 s42, 32, 0\n\t" \
  "s_add_i32 s41, s56, 1950\n\t" \
  "s_sub_i32 s76, s92, s51\n\t" \
  "s_sub_i32 s77, s93, s53\n\t" \
  "s_cmp_eq_u32 s76, 0\n\t" \
  "s_cbranch_scc1 21f\n\t" \
  "v_cvt_f32_u32 v117, s76\n\t" \
  "v_cvt_f32_u32 v116, s77\n\t" \
  "v_rcp_f32 v117, v117\n\t" \
  "s_mov_b32 s51, s92\n\t" \
  "v_mul_f32 v117, v117, v116\n\t" \
  "v_max_f32 v117, 0x3f028f5c, v117\n\t" \
  "v_min_f32 v117, 0x3ffeb852, v117\n\t" \
  "v_rcp_f32 v116, v117\n\t" \
  "v_mul_f32 v117, 0x4f000000, v117\n\t" \
  "v_mul_f32 v116, 0x47800000, v116\n\t" \
  "v_cvt_u32_f32 v117, v117\n\t" \
  "v_cvt_u32_f32 v116, v116\n\t" \
  "s_mov_b32 s53, s93\n\t" \
  "v_readfirstlane_b32 s46, v117\n\t" \
  "v_readfirstlane_b32 s52, v116\n\t" \
  "21:\n\t" \
  "v_mov_b32 v0, -1\n\t" \
  "v_mov_b32 v1, -1\n\t" \
  "v_mov_b32 v2, -1\n\t" \
  "v_mov_b32 v3, -1\n\t" \
  "s_and_b32 s76, s49, 3\n\t" \
  "s_mul_i32 s76, s76, 0xc000\n\t" \
  "s_add_i32 s76, s76, 0x40000\n\t" \
  "buffer_store_dwordx4 v[0:3], v110, %[rh], s76 offen sc1\n\t" \
  "s_add_i32 s76, s44, 1\n\t" \
  "s_and_b32 s76, s76, 3\n\t" \
  "s_lshl_b32 s76, s76, 18\n\t" \
  "s_add_i32 s77, s76, 0x10000\n\t" \
  "buffer_store_dwordx4 v[0:3], v107, %[rh], s76 offen sc1\n\t" \
  "buffer_store_dwordx4 v[0:3], v107, %[rh], s77 offen sc1\n\t" \
  "buffer_store_dwordx4 v[0:3], v111, %[rh], s76 offen sc1\n\t" \
  "s_add_i32 s44, s44, 1\n\t" \
  "s_and_b32 s58, s58, 4\n\t" \
  "s_and_b32 s76, s44, 64\n\t" \
  "s_or_b32 s42, s42, s76\n\t" \
  "s_cbranch_scc0 20b\n\t" \
  "s_branch 99f\n\t" \
  "14:\n\t" \
  "s_sleep 3\n\t" \
  "s_add_u32 s59, s59, 0x10000\n\t" \
  "s_cmp_lt_u32 s59, 0x400000\n\t" \
  "s_cbranch_scc1 13b\n\t" \
  "s_mov_b64 exec, -1\n\t" \
  "s_mov_b32 s42, 16\n\t" \
  "s_branch 99f\n\t" \
  "8:\n\t" \
  "s_bitcmp1_b32 s58, 1\n\t" \
  "s_cbranch_scc1 4b\n\t" \
  "s_bitcmp1_b32 s58, 2\n\t" \
  "s_cbranch_scc1 5b\n\t" \
  "v_max3_u32 v116, v32, v33, v34\n\t" \
  "v_max3_u32 v117, v35, v36, v37\n\t" \
  "v_max3_u32 v116, v116, v38, v39\n\t" \
  "v_max_u32 v116, v116, v117\n\t" \
  "v_cmp_eq_u32 vcc, -1, v116\n\t" \
  "s_cbranch_vccz 5b\n\t" \
  "s_branch 0f\n\t" \
  "9:\n\t" \
  "s_bitcmp1_b32 s58, 2\n\t" \
  "s_cbranch_scc1 6b\n\t" \
  "v_max3_u32 v116, v40, v41, v42\n\t" \
  "v_max3_u32 v117, v43, v44, v45\n\t" \
  "v_max3_u32 v116, v116, v46, v47\n\t" \
  "v_max_u32 v116, v116, v117\n\t" \
  "v_cmp_eq_u32 vcc, -1, v116\n\t" \
  "s_cbranch_vccz 6b\n\t" \
  "s_or_b32 s58, s58, 2\n\t" \
  "0:\n\t" \
  "s_or_b32 s58, s58, 1\n\t" \
  "11:\n\t" \
  "buffer_load_dwordx4 v[32:35], v100, %[rh], s40 offen sc1\n\t" \
  "buffer_load_dwordx4 v[36:39], v100, %[rh], s76 offen sc1\n\t" \
  "buffer_load_dwordx4 v[40:43], v100, %[rh], s77 offen sc1\n\t" \
  "buffer_load_dwordx4 v[44:47], v100, %[rh], s78 offen sc1\n\t" \
  "s_waitcnt lgkmcnt(0)\n\t" \
  "s_branch 10b\n\t" \
  "17:\n\t" \
  "s_add_u32 s59, s59, 1\n\t" \
  "s_bitcmp1_b32 s58, 2\n\t" \
  "s_cbranch_scc1 3b\n\t" \
  "s_waitcnt vmcnt(0)\n\t" \
  "v_max_u32 v116, v32, v33\n\t" \
  "v_max3_u32 v116, v116, v34, v35\n\t" \
  "v_max3_u32 v116, v116, v36, v37\n\t" \
  "v_max3_u32 v116, v116, v38, v39\n\t" \
  "v_max3_u32 v116, v116, v40, v41\n\t" \
  "v_max3_u32 v116, v116, v42, v43\n\t" \
  "v_max3_u32 v116, v116, v44, v45\n\t" \
  "v_max3_u32 v116, v116, v46, v47\n\t" \
  "v_cmp_eq_u32 vcc, -1, v116\n\t" \
  "s_cbranch_vccz 3b\n\t" \
  "s_bitcmp1_b32 s59, 6\n\t" \
  "s_cbranch_scc1 12f\n\t" \
  "buffer_load_dwordx4 v[32:35], v100, %[rh], s40 offen sc1\n\t" \
  "buffer_load_dwordx4 v[36:39], v100, %[rh], s76 offen sc1\n\t" \
  "buffer_load_dwordx4 v[40:43], v100, %[rh], s77 offen sc1\n\t" \
  "buffer_load_dwordx4 v[44:47], v100, %[rh], s78 offen sc1\n\t" \
  "s_branch 17b\n\t" \
  "12:\n\t" \
  "s_mov_b32 s42, s58\n\t" \
  "s_branch 99f\n\t" \
  "99:\n\t" \
  "s_waitcnt vmcnt(0) lgkmcnt(0)\n\t" \
  ""
#define VO_BODY \
  "s_cmp_eq_u32 %[wv], 1\n\t" \
  "s_cbranch_scc1 91f\n\t" \
  "s_cmp_eq_u32 %[wv], 2\n\t" \
  "s_cbranch_scc1 92f\n\t" \
  "s_cmp_eq_u32 %[wv], 3\n\t" \
  "s_cbranch_scc1 93f\n\t" \
  "90:\n\t" \
  "s_mov_b32 s58, s42\n\t" \
  "20:\n\t" \
  "s_and_b32 s76, s44, 7\n\t" \
  "s_lshl_b32 s76, s76, 15\n\t" \
  "s_add_i32 s40, s54, s76\n\t" \
  "s_and_b32 s76, s44, 3\n\t" \
  "s_lshl_b32 s45, s76, 14\n\t" \
  "s_lshl_b32 s47, s76, 20\n\t" \
  "s_add_i32 s47, s47, s55\n\t" \
  "s_lshr_b32 s76, s47, 21\n\t" \
  "s_bitcmp1_b64 %[sel], s76\n\t" \
  "s_cselect_b32 s76, 0x2000, 0\n\t" \
  "s_add_i32 s47, s47, s76\n\t" \
  "s_add_i32 s49, s44, -1\n\t" \
  "s_and_b32 s48, s49, 2\n\t" \
  "s_lshl_b32 s48, s48, 13\n\t" \
  "s_and_b32 s76, s49, 1\n\t" \
  "s_lshl_b32 s76, s76, 12\n\t" \
  "s_add_i32 s48, s48, s76\n\t" \
  "s_lshl_b32 s50, s53, 8\n\t" \
  "s_mov_b32 s59, 0\n\t" \
  "s_mov_b32 s92, s51\n\t" \
  "s_mov_b32 s93, s53\n\t" \
  "s_xor_b32 s76, s40, 0x400\n\t" \
  "s_xor_b32 s77, s40, 0x800\n\t" \
  "s_xor_b32 s78, s40, 0xc00\n\t" \
  "s_cmp_lg_u32 s58, 0\n\t" \
  "s_cbranch_scc1 11f\n\t" \
  "10:\n\t" \
  "ds_read_b128 v[48:51], v102 offset:0\n\t" \
  "ds_read_b128 v[52:55], v102 offset:1024\n\t" \
  "ds_read_b128 v[56:59], v102 offset:2048\n\t" \
  "ds_read_b128 v[60:63], v102 offset:3072\n\t" \
  "ds_read_b128 v[0:3], v102 offset:16384\n\t" \
  "ds_read_b128 v[4:7], v102 offset:17408\n\t" \
  "ds_read_b128 v[8:11], v102 offset:18432\n\t" \
  "ds_read_b128 v[12:15], v102 offset:19456\n\t" \
  "ds_read_b128 v[16:19], v102 offset:32768\n\t" \
  "ds_read_b128 v[20:23], v102 offset:33792\n\t" \
  "ds_read_b128 v[24:27], v102 offset:34816\n\t" \
  "ds_read_b128 v[28:31], v102 offset:35840\n\t" \
  "s_waitcnt lgkmcnt(4)\n\t" \
  "ds_read_b128 v[80:83], v102 offset:49152\n\t" \
  "ds_read_b128 v[84:87], v102 offset:50176\n\t" \
  "ds_read_b128 v[88:91], v102 offset:51200\n\t" \
  "ds_read_b128 v[92:95], v102 offset:52224\n\t" \
  "46:\n\t" \
  "s_bitcmp1_b32 s58, 1\n\t" \
  "s_cselect_b64 s[80:81], -1, 0\n\t" \
  "s_cmp_lg_u32 s58, 0\n\t" \
  "s_cbranch_scc1 17f\n\t" \
  "s_mov_b64 exec, 0xff\n\t" \
  "16:\n\t" \
  "s_memtime vcc\n\t" \
  "s_waitcnt lgkmcnt(0)\n\t" \
  "s_sub_i32 s79, s41, vcc_lo\n\t" \
  "s_cmp_gt_i32 s79, 64\n\t" \
  "s_cbranch_scc1 16b\n\t" \
  "13:\n\t" \
  "buffer_load_dword v116, v112, %[rh], s48 offen sc1\n\t" \
  "s_memtime vcc\n\t" \
  "s_memrealtime s[82:83]\n\t" \
  "s_nop 4\n\t" \
  "s_memrealtime s[84:85]\n\t" \
  "s_nop 4\n\t" \
  "s_memrealtime s[56:57]\n\t" \
  "s_nop 4\n\t" \
  "s_memrealtime s[86:87]\n\t" \
  "s_nop 4\n\t" \
  "s_memrealtime s[88:89]\n\t" \
  "s_nop 4\n\t" \
  "s_memrealtime s[90:91]\n\t" \
  "s_mov_b32 s92, 0\n\t" \
  "s_waitcnt lgkmcnt(0)\n\t" \
  "s_cmp_eq_u32 s82, s84\n\t" \
  "s_addc_u32 s92, s92, 0\n\t" \
  "s_cmp_eq_u32 s82, s56\n\t" \
  "s_addc_u32 s92, s92, 0\n\t" \
  "s_cmp_eq_u32 s82, s86\n\t" \
  "s_addc_u32 s92, s92, 0\n\t" \
  "s_cmp_eq_u32 s82, s88\n\t" \
  "s_addc_u32 s92, s92, 0\n\t" \
  "s_cmp_eq_u32 s82, s90\n\t" \
  "s_addc_u32 s92, s92, 0\n\t" \
  "s_mul_i32 s92, s92, 24\n\t" \
  "s_add_i32 s92, s92, vcc_lo\n\t" \
  "s_lshl4_add_u32 s93, s82, 64\n\t" \
  "s_lshl_b32 s50, s93, 8\n\t" \
  "s_waitcnt vmcnt(0)\n\t" \
  "15:\n\t" \
  "v_mov_b32 v117, 20000\n\t" \
  "v_cmp_ne_u32_sdwa vcc, v116, s49 src0_sel:BYTE_0 src1_sel:DWORD\n\t" \
  "v_subrev_u32 v116, s50, v116\n\t" \
  "v_ashrrev_i32 v116, 8, v116\n\t" \
  "s_nop 1\n\t" \
  "v_max_i32_dpp v116, v116, v116 quad_perm:[1,0,3,2] row_mask:0xf bank_mask:0xf\n\t" \
  "s_cbranch_vccnz 14f\n\t" \
  "s_nop 0\n\t" \
  "v_max_i32_dpp v116, v116, v116 quad_perm:[2,3,0,1] row_mask:0xf bank_mask:0xf\n\t" \
  "s_nop 1\n\t" \
  "v_max_i32_dpp v116, v116, v116 row_half_mirror row_mask:0xf bank_mask:0xf\n\t" \
  "v_med3_i32 v116, v116, 0, v117\n\t" \
  "v_mul_u32_u24 v116, s52, v116\n\t" \
  "v_lshrrev_b32 v116, 16, v116\n\t" \
  "s_mov_b64 exec, -1\n\t" \
  "v_readfirstlane_b32 s84, v116\n\t" \
  "s_add_i32 s84, s84, s92\n\t" \
  ".p2align 6\n\t" \
  "s_nop 0\n\t" \
  "s_nop 0\n\t" \
  "s_nop 0\n\t" \
  "s_nop 0\n\t" \
  "s_nop 0\n\t" \
  "s_nop 0\n\t" \
  "s_nop 0\n\t" \
  "s_nop 0\n\t" \
  "1:\n\t" \
  "s_memtime vcc\n\t" \
  "s_waitcnt lgkmcnt(0)\n\t" \
  "s_sub_i32 s79, s84, vcc_lo\n\t" \
  "s_cmp_gt_i32 s79, 128\n\t" \
  "s_cbranch_scc1 1b\n\t" \
  ".p2align 4\n\t" \
  "2:\n\t" \
  "s_sub_i32 s79, s79, 33\n\t" \
  "s_cmp_gt_i32 s79, 56\n\t" \
  "s_cbranch_scc1 2b\n\t" \
  "buffer_load_dwordx4 v[32:35], v100, %[rh], s40 offen sc1\n\t" \
  "s_nop 12\n\t" \
  "buffer_load_dwordx4 v[36:39], v100, %[rh], s76 offen sc1\n\t" \
  "s_nop 12\n\t" \
  "buffer_load_dwordx4 v[40:43], v100, %[rh], s77 offen sc1\n\t" \
  "s_nop 12\n\t" \
  "buffer_load_dwordx4 v[44:47], v100, %[rh], s78 offen sc1\n\t" \
  "3:\n\t" \
  "v_mov_b64 v[72:73], 0\n\t" \
  "v_mov_b64 v[74:75], 0\n\t" \
  "v_mov_b64 v[76:77], 0\n\t" \
  "v_mov_b64 v[78:79], 0\n\t" \
  "s_waitcnt lgkmcnt(0)\n\t" \
  "s_waitcnt vmcnt(3)\n\t" \
  "v_smfmac_f32_16x16x64_bf16 v[72:75], v[32:35], v[48:55], v104\n\t" \
  "ds_write_b128 v101, v[32:35] offset:0\n\t" \
  "v_smfmac_f32_16x16x64_bf16 v[76:79], v[32:35], v[56:63], v104\n\t" \
  "s_waitcnt vmcnt(2)\n\t" \
  "ds_write_b128 v101, v[36:39] offset:1024\n\t" \
  "v_smfmac_f32_16x16x64_bf16 v[72:75], v[36:39], a[0:7], v104\n\t" \
  "v_smfmac_f32_16x16x64_bf16 v[76:79], v[36:39], a[8:15], v104\n\t" \
  "s_nop 3\n\t" \
  "v_cmp_u_f32 vcc, v72, v73\n\t" \
  "v_cmp_u_f32 s[82:83], v74, v75\n\t" \
  "s_waitcnt vmcnt(1)\n\t" \
  "v_smfmac_f32_16x16x64_bf16 v[72:75], v[40:43], a[16:23], v104\n\t" \
  "s_or_b64 vcc, vcc, s[82:83]\n\t" \
  "s_or_b64 vcc, vcc, s[80:81]\n\t" \
  "v_smfmac_f32_16x16x64_bf16 v[76:79], v[40:43], a[24:31], v104\n\t" \
  "s_waitcnt lgkmcnt(0)\n\t" \
  "s_cbranch_scc1 8f\n\t" \
  "5:\n\t" \
  "s_barrier\n\t" \
  "4:\n\t" \
  "s_memtime s[56:57]\n\t" \
  "ds_read_b128 v[48:51], v101 offset:4096\n\t" \
  "ds_read_b128 v[52:55], v101 offset:5120\n\t" \
  "ds_read_b128 v[56:59], v101 offset:9216\n\t" \
  "s_waitcnt vmcnt(0)\n\t" \
  "v_smfmac_f32_16x16x64_bf16 v[72:75], v[44:47], a[32:39], v104\n\t" \
  "ds_read_b128 v[60:63], v101 offset:8192\n\t" \
  "ds_read_b128 v[64:67], v101 offset:13312\n\t" \
  "v_smfmac_f32_16x16x64_bf16 v[76:79], v[44:47], a[40:47], v104\n\t" \
  "ds_read_b128 v[68:71], v101 offset:12288\n\t" \
  "ds_write_b128 v101, v[40:43] offset:2048\n\t" \
  "s_nop 1\n\t" \
  "v_cmp_u_f32 vcc, v72, v73\n\t" \
  "v_cmp_u_f32 s[82:83], v74, v75\n\t" \
  "s_waitcnt lgkmcnt(6)\n\t" \
  "v_smfmac_f32_16x16x64_bf16 v[72:75], v[48:51], v[0:7], v104\n\t" \
  "s_or_b64 vcc, vcc, s[82:83]\n\t" \
  "v_smfmac_f32_16x16x64_bf16 v[76:79], v[48:51], v[8:15], v104\n\t" \
  "s_cbranch_scc1 9f\n\t" \
  "6:\n\t" \
  "s_waitcnt lgkmcnt(5)\n\t" \
  "v_smfmac_f32_16x16x64_bf16 v[72:75], v[52:55], a[48:55], v104\n\t" \
  "ds_write_b128 v101, v[44:47] offset:3072\n\t" \
  "v_smfmac_f32_16x16x64_bf16 v[76:79], v[52:55], a[56:63], v104\n\t" \
  "v_mov_b64 v[40:41], 0\n\t" \
  "s_waitcnt lgkmcnt(5)\n\t" \
  "v_smfmac_f32_16x16x64_bf16 v[72:75], v[56:59], a[96:103], v104\n\t" \
  "v_mov_b64 v[42:43], 0\n\t" \
  "v_smfmac_f32_16x16x64_bf16 v[76:79], v[56:59], a[104:111], v104\n\t" \
  "s_waitcnt lgkmcnt(4)\n\t" \
  "v_smfmac_f32_16x16x64_bf16 v[72:75], v[60:63], v[16:23], v104\n\t" \
  "s_waitcnt lgkmcnt(0)\n\t" \
  "s_barrier\n\t" \
  "v_smfmac_f32_16x16x64_bf16 v[76:79], v[60:63], v[24:31], v104\n\t" \
  "ds_read_b128 v[48:51], v101 offset:6144\n\t" \
  "ds_read_b128 v[52:55], v101 offset:10240\n\t" \
  "v_smfmac_f32_16x16x64_bf16 v[72:75], v[64:67], a[144:151], v104\n\t" \
  "ds_read_b128 v[56:59], v101 offset:14336\n\t" \
  "ds_read_b128 v[60:63], v101 offset:7168\n\t" \
  "v_smfmac_f32_16x16x64_bf16 v[76:79], v[64:67], a[152:159], v104\n\t" \
  "ds_read_b128 v[32:35], v101 offset:11264\n\t" \
  "ds_read_b128 v[36:39], v101 offset:15360\n\t" \
  "v_smfmac_f32_16x16x64_bf16 v[72:75], v[68:71], v[80:87], v104\n\t" \
  "ds_read_b128 v[0:3], v103 offset:0\n\t" \
  "v_mov_b64 v[44:45], 0\n\t" \
  "v_smfmac_f32_16x16x64_bf16 v[76:79], v[68:71], v[88:95], v104\n\t" \
  "v_mov_b64 v[46:47], 0\n\t" \
  "s_waitcnt lgkmcnt(5)\n\t" \
  "v_smfmac_f32_16x16x64_bf16 v[72:75], v[48:51], a[64:71], v104\n\t" \
  "ds_read_b128 v[4:7], v103 offset:1024\n\t" \
  "v_mov_b64 v[80:81], 0\n\t" \
  "v_smfmac_f32_16x16x64_bf16 v[72:75], v[52:55], a[112:119], v104\n\t" \
  "v_mov_b64 v[82:83], 0\n\t" \
  "s_waitcnt lgkmcnt(2)\n\t" \
  "v_smfmac_f32_16x16x64_bf16 v[72:75], v[56:59], a[160:167], v104\n\t" \
  "ds_read_b128 v[8:11], v103 offset:2048\n\t" \
  "v_mov_b64 v[84:85], 0\n\t" \
  "v_smfmac_f32_16x16x64_bf16 v[72:75], v[60:63], a[80:87], v104\n\t" \
  "ds_read_b128 v[12:15], v103 offset:3072\n\t" \
  "v_mov_b64 v[86:87], 0\n\t" \
  "v_smfmac_f32_16x16x64_bf16 v[72:75], v[32:35], a[128:135], v104\n\t" \
  "ds_read_b128 v[16:19], v103 offset:16384\n\t" \
  "v_mov_b64 v[88:89], 0\n\t" \
  "v_smfmac_f32_16x16x64_bf16 v[72:75], v[36:39], a[176:183], v104\n\t" \
  "ds_read_b128 v[20:23], v103 offset:17408\n\t" \
  "v_mov_b64 v[90:91], 0\n\t" \
  "v_smfmac_f32_16x16x64_bf16 v[76:79], v[48:51], a[72:79], v104\n\t" \
  "ds_read_b128 v[24:27], v103 offset:18432\n\t" \
  "v_mov_b64 v[92:93], 0\n\t" \
  "v_smfmac_f32_16x16x64_bf16 v[76:79], v[52:55], a[120:127], v104\n\t" \
  "v_add_f32 v96, v72, v74\n\t" \
  "v_add_f32 v97, v73, v75\n\t" \
  "v_cvt_pk_bf16_f32 v96, v96, v97\n\t" \
  "ds_write_b32 v105, v96\n\t" \
  "v_smfmac_f32_16x16x64_bf16 v[76:79], v[56:59], a[168:175], v104\n\t" \
  "ds_read_b128 v[28:31], v103 offset:19456\n\t" \
  "v_mov_b64 v[94:95], 0\n\t" \
  "v_smfmac_f32_16x16x64_bf16 v[76:79], v[60:63], a[88:95], v104\n\t" \
  "v_smfmac_f32_16x16x64_bf16 v[76:79], v[32:35], a[136:143], v104\n\t" \
  "v_smfmac_f32_16x16x64_bf16 v[76:79], v[36:39], a[184:191], v104\n\t" \
  "s_waitcnt lgkmcnt(1)\n\t" \
  "s_barrier\n\t" \
  "ds_read_b64_tr_b16 v[96:97], v107 offset:0\n\t" \
  "ds_read_b64_tr_b16 v[98:99], v107 offset:64\n\t" \
  "ds_read_b64_tr_b16 v[56:57], v107 offset:1024\n\t" \
  "ds_read_b64_tr_b16 v[58:59], v107 offset:1088\n\t" \
  "s_nop 1\n\t" \
  "v_add_f32 v116, v76, v78\n\t" \
  "v_add_f32 v117, v77, v79\n\t" \
  "v_cvt_pk_bf16_f32 v116, v116, v117\n\t" \
  "ds_write_b32 v105, v116 offset:256\n\t" \
  "s_waitcnt lgkmcnt(3)\n\t" \
  "v_smfmac_f32_16x16x64_bf16 v[40:43], v[96:99], a[192:199], v106\n\t" \
  "v_smfmac_f32_16x16x64_bf16 v[44:47], v[96:99], a[224:231], v106\n\t" \
  "s_waitcnt lgkmcnt(0)\n\t" \
  "s_barrier\n\t" \
  "ds_read_b64_tr_b16 v[52:53], v107 offset:256\n\t" \
  "ds_read_b64_tr_b16 v[54:55], v107 offset:320\n\t" \
  "v_smfmac_f32_16x16x64_bf16 v[40:43], v[56:59], a[208:215], v106\n\t" \
  "ds_read_b64_tr_b16 v[60:61], v107 offset:1280\n\t" \
  "ds_read_b64_tr_b16 v[62:63], v107 offset:1344\n\t" \
  "v_smfmac_f32_16x16x64_bf16 v[44:47], v[56:59], v[0:7], v106\n\t" \
  "v_smfmac_f32_16x16x64_bf16 v[80:83], v[96:99], a[240:247], v106\n\t" \
  "v_mov_b64 v[32:33], 0\n\t" \
  "v_smfmac_f32_16x16x64_bf16 v[84:87], v[96:99], v[136:143], v106\n\t" \
  "v_mov_b64 v[34:35], 0\n\t" \
  "v_smfmac_f32_16x16x64_bf16 v[80:83], v[56:59], v[120:127], v106\n\t" \
  "v_mov_b64 v[36:37], 0\n\t" \
  "v_smfmac_f32_16x16x64_bf16 v[84:87], v[56:59], v[16:23], v106\n\t" \
  "s_waitcnt lgkmcnt(2)\n\t" \
  "v_smfmac_f32_16x16x64_bf16 v[40:43], v[52:55], a[200:207], v106\n\t" \
  "v_smfmac_f32_16x16x64_bf16 v[44:47], v[52:55], a[232:239], v106\n\t" \
  "s_xor_b32 s76, s47, 0x40000\n\t" \
  "s_waitcnt lgkmcnt(0)\n\t" \
  "v_smfmac_f32_16x16x64_bf16 v[40:43], v[60:63], a[216:223], v106\n\t" \
  "v_mov_b64 v[38:39], 0\n\t" \
  "s_xor_b32 s77, s47, 0x80000\n\t" \
  "v_smfmac_f32_16x16x64_bf16 v[44:47], v[60:63], v[8:15], v106\n\t" \
  "s_xor_b32 s78, s47, 0xc0000\n\t" \
  "v_smfmac_f32_16x16x64_bf16 v[80:83], v[52:55], a[248:255], v106\n\t" \
  "ds_read_b128 v[0:3], v103 offset:32768\n\t" \
  "v_cvt_pk_bf16_f32 v64, v40, v41\n\t" \
  "v_smfmac_f32_16x16x64_bf16 v[84:87], v[52:55], v[144:151], v106\n\t" \
  "ds_read_b128 v[4:7], v103 offset:33792\n\t" \
  "v_cvt_pk_bf16_f32 v65, v42, v43\n\t" \
  "v_smfmac_f32_16x16x64_bf16 v[80:83], v[60:63], v[128:135], v106\n\t" \
  "ds_read_b128 v[8:11], v103 offset:34816\n\t" \
  "v_cvt_pk_bf16_f32 v66, v44, v45\n\t" \
  "v_smfmac_f32_16x16x64_bf16 v[84:87], v[60:63], v[24:31], v106\n\t" \
  "ds_read_b128 v[12:15], v103 offset:35840\n\t" \
  "v_cvt_pk_bf16_f32 v67, v46, v47\n\t" \
  "v_smfmac_f32_16x16x64_bf16 v[88:91], v[96:99], v[152:159], v106\n\t" \
  "v_permlane16_swap_b32 v64, v66\n\t" \
  "v_cvt_pk_bf16_f32 v68, v80, v81\n\t" \
  "v_smfmac_f32_16x16x64_bf16 v[92:95], v[96:99], v[184:191], v106\n\t" \
  "v_permlane16_swap_b32 v65, v67\n\t" \
  "buffer_store_dwordx4 v[64:67], v108, %[ru], s47 offen sc1\n\t" \
  "v_cvt_pk_bf16_f32 v69, v82, v83\n\t" \
  "v_smfmac_f32_16x16x64_bf16 v[88:91], v[52:55], v[160:167], v106\n\t" \
  "v_cvt_pk_bf16_f32 v70, v84, v85\n\t" \
  "v_smfmac_f32_16x16x64_bf16 v[92:95], v[52:55], v[192:199], v106\n\t" \
  "v_cvt_pk_bf16_f32 v71, v86, v87\n\t" \
  "v_smfmac_f32_16x16x64_bf16 v[88:91], v[56:59], v[168:175], v106\n\t" \
  "ds_read_b128 v[16:19], v103 offset:49152\n\t" \
  "v_permlane16_swap_b32 v68, v70\n\t" \
  "s_waitcnt lgkmcnt(3)\n\t" \
  "v_smfmac_f32_16x16x64_bf16 v[92:95], v[56:59], v[0:7], v106\n\t" \
  "ds_read_b128 v[20:23], v103 offset:50176\n\t" \
  "v_permlane16_swap_b32 v69, v71\n\t" \
  "buffer_store_dwordx4 v[68:71], v108, %[ru], s76 offen sc1\n\t" \
  "v_smfmac_f32_16x16x64_bf16 v[88:91], v[60:63], v[176:183], v106\n\t" \
  "ds_read_b128 v[24:27], v103 offset:51200\n\t" \
  "s_waitcnt lgkmcnt(3)\n\t" \
  "v_smfmac_f32_16x16x64_bf16 v[92:95], v[60:63], v[8:15], v106\n\t" \
  "ds_read_b128 v[28:31], v103 offset:52224\n\t" \
  "v_smfmac_f32_16x16x64_bf16 v[32:35], v[96:99], v[200:207], v106\n\t" \
  "v_cvt_pk_bf16_f32 v72, v88, v89\n\t" \
  "v_smfmac_f32_16x16x64_bf16 v[36:39], v[96:99], v[232:239], v106\n\t" \
  "v_cvt_pk_bf16_f32 v73, v90, v91\n\t" \
  "v_smfmac_f32_16x16x64_bf16 v[32:35], v[52:55], v[208:215], v106\n\t" \
  "v_cvt_pk_bf16_f32 v74, v92, v93\n\t" \
  "v_smfmac_f32_16x16x64_bf16 v[36:39], v[52:55], v[240:247], v106\n\t" \
  "v_cvt_pk_bf16_f32 v75, v94, v95\n\t" \
  "v_smfmac_f32_16x16x64_bf16 v[32:35], v[56:59], v[216:223], v106\n\t" \
  "v_permlane16_swap_b32 v72, v74\n\t" \
  "v_smfmac_f32_16x16x64_bf16 v[32:35], v[60:63], v[224:231], v106\n\t" \
  "v_permlane16_swap_b32 v73, v75\n\t" \
  "buffer_store_dwordx4 v[72:75], v108, %[ru], s77 offen sc1\n\t" \
  "s_waitcnt lgkmcnt(2)\n\t" \
  "v_smfmac_f32_16x16x64_bf16 v[36:39], v[56:59], v[16:23], v106\n\t" \
  "s_waitcnt lgkmcnt(0)\n\t" \
  "v_smfmac_f32_16x16x64_bf16 v[36:39], v[60:63], v[24:31], v106\n\t" \
  "v_cvt_pk_bf16_f32 v76, v32, v33\n\t" \
  "v_cvt_pk_bf16_f32 v77, v34, v35\n\t" \
  "s_nop 5\n\t" \
  "v_cvt_pk_bf16_f32 v78, v36, v37\n\t" \
  "v_cvt_pk_bf16_f32 v79, v38, v39\n\t" \
  "s_nop 0\n\t" \
  "v_permlane16_swap_b32 v76, v78\n\t" \
  "v_permlane16_swap_b32 v77, v79\n\t" \
  "buffer_store_dwordx4 v[76:79], v108, %[ru], s78 offen sc1\n\t" \
  "s_memtime s[76:77]\n\t" \
  "s_mov_b32 s42, 0\n\t" \
  "s_waitcnt lgkmcnt(0)\n\t" \
  "s_mov_b32 s57, s76\n\t" \
  "s_add_i32 s41, s56, 1950\n\t" \
  "s_sub_i32 s76, s92, s51\n\t" \
  "s_sub_i32 s77, s93, s53\n\t" \
  "s_cmp_eq_u32 s76, 0\n\t" \
  "s_cbranch_scc1 21f\n\t" \
  "v_cvt_f32_u32 v117, s76\n\t" \
  "v_cvt_f32_u32 v116, s77\n\t" \
  "v_rcp_f32 v117, v117\n\t" \
  "s_mov_b32 s51, s92\n\t" \
  "v_mul_f32 v117, v117, v116\n\t" \
  "v_max_f32 v117, 0x3f028f5c, v117\n\t" \
  "v_min_f32 v117, 0x3ffeb852, v117\n\t" \
  "v_rcp_f32 v116, v117\n\t" \
  "v_mul_f32 v117, 0x4f000000, v117\n\t" \
  "v_mul_f32 v116, 0x47800000, v116\n\t" \
  "v_cvt_u32_f32 v117, v117\n\t" \
  "v_cvt_u32_f32 v116, v116\n\t" \
  "s_mov_b32 s53, s93\n\t" \
  "v_readfirstlane_b32 s46, v117\n\t" \
  "v_readfirstlane_b32 s52, v116\n\t" \
  "21:\n\t" \
  "s_add_i32 s44, s44, 1\n\t" \
  "s_and_b32 s58, s58, 4\n\t" \
  "s_and_b32 s76, s44, 64\n\t" \
  "s_or_b32 s42, s42, s76\n\t" \
  "s_cbranch_scc0 20b\n\t" \
  "s_branch 99f\n\t" \
  "14:\n\t" \
  "s_sleep 3\n\t" \
  "s_add_u32 s59, s59, 0x10000\n\t" \
  "s_cmp_lt_u32 s59, 0x400000\n\t" \
  "s_cbranch_scc1 13b\n\t" \
  "s_mov_b64 exec, -1\n\t" \
  "s_mov_b32 s42, 16\n\t" \
  "s_branch 99f\n\t" \
  "8:\n\t" \
  "s_bitcmp1_b32 s58, 1\n\t" \
  "s_cbranch_scc1 4b\n\t" \
  "s_bitcmp1_b32 s58, 2\n\t" \
  "s_cbranch_scc1 5b\n\t" \
  "v_max3_u32 v116, v32, v33, v34\n\t" \
  "v_max3_u32 v117, v35, v36, v37\n\t" \
  "v_max3_u32 v116, v116, v38, v39\n\t" \
  "v_max_u32 v116, v116, v117\n\t" \
  "v_cmp_eq_u32 vcc, -1, v116\n\t" \
  "s_cbranch_vccz 5b\n\t" \
  "s_branch 0f\n\t" \
  "9:\n\t" \
  "s_bitcmp1_b32 s58, 2\n\t" \
  "s_cbranch_scc1 6b\n\t" \
  "v_max3_u32 v116, v40, v41, v42\n\t" \
  "v_max3_u32 v117, v43, v44, v45\n\t" \
  "v_max3_u32 v116, v116, v46, v47\n\t" \
  "v_max_u32 v116, v116, v117\n\t" \
  "v_cmp_eq_u32 vcc, -1, v116\n\t" \
  "s_cbranch_vccz 6b\n\t" \
  "s_or_b32 s58, s58, 2\n\t" \
  "0:\n\t" \
  "s_or_b32 s58, s58, 1\n\t" \
  "11:\n\t" \
  "buffer_load_dwordx4 v[32:35], v100, %[rh], s40 offen sc1\n\t" \
  "buffer_load_dwordx4 v[36:39], v100, %[rh], s76 offen sc1\n\t" \
  "buffer_load_dwordx4 v[40:43], v100, %[rh], s77 offen sc1\n\t" \
  "buffer_load_dwordx4 v[44:47], v100, %[rh], s78 offen sc1\n\t" \
  "s_waitcnt lgkmcnt(0)\n\t" \
  "s_branch 10b\n\t" \
  "17:\n\t" \
  "s_add_u32 s59, s59, 1\n\t" \
  "s_bitcmp1_b32 s58, 2\n\t" \
  "s_cbranch_scc1 3b\n\t" \
  "s_waitcnt vmcnt(0)\n\t" \
  "v_max_u32 v116, v32, v33\n\t" \
  "v_max3_u32 v116, v116, v34, v35\n\t" \
  "v_max3_u32 v116, v116, v36, v37\n\t" \
  "v_max3_u32 v116, v116, v38, v39\n\t" \
  "v_max3_u32 v116, v116, v40, v41\n\t" \
  "v_max3_u32 v116, v116, v42, v43\n\t" \
  "v_max3_u32 v116, v116, v44, v45\n\t" \
  "v_max3_u32 v116, v116, v46, v47\n\t" \
  "v_cmp_eq_u32 vcc, -1, v116\n\t" \
  "s_cbranch_vccz 3b\n\t" \
  "s_bitcmp1_b32 s59, 6\n\t" \
  "s_cbranch_scc1 12f\n\t" \
  "buffer_load_dwordx4 v[32:35], v100, %[rh], s40 offen sc1\n\t" \
  "buffer_load_dwordx4 v[36:39], v100, %[rh], s76 offen sc1\n\t" \
  "buffer_load_dwordx4 v[40:43], v100, %[rh], s77 offen sc1\n\t" \
  "buffer_load_dwordx4 v[44:47], v100, %[rh], s78 offen sc1\n\t" \
  "s_branch 17b\n\t" \
  "12:\n\t" \
  "s_mov_b32 s42, s58\n\t" \
  "s_branch 99f\n\t" \
  "91:\n\t" \
  "s_mov_b32 s58, s42\n\t" \
  "20:\n\t" \
  "s_and_b32 s76, s44, 7\n\t" \
  "s_lshl_b32 s76, s76, 15\n\t" \
  "s_add_i32 s40, s54, s76\n\t" \
  "s_and_b32 s76, s44, 3\n\t" \
  "s_lshl_b32 s45, s76, 14\n\t" \
  "s_lshl_b32 s47, s76, 20\n\t" \
  "s_add_i32 s47, s47, s55\n\t" \
  "s_lshr_b32 s76, s47, 21\n\t" \
  "s_bitcmp1_b64 %[sel], s76\n\t" \
  "s_cselect_b32 s76, 0x2000, 0\n\t" \
  "s_add_i32 s47, s47, s76\n\t" \
  "s_add_i32 s49, s44, -1\n\t" \
  "s_and_b32 s48, s49, 2\n\t" \
  "s_lshl_b32 s48, s48, 13\n\t" \
  "s_and_b32 s76, s49, 1\n\t" \
  "s_lshl_b32 s76, s76, 12\n\t" \
  "s_add_i32 s48, s48, s76\n\t" \
  "s_lshl_b32 s50, s53, 8\n\t" \
  "s_mov_b32 s59, 0\n\t" \
  "s_mov_b32 s92, s51\n\t" \
  "s_mov_b32 s93, s53\n\t" \
  "s_xor_b32 s76, s40, 0x400\n\t" \
  "s_xor_b32 s77, s40, 0x800\n\t" \
  "s_xor_b32 s78, s40, 0xc00\n\t" \
  "s_cmp_lg_u32 s58, 0\n\t" \
  "s_cbranch_scc1 11f\n\t" \
  "10:\n\t" \
  "ds_read_b128 v[48:51], v102 offset:16384\n\t" \
  "ds_read_b128 v[52:55], v102 offset:17408\n\t" \
  "ds_read_b128 v[56:59], v102 offset:18432\n\t" \
  "ds_read_b128 v[60:63], v102 offset:19456\n\t" \
  "ds_read_b128 v[0:3], v102 offset:32768\n\t" \
  "ds_read_b128 v[4:7], v102 offset:33792\n\t" \
  "ds_read_b128 v[8:11], v102 offset:34816\n\t" \
  "ds_read_b128 v[12:15], v102 offset:35840\n\t" \
  "ds_read_b128 v[16:19], v102 offset:49152\n\t" \
  "ds_read_b128 v[20:23], v102 offset:50176\n\t" \
  "ds_read_b128 v[24:27], v102 offset:51200\n\t" \
  "ds_read_b128 v[28:31], v102 offset:52224\n\t" \
  "s_waitcnt lgkmcnt(4)\n\t" \
  "ds_read_b128 v[80:83], v102 offset:0\n\t" \
  "ds_read_b128 v[84:87], v102 offset:1024\n\t" \
  "ds_read_b128 v[88:91], v102 offset:2048\n\t" \
  "ds_read_b128 v[92:95], v102 offset:3072\n\t" \
  "46:\n\t" \
  "s_bitcmp1_b32 s58, 1\n\t" \
  "s_cselect_b64 s[80:81], -1, 0\n\t" \
  "s_cmp_lg_u32 s58, 0\n\t" \
  "s_cbranch_scc1 17f\n\t" \
  "s_mov_b64 exec, 0xff\n\t" \
  "16:\n\t" \
  "s_memtime vcc\n\t" \
  "s_waitcnt lgkmcnt(0)\n\t" \
  "s_sub_i32 s79, s41, vcc_lo\n\t" \
  "s_cmp_gt_i32 s79, 64\n\t" \
  "s_cbranch_scc1 16b\n\t" \
  "13:\n\t" \
  "buffer_load_dword v116, v112, %[rh], s48 offen sc1\n\t" \
  "s_memtime vcc\n\t" \
  "s_memrealtime s[82:83]\n\t" \
  "s_nop 4\n\t" \
  "s_memrealtime s[84:85]\n\t" \
  "s_nop 4\n\t" \
  "s_memrealtime s[56:57]\n\t" \
  "s_nop 4\n\t" \
  "s_memrealtime s[86:87]\n\t" \
  "s_nop 4\n\t" \
  "s_memrealtime s[88:89]\n\t" \
  "s_nop 4\n\t" \
  "s_memrealtime s[90:91]\n\t" \
  "s_mov_b32 s92, 0\n\t" \
  "s_waitcnt lgkmcnt(0)\n\t" \
  "s_cmp_eq_u32 s82, s84\n\t" \
  "s_addc_u32 s92, s92, 0\n\t" \
  "s_cmp_eq_u32 s82, s56\n\t" \
  "s_addc_u32 s92, s92, 0\n\t" \
  "s_cmp_eq_u32 s82, s86\n\t" \
  "s_addc_u32 s92, s92, 0\n\t" \
  "s_cmp_eq_u32 s82, s88\n\t" \
  "s_addc_u32 s92, s92, 0\n\t" \
  "s_cmp_eq_u32 s82, s90\n\t" \
  "s_addc_u32 s92, s92, 0\n\t" \
  "s_mul_i32 s92, s92, 24\n\t" \
  "s_add_i32 s92, s92, vcc_lo\n\t" \
  "s_lshl4_add_u32 s93, s82, 64\n\t" \
  "s_lshl_b32 s50, s93, 8\n\t" \
  "s_waitcnt vmcnt(0)\n\t" \
  "15:\n\t" \
  "v_mov_b32 v117, 20000\n\t" \
  "v_cmp_ne_u32_sdwa vcc, v116, s49 src0_sel:BYTE_0 src1_sel:DWORD\n\t" \
  "v_subrev_u32 v116, s50, v116\n\t" \
  "v_ashrrev_i32 v116, 8, v116\n\t" \
  "s_nop 1\n\t" \
  "v_max_i32_dpp v116, v116, v116 quad_perm:[1,0,3,2] row_mask:0xf bank_mask:0xf\n\t" \
  "s_cbranch_vccnz 14f\n\t" \
  "s_nop 0\n\t" \
  "v_max_i32_dpp v116, v116, v116 quad_perm:[2,3,0,1] row_mask:0xf bank_mask:0xf\n\t" \
  "s_nop 1\n\t" \
  "v_max_i32_dpp v116, v116, v116 row_half_mirror row_mask:0xf bank_mask:0xf\n\t" \
  "v_med3_i32 v116, v116, 0, v117\n\t" \
  "v_mul_u32_u24 v116, s52, v116\n\t" \
  "v_lshrrev_b32 v116, 16, v116\n\t" \
  "s_mov_b64 exec, -1\n\t" \
  "v_readfirstlane_b32 s84, v116\n\t" \
  "s_add_i32 s84, s84, s92\n\t" \
  ".p2align 6\n\t" \
  "s_nop 0\n\t" \
  "s_nop 0\n\t" \
  "s_nop 0\n\t" \
  "s_nop 0\n\t" \
  "s_nop 0\n\t" \
  "s_nop 0\n\t" \
  "s_nop 0\n\t" \
  "s_nop 0\n\t" \
  "1:\n\t" \
  "s_memtime vcc\n\t" \
  "s_waitcnt lgkmcnt(0)\n\t" \
  "s_sub_i32 s79, s84, vcc_lo\n\t" \
  "s_cmp_gt_i32 s79, 128\n\t" \
  "s_cbranch_scc1 1b\n\t" \
  ".p2align 4\n\t" \
  "2:\n\t" \
  "s_sub_i32 s79, s79, 33\n\t" \
  "s_cmp_gt_i32 s79, 56\n\t" \
  "s_cbranch_scc1 2b\n\t" \
  "buffer_load_dwordx4 v[32:35], v100, %[rh], s40 offen sc1\n\t" \
  "s_nop 12\n\t" \
  "buffer_load_dwordx4 v[36:39], v100, %[rh], s76 offen sc1\n\t" \
  "s_nop 12\n\t" \
  "buffer_load_dwordx4 v[40:43], v100, %[rh], s77 offen sc1\n\t" \
  "s_nop 12\n\t" \
  "buffer_load_dwordx4 v[44:47], v100, %[rh], s78 offen sc1\n\t" \
  "3:\n\t" \
  "v_mov_b64 v[72:73], 0\n\t" \
  "v_mov_b64 v[74:75], 0\n\t" \
  "v_mov_b64 v[76:77], 0\n\t" \
  "v_mov_b64 v[78:79], 0\n\t" \
  "s_waitcnt lgkmcnt(0)\n\t" \
  "s_waitcnt vmcnt(3)\n\t" \
  "v_smfmac_f32_16x16x64_bf16 v[72:75], v[32:35], v[48:55], v104\n\t" \
  "ds_write_b128 v101, v[32:35] offset:4096\n\t" \
  "v_smfmac_f32_16x16x64_bf16 v[76:79], v[32:35], v[56:63], v104\n\t" \
  "s_waitcnt vmcnt(2)\n\t" \
  "ds_write_b128 v101, v[36:39] offset:5120\n\t" \
  "v_smfmac_f32_16x16x64_bf16 v[72:75], v[36:39], a[48:55], v104\n\t" \
  "v_smfmac_f32_16x16x64_bf16 v[76:79], v[36:39], a[56:63], v104\n\t" \
  "s_nop 3\n\t" \
  "v_cmp_u_f32 vcc, v72, v73\n\t" \
  "v_cmp_u_f32 s[82:83], v74, v75\n\t" \
  "s_waitcnt vmcnt(1)\n\t" \
  "v_smfmac_f32_16x16x64_bf16 v[72:75], v[40:43], a[64:71], v104\n\t" \
  "s_or_b64 vcc, vcc, s[82:83]\n\t" \
  "s_or_b64 vcc, vcc, s[80:81]\n\t" \
  "v_smfmac_f32_16x16x64_bf16 v[76:79], v[40:43], a[72:79], v104\n\t" \
  "s_waitcnt lgkmcnt(0)\n\t" \
  "s_cbranch_scc1 8f\n\t" \
  "5:\n\t" \
  "s_barrier\n\t" \
  "4:\n\t" \
  "s_memtime s[56:57]\n\t" \
  "ds_read_b128 v[48:51], v101 offset:8192\n\t" \
  "ds_read_b128 v[52:55], v101 offset:9216\n\t" \
  "ds_read_b128 v[56:59], v101 offset:13312\n\t" \
  "s_waitcnt vmcnt(0)\n\t" \
  "v_smfmac_f32_16x16x64_bf16 v[72:75], v[44:47], a[80:87], v104\n\t" \
  "ds_read_b128 v[60:63], v101 offset:12288\n\t" \
  "ds_read_b128 v[64:67], v101 offset:1024\n\t" \
  "v_smfmac_f32_16x16x64_bf16 v[76:79], v[44:47], a[88:95], v104\n\t" \
  "ds_read_b128 v[68:71], v101 offset:0\n\t" \
  "ds_write_b128 v101, v[40:43] offset:6144\n\t" \
  "s_nop 1\n\t" \
  "v_cmp_u_f32 vcc, v72, v73\n\t" \
  "v_cmp_u_f32 s[82:83], v74, v75\n\t" \
  "s_waitcnt lgkmcnt(6)\n\t" \
  "v_smfmac_f32_16x16x64_bf16 v[72:75], v[48:51], v[0:7], v104\n\t" \
  "s_or_b64 vcc, vcc, s[82:83]\n\t" \
  "v_smfmac_f32_16x16x64_bf16 v[76:79], v[48:51], v[8:15], v104\n\t" \
  "s_cbranch_scc1 9f\n\t" \
  "6:\n\t" \
  "s_waitcnt lgkmcnt(5)\n\t" \
  "v_smfmac_f32_16x16x64_bf16 v[72:75], v[52:55], a[96:103], v104\n\t" \
  "ds_write_b128 v101, v[44:47] offset:7168\n\t" \
  "v_smfmac_f32_16x16x64_bf16 v[76:79], v[52:55], a[104:111], v104\n\t" \
  "v_mov_b64 v[40:41], 0\n\t" \
  "s_waitcnt lgkmcnt(5)\n\t" \
  "v_smfmac_f32_16x16x64_bf16 v[72:75], v[56:59], a[144:151], v104\n\t" \
  "v_mov_b64 v[42:43], 0\n\t" \
  "v_smfmac_f32_16x16x64_bf16 v[76:79], v[56:59], a[152:159], v104\n\t" \
  "s_waitcnt lgkmcnt(4)\n\t" \
  "v_smfmac_f32_16x16x64_bf16 v[72:75], v[60:63], v[16:23], v104\n\t" \
  "s_waitcnt lgkmcnt(0)\n\t" \
  "s_barrier\n\t" \
  "v_smfmac_f32_16x16x64_bf16 v[76:79], v[60:63], v[24:31], v104\n\t" \
  "ds_read_b128 v[48:51], v101 offset:10240\n\t" \
  "ds_read_b128 v[52:55], v101 offset:14336\n\t" \
  "v_smfmac_f32_16x16x64_bf16 v[72:75], v[64:67], a[0:7], v104\n\t" \
  "ds_read_b128 v[56:59], v101 offset:2048\n\t" \
  "ds_read_b128 v[60:63], v101 offset:11264\n\t" \
  "v_smfmac_f32_16x16x64_bf16 v[76:79], v[64:67], a[8:15], v104\n\t" \
  "ds_read_b128 v[32:35], v101 offset:15360\n\t" \
  "ds_read_b128 v[36:39], v101 offset:3072\n\t" \
  "v_smfmac_f32_16x16x64_bf16 v[72:75], v[68:71], v[80:87], v104\n\t" \
  "ds_read_b128 v[0:3], v103 offset:0\n\t" \
  "v_mov_b64 v[44:45], 0\n\t" \
  "v_smfmac_f32_16x16x64_bf16 v[76:79], v[68:71], v[88:95], v104\n\t" \
  "v_mov_b64 v[46:47], 0\n\t" \
  "s_waitcnt lgkmcnt(5)\n\t" \
  "v_smfmac_f32_16x16x64_bf16 v[72:75], v[48:51], a[112:119], v104\n\t" \
  "ds_read_b128 v[4:7], v103 offset:1024\n\t" \
  "v_mov_b64 v[80:81], 0\n\t" \
  "v_smfmac_f32_16x16x64_bf16 v[72:75], v[52:55], a[160:167], v104\n\t" \
  "v_mov_b64 v[82:83], 0\n\t" \
  "s_waitcnt lgkmcnt(2)\n\t" \
  "v_smfmac_f32_16x16x64_bf16 v[72:75], v[56:59], a[16:23], v104\n\t" \
  "ds_read_b128 v[8:11], v103 offset:2048\n\t" \
  "v_mov_b64 v[84:85], 0\n\t" \
  "v_smfmac_f32_16x16x64_bf16 v[72:75], v[60:63], a[128:135], v104\n\t" \
  "ds_read_b128 v[12:15], v103 offset:3072\n\t" \
  "v_mov_b64 v[86:87], 0\n\t" \
  "v_smfmac_f32_16x16x64_bf16 v[72:75], v[32:35], a[176:183], v104\n\t" \
  "ds_read_b128 v[16:19], v103 offset:16384\n\t" \
  "v_mov_b64 v[88:89], 0\n\t" \
  "v_smfmac_f32_16x16x64_bf16 v[72:75], v[36:39], a[32:39], v104\n\t" \
  "ds_read_b128 v[20:23], v103 offset:17408\n\t" \
  "v_mov_b64 v[90:91], 0\n\t" \
  "v_smfmac_f32_16x16x64_bf16 v[76:79], v[48:51], a[120:127], v104\n\t" \
  "ds_read_b128 v[24:27], v103 offset:18432\n\t" \
  "v_mov_b64 v[92:93], 0\n\t" \
  "v_smfmac_f32_16x16x64_bf16 v[76:79], v[52:55], a[168:175], v104\n\t" \
  "v_add_f32 v96, v72, v74\n\t" \
  "v_add_f32 v97, v73, v75\n\t" \
  "v_cvt_pk_bf16_f32 v96, v96, v97\n\t" \
  "ds_write_b32 v105, v96\n\t" \
  "v_smfmac_f32_16x16x64_bf16 v[76:79], v[56:59], a[24:31], v104\n\t" \
  "ds_read_b128 v[28:31], v103 offset:19456\n\t" \
  "v_mov_b64 v[94:95], 0\n\t" \
  "v_smfmac_f32_16x16x64_bf16 v[76:79], v[60:63], a[136:143], v104\n\t" \
  "v_smfmac_f32_16x16x64_bf16 v[76:79], v[32:35], a[184:191], v104\n\t" \
  "v_smfmac_f32_16x16x64_bf16 v[76:79], v[36:39], a[40:47], v104\n\t" \
  "s_waitcnt lgkmcnt(1)\n\t" \
  "s_barrier\n\t" \
  "ds_read_b64_tr_b16 v[96:97], v107 offset:0\n\t" \
  "ds_read_b64_tr_b16 v[98:99], v107 offset:64\n\t" \
  "ds_read_b64_tr_b16 v[56:57], v107 offset:1024\n\t" \
  "ds_read_b64_tr_b16 v[58:59], v107 offset:1088\n\t" \
  "s_nop 1\n\t" \
  "v_add_f32 v116, v76, v78\n\t" \
  "v_add_f32 v117, v77, v79\n\t" \
  "v_cvt_pk_bf16_f32 v116, v116, v117\n\t" \
  "ds_write_b32 v105, v116 offset:256\n\t" \
  "s_waitcnt lgkmcnt(3)\n\t" \
  "v_smfmac_f32_16x16x64_bf16 v[40:43], v[96:99], a[192:199], v106\n\t" \
  "v_smfmac_f32_16x16x64_bf16 v[44:47], v[96:99], a[224:231], v106\n\t" \
  "s_waitcnt lgkmcnt(0)\n\t" \
  "s_barrier\n\t" \
  "ds_read_b64_tr_b16 v[52:53], v107 offset:256\n\t" \
  "ds_read_b64_tr_b16 v[54:55], v107 offset:320\n\t" \
  "v_smfmac_f32_16x16x64_bf16 v[40:43], v[56:59], a[208:215], v106\n\t" \
  "ds_read_b64_tr_b16 v[60:61], v107 offset:1280\n\t" \
  "ds_read_b64_tr_b16 v[62:63], v107 offset:1344\n\t" \
  "v_smfmac_f32_16x16x64_bf16 v[44:47], v[56:59], v[0:7], v106\n\t" \
  "v_smfmac_f32_16x16x64_bf16 v[80:83], v[96:99], a[240:247], v106\n\t" \
  "v_mov_b64 v[32:33], 0\n\t" \
  "v_smfmac_f32_16x16x64_bf16 v[84:87], v[96:99], v[136:143], v106\n\t" \
  "v_mov_b64 v[34:35], 0\n\t" \
  "v_smfmac_f32_16x16x64_bf16 v[80:83], v[56:59], v[120:127], v106\n\t" \
  "v_mov_b64 v[36:37], 0\n\t" \
  "v_smfmac_f32_16x16x64_bf16 v[84:87], v[56:59], v[16:23], v106\n\t" \
  "s_waitcnt lgkmcnt(2)\n\t" \
  "v_smfmac_f32_16x16x64_bf16 v[40:43], v[52:55], a[200:207], v106\n\t" \
  "v_smfmac_f32_16x16x64_bf16 v[44:47], v[52:55], a[232:239], v106\n\t" \
  "s_xor_b32 s76, s47, 0x40000\n\t" \
  "s_waitcnt lgkmcnt(0)\n\t" \
  "v_smfmac_f32_16x16x64_bf16 v[40:43], v[60:63], a[216:223], v106\n\t" \
  "v_mov_b64 v[38:39], 0\n\t" \
  "s_xor_b32 s77, s47, 0x80000\n\t" \
  "v_smfmac_f32_16x16x64_bf16 v[44:47], v[60:63], v[8:15], v106\n\t" \
  "s_xor_b32 s78, s47, 0xc0000\n\t" \
  "v_smfmac_f32_16x16x64_bf16 v[80:83], v[52:55], a[248:255], v106\n\t" \
  "ds_read_b128 v[0:3], v103 offset:32768\n\t" \
  "v_cvt_pk_bf16_f32 v64, v40, v41\n\t" \
  "v_smfmac_f32_16x16x64_bf16 v[84:87], v[52:55], v[144:151], v106\n\t" \
  "ds_read_b128 v[4:7], v103 offset:33792\n\t" \
  "v_cvt_pk_bf16_f32 v65, v42, v43\n\t" \
  "v_smfmac_f32_16x16x64_bf16 v[80:83], v[60:63], v[128:135], v106\n\t" \
  "ds_read_b128 v[8:11], v103 offset:34816\n\t" \
  "v_cvt_pk_bf16_f32 v66, v44, v45\n\t" \
  "v_smfmac_f32_16x16x64_bf16 v[84:87], v[60:63], v[24:31], v106\n\t" \
  "ds_read_b128 v[12:15], v103 offset:35840\n\t" \
  "v_cvt_pk_bf16_f32 v67, v46, v47\n\t" \
  "v_smfmac_f32_16x16x64_bf16 v[88:91], v[96:99], v[152:159], v106\n\t" \
  "v_permlane16_swap_b32 v64, v66\n\t" \
  "v_cvt_pk_bf16_f32 v68, v80, v81\n\t" \
  "v_smfmac_f32_16x16x64_bf16 v[92:95], v[96:99], v[184:191], v106\n\t" \
  "v_permlane16_swap_b32 v65, v67\n\t" \
  "v_cvt_pk_bf16_f32 v69, v82, v83\n\t" \
  "v_smfmac_f32_16x16x64_bf16 v[88:91], v[52:55], v[160:167], v106\n\t" \
  "buffer_store_dwordx4 v[64:67], v108, %[ru], s47 offen sc1\n\t" \
  "v_cvt_pk_bf16_f32 v70, v84, v85\n\t" \
  "v_smfmac_f32_16x16x64_bf16 v[92:95], v[52:55], v[192:199], v106\n\t" \
  "v_cvt_pk_bf16_f32 v71, v86, v87\n\t" \
  "v_smfmac_f32_16x16x64_bf16 v[88:91], v[56:59], v[168:175], v106\n\t" \
  "ds_read_b128 v[16:19], v103 offset:49152\n\t" \
  "v_permlane16_swap_b32 v68, v70\n\t" \
  "s_waitcnt lgkmcnt(3)\n\t" \
  "v_smfmac_f32_16x16x64_bf16 v[92:95], v[56:59], v[0:7], v106\n\t" \
  "ds_read_b128 v[20:23], v103 offset:50176\n\t" \
  "v_permlane16_swap_b32 v69, v71\n\t" \
  "v_smfmac_f32_16x16x64_bf16 v[88:91], v[60:63], v[176:183], v106\n\t" \
  "ds_read_b128 v[24:27], v103 offset:51200\n\t" \
  "buffer_store_dwordx4 v[68:71], v108, %[ru], s76 offen sc1\n\t" \
  "s_waitcnt lgkmcnt(3)\n\t" \
  "v_smfmac_f32_16x16x64_bf16 v[92:95], v[60:63], v[8:15], v106\n\t" \
  "ds_read_b128 v[28:31], v103 offset:52224\n\t" \
  "v_smfmac_f32_16x16x64_bf16 v[32:35], v[96:99], v[200:207], v106\n\t" \
  "v_cvt_pk_bf16_f32 v72, v88, v89\n\t" \
  "v_smfmac_f32_16x16x64_bf16 v[36:39], v[96:99], v[232:239], v106\n\t" \
  "v_cvt_pk_bf16_f32 v73, v90, v91\n\t" \
  "v_smfmac_f32_16x16x64_bf16 v[32:35], v[52:55], v[208:215], v106\n\t" \
  "v_cvt_pk_bf16_f32 v74, v92, v93\n\t" \
  "v_smfmac_f32_16x16x64_bf16 v[36:39], v[52:55], v[240:247], v106\n\t" \
  "v_cvt_pk_bf16_f32 v75, v94, v95\n\t" \
  "v_smfmac_f32_16x16x64_bf16 v[32:35], v[56:59], v[216:223], v106\n\t" \
  "v_permlane16_swap_b32 v72, v74\n\t" \
  "v_smfmac_f32_16x16x64_bf16 v[32:35], v[60:63], v[224:231], v106\n\t" \
  "v_permlane16_swap_b32 v73, v75\n\t" \
  "s_waitcnt lgkmcnt(2)\n\t" \
  "v_smfmac_f32_16x16x64_bf16 v[36:39], v[56:59], v[16:23], v106\n\t" \
  "buffer_store_dwordx4 v[72:75], v108, %[ru], s77 offen sc1\n\t" \
  "s_waitcnt lgkmcnt(0)\n\t" \
  "v_smfmac_f32_16x16x64_bf16 v[36:39], v[60:63], v[24:31], v106\n\t" \
  "v_cvt_pk_bf16_f32 v76, v32, v33\n\t" \
  "v_cvt_pk_bf16_f32 v77, v34, v35\n\t" \
  "s_nop 5\n\t" \
  "v_cvt_pk_bf16_f32 v78, v36, v37\n\t" \
  "v_cvt_pk_bf16_f32 v79, v38, v39\n\t" \
  "s_nop 0\n\t" \
  "v_permlane16_swap_b32 v76, v78\n\t" \
  "v_permlane16_swap_b32 v77, v79\n\t" \
  "buffer_store_dwordx4 v[76:79], v108, %[ru], s78 offen sc1\n\t" \
  "s_memtime s[76:77]\n\t" \
  "s_mov_b32 s42, 0\n\t" \
  "s_waitcnt lgkmcnt(0)\n\t" \
  "s_mov_b32 s57, s76\n\t" \
  "s_add_i32 s41, s56, 1950\n\t" \
  "s_sub_i32 s76, s92, s51\n\t" \
  "s_sub_i32 s77, s93, s53\n\t" \
  "s_cmp_eq_u32 s76, 0\n\t" \
  "s_cbranch_scc1 21f\n\t" \
  "v_cvt_f32_u32 v117, s76\n\t" \
  "v_cvt_f32_u32 v116, s77\n\t" \
  "v_rcp_f32 v117, v117\n\t" \
  "s_mov_b32 s51, s92\n\t" \
  "v_mul_f32 v117, v117, v116\n\t" \
  "v_max_f32 v117, 0x3f028f5c, v117\n\t" \
  "v_min_f32 v117, 0x3ffeb852, v117\n\t" \
  "v_rcp_f32 v116, v117\n\t" \
  "v_mul_f32 v117, 0x4f000000, v117\n\t" \
  "v_mul_f32 v116, 0x47800000, v116\n\t" \
  "v_cvt_u32_f32 v117, v117\n\t" \
  "v_cvt_u32_f32 v116, v116\n\t" \
  "s_mov_b32 s53, s93\n\t" \
  "v_readfirstlane_b32 s46, v117\n\t" \
  "v_readfirstlane_b32 s52, v116\n\t" \
  "21:\n\t" \
  "s_add_i32 s44, s44, 1\n\t" \
  "s_and_b32 s58, s58, 4\n\t" \
  "s_and_b32 s76, s44, 64\n\t" \
  "s_or_b32 s42, s42, s76\n\t" \
  "s_cbranch_scc0 20b\n\t" \
  "s_branch 99f\n\t" \
  "14:\n\t" \
  "s_sleep 3\n\t" \
  "s_add_u32 s59, s59, 0x10000\n\t" \
  "s_cmp_lt_u32 s59, 0x400000\n\t" \
  "s_cbranch_scc1 13b\n\t" \
  "s_mov_b64 exec, -1\n\t" \
  "s_mov_b32 s42, 16\n\t" \
  "s_branch 99f\n\t" \
  "8:\n\t" \
  "s_bitcmp1_b32 s58, 1\n\t" \
  "s_cbranch_scc1 4b\n\t" \
  "s_bitcmp1_b32 s58, 2\n\t" \
  "s_cbranch_scc1 5b\n\t" \
  "v_max3_u32 v116, v32, v33, v34\n\t" \
  "v_max3_u32 v117, v35, v36, v37\n\t" \
  "v_max3_u32 v116, v116, v38, v39\n\t" \
  "v_max_u32 v116, v116, v117\n\t" \
  "v_cmp_eq_u32 vcc, -1, v116\n\t" \
  "s_cbranch_vccz 5b\n\t" \
  "s_branch 0f\n\t" \
  "9:\n\t" \
  "s_bitcmp1_b32 s58, 2\n\t" \
  "s_cbranch_scc1 6b\n\t" \
  "v_max3_u32 v116, v40, v41, v42\n\t" \
  "v_max3_u32 v117, v43, v44, v45\n\t" \
  "v_max3_u32 v116, v116, v46, v47\n\t" \
  "v_max_u32 v116, v116, v117\n\t" \
  "v_cmp_eq_u32 vcc, -1, v116\n\t" \
  "s_cbranch_vccz 6b\n\t" \
  "s_or_b32 s58, s58, 2\n\t" \
  "0:\n\t" \
  "s_or_b32 s58, s58, 1\n\t" \
  "11:\n\t" \
  "buffer_load_dwordx4 v[32:35], v100, %[rh], s40 offen sc1\n\t" \
  "buffer_load_dwordx4 v[36:39], v100, %[rh], s76 offen sc1\n\t" \
  "buffer_load_dwordx4 v[40:43], v100, %[rh], s77 offen sc1\n\t" \
  "buffer_load_dwordx4 v[44:47], v100, %[rh], s78 offen sc1\n\t" \
  "s_waitcnt lgkmcnt(0)\n\t" \
  "s_branch 10b\n\t" \
  "17:\n\t" \
  "s_add_u32 s59, s59, 1\n\t" \
  "s_bitcmp1_b32 s58, 2\n\t" \
  "s_cbranch_scc1 3b\n\t" \
  "s_waitcnt vmcnt(0)\n\t" \
  "v_max_u32 v116, v32, v33\n\t" \
  "v_max3_u32 v116, v116, v34, v35\n\t" \
  "v_max3_u32 v116, v116, v36, v37\n\t" \
  "v_max3_u32 v116, v116, v38, v39\n\t" \
  "v_max3_u32 v116, v116, v40, v41\n\t" \
  "v_max3_u32 v116, v116, v42, v43\n\t" \
  "v_max3_u32 v116, v116, v44, v45\n\t" \
  "v_max3_u32 v116, v116, v46, v47\n\t" \
  "v_cmp_eq_u32 vcc, -1, v116\n\t" \
  "s_cbranch_vccz 3b\n\t" \
  "s_bitcmp1_b32 s59, 6\n\t" \
  "s_cbranch_scc1 12f\n\t" \
  "buffer_load_dwordx4 v[32:35], v100, %[rh], s40 offen sc1\n\t" \
  "buffer_load_dwordx4 v[36:39], v100, %[rh], s76 offen sc1\n\t" \
  "buffer_load_dwordx4 v[40:43], v100, %[rh], s77 offen sc1\n\t" \
  "buffer_load_dwordx4 v[44:47], v100, %[rh], s78 offen sc1\n\t" \
  "s_branch 17b\n\t" \
  "12:\n\t" \
  "s_mov_b32 s42, s58\n\t" \
  "s_branch 99f\n\t" \
  "92:\n\t" \
  "s_mov_b32 s58, s42\n\t" \
  "20:\n\t" \
  "s_and_b32 s76, s44, 7\n\t" \
  "s_lshl_b32 s76, s76, 15\n\t" \
  "s_add_i32 s40, s54, s76\n\t" \
  "s_and_b32 s76, s44, 3\n\t" \
  "s_lshl_b32 s45, s76, 14\n\t" \
  "s_lshl_b32 s47, s76, 20\n\t" \
  "s_add_i32 s47, s47, s55\n\t" \
  "s_lshr_b32 s76, s47, 21\n\t" \
  "s_bitcmp1_b64 %[sel], s76\n\t" \
  "s_cselect_b32 s76, 0x2000, 0\n\t" \
  "s_add_i32 s47, s47, s76\n\t" \
  "s_add_i32 s49, s44, -1\n\t" \
  "s_and_b32 s48, s49, 2\n\t" \
  "s_lshl_b32 s48, s48, 13\n\t" \
  "s_and_b32 s76, s49, 1\n\t" \
  "s_lshl_b32 s76, s76, 12\n\t" \
  "s_add_i32 s48, s48, s76\n\t" \
  "s_lshl_b32 s50, s53, 8\n\t" \
  "s_mov_b32 s59, 0\n\t" \
  "s_mov_b32 s92, s51\n\t" \
  "s_mov_b32 s93, s53\n\t" \
  "s_xor_b32 s76, s40, 0x400\n\t" \
  "s_xor_b32 s77, s40, 0x800\n\t" \
  "s_xor_b32 s78, s40, 0xc00\n\t" \
  "s_cmp_lg_u32 s58, 0\n\t" \
  "s_cbranch_scc1 11f\n\t" \
  "10:\n\t" \
  "ds_read_b128 v[48:51], v102 offset:32768\n\t" \
  "ds_read_b128 v[52:55], v102 offset:33792\n\t" \
  "ds_read_b128 v[56:59], v102 offset:34816\n\t" \
  "ds_read_b128 v[60:63], v102 offset:35840\n\t" \
  "ds_read_b128 v[0:3], v102 offset:49152\n\t" \
  "ds_read_b128 v[4:7], v102 offset:50176\n\t" \
  "ds_read_b128 v[8:11], v102 offset:51200\n\t" \
  "ds_read_b128 v[12:15], v102 offset:52224\n\t" \
  "ds_read_b128 v[16:19], v102 offset:0\n\t" \
  "ds_read_b128 v[20:23], v102 offset:1024\n\t" \
  "ds_read_b128 v[24:27], v102 offset:2048\n\t" \
  "ds_read_b128 v[28:31], v102 offset:3072\n\t" \
  "s_waitcnt lgkmcnt(4)\n\t" \
  "ds_read_b128 v[80:83], v102 offset:16384\n\t" \
  "ds_read_b128 v[84:87], v102 offset:17408\n\t" \
  "ds_read_b128 v[88:91], v102 offset:18432\n\t" \
  "ds_read_b128 v[92:95], v102 offset:19456\n\t" \
  "46:\n\t" \
  "s_bitcmp1_b32 s58, 1\n\t" \
  "s_cselect_b64 s[80:81], -1, 0\n\t" \
  "s_cmp_lg_u32 s58, 0\n\t" \
  "s_cbranch_scc1 17f\n\t" \
  "s_mov_b64 exec, 0xff\n\t" \
  "16:\n\t" \
  "s_memtime vcc\n\t" \
  "s_waitcnt lgkmcnt(0)\n\t" \
  "s_sub_i32 s79, s41, vcc_lo\n\t" \
  "s_cmp_gt_i32 s79, 64\n\t" \
  "s_cbranch_scc1 16b\n\t" \
  "13:\n\t" \
  "buffer_load_dword v116, v112, %[rh], s48 offen sc1\n\t" \
  "s_memtime vcc\n\t" \
  "s_memrealtime s[82:83]\n\t" \
  "s_nop 4\n\t" \
  "s_memrealtime s[84:85]\n\t" \
  "s_nop 4\n\t" \
  "s_memrealtime s[56:57]\n\t" \
  "s_nop 4\n\t" \
  "s_memrealtime s[86:87]\n\t" \
  "s_nop 4\n\t" \
  "s_memrealtime s[88:89]\n\t" \
  "s_nop 4\n\t" \
  "s_memrealtime s[90:91]\n\t" \
  "s_mov_b32 s92, 0\n\t" \
  "s_waitcnt lgkmcnt(0)\n\t" \
  "s_cmp_eq_u32 s82, s84\n\t" \
  "s_addc_u32 s92, s92, 0\n\t" \
  "s_cmp_eq_u32 s82, s56\n\t" \
  "s_addc_u32 s92, s92, 0\n\t" \
  "s_cmp_eq_u32 s82, s86\n\t" \
  "s_addc_u32 s92, s92, 0\n\t" \
  "s_cmp_eq_u32 s82, s88\n\t" \
  "s_addc_u32 s92, s92, 0\n\t" \
  "s_cmp_eq_u32 s82, s90\n\t" \
  "s_addc_u32 s92, s92, 0\n\t" \
  "s_mul_i32 s92, s92, 24\n\t" \
  "s_add_i32 s92, s92, vcc_lo\n\t" \
  "s_lshl4_add_u32 s93, s82, 64\n\t" \
  "s_lshl_b32 s50, s93, 8\n\t" \
  "s_waitcnt vmcnt(0)\n\t" \
  "15:\n\t" \
  "v_mov_b32 v117, 20000\n\t" \
  "v_cmp_ne_u32_sdwa vcc, v116, s49 src0_sel:BYTE_0 src1_sel:DWORD\n\t" \
  "v_subrev_u32 v116, s50, v116\n\t" \
  "v_ashrrev_i32 v116, 8, v116\n\t" \
  "s_nop 1\n\t" \
  "v_max_i32_dpp v116, v116, v116 quad_perm:[1,0,3,2] row_mask:0xf bank_mask:0xf\n\t" \
  "s_cbranch_vccnz 14f\n\t" \
  "s_nop 0\n\t" \
  "v_max_i32_dpp v116, v116, v116 quad_perm:[2,3,0,1] row_mask:0xf bank_mask:0xf\n\t" \
  "s_nop 1\n\t" \
  "v_max_i32_dpp v116, v116, v116 row_half_mirror row_mask:0xf bank_mask:0xf\n\t" \
  "v_med3_i32 v116, v116, 0, v117\n\t" \
  "v_mul_u32_u24 v116, s52, v116\n\t" \
  "v_lshrrev_b32 v116, 16, v116\n\t" \
  "s_mov_b64 exec, -1\n\t" \
  "v_readfirstlane_b32 s84, v116\n\t" \
  "s_add_i32 s84, s84, s92\n\t" \
  ".p2align 6\n\t" \
  "s_nop 0\n\t" \
  "s_nop 0\n\t" \
  "s_nop 0\n\t" \
  "s_nop 0\n\t" \
  "s_nop 0\n\t" \
  "s_nop 0\n\t" \
  "s_nop 0\n\t" \
  "s_nop 0\n\t" \
  "1:\n\t" \
  "s_memtime vcc\n\t" \
  "s_waitcnt lgkmcnt(0)\n\t" \
  "s_sub_i32 s79, s84, vcc_lo\n\t" \
  "s_cmp_gt_i32 s79, 128\n\t" \
  "s_cbranch_scc1 1b\n\t" \
  ".p2align 4\n\t" \
  "2:\n\t" \
  "s_sub_i32 s79, s79, 33\n\t" \
  "s_cmp_gt_i32 s79, 56\n\t" \
  "s_cbranch_scc1 2b\n\t" \
  "buffer_load_dwordx4 v[32:35], v100, %[rh], s40 offen sc1\n\t" \
  "s_nop 12\n\t" \
  "buffer_load_dwordx4 v[36:39], v100, %[rh], s76 offen sc1\n\t" \
  "s_nop 12\n\t" \
  "buffer_load_dwordx4 v[40:43], v100, %[rh], s77 offen sc1\n\t" \
  "s_nop 12\n\t" \
  "buffer_load_dwordx4 v[44:47], v100, %[rh], s78 offen sc1\n\t" \
  "3:\n\t" \
  "v_mov_b64 v[72:73], 0\n\t" \
  "v_mov_b64 v[74:75], 0\n\t" \
  "v_mov_b64 v[76:77], 0\n\t" \
  "v_mov_b64 v[78:79], 0\n\t" \
  "s_waitcnt lgkmcnt(0)\n\t" \
  "s_waitcnt vmcnt(3)\n\t" \
  "v_smfmac_f32_16x16x64_bf16 v[72:75], v[32:35], v[48:55], v104\n\t" \
  "ds_write_b128 v101, v[32:35] offset:8192\n\t" \
  "v_smfmac_f32_16x16x64_bf16 v[76:79], v[32:35], v[56:63], v104\n\t" \
  "s_waitcnt vmcnt(2)\n\t" \
  "ds_write_b128 v101, v[36:39] offset:9216\n\t" \
  "v_smfmac_f32_16x16x64_bf16 v[72:75], v[36:39], a[96:103], v104\n\t" \
  "v_smfmac_f32_16x16x64_bf16 v[76:79], v[36:39], a[104:111], v104\n\t" \
  "s_nop 3\n\t" \
  "v_cmp_u_f32 vcc, v72, v73\n\t" \
  "v_cmp_u_f32 s[82:83], v74, v75\n\t" \
  "s_waitcnt vmcnt(1)\n\t" \
  "v_smfmac_f32_16x16x64_bf16 v[72:75], v[40:43], a[112:119], v104\n\t" \
  "s_or_b64 vcc, vcc, s[82:83]\n\t" \
  "s_or_b64 vcc, vcc, s[80:81]\n\t" \
  "v_smfmac_f32_16x16x64_bf16 v[76:79], v[40:43], a[120:127], v104\n\t" \
  "s_waitcnt lgkmcnt(0)\n\t" \
  "s_cbranch_scc1 8f\n\t" \
  "5:\n\t" \
  "s_barrier\n\t" \
  "4:\n\t" \
  "s_memtime s[56:57]\n\t" \
  "ds_read_b128 v[48:51], v101 offset:12288\n\t" \
  "ds_read_b128 v[52:55], v101 offset:13312\n\t" \
  "ds_read_b128 v[56:59], v101 offset:1024\n\t" \
  "s_waitcnt vmcnt(0)\n\t" \
  "v_smfmac_f32_16x16x64_bf16 v[72:75], v[44:47], a[128:135], v104\n\t" \
  "ds_read_b128 v[60:63], v101 offset:0\n\t" \
  "ds_read_b128 v[64:67], v101 offset:5120\n\t" \
  "v_smfmac_f32_16x16x64_bf16 v[76:79], v[44:47], a[136:143], v104\n\t" \
  "ds_read_b128 v[68:71], v101 offset:4096\n\t" \
  "ds_write_b128 v101, v[40:43] offset:10240\n\t" \
  "s_nop 1\n\t" \
  "v_cmp_u_f32 vcc, v72, v73\n\t" \
  "v_cmp_u_f32 s[82:83], v74, v75\n\t" \
  "s_waitcnt lgkmcnt(6)\n\t" \
  "v_smfmac_f32_16x16x64_bf16 v[72:75], v[48:51], v[0:7], v104\n\t" \
  "s_or_b64 vcc, vcc, s[82:83]\n\t" \
  "v_smfmac_f32_16x16x64_bf16 v[76:79], v[48:51], v[8:15], v104\n\t" \
  "s_cbranch_scc1 9f\n\t" \
  "6:\n\t" \
  "s_waitcnt lgkmcnt(5)\n\t" \
  "v_smfmac_f32_16x16x64_bf16 v[72:75], v[52:55], a[144:151], v104\n\t" \
  "ds_write_b128 v101, v[44:47] offset:11264\n\t" \
  "v_smfmac_f32_16x16x64_bf16 v[76:79], v[52:55], a[152:159], v104\n\t" \
  "v_mov_b64 v[40:41], 0\n\t" \
  "s_waitcnt lgkmcnt(5)\n\t" \
  "v_smfmac_f32_16x16x64_bf16 v[72:75], v[56:59], a[0:7], v104\n\t" \
  "v_mov_b64 v[42:43], 0\n\t" \
  "v_smfmac_f32_16x16x64_bf16 v[76:79], v[56:59], a[8:15], v104\n\t" \
  "s_waitcnt lgkmcnt(4)\n\t" \
  "v_smfmac_f32_16x16x64_bf16 v[72:75], v[60:63], v[16:23], v104\n\t" \
  "s_waitcnt lgkmcnt(0)\n\t" \
  "s_barrier\n\t" \
  "v_smfmac_f32_16x16x64_bf16 v[76:79], v[60:63], v[24:31], v104\n\t" \
  "ds_read_b128 v[48:51], v101 offset:14336\n\t" \
  "ds_read_b128 v[52:55], v101 offset:2048\n\t" \
  "v_smfmac_f32_16x16x64_bf16 v[72:75], v[64:67], a[48:55], v104\n\t" \
  "ds_read_b128 v[56:59], v101 offset:6144\n\t" \
  "ds_read_b128 v[60:63], v101 offset:15360\n\t" \
  "v_smfmac_f32_16x16x64_bf16 v[76:79], v[64:67], a[56:63], v104\n\t" \
  "ds_read_b128 v[32:35], v101 offset:3072\n\t" \
  "ds_read_b128 v[36:39], v101 offset:7168\n\t" \
  "v_smfmac_f32_16x16x64_bf16 v[72:75], v[68:71], v[80:87], v104\n\t" \
  "ds_read_b128 v[0:3], v103 offset:0\n\t" \
  "v_mov_b64 v[44:45], 0\n\t" \
  "v_smfmac_f32_16x16x64_bf16 v[76:79], v[68:71], v[88:95], v104\n\t" \
  "v_mov_b64 v[46:47], 0\n\t" \
  "s_waitcnt lgkmcnt(5)\n\t" \
  "v_smfmac_f32_16x16x64_bf16 v[72:75], v[48:51], a[160:167], v104\n\t" \
  "ds_read_b128 v[4:7], v103 offset:1024\n\t" \
  "v_mov_b64 v[80:81], 0\n\t" \
  "v_smfmac_f32_16x16x64_bf16 v[72:75], v[52:55], a[16:23], v104\n\t" \
  "v_mov_b64 v[82:83], 0\n\t" \
  "s_waitcnt lgkmcnt(2)\n\t" \
  "v_smfmac_f32_16x16x64_bf16 v[72:75], v[56:59], a[64:71], v104\n\t" \
  "ds_read_b128 v[8:11], v103 offset:2048\n\t" \
  "v_mov_b64 v[84:85], 0\n\t" \
  "v_smfmac_f32_16x16x64_bf16 v[72:75], v[60:63], a[176:183], v104\n\t" \
  "ds_read_b128 v[12:15], v103 offset:3072\n\t" \
  "v_mov_b64 v[86:87], 0\n\t" \
  "v_smfmac_f32_16x16x64_bf16 v[72:75], v[32:35], a[32:39], v104\n\t" \
  "ds_read_b128 v[16:19], v103 offset:16384\n\t" \
  "v_mov_b64 v[88:89], 0\n\t" \
  "v_smfmac_f32_16x16x64_bf16 v[72:75], v[36:39], a[80:87], v104\n\t" \
  "ds_read_b128 v[20:23], v103 offset:17408\n\t" \
  "v_mov_b64 v[90:91], 0\n\t" \
  "v_smfmac_f32_16x16x64_bf16 v[76:79], v[48:51], a[168:175], v104\n\t" \
  "ds_read_b128 v[24:27], v103 offset:18432\n\t" \
  "v_mov_b64 v[92:93], 0\n\t" \
  "v_smfmac_f32_16x16x64_bf16 v[76:79], v[52:55], a[24:31], v104\n\t" \
  "v_add_f32 v96, v72, v74\n\t" \
  "v_add_f32 v97, v73, v75\n\t" \
  "v_cvt_pk_bf16_f32 v96, v96, v97\n\t" \
  "ds_write_b32 v105, v96\n\t" \
  "v_smfmac_f32_16x16x64_bf16 v[76:79], v[56:59], a[72:79], v104\n\t" \
  "ds_read_b128 v[28:31], v103 offset:19456\n\t" \
  "v_mov_b64 v[94:95], 0\n\t" \
  "v_smfmac_f32_16x16x64_bf16 v[76:79], v[60:63], a[184:191], v104\n\t" \
  "v_smfmac_f32_16x16x64_bf16 v[76:79], v[32:35], a[40:47], v104\n\t" \
  "v_smfmac_f32_16x16x64_bf16 v[76:79], v[36:39], a[88:95], v104\n\t" \
  "s_waitcnt lgkmcnt(1)\n\t" \
  "s_barrier\n\t" \
  "ds_read_b64_tr_b16 v[96:97], v107 offset:0\n\t" \
  "ds_read_b64_tr_b16 v[98:99], v107 offset:64\n\t" \
  "ds_read_b64_tr_b16 v[56:57], v107 offset:1024\n\t" \
  "ds_read_b64_tr_b16 v[58:59], v107 offset:1088\n\t" \
  "s_nop 1\n\t" \
  "v_add_f32 v116, v76, v78\n\t" \
  "v_add_f32 v117, v77, v79\n\t" \
  "v_cvt_pk_bf16_f32 v116, v116, v117\n\t" \
  "ds_write_b32 v105, v116 offset:256\n\t" \
  "s_waitcnt lgkmcnt(3)\n\t" \
  "v_smfmac_f32_16x16x64_bf16 v[40:43], v[96:99], a[192:199], v106\n\t" \
  "v_smfmac_f32_16x16x64_bf16 v[44:47], v[96:99], a[224:231], v106\n\t" \
  "s_waitcnt lgkmcnt(0)\n\t" \
  "s_barrier\n\t" \
  "ds_read_b64_tr_b16 v[52:53], v107 offset:256\n\t" \
  "ds_read_b64_tr_b16 v[54:55], v107 offset:320\n\t" \
  "v_smfmac_f32_16x16x64_bf16 v[40:43], v[56:59], a[208:215], v106\n\t" \
  "ds_read_b64_tr_b16 v[60:61], v107 offset:1280\n\t" \
  "ds_read_b64_tr_b16 v[62:63], v107 offset:1344\n\t" \
  "v_smfmac_f32_16x16x64_bf16 v[44:47], v[56:59], v[0:7], v106\n\t" \
  "v_smfmac_f32_16x16x64_bf16 v[80:83], v[96:99], a[240:247], v106\n\t" \
  "v_mov_b64 v[32:33], 0\n\t" \
  "v_smfmac_f32_16x16x64_bf16 v[84:87], v[96:99], v[136:143], v106\n\t" \
  "v_mov_b64 v[34:35], 0\n\t" \
  "v_smfmac_f32_16x16x64_bf16 v[80:83], v[56:59], v[120:127], v106\n\t" \
  "v_mov_b64 v[36:37], 0\n\t" \
  "v_smfmac_f32_16x16x64_bf16 v[84:87], v[56:59], v[16:23], v106\n\t" \
  "s_waitcnt lgkmcnt(2)\n\t" \
  "v_smfmac_f32_16x16x64_bf16 v[40:43], v[52:55], a[200:207], v106\n\t" \
  "v_smfmac_f32_16x16x64_bf16 v[44:47], v[52:55], a[232:239], v106\n\t" \
  "s_xor_b32 s76, s47, 0x40000\n\t" \
  "s_waitcnt lgkmcnt(0)\n\t" \
  "v_smfmac_f32_16x16x64_bf16 v[40:43], v[60:63], a[216:223], v106\n\t" \
  "v_mov_b64 v[38:39], 0\n\t" \
  "s_xor_b32 s77, s47, 0x80000\n\t" \
  "v_smfmac_f32_16x16x64_bf16 v[44:47], v[60:63], v[8:15], v106\n\t" \
  "s_xor_b32 s78, s47, 0xc0000\n\t" \
  "v_smfmac_f32_16x16x64_bf16 v[80:83], v[52:55], a[248:255], v106\n\t" \
  "ds_read_b128 v[0:3], v103 offset:32768\n\t" \
  "v_cvt_pk_bf16_f32 v64, v40, v41\n\t" \
  "v_smfmac_f32_16x16x64_bf16 v[84:87], v[52:55], v[144:151], v106\n\t" \
  "ds_read_b128 v[4:7], v103 offset:33792\n\t" \
  "v_cvt_pk_bf16_f32 v65, v42, v43\n\t" \
  "v_smfmac_f32_16x16x64_bf16 v[80:83], v[60:63], v[128:135], v106\n\t" \
  "ds_read_b128 v[8:11], v103 offset:34816\n\t" \
  "v_cvt_pk_bf16_f32 v66, v44, v45\n\t" \
  "v_smfmac_f32_16x16x64_bf16 v[84:87], v[60:63], v[24:31], v106\n\t" \
  "ds_read_b128 v[12:15], v103 offset:35840\n\t" \
  "v_cvt_pk_bf16_f32 v67, v46, v47\n\t" \
  "v_smfmac_f32_16x16x64_bf16 v[88:91], v[96:99], v[152:159], v106\n\t" \
  "v_permlane16_swap_b32 v64, v66\n\t" \
  "v_cvt_pk_bf16_f32 v68, v80, v81\n\t" \
  "v_smfmac_f32_16x16x64_bf16 v[92:95], v[96:99], v[184:191], v106\n\t" \
  "v_permlane16_swap_b32 v65, v67\n\t" \
  "v_cvt_pk_bf16_f32 v69, v82, v83\n\t" \
  "v_smfmac_f32_16x16x64_bf16 v[88:91], v[52:55], v[160:167], v106\n\t" \
  "v_cvt_pk_bf16_f32 v70, v84, v85\n\t" \
  "v_smfmac_f32_16x16x64_bf16 v[92:95], v[52:55], v[192:199], v106\n\t" \
  "buffer_store_dwordx4 v[64:67], v108, %[ru], s47 offen sc1\n\t" \
  "v_cvt_pk_bf16_f32 v71, v86, v87\n\t" \
  "v_smfmac_f32_16x16x64_bf16 v[88:91], v[56:59], v[168:175], v106\n\t" \
  "ds_read_b128 v[16:19], v103 offset:49152\n\t" \
  "v_permlane16_swap_b32 v68, v70\n\t" \
  "s_waitcnt lgkmcnt(3)\n\t" \
  "v_smfmac_f32_16x16x64_bf16 v[92:95], v[56:59], v[0:7], v106\n\t" \
  "ds_read_b128 v[20:23], v103 offset:50176\n\t" \
  "v_permlane16_swap_b32 v69, v71\n\t" \
  "v_smfmac_f32_16x16x64_bf16 v[88:91], v[60:63], v[176:183], v106\n\t" \
  "ds_read_b128 v[24:27], v103 offset:51200\n\t" \
  "s_waitcnt lgkmcnt(3)\n\t" \
  "v_smfmac_f32_16x16x64_bf16 v[92:95], v[60:63], v[8:15], v106\n\t" \
  "ds_read_b128 v[28:31], v103 offset:52224\n\t" \
  "buffer_store_dwordx4 v[68:71], v108, %[ru], s76 offen sc1\n\t" \
  "v_smfmac_f32_16x16x64_bf16 v[32:35], v[96:99], v[200:207], v106\n\t" \
  "v_cvt_pk_bf16_f32 v72, v88, v89\n\t" \
  "v_smfmac_f32_16x16x64_bf16 v[36:39], v[96:99], v[232:239], v106\n\t" \
  "v_cvt_pk_bf16_f32 v73, v90, v91\n\t" \
  "v_smfmac_f32_16x16x64_bf16 v[32:35], v[52:55], v[208:215], v106\n\t" \
  "v_cvt_pk_bf16_f32 v74, v92, v93\n\t" \
  "v_smfmac_f32_16x16x64_bf16 v[36:39], v[52:55], v[240:247], v106\n\t" \
  "v_cvt_pk_bf16_f32 v75, v94, v95\n\t" \
  "v_smfmac_f32_16x16x64_bf16 v[32:35], v[56:59], v[216:223], v106\n\t" \
  "v_permlane16_swap_b32 v72, v74\n\t" \
  "v_smfmac_f32_16x16x64_bf16 v[32:35], v[60:63], v[224:231], v106\n\t" \
  "v_permlane16_swap_b32 v73, v75\n\t" \
  "s_waitcnt lgkmcnt(2)\n\t" \
  "v_smfmac_f32_16x16x64_bf16 v[36:39], v[56:59], v[16:23], v106\n\t" \
  "s_waitcnt lgkmcnt(0)\n\t" \
  "v_smfmac_f32_16x16x64_bf16 v[36:39], v[60:63], v[24:31], v106\n\t" \
  "buffer_store_dwordx4 v[72:75], v108, %[ru], s77 offen sc1\n\t" \
  "v_cvt_pk_bf16_f32 v76, v32, v33\n\t" \
  "v_cvt_pk_bf16_f32 v77, v34, v35\n\t" \
  "s_nop 5\n\t" \
  "v_cvt_pk_bf16_f32 v78, v36, v37\n\t" \
  "v_cvt_pk_bf16_f32 v79, v38, v39\n\t" \
  "s_nop 0\n\t" \
  "v_permlane16_swap_b32 v76, v78\n\t" \
  "v_permlane16_swap_b32 v77, v79\n\t" \
  "buffer_store_dwordx4 v[76:79], v108, %[ru], s78 offen sc1\n\t" \
  "s_memtime s[76:77]\n\t" \
  "s_mov_b32 s42, 0\n\t" \
  "s_waitcnt lgkmcnt(0)\n\t" \
  "s_mov_b32 s57, s76\n\t" \
  "s_add_i32 s41, s56, 1950\n\t" \
  "s_sub_i32 s76, s92, s51\n\t" \
  "s_sub_i32 s77, s93, s53\n\t" \
  "s_cmp_eq_u32 s76, 0\n\t" \
  "s_cbranch_scc1 21f\n\t" \
  "v_cvt_f32_u32 v117, s76\n\t" \
  "v_cvt_f32_u32 v116, s77\n\t" \
  "v_rcp_f32 v117, v117\n\t" \
  "s_mov_b32 s51, s92\n\t" \
  "v_mul_f32 v117, v117, v116\n\t" \
  "v_max_f32 v117, 0x3f028f5c, v117\n\t" \
  "v_min_f32 v117, 0x3ffeb852, v117\n\t" \
  "v_rcp_f32 v116, v117\n\t" \
  "v_mul_f32 v117, 0x4f000000, v117\n\t" \
  "v_mul_f32 v116, 0x47800000, v116\n\t" \
  "v_cvt_u32_f32 v117, v117\n\t" \
  "v_cvt_u32_f32 v116, v116\n\t" \
  "s_mov_b32 s53, s93\n\t" \
  "v_readfirstlane_b32 s46, v117\n\t" \
  "v_readfirstlane_b32 s52, v116\n\t" \
  "21:\n\t" \
  "s_add_i32 s44, s44, 1\n\t" \
  "s_and_b32 s58, s58, 4\n\t" \
  "s_and_b32 s76, s44, 64\n\t" \
  "s_or_b32 s42, s42, s76\n\t" \
  "s_cbranch_scc0 20b\n\t" \
  "s_branch 99f\n\t" \
  "14:\n\t" \
  "s_sleep 3\n\t" \
  "s_add_u32 s59, s59, 0x10000\n\t" \
  "s_cmp_lt_u32 s59, 0x400000\n\t" \
  "s_cbranch_scc1 13b\n\t" \
  "s_mov_b64 exec, -1\n\t" \
  "s_mov_b32 s42, 16\n\t" \
  "s_branch 99f\n\t" \
  "8:\n\t" \
  "s_bitcmp1_b32 s58, 1\n\t" \
  "s_cbranch_scc1 4b\n\t" \
  "s_bitcmp1_b32 s58, 2\n\t" \
  "s_cbranch_scc1 5b\n\t" \
  "v_max3_u32 v116, v32, v33, v34\n\t" \
  "v_max3_u32 v117, v35, v36, v37\n\t" \
  "v_max3_u32 v116, v116, v38, v39\n\t" \
  "v_max_u32 v116, v116, v117\n\t" \
  "v_cmp_eq_u32 vcc, -1, v116\n\t" \
  "s_cbranch_vccz 5b\n\t" \
  "s_branch 0f\n\t" \
  "9:\n\t" \
  "s_bitcmp1_b32 s58, 2\n\t" \
  "s_cbranch_scc1 6b\n\t" \
  "v_max3_u32 v116, v40, v41, v42\n\t" \
  "v_max3_u32 v117, v43, v44, v45\n\t" \
  "v_max3_u32 v116, v116, v46, v47\n\t" \
  "v_max_u32 v116, v116, v117\n\t" \
  "v_cmp_eq_u32 vcc, -1, v116\n\t" \
  "s_cbranch_vccz 6b\n\t" \
  "s_or_b32 s58, s58, 2\n\t" \
  "0:\n\t" \
  "s_or_b32 s58, s58, 1\n\t" \
  "11:\n\t" \
  "buffer_load_dwordx4 v[32:35], v100, %[rh], s40 offen sc1\n\t" \
  "buffer_load_dwordx4 v[36:39], v100, %[rh], s76 offen sc1\n\t" \
  "buffer_load_dwordx4 v[40:43], v100, %[rh], s77 offen sc1\n\t" \
  "buffer_load_dwordx4 v[44:47], v100, %[rh], s78 offen sc1\n\t" \
  "s_waitcnt lgkmcnt(0)\n\t" \
  "s_branch 10b\n\t" \
  "17:\n\t" \
  "s_add_u32 s59, s59, 1\n\t" \
  "s_bitcmp1_b32 s58, 2\n\t" \
  "s_cbranch_scc1 3b\n\t" \
  "s_waitcnt vmcnt(0)\n\t" \
  "v_max_u32 v116, v32, v33\n\t" \
  "v_max3_u32 v116, v116, v34, v35\n\t" \
  "v_max3_u32 v116, v116, v36, v37\n\t" \
  "v_max3_u32 v116, v116, v38, v39\n\t" \
  "v_max3_u32 v116, v116, v40, v41\n\t" \
  "v_max3_u32 v116, v116, v42, v43\n\t" \
  "v_max3_u32 v116, v116, v44, v45\n\t" \
  "v_max3_u32 v116, v116, v46, v47\n\t" \
  "v_cmp_eq_u32 vcc, -1, v116\n\t" \
  "s_cbranch_vccz 3b\n\t" \
  "s_bitcmp1_b32 s59, 6\n\t" \
  "s_cbranch_scc1 12f\n\t" \
  "buffer_load_dwordx4 v[32:35], v100, %[rh], s40 offen sc1\n\t" \
  "buffer_load_dwordx4 v[36:39], v100, %[rh], s76 offen sc1\n\t" \
  "buffer_load_dwordx4 v[40:43], v100, %[rh], s77 offen sc1\n\t" \
  "buffer_load_dwordx4 v[44:47], v100, %[rh], s78 offen sc1\n\t" \
  "s_branch 17b\n\t" \
  "12:\n\t" \
  "s_mov_b32 s42, s58\n\t" \
  "s_branch 99f\n\t" \
  "93:\n\t" \
  "s_mov_b32 s58, s42\n\t" \
  "20:\n\t" \
  "s_and_b32 s76, s44, 7\n\t" \
  "s_lshl_b32 s76, s76, 15\n\t" \
  "s_add_i32 s40, s54, s76\n\t" \
  "s_and_b32 s76, s44, 3\n\t" \
  "s_lshl_b32 s45, s76, 14\n\t" \
  "s_lshl_b32 s47, s76, 20\n\t" \
  "s_add_i32 s47, s47, s55\n\t" \
  "s_lshr_b32 s76, s47, 21\n\t" \
  "s_bitcmp1_b64 %[sel], s76\n\t" \
  "s_cselect_b32 s76, 0x2000, 0\n\t" \
  "s_add_i32 s47, s47, s76\n\t" \
  "s_add_i32 s49, s44, -1\n\t" \
  "s_and_b32 s48, s49, 2\n\t" \
  "s_lshl_b32 s48, s48, 13\n\t" \
  "s_and_b32 s76, s49, 1\n\t" \
  "s_lshl_b32 s76, s76, 12\n\t" \
  "s_add_i32 s48, s48, s76\n\t" \
  "s_lshl_b32 s50, s53, 8\n\t" \
  "s_mov_b32 s59, 0\n\t" \
  "s_mov_b32 s92, s51\n\t" \
  "s_mov_b32 s93, s53\n\t" \
  "s_xor_b32 s76, s40, 0x400\n\t" \
  "s_xor_b32 s77, s40, 0x800\n\t" \
  "s_xor_b32 s78, s40, 0xc00\n\t" \
  "s_cmp_lg_u32 s58, 0\n\t" \
  "s_cbranch_scc1 11f\n\t" \
  "10:\n\t" \
  "ds_read_b128 v[48:51], v102 offset:49152\n\t" \
  "ds_read_b128 v[52:55], v102 offset:50176\n\t" \
  "ds_read_b128 v[56:59], v102 offset:51200\n\t" \
  "ds_read_b128 v[60:63], v102 offset:52224\n\t" \
  "ds_read_b128 v[0:3], v102 offset:0\n\t" \
  "ds_read_b128 v[4:7], v102 offset:1024\n\t" \
  "ds_read_b128 v[8:11], v102 offset:2048\n\t" \
  "ds_read_b128 v[12:15], v102 offset:3072\n\t" \
  "ds_read_b128 v[16:19], v102 offset:16384\n\t" \
  "ds_read_b128 v[20:23], v102 offset:17408\n\t" \
  "ds_read_b128 v[24:27], v102 offset:18432\n\t" \
  "ds_read_b128 v[28:31], v102 offset:19456\n\t" \
  "s_waitcnt lgkmcnt(4)\n\t" \
  "ds_read_b128 v[80:83], v102 offset:32768\n\t" \
  "ds_read_b128 v[84:87], v102 offset:33792\n\t" \
  "ds_read_b128 v[88:91], v102 offset:34816\n\t" \
  "ds_read_b128 v[92:95], v102 offset:35840\n\t" \
  "46:\n\t" \
  "s_bitcmp1_b32 s58, 1\n\t" \
  "s_cselect_b64 s[80:81], -1, 0\n\t" \
  "s_cmp_lg_u32 s58, 0\n\t" \
  "s_cbranch_scc1 17f\n\t" \
  "s_mov_b64 exec, 0xff\n\t" \
  "16:\n\t" \
  "s_memtime vcc\n\t" \
  "s_waitcnt lgkmcnt(0)\n\t" \
  "s_sub_i32 s79, s41, vcc_lo\n\t" \
  "s_cmp_gt_i32 s79, 64\n\t" \
  "s_cbranch_scc1 16b\n\t" \
  "13:\n\t" \
  "buffer_load_dword v116, v112, %[rh], s48 offen sc1\n\t" \
  "s_memtime vcc\n\t" \
  "s_memrealtime s[82:83]\n\t" \
  "s_nop 4\n\t" \
  "s_memrealtime s[84:85]\n\t" \
  "s_nop 4\n\t" \
  "s_memrealtime s[56:57]\n\t" \
  "s_nop 4\n\t" \
  "s_memrealtime s[86:87]\n\t" \
  "s_nop 4\n\t" \
  "s_memrealtime s[88:89]\n\t" \
  "s_nop 4\n\t" \
  "s_memrealtime s[90:91]\n\t" \
  "s_mov_b32 s92, 0\n\t" \
  "s_waitcnt lgkmcnt(0)\n\t" \
  "s_cmp_eq_u32 s82, s84\n\t" \
  "s_addc_u32 s92, s92, 0\n\t" \
  "s_cmp_eq_u32 s82, s56\n\t" \
  "s_addc_u32 s92, s92, 0\n\t" \
  "s_cmp_eq_u32 s82, s86\n\t" \
  "s_addc_u32 s92, s92, 0\n\t" \
  "s_cmp_eq_u32 s82, s88\n\t" \
  "s_addc_u32 s92, s92, 0\n\t" \
  "s_cmp_eq_u32 s82, s90\n\t" \
  "s_addc_u32 s92, s92, 0\n\t" \
  "s_mul_i32 s92, s92, 24\n\t" \
  "s_add_i32 s92, s92, vcc_lo\n\t" \
  "s_lshl4_add_u32 s93, s82, 64\n\t" \
  "s_lshl_b32 s50, s93, 8\n\t" \
  "s_waitcnt vmcnt(0)\n\t" \
  "15:\n\t" \
  "v_mov_b32 v117, 20000\n\t" \
  "v_cmp_ne_u32_sdwa vcc, v116, s49 src0_sel:BYTE_0 src1_sel:DWORD\n\t" \
  "v_subrev_u32 v116, s50, v116\n\t" \
  "v_ashrrev_i32 v116, 8, v116\n\t" \
  "s_nop 1\n\t" \
  "v_max_i32_dpp v116, v116, v116 quad_perm:[1,0,3,2] row_mask:0xf bank_mask:0xf\n\t" \
  "s_cbranch_vccnz 14f\n\t" \
  "s_nop 0\n\t" \
  "v_max_i32_dpp v116, v116, v116 quad_perm:[2,3,0,1] row_mask:0xf bank_mask:0xf\n\t" \
  "s_nop 1\n\t" \
  "v_max_i32_dpp v116, v116, v116 row_half_mirror row_mask:0xf bank_mask:0xf\n\t" \
  "v_med3_i32 v116, v116, 0, v117\n\t" \
  "v_mul_u32_u24 v116, s52, v116\n\t" \
  "v_lshrrev_b32 v116, 16, v116\n\t" \
  "s_mov_b64 exec, -1\n\t" \
  "v_readfirstlane_b32 s84, v116\n\t" \
  "s_add_i32 s84, s84, s92\n\t" \
  ".p2align 6\n\t" \
  "s_nop 0\n\t" \
  "s_nop 0\n\t" \
  "s_nop 0\n\t" \
  "s_nop 0\n\t" \
  "s_nop 0\n\t" \
  "s_nop 0\n\t" \
  "s_nop 0\n\t" \
  "s_nop 0\n\t" \
  "1:\n\t" \
  "s_memtime vcc\n\t" \
  "s_waitcnt lgkmcnt(0)\n\t" \
  "s_sub_i32 s79, s84, vcc_lo\n\t" \
  "s_cmp_gt_i32 s79, 128\n\t" \
  "s_cbranch_scc1 1b\n\t" \
  ".p2align 4\n\t" \
  "2:\n\t" \
  "s_sub_i32 s79, s79, 33\n\t" \
  "s_cmp_gt_i32 s79, 56\n\t" \
  "s_cbranch_scc1 2b\n\t" \
  "buffer_load_dwordx4 v[32:35], v100, %[rh], s40 offen sc1\n\t" \
  "s_nop 12\n\t" \
  "buffer_load_dwordx4 v[36:39], v100, %[rh], s76 offen sc1\n\t" \
  "s_nop 12\n\t" \
  "buffer_load_dwordx4 v[40:43], v100, %[rh], s77 offen sc1\n\t" \
  "s_nop 12\n\t" \
  "buffer_load_dwordx4 v[44:47], v100, %[rh], s78 offen sc1\n\t" \
  "3:\n\t" \
  "v_mov_b64 v[72:73], 0\n\t" \
  "v_mov_b64 v[74:75], 0\n\t" \
  "v_mov_b64 v[76:77], 0\n\t" \
  "v_mov_b64 v[78:79], 0\n\t" \
  "s_waitcnt lgkmcnt(0)\n\t" \
  "s_waitcnt vmcnt(3)\n\t" \
  "v_smfmac_f32_16x16x64_bf16 v[72:75], v[32:35], v[48:55], v104\n\t" \
  "ds_write_b128 v101, v[32:35] offset:12288\n\t" \
  "v_smfmac_f32_16x16x64_bf16 v[76:79], v[32:35], v[56:63], v104\n\t" \
  "s_waitcnt vmcnt(2)\n\t" \
  "ds_write_b128 v101, v[36:39] offset:13312\n\t" \
  "v_smfmac_f32_16x16x64_bf16 v[72:75], v[36:39], a[144:151], v104\n\t" \
  "v_smfmac_f32_16x16x64_bf16 v[76:79], v[36:39], a[152:159], v104\n\t" \
  "s_nop 3\n\t" \
  "v_cmp_u_f32 vcc, v72, v73\n\t" \
  "v_cmp_u_f32 s[82:83], v74, v75\n\t" \
  "s_waitcnt vmcnt(1)\n\t" \
  "v_smfmac_f32_16x16x64_bf16 v[72:75], v[40:43], a[160:167], v104\n\t" \
  "s_or_b64 vcc, vcc, s[82:83]\n\t" \
  "s_or_b64 vcc, vcc, s[80:81]\n\t" \
  "v_smfmac_f32_16x16x64_bf16 v[76:79], v[40:43], a[168:175], v104\n\t" \
  "s_waitcnt lgkmcnt(0)\n\t" \
  "s_cbranch_scc1 8f\n\t" \
  "5:\n\t" \
  "s_barrier\n\t" \
  "4:\n\t" \
  "s_memtime s[56:57]\n\t" \
  "ds_read_b128 v[48:51], v101 offset:0\n\t" \
  "ds_read_b128 v[52:55], v101 offset:1024\n\t" \
  "ds_read_b128 v[56:59], v101 offset:5120\n\t" \
  "s_waitcnt vmcnt(0)\n\t" \
  "v_smfmac_f32_16x16x64_bf16 v[72:75], v[44:47], a[176:183], v104\n\t" \
  "ds_read_b128 v[60:63], v101 offset:4096\n\t" \
  "ds_read_b128 v[64:67], v101 offset:9216\n\t" \
  "v_smfmac_f32_16x16x64_bf16 v[76:79], v[44:47], a[184:191], v104\n\t" \
  "ds_read_b128 v[68:71], v101 offset:8192\n\t" \
  "ds_write_b128 v101, v[40:43] offset:14336\n\t" \
  "s_nop 1\n\t" \
  "v_cmp_u_f32 vcc, v72, v73\n\t" \
  "v_cmp_u_f32 s[82:83], v74, v75\n\t" \
  "s_waitcnt lgkmcnt(6)\n\t" \
  "v_smfmac_f32_16x16x64_bf16 v[72:75], v[48:51], v[0:7], v104\n\t" \
  "s_or_b64 vcc, vcc, s[82:83]\n\t" \
  "v_smfmac_f32_16x16x64_bf16 v[76:79], v[48:51], v[8:15], v104\n\t" \
  "s_cbranch_scc1 9f\n\t" \
  "6:\n\t" \
  "s_waitcnt lgkmcnt(5)\n\t" \
  "v_smfmac_f32_16x16x64_bf16 v[72:75], v[52:55], a[0:7], v104\n\t" \
  "ds_write_b128 v101, v[44:47] offset:15360\n\t" \
  "v_smfmac_f32_16x16x64_bf16 v[76:79], v[52:55], a[8:15], v104\n\t" \
  "v_mov_b64 v[40:41], 0\n\t" \
  "s_waitcnt lgkmcnt(5)\n\t" \
  "v_smfmac_f32_16x16x64_bf16 v[72:75], v[56:59], a[48:55], v104\n\t" \
  "v_mov_b64 v[42:43], 0\n\t" \
  "v_smfmac_f32_16x16x64_bf16 v[76:79], v[56:59], a[56:63], v104\n\t" \
  "s_waitcnt lgkmcnt(4)\n\t" \
  "v_smfmac_f32_16x16x64_bf16 v[72:75], v[60:63], v[16:23], v104\n\t" \
  "s_waitcnt lgkmcnt(0)\n\t" \
  "s_barrier\n\t" \
  "v_smfmac_f32_16x16x64_bf16 v[76:79], v[60:63], v[24:31], v104\n\t" \
  "ds_read_b128 v[48:51], v101 offset:2048\n\t" \
  "ds_read_b128 v[52:55], v101 offset:6144\n\t" \
  "v_smfmac_f32_16x16x64_bf16 v[72:75], v[64:67], a[96:103], v104\n\t" \
  "ds_read_b128 v[56:59], v101 offset:10240\n\t" \
  "ds_read_b128 v[60:63], v101 offset:3072\n\t" \
  "v_smfmac_f32_16x16x64_bf16 v[76:79], v[64:67], a[104:111], v104\n\t" \
  "ds_read_b128 v[32:35], v101 offset:7168\n\t" \
  "ds_read_b128 v[36:39], v101 offset:11264\n\t" \
  "v_smfmac_f32_16x16x64_bf16 v[72:75], v[68:71], v[80:87], v104\n\t" \
  "ds_read_b128 v[0:3], v103 offset:0\n\t" \
  "v_mov_b64 v[44:45], 0\n\t" \
  "v_smfmac_f32_16x16x64_bf16 v[76:79], v[68:71], v[88:95], v104\n\t" \
  "v_mov_b64 v[46:47], 0\n\t" \
  "s_waitcnt lgkmcnt(5)\n\t" \
  "v_smfmac_f32_16x16x64_bf16 v[72:75], v[48:51], a[16:23], v104\n\t" \
  "ds_read_b128 v[4:7], v103 offset:1024\n\t" \
  "v_mov_b64 v[80:81], 0\n\t" \
  "v_smfmac_f32_16x16x64_bf16 v[72:75], v[52:55], a[64:71], v104\n\t" \
  "v_mov_b64 v[82:83], 0\n\t" \
  "s_waitcnt lgkmcnt(2)\n\t" \
  "v_smfmac_f32_16x16x64_bf16 v[72:75], v[56:59], a[112:119], v104\n\t" \
  "ds_read_b128 v[8:11], v103 offset:2048\n\t" \
  "v_mov_b64 v[84:85], 0\n\t" \
  "v_smfmac_f32_16x16x64_bf16 v[72:75], v[60:63], a[32:39], v104\n\t" \
  "ds_read_b128 v[12:15], v103 offset:3072\n\t" \
  "v_mov_b64 v[86:87], 0\n\t" \
  "v_smfmac_f32_16x16x64_bf16 v[72:75], v[32:35], a[80:87], v104\n\t" \
  "ds_read_b128 v[16:19], v103 offset:16384\n\t" \
  "v_mov_b64 v[88:89], 0\n\t" \
  "v_smfmac_f32_16x16x64_bf16 v[72:75], v[36:39], a[128:135], v104\n\t" \
  "ds_read_b128 v[20:23], v103 offset:17408\n\t" \
  "v_mov_b64 v[90:91], 0\n\t" \
  "v_smfmac_f32_16x16x64_bf16 v[76:79], v[48:51], a[24:31], v104\n\t" \
  "ds_read_b128 v[24:27], v103 offset:18432\n\t" \
  "v_mov_b64 v[92:93], 0\n\t" \
  "v_smfmac_f32_16x16x64_bf16 v[76:79], v[52:55], a[72:79], v104\n\t" \
  "v_add_f32 v96, v72, v74\n\t" \
  "v_add_f32 v97, v73, v75\n\t" \
  "v_cvt_pk_bf16_f32 v96, v96, v97\n\t" \
  "ds_write_b32 v105, v96\n\t" \
  "v_smfmac_f32_16x16x64_bf16 v[76:79], v[56:59], a[120:127], v104\n\t" \
  "ds_read_b128 v[28:31], v103 offset:19456\n\t" \
  "v_mov_b64 v[94:95], 0\n\t" \
  "v_smfmac_f32_16x16x64_bf16 v[76:79], v[60:63], a[40:47], v104\n\t" \
  "v_smfmac_f32_16x16x64_bf16 v[76:79], v[32:35], a[88:95], v104\n\t" \
  "v_smfmac_f32_16x16x64_bf16 v[76:79], v[36:39], a[136:143], v104\n\t" \
  "s_waitcnt lgkmcnt(1)\n\t" \
  "s_barrier\n\t" \
  "ds_read_b64_tr_b16 v[96:97], v107 offset:0\n\t" \
  "ds_read_b64_tr_b16 v[98:99], v107 offset:64\n\t" \
  "ds_read_b64_tr_b16 v[56:57], v107 offset:1024\n\t" \
  "ds_read_b64_tr_b16 v[58:59], v107 offset:1088\n\t" \
  "s_nop 1\n\t" \
  "v_add_f32 v116, v76, v78\n\t" \
  "v_add_f32 v117, v77, v79\n\t" \
  "v_cvt_pk_bf16_f32 v116, v116, v117\n\t" \
  "ds_write_b32 v105, v116 offset:256\n\t" \
  "s_waitcnt lgkmcnt(3)\n\t" \
  "v_smfmac_f32_16x16x64_bf16 v[40:43], v[96:99], a[192:199], v106\n\t" \
  "v_smfmac_f32_16x16x64_bf16 v[44:47], v[96:99], a[224:231], v106\n\t" \
  "s_waitcnt lgkmcnt(0)\n\t" \
  "s_barrier\n\t" \
  "ds_read_b64_tr_b16 v[52:53], v107 offset:256\n\t" \
  "ds_read_b64_tr_b16 v[54:55], v107 offset:320\n\t" \
  "v_smfmac_f32_16x16x64_bf16 v[40:43], v[56:59], a[208:215], v106\n\t" \
  "ds_read_b64_tr_b16 v[60:61], v107 offset:1280\n\t" \
  "ds_read_b64_tr_b16 v[62:63], v107 offset:1344\n\t" \
  "v_smfmac_f32_16x16x64_bf16 v[44:47], v[56:59], v[0:7], v106\n\t" \
  "v_smfmac_f32_16x16x64_bf16 v[80:83], v[96:99], a[240:247], v106\n\t" \
  "v_mov_b64 v[32:33], 0\n\t" \
  "v_smfmac_f32_16x16x64_bf16 v[84:87], v[96:99], v[136:143], v106\n\t" \
  "v_mov_b64 v[34:35], 0\n\t" \
  "v_smfmac_f32_16x16x64_bf16 v[80:83], v[56:59], v[120:127], v106\n\t" \
  "v_mov_b64 v[36:37], 0\n\t" \
  "v_smfmac_f32_16x16x64_bf16 v[84:87], v[56:59], v[16:23], v106\n\t" \
  "s_waitcnt lgkmcnt(2)\n\t" \
  "v_smfmac_f32_16x16x64_bf16 v[40:43], v[52:55], a[200:207], v106\n\t" \
  "v_smfmac_f32_16x16x64_bf16 v[44:47], v[52:55], a[232:239], v106\n\t" \
  "s_xor_b32 s76, s47, 0x40000\n\t" \
  "s_waitcnt lgkmcnt(0)\n\t" \
  "v_smfmac_f32_16x16x64_bf16 v[40:43], v[60:63], a[216:223], v106\n\t" \
  "v_mov_b64 v[38:39], 0\n\t" \
  "s_xor_b32 s77, s47, 0x80000\n\t" \
  "v_smfmac_f32_16x16x64_bf16 v[44:47], v[60:63], v[8:15], v106\n\t" \
  "s_xor_b32 s78, s47, 0xc0000\n\t" \
  "v_smfmac_f32_16x16x64_bf16 v[80:83], v[52:55], a[248:255], v106\n\t" \
  "ds_read_b128 v[0:3], v103 offset:32768\n\t" \
  "v_cvt_pk_bf16_f32 v64, v40, v41\n\t" \
  "v_smfmac_f32_16x16x64_bf16 v[84:87], v[52:55], v[144:151], v106\n\t" \
  "ds_read_b128 v[4:7], v103 offset:33792\n\t" \
  "v_cvt_pk_bf16_f32 v65, v42, v43\n\t" \
  "v_smfmac_f32_16x16x64_bf16 v[80:83], v[60:63], v[128:135], v106\n\t" \
  "ds_read_b128 v[8:11], v103 offset:34816\n\t" \
  "v_cvt_pk_bf16_f32 v66, v44, v45\n\t" \
  "v_smfmac_f32_16x16x64_bf16 v[84:87], v[60:63], v[24:31], v106\n\t" \
  "ds_read_b128 v[12:15], v103 offset:35840\n\t" \
  "v_cvt_pk_bf16_f32 v67, v46, v47\n\t" \
  "v_smfmac_f32_16x16x64_bf16 v[88:91], v[96:99], v[152:159], v106\n\t" \
  "v_permlane16_swap_b32 v64, v66\n\t" \
  "v_cvt_pk_bf16_f32 v68, v80, v81\n\t" \
  "v_smfmac_f32_16x16x64_bf16 v[92:95], v[96:99], v[184:191], v106\n\t" \
  "v_permlane16_swap_b32 v65, v67\n\t" \
  "v_cvt_pk_bf16_f32 v69, v82, v83\n\t" \
  "v_smfmac_f32_16x16x64_bf16 v[88:91], v[52:55], v[160:167], v106\n\t" \
  "v_cvt_pk_bf16_f32 v70, v84, v85\n\t" \
  "v_smfmac_f32_16x16x64_bf16 v[92:95], v[52:55], v[192:199], v106\n\t" \
  "v_cvt_pk_bf16_f32 v71, v86, v87\n\t" \
  "v_smfmac_f32_16x16x64_bf16 v[88:91], v[56:59], v[168:175], v106\n\t" \
  "ds_read_b128 v[16:19], v103 offset:49152\n\t" \
  "buffer_store_dwordx4 v[64:67], v108, %[ru], s47 offen sc1\n\t" \
  "v_permlane16_swap_b32 v68, v70\n\t" \
  "s_waitcnt lgkmcnt(3)\n\t" \
  "v_smfmac_f32_16x16x64_bf16 v[92:95], v[56:59], v[0:7], v106\n\t" \
  "ds_read_b128 v[20:23], v103 offset:50176\n\t" \
  "v_permlane16_swap_b32 v69, v71\n\t" \
  "v_smfmac_f32_16x16x64_bf16 v[88:91], v[60:63], v[176:183], v106\n\t" \
  "ds_read_b128 v[24:27], v103 offset:51200\n\t" \
  "s_waitcnt lgkmcnt(3)\n\t" \
  "v_smfmac_f32_16x16x64_bf16 v[92:95], v[60:63], v[8:15], v106\n\t" \
  "ds_read_b128 v[28:31], v103 offset:52224\n\t" \
  "v_smfmac_f32_16x16x64_bf16 v[32:35], v[96:99], v[200:207], v106\n\t" \
  "buffer_store_dwordx4 v[68:71], v108, %[ru], s76 offen sc1\n\t" \
  "v_cvt_pk_bf16_f32 v72, v88, v89\n\t" \
  "v_smfmac_f32_16x16x64_bf16 v[36:39], v[96:99], v[232:239], v106\n\t" \
  "v_cvt_pk_bf16_f32 v73, v90, v91\n\t" \
  "v_smfmac_f32_16x16x64_bf16 v[32:35], v[52:55], v[208:215], v106\n\t" \
  "v_cvt_pk_bf16_f32 v74, v92, v93\n\t" \
  "v_smfmac_f32_16x16x64_bf16 v[36:39], v[52:55], v[240:247], v106\n\t" \
  "v_cvt_pk_bf16_f32 v75, v94, v95\n\t" \
  "v_smfmac_f32_16x16x64_bf16 v[32:35], v[56:59], v[216:223], v106\n\t" \
  "v_permlane16_swap_b32 v72, v74\n\t" \
  "v_smfmac_f32_16x16x64_bf16 v[32:35], v[60:63], v[224:231], v106\n\t" \
  "v_permlane16_swap_b32 v73, v75\n\t" \
  "s_waitcnt lgkmcnt(2)\n\t" \
  "v_smfmac_f32_16x16x64_bf16 v[36:39], v[56:59], v[16:23], v106\n\t" \
  "s_waitcnt lgkmcnt(0)\n\t" \
  "v_smfmac_f32_16x16x64_bf16 v[36:39], v[60:63], v[24:31], v106\n\t" \
  "buffer_store_dwordx4 v[72:75], v108, %[ru], s77 offen sc1\n\t" \
  "v_cvt_pk_bf16_f32 v76, v32, v33\n\t" \
  "v_cvt_pk_bf16_f32 v77, v34, v35\n\t" \
  "s_nop 5\n\t" \
  "v_cvt_pk_bf16_f32 v78, v36, v37\n\t" \
  "v_cvt_pk_bf16_f32 v79, v38, v39\n\t" \
  "s_nop 0\n\t" \
  "v_permlane16_swap_b32 v76, v78\n\t" \
  "v_permlane16_swap_b32 v77, v79\n\t" \
  "buffer_store_dwordx4 v[76:79], v108, %[ru], s78 offen sc1\n\t" \
  "s_memtime s[76:77]\n\t" \
  "s_mov_b32 s42, 0\n\t" \
  "s_waitcnt lgkmcnt(0)\n\t" \
  "s_mov_b32 s57, s76\n\t" \
  "s_add_i32 s41, s56, 1950\n\t" \
  "s_sub_i32 s76, s92, s51\n\t" \
  "s_sub_i32 s77, s93, s53\n\t" \
  "s_cmp_eq_u32 s76, 0\n\t" \
  "s_cbranch_scc1 21f\n\t" \
  "v_cvt_f32_u32 v117, s76\n\t" \
  "v_cvt_f32_u32 v116, s77\n\t" \
  "v_rcp_f32 v117, v117\n\t" \
  "s_mov_b32 s51, s92\n\t" \
  "v_mul_f32 v117, v117, v116\n\t" \
  "v_max_f32 v117, 0x3f028f5c, v117\n\t" \
  "v_min_f32 v117, 0x3ffeb852, v117\n\t" \
  "v_rcp_f32 v116, v117\n\t" \
  "v_mul_f32 v117, 0x4f000000, v117\n\t" \
  "v_mul_f32 v116, 0x47800000, v116\n\t" \
  "v_cvt_u32_f32 v117, v117\n\t" \
  "v_cvt_u32_f32 v116, v116\n\t" \
  "s_mov_b32 s53, s93\n\t" \
  "v_readfirstlane_b32 s46, v117\n\t" \
  "v_readfirstlane_b32 s52, v116\n\t" \
  "21:\n\t" \
  "s_add_i32 s44, s44, 1\n\t" \
  "s_and_b32 s58, s58, 4\n\t" \
  "s_and_b32 s76, s44, 64\n\t" \
  "s_or_b32 s42, s42, s76\n\t" \
  "s_cbranch_scc0 20b\n\t" \
  "s_branch 99f\n\t" \
  "14:\n\t" \
  "s_sleep 3\n\t" \
  "s_add_u32 s59, s59, 0x10000\n\t" \
  "s_cmp_lt_u32 s59, 0x400000\n\t" \
  "s_cbranch_scc1 13b\n\t" \
  "s_mov_b64 exec, -1\n\t" \
  "s_mov_b32 s42, 16\n\t" \
  "s_branch 99f\n\t" \
  "8:\n\t" \
  "s_bitcmp1_b32 s58, 1\n\t" \
  "s_cbranch_scc1 4b\n\t" \
  "s_bitcmp1_b32 s58, 2\n\t" \
  "s_cbranch_scc1 5b\n\t" \
  "v_max3_u32 v116, v32, v33, v34\n\t" \
  "v_max3_u32 v117, v35, v36, v37\n\t" \
  "v_max3_u32 v116, v116, v38, v39\n\t" \
  "v_max_u32 v116, v116, v117\n\t" \
  "v_cmp_eq_u32 vcc, -1, v116\n\t" \
  "s_cbranch_vccz 5b\n\t" \
  "s_branch 0f\n\t" \
  "9:\n\t" \
  "s_bitcmp1_b32 s58, 2\n\t" \
  "s_cbranch_scc1 6b\n\t" \
  "v_max3_u32 v116, v40, v41, v42\n\t" \
  "v_max3_u32 v117, v43, v44, v45\n\t" \
  "v_max3_u32 v116, v116, v46, v47\n\t" \
  "v_max_u32 v116, v116, v117\n\t" \
  "v_cmp_eq_u32 vcc, -1, v116\n\t" \
  "s_cbranch_vccz 6b\n\t" \
  "s_or_b32 s58, s58, 2\n\t" \
  "0:\n\t" \
  "s_or_b32 s58, s58, 1\n\t" \
  "11:\n\t" \
  "buffer_load_dwordx4 v[32:35], v100, %[rh], s40 offen sc1\n\t" \
  "buffer_load_dwordx4 v[36:39], v100, %[rh], s76 offen sc1\n\t" \
  "buffer_load_dwordx4 v[40:43], v100, %[rh], s77 offen sc1\n\t" \
  "buffer_load_dwordx4 v[44:47], v100, %[rh], s78 offen sc1\n\t" \
  "s_waitcnt lgkmcnt(0)\n\t" \
  "s_branch 10b\n\t" \
  "17:\n\t" \
  "s_add_u32 s59, s59, 1\n\t" \
  "s_bitcmp1_b32 s58, 2\n\t" \
  "s_cbranch_scc1 3b\n\t" \
  "s_waitcnt vmcnt(0)\n\t" \
  "v_max_u32 v116, v32, v33\n\t" \
  "v_max3_u32 v116, v116, v34, v35\n\t" \
  "v_max3_u32 v116, v116, v36, v37\n\t" \
  "v_max3_u32 v116, v116, v38, v39\n\t" \
  "v_max3_u32 v116, v116, v40, v41\n\t" \
  "v_max3_u32 v116, v116, v42, v43\n\t" \
  "v_max3_u32 v116, v116, v44, v45\n\t" \
  "v_max3_u32 v116, v116, v46, v47\n\t" \
  "v_cmp_eq_u32 vcc, -1, v116\n\t" \
  "s_cbranch_vccz 3b\n\t" \
  "s_bitcmp1_b32 s59, 6\n\t" \
  "s_cbranch_scc1 12f\n\t" \
  "buffer_load_dwordx4 v[32:35], v100, %[rh], s40 offen sc1\n\t" \
  "buffer_load_dwordx4 v[36:39], v100, %[rh], s76 offen sc1\n\t" \
  "buffer_load_dwordx4 v[40:43], v100, %[rh], s77 offen sc1\n\t" \
  "buffer_load_dwordx4 v[44:47], v100, %[rh], s78 offen sc1\n\t" \
  "s_branch 17b\n\t" \
  "12:\n\t" \
  "s_mov_b32 s42, s58\n\t" \
  "s_branch 99f\n\t" \
  "99:\n\t" \
  "s_waitcnt vmcnt(0) lgkmcnt(0)\n\t" \
  ".Lchain_last:\n\t" \
  ""
#define CB_BODY \
  "s_cmp_gt_u32 %[wv], 1\n\t" \
  "s_cbranch_scc1 91f\n\t" \
  "s_mov_b32 s58, s42\n\t" \
  "s_mov_b32 s59, 0\n\t" \
  "s_bitcmp1_b32 s58, 4\n\t" \
  "s_cbranch_scc1 26f\n\t" \
  "s_bitcmp1_b32 s58, 3\n\t" \
  "s_cbranch_scc1 34f\n\t" \
  "s_bitcmp1_b32 s58, 1\n\t" \
  "s_cbranch_scc1 18f\n\t" \
  "17:\n\t" \
  "19:\n\t" \
  "s_add_i32 s85, s40, -1\n\t" \
  "s_and_b32 s82, s85, 2\n\t" \
  "s_lshl_b32 s82, s82, 13\n\t" \
  "s_and_b32 s80, s85, 1\n\t" \
  "s_lshl_b32 s80, s80, 12\n\t" \
  "s_add_i32 s82, s82, s80\n\t" \
  "s_add_i32 s82, s82, 0x84000\n\t" \
  "s_mov_b32 exec_hi, 0\n\t" \
  "buffer_load_dword v184, v200, %[rh], s82 offen sc1\n\t" \
  "buffer_load_dword v186, v203, %[ru], 0 offen sc1\n\t" \
  "s_mov_b64 exec, -1\n\t" \
  "20:\n\t" \
  "s_and_b32 s80, s40, 3\n\t" \
  "s_mul_i32 s77, s80, 0xc000\n\t" \
  "s_add_i32 s77, s77, 0x40000\n\t" \
  "s_lshl_b32 s78, s80, 14\n\t" \
  "s_add_i32 s78, s78, 0x70000\n\t" \
  "s_mov_b32 s57, 0\n\t" \
  "s_add_i32 s96, s77, 0x1000\n\t" \
  "s_lshl_b32 s88, s80, 20\n\t" \
  "s_add_i32 s88, s88, s48\n\t" \
  "s_lshr_b32 s80, s88, 21\n\t" \
  "s_bitcmp1_b64 %[sel], s80\n\t" \
  "s_cselect_b32 s80, 0x2000, 0\n\t" \
  "s_add_i32 s88, s88, s80\n\t" \
  "s_xor_b32 s89, s88, 0x800\n\t" \
  "s_xor_b32 s90, s88, 0x1000\n\t" \
  "s_xor_b32 s91, s88, 0x1800\n\t" \
  "s_add_i32 s92, s77, s50\n\t" \
  "s_xor_b32 s93, s92, 0x100\n\t" \
  "s_xor_b32 s94, s92, 0x200\n\t" \
  "s_xor_b32 s95, s92, 0x300\n\t" \
  "s_add_i32 s97, s40, 1\n\t" \
  "s_and_b32 s97, s97, 7\n\t" \
  "s_lshl_b32 s97, s97, 15\n\t" \
  "s_add_i32 s97, s97, s49\n\t" \
  "s_memtime vcc\n\t" \
  "s_memrealtime s[86:87]\n\t" \
  "s_nop 4\n\t" \
  "s_memrealtime s[60:61]\n\t" \
  "s_nop 4\n\t" \
  "s_memrealtime s[62:63]\n\t" \
  "s_nop 4\n\t" \
  "s_memrealtime s[64:65]\n\t" \
  "s_nop 4\n\t" \
  "s_memrealtime s[66:67]\n\t" \
  "s_nop 4\n\t" \
  "s_memrealtime s[68:69]\n\t" \
  "s_mov_b32 s79, 0\n\t" \
  "s_waitcnt lgkmcnt(0)\n\t" \
  "s_cmp_eq_u32 s86, s60\n\t" \
  "s_addc_u32 s79, s79, 0\n\t" \
  "s_cmp_eq_u32 s86, s62\n\t" \
  "s_addc_u32 s79, s79, 0\n\t" \
  "s_cmp_eq_u32 s86, s64\n\t" \
  "s_addc_u32 s79, s79, 0\n\t" \
  "s_cmp_eq_u32 s86, s66\n\t" \
  "s_addc_u32 s79, s79, 0\n\t" \
  "s_cmp_eq_u32 s86, s68\n\t" \
  "s_addc_u32 s79, s79, 0\n\t" \
  "s_mul_i32 s79, s79, 24\n\t" \
  "s_add_i32 s79, s79, vcc_lo\n\t" \
  "s_lshl4_add_u32 s80, s86, 64\n\t" \
  "s_sub_i32 s86, s79, s41\n\t" \
  "s_sub_i32 s87, s80, s43\n\t" \
  "s_cmp_lt_u32 s86, 4000\n\t" \
  "s_cbranch_scc1 21f\n\t" \
  "v_cvt_f32_u32 v165, s86\n\t" \
  "v_cvt_f32_u32 v164, s87\n\t" \
  "v_rcp_f32 v165, v165\n\t" \
  "s_nop 0\n\t" \
  "v_mul_f32 v165, v165, v164\n\t" \
  "v_max_f32 v165, 0x3f028f5c, v165\n\t" \
  "v_min_f32 v165, 0x3ffeb852, v165\n\t" \
  "v_rcp_f32 v164, v165\n\t" \
  "v_mul_f32 v165, 0x4f000000, v165\n\t" \
  "v_mul_f32 v164, 0x47800000, v164\n\t" \
  "v_cvt_u32_f32 v165, v165\n\t" \
  "v_cvt_u32_f32 v164, v164\n\t" \
  "s_nop 0\n\t" \
  "v_readfirstlane_b32 s45, v165\n\t" \
  "v_readfirstlane_b32 s44, v164\n\t" \
  "21:\n\t" \
  "s_mov_b32 s41, s79\n\t" \
  "s_mov_b32 s43, s80\n\t" \
  "s_lshl_b32 s83, s43, 8\n\t" \
  "s_mov_b32 s84, 610\n\t" \
  "s_mov_b32 exec_hi, 0\n\t" \
  "s_waitcnt vmcnt(0)\n\t" \
  "v_cmp_ne_u32_sdwa vcc, v184, s85 src0_sel:BYTE_0 src1_sel:DWORD\n\t" \
  "v_subrev_u32 v184, s83, v184\n\t" \
  "v_ashrrev_i32 v184, 8, v184\n\t" \
  "s_cbranch_vccnz 22f\n\t" \
  "s_nop 0\n\t" \
  "v_max_i32_dpp v184, v184, v184 quad_perm:[1,0,3,2] row_mask:0xf bank_mask:0xf\n\t" \
  "s_nop 1\n\t" \
  "v_max_i32_dpp v184, v184, v184 quad_perm:[2,3,0,1] row_mask:0xf bank_mask:0xf\n\t" \
  "s_nop 1\n\t" \
  "v_max_i32_dpp v184, v184, v184 row_half_mirror row_mask:0xf bank_mask:0xf\n\t" \
  "s_nop 1\n\t" \
  "v_max_i32_dpp v184, v184, v184 row_mirror row_mask:0xf bank_mask:0xf\n\t" \
  "s_nop 0\n\t" \
  "v_readlane_b32 s81, v184, 0\n\t" \
  "v_readlane_b32 s82, v184, 16\n\t" \
  "s_max_i32 s81, s81, s82\n\t" \
  "s_add_i32 s76, s81, s43\n\t" \
  "s_add_i32 s81, s81, s53\n\t" \
  "s_max_i32 s81, s81, 0\n\t" \
  "s_min_i32 s81, s81, 20000\n\t" \
  "s_mul_i32 s81, s81, s44\n\t" \
  "s_lshr_b32 s81, s81, 16\n\t" \
  "s_add_i32 s81, s81, -900\n\t" \
  "24:\n\t" \
  "s_add_i32 s81, s81, s41\n\t" \
  ".p2align 6\n\t" \
  "s_nop 0\n\t" \
  "s_nop 0\n\t" \
  "s_nop 0\n\t" \
  "s_nop 0\n\t" \
  "s_nop 0\n\t" \
  "s_nop 0\n\t" \
  "s_nop 0\n\t" \
  "s_nop 0\n\t" \
  "1:\n\t" \
  "s_memtime vcc\n\t" \
  "s_waitcnt lgkmcnt(0)\n\t" \
  "s_sub_i32 s79, s81, vcc_lo\n\t" \
  "s_cmp_gt_i32 s79, 128\n\t" \
  "s_cbranch_scc1 1b\n\t" \
  ".p2align 4\n\t" \
  "2:\n\t" \
  "s_sub_i32 s79, s79, 33\n\t" \
  "s_cmp_gt_i32 s79, 56\n\t" \
  "s_cbranch_scc1 2b\n\t" \
  "buffer_load_dword v187, v200, %[rh], s78 offen sc1\n\t" \
  "s_memtime vcc\n\t" \
  "s_memrealtime s[86:87]\n\t" \
  "s_nop 4\n\t" \
  "s_memrealtime s[60:61]\n\t" \
  "s_nop 4\n\t" \
  "s_memrealtime s[62:63]\n\t" \
  "s_nop 4\n\t" \
  "s_memrealtime s[64:65]\n\t" \
  "s_nop 4\n\t" \
  "s_memrealtime s[66:67]\n\t" \
  "s_nop 4\n\t" \
  "s_memrealtime s[68:69]\n\t" \
  "s_mov_b32 s79, 0\n\t" \
  "s_waitcnt lgkmcnt(0)\n\t" \
  "s_cmp_eq_u32 s86, s60\n\t" \
  "s_addc_u32 s79, s79, 0\n\t" \
  "s_cmp_eq_u32 s86, s62\n\t" \
  "s_addc_u32 s79, s79, 0\n\t" \
  "s_cmp_eq_u32 s86, s64\n\t" \
  "s_addc_u32 s79, s79, 0\n\t" \
  "s_cmp_eq_u32 s86, s66\n\t" \
  "s_addc_u32 s79, s79, 0\n\t" \
  "s_cmp_eq_u32 s86, s68\n\t" \
  "s_addc_u32 s79, s79, 0\n\t" \
  "s_mul_i32 s79, s79, 24\n\t" \
  "s_add_i32 s79, s79, vcc_lo\n\t" \
  "s_lshl4_add_u32 s80, s86, 64\n\t" \
  "s_mov_b32 s41, s79\n\t" \
  "s_mov_b32 s43, s80\n\t" \
  "s_lshl_b32 s83, s43, 8\n\t" \
  "s_add_i32 s98, s40, 2\n\t" \
  "v_cmp_gt_i32 vcc, s98, v186\n\t" \
  "s_and_b32 s54, s98, 15\n\t" \
  "s_lshl_b32 s54, s54, 22\n\t" \
  "s_add_i32 s54, s54, s51\n\t" \
  "s_lshr_b32 s80, s54, 21\n\t" \
  "s_bitcmp0_b64 %[sel], s80\n\t" \
  "s_cselect_b32 s80, 0x2000, 0\n\t" \
  "s_add_i32 s54, s54, s80\n\t" \
  "s_cmp_eq_u64 vcc, 0\n\t" \
  "s_cselect_b32 s96, 0x70000000, 0\n\t" \
  "s_cselect_b32 s98, 0, s98\n\t" \
  "13:\n\t" \
  "s_waitcnt vmcnt(0)\n\t" \
  "s_memtime s[80:81]\n\t" \
  "v_cmp_ne_u32_sdwa vcc, v187, s40 src0_sel:BYTE_0 src1_sel:DWORD\n\t" \
  "v_subrev_u32 v184, s83, v187\n\t" \
  "v_ashrrev_i32 v184, 8, v184\n\t" \
  "s_nop 1\n\t" \
  "v_max_i32_dpp v184, v184, v184 quad_perm:[1,0,3,2] row_mask:0xf bank_mask:0xf\n\t" \
  "s_nop 1\n\t" \
  "v_max_i32_dpp v184, v184, v184 quad_perm:[2,3,0,1] row_mask:0xf bank_mask:0xf\n\t" \
  "s_cbranch_vccnz 14f\n\t" \
  "15:\n\t" \
  "s_nop 0\n\t" \
  "v_max_i32_dpp v184, v184, v184 row_half_mirror row_mask:0xf bank_mask:0xf\n\t" \
  "s_nop 1\n\t" \
  "v_max_i32_dpp v184, v184, v184 row_mirror row_mask:0xf bank_mask:0xf\n\t" \
  "s_mov_b64 exec, -1\n\t" \
  "v_readlane_b32 s57, v184, 0\n\t" \
  "v_readlane_b32 s82, v184, 16\n\t" \
  "s_waitcnt lgkmcnt(0)\n\t" \
  "s_sub_i32 s79, s41, s80\n\t" \
  "s_max_i32 s57, s57, s82\n\t" \
  "s_add_i32 s81, s57, 212\n\t" \
  "s_max_i32 s81, s81, 0\n\t" \
  "s_min_i32 s81, s81, 20000\n\t" \
  "s_mul_i32 s81, s81, s44\n\t" \
  "s_lshr_b32 s81, s81, 16\n\t" \
  "s_add_i32 s79, s79, s81\n\t" \
  "s_add_i32 s79, s79, -330\n\t" \
  ".p2align 4\n\t" \
  "3:\n\t" \
  "s_sub_i32 s79, s79, 33\n\t" \
  "s_cmp_gt_i32 s79, 56\n\t" \
  "s_cbranch_scc1 3b\n\t" \
  "buffer_load_dwordx4 v[72:75], v192, %[ru], s88 offen offset:0 sc1\n\t" \
  "s_nop 4\n\t" \
  "buffer_load_dwordx4 v[76:79], v192, %[ru], s88 offen offset:1024 sc1\n\t" \
  "s_nop 4\n\t" \
  "s_add_i32 s79, s79, 12\n\t" \
  ".p2align 4\n\t" \
  "4:\n\t" \
  "s_sub_i32 s79, s79, 33\n\t" \
  "s_cmp_gt_i32 s79, 56\n\t" \
  "s_cbranch_scc1 4b\n\t" \
  "buffer_load_dwordx4 v[80:83], v192, %[ru], s89 offen offset:0 sc1\n\t" \
  "s_nop 4\n\t" \
  "buffer_load_dwordx4 v[84:87], v192, %[ru], s89 offen offset:1024 sc1\n\t" \
  "s_nop 4\n\t" \
  "s_add_i32 s79, s79, 22\n\t" \
  ".p2align 4\n\t" \
  "5:\n\t" \
  "s_sub_i32 s79, s79, 33\n\t" \
  "s_cmp_gt_i32 s79, 56\n\t" \
  "s_cbranch_scc1 5b\n\t" \
  "buffer_load_dwordx4 v[88:91], v192, %[ru], s90 offen offset:0 sc1\n\t" \
  "buffer_load_dwordx4 v[92:95], v192, %[ru], s90 offen offset:1024 sc1\n\t" \
  "s_add_i32 s79, s79, 122\n\t" \
  ".p2align 4\n\t" \
  "9:\n\t" \
  "s_sub_i32 s79, s79, 33\n\t" \
  "s_cmp_gt_i32 s79, 56\n\t" \
  "s_cbranch_scc1 9b\n\t" \
  "buffer_load_dwordx4 v[104:107], v194, %[rh], s92 offen sc1\n\t" \
  "s_nop 4\n\t" \
  "buffer_load_dwordx4 v[108:111], v194, %[rh], s93 offen sc1\n\t" \
  "s_nop 4\n\t" \
  "buffer_load_dwordx4 v[112:115], v194, %[rh], s94 offen sc1\n\t" \
  "s_nop 4\n\t" \
  "buffer_load_dwordx4 v[116:119], v194, %[rh], s95 offen sc1\n\t" \
  "s_nop 4\n\t" \
  "buffer_load_dwordx4 v[96:99], v192, %[ru], s91 offen offset:0 sc1\n\t" \
  "s_nop 4\n\t" \
  "buffer_load_dwordx4 v[100:103], v192, %[ru], s91 offen offset:1024 sc1\n\t" \
  "s_nop 4\n\t" \
  "s_mov_b64 exec, 0xffff\n\t" \
  "buffer_load_dword v185, v203, %[ru], s96 offen sc1\n\t" \
  "s_mov_b64 exec, -1\n\t" \
  "10:\n\t" \
  "s_waitcnt vmcnt(6)\n\t" \
  "v_mfma_f32_16x16x32_bf16 v[136:139], v[104:107], v[72:75], 0\n\t" \
  "v_mfma_f32_16x16x32_bf16 v[140:143], v[104:107], v[76:79], 0\n\t" \
  "s_waitcnt vmcnt(5)\n\t" \
  "v_mfma_f32_16x16x32_bf16 v[136:139], v[108:111], v[80:83], v[136:139]\n\t" \
  "v_mfma_f32_16x16x32_bf16 v[140:143], v[108:111], v[84:87], v[140:143]\n\t" \
  "s_waitcnt vmcnt(4)\n\t" \
  "v_mfma_f32_16x16x32_bf16 v[136:139], v[112:115], v[88:91], v[136:139]\n\t" \
  "v_mfma_f32_16x16x32_bf16 v[140:143], v[112:115], v[92:95], v[140:143]\n\t" \
  "s_waitcnt vmcnt(2)\n\t" \
  "v_mfma_f32_16x16x32_bf16 v[136:139], v[116:119], v[96:99], v[136:139]\n\t" \
  "s_waitcnt vmcnt(1)\n\t" \
  "v_mfma_f32_16x16x32_bf16 v[140:143], v[116:119], v[100:103], v[140:143]\n\t" \
  "s_barrier\n\t" \
  "ds_read2st64_b64 v[144:147], v195 offset1:1\n\t" \
  "s_nop 1\n\t" \
  "v_permlane32_swap_b32 v136, v138\n\t" \
  "v_permlane32_swap_b32 v137, v139\n\t" \
  "s_nop 1\n\t" \
  "v_permlane32_swap_b32 v140, v142\n\t" \
  "v_permlane32_swap_b32 v141, v143\n\t" \
  "v_pk_add_f32 v[136:137], v[136:137], v[142:143]\n\t" \
  "s_waitcnt lgkmcnt(0)\n\t" \
  "v_pk_add_f32 v[136:137], v[136:137], v[144:145]\n\t" \
  "v_pk_add_f32 v[136:137], v[136:137], v[146:147]\n\t" \
  "v_cvt_pk_bf16_f32 v148, v136, v137\n\t" \
  "s_nop 1\n\t" \
  "v_mov_b32_dpp v149, v148 row_half_mirror row_mask:0xf bank_mask:0xf\n\t" \
  "v_perm_b32 v150, v148, v149, v205\n\t" \
  "v_cmpx_o_f16_sdwa vcc, v150, v150 src0_sel:WORD_0 src1_sel:WORD_1\n\t" \
  "6:\n\t" \
  "buffer_store_dword v150, v201, %[rh], s97 offen sc1\n\t" \
  "s_memtime s[82:83]\n\t" \
  "s_mov_b64 exec, -1\n\t" \
  "s_cmp_eq_u64 vcc, -1\n\t" \
  "s_cbranch_scc0 8f\n\t" \
  "v_mov_b32 v151, 0\n\t" \
  "5:\n\t" \
  "ds_write_b32 v206, v151\n\t" \
  "s_mov_b64 exec, 0xffff\n\t" \
  "s_waitcnt vmcnt(1)\n\t" \
  "v_cmp_gt_i32 vcc, s98, v185\n\t" \
  "s_mov_b64 exec, -1\n\t" \
  "s_cmp_eq_u64 vcc, 0\n\t" \
  "s_cselect_b32 s54, s54, 0x70000000\n\t" \
  "buffer_load_dwordx4 v[0:3], v210, %[ru], s54 offen offset:0 sc1\n\t" \
  "buffer_load_dwordx4 v[4:7], v210, %[ru], s54 offen offset:1024 sc1\n\t" \
  "s_add_i32 s54, s54, 0x20000\n\t" \
  "buffer_load_dwordx4 v[8:11], v210, %[ru], s54 offen offset:0 sc1\n\t" \
  "buffer_load_dwordx4 v[12:15], v210, %[ru], s54 offen offset:1024 sc1\n\t" \
  "s_add_i32 s54, s54, 0x20000\n\t" \
  "s_waitcnt lgkmcnt(0)\n\t" \
  "s_mov_b32 s56, s82\n\t" \
  "s_barrier\n\t" \
  "ds_read2_b32 v[148:149], v207 offset1:8\n\t" \
  "s_waitcnt lgkmcnt(0)\n\t" \
  "v_or_b32 v148, v148, v149\n\t" \
  "v_cmp_ne_u32 vcc, 0, v148\n\t" \
  "s_cbranch_vccnz 12f\n\t" \
  "s_and_b32 s58, s58, 4\n\t" \
  "s_add_i32 s80, s57, s43\n\t" \
  "s_sub_i32 s80, s80, s76\n\t" \
  "s_add_i32 s53, s53, -12\n\t" \
  "s_max_i32 s80, s80, s53\n\t" \
  "s_add_i32 s53, s53, 24\n\t" \
  "s_min_i32 s53, s53, s80\n\t" \
  "s_cmp_lt_u32 s54, 0x70000000\n\t" \
  "s_cbranch_scc1 48f\n\t" \
  "25:\n\t" \
  "s_add_i32 s82, s40, 2\n\t" \
  "s_min_i32 s83, s82, 63\n\t" \
  "s_mov_b64 exec, 0xffff\n\t" \
  "v_cmp_gt_i32 vcc, s83, v186\n\t" \
  "s_mov_b64 exec, -1\n\t" \
  "s_and_b32 s54, s82, 15\n\t" \
  "s_lshl_b32 s54, s54, 22\n\t" \
  "s_add_i32 s54, s54, s51\n\t" \
  "s_lshr_b32 s80, s54, 21\n\t" \
  "s_bitcmp0_b64 %[sel], s80\n\t" \
  "s_cselect_b32 s80, 0x2000, 0\n\t" \
  "s_add_i32 s54, s54, s80\n\t" \
  "s_cbranch_vccnz 27f\n\t" \
  "28:\n\t" \
  "s_cmp_ge_i32 s40, 62\n\t" \
  "s_cbranch_scc1 37f\n\t" \
  "buffer_load_dwordx4 v[0:3], v210, %[ru], s54 offen offset:0 sc1\n\t" \
  "buffer_load_dwordx4 v[4:7], v210, %[ru], s54 offen offset:1024 sc1\n\t" \
  "s_add_i32 s54, s54, 0x20000\n\t" \
  "buffer_load_dwordx4 v[8:11], v210, %[ru], s54 offen offset:0 sc1\n\t" \
  "buffer_load_dwordx4 v[12:15], v210, %[ru], s54 offen offset:1024 sc1\n\t" \
  "s_add_i32 s54, s54, 0x20000\n\t" \
  "48:\n\t" \
  "s_and_b32 s76, s40, 3\n\t" \
  "s_lshl_b32 s76, s76, 18\n\t" \
  "s_bitcmp1_b32 s58, 4\n\t" \
  "s_cbranch_scc1 29f\n\t" \
  "s_sub_i32 s80, s56, s41\n\t" \
  "s_mul_hi_u32 s80, s80, s45\n\t" \
  "s_lshl_b32 s80, s80, 1\n\t" \
  "s_sub_i32 s80, s80, s57\n\t" \
  "s_min_u32 s47, s47, s46\n\t" \
  "s_min_u32 s47, s47, s80\n\t" \
  "v_mov_b32 v188, s47\n\t" \
  "s_mov_b32 s47, s46\n\t" \
  "s_mov_b32 s46, s80\n\t" \
  "buffer_store_dword v188, v202, %[rh], s52 offen sc1\n\t" \
  "v_mov_b64 v[188:189], -1\n\t" \
  "v_mov_b64 v[190:191], -1\n\t" \
  "v_mov_b32 v187, s40\n\t" \
  "s_add_i32 s80, s40, -1\n\t" \
  "s_and_b32 s80, s80, 7\n\t" \
  "s_lshl_b32 s80, s80, 15\n\t" \
  "s_add_i32 s80, s80, s49\n\t" \
  "buffer_store_dwordx4 v[188:191], v208, %[rh], s80 offen sc1\n\t" \
  "buffer_store_dword v187, v209, %[ru], 0 offen sc1\n\t" \
  "ds_write_b128 v197, v[72:75] offset:16384\n\t" \
  "ds_write_b128 v197, v[76:79] offset:17408\n\t" \
  "ds_write_b128 v197, v[80:83] offset:18432\n\t" \
  "ds_write_b128 v197, v[84:87] offset:19456\n\t" \
  "ds_write_b128 v197, v[88:91] offset:20480\n\t" \
  "ds_write_b128 v197, v[92:95] offset:21504\n\t" \
  "ds_write_b128 v197, v[96:99] offset:22528\n\t" \
  "ds_write_b128 v197, v[100:103] offset:23552\n\t" \
  "29:\n\t" \
  "s_and_b32 s58, s58, 36\n\t" \
  "buffer_load_dwordx4 v[16:19], v210, %[ru], s54 offen offset:0 sc1\n\t" \
  "buffer_load_dwordx4 v[20:23], v210, %[ru], s54 offen offset:1024 sc1\n\t" \
  "s_add_i32 s54, s54, 0x20000\n\t" \
  "buffer_load_dwordx4 v[24:27], v210, %[ru], s54 offen offset:0 sc1\n\t" \
  "buffer_load_dwordx4 v[28:31], v210, %[ru], s54 offen offset:1024 sc1\n\t" \
  "s_add_i32 s54, s54, 0x20000\n\t" \
  "buffer_load_dwordx4 v[32:35], v210, %[ru], s54 offen offset:0 sc1\n\t" \
  "buffer_load_dwordx4 v[36:39], v210, %[ru], s54 offen offset:1024 sc1\n\t" \
  "s_add_i32 s54, s54, 0x20000\n\t" \
  "buffer_load_dwordx4 v[40:43], v210, %[ru], s54 offen offset:0 sc1\n\t" \
  "buffer_load_dwordx4 v[44:47], v210, %[ru], s54 offen offset:1024 sc1\n\t" \
  "s_add_i32 s54, s54, 0x20000\n\t" \
  "buffer_load_dwordx4 a[0:3], v211, %[rh], s76 offen offset:0 sc1\n\t" \
  "buffer_load_dwordx4 a[4:7], v211, %[rh], s76 offen offset:1024 sc1\n\t" \
  "buffer_load_dwordx4 v[48:51], v210, %[ru], s54 offen offset:0 sc1\n\t" \
  "buffer_load_dwordx4 v[52:55], v210, %[ru], s54 offen offset:1024 sc1\n\t" \
  "s_add_i32 s54, s54, 0x20000\n\t" \
  "buffer_load_dwordx4 v[56:59], v210, %[ru], s54 offen offset:0 sc1\n\t" \
  "buffer_load_dwordx4 v[60:63], v210, %[ru], s54 offen offset:1024 sc1\n\t" \
  "s_add_i32 s54, s54, 0x20000\n\t" \
  "buffer_load_dwordx4 a[8:11], v212, %[rh], s76 offen offset:0 sc1\n\t" \
  "buffer_load_dwordx4 a[12:15], v212, %[rh], s76 offen offset:1024 sc1\n\t" \
  "s_waitcnt lgkmcnt(0)\n\t" \
  "s_barrier\n\t" \
  "buffer_load_dwordx4 v[64:67], v210, %[ru], s54 offen offset:0 sc1\n\t" \
  "buffer_load_dwordx4 v[68:71], v210, %[ru], s54 offen offset:1024 sc1\n\t" \
  "s_add_i32 s54, s54, 0x20000\n\t" \
  "buffer_load_dwordx4 v[104:107], v210, %[ru], s54 offen offset:0 sc1\n\t" \
  "buffer_load_dwordx4 v[108:111], v210, %[ru], s54 offen offset:1024 sc1\n\t" \
  "s_add_i32 s54, s54, 0x20000\n\t" \
  "buffer_load_dwordx4 a[16:19], v213, %[rh], s76 offen offset:0 sc1\n\t" \
  "buffer_load_dwordx4 a[20:23], v213, %[rh], s76 offen offset:1024 sc1\n\t" \
  "buffer_load_dwordx4 v[112:115], v210, %[ru], s54 offen offset:0 sc1\n\t" \
  "buffer_load_dwordx4 v[116:119], v210, %[ru], s54 offen offset:1024 sc1\n\t" \
  "s_add_i32 s54, s54, 0x20000\n\t" \
  "buffer_load_dwordx4 v[120:123], v210, %[ru], s54 offen offset:0 sc1\n\t" \
  "buffer_load_dwordx4 v[124:127], v210, %[ru], s54 offen offset:1024 sc1\n\t" \
  "s_add_i32 s54, s54, 0x20000\n\t" \
  "buffer_load_dwordx4 a[24:27], v214, %[rh], s76 offen offset:0 sc1\n\t" \
  "buffer_load_dwordx4 a[28:31], v214, %[rh], s76 offen offset:1024 sc1\n\t" \
  "ds_read_b128 v[160:163], v193 offset:0\n\t" \
  "ds_read_b128 v[164:167], v193 offset:256\n\t" \
  "ds_read_b128 v[168:171], v193 offset:2048\n\t" \
  "ds_read_b128 v[172:175], v193 offset:2304\n\t" \
  "ds_read_b128 v[176:179], v193 offset:4096\n\t" \
  "ds_read_b128 v[180:183], v193 offset:4352\n\t" \
  "s_waitcnt lgkmcnt(2)\n\t" \
  "v_pk_add_f32 v[160:161], v[160:161], v[168:169]\n\t" \
  "v_pk_add_f32 v[162:163], v[162:163], v[170:171]\n\t" \
  "v_pk_add_f32 v[164:165], v[164:165], v[172:173]\n\t" \
  "v_pk_add_f32 v[166:167], v[166:167], v[174:175]\n\t" \
  "s_waitcnt lgkmcnt(0)\n\t" \
  "v_pk_add_f32 v[160:161], v[160:161], v[176:177]\n\t" \
  "v_pk_add_f32 v[162:163], v[162:163], v[178:179]\n\t" \
  "v_pk_add_f32 v[164:165], v[164:165], v[180:181]\n\t" \
  "v_pk_add_f32 v[166:167], v[166:167], v[182:183]\n\t" \
  "v_cvt_pk_bf16_f32 v160, v160, v161\n\t" \
  "v_cvt_pk_bf16_f32 v161, v162, v163\n\t" \
  "v_cvt_pk_bf16_f32 v162, v164, v165\n\t" \
  "v_cvt_pk_bf16_f32 v163, v166, v167\n\t" \
  "buffer_load_dwordx4 v[128:131], v210, %[ru], s54 offen offset:0 sc1\n\t" \
  "buffer_load_dwordx4 v[132:135], v210, %[ru], s54 offen offset:1024 sc1\n\t" \
  "s_add_i32 s54, s54, 0x20000\n\t" \
  "buffer_load_dwordx4 v[136:139], v210, %[ru], s54 offen offset:0 sc1\n\t" \
  "buffer_load_dwordx4 v[140:143], v210, %[ru], s54 offen offset:1024 sc1\n\t" \
  "s_add_i32 s54, s54, 0x20000\n\t" \
  "s_waitcnt vmcnt(32)\n\t" \
  "v_swap_b32 v1, v8\n\t" \
  "v_swap_b32 v3, v10\n\t" \
  "v_swap_b32 v5, v12\n\t" \
  "v_swap_b32 v7, v14\n\t" \
  "ds_write_b128 v204, v[0:3] offset:0\n\t" \
  "ds_write_b128 v204, v[8:11] offset:1024\n\t" \
  "ds_write_b128 v204, v[4:7] offset:2048\n\t" \
  "ds_write_b128 v204, v[12:15] offset:3072\n\t" \
  "buffer_load_dwordx4 v[144:147], v210, %[ru], s54 offen offset:0 sc1\n\t" \
  "buffer_load_dwordx4 v[148:151], v210, %[ru], s54 offen offset:1024 sc1\n\t" \
  "s_add_i32 s54, s54, 0x20000\n\t" \
  "buffer_load_dwordx4 v[152:155], v210, %[ru], s54 offen offset:0 sc1\n\t" \
  "buffer_load_dwordx4 v[156:159], v210, %[ru], s54 offen offset:1024 sc1\n\t" \
  "s_add_i32 s54, s54, 0x20000\n\t" \
  "s_waitcnt vmcnt(32)\n\t" \
  "v_swap_b32 v17, v24\n\t" \
  "v_swap_b32 v19, v26\n\t" \
  "v_swap_b32 v21, v28\n\t" \
  "v_swap_b32 v23, v30\n\t" \
  "ds_write_b128 v204, v[16:19] offset:4096\n\t" \
  "ds_write_b128 v204, v[24:27] offset:5120\n\t" \
  "ds_write_b128 v204, v[20:23] offset:6144\n\t" \
  "ds_write_b128 v204, v[28:31] offset:7168\n\t" \
  "s_waitcnt vmcnt(28)\n\t" \
  "v_swap_b32 v33, v40\n\t" \
  "v_swap_b32 v35, v42\n\t" \
  "v_swap_b32 v37, v44\n\t" \
  "v_swap_b32 v39, v46\n\t" \
  "ds_write_b128 v204, v[32:35] offset:8192\n\t" \
  "ds_write_b128 v204, v[40:43] offset:9216\n\t" \
  "ds_write_b128 v204, v[36:39] offset:10240\n\t" \
  "ds_write_b128 v204, v[44:47] offset:11264\n\t" \
  "s_waitcnt vmcnt(22)\n\t" \
  "v_swap_b32 v49, v56\n\t" \
  "v_swap_b32 v51, v58\n\t" \
  "v_swap_b32 v53, v60\n\t" \
  "v_swap_b32 v55, v62\n\t" \
  "ds_write_b128 v204, v[48:51] offset:12288\n\t" \
  "ds_write_b128 v204, v[56:59] offset:13312\n\t" \
  "ds_write_b128 v204, v[52:55] offset:14336\n\t" \
  "ds_write_b128 v204, v[60:63] offset:15360\n\t" \
  "s_waitcnt vmcnt(16)\n\t" \
  "v_swap_b32 v65, v104\n\t" \
  "v_swap_b32 v67, v106\n\t" \
  "v_swap_b32 v69, v108\n\t" \
  "v_swap_b32 v71, v110\n\t" \
  "ds_write_b128 v204, v[64:67] offset:16384\n\t" \
  "ds_write_b128 v204, v[104:107] offset:17408\n\t" \
  "ds_write_b128 v204, v[68:71] offset:18432\n\t" \
  "ds_write_b128 v204, v[108:111] offset:19456\n\t" \
  "s_waitcnt vmcnt(10)\n\t" \
  "v_swap_b32 v113, v120\n\t" \
  "v_swap_b32 v115, v122\n\t" \
  "v_swap_b32 v117, v124\n\t" \
  "v_swap_b32 v119, v126\n\t" \
  "ds_write_b128 v204, v[112:115] offset:20480\n\t" \
  "ds_write_b128 v204, v[120:123] offset:21504\n\t" \
  "ds_write_b128 v204, v[116:119] offset:22528\n\t" \
  "ds_write_b128 v204, v[124:127] offset:23552\n\t" \
  "s_waitcnt vmcnt(4)\n\t" \
  "v_swap_b32 v129, v136\n\t" \
  "v_swap_b32 v131, v138\n\t" \
  "v_swap_b32 v133, v140\n\t" \
  "v_swap_b32 v135, v142\n\t" \
  "ds_write_b128 v204, v[128:131] offset:24576\n\t" \
  "ds_write_b128 v204, v[136:139] offset:25600\n\t" \
  "ds_write_b128 v204, v[132:135] offset:26624\n\t" \
  "ds_write_b128 v204, v[140:143] offset:27648\n\t" \
  "s_waitcnt vmcnt(0)\n\t" \
  "v_swap_b32 v145, v152\n\t" \
  "v_swap_b32 v147, v154\n\t" \
  "v_swap_b32 v149, v156\n\t" \
  "v_swap_b32 v151, v158\n\t" \
  "ds_write_b128 v204, v[144:147] offset:28672\n\t" \
  "ds_write_b128 v204, v[152:155] offset:29696\n\t" \
  "ds_write_b128 v204, v[148:151] offset:30720\n\t" \
  "ds_write_b128 v204, v[156:159] offset:31744\n\t" \
  "s_waitcnt lgkmcnt(0)\n\t" \
  "s_cmp_lt_i32 s40, 0\n\t" \
  "s_cbranch_scc1 39f\n\t" \
  "38:\n\t" \
  "s_add_i32 s80, s40, -2\n\t" \
  "s_lshl_b32 s80, s80, 17\n\t" \
  "s_min_u32 s80, s80, 0x70000000\n\t" \
  "buffer_store_dwordx4 v[160:163], v196, %[ro], s80 offen sc1\n\t" \
  "ds_read_b128 a[32:35], v198 offset:0\n\t" \
  "ds_read_b128 a[36:39], v198 offset:1024\n\t" \
  "ds_read_b128 a[40:43], v198 offset:2048\n\t" \
  "ds_read_b128 a[44:47], v198 offset:3072\n\t" \
  "ds_read_b128 a[48:51], v198 offset:4096\n\t" \
  "ds_read_b128 a[52:55], v198 offset:5120\n\t" \
  "ds_read_b128 a[56:59], v198 offset:6144\n\t" \
  "ds_read_b128 a[60:63], v198 offset:7168\n\t" \
  "ds_read_b128 a[64:67], v198 offset:8192\n\t" \
  "ds_read_b128 a[68:71], v198 offset:9216\n\t" \
  "ds_read_b128 a[72:75], v198 offset:10240\n\t" \
  "ds_read_b128 a[76:79], v198 offset:11264\n\t" \
  "ds_read_b128 a[80:83], v198 offset:12288\n\t" \
  "ds_read_b128 a[84:87], v198 offset:13312\n\t" \
  "ds_read_b128 a[88:91], v198 offset:14336\n\t" \
  "ds_read_b128 a[92:95], v198 offset:15360\n\t" \
  "s_waitcnt vmcnt(1) lgkmcnt(0)\n\t" \
  "31:\n\t" \
  "v_mfma_f32_16x16x32_bf16 v[168:171], a[32:35], a[0:3], 0\n\t" \
  "v_mfma_f32_16x16x32_bf16 v[172:175], a[64:67], a[0:3], 0\n\t" \
  "v_mfma_f32_16x16x32_bf16 v[168:171], a[36:39], a[4:7], v[168:171]\n\t" \
  "v_mfma_f32_16x16x32_bf16 v[172:175], a[68:71], a[4:7], v[172:175]\n\t" \
  "v_mfma_f32_16x16x32_bf16 v[168:171], a[40:43], a[8:11], v[168:171]\n\t" \
  "v_mfma_f32_16x16x32_bf16 v[172:175], a[72:75], a[8:11], v[172:175]\n\t" \
  "v_mfma_f32_16x16x32_bf16 v[168:171], a[44:47], a[12:15], v[168:171]\n\t" \
  "v_mfma_f32_16x16x32_bf16 v[172:175], a[76:79], a[12:15], v[172:175]\n\t" \
  "v_mfma_f32_16x16x32_bf16 v[168:171], a[48:51], a[16:19], v[168:171]\n\t" \
  "v_mfma_f32_16x16x32_bf16 v[172:175], a[80:83], a[16:19], v[172:175]\n\t" \
  "v_mfma_f32_16x16x32_bf16 v[168:171], a[52:55], a[20:23], v[168:171]\n\t" \
  "v_mfma_f32_16x16x32_bf16 v[172:175], a[84:87], a[20:23], v[172:175]\n\t" \
  "v_mfma_f32_16x16x32_bf16 v[168:171], a[56:59], a[24:27], v[168:171]\n\t" \
  "v_mfma_f32_16x16x32_bf16 v[172:175], a[88:91], a[24:27], v[172:175]\n\t" \
  "v_mfma_f32_16x16x32_bf16 v[168:171], a[60:63], a[28:31], v[168:171]\n\t" \
  "v_mfma_f32_16x16x32_bf16 v[172:175], a[92:95], a[28:31], v[172:175]\n\t" \
  "s_nop 7\n\t" \
  "39:\n\t" \
  "s_add_i32 s40, s40, 2\n\t" \
  "v_cmp_u_f32 vcc, v168, v168\n\t" \
  "s_cbranch_vccnz 32f\n\t" \
  "33:\n\t" \
  "s_barrier\n\t" \
  "s_add_i32 s85, s40, -1\n\t" \
  "s_cmp_ge_i32 s40, 64\n\t" \
  "s_cbranch_scc1 47f\n\t" \
  "s_and_b32 s82, s85, 2\n\t" \
  "s_lshl_b32 s82, s82, 13\n\t" \
  "s_and_b32 s80, s85, 1\n\t" \
  "s_lshl_b32 s80, s80, 12\n\t" \
  "s_add_i32 s82, s82, s80\n\t" \
  "s_add_i32 s82, s82, 0x84000\n\t" \
  "s_mov_b32 exec_hi, 0\n\t" \
  "buffer_load_dword v184, v200, %[rh], s82 offen sc1\n\t" \
  "buffer_load_dword v186, v203, %[ru], 0 offen sc1\n\t" \
  "s_mov_b64 exec, -1\n\t" \
  "47:\n\t" \
  "ds_write_b128 v199, v[168:171] offset:0\n\t" \
  "ds_write_b128 v199, v[172:175] offset:1024\n\t" \
  "s_mov_b32 s59, 0\n\t" \
  "s_and_b32 s58, s58, 4\n\t" \
  "s_cmp_lt_i32 s40, 64\n\t" \
  "s_cbranch_scc1 20b\n\t" \
  "s_waitcnt lgkmcnt(0)\n\t" \
  "s_barrier\n\t" \
  "ds_read_b128 v[160:163], v193 offset:0\n\t" \
  "ds_read_b128 v[164:167], v193 offset:256\n\t" \
  "ds_read_b128 v[168:171], v193 offset:2048\n\t" \
  "ds_read_b128 v[172:175], v193 offset:2304\n\t" \
  "ds_read_b128 v[176:179], v193 offset:4096\n\t" \
  "ds_read_b128 v[180:183], v193 offset:4352\n\t" \
  "s_waitcnt lgkmcnt(2)\n\t" \
  "v_pk_add_f32 v[160:161], v[160:161], v[168:169]\n\t" \
  "v_pk_add_f32 v[162:163], v[162:163], v[170:171]\n\t" \
  "v_pk_add_f32 v[164:165], v[164:165], v[172:173]\n\t" \
  "v_pk_add_f32 v[166:167], v[166:167], v[174:175]\n\t" \
  "s_waitcnt lgkmcnt(0)\n\t" \
  "v_pk_add_f32 v[160:161], v[160:161], v[176:177]\n\t" \
  "v_pk_add_f32 v[162:163], v[162:163], v[178:179]\n\t" \
  "v_pk_add_f32 v[164:165], v[164:165], v[180:181]\n\t" \
  "v_pk_add_f32 v[166:167], v[166:167], v[182:183]\n\t" \
  "v_cvt_pk_bf16_f32 v160, v160, v161\n\t" \
  "v_cvt_pk_bf16_f32 v161, v162, v163\n\t" \
  "v_cvt_pk_bf16_f32 v162, v164, v165\n\t" \
  "v_cvt_pk_bf16_f32 v163, v166, v167\n\t" \
  "s_add_i32 s80, s40, -2\n\t" \
  "s_lshl_b32 s80, s80, 17\n\t" \
  "s_min_u32 s80, s80, 0x70000000\n\t" \
  "buffer_store_dwordx4 v[160:163], v196, %[ro], s80 offen sc1\n\t" \
  "s_mov_b32 s58, 64\n\t" \
  "s_branch 7f\n\t" \
  "22:\n\t" \
  "s_mov_b32 s81, 0\n\t" \
  "s_cmp_eq_u32 s40, 0\n\t" \
  "s_cbranch_scc1 24b\n\t" \
  "s_bitcmp1_b32 s58, 2\n\t" \
  "s_cbranch_scc1 24b\n\t" \
  "s_mov_b64 exec, -1\n\t" \
  "s_sleep 10\n\t" \
  "s_add_u32 s59, s59, 0x10000\n\t" \
  "s_cmp_lt_u32 s59, 0x400000\n\t" \
  "s_cbranch_scc1 19b\n\t" \
  "s_or_b32 s58, s58, 1\n\t" \
  "s_branch 7f\n\t" \
  "14:\n\t" \
  "v_mov_b32 v184, 0\n\t" \
  "s_bitcmp1_b32 s58, 2\n\t" \
  "s_nop 0\n\t" \
  "s_cbranch_scc1 15b\n\t" \
  "s_mov_b32 s84, 910\n\t" \
  "s_add_u32 s59, s59, 0x10000\n\t" \
  "s_cmp_lt_u32 s59, 0x400000\n\t" \
  "s_mov_b64 exec, vcc\n\t" \
  "buffer_load_dword v187, v200, %[rh], s78 offen sc1\n\t" \
  "s_mov_b32 exec_lo, -1\n\t" \
  "s_cbranch_scc1 13b\n\t" \
  "s_mov_b64 exec, -1\n\t" \
  "s_or_b32 s58, s58, 1\n\t" \
  "s_branch 7f\n\t" \
  "27:\n\t" \
  "s_bitcmp1_b32 s58, 2\n\t" \
  "s_cbranch_scc1 28b\n\t" \
  "s_bitcmp1_b32 s58, 4\n\t" \
  "s_cbranch_scc1 23f\n\t" \
  "s_sub_i32 s80, s56, s41\n\t" \
  "s_mul_hi_u32 s80, s80, s45\n\t" \
  "s_lshl_b32 s80, s80, 1\n\t" \
  "s_sub_i32 s80, s80, s57\n\t" \
  "s_min_u32 s47, s47, s46\n\t" \
  "s_min_u32 s47, s47, s80\n\t" \
  "v_mov_b32 v188, s47\n\t" \
  "s_mov_b32 s47, s46\n\t" \
  "s_mov_b32 s46, s80\n\t" \
  "buffer_store_dword v188, v202, %[rh], s52 offen sc1\n\t" \
  "v_mov_b64 v[188:189], -1\n\t" \
  "v_mov_b64 v[190:191], -1\n\t" \
  "v_mov_b32 v187, s40\n\t" \
  "s_add_i32 s80, s40, -1\n\t" \
  "s_and_b32 s80, s80, 7\n\t" \
  "s_lshl_b32 s80, s80, 15\n\t" \
  "s_add_i32 s80, s80, s49\n\t" \
  "buffer_store_dwordx4 v[188:191], v208, %[rh], s80 offen sc1\n\t" \
  "buffer_store_dword v187, v209, %[ru], 0 offen sc1\n\t" \
  "ds_write_b128 v197, v[72:75] offset:16384\n\t" \
  "ds_write_b128 v197, v[76:79] offset:17408\n\t" \
  "ds_write_b128 v197, v[80:83] offset:18432\n\t" \
  "ds_write_b128 v197, v[84:87] offset:19456\n\t" \
  "ds_write_b128 v197, v[88:91] offset:20480\n\t" \
  "ds_write_b128 v197, v[92:95] offset:21504\n\t" \
  "ds_write_b128 v197, v[96:99] offset:22528\n\t" \
  "ds_write_b128 v197, v[100:103] offset:23552\n\t" \
  "s_or_b32 s58, s58, 16\n\t" \
  "23:\n\t" \
  "s_add_u32 s59, s59, 0x1000000\n\t" \
  "s_cmp_lt_u32 s59, 0x40000000\n\t" \
  "s_cbranch_scc1 26f\n\t" \
  "s_or_b32 s58, s58, 1\n\t" \
  "s_branch 7f\n\t" \
  "26:\n\t" \
  "buffer_load_dword v186, v203, %[ru], 0 offen sc1\n\t" \
  "s_waitcnt vmcnt(0)\n\t" \
  "s_branch 25b\n\t" \
  "37:\n\t" \
  "s_and_b32 s76, s40, 3\n\t" \
  "s_lshl_b32 s76, s76, 18\n\t" \
  "buffer_load_dwordx4 a[0:3], v211, %[rh], s76 offen offset:0 sc1\n\t" \
  "buffer_load_dwordx4 a[4:7], v211, %[rh], s76 offen offset:1024 sc1\n\t" \
  "buffer_load_dwordx4 a[8:11], v212, %[rh], s76 offen offset:0 sc1\n\t" \
  "buffer_load_dwordx4 a[12:15], v212, %[rh], s76 offen offset:1024 sc1\n\t" \
  "buffer_load_dwordx4 a[16:19], v213, %[rh], s76 offen offset:0 sc1\n\t" \
  "buffer_load_dwordx4 a[20:23], v213, %[rh], s76 offen offset:1024 sc1\n\t" \
  "buffer_load_dwordx4 a[24:27], v214, %[rh], s76 offen offset:0 sc1\n\t" \
  "buffer_load_dwordx4 a[28:31], v214, %[rh], s76 offen offset:1024 sc1\n\t" \
  "s_bitcmp1_b32 s58, 4\n\t" \
  "s_cbranch_scc1 29f\n\t" \
  "s_sub_i32 s80, s56, s41\n\t" \
  "s_mul_hi_u32 s80, s80, s45\n\t" \
  "s_lshl_b32 s80, s80, 1\n\t" \
  "s_sub_i32 s80, s80, s57\n\t" \
  "s_min_u32 s47, s47, s46\n\t" \
  "s_min_u32 s47, s47, s80\n\t" \
  "v_mov_b32 v188, s47\n\t" \
  "s_mov_b32 s47, s46\n\t" \
  "s_mov_b32 s46, s80\n\t" \
  "buffer_store_dword v188, v202, %[rh], s52 offen sc1\n\t" \
  "v_mov_b64 v[188:189], -1\n\t" \
  "v_mov_b64 v[190:191], -1\n\t" \
  "v_mov_b32 v187, s40\n\t" \
  "s_add_i32 s80, s40, -1\n\t" \
  "s_and_b32 s80, s80, 7\n\t" \
  "s_lshl_b32 s80, s80, 15\n\t" \
  "s_add_i32 s80, s80, s49\n\t" \
  "buffer_store_dwordx4 v[188:191], v208, %[rh], s80 offen sc1\n\t" \
  "buffer_store_dword v187, v209, %[ru], 0 offen sc1\n\t" \
  "ds_write_b128 v197, v[72:75] offset:16384\n\t" \
  "ds_write_b128 v197, v[76:79] offset:17408\n\t" \
  "ds_write_b128 v197, v[80:83] offset:18432\n\t" \
  "ds_write_b128 v197, v[84:87] offset:19456\n\t" \
  "ds_write_b128 v197, v[88:91] offset:20480\n\t" \
  "ds_write_b128 v197, v[92:95] offset:21504\n\t" \
  "ds_write_b128 v197, v[96:99] offset:22528\n\t" \
  "ds_write_b128 v197, v[100:103] offset:23552\n\t" \
  "29:\n\t" \
  "s_and_b32 s58, s58, 36\n\t" \
  "s_waitcnt lgkmcnt(0)\n\t" \
  "s_barrier\n\t" \
  "ds_read_b128 v[160:163], v193 offset:0\n\t" \
  "ds_read_b128 v[164:167], v193 offset:256\n\t" \
  "ds_read_b128 v[168:171], v193 offset:2048\n\t" \
  "ds_read_b128 v[172:175], v193 offset:2304\n\t" \
  "ds_read_b128 v[176:179], v193 offset:4096\n\t" \
  "ds_read_b128 v[180:183], v193 offset:4352\n\t" \
  "s_waitcnt lgkmcnt(2)\n\t" \
  "v_pk_add_f32 v[160:161], v[160:161], v[168:169]\n\t" \
  "v_pk_add_f32 v[162:163], v[162:163], v[170:171]\n\t" \
  "v_pk_add_f32 v[164:165], v[164:165], v[172:173]\n\t" \
  "v_pk_add_f32 v[166:167], v[166:167], v[174:175]\n\t" \
  "s_waitcnt lgkmcnt(0)\n\t" \
  "v_pk_add_f32 v[160:161], v[160:161], v[176:177]\n\t" \
  "v_pk_add_f32 v[162:163], v[162:163], v[178:179]\n\t" \
  "v_pk_add_f32 v[164:165], v[164:165], v[180:181]\n\t" \
  "v_pk_add_f32 v[166:167], v[166:167], v[182:183]\n\t" \
  "v_cvt_pk_bf16_f32 v160, v160, v161\n\t" \
  "v_cvt_pk_bf16_f32 v161, v162, v163\n\t" \
  "v_cvt_pk_bf16_f32 v162, v164, v165\n\t" \
  "v_cvt_pk_bf16_f32 v163, v166, v167\n\t" \
  "s_branch 38b\n\t" \
  "8:\n\t" \
  "s_mov_b64 vcc, -1\n\t" \
  "s_and_b32 s80, s58, 6\n\t" \
  "s_cbranch_scc1 6b\n\t" \
  "v_mov_b32 v151, 1\n\t" \
  "s_branch 5b\n\t" \
  "32:\n\t" \
  "s_and_b32 s80, s58, 36\n\t" \
  "s_cbranch_scc1 33b\n\t" \
  "s_add_i32 s40, s40, -2\n\t" \
  "34:\n\t" \
  "s_or_b32 s59, s59, 128\n\t" \
  "s_and_b32 s76, s40, 3\n\t" \
  "s_lshl_b32 s76, s76, 18\n\t" \
  "35:\n\t" \
  "buffer_load_dwordx4 a[0:3], v211, %[rh], s76 offen offset:0 sc1\n\t" \
  "buffer_load_dwordx4 a[4:7], v211, %[rh], s76 offen offset:1024 sc1\n\t" \
  "buffer_load_dwordx4 a[8:11], v212, %[rh], s76 offen offset:0 sc1\n\t" \
  "buffer_load_dwordx4 a[12:15], v212, %[rh], s76 offen offset:1024 sc1\n\t" \
  "buffer_load_dwordx4 a[16:19], v213, %[rh], s76 offen offset:0 sc1\n\t" \
  "buffer_load_dwordx4 a[20:23], v213, %[rh], s76 offen offset:1024 sc1\n\t" \
  "buffer_load_dwordx4 a[24:27], v214, %[rh], s76 offen offset:0 sc1\n\t" \
  "buffer_load_dwordx4 a[28:31], v214, %[rh], s76 offen offset:1024 sc1\n\t" \
  "s_waitcnt vmcnt(0)\n\t" \
  "s_bitcmp1_b32 s58, 2\n\t" \
  "s_cbranch_scc1 36f\n\t" \
  "v_accvgpr_read_b32 v0, a0\n\t" \
  "v_accvgpr_read_b32 v1, a1\n\t" \
  "v_accvgpr_read_b32 v2, a2\n\t" \
  "v_accvgpr_read_b32 v3, a3\n\t" \
  "v_accvgpr_read_b32 v4, a4\n\t" \
  "v_accvgpr_read_b32 v5, a5\n\t" \
  "v_accvgpr_read_b32 v6, a6\n\t" \
  "v_accvgpr_read_b32 v7, a7\n\t" \
  "v_accvgpr_read_b32 v8, a8\n\t" \
  "v_accvgpr_read_b32 v9, a9\n\t" \
  "v_accvgpr_read_b32 v10, a10\n\t" \
  "v_accvgpr_read_b32 v11, a11\n\t" \
  "v_accvgpr_read_b32 v12, a12\n\t" \
  "v_accvgpr_read_b32 v13, a13\n\t" \
  "v_accvgpr_read_b32 v14, a14\n\t" \
  "v_accvgpr_read_b32 v15, a15\n\t" \
  "v_accvgpr_read_b32 v16, a16\n\t" \
  "v_accvgpr_read_b32 v17, a17\n\t" \
  "v_accvgpr_read_b32 v18, a18\n\t" \
  "v_accvgpr_read_b32 v19, a19\n\t" \
  "v_accvgpr_read_b32 v20, a20\n\t" \
  "v_accvgpr_read_b32 v21, a21\n\t" \
  "v_accvgpr_read_b32 v22, a22\n\t" \
  "v_accvgpr_read_b32 v23, a23\n\t" \
  "v_accvgpr_read_b32 v24, a24\n\t" \
  "v_accvgpr_read_b32 v25, a25\n\t" \
  "v_accvgpr_read_b32 v26, a26\n\t" \
  "v_accvgpr_read_b32 v27, a27\n\t" \
  "v_accvgpr_read_b32 v28, a28\n\t" \
  "v_accvgpr_read_b32 v29, a29\n\t" \
  "v_accvgpr_read_b32 v30, a30\n\t" \
  "v_accvgpr_read_b32 v31, a31\n\t" \
  "v_max_u32 v148, v0, v1\n\t" \
  "v_max3_u32 v148, v148, v2, v3\n\t" \
  "v_max3_u32 v148, v148, v4, v5\n\t" \
  "v_max3_u32 v148, v148, v6, v7\n\t" \
  "v_max3_u32 v148, v148, v8, v9\n\t" \
  "v_max3_u32 v148, v148, v10, v11\n\t" \
  "v_max3_u32 v148, v148, v12, v13\n\t" \
  "v_max3_u32 v148, v148, v14, v15\n\t" \
  "v_max3_u32 v148, v148, v16, v17\n\t" \
  "v_max3_u32 v148, v148, v18, v19\n\t" \
  "v_max3_u32 v148, v148, v20, v21\n\t" \
  "v_max3_u32 v148, v148, v22, v23\n\t" \
  "v_max3_u32 v148, v148, v24, v25\n\t" \
  "v_max3_u32 v148, v148, v26, v27\n\t" \
  "v_max3_u32 v148, v148, v28, v29\n\t" \
  "v_max3_u32 v148, v148, v30, v31\n\t" \
  "v_cmp_eq_u32 vcc, -1, v148\n\t" \
  "s_cbranch_vccz 36f\n\t" \
  "s_add_u32 s59, s59, 0x1000000\n\t" \
  "s_cmp_lt_u32 s59, 0x10000000\n\t" \
  "s_cbranch_scc1 35b\n\t" \
  "s_or_b32 s58, s58, 9\n\t" \
  "s_branch 7f\n\t" \
  "36:\n\t" \
  "s_and_b32 s58, s58, 4\n\t" \
  "s_or_b32 s58, s58, 32\n\t" \
  "s_branch 31b\n\t" \
  "18:\n\t" \
  "v_mov_b32 v186, -1\n\t" \
  "s_and_b32 s80, s40, 3\n\t" \
  "s_mul_i32 s77, s80, 0xc000\n\t" \
  "s_add_i32 s77, s77, 0x40000\n\t" \
  "s_lshl_b32 s78, s80, 14\n\t" \
  "s_add_i32 s78, s78, 0x70000\n\t" \
  "s_mov_b32 s57, 0\n\t" \
  "s_add_i32 s96, s77, 0x1000\n\t" \
  "s_lshl_b32 s88, s80, 20\n\t" \
  "s_add_i32 s88, s88, s48\n\t" \
  "s_lshr_b32 s80, s88, 21\n\t" \
  "s_bitcmp1_b64 %[sel], s80\n\t" \
  "s_cselect_b32 s80, 0x2000, 0\n\t" \
  "s_add_i32 s88, s88, s80\n\t" \
  "s_xor_b32 s89, s88, 0x800\n\t" \
  "s_xor_b32 s90, s88, 0x1000\n\t" \
  "s_xor_b32 s91, s88, 0x1800\n\t" \
  "s_add_i32 s92, s77, s50\n\t" \
  "s_xor_b32 s93, s92, 0x100\n\t" \
  "s_xor_b32 s94, s92, 0x200\n\t" \
  "s_xor_b32 s95, s92, 0x300\n\t" \
  "s_add_i32 s97, s40, 1\n\t" \
  "s_and_b32 s97, s97, 7\n\t" \
  "s_lshl_b32 s97, s97, 15\n\t" \
  "s_add_i32 s97, s97, s49\n\t" \
  "12:\n\t" \
  "s_or_b32 s58, s58, 3\n\t" \
  "s_mov_b32 s54, 0x70000000\n\t" \
  "11:\n\t" \
  "buffer_load_dwordx4 v[72:75], v192, %[ru], s88 offen offset:0 sc1\n\t" \
  "buffer_load_dwordx4 v[76:79], v192, %[ru], s88 offen offset:1024 sc1\n\t" \
  "buffer_load_dwordx4 v[80:83], v192, %[ru], s89 offen offset:0 sc1\n\t" \
  "buffer_load_dwordx4 v[84:87], v192, %[ru], s89 offen offset:1024 sc1\n\t" \
  "buffer_load_dwordx4 v[88:91], v192, %[ru], s90 offen offset:0 sc1\n\t" \
  "buffer_load_dwordx4 v[92:95], v192, %[ru], s90 offen offset:1024 sc1\n\t" \
  "buffer_load_dwordx4 v[96:99], v192, %[ru], s91 offen offset:0 sc1\n\t" \
  "buffer_load_dwordx4 v[100:103], v192, %[ru], s91 offen offset:1024 sc1\n\t" \
  "buffer_load_dwordx4 v[104:107], v194, %[rh], s92 offen sc1\n\t" \
  "buffer_load_dwordx4 v[108:111], v194, %[rh], s93 offen sc1\n\t" \
  "buffer_load_dwordx4 v[112:115], v194, %[rh], s94 offen sc1\n\t" \
  "buffer_load_dwordx4 v[116:119], v194, %[rh], s95 offen sc1\n\t" \
  "s_waitcnt vmcnt(0)\n\t" \
  "s_add_u32 s59, s59, 1\n\t" \
  "s_bitcmp1_b32 s58, 2\n\t" \
  "s_cbranch_scc1 10b\n\t" \
  "v_max_u32 v148, v72, v73\n\t" \
  "v_max3_u32 v148, v148, v74, v75\n\t" \
  "v_max3_u32 v148, v148, v76, v77\n\t" \
  "v_max3_u32 v148, v148, v78, v79\n\t" \
  "v_max3_u32 v148, v148, v80, v81\n\t" \
  "v_max3_u32 v148, v148, v82, v83\n\t" \
  "v_max3_u32 v148, v148, v84, v85\n\t" \
  "v_max3_u32 v148, v148, v86, v87\n\t" \
  "v_max3_u32 v148, v148, v88, v89\n\t" \
  "v_max3_u32 v148, v148, v90, v91\n\t" \
  "v_max3_u32 v148, v148, v92, v93\n\t" \
  "v_max3_u32 v148, v148, v94, v95\n\t" \
  "v_max3_u32 v148, v148, v96, v97\n\t" \
  "v_max3_u32 v148, v148, v98, v99\n\t" \
  "v_max3_u32 v148, v148, v100, v101\n\t" \
  "v_max3_u32 v148, v148, v102, v103\n\t" \
  "v_max3_u32 v148, v148, v104, v105\n\t" \
  "v_max3_u32 v148, v148, v106, v107\n\t" \
  "v_max3_u32 v148, v148, v108, v109\n\t" \
  "v_max3_u32 v148, v148, v110, v111\n\t" \
  "v_max3_u32 v148, v148, v112, v113\n\t" \
  "v_max3_u32 v148, v148, v114, v115\n\t" \
  "v_max3_u32 v148, v148, v116, v117\n\t" \
  "v_max3_u32 v148, v148, v118, v119\n\t" \
  "v_cmp_eq_u32 vcc, -1, v148\n\t" \
  "s_cbranch_vccz 10b\n\t" \
  "s_bitcmp1_b32 s59, 6\n\t" \
  "s_cbranch_scc0 11b\n\t" \
  "7:\n\t" \
  "s_branch 92f\n\t" \
  "91:\n\t" \
  "s_mov_b32 s90, s41\n\t" \
  "s_mov_b32 s91, s43\n\t" \
  "s_mov_b32 s58, s42\n\t" \
  "s_mov_b32 s59, 0\n\t" \
  "s_bitcmp1_b32 s58, 4\n\t" \
  "s_cbranch_scc1 37f\n\t" \
  "s_bitcmp1_b32 s58, 3\n\t" \
  "s_cbranch_scc1 34f\n\t" \
  "s_bitcmp1_b32 s58, 1\n\t" \
  "s_cbranch_scc1 18f\n\t" \
  "17:\n\t" \
  "v_mov_b64 v[40:41], 0\n\t" \
  "v_mov_b64 v[42:43], 0\n\t" \
  "v_mov_b64 v[44:45], 0\n\t" \
  "v_mov_b64 v[46:47], 0\n\t" \
  "v_mov_b64 v[48:49], 0\n\t" \
  "v_mov_b64 v[50:51], 0\n\t" \
  "v_mov_b64 v[52:53], 0\n\t" \
  "v_mov_b64 v[54:55], 0\n\t" \
  "v_mov_b64 v[56:57], 0\n\t" \
  "v_mov_b64 v[58:59], 0\n\t" \
  "v_mov_b64 v[60:61], 0\n\t" \
  "v_mov_b64 v[62:63], 0\n\t" \
  "v_mov_b64 v[64:65], 0\n\t" \
  "v_mov_b64 v[66:67], 0\n\t" \
  "v_mov_b64 v[68:69], 0\n\t" \
  "v_mov_b64 v[70:71], 0\n\t" \
  "v_mov_b64 v[72:73], 0\n\t" \
  "v_mov_b64 v[74:75], 0\n\t" \
  "v_mov_b64 v[76:77], 0\n\t" \
  "v_mov_b64 v[78:79], 0\n\t" \
  "v_mov_b64 v[80:81], 0\n\t" \
  "v_mov_b64 v[82:83], 0\n\t" \
  "v_mov_b64 v[84:85], 0\n\t" \
  "v_mov_b64 v[86:87], 0\n\t" \
  "v_mov_b64 v[88:89], 0\n\t" \
  "v_mov_b64 v[90:91], 0\n\t" \
  "v_mov_b64 v[92:93], 0\n\t" \
  "v_mov_b64 v[94:95], 0\n\t" \
  "v_mov_b64 v[96:97], 0\n\t" \
  "v_mov_b64 v[98:99], 0\n\t" \
  "v_mov_b64 v[100:101], 0\n\t" \
  "v_mov_b64 v[102:103], 0\n\t" \
  "v_mov_b64 v[136:137], 0\n\t" \
  "v_mov_b64 v[138:139], 0\n\t" \
  "v_mov_b64 v[140:141], 0\n\t" \
  "v_mov_b64 v[142:143], 0\n\t" \
  "19:\n\t" \
  "s_add_i32 s85, s40, -1\n\t" \
  "s_and_b32 s82, s85, 2\n\t" \
  "s_lshl_b32 s82, s82, 13\n\t" \
  "s_and_b32 s80, s85, 1\n\t" \
  "s_lshl_b32 s80, s80, 12\n\t" \
  "s_add_i32 s82, s82, s80\n\t" \
  "s_add_i32 s82, s82, 0x84000\n\t" \
  "s_mov_b32 exec_hi, 0\n\t" \
  "buffer_load_dword v184, v200, %[rh], s82 offen sc1\n\t" \
  "buffer_load_dword v185, v201, %[rh], s52 offen sc1\n\t" \
  "s_mov_b64 exec, -1\n\t" \
  "20:\n\t" \
  "s_and_b32 s80, s40, 3\n\t" \
  "s_mul_i32 s77, s80, 0xc000\n\t" \
  "s_add_i32 s77, s77, 0x40000\n\t" \
  "s_lshl_b32 s78, s80, 14\n\t" \
  "s_add_i32 s78, s78, 0x70000\n\t" \
  "s_mov_b32 s57, 0\n\t" \
  "s_add_i32 s96, s77, 0x1000\n\t" \
  "s_and_b32 s98, s40, 2\n\t" \
  "s_lshl_b32 s98, s98, 13\n\t" \
  "s_and_b32 s80, s40, 1\n\t" \
  "s_lshl_b32 s80, s80, 12\n\t" \
  "s_add_i32 s98, s98, s80\n\t" \
  "s_add_i32 s98, s98, 0x84000\n\t" \
  "s_memtime vcc\n\t" \
  "s_memrealtime s[86:87]\n\t" \
  "s_nop 4\n\t" \
  "s_memrealtime s[60:61]\n\t" \
  "s_nop 4\n\t" \
  "s_memrealtime s[62:63]\n\t" \
  "s_nop 4\n\t" \
  "s_memrealtime s[64:65]\n\t" \
  "s_nop 4\n\t" \
  "s_memrealtime s[66:67]\n\t" \
  "s_nop 4\n\t" \
  "s_memrealtime s[68:69]\n\t" \
  "s_mov_b32 s79, 0\n\t" \
  "s_waitcnt lgkmcnt(0)\n\t" \
  "s_cmp_eq_u32 s86, s60\n\t" \
  "s_addc_u32 s79, s79, 0\n\t" \
  "s_cmp_eq_u32 s86, s62\n\t" \
  "s_addc_u32 s79, s79, 0\n\t" \
  "s_cmp_eq_u32 s86, s64\n\t" \
  "s_addc_u32 s79, s79, 0\n\t" \
  "s_cmp_eq_u32 s86, s66\n\t" \
  "s_addc_u32 s79, s79, 0\n\t" \
  "s_cmp_eq_u32 s86, s68\n\t" \
  "s_addc_u32 s79, s79, 0\n\t" \
  "s_mul_i32 s79, s79, 24\n\t" \
  "s_add_i32 s79, s79, vcc_lo\n\t" \
  "s_lshl4_add_u32 s80, s86, 64\n\t" \
  "s_mov_b32 s41, s79\n\t" \
  "s_mov_b32 s43, s80\n\t" \
  "s_lshl_b32 s83, s43, 8\n\t" \
  "s_mov_b32 s84, 610\n\t" \
  "s_mov_b32 exec_hi, 0\n\t" \
  "s_waitcnt vmcnt(0)\n\t" \
  "v_cmp_ne_u32_sdwa vcc, v184, s85 src0_sel:BYTE_0 src1_sel:DWORD\n\t" \
  "v_subrev_u32 v184, s83, v184\n\t" \
  "v_ashrrev_i32 v184, 8, v184\n\t" \
  "s_cbranch_vccnz 22f\n\t" \
  "v_max_i32_dpp v185, v185, v185 quad_perm:[1,0,3,2] row_mask:0xf bank_mask:0xf\n\t" \
  "v_max_i32_dpp v184, v184, v184 quad_perm:[1,0,3,2] row_mask:0xf bank_mask:0xf\n\t" \
  "s_nop 0\n\t" \
  "v_max_i32_dpp v185, v185, v185 quad_perm:[2,3,0,1] row_mask:0xf bank_mask:0xf\n\t" \
  "v_max_i32_dpp v184, v184, v184 quad_perm:[2,3,0,1] row_mask:0xf bank_mask:0xf\n\t" \
  "s_nop 0\n\t" \
  "v_max_i32_dpp v185, v185, v185 row_half_mirror row_mask:0xf bank_mask:0xf\n\t" \
  "v_max_i32_dpp v184, v184, v184 row_half_mirror row_mask:0xf bank_mask:0xf\n\t" \
  "v_readlane_b32 s54, v185, 0\n\t" \
  "s_nop 0\n\t" \
  "v_max_i32_dpp v184, v184, v184 row_mirror row_mask:0xf bank_mask:0xf\n\t" \
  "s_nop 0\n\t" \
  "v_readlane_b32 s81, v184, 0\n\t" \
  "v_readlane_b32 s82, v184, 16\n\t" \
  "s_max_i32 s81, s81, s82\n\t" \
  "s_add_i32 s76, s81, s43\n\t" \
  "s_add_i32 s81, s81, s53\n\t" \
  "s_max_i32 s81, s81, 0\n\t" \
  "s_min_i32 s81, s81, 20000\n\t" \
  "s_mul_i32 s81, s81, s44\n\t" \
  "s_lshr_b32 s81, s81, 16\n\t" \
  "s_add_i32 s81, s81, -1020\n\t" \
  "24:\n\t" \
  "s_add_i32 s81, s81, s41\n\t" \
  "s_min_u32 s54, s54, 1760\n\t" \
  "s_add_i32 s54, s54, 240\n\t" \
  ".p2align 6\n\t" \
  "s_nop 0\n\t" \
  "s_nop 0\n\t" \
  "s_nop 0\n\t" \
  "s_nop 0\n\t" \
  "s_nop 0\n\t" \
  "s_nop 0\n\t" \
  "s_nop 0\n\t" \
  "s_nop 0\n\t" \
  "1:\n\t" \
  "s_memtime vcc\n\t" \
  "s_waitcnt lgkmcnt(0)\n\t" \
  "s_sub_i32 s79, s81, vcc_lo\n\t" \
  "s_cmp_gt_i32 s79, 128\n\t" \
  "s_cbranch_scc1 1b\n\t" \
  ".p2align 4\n\t" \
  "2:\n\t" \
  "s_sub_i32 s79, s79, 33\n\t" \
  "s_cmp_gt_i32 s79, 56\n\t" \
  "s_cbranch_scc1 2b\n\t" \
  "buffer_load_dword v187, v200, %[rh], s78 offen sc1\n\t" \
  "s_memtime vcc\n\t" \
  "s_memrealtime s[86:87]\n\t" \
  "s_nop 4\n\t" \
  "s_memrealtime s[60:61]\n\t" \
  "s_nop 4\n\t" \
  "s_memrealtime s[62:63]\n\t" \
  "s_nop 4\n\t" \
  "s_memrealtime s[64:65]\n\t" \
  "s_nop 4\n\t" \
  "s_memrealtime s[66:67]\n\t" \
  "s_nop 4\n\t" \
  "s_memrealtime s[68:69]\n\t" \
  "s_mov_b32 s79, 0\n\t" \
  "s_waitcnt lgkmcnt(0)\n\t" \
  "s_mov_b64 exec, -1\n\t" \
  "ds_read_b128 a[0:3], v194 offset:0\n\t" \
  "ds_read_b128 a[4:7], v194 offset:1024\n\t" \
  "ds_read_b128 a[8:11], v194 offset:2048\n\t" \
  "ds_read_b128 a[12:15], v194 offset:3072\n\t" \
  "ds_read_b128 a[16:19], v194 offset:4096\n\t" \
  "ds_read_b128 a[20:23], v194 offset:5120\n\t" \
  "ds_read_b128 a[24:27], v194 offset:6144\n\t" \
  "ds_read_b128 a[28:31], v194 offset:7168\n\t" \
  "ds_read_b128 a[32:35], v194 offset:8192\n\t" \
  "ds_read_b128 a[36:39], v194 offset:9216\n\t" \
  "ds_read_b128 a[40:43], v194 offset:10240\n\t" \
  "ds_read_b128 a[44:47], v194 offset:11264\n\t" \
  "ds_read_b128 a[48:51], v194 offset:12288\n\t" \
  "ds_read_b128 a[52:55], v194 offset:13312\n\t" \
  "ds_read_b128 a[56:59], v194 offset:14336\n\t" \
  "ds_read_b128 a[60:63], v194 offset:15360\n\t" \
  "ds_read_b128 a[64:67], v194 offset:16384\n\t" \
  "ds_read_b128 a[68:71], v194 offset:17408\n\t" \
  "ds_read_b128 a[72:75], v194 offset:18432\n\t" \
  "ds_read_b128 a[76:79], v194 offset:19456\n\t" \
  "ds_read_b128 a[80:83], v194 offset:20480\n\t" \
  "ds_read_b128 a[84:87], v194 offset:21504\n\t" \
  "ds_read_b128 a[88:91], v194 offset:22528\n\t" \
  "ds_read_b128 a[92:95], v194 offset:23552\n\t" \
  "ds_read_b128 a[96:99], v194 offset:24576\n\t" \
  "ds_read_b128 a[100:103], v194 offset:25600\n\t" \
  "ds_read_b128 a[104:107], v194 offset:26624\n\t" \
  "ds_read_b128 a[108:111], v194 offset:27648\n\t" \
  "ds_read_b128 a[112:115], v194 offset:28672\n\t" \
  "ds_read_b128 a[116:119], v194 offset:29696\n\t" \
  "ds_read_b128 a[120:123], v194 offset:30720\n\t" \
  "ds_read_b128 a[124:127], v194 offset:31744\n\t" \
  "s_mov_b32 exec_hi, 0\n\t" \
  "s_cmp_eq_u32 s86, s60\n\t" \
  "s_addc_u32 s79, s79, 0\n\t" \
  "s_cmp_eq_u32 s86, s62\n\t" \
  "s_addc_u32 s79, s79, 0\n\t" \
  "s_cmp_eq_u32 s86, s64\n\t" \
  "s_addc_u32 s79, s79, 0\n\t" \
  "s_cmp_eq_u32 s86, s66\n\t" \
  "s_addc_u32 s79, s79, 0\n\t" \
  "s_cmp_eq_u32 s86, s68\n\t" \
  "s_addc_u32 s79, s79, 0\n\t" \
  "s_mul_i32 s79, s79, 24\n\t" \
  "s_add_i32 s79, s79, vcc_lo\n\t" \
  "s_lshl4_add_u32 s80, s86, 64\n\t" \
  "s_mov_b32 s41, s79\n\t" \
  "s_mov_b32 s43, s80\n\t" \
  "s_lshl_b32 s83, s43, 8\n\t" \
  "s_add_i32 s54, s54, s43\n\t" \
  "13:\n\t" \
  "s_waitcnt vmcnt(0)\n\t" \
  "s_memtime s[80:81]\n\t" \
  "v_cmp_ne_u32_sdwa vcc, v187, s40 src0_sel:BYTE_0 src1_sel:DWORD\n\t" \
  "v_subrev_u32 v184, s83, v187\n\t" \
  "v_ashrrev_i32 v184, 8, v184\n\t" \
  "s_nop 1\n\t" \
  "v_max_i32_dpp v184, v184, v184 quad_perm:[1,0,3,2] row_mask:0xf bank_mask:0xf\n\t" \
  "s_nop 1\n\t" \
  "v_max_i32_dpp v184, v184, v184 quad_perm:[2,3,0,1] row_mask:0xf bank_mask:0xf\n\t" \
  "s_cbranch_vccnz 14f\n\t" \
  "15:\n\t" \
  "s_nop 0\n\t" \
  "v_max_i32_dpp v184, v184, v184 row_half_mirror row_mask:0xf bank_mask:0xf\n\t" \
  "s_nop 1\n\t" \
  "v_max_i32_dpp v184, v184, v184 row_mirror row_mask:0xf bank_mask:0xf\n\t" \
  "s_mov_b64 exec, -1\n\t" \
  "v_readlane_b32 s57, v184, 0\n\t" \
  "v_readlane_b32 s82, v184, 16\n\t" \
  "s_waitcnt lgkmcnt(0)\n\t" \
  "s_sub_i32 s79, s41, s80\n\t" \
  "s_max_i32 s57, s57, s82\n\t" \
  "s_add_i32 s81, s57, 212\n\t" \
  "s_max_i32 s81, s81, 0\n\t" \
  "s_min_i32 s81, s81, 20000\n\t" \
  "s_mul_i32 s81, s81, s44\n\t" \
  "s_lshr_b32 s81, s81, 16\n\t" \
  "s_add_i32 s79, s79, s81\n\t" \
  "s_sub_i32 s85, s84, s79\n\t" \
  "s_max_i32 s85, s85, 0\n\t" \
  "s_mul_hi_u32 s85, s85, s45\n\t" \
  "s_lshl1_add_u32 s85, s85, s57\n\t" \
  "s_add_i32 s85, s85, s54\n\t" \
  "s_lshl_b32 s85, s85, 8\n\t" \
  "s_or_b32 s85, s85, s40\n\t" \
  "v_mov_b32 v184, s85\n\t" \
  "buffer_store_dword v184, v202, %[rh], s98 offen sc1\n\t" \
  "s_add_i32 s79, s79, -620\n\t" \
  ".p2align 4\n\t" \
  "3:\n\t" \
  "s_sub_i32 s79, s79, 33\n\t" \
  "s_cmp_gt_i32 s79, 56\n\t" \
  "s_cbranch_scc1 3b\n\t" \
  "buffer_load_dwordx4 v[0:3], v193, %[rh], s77 offen offset:0 sc1\n\t" \
  "s_nop 4\n\t" \
  "buffer_load_dwordx4 v[4:7], v193, %[rh], s77 offen offset:1024 sc1\n\t" \
  "s_nop 4\n\t" \
  "buffer_load_dwordx4 v[8:11], v193, %[rh], s77 offen offset:2048 sc1\n\t" \
  "s_nop 4\n\t" \
  "buffer_load_dwordx4 v[12:15], v193, %[rh], s77 offen offset:3072 sc1\n\t" \
  "s_nop 4\n\t" \
  "buffer_load_dwordx4 v[16:19], v193, %[rh], s96 offen offset:0 sc1\n\t" \
  "s_nop 4\n\t" \
  "buffer_load_dwordx4 v[20:23], v193, %[rh], s96 offen offset:1024 sc1\n\t" \
  "s_nop 4\n\t" \
  "buffer_load_dwordx4 v[24:27], v193, %[rh], s96 offen offset:2048 sc1\n\t" \
  "s_nop 4\n\t" \
  "buffer_load_dwordx4 v[28:31], v193, %[rh], s96 offen offset:3072 sc1\n\t" \
  "s_add_i32 s79, s79, 267\n\t" \
  ".p2align 4\n\t" \
  "4:\n\t" \
  "s_sub_i32 s79, s79, 33\n\t" \
  "s_cmp_gt_i32 s79, 56\n\t" \
  "s_cbranch_scc1 4b\n\t" \
  "buffer_load_dwordx4 v[32:35], v195, %[rh], s77 offen offset:0 sc1\n\t" \
  "buffer_load_dwordx4 v[36:39], v195, %[rh], s77 offen offset:2048 sc1\n\t" \
  "10:\n\t" \
  "s_waitcnt vmcnt(9)\n\t" \
  "v_smfmac_f32_16x16x64_bf16 v[40:43], v[0:3], a[0:7], v203\n\t" \
  "v_smfmac_f32_16x16x64_bf16 v[56:59], v[0:3], a[8:15], v203\n\t" \
  "s_waitcnt vmcnt(8)\n\t" \
  "v_smfmac_f32_16x16x64_bf16 v[44:47], v[4:7], a[16:23], v203\n\t" \
  "v_cvt_pk_bf16_f32 v40, v40, v41\n\t" \
  "v_cvt_pk_bf16_f32 v41, v42, v43\n\t" \
  "v_smfmac_f32_16x16x64_bf16 v[60:63], v[4:7], a[24:31], v203\n\t" \
  "v_cvt_pk_bf16_f32 v56, v56, v57\n\t" \
  "v_cvt_pk_bf16_f32 v57, v58, v59\n\t" \
  "s_waitcnt vmcnt(7)\n\t" \
  "v_smfmac_f32_16x16x64_bf16 v[48:51], v[8:11], a[32:39], v203\n\t" \
  "v_cvt_pk_bf16_f32 v42, v44, v45\n\t" \
  "v_cvt_pk_bf16_f32 v43, v46, v47\n\t" \
  "v_smfmac_f32_16x16x64_bf16 v[64:67], v[8:11], a[40:47], v203\n\t" \
  "v_cvt_pk_bf16_f32 v58, v60, v61\n\t" \
  "v_cvt_pk_bf16_f32 v59, v62, v63\n\t" \
  "s_waitcnt vmcnt(6)\n\t" \
  "v_smfmac_f32_16x16x64_bf16 v[52:55], v[12:15], a[48:55], v203\n\t" \
  "v_cvt_pk_bf16_f32 v44, v48, v49\n\t" \
  "v_cvt_pk_bf16_f32 v45, v50, v51\n\t" \
  "v_smfmac_f32_16x16x64_bf16 v[68:71], v[12:15], a[56:63], v203\n\t" \
  "v_cvt_pk_bf16_f32 v60, v64, v65\n\t" \
  "v_cvt_pk_bf16_f32 v61, v66, v67\n\t" \
  "s_waitcnt vmcnt(5)\n\t" \
  "v_smfmac_f32_16x16x64_bf16 v[72:75], v[16:19], a[64:71], v203\n\t" \
  "v_cvt_pk_bf16_f32 v46, v52, v53\n\t" \
  "v_cvt_pk_bf16_f32 v47, v54, v55\n\t" \
  "v_smfmac_f32_16x16x64_bf16 v[88:91], v[16:19], a[72:79], v203\n\t" \
  "v_cvt_pk_bf16_f32 v62, v68, v69\n\t" \
  "v_cvt_pk_bf16_f32 v63, v70, v71\n\t" \
  "s_waitcnt vmcnt(4)\n\t" \
  "v_smfmac_f32_16x16x64_bf16 v[76:79], v[20:23], a[80:87], v203\n\t" \
  "v_cvt_pk_bf16_f32 v72, v72, v73\n\t" \
  "v_cvt_pk_bf16_f32 v73, v74, v75\n\t" \
  "v_smfmac_f32_16x16x64_bf16 v[92:95], v[20:23], a[88:95], v203\n\t" \
  "v_cvt_pk_bf16_f32 v88, v88, v89\n\t" \
  "v_cvt_pk_bf16_f32 v89, v90, v91\n\t" \
  "s_waitcnt vmcnt(3)\n\t" \
  "v_smfmac_f32_16x16x64_bf16 v[80:83], v[24:27], a[96:103], v203\n\t" \
  "v_cvt_pk_bf16_f32 v74, v76, v77\n\t" \
  "v_cvt_pk_bf16_f32 v75, v78, v79\n\t" \
  "v_smfmac_f32_16x16x64_bf16 v[96:99], v[24:27], a[104:111], v203\n\t" \
  "v_cvt_pk_bf16_f32 v90, v92, v93\n\t" \
  "v_cvt_pk_bf16_f32 v91, v94, v95\n\t" \
  "s_waitcnt vmcnt(2)\n\t" \
  "v_smfmac_f32_16x16x64_bf16 v[84:87], v[28:31], a[112:119], v203\n\t" \
  "v_cvt_pk_bf16_f32 v76, v80, v81\n\t" \
  "v_cvt_pk_bf16_f32 v77, v82, v83\n\t" \
  "v_smfmac_f32_16x16x64_bf16 v[100:103], v[28:31], a[120:127], v203\n\t" \
  "v_cvt_pk_bf16_f32 v92, v96, v97\n\t" \
  "v_cvt_pk_bf16_f32 v93, v98, v99\n\t" \
  "s_nop 1\n\t" \
  "v_cvt_pk_bf16_f32 v78, v84, v85\n\t" \
  "v_cvt_pk_bf16_f32 v79, v86, v87\n\t" \
  "s_nop 1\n\t" \
  "v_cvt_pk_bf16_f32 v94, v100, v101\n\t" \
  "v_cvt_pk_bf16_f32 v95, v102, v103\n\t" \
  "s_waitcnt vmcnt(1)\n\t" \
  "v_smfmac_f32_16x16x64_bf16 v[136:139], v[32:35], v[40:47], v204\n\t" \
  "v_smfmac_f32_16x16x64_bf16 v[140:143], v[32:35], v[56:63], v204\n\t" \
  "s_waitcnt vmcnt(0)\n\t" \
  "v_smfmac_f32_16x16x64_bf16 v[136:139], v[36:39], v[72:79], v204\n\t" \
  "v_smfmac_f32_16x16x64_bf16 v[140:143], v[36:39], v[88:95], v204\n\t" \
  "s_nop 3\n\t" \
  "v_permlane32_swap_b32 v136, v138\n\t" \
  "v_permlane32_swap_b32 v137, v139\n\t" \
  "v_pk_add_f32 v[136:137], v[136:137], v[138:139]\n\t" \
  "s_nop 0\n\t" \
  "v_permlane32_swap_b32 v140, v142\n\t" \
  "v_permlane32_swap_b32 v141, v143\n\t" \
  "v_pk_add_f32 v[138:139], v[140:141], v[142:143]\n\t" \
  "ds_write2st64_b64 v197, v[136:137], v[138:139] offset1:2\n\t" \
  "s_waitcnt lgkmcnt(0)\n\t" \
  "s_barrier\n\t" \
  "s_sub_i32 s86, s41, s90\n\t" \
  "s_sub_i32 s87, s43, s91\n\t" \
  "s_mov_b32 s90, s41\n\t" \
  "s_mov_b32 s91, s43\n\t" \
  "s_cmp_lt_u32 s86, 4000\n\t" \
  "s_cbranch_scc1 21f\n\t" \
  "v_cvt_f32_u32 v165, s86\n\t" \
  "v_cvt_f32_u32 v164, s87\n\t" \
  "v_rcp_f32 v165, v165\n\t" \
  "s_nop 0\n\t" \
  "v_mul_f32 v165, v165, v164\n\t" \
  "v_max_f32 v165, 0x3f028f5c, v165\n\t" \
  "v_min_f32 v165, 0x3ffeb852, v165\n\t" \
  "v_rcp_f32 v164, v165\n\t" \
  "v_mul_f32 v165, 0x4f000000, v165\n\t" \
  "v_mul_f32 v164, 0x47800000, v164\n\t" \
  "v_cvt_u32_f32 v165, v165\n\t" \
  "v_cvt_u32_f32 v164, v164\n\t" \
  "s_nop 0\n\t" \
  "v_readfirstlane_b32 s45, v165\n\t" \
  "v_readfirstlane_b32 s44, v164\n\t" \
  "21:\n\t" \
  "s_barrier\n\t" \
  "ds_read2_b32 v[148:149], v207 offset1:8\n\t" \
  "s_waitcnt lgkmcnt(0)\n\t" \
  "v_or_b32 v148, v148, v149\n\t" \
  "v_cmp_ne_u32 vcc, 0, v148\n\t" \
  "s_cbranch_vccnz 12f\n\t" \
  "s_and_b32 s58, s58, 4\n\t" \
  "s_add_i32 s80, s57, s43\n\t" \
  "s_sub_i32 s80, s80, s76\n\t" \
  "s_add_i32 s53, s53, -12\n\t" \
  "s_max_i32 s80, s80, s53\n\t" \
  "s_add_i32 s53, s53, 24\n\t" \
  "s_min_i32 s53, s53, s80\n\t" \
  "s_and_b32 s76, s40, 3\n\t" \
  "s_lshl_b32 s76, s76, 18\n\t" \
  "s_add_i32 s77, s76, 0x10000\n\t" \
  "s_add_i32 s78, s76, 0x1000\n\t" \
  "s_add_i32 s79, s76, 0x11000\n\t" \
  "s_mov_b32 s97, 1\n\t" \
  "s_cmp_ge_i32 s40, 62\n\t" \
  "s_cbranch_scc1 31f\n\t" \
  "s_and_b32 s82, s40, 3\n\t" \
  "s_lshl_b32 s82, s82, 20\n\t" \
  "s_add_i32 s82, s82, s48\n\t" \
  "s_andn2_b32 s82, s82, 0x1800\n\t" \
  "s_lshr_b32 s80, s82, 21\n\t" \
  "s_bitcmp1_b64 %[sel], s80\n\t" \
  "s_cselect_b32 s80, 0x2000, 0\n\t" \
  "s_add_i32 s82, s82, s80\n\t" \
  "s_add_i32 s83, s82, 0x1000\n\t" \
  "v_mov_b64 v[188:189], -1\n\t" \
  "v_mov_b64 v[190:191], -1\n\t" \
  "s_sleep 0\n\t" \
  "buffer_store_dwordx4 v[188:191], v192, %[ru], s82 offen offset:0 sc1\n\t" \
  "buffer_store_dwordx4 v[188:191], v192, %[ru], s82 offen offset:1024 sc1\n\t" \
  "buffer_store_dwordx4 v[188:191], v192, %[ru], s82 offen offset:2048 sc1\n\t" \
  "buffer_store_dwordx4 v[188:191], v192, %[ru], s82 offen offset:3072 sc1\n\t" \
  "buffer_store_dwordx4 v[188:191], v192, %[ru], s83 offen offset:0 sc1\n\t" \
  "buffer_store_dwordx4 v[188:191], v192, %[ru], s83 offen offset:1024 sc1\n\t" \
  "buffer_store_dwordx4 v[188:191], v192, %[ru], s83 offen offset:2048 sc1\n\t" \
  "buffer_store_dwordx4 v[188:191], v192, %[ru], s83 offen offset:3072 sc1\n\t" \
  "31:\n\t" \
  "buffer_load_dwordx4 v[0:3], v210, %[rh], s76 offen offset:0 sc1\n\t" \
  "s_nop 10\n\t" \
  "buffer_load_dwordx4 v[4:7], v210, %[rh], s76 offen offset:1024 sc1\n\t" \
  "s_nop 10\n\t" \
  "buffer_load_dwordx4 v[8:11], v210, %[rh], s77 offen offset:0 sc1\n\t" \
  "s_nop 10\n\t" \
  "buffer_load_dwordx4 v[12:15], v210, %[rh], s77 offen offset:1024 sc1\n\t" \
  "s_cmp_eq_u32 s97, 0\n\t" \
  "s_cbranch_scc1 26f\n\t" \
  "s_barrier\n\t" \
  "s_mov_b32 s97, 0\n\t" \
  "26:\n\t" \
  "buffer_load_dwordx4 v[16:19], v210, %[rh], s76 offen offset:2048 sc1\n\t" \
  "s_nop 10\n\t" \
  "buffer_load_dwordx4 v[20:23], v210, %[rh], s76 offen offset:3072 sc1\n\t" \
  "s_nop 10\n\t" \
  "buffer_load_dwordx4 v[24:27], v210, %[rh], s77 offen offset:2048 sc1\n\t" \
  "s_nop 10\n\t" \
  "buffer_load_dwordx4 v[28:31], v210, %[rh], s77 offen offset:3072 sc1\n\t" \
  "s_nop 10\n\t" \
  "buffer_load_dwordx4 v[32:35], v210, %[rh], s78 offen offset:0 sc1\n\t" \
  "s_nop 10\n\t" \
  "buffer_load_dwordx4 v[36:39], v210, %[rh], s78 offen offset:1024 sc1\n\t" \
  "s_nop 10\n\t" \
  "buffer_load_dwordx4 v[40:43], v210, %[rh], s79 offen offset:0 sc1\n\t" \
  "s_nop 10\n\t" \
  "buffer_load_dwordx4 v[44:47], v210, %[rh], s79 offen offset:1024 sc1\n\t" \
  "s_nop 10\n\t" \
  "buffer_load_dwordx4 v[48:51], v210, %[rh], s78 offen offset:2048 sc1\n\t" \
  "s_nop 10\n\t" \
  "buffer_load_dwordx4 v[52:55], v210, %[rh], s78 offen offset:3072 sc1\n\t" \
  "s_nop 10\n\t" \
  "buffer_load_dwordx4 v[56:59], v210, %[rh], s79 offen offset:2048 sc1\n\t" \
  "s_nop 10\n\t" \
  "buffer_load_dwordx4 v[60:63], v210, %[rh], s79 offen offset:3072 sc1\n\t" \
  "s_nop 10\n\t" \
  "buffer_load_dwordx4 v[64:67], v211, %[rh], s76 offen offset:0 sc1\n\t" \
  "s_nop 10\n\t" \
  "buffer_load_dwordx4 v[68:71], v211, %[rh], s76 offen offset:1024 sc1\n\t" \
  "s_nop 10\n\t" \
  "buffer_load_dwordx4 v[72:75], v211, %[rh], s77 offen offset:0 sc1\n\t" \
  "s_nop 10\n\t" \
  "buffer_load_dwordx4 v[76:79], v211, %[rh], s77 offen offset:1024 sc1\n\t" \
  "s_nop 10\n\t" \
  "buffer_load_dwordx4 v[80:83], v211, %[rh], s76 offen offset:2048 sc1\n\t" \
  "s_nop 10\n\t" \
  "buffer_load_dwordx4 v[84:87], v211, %[rh], s76 offen offset:3072 sc1\n\t" \
  "s_nop 10\n\t" \
  "buffer_load_dwordx4 v[88:91], v211, %[rh], s77 offen offset:2048 sc1\n\t" \
  "s_nop 10\n\t" \
  "buffer_load_dwordx4 v[92:95], v211, %[rh], s77 offen offset:3072 sc1\n\t" \
  "s_nop 10\n\t" \
  "v_mov_b64 v[136:137], 0\n\t" \
  "v_mov_b64 v[138:139], 0\n\t" \
  "v_mov_b64 v[140:141], 0\n\t" \
  "v_mov_b64 v[142:143], 0\n\t" \
  "s_waitcnt vmcnt(22)\n\t" \
  "v_swap_b32 v1, v4\n\t" \
  "v_swap_b32 v3, v6\n\t" \
  "s_nop 1\n\t" \
  "v_mfma_f32_16x16x32_bf16 v[168:171], a[0:3], v[0:3], 0\n\t" \
  "buffer_load_dwordx4 v[96:99], v211, %[rh], s78 offen offset:0 sc1\n\t" \
  "v_mfma_f32_16x16x32_bf16 v[172:175], a[8:11], v[0:3], 0\n\t" \
  "s_waitcnt vmcnt(21)\n\t" \
  "v_swap_b32 v9, v12\n\t" \
  "v_mfma_f32_16x16x32_bf16 v[168:171], a[4:7], v[4:7], v[168:171]\n\t" \
  "v_swap_b32 v11, v14\n\t" \
  "v_mfma_f32_16x16x32_bf16 v[172:175], a[12:15], v[4:7], v[172:175]\n\t" \
  "buffer_load_dwordx4 v[100:103], v211, %[rh], s78 offen offset:1024 sc1\n\t" \
  "v_mfma_f32_16x16x32_bf16 v[176:179], a[0:3], v[8:11], 0\n\t" \
  "buffer_load_dwordx4 v[104:107], v211, %[rh], s79 offen offset:0 sc1\n\t" \
  "v_mfma_f32_16x16x32_bf16 v[180:183], a[8:11], v[8:11], 0\n\t" \
  "s_waitcnt vmcnt(21)\n\t" \
  "v_swap_b32 v17, v20\n\t" \
  "v_mfma_f32_16x16x32_bf16 v[176:179], a[4:7], v[12:15], v[176:179]\n\t" \
  "v_swap_b32 v19, v22\n\t" \
  "v_mfma_f32_16x16x32_bf16 v[180:183], a[12:15], v[12:15], v[180:183]\n\t" \
  "buffer_load_dwordx4 v[108:111], v211, %[rh], s79 offen offset:1024 sc1\n\t" \
  "v_mfma_f32_16x16x32_bf16 v[168:171], a[16:19], v[16:19], v[168:171]\n\t" \
  "buffer_load_dwordx4 v[112:115], v211, %[rh], s78 offen offset:2048 sc1\n\t" \
  "v_mfma_f32_16x16x32_bf16 v[172:175], a[24:27], v[16:19], v[172:175]\n\t" \
  "s_waitcnt vmcnt(21)\n\t" \
  "v_swap_b32 v25, v28\n\t" \
  "v_mfma_f32_16x16x32_bf16 v[168:171], a[20:23], v[20:23], v[168:171]\n\t" \
  "v_swap_b32 v27, v30\n\t" \
  "v_mfma_f32_16x16x32_bf16 v[172:175], a[28:31], v[20:23], v[172:175]\n\t" \
  "buffer_load_dwordx4 v[116:119], v211, %[rh], s78 offen offset:3072 sc1\n\t" \
  "v_mfma_f32_16x16x32_bf16 v[176:179], a[16:19], v[24:27], v[176:179]\n\t" \
  "buffer_load_dwordx4 v[120:123], v211, %[rh], s79 offen offset:2048 sc1\n\t" \
  "v_mfma_f32_16x16x32_bf16 v[180:183], a[24:27], v[24:27], v[180:183]\n\t" \
  "s_waitcnt vmcnt(21)\n\t" \
  "v_swap_b32 v33, v36\n\t" \
  "v_mfma_f32_16x16x32_bf16 v[176:179], a[20:23], v[28:31], v[176:179]\n\t" \
  "v_swap_b32 v35, v38\n\t" \
  "v_mfma_f32_16x16x32_bf16 v[180:183], a[28:31], v[28:31], v[180:183]\n\t" \
  "buffer_load_dwordx4 v[124:127], v211, %[rh], s79 offen offset:3072 sc1\n\t" \
  "v_mfma_f32_16x16x32_bf16 v[168:171], a[32:35], v[32:35], v[168:171]\n\t" \
  "v_mfma_f32_16x16x32_bf16 v[172:175], a[40:43], v[32:35], v[172:175]\n\t" \
  "s_waitcnt vmcnt(20)\n\t" \
  "v_swap_b32 v41, v44\n\t" \
  "v_mfma_f32_16x16x32_bf16 v[168:171], a[36:39], v[36:39], v[168:171]\n\t" \
  "v_swap_b32 v43, v46\n\t" \
  "v_mfma_f32_16x16x32_bf16 v[172:175], a[44:47], v[36:39], v[172:175]\n\t" \
  "s_nop 0\n\t" \
  "v_mfma_f32_16x16x32_bf16 v[176:179], a[32:35], v[40:43], v[176:179]\n\t" \
  "v_mfma_f32_16x16x32_bf16 v[180:183], a[40:43], v[40:43], v[180:183]\n\t" \
  "s_waitcnt vmcnt(18)\n\t" \
  "v_swap_b32 v49, v52\n\t" \
  "v_mfma_f32_16x16x32_bf16 v[176:179], a[36:39], v[44:47], v[176:179]\n\t" \
  "v_swap_b32 v51, v54\n\t" \
  "v_mfma_f32_16x16x32_bf16 v[180:183], a[44:47], v[44:47], v[180:183]\n\t" \
  "v_mov_b64 v[40:41], 0\n\t" \
  "v_mov_b64 v[42:43], 0\n\t" \
  "v_mfma_f32_16x16x32_bf16 v[168:171], a[48:51], v[48:51], v[168:171]\n\t" \
  "v_mov_b64 v[44:45], 0\n\t" \
  "v_mov_b64 v[46:47], 0\n\t" \
  "v_mfma_f32_16x16x32_bf16 v[172:175], a[56:59], v[48:51], v[172:175]\n\t" \
  "s_waitcnt vmcnt(16)\n\t" \
  "v_swap_b32 v57, v60\n\t" \
  "v_mfma_f32_16x16x32_bf16 v[168:171], a[52:55], v[52:55], v[168:171]\n\t" \
  "v_swap_b32 v59, v62\n\t" \
  "v_mfma_f32_16x16x32_bf16 v[172:175], a[60:63], v[52:55], v[172:175]\n\t" \
  "v_mov_b64 v[48:49], 0\n\t" \
  "v_mov_b64 v[50:51], 0\n\t" \
  "v_mfma_f32_16x16x32_bf16 v[176:179], a[48:51], v[56:59], v[176:179]\n\t" \
  "v_mov_b64 v[52:53], 0\n\t" \
  "v_mov_b64 v[54:55], 0\n\t" \
  "v_mfma_f32_16x16x32_bf16 v[180:183], a[56:59], v[56:59], v[180:183]\n\t" \
  "s_waitcnt vmcnt(14)\n\t" \
  "v_swap_b32 v65, v68\n\t" \
  "v_mfma_f32_16x16x32_bf16 v[176:179], a[52:55], v[60:63], v[176:179]\n\t" \
  "v_swap_b32 v67, v70\n\t" \
  "v_mfma_f32_16x16x32_bf16 v[180:183], a[60:63], v[60:63], v[180:183]\n\t" \
  "v_mov_b64 v[56:57], 0\n\t" \
  "v_mov_b64 v[58:59], 0\n\t" \
  "v_mfma_f32_16x16x32_bf16 v[168:171], a[64:67], v[64:67], v[168:171]\n\t" \
  "v_mov_b64 v[60:61], 0\n\t" \
  "v_mov_b64 v[62:63], 0\n\t" \
  "v_mfma_f32_16x16x32_bf16 v[172:175], a[72:75], v[64:67], v[172:175]\n\t" \
  "s_waitcnt vmcnt(12)\n\t" \
  "v_swap_b32 v73, v76\n\t" \
  "v_mfma_f32_16x16x32_bf16 v[168:171], a[68:71], v[68:71], v[168:171]\n\t" \
  "v_swap_b32 v75, v78\n\t" \
  "v_mfma_f32_16x16x32_bf16 v[172:175], a[76:79], v[68:71], v[172:175]\n\t" \
  "v_mov_b64 v[64:65], 0\n\t" \
  "v_mov_b64 v[66:67], 0\n\t" \
  "v_mfma_f32_16x16x32_bf16 v[176:179], a[64:67], v[72:75], v[176:179]\n\t" \
  "v_mov_b64 v[68:69], 0\n\t" \
  "v_mov_b64 v[70:71], 0\n\t" \
  "v_mfma_f32_16x16x32_bf16 v[180:183], a[72:75], v[72:75], v[180:183]\n\t" \
  "s_waitcnt vmcnt(10)\n\t" \
  "v_swap_b32 v81, v84\n\t" \
  "v_mfma_f32_16x16x32_bf16 v[176:179], a[68:71], v[76:79], v[176:179]\n\t" \
  "v_swap_b32 v83, v86\n\t" \
  "v_mfma_f32_16x16x32_bf16 v[180:183], a[76:79], v[76:79], v[180:183]\n\t" \
  "v_mov_b64 v[72:73], 0\n\t" \
  "v_mov_b64 v[74:75], 0\n\t" \
  "v_mfma_f32_16x16x32_bf16 v[168:171], a[80:83], v[80:83], v[168:171]\n\t" \
  "v_mov_b64 v[76:77], 0\n\t" \
  "v_mov_b64 v[78:79], 0\n\t" \
  "v_mfma_f32_16x16x32_bf16 v[172:175], a[88:91], v[80:83], v[172:175]\n\t" \
  "s_waitcnt vmcnt(8)\n\t" \
  "v_swap_b32 v89, v92\n\t" \
  "v_mfma_f32_16x16x32_bf16 v[168:171], a[84:87], v[84:87], v[168:171]\n\t" \
  "v_swap_b32 v91, v94\n\t" \
  "v_mfma_f32_16x16x32_bf16 v[172:175], a[92:95], v[84:87], v[172:175]\n\t" \
  "v_mov_b64 v[80:81], 0\n\t" \
  "v_mov_b64 v[82:83], 0\n\t" \
  "v_mfma_f32_16x16x32_bf16 v[176:179], a[80:83], v[88:91], v[176:179]\n\t" \
  "v_mov_b64 v[84:85], 0\n\t" \
  "v_mov_b64 v[86:87], 0\n\t" \
  "v_mfma_f32_16x16x32_bf16 v[180:183], a[88:91], v[88:91], v[180:183]\n\t" \
  "s_waitcnt vmcnt(6)\n\t" \
  "v_swap_b32 v97, v100\n\t" \
  "v_mfma_f32_16x16x32_bf16 v[176:179], a[84:87], v[92:95], v[176:179]\n\t" \
  "v_swap_b32 v99, v102\n\t" \
  "v_mfma_f32_16x16x32_bf16 v[180:183], a[92:95], v[92:95], v[180:183]\n\t" \
  "v_mov_b64 v[88:89], 0\n\t" \
  "v_mov_b64 v[90:91], 0\n\t" \
  "v_mfma_f32_16x16x32_bf16 v[168:171], a[96:99], v[96:99], v[168:171]\n\t" \
  "v_mov_b64 v[92:93], 0\n\t" \
  "v_mov_b64 v[94:95], 0\n\t" \
  "v_mfma_f32_16x16x32_bf16 v[172:175], a[104:107], v[96:99], v[172:175]\n\t" \
  "s_waitcnt vmcnt(4)\n\t" \
  "v_swap_b32 v105, v108\n\t" \
  "v_mfma_f32_16x16x32_bf16 v[168:171], a[100:103], v[100:103], v[168:171]\n\t" \
  "v_swap_b32 v107, v110\n\t" \
  "v_mfma_f32_16x16x32_bf16 v[172:175], a[108:111], v[100:103], v[172:175]\n\t" \
  "v_mov_b64 v[96:97], 0\n\t" \
  "v_mov_b64 v[98:99], 0\n\t" \
  "v_mfma_f32_16x16x32_bf16 v[176:179], a[96:99], v[104:107], v[176:179]\n\t" \
  "v_mov_b64 v[100:101], 0\n\t" \
  "v_mov_b64 v[102:103], 0\n\t" \
  "v_mfma_f32_16x16x32_bf16 v[180:183], a[104:107], v[104:107], v[180:183]\n\t" \
  "s_waitcnt vmcnt(2)\n\t" \
  "v_swap_b32 v113, v116\n\t" \
  "v_mfma_f32_16x16x32_bf16 v[176:179], a[100:103], v[108:111], v[176:179]\n\t" \
  "v_swap_b32 v115, v118\n\t" \
  "v_mfma_f32_16x16x32_bf16 v[180:183], a[108:111], v[108:111], v[180:183]\n\t" \
  "s_nop 0\n\t" \
  "s_add_i32 s85, s40, 1\n\t" \
  "s_and_b32 s82, s85, 2\n\t" \
  "s_lshl_b32 s82, s82, 13\n\t" \
  "s_and_b32 s80, s85, 1\n\t" \
  "s_lshl_b32 s80, s80, 12\n\t" \
  "s_add_i32 s82, s82, s80\n\t" \
  "s_add_i32 s82, s82, 0x84000\n\t" \
  "s_mov_b32 exec_hi, 0\n\t" \
  "buffer_load_dword v184, v200, %[rh], s82 offen sc1\n\t" \
  "buffer_load_dword v185, v201, %[rh], s52 offen sc1\n\t" \
  "s_mov_b64 exec, -1\n\t" \
  "v_mfma_f32_16x16x32_bf16 v[168:171], a[112:115], v[112:115], v[168:171]\n\t" \
  "v_mfma_f32_16x16x32_bf16 v[172:175], a[120:123], v[112:115], v[172:175]\n\t" \
  "s_waitcnt vmcnt(2)\n\t" \
  "v_swap_b32 v121, v124\n\t" \
  "v_mfma_f32_16x16x32_bf16 v[168:171], a[116:119], v[116:119], v[168:171]\n\t" \
  "v_swap_b32 v123, v126\n\t" \
  "v_mfma_f32_16x16x32_bf16 v[172:175], a[124:127], v[116:119], v[172:175]\n\t" \
  "s_nop 0\n\t" \
  "v_mfma_f32_16x16x32_bf16 v[176:179], a[112:115], v[120:123], v[176:179]\n\t" \
  "v_mfma_f32_16x16x32_bf16 v[180:183], a[120:123], v[120:123], v[180:183]\n\t" \
  "v_mfma_f32_16x16x32_bf16 v[176:179], a[116:119], v[124:127], v[176:179]\n\t" \
  "v_mfma_f32_16x16x32_bf16 v[180:183], a[124:127], v[124:127], v[180:183]\n\t" \
  "s_nop 6\n\t" \
  "39:\n\t" \
  "s_add_i32 s40, s40, 2\n\t" \
  "v_cmp_u_f32 vcc, v168, v176\n\t" \
  "s_cbranch_vccnz 32f\n\t" \
  "33:\n\t" \
  "s_barrier\n\t" \
  "s_add_i32 s85, s40, -1\n\t" \
  "s_cmp_ge_i32 s40, 64\n\t" \
  "s_cbranch_scc1 47f\n\t" \
  "47:\n\t" \
  "ds_write_b128 v199, v[168:171] offset:0\n\t" \
  "ds_write_b128 v199, v[172:175] offset:1024\n\t" \
  "ds_write_b128 v199, v[176:179] offset:8192\n\t" \
  "ds_write_b128 v199, v[180:183] offset:9216\n\t" \
  "s_mov_b32 s59, 0\n\t" \
  "s_and_b32 s58, s58, 4\n\t" \
  "s_cmp_lt_i32 s40, 64\n\t" \
  "s_cbranch_scc1 20b\n\t" \
  "s_waitcnt lgkmcnt(0)\n\t" \
  "s_barrier\n\t" \
  "s_mov_b32 s58, 64\n\t" \
  "s_branch 7f\n\t" \
  "22:\n\t" \
  "s_mov_b32 s81, 0\n\t" \
  "s_mov_b32 s54, s46\n\t" \
  "s_cmp_eq_u32 s40, 0\n\t" \
  "s_cbranch_scc1 24b\n\t" \
  "s_bitcmp1_b32 s58, 2\n\t" \
  "s_cbranch_scc1 24b\n\t" \
  "s_mov_b64 exec, -1\n\t" \
  "s_sleep 10\n\t" \
  "s_add_u32 s59, s59, 0x10000\n\t" \
  "s_cmp_lt_u32 s59, 0x400000\n\t" \
  "s_cbranch_scc1 19b\n\t" \
  "s_or_b32 s58, s58, 1\n\t" \
  "s_branch 7f\n\t" \
  "14:\n\t" \
  "v_mov_b32 v184, 0\n\t" \
  "s_bitcmp1_b32 s58, 2\n\t" \
  "s_nop 0\n\t" \
  "s_cbranch_scc1 15b\n\t" \
  "s_mov_b32 s84, 910\n\t" \
  "s_add_u32 s59, s59, 0x10000\n\t" \
  "s_cmp_lt_u32 s59, 0x400000\n\t" \
  "s_mov_b64 exec, vcc\n\t" \
  "buffer_load_dword v187, v200, %[rh], s78 offen sc1\n\t" \
  "s_mov_b32 exec_lo, -1\n\t" \
  "s_cbranch_scc1 13b\n\t" \
  "s_mov_b64 exec, -1\n\t" \
  "s_or_b32 s58, s58, 1\n\t" \
  "s_branch 7f\n\t" \
  "37:\n\t" \
  "s_barrier\n\t" \
  "s_barrier\n\t" \
  "s_add_i32 s40, s40, 2\n\t" \
  "s_and_b32 s58, s58, 4\n\t" \
  "s_branch 17b\n\t" \
  "32:\n\t" \
  "s_and_b32 s80, s58, 36\n\t" \
  "s_cbranch_scc1 33b\n\t" \
  "s_add_i32 s40, s40, -2\n\t" \
  "34:\n\t" \
  "s_or_b32 s59, s59, 128\n\t" \
  "s_and_b32 s76, s40, 3\n\t" \
  "s_lshl_b32 s76, s76, 18\n\t" \
  "s_add_i32 s77, s76, 0x10000\n\t" \
  "s_add_i32 s78, s76, 0x1000\n\t" \
  "s_add_i32 s79, s76, 0x11000\n\t" \
  "35:\n\t" \
  "buffer_load_dwordx4 v[0:3], v210, %[rh], s76 offen offset:0 sc1\n\t" \
  "buffer_load_dwordx4 v[4:7], v210, %[rh], s76 offen offset:1024 sc1\n\t" \
  "buffer_load_dwordx4 v[8:11], v210, %[rh], s77 offen offset:0 sc1\n\t" \
  "buffer_load_dwordx4 v[12:15], v210, %[rh], s77 offen offset:1024 sc1\n\t" \
  "buffer_load_dwordx4 v[16:19], v210, %[rh], s76 offen offset:2048 sc1\n\t" \
  "buffer_load_dwordx4 v[20:23], v210, %[rh], s76 offen offset:3072 sc1\n\t" \
  "buffer_load_dwordx4 v[24:27], v210, %[rh], s77 offen offset:2048 sc1\n\t" \
  "buffer_load_dwordx4 v[28:31], v210, %[rh], s77 offen offset:3072 sc1\n\t" \
  "buffer_load_dwordx4 v[32:35], v210, %[rh], s78 offen offset:0 sc1\n\t" \
  "buffer_load_dwordx4 v[36:39], v210, %[rh], s78 offen offset:1024 sc1\n\t" \
  "buffer_load_dwordx4 v[40:43], v210, %[rh], s79 offen offset:0 sc1\n\t" \
  "buffer_load_dwordx4 v[44:47], v210, %[rh], s79 offen offset:1024 sc1\n\t" \
  "buffer_load_dwordx4 v[48:51], v210, %[rh], s78 offen offset:2048 sc1\n\t" \
  "buffer_load_dwordx4 v[52:55], v210, %[rh], s78 offen offset:3072 sc1\n\t" \
  "buffer_load_dwordx4 v[56:59], v210, %[rh], s79 offen offset:2048 sc1\n\t" \
  "buffer_load_dwordx4 v[60:63], v210, %[rh], s79 offen offset:3072 sc1\n\t" \
  "buffer_load_dwordx4 v[64:67], v211, %[rh], s76 offen offset:0 sc1\n\t" \
  "buffer_load_dwordx4 v[68:71], v211, %[rh], s76 offen offset:1024 sc1\n\t" \
  "buffer_load_dwordx4 v[72:75], v211, %[rh], s77 offen offset:0 sc1\n\t" \
  "buffer_load_dwordx4 v[76:79], v211, %[rh], s77 offen offset:1024 sc1\n\t" \
  "buffer_load_dwordx4 v[80:83], v211, %[rh], s76 offen offset:2048 sc1\n\t" \
  "buffer_load_dwordx4 v[84:87], v211, %[rh], s76 offen offset:3072 sc1\n\t" \
  "buffer_load_dwordx4 v[88:91], v211, %[rh], s77 offen offset:2048 sc1\n\t" \
  "buffer_load_dwordx4 v[92:95], v211, %[rh], s77 offen offset:3072 sc1\n\t" \
  "buffer_load_dwordx4 v[96:99], v211, %[rh], s78 offen offset:0 sc1\n\t" \
  "buffer_load_dwordx4 v[100:103], v211, %[rh], s78 offen offset:1024 sc1\n\t" \
  "buffer_load_dwordx4 v[104:107], v211, %[rh], s79 offen offset:0 sc1\n\t" \
  "buffer_load_dwordx4 v[108:111], v211, %[rh], s79 offen offset:1024 sc1\n\t" \
  "buffer_load_dwordx4 v[112:115], v211, %[rh], s78 offen offset:2048 sc1\n\t" \
  "buffer_load_dwordx4 v[116:119], v211, %[rh], s78 offen offset:3072 sc1\n\t" \
  "buffer_load_dwordx4 v[120:123], v211, %[rh], s79 offen offset:2048 sc1\n\t" \
  "buffer_load_dwordx4 v[124:127], v211, %[rh], s79 offen offset:3072 sc1\n\t" \
  "s_waitcnt vmcnt(0)\n\t" \
  "s_bitcmp1_b32 s58, 2\n\t" \
  "s_cbranch_scc1 36f\n\t" \
  "v_max_u32 v148, v0, v1\n\t" \
  "v_max3_u32 v148, v148, v2, v3\n\t" \
  "v_max3_u32 v148, v148, v4, v5\n\t" \
  "v_max3_u32 v148, v148, v6, v7\n\t" \
  "v_max3_u32 v148, v148, v8, v9\n\t" \
  "v_max3_u32 v148, v148, v10, v11\n\t" \
  "v_max3_u32 v148, v148, v12, v13\n\t" \
  "v_max3_u32 v148, v148, v14, v15\n\t" \
  "v_max3_u32 v148, v148, v16, v17\n\t" \
  "v_max3_u32 v148, v148, v18, v19\n\t" \
  "v_max3_u32 v148, v148, v20, v21\n\t" \
  "v_max3_u32 v148, v148, v22, v23\n\t" \
  "v_max3_u32 v148, v148, v24, v25\n\t" \
  "v_max3_u32 v148, v148, v26, v27\n\t" \
  "v_max3_u32 v148, v148, v28, v29\n\t" \
  "v_max3_u32 v148, v148, v30, v31\n\t" \
  "v_max3_u32 v148, v148, v32, v33\n\t" \
  "v_max3_u32 v148, v148, v34, v35\n\t" \
  "v_max3_u32 v148, v148, v36, v37\n\t" \
  "v_max3_u32 v148, v148, v38, v39\n\t" \
  "v_max3_u32 v148, v148, v40, v41\n\t" \
  "v_max3_u32 v148, v148, v42, v43\n\t" \
  "v_max3_u32 v148, v148, v44, v45\n\t" \
  "v_max3_u32 v148, v148, v46, v47\n\t" \
  "v_max3_u32 v148, v148, v48, v49\n\t" \
  "v_max3_u32 v148, v148, v50, v51\n\t" \
  "v_max3_u32 v148, v148, v52, v53\n\t" \
  "v_max3_u32 v148, v148, v54, v55\n\t" \
  "v_max3_u32 v148, v148, v56, v57\n\t" \
  "v_max3_u32 v148, v148, v58, v59\n\t" \
  "v_max3_u32 v148, v148, v60, v61\n\t" \
  "v_max3_u32 v148, v148, v62, v63\n\t" \
  "v_max3_u32 v148, v148, v64, v65\n\t" \
  "v_max3_u32 v148, v148, v66, v67\n\t" \
  "v_max3_u32 v148, v148, v68, v69\n\t" \
  "v_max3_u32 v148, v148, v70, v71\n\t" \
  "v_max3_u32 v148, v148, v72, v73\n\t" \
  "v_max3_u32 v148, v148, v74, v75\n\t" \
  "v_max3_u32 v148, v148, v76, v77\n\t" \
  "v_max3_u32 v148, v148, v78, v79\n\t" \
  "v_max3_u32 v148, v148, v80, v81\n\t" \
  "v_max3_u32 v148, v148, v82, v83\n\t" \
  "v_max3_u32 v148, v148, v84, v85\n\t" \
  "v_max3_u32 v148, v148, v86, v87\n\t" \
  "v_max3_u32 v148, v148, v88, v89\n\t" \
  "v_max3_u32 v148, v148, v90, v91\n\t" \
  "v_max3_u32 v148, v148, v92, v93\n\t" \
  "v_max3_u32 v148, v148, v94, v95\n\t" \
  "v_max3_u32 v148, v148, v96, v97\n\t" \
  "v_max3_u32 v148, v148, v98, v99\n\t" \
  "v_max3_u32 v148, v148, v100, v101\n\t" \
  "v_max3_u32 v148, v148, v102, v103\n\t" \
  "v_max3_u32 v148, v148, v104, v105\n\t" \
  "v_max3_u32 v148, v148, v106, v107\n\t" \
  "v_max3_u32 v148, v148, v108, v109\n\t" \
  "v_max3_u32 v148, v148, v110, v111\n\t" \
  "v_max3_u32 v148, v148, v112, v113\n\t" \
  "v_max3_u32 v148, v148, v114, v115\n\t" \
  "v_max3_u32 v148, v148, v116, v117\n\t" \
  "v_max3_u32 v148, v148, v118, v119\n\t" \
  "v_max3_u32 v148, v148, v120, v121\n\t" \
  "v_max3_u32 v148, v148, v122, v123\n\t" \
  "v_max3_u32 v148, v148, v124, v125\n\t" \
  "v_max3_u32 v148, v148, v126, v127\n\t" \
  "v_cmp_eq_u32 vcc, -1, v148\n\t" \
  "s_cbranch_vccz 36f\n\t" \
  "s_add_u32 s59, s59, 0x1000000\n\t" \
  "s_cmp_lt_u32 s59, 0x10000000\n\t" \
  "s_cbranch_scc1 35b\n\t" \
  "s_or_b32 s58, s58, 9\n\t" \
  "s_branch 7f\n\t" \
  "36:\n\t" \
  "s_mov_b32 s97, 0\n\t" \
  "s_and_b32 s58, s58, 4\n\t" \
  "s_or_b32 s58, s58, 32\n\t" \
  "s_branch 31b\n\t" \
  "18:\n\t" \
  "v_mov_b32 v186, -1\n\t" \
  "s_and_b32 s80, s40, 3\n\t" \
  "s_mul_i32 s77, s80, 0xc000\n\t" \
  "s_add_i32 s77, s77, 0x40000\n\t" \
  "s_lshl_b32 s78, s80, 14\n\t" \
  "s_add_i32 s78, s78, 0x70000\n\t" \
  "s_mov_b32 s57, 0\n\t" \
  "s_add_i32 s96, s77, 0x1000\n\t" \
  "s_and_b32 s98, s40, 2\n\t" \
  "s_lshl_b32 s98, s98, 13\n\t" \
  "s_and_b32 s80, s40, 1\n\t" \
  "s_lshl_b32 s80, s80, 12\n\t" \
  "s_add_i32 s98, s98, s80\n\t" \
  "s_add_i32 s98, s98, 0x84000\n\t" \
  "12:\n\t" \
  "s_or_b32 s58, s58, 3\n\t" \
  "11:\n\t" \
  "buffer_load_dwordx4 v[0:3], v193, %[rh], s77 offen offset:0 sc1\n\t" \
  "buffer_load_dwordx4 v[4:7], v193, %[rh], s77 offen offset:1024 sc1\n\t" \
  "buffer_load_dwordx4 v[8:11], v193, %[rh], s77 offen offset:2048 sc1\n\t" \
  "buffer_load_dwordx4 v[12:15], v193, %[rh], s77 offen offset:3072 sc1\n\t" \
  "buffer_load_dwordx4 v[16:19], v193, %[rh], s96 offen offset:0 sc1\n\t" \
  "buffer_load_dwordx4 v[20:23], v193, %[rh], s96 offen offset:1024 sc1\n\t" \
  "buffer_load_dwordx4 v[24:27], v193, %[rh], s96 offen offset:2048 sc1\n\t" \
  "buffer_load_dwordx4 v[28:31], v193, %[rh], s96 offen offset:3072 sc1\n\t" \
  "buffer_load_dwordx4 v[32:35], v195, %[rh], s77 offen offset:0 sc1\n\t" \
  "buffer_load_dwordx4 v[36:39], v195, %[rh], s77 offen offset:2048 sc1\n\t" \
  "v_mov_b64 v[40:41], 0\n\t" \
  "v_mov_b64 v[42:43], 0\n\t" \
  "v_mov_b64 v[44:45], 0\n\t" \
  "v_mov_b64 v[46:47], 0\n\t" \
  "v_mov_b64 v[48:49], 0\n\t" \
  "v_mov_b64 v[50:51], 0\n\t" \
  "v_mov_b64 v[52:53], 0\n\t" \
  "v_mov_b64 v[54:55], 0\n\t" \
  "v_mov_b64 v[56:57], 0\n\t" \
  "v_mov_b64 v[58:59], 0\n\t" \
  "v_mov_b64 v[60:61], 0\n\t" \
  "v_mov_b64 v[62:63], 0\n\t" \
  "v_mov_b64 v[64:65], 0\n\t" \
  "v_mov_b64 v[66:67], 0\n\t" \
  "v_mov_b64 v[68:69], 0\n\t" \
  "v_mov_b64 v[70:71], 0\n\t" \
  "v_mov_b64 v[72:73], 0\n\t" \
  "v_mov_b64 v[74:75], 0\n\t" \
  "v_mov_b64 v[76:77], 0\n\t" \
  "v_mov_b64 v[78:79], 0\n\t" \
  "v_mov_b64 v[80:81], 0\n\t" \
  "v_mov_b64 v[82:83], 0\n\t" \
  "v_mov_b64 v[84:85], 0\n\t" \
  "v_mov_b64 v[86:87], 0\n\t" \
  "v_mov_b64 v[88:89], 0\n\t" \
  "v_mov_b64 v[90:91], 0\n\t" \
  "v_mov_b64 v[92:93], 0\n\t" \
  "v_mov_b64 v[94:95], 0\n\t" \
  "v_mov_b64 v[96:97], 0\n\t" \
  "v_mov_b64 v[98:99], 0\n\t" \
  "v_mov_b64 v[100:101], 0\n\t" \
  "v_mov_b64 v[102:103], 0\n\t" \
  "v_mov_b64 v[136:137], 0\n\t" \
  "v_mov_b64 v[138:139], 0\n\t" \
  "v_mov_b64 v[140:141], 0\n\t" \
  "v_mov_b64 v[142:143], 0\n\t" \
  "s_waitcnt vmcnt(0)\n\t" \
  "s_add_u32 s59, s59, 1\n\t" \
  "s_bitcmp1_b32 s58, 2\n\t" \
  "s_cbranch_scc1 10b\n\t" \
  "v_max_u32 v148, v0, v1\n\t" \
  "v_max3_u32 v148, v148, v2, v3\n\t" \
  "v_max3_u32 v148, v148, v4, v5\n\t" \
  "v_max3_u32 v148, v148, v6, v7\n\t" \
  "v_max3_u32 v148, v148, v8, v9\n\t" \
  "v_max3_u32 v148, v148, v10, v11\n\t" \
  "v_max3_u32 v148, v148, v12, v13\n\t" \
  "v_max3_u32 v148, v148, v14, v15\n\t" \
  "v_max3_u32 v148, v148, v16, v17\n\t" \
  "v_max3_u32 v148, v148, v18, v19\n\t" \
  "v_max3_u32 v148, v148, v20, v21\n\t" \
  "v_max3_u32 v148, v148, v22, v23\n\t" \
  "v_max3_u32 v148, v148, v24, v25\n\t" \
  "v_max3_u32 v148, v148, v26, v27\n\t" \
  "v_max3_u32 v148, v148, v28, v29\n\t" \
  "v_max3_u32 v148, v148, v30, v31\n\t" \
  "v_max3_u32 v148, v148, v32, v33\n\t" \
  "v_max3_u32 v148, v148, v34, v35\n\t" \
  "v_max3_u32 v148, v148, v36, v37\n\t" \
  "v_max3_u32 v148, v148, v38, v39\n\t" \
  "v_cmp_eq_u32 vcc, -1, v148\n\t" \
  "s_cbranch_vccz 10b\n\t" \
  "s_bitcmp1_b32 s59, 6\n\t" \
  "s_cbranch_scc0 11b\n\t" \
  "7:\n\t" \
  "92:\n\t" \
  "s_mov_b32 s42, s58\n\t" \
  "s_waitcnt vmcnt(0) lgkmcnt(0)\n\t" \
  ""

#define V53 "v0", "v1", "v2", "v3", "v4", "v5", "v6", "v7", "v8", "v9", "v10", "v11", "v12", "v13", "v14", "v15", "v16", "v17", \
  "v18", "v19", "v20", "v21", "v22", "v23", "v24", "v25", "v26", "v27", "v28", "v29", "v30", "v31", "v32", "v33", "v34", "v35", \
  "v36", "v37", "v38", "v39", "v40", "v41", "v42", "v43", "v44", "v45", "v46", "v47", "v48", "v49", "v50", "v51", "v52"
#define V100 V53, "v53", "v54", "v55", "v56", "v57", "v58", "v59", "v60", "v61", "v62", "v63", "v64", "v65", "v66", "v67", "v68", "v69", "v70", \
  "v71", "v72", "v73", "v74", "v75", "v76", "v77", "v78", "v79", "v80", "v81", "v82", "v83", "v84", "v85", "v86", "v87", "v88", \
  "v89", "v90", "v91", "v116", "v117", "v118", "v119"
#define STEP_PV
#define CB_CL "s60", "s61", "s62", "s63", "s64", "s65", "s66", "s67", "s68", "s69",
#define STEPS(BODY, ...) asm volatile(BODY : "=&{v[92:99]}"(X), "+{s[40:55]}"(P) STEP_PV \
  : "{v[120:151]}"(W.v[0]), "{v[152:183]}"(W.v[1]), "{v[184:215]}"(W.v[2]), "{v[216:247]}"(W.v[3]), "{v[100:115]}"(C), [rh] "s"(rh), [wv] "s"(w), [ru] "s"(rum), [sel] "s"(sel) __VA_ARGS__ \
  : "memory", "vcc", "scc", "s56", "s57", "s58", "s59", "s76", "s77", "s78", "s79", "s80", "s81", "s82", "s83", "s84", "s85", "s86", "s87", "s88", "s89", \
    "s90", "s91", "s92", "s93", "s94", "s95", "s96", "s97", V100)
__device__ __forceinline__ void keep_a(const Wts& W) {
#pragma unroll
  for (int i = 0; i < 32; ++i) asm volatile("" :: "a"(W.a[i]));
}
__device__ __forceinline__ char* lds_base(char* smem, int off) { asm volatile("" : "+v"(off)); return smem + off; }
__device__ __forceinline__ u32x2 tr4(unsigned p) { return __builtin_bit_cast(u32x2, __builtin_amdgcn_ds_read_tr16_b64_v4i16((LAS bf16x4*)(uintptr_t)p)); }
__device__ __forceinline__ float rb1(float x) { return __uint_as_float(pk2(0.f, x)); }

__device__ __forceinline__ void steps_init(u32x16& P, int h, int w, unsigned place) {
#pragma unroll
  for (int i = 0; i < 16; ++i) P[i] = 0;
  P[2] = 1; P[3] = CDUR; P[14] = H_MEM + (w >> 1) * CHK + (w & 1) * 4096 + (h & RHO) * 1024; P[15] = place;
}
__device__ __forceinline__ void steps_enter(u32x16& P, Clk& ck, bool dead) {
  ck.sync();
  P[2] = (P[2] & 3u) | (dead ? 4u : 0u); P[6] = ck.c2t << 15; P[11] = ck.ce; P[12] = ck.t2c; P[13] = ck.te;
#pragma unroll
  for (int i = 0; i < 16; ++i) P[i] = __builtin_amdgcn_readfirstlane(P[i]);
}

__device__ __forceinline__ void role_qk(const ChainArgs& a, int h, char* smem, const int w, Gate& G) {
  int lane = opaque_tid(0);
  const int b = h; (void)b;
  u64* prof = a.prof; (void)prof;
  PDECL(L_PRF, 4);
  const float CS = 0.08838834764831845f * 1.4426950408889634f;
  GAS unsigned* abortw = (GAS unsigned*)(a.sync + SY_ABORT);
  bool dead = false;
  Wts W;
  Clk ck; ck.init();
  u32x4 gv;
  if (QK_D) while (__builtin_amdgcn_s_memrealtime() - G.entry < QK_D) __builtin_amdgcn_s_sleep(8);
  load_weights<0>(a, h, w, lane, smem, W, G, gv);
  TP(1);
  if (!G.look(a.sync, gv)) return;
  PZERO;
  const Sel sel = G.sel;
  const __amdgpu_buffer_rsrc_t rh = rsrc_hot(a.sync, sel), rum = rsrc_u(a.sync);
  auto pm_o = [&](int l) { return 2 * CHK + h * 256 + (l & 1) * 128 + (l >> 3) * 16 + (l & 6) * 2; };
  auto fac = [&](int l, int hf) { return ((l & 7) >> 1) == 2 * (l >> 5) + ((h >> 1) & 1) && ((l >> 3) & 1) == hf ? 1.f : 0.f; };
  u32x16 C, P;
  {
    const int r = lane & 15, g = lane >> 4;
    const unsigned l0 = (unsigned)(uintptr_t)(LAS char*)smem;
    C[0] = lane * 16; C[1] = l0 + L_MEM + lane * 16; C[2] = l0 + L_WTS + w * 4096 + lane * 16; C[3] = C[2] + 65536;
    C[4] = (lane & 2) ? 0xEEEE : 0x4444;
    C[5] = l0 + L_XCH + (16 * w + 2 * (r >> 2) + (r & 1)) * 32 + g * 8 + ((r >> 1) & 1) * 4;
    C[6] = l0 + L_XK + (32 * w + r) * 16 + g * 4;
    C[7] = w == 3 ? H_PX + (h >> 3) * CHK + (h & 7) * 1024 + lane * 16 : w ? (2 * h + 1) * CHK + (w - 1) * 4096 + lane * 16 : l0 + L_XCH + (8 * g + (r >> 2)) * 32 + (r & 3) * 8;
    C[8] = l0 + L_XK + (8 * g + (r >> 2)) * 16 + (r & 1) * 8;
    C[9] = w ? (lane & 1) * 4096 : (lane == 0 ? H_ANN + h * 128 : 0x7fffffffu);
    C[10] = w == 0 ? (h >> 4) * CHK + (h & 15) * 512 + ((lane >> 1) & 1) * 256 + (lane >> 5) * 128 + (lane & 1) * 64 + ((lane >> 3) & 3) * 16 + ((lane >> 2) & 1) * 8 :
            w < 3 ? H_PX + ((w - 1) * 4 + (h >> 3)) * CHK + (h & 7) * 1024 + lane * 16 :
            lane < 32 ? (h >> 4) * CHK + (h & 15) * 512 + lane * 16 : (lane < 48 ? 2 * CHK + h * 256 + (lane - 32) * 16 : 0x7fffffffu);
    C[11] = w == 0 ? pm_o(lane) : (lane < (w == 3 ? 32 : 16) ? H_PX + (w == 3 ? 8 + g : 7 + w) * CHK + hpos(h) * 256 + r * 16 : 0x7fffffffu);
    C[12] = H_TR + (8 * w + (lane & 7)) * 128; C[13] = l0 + L_PRF + w * 1024 + (lane >> 6);
    unsigned mk = 0;
#pragma unroll
    for (int i = 0; i < 8; ++i) mk |= (16 * ((i >> 1) & 1) + 8 * (g & 1) + 4 * (g >> 1) + 2 * (i & 1) + (i >> 2) > 16 * (w - 1) + r ? 1u : 0u) << i;
    C[14] = w ? mk : __float_as_uint(fac(lane, 0)); C[15] = __float_as_uint(fac(lane, 1));
    asm volatile("" : "+v"(C));
  }
  steps_init(P, h, w, 2 * h * CHK + max(3 * w - 4, 0) * 1024);
  u32x4 k0[2];
  start_wait(a.sync, abortw, lane, dead, CT_Q + ct_near(sel) + h, 1u, [&]() { for (int i = 0; i < 2; ++i) k0[i] = ld16(rum, x_slot(1, 0, h, sel) + (2 * w + i) * 1024 + lane * 16); });
  G.arrive(a.sync);
#pragma unroll
  for (int i = 0; i < 2; ++i) *(u32x4*)(smem + L_KX + (2 * w + i) * 1024 + lane * 16) = k0[i];
  SSTAMP(96 + h);
  TP(2);
  GAS unsigned* kcp = ctr(a, CT_Q + ct_near(sel) + h);
  unsigned spins = 0;
  while (true) {
    u32x8 X; u32x16 pv; (void)pv;
    steps_enter(P, ck, dead);
    STEPS(QK_BODY, , [cs] "s"(CS), [kc] "s"(kcp));
    keep_a(W);
    const unsigned res = __builtin_amdgcn_readfirstlane(P[2]), s = __builtin_amdgcn_readfirstlane(P[4]);
    lane = opaque_tid(0);
    if (res & 8u) {
      auto sw = [](float v, int k) { return __int_as_float(k == 0 ? __builtin_amdgcn_mov_dpp(__float_as_int(v), 0xB1, 0xf, 0xf, false) :
                                                           k == 1 ? __builtin_amdgcn_mov_dpp(__float_as_int(v), 0x4E, 0xf, 0xf, false) :
                                                                    __builtin_amdgcn_mov_dpp(__float_as_int(v), 0x141, 0xf, 0xf, false)); };
      float x[5], d = fmaxf(__uint_as_float(X[4]), 0.f);
      d = fmaxf(d, sw(d, 0)); d = fmaxf(d, sw(d, 1)); d = fmaxf(d, sw(d, 2));
#pragma unroll
      for (int i = 0; i < 5; ++i) x[i] = __builtin_amdgcn_exp2f(__uint_as_float(X[i]) - d);
      float z = ((x[0] + x[1]) + (x[2] + x[3])) + x[4];
      z += sw(z, 0); z += sw(z, 1); z += sw(z, 2);
      z = __builtin_amdgcn_rcpf(z);
      const float y = sw(x[4], 0), f = __builtin_amdgcn_exp2f(-d) * z;
      const unsigned pm = (lane & 1) ? pk2(fac(lane, 0) * f, fac(lane, 1) * f) : pk2(x[4] * z, y * z);
      if (!(__uint_as_float(X[5]) < 1.0e18f))
        __builtin_amdgcn_raw_buffer_store_b32(nosent(pm), rh, pm_o(lane), H_P + ((s - 1) & 3) * 3 * CHK, ST_AUX);
    }
    if (res & 64u) break;
    if (res & 32u) {
      u32x4 k[3];
      start_wait(a.sync, abortw, lane, dead, CT_Q + ct_near(sel) + h, s + 1, [&]() { for (int i = 0; i < 3; ++i) k[i] = ld16(rum, x_slot(1, s, h, sel) + max(3 * w - 4 + i, 0) * 1024 + lane * 16); });
#pragma unroll
      for (int i = 0; i < 3; ++i) *(u32x4*)(smem + L_KX + max(3 * w - 4 + i, 0) * 1024 + lane * 16) = k[i];
    }
    if (res & 17u) {
      spins |= 63u;
      if (dead || poll_fail(spins, abortw, lane)) dead = true;
    }
  }
  PDUMP(0);
}

__device__ __forceinline__ void role_vo(const ChainArgs& a, int h, char* smem, const int w, Gate& G) {
  int lane = opaque_tid(0);
  const int b = 32 + h; (void)b;
  u64* prof = a.prof; (void)prof;
  PDECL(L_PRF, 4);
  GAS unsigned* abortw = (GAS unsigned*)(a.sync + SY_ABORT);
  bool dead = false;
  Wts W;
  Clk ck; ck.init();
  u32x4 gv;
  load_weights<1>(a, h, w, lane, smem, W, G, gv);
  if (!G.look(a.sync, gv)) return;
  PZERO;
  const Sel sel = G.sel;
  const __amdgpu_buffer_rsrc_t rh = rsrc_hot(a.sync, sel), rum = rsrc_u(a.sync);
  u32x16 C, P;
  {
    const int r = lane & 15, g = lane >> 4;
    const unsigned l0 = (unsigned)(uintptr_t)(LAS char*)smem;
#pragma unroll
    for (int i = 0; i < 16; ++i) C[i] = H_TR + (8 * w + (lane & 7)) * 128;
    C[0] = lane * 16; C[1] = l0 + L_MEM + lane * 16; C[2] = l0 + L_WTS + w * 4096 + lane * 16; C[3] = C[2] + 65536;
    C[4] = (lane & 2) ? 0xEEEE : 0x4444;
    C[5] = l0 + L_XCH + (32 * w + r) * 16 + g * 4;
    C[6] = (lane & 8) ? 0xEEEE : 0x4444;
    C[7] = l0 + L_XCH + (32 * (g >> 1) + 8 * (g & 1) + (r >> 2)) * 16 + (r & 1) * 8;
    C[8] = ((g & 1) * 8 + (g >> 1)) * CHK + r * 16;
    C[13] = l0 + L_PRF + w * 1024 + (lane >> 6);
    asm volatile("" : "+v"(C));
  }
  start_wait(a.sync, abortw, lane, dead, -1, 0u, [] {});
  G.arrive(a.sync);
  SSTAMP(128 + h);
  steps_init(P, h, w, SY_UM + (16 * (h & 3) + 2 * w) * CHK + hpos(h) * 256);
  unsigned spins = 0;
  while (true) {
    u32x8 X; u32x16 pv; (void)pv;
    steps_enter(P, ck, dead);
    STEPS(VO_BODY);
    keep_a(W);
    lane = opaque_tid(0);
    if (__builtin_amdgcn_readfirstlane(P[2]) & 64u) break;
    spins |= 63u;
    if (dead || poll_fail(spins, abortw, lane)) dead = true;
  }
  PDUMP(0);
}

__device__ __forceinline__ void calibrate(const ChainArgs& a, int cal, int slot, u64 entry) {
  const u32x4 rs = own_rsrc(a.sync, 64 * PAGE);
  const unsigned so = cal * PAGE + (slot >> 1) * CHK + (slot & 1) * 4096;
  unsigned best[2] = {1u << 30, 1u << 30};
  bool sure;
  {
    unsigned z = 0;
    asm volatile("s_mov_b64 exec, 0xffff\n\tbuffer_atomic_or %0, %1, %2, %3 offen sc0 sc1\n\ts_mov_b64 exec, -1\n\ts_waitcnt vmcnt(0)"
                 : "+v"(z) : "v"((opaque_tid(0) & 8) * 1024 + (opaque_tid(0) & 7) * 256), "s"(rs), "s"(so) : "memory");
  }
  for (int i = 0;; ++i) {
    sure = min(best[0], best[1]) < CALT && max(best[0], best[1]) - min(best[0], best[1]) > 110u;
    if (i >= CALN && (sure || __builtin_amdgcn_s_memrealtime() - entry > CALM)) break;
#pragma unroll
    for (int hf = 0; hf < 2; ++hf) {
      u64 c0, c1;
      unsigned z = 0;
      asm volatile("s_mov_b64 exec, 1\n\ts_memtime %0\n\ts_waitcnt lgkmcnt(0)\n\tbuffer_atomic_or %2, off, %3, %4 sc0 sc1\n\ts_waitcnt vmcnt(0)\n\ts_memtime %1\n\ts_mov_b64 exec, -1\n\ts_waitcnt lgkmcnt(0)"
                   : "=&s"(c0), "=&s"(c1), "+v"(z) : "s"(rs), "s"(so + hf * 8192 + (i & 7) * 256) : "memory");
      best[hf] = min(best[hf], (unsigned)(c1 - c0));
    }
  }
  const u64 hs = (__builtin_amdgcn_s_memrealtime() & ~3ull) | (sure ? 2u : 0u) | ((best[1] < best[0] ? 1u : 0u) ^ die_half());
  if (opaque_tid(0) == 0) __hip_atomic_store((GAS u64*)ctr(a, CT_CAL + (slot >> 1)) + (slot & 1), hs, __ATOMIC_RELAXED, __HIP_MEMORY_SCOPE_AGENT);
}
__device__ __forceinline__ u32x4 pk8(f32x4 a, f32x4 b) { return (u32x4){pk2(a[0], a[1]), pk2(a[2], a[3]), pk2(b[0], b[1]), pk2(b[2], b[3])}; }
__device__ __forceinline__ unsigned pk2v(float a, float b) { unsigned r; asm volatile("v_cvt_pk_bf16_f32 %0, %1, %2" : "=v"(r) : "v"(a), "v"(b)); return r; }
__device__ __forceinline__ u32x4 pk8v(f32x4 a, f32x4 b) { asm volatile("s_nop 7"); return (u32x4){pk2v(a[0], a[1]), pk2v(a[2], a[3]), pk2v(b[0], b[1]), pk2v(b[2], b[3])}; }
__device__ __forceinline__ void st16g(u16* p, u32x4 v) { asm volatile("global_store_dwordx4 %0, %1, off " OUTP "\n\ts_nop 1" :: "v"(p), "v"(v) : "memory"); }
__device__ __forceinline__ void role_cb(const ChainArgs& a, int cbi, char* smem, const int w, Gate& G) {
  int lane = opaque_tid(0);
  const int b = 64 + cbi; (void)b;
  const int par = cbi >> 5, ci = cbi & 31, J = ci >> 3;
  u64* prof = a.prof; (void)prof;
  PDECL(65536, 8); PZERO;
  GAS unsigned* abortw = (GAS unsigned*)(a.sync + SY_ABORT);
  bool dead = false;
  const int tw = 2 * ci + (w & 1);
  const unsigned mo = (tw >> 5) * CHK + (tw & 31) * 256;
  Sel sel = 0;
  auto start = [&]() {
    lane = opaque_tid(0);
    SSTAMP(0 + ci);
    unsigned m0 = 0;
    if (par == 0 && w < 2) {
      const u16* m = a.mem0 + (2 * (lane >> 4) + ((lane >> 2) & 1)) * DM + 16 * tw + (lane & 8);
      m0 = m[lane & 3] | (unsigned)m[7 - (lane & 3)] << 16;
    }
    if (!G.look(a.sync, (u32x4){0u, 0u, 0u, 0u})) return false;
    sel = G.sel;
    SSTAMP(64 + ci);
    const __amdgpu_buffer_rsrc_t rh = rsrc_hot(a.sync, sel), rum = rsrc_u(a.sync);
    const unsigned s1v = ~(unsigned)(lane >> 6);
    const u32x4 sv = {s1v, s1v, s1v, s1v};
    auto um = [&](int k) {
#pragma unroll
      for (int i = 0; i < 4; ++i) st16(rum, near_half(SY_UM + (64 * k + tw) * CHK, sel) + (w >> 1) * 4096 + i * 1024 + lane * 16, sv);
    };
    um(par);
    const unsigned dv = cbi == H_DLT / CHK ? K_DEF * TU : s1v;
#pragma unroll
    for (int i = 0; i < 2; ++i) st16(rh, cbi >= 2 && cbi < 45 ? (cbi < 35 ? cbi * CHK : H_PX + (cbi - 35) * CHK) + w * 2048 + i * 1024 + lane * 16 : 0x7fffffffu, (u32x4){dv, s1v, s1v, s1v});
    if (par == 0 && w < 2) st4(rh, H_MEM + mo + lane * 4, nosent(m0));
    asm volatile("s_waitcnt vmcnt(0)" ::: "memory");
    lds_barrier();
    if (w == 0 && lane == 0) ctr_add(a, CT_INIT + J, 1u);
    SSTAMP(256 + ci);
    TP(6);
    G.arrive(a.sync);
    um(par + 2);
    {
      const __amdgpu_buffer_rsrc_t rx = mk_rsrc((void*)a.X, 4u << 20);
      u32x4 v[16];
#pragma unroll
      for (int i = 0; i < 16; ++i) v[i] = __builtin_amdgcn_raw_buffer_load_b128(rx, (unsigned)(cbi * 4096 + w * 1024 + lane * 16), (unsigned)(i * 262144), 0);
#pragma unroll
      for (int i = 0; i < 16; ++i) asm volatile("" :: "v"(v[i]));
    }
    SSTAMP(224 + ci);
    start_wait(a.sync, abortw, lane, dead, -1, 0u, [] {});
    return true;
  };
  if (!start()) return;
  Wts W;
#pragma unroll
  for (int i = 0; i < 32; ++i) asm volatile("" : "=a"(W.a[i]));
  u32x16 C, P; u32x8 C2;
  {
    const unsigned l0 = (unsigned)(uintptr_t)(LAS char*)smem, oor = 0x7fffffffu;
    const int r = lane & 7, t = (lane >> 3) & 1, g = lane >> 4, e = w & 1;
    const bool L = w < 2;
    C[0] = lane * 16;
    C[1] = L ? l0 + 32768 + e * 8192 + (lane & 2) * 512 + ((lane & 1) * 32 + (lane >> 2)) * 16 : e * CHK + t * 512 + (g & 1) * 256 + (r >> 2) * 128 + (g >> 1) * 64 + (r & 3) * 16;
    C[2] = L ? (4 * t + g) * 1024 + r * 16 : l0 + 81920 + e * 32768 + lane * 16;
    C[3] = L ? l0 + e * 1024 + lane * 8 : 2 * CHK + e * 4096 + (4 * (g >> 1) + 2 * t + (g & 1)) * 256 + 128 + r * 16;
    C[4] = ((16 * e + (lane >> 2)) * OUTW + DM + 32 * ci + 8 * (lane & 3)) * 2;
    C[5] = l0 + (L ? e * 8192 + lane * 16 : e * 512 + lane * 8); C[6] = l0 + 16384 + lane * 16; C[7] = l0 + 32768 + (L ? e * 8192 + 4096 : e * 2048) + lane * 16;
    C[8] = (lane & 31) * 128;
    C[9] = L ? (8 * (g & 1) + 4 * (g >> 1) + 2 * t + (r >> 2)) * 16 + 4 * (r < 4 ? r : 7 - r) : (8 * J + r) * 128;
    C[10] = (lane | e) ? oor : ci * 128;
    C[11] = L ? SY_CT + (CT_U + ct_near(sel) + 16 * e + (lane & 15)) * 128 : (t ? 0xEEEE : 0x4444); C[12] = L ? l0 + 81920 + e * 32768 + lane * 16 : ((lane & 2) ? 0xEEEE : 0x4444); C[13] = r < 4 ? 0x01000504u : 0x07060302u;
    C[14] = l0 + 49152 + 32 * e + (lane >> 6); C[15] = l0 + 49152 + (lane >> 6);
    C2[0] = L ? (lane < 16 ? lane * 16 : oor) : ux_tile(2 * ci) + lane * 16;
    C2[1] = lane == 0 && e && ci < 8 ? SY_CT + (CT_DONE + ct_near(~sel) + 8 * par + ci) * 128 : oor;
    C2[2] = (L ? ux_tile(2 * ci) : H_PX + 2 * e * CHK) + lane * 16;
#pragma unroll
    for (int n = 0; n < 4; ++n) C2[3 + n] = L ? H_PX + (8 + e) * CHK + (J ^ n) * 2048 + lane * 16 : C2[2] + CHK;
    C2[7] = l0 + 65536 + w * 2048 - 64 + (lane >> 6);
    asm volatile("" : "+v"(C), "+v"(C2));
#pragma unroll
    for (int i = 0; i < 16; ++i) P[i] = par - 2;
    P[2] = 48; P[6] = P[7] = K_DEF * TU; P[8] = SY_UM + tw * CHK + J * 2048; P[9] = H_MEM + mo; P[10] = 2 * CHK + J * 256; P[11] = e * 16 * UXH; P[13] = 1780; P[12] = H_DLT + par * 4096;
  }
  Clk ck; ck.init();
  unsigned spins = 0;
  while (true) {
    u32x16 pv; (void)pv;
    unsigned res = __builtin_amdgcn_readfirstlane(P[2]);
    if (res & 64u) break;
    const u32x4 rh2 = own_rsrc(a.sync + near_half(SY_HOT, sel), H_BYTES - 8192), ra = own_rsrc(a.sync, SY_BYTES), ro = own_rsrc(a.out, S_LEN * OUTW * 2);
    if (res & 1u) {
      spins |= 63u;
      if (dead || poll_fail(spins, abortw, opaque_tid(0))) dead = true;
    }
    ck.sync();
    P[2] = (res & 58u) | (dead ? 4u : 0u); P[1] = ck.ce; P[3] = ck.te; P[4] = ck.t2c; P[5] = ck.c2t << 15;
#pragma unroll
    for (int i = 0; i < 16; ++i) P[i] = __builtin_amdgcn_readfirstlane(P[i]);
    asm volatile(CB_BODY : "+{s[40:55]}"(P) STEP_PV
                 : "{v[192:207]}"(C), "{v[208:215]}"(C2), [rh] "s"(rh2), [ru] "s"(ra), [ro] "s"(ro), [wv] "s"(w), [sel] "s"(sel)
                 : "memory", "vcc", "scc", CB_CL "s56", "s57", "s58", "s59", "s76", "s77", "s78", "s79", "s80", "s81", "s82", "s83", "s84", "s85", "s86", "s87", "s88", "s89", "s90", "s91", "s92", "s93", "s94", "s95", "s96", "s97", "s98", "s99", "s100", "s101", V53, "v53", "v54", "v55", "v56", "v57", "v58", "v59", "v60", "v61", "v62", "v63", "v64", "v65", "v66", "v67", "v68", "v69", "v70", "v71", "v72", "v73", "v74", "v75", "v76", "v77", "v78", "v79", "v80", "v81", "v82", "v83", "v84", "v85", "v86", "v87", "v88", "v89", "v90", "v91", "v92", "v93", "v94", "v95", "v96", "v97", "v98", "v99", "v100", "v101", "v102", "v103", "v104", "v105", "v106", "v107", "v108", "v109", "v110", "v111", "v112", "v113", "v114", "v115", "v116", "v117", "v118", "v119", "v120", "v121", "v122", "v123", "v124", "v125", "v126", "v127", "v128", "v129", "v130", "v131", "v132", "v133", "v134", "v135", "v136", "v137", "v138", "v139", "v140", "v141", "v142", "v143", "v144", "v145", "v146", "v147", "v148", "v149", "v150", "v151", "v152", "v153", "v154", "v155", "v156", "v157", "v158", "v159", "v160", "v161", "v162", "v163", "v164", "v165", "v166", "v167", "v168", "v169", "v170", "v171", "v172", "v173", "v174", "v175", "v176", "v177", "v178", "v179", "v180", "v181", "v182", "v183", "v184", "v185", "v186", "v187", "v188", "v189", "v190", "v191");
    keep_a(W);
  }
  PDUMP(2);
}

__device__ __forceinline__ void bg_drain() {
  asm volatile("s_waitcnt vmcnt(0)" ::: "memory");
  __builtin_amdgcn_s_barrier();
  asm volatile("" ::: "memory");
}
#define BG_SLP 16
#define A3_SLP 48
__device__ __forceinline__ float blo(unsigned d) { return __uint_as_float(d << 16); }
__device__ __forceinline__ float bhi(unsigned d) { return __uint_as_float(d & 0xffff0000u); }
struct A1 {
  u32x4 kk[11], vv[8], sh[4];
  __device__ __forceinline__ void load(const ChainArgs& a, int x, int n) {
    const int lane = opaque_tid(0);
    const int d = (4 * x + (n & 3)) * 64 + 8 * (lane & 7), tb = (n >> 2) * 64 + 8 * (lane >> 3);
    const __amdgpu_buffer_rsrc_t rk = mk_rsrc(a.Ak, 4u << 20), rv = mk_rsrc(a.Av, 4u << 20), rs = mk_rsrc(a.Ashift, 8192);
#pragma unroll
    for (int i = 0; i < 4; ++i) sh[i] = __builtin_amdgcn_raw_buffer_load_b128(rs, d * 8 + 16 * i, 0, 0);
#pragma unroll
    for (int i = 0; i < 11; ++i) kk[i] = __builtin_amdgcn_raw_buffer_load_b128(rk, tb - 3 + i < 0 ? 0x7fffffffu : ((tb - 3 + i) * D_A + d) * 2, 0, 2);
#pragma unroll
    for (int i = 0; i < 8; ++i) vv[i] = __builtin_amdgcn_raw_buffer_load_b128(rv, ((tb + i) * D_A + d) * 2, 0, 2);
  }
  __device__ __forceinline__ void finish(const ChainArgs& a, int x, int n, char* T, Sel sel) {
    const int lane = opaque_tid(0);
#pragma unroll
    for (int c = 0; c < 8; ++c) {
      const unsigned t01 = sh[c >> 1][2 * (c & 1)], t23 = sh[c >> 1][2 * (c & 1) + 1];
      const float s0 = blo(t01), s1 = bhi(t01), s2 = blo(t23), s3 = bhi(t23);
      float kf[11];
#pragma unroll
      for (int i = 0; i < 11; ++i) kf[i] = (c & 1) ? bhi(kk[i][c >> 1]) : blo(kk[i][c >> 1]);
      u32x4 o;
#pragma unroll
      for (int i = 0; i < 8; i += 2) {
        float r[2];
#pragma unroll
        for (int j = 0; j < 2; ++j)
          r[j] = fmaf(kf[i + j + 3], s0, fmaf(kf[i + j + 2], s1, fmaf(kf[i + j + 1], s2, kf[i + j] * s3))) * ((c & 1) ? bhi(vv[i + j][c >> 1]) : blo(vv[i + j][c >> 1]));
        o[i >> 1] = pk2(r[0], r[1]);
      }
      *(u32x4*)(T + (8 * (lane & 7) + c) * 128 + (((lane >> 3) ^ (lane & 7)) * 16)) = o;
    }
    const __amdgpu_buffer_rsrc_t rkv = rsrc_u(a.sync);
#pragma unroll
    for (int i = 0; i < 8; ++i)
      st16(rkv, kv_row((4 * x + (n & 3)) * 64, sel) + (4 * i + (lane >> 4)) * CHK + ((lane >> 3) & 1) * 4096 + (n >> 2) * 128 + (lane & 7) * 16,
           *(const u32x4*)(T + (8 * i + (lane >> 3)) * 128 + (((lane & 7) ^ i) * 16)));
  }
};
#define WAH 4
#define DSP 4
#define SIGQ 2
__device__ __forceinline__ void task_w(const ChainArgs& a, int kind, int h, char* smem, int wv, Gate& G) {
  const int lane = opaque_tid(0), w = wv, r = lane & 15, g = lane >> 4;
  const __amdgpu_buffer_rsrc_t rw = mk_rsrc(kind == 0 ? a.Wq : (kind == 1 ? a.Wk : a.Wv), 8u << 20), rx = mk_rsrc(a.X, 4u << 20);
  const unsigned lds0 = (unsigned)(uintptr_t)(LAS char*)smem + w * 4096;
  unsigned vx[2], vw[4];
#pragma unroll
  for (int j = 0; j < 2; ++j) {
    const int rr = 8 * j + (lane >> 3);
    const int tb = kind == 0 ? rr : (kind == 1 ? 16 * ((rr >> 1) & 1) + 8 * ((rr >> 2) & 1) + 4 * (rr >> 3) + 2 * (rr & 1) : 8 * (rr >> 2) + (rr & 3));
    vx[j] = tb * DM * 2 + ((lane & 7) ^ (rr >> 1)) * 16;
  }
#pragma unroll
  for (int j = 0; j < 4; ++j) { const int row = (w * 4 + j) * 4 + g; vw[j] = row * HD * 2 + (r ^ swz_b(row)) * 16; }
  const u64 wo = kind == 2 ? bg_order(2) : (kind ? bg_order(1) : bg_order(0));
  auto xo = [&](int q) { return ((int)((wo >> (16 * q + 4 * w)) & 15) ^ (h & RHO)) * 128; };
  unsigned dT = (kind == 0 ? 16 : (kind == 1 ? 1 : 4)) * DM * 2;
  asm volatile("s_nop 4" : "+s"(dT));
  const unsigned vx0 = ((w & 1) ? vx[1] : vx[0]) + (w >> 1) * dT;
  auto st_w = [&](int n) {
#pragma unroll
    for (int j = 0; j < 4; ++j) dma16(rw, vw[j], (64 * ((int)((wo >> (4 * n)) & 15) ^ (h & RHO)) * HD + 128 * h) * 2, lds0 + (n & 7) * 16384 + j * 1024, W_NT & 16);
    dma16(rx, vx0, ((int)((wo >> (4 * n)) & 15) ^ (h & RHO)) * 128, lds0 - 3072 * w + 131072 + (n & 7) * 4096);
  };
  auto st_x = [&](int s, int b, int q, int j, bool pad) { dma16(rx, vx[j & 1], s * 65536 + (j >> 1) * dT + xo(q), lds0 + (b * 4 + q) * 16384 + j * 1024, false, pad); };
  if (kind < 2) {
    const __amdgpu_buffer_rsrc_t rs = mk_rsrc(a.Wv, 8u << 20);
#pragma unroll
    for (int n = 0; n < 4 * V_PF; ++n) dma16(rs, vw[n & 3], (64 * ((int)((bg_order(2) >> (4 * (V_PF0 + 2 * (n >> 2) + kind))) & 15) ^ (h & RHO)) * HD + 128 * h) * 2, lds0);
  }
  if (KQ_D && kind < 2) while (__builtin_amdgcn_s_memrealtime() - G.entry < KQ_D) __builtin_amdgcn_s_sleep(8);
  __builtin_amdgcn_s_waitcnt(0x0F70);
  u32x4 W[32][2], gv;
  char* xb[3][2];
#pragma unroll
  for (int i = 0; i < 6; ++i) xb[i >> 1][i & 1] = lds_base(smem, (i >> 1) * 65536 + (r >> 3) * 1024 + (r & 7) * 128 + ((4 * (i & 1) + g) ^ (r >> 1)) * 16);
  f32x4 acc[2][4];
#pragma unroll
  for (int i = 0; i < 4; ++i) acc[1][i] = vzero();
#pragma unroll
  for (int n = 0; n < WAH; ++n) st_w(n);
#pragma unroll
  for (int n = 0; n < 16; ++n) {
    asm volatile("s_waitcnt vmcnt(%0)" :: "n"(4 * WAH - 4 + (n < 16 - WAH ? WAH - 1 : 15 - n)) : "memory");
    lds_barrier();
    u32x4 f[4], y[4];
#pragma unroll
    for (int i = 0; i < 4; ++i) {
#pragma unroll
      for (int q = 0; q < 2; ++q) {
        const int row = (i & 1) * 32 + 8 * g + 4 * q + (r >> 2);
        const u32x2 u = __builtin_bit_cast(u32x2, __builtin_amdgcn_ds_read_tr16_b64_v4i16((LAS bf16x4*)(smem + (n & 7) * 16384 + row * 256 + (((4 * w + (r & 3)) ^ swz_b(row)) * 16) + 8 * (i >> 1))));
        f[i][2 * q] = u[0]; f[i][2 * q + 1] = u[1];
      }
      y[i] = *(const u32x4*)(xb[2][i & 1] + (n & 7) * 4096 + (i >> 1) * 2048);
    }
    SB0;
    if (n == 16 - WAH) G.ask(a.sync, gv);
    if (n == 15) asm volatile("" : "+v"(gv));
    if (n + WAH < 16) st_w(n + WAH);
    else {
#pragma unroll
      for (int j = 0; j < 4; ++j) st_x(1 + ((n + WAH - 16) >> 2), (n + WAH - 16) >> 2, (n + WAH) & 3, j, true);
    }
#pragma unroll
    for (int i = 0; i < 8; ++i)
      asm volatile("s_nop 1\n\tv_mfma_f32_16x16x32_bf16 %0, %1, %2, %0" : "+v"(acc[1][i & 3]) : "v"(f[2 * (i & 1) + (i >> 2)]), "v"(y[(i & 2) + (i >> 2)]));
#pragma unroll
    for (int i = 0; i < 4; ++i) { asm volatile("" : "+a"(f[i])); W[2 * n + (i & 1)][i >> 1] = f[i]; }
  }
  if (!G.look(a.sync, gv)) { bg_drain(); return; }
  TP(4);
  SSTAMP(160 + h);
  const Sel sel = G.sel;
  const __amdgpu_buffer_rsrc_t ru = mk_rsrc(a.sync, SY_BYTES);
  const unsigned sgo = (w | lane) ? 0x7fffffffu : SY_CT + (kind == 2 ? CT_V + ct_near(~sel) : CT_Q + 32 * kind + ct_near(sel)) * 128 + h * 128;
#pragma unroll
  for (int T = 0; T < 2; ++T) st16s(pk8v(acc[1][2 * T], acc[1][2 * T + 1]), ru, lane * 16 + T * 4096, x_slot(kind, 0, h, sel) + w * 1024);
  SB0;
  const u32x4 rx2 = own_rsrc(a.X, 4u << 20);
  unsigned vq[4][4];
#pragma unroll
  for (int i = 0; i < 16; ++i) { vq[i >> 2][i & 3] = vx[i & 1] + ((i >> 1) & 1) * dT + xo(i >> 2); asm volatile("" : "+v"(vq[i >> 2][i & 3])); }
  u32x4 xf[4][2], o;
  auto rd = [&](int b, int k, int T) { xf[k & 3][T] = *(const u32x4*)(xb[b][k & 1] + (k >> 1) * 4096 + T * 2048); };
  auto signal = [&](int s) {
    st4(ru, sgo, s);
    if (w == 0 && lane == 0 && s >= 0 && (s < PSEG || (s & 7) == 0 || ((s & 15) == 2 && PSEG > 2) || s > 61)) {
    }
  };
  if (kind < KQ_NOW) { bg_drain(); signal(0); }
  G.arrive(a.sync);
#pragma unroll
  for (int i = 4 * WAH; i < 28; ++i) st_x(1 + (i >> 4), i >> 4, (i >> 2) & 3, i & 3, true);
  wait_vm<26>();
  lds_barrier();
#pragma unroll
  for (int i = 0; i < 6; ++i) rd(0, i >> 1, i & 1);
  WST(kind == 2, 18);
  auto seg = [&](int s, int b) {
    const unsigned so = x_slot(kind, s - 1, h, sel) + w * 1024;
    WST(kind == 2 && s > 0 && s < 4, 18 + s);
#pragma unroll
    for (int k = 0; k < 32; ++k)
#pragma unroll
      for (int i = 0; i < 4; ++i) {
        if (k == 0) asm volatile("v_mfma_f32_16x16x32_bf16 %0, %1, %2, 0" : "=&v"(acc[b][i]) : "a"(W[k][i & 1]), "v"(xf[k & 3][i >> 1]));
        else asm volatile("v_mfma_f32_16x16x32_bf16 %0, %1, %2, %0" : "+v"(acc[b][i]) : "a"(W[k][i & 1]), "v"(xf[k & 3][i >> 1]));
        SB0;
        const int q = k >> 3, n = 4 * k + i;
        if ((k & 7) == 5 && i == 0) {
          if (q == SIGQ) wait_vm<4 * SIGQ>(); else wait_vm<20>();
          if (q == 1 && __builtin_expect(s - 2u < W_NOW, 0)) { wait_vm<4>(); __builtin_amdgcn_s_barrier(); signal(s - 1); }
          else if (q == 0 && b == 0 && __builtin_expect(s == 1, 0)) { wait_vm<32 - 4 * WAH>(); __builtin_amdgcn_s_barrier(); signal(0); }
          else __builtin_amdgcn_s_barrier();
          asm volatile("" ::: "memory");
        }
        if (i == 1 || i == 2) rd(k + 3 < 32 ? b : b ^ 1, (k + 3) & 31, i - 1);
        {
          const int m = (n + 128 - 23) & 127, j = (m & 31) / DSP, q0 = m >> 5;
          if (j < 4 && (n == 0 || ((m & 31) % DSP == 0 && n != 127))) asm volatile("s_add_i32 m0, %0, %1" :: "s"(lds0), "n"(((b ^ (q0 == 0) ^ (n < 23)) * 4 + ((q0 + 3) & 3)) * 16384 + j * 1024) : "m0", "scc");
          if (n == 0) asm volatile("s_nop 0");
          if (j < 4 && (m & 31) % DSP == 1)
            asm volatile("buffer_load_dwordx4 %0, %1, %2 offen lds" :: "v"(vq[(q0 + 3) & 3][j & 3]), "s"(rx2), "s"((s + 2 - (q0 == 0) - (n < 23)) * 65536) : "memory");
        }
        if (k == 8 * SIGQ + 5 && i == 2) signal(s - 1);
        if (k >= 1 && k < 5 && (i == 0 || i == 3)) { const f32x4 x = acc[b ^ 1][k - 1]; o[2 * (~k & 1) + (i & 1)] = pk2v(x[2 * (i & 1)], x[2 * (i & 1) + 1]); }
        if ((k == 2 || k == 4) && i == 3) { __builtin_amdgcn_raw_buffer_store_b128(o, ru, (unsigned)(lane * 16 + (k >> 2) * 4096), so, ST_AUX); asm volatile("s_nop 0" :: "v"(o)); }
        SB0;
      }
  };
  A1 a1;
#pragma unroll
  for (int i = 0; i < 11; ++i) asm volatile("" : "=v"(a1.kk[i]), "=v"(a1.vv[i & 7]), "=v"(a1.sh[i & 3]));
  const int wi = 32 * (2 - kind) + 4 * (h & 7) + w;
  for (int s = 1, ph = 0;; ++ph) {
    const int n = ph == 1 ? wi : (ph == 2 && wi >= 64 ? 32 + wi : 256);
    for (const int e = s + (ph ? A1_D : A1_0); s < e || ph == 4; s += 2) {
      seg(s, 0);
      if (s == NSEG - 1) break;
      seg(s + 1, 1);
    }
    if (ph == 4) break;
    if (ph == 3 && lane == 0) ctr_add(a, CT_A1 + ct_near(~sel) + (h >> 3), 1u);
    if (n < 128) a1.finish(a, h >> 3, n, smem + 131072 + w * 8192, sel);
    a1.load(a, h >> 3, ph == 0 ? wi : (ph == 1 && wi >= 64 ? 32 + wi : 256));
    SB0;
  }
#pragma unroll
  for (int T = 0; T < 2; ++T) st16s(pk8v(acc[0][2 * T], acc[0][2 * T + 1]), ru, lane * 16 + T * 4096, x_slot(kind, NSEG - 1, h, sel) + w * 1024);
  bg_drain();
  signal(NSEG - 1);
}
constexpr int A2_CSTR = 4160;
constexpr int A2_UOFF = 4 * A2_CSTR + 16 * 80;
constexpr int A2_USTR = 80 * 80;
constexpr int A2_WLDS = 4 * A2_CSTR + 2 * A2_USTR;
__device__ __forceinline__ void a2_wave(const ChainArgs& a, int d0, int d1, char* smem, int w, Sel sel) {
  const int lane = opaque_tid(0);
  char* Wb = smem + 1024 + w * A2_WLDS;
  char* U = Wb + A2_UOFF;
  const __amdgpu_buffer_rsrc_t rf = mk_rsrc((void*)a.Assm, 4u << 20), rd_ = mk_rsrc((void*)a.AD, 2048);
  const __amdgpu_buffer_rsrc_t rkv = rsrc_u(a.sync);
  u32x4 hi[4], uv[2][4];
  u32x2 lo[4];
  unsigned dn;
  auto ld_h = [&](int d) {
#pragma unroll
    for (int i = 0; i < 4; ++i) {
      hi[i] = __builtin_amdgcn_raw_buffer_load_b128(rf, (unsigned)((255 - 64 * i - lane) * 16), (unsigned)(d * 4096), 2);
      lo[i] = __builtin_amdgcn_raw_buffer_load_b64(rf, i == 3 && lane == 63 ? 0x7fffffffu : (255 - 64 * i - lane) * 16 - 8, (unsigned)(d * 4096), 2);
    }
    dn = __builtin_amdgcn_raw_buffer_load_b16(rd_, 0, 2 * d, 0);
  };
  auto ld_u = [&](int d) {
#pragma unroll
    for (int i = 0; i < 8; ++i) uv[i & 1][i >> 1] = __builtin_amdgcn_raw_buffer_load_b128(rkv, (unsigned)max(((i >> 1) * 64 + lane) * 16 - 8 * (i & 1), 0), kv_row(d, sel), LD_AUX);
  };
  ld_h(d0);
  {
    bool dead = false;
    ctr_wait<BG_SLP>(ctr(a, CT_A1 + ct_near(~sel) + (d0 >> 8)), 96u, (GAS unsigned*)(a.sync + SY_ABORT), lane, dead);
  }
  ld_u(d0);
  if (lane < 12) *(f32x4*)(Wb + (lane / 3) * A2_CSTR + (256 + lane % 3) * 16) = vzero();
#pragma unroll
  for (int i = 0; i < 4; ++i) if (64 * (i & 1) + lane < 80) *(f32x4*)(U + (i >> 1) * A2_USTR - 16 * 80 + (64 * (i & 1) + lane) * 16) = vzero();
  const int r = lane & 15, g = lane >> 4;
#pragma unroll 1
  for (int d = d0; d < d1; d += 96) {
    u64 at[6]; (void)at;
    AST(0);
#pragma unroll
    for (int i = 0; i < 4; ++i) {
      const unsigned src[6] = {lo[i][0], lo[i][1], hi[i][0], hi[i][1], hi[i][2], hi[i][3]};
      unsigned f[10];
#pragma unroll
      for (int x = 0; x < 10; ++x) {
        if ((x & 1) == 0) f[x] = __builtin_amdgcn_alignbit(src[5 - x / 2], src[5 - x / 2], 16);
        else f[x] = __builtin_amdgcn_perm(src[5 - (x + 1) / 2], src[5 - (x - 1) / 2], 0x07060100u);
      }
#pragma unroll
      for (int m = 0; m < 4; ++m) *(u32x4*)(Wb + m * A2_CSTR + (64 * i + lane) * 16) = (u32x4){f[3 - m], f[5 - m], f[7 - m], f[9 - m]};
    }
    if (lane == 0) uv[1][0] = (u32x4){0u, 0u, uv[1][0][0], uv[1][0][1]};
#pragma unroll
    for (int i = 0; i < 8; ++i) {
      const int ch = (i >> 1) * 64 + lane;
      *(u32x4*)(U + (i & 1) * A2_USTR + (ch >> 2) * 80 + (ch & 3) * 16) = uv[i & 1][i >> 1];
    }
    const float Dd = blo(dn);
    asm volatile("" :: "v"(Dd) : "memory");
    AST(1);
    ld_h(d + 96 < d1 ? d + 96 : D_A); ld_u(d + 96 < d1 ? d + 96 : D_A);
    f32x4 acc[4][2];
#pragma unroll
    for (int i = 0; i < 8; ++i) acc[i >> 1][i & 1] = vzero();
    const char* pa = Wb + (r & 3) * A2_CSTR + 16 * (255 - (r >> 2) + g) - 3 * 1024 - 64;
    const char* pb = U + (r - 1) * 80 + 16 * g;
    bf16x8 fa[2][4], fb[2][8];
    auto rd = [&](int b, int n, int k) {
      if (n == 0 || n > 8) fa[b][n ? n - 8 : 0] = *(const bf16x8*)(pa + 1024 * (3 - (n ? n - 8 : 0)) + 64 * (1 - k));
      else fb[b][n - 1] = *(const bf16x8*)(pb + ((n - 1) & 1) * A2_USTR + 80 * (16 * ((n - 1) >> 1) + 1 - k));
    };
#pragma unroll
    for (int n = 0; n < 12; ++n) rd(0, n, 0);
    AST(2);
#pragma unroll 1
    for (int k = 0; k < 16; k += 2) {
#pragma unroll
      for (int b = 0; b < 2; ++b) {
        int q = 0;
#pragma unroll
        for (int j = 0; j < 4; ++j)
#pragma unroll
          for (int i = j; i < 4; ++i)
#pragma unroll
            for (int mt = 0; mt < 2; ++mt) {
              asm volatile("v_mfma_f32_16x16x32_bf16 %0, %1, %2, %0" : "+v"(acc[i][mt]) : "v"(fa[b][j]), "v"(fb[b][2 * (i - j) + mt]));
              SB0;
              if (q < 12) rd(b ^ 1, q, 1);
              if (q == 12) { pa -= 64; pb -= 80; }
              ++q;
              SB0;
            }
      }
    }
    AST(3);
    u32x4 kv[4];
#pragma unroll
    for (int i = 0; i < 4; ++i) kv[i] = *(const u32x4*)(U + (16 * i + r) * 80 + 16 * g);
    asm volatile("s_nop 7\n\ts_waitcnt vmcnt(0)" ::: "memory");
#pragma unroll
    for (int i = 0; i < 8; ++i) asm volatile("" : "+v"(acc[i >> 1][i & 1]));
    if (lane == 0 && d > d0) ctr_add(a, CT_A2 + ct_near(~sel) + ((d - 96) >> 6), 1u);
#pragma unroll
    for (int i = 0; i < 4; ++i) {
      const f32x4 x = acc[i][1], y = acc[i][0];
      st16s((u32x4){pk2(fmaf(blo(kv[i][0]), Dd, x[0]), fmaf(bhi(kv[i][0]), Dd, x[1])), pk2(fmaf(blo(kv[i][1]), Dd, x[2]), fmaf(bhi(kv[i][1]), Dd, x[3])),
                    pk2(fmaf(blo(kv[i][2]), Dd, y[0]), fmaf(bhi(kv[i][2]), Dd, y[1])), pk2(fmaf(blo(kv[i][3]), Dd, y[2]), fmaf(bhi(kv[i][3]), Dd, y[3]))},
            rkv, (unsigned)(i * 1024 + r * 64 + g * 16), kv_row(d, sel));
    }
    AST(4);
  }
  wait_vm<0>();
  if (lane == 0) ctr_add(a, CT_A2 + ct_near(~sel) + ((d0 + (d1 - 1 - d0) / 96 * 96) >> 6), 1u);
}
__device__ __forceinline__ void task_u(const ChainArgs& a, int h, char* smem, int wv, Gate& G) {
  const int lane = opaque_tid(0);
  const int w = wv;
  GAS unsigned* abortw = (GAS unsigned*)(a.sync + SY_ABORT);
  bool dead = false;
  char* sb = smem + w * 32768;
  const __amdgpu_buffer_rsrc_t rwo = mk_rsrc(a.Wo, 8u << 20);
  const unsigned lds0 = (unsigned)(uintptr_t)(LAS char*)sb;
  unsigned wo4[4];
#pragma unroll
  for (int i = 0; i < 4; ++i) { const int row = i * 4 + (lane >> 4); wo4[i] = row * DM * 2 + (((lane & 15) ^ swz_b(row)) * 16); }
  asm volatile("s_nop 4");
  auto stage = [&](int n) {
    const int p = (U_ORD >> (4 * n)) & 3;
#pragma unroll
    for (int i = 0; i < 16; ++i)
      dma16(rwo, wo4[i & 3], (unsigned)(((128 * h + 64 * (p & 1) + 16 * (i >> 2)) * DM + 256 * w + 128 * (p >> 1)) * 2), lds0 + (n & 1) * 16384 + i * 1024, W_NT & 2);
  };
  __builtin_amdgcn_s_waitcnt(0x0F70);
  u32x4 W[16][4];
  stage(0); stage(1);
#pragma unroll
  for (int n = 0; n < 4; ++n) {
    const int p = (U_ORD >> (4 * n)) & 3;
    if (n < 3) wait_vm<16>(); else wait_vm<0>();
#pragma unroll
    for (int t = 0; t < 8; ++t) {
      const u32x8 f = tr_frag(sb + (n & 1) * 16384, t, lane);
      u32x4 f0 = {f[0], f[1], f[2], f[3]}, f1 = {f[4], f[5], f[6], f[7]};
      asm volatile("" : "+a"(f0), "+a"(f1));
      W[8 * (p >> 1) + t][2 * (p & 1)] = f0; W[8 * (p >> 1) + t][2 * (p & 1) + 1] = f1;
    }
    asm volatile("s_waitcnt lgkmcnt(0)" ::: "memory");
    if (n + 2 < 4) stage(n + 2);
  }
  lds_barrier();
  if (!G.pass(a.sync)) return;
  SSTAMP(192 + h);
  const Sel sel = G.sel;
  const u32x4 ru = own_rsrc(a.sync, SY_BYTES);
  u32x4 V[2][8], pc[2], o[2];
  f32x4 c[2][2];
  unsigned cv, cw, vo[8], lk[2], sgo = (w | lane) ? 0x7fffffffu : SY_CT + (CT_U + ct_near(sel) + h) * 128, nd = (lane & 1) ? 3 - URING : 2;
#pragma unroll
  for (int i = 0; i < 8; ++i) { vo[i] = lane * 16 + ux_tile(16 * w + 2 * i); asm volatile("" : "+v"(vo[i])); }
#pragma unroll
  for (int i = 0; i < 2; ++i) lk[i] = SY_CT + (ct_near(~sel) + ((lane & 1) ? CT_DONE + 8 * i + (h & 7) : CT_V + h)) * 128;
  char* vl = lds_base(smem, lane * 16);
  const unsigned vl2 = lane * 16 + w * 2048;
  auto ld_v = [&](int s, bool pad = false) {
    if (pad) asm volatile("s_nop 4\n\tbuffer_load_dwordx4 %0, %2, %3, %4 offen sc1\n\tbuffer_load_dwordx4 %1, %2, %3, %4 offen offset:1024 sc1" : "=&v"(pc[0]), "=&v"(pc[1]) : "v"(vl2), "s"(ru), "s"(x_slot(2, s, h, sel)) : "memory");
    else asm volatile("buffer_load_dwordx4 %0, %2, %3, %4 offen sc1\n\tbuffer_load_dwordx4 %1, %2, %3, %4 offen offset:1024 sc1" : "=&v"(pc[0]), "=&v"(pc[1]) : "v"(vl2), "s"(ru), "s"(x_slot(2, s, h, sel)) : "memory");
  };
  auto signal = [&](int s) {
    asm volatile("buffer_store_dword %0, %1, %2, 0 offen sc1" :: "v"(s), "v"(sgo), "s"(ru) : "memory");
  };
  auto st_u = [&](int n, unsigned ub) { asm volatile("buffer_store_dwordx4 %0, %1, %2, %3 offen offset:%4 sc1" :: "v"(o[n & 1]), "v"(vo[n >> 1]), "s"(ru), "s"(ub), "n"((n & 1) * 1024) : "memory"); };
  auto fetch = [&](int s) {
    for (unsigned spins = 0;;) {
      asm volatile("buffer_load_dword %0, %1, %2, 0 offen sc1" : "=&v"(cw) : "v"(lk[0]), "s"(ru) : "memory");
      ld_v(s, true);
      asm volatile("s_waitcnt vmcnt(0)" : "+v"(pc[0]), "+v"(pc[1]), "+v"(cw) :: "memory");
      if (s && spins == 0) { __builtin_amdgcn_s_barrier(); signal(0); }
      if ((int)__builtin_amdgcn_readfirstlane(cw) >= s) break;
      __builtin_amdgcn_s_sleep(2);
      if (poll_fail(spins, abortw, lane)) { dead = true; break; }
    }
    WST(s == 0, 24);
    if (s == 0) TP(3);
#pragma unroll
    for (int i = 0; i < 2; ++i) *(u32x4*)(vl + 16384 + w * 2048 + i * 1024) = pc[i];
    lds_barrier();
#pragma unroll
    for (int i = 0; i < 8; ++i) V[s][i] = *(const u32x4*)(vl + 16384 + i * 1024);
  };
#pragma unroll
  for (int i = 0; i < 8; ++i) asm volatile("" : "=v"(V[0][i]));
  u64 bad = 0;
  unsigned up = 0x7f000000u;
  c[1][0] = c[1][1] = vzero();
  auto seg = [&](int s, int b) {
    const unsigned ub = s < 0 ? 0x7f000000u : ux_base(s, h, sel);
    WST(s >= 0 && s < 4, 25 + s);
#pragma unroll
    for (int n = 0; n < 16; ++n)
#pragma unroll
      for (int q = 0; q < 8; ++q) {
        const int kp = q >> 1, m = q & 1;
        if (kp == 0) asm volatile("v_mfma_f32_16x16x32_bf16 %0, %1, %2, 0" : "=&v"(c[n & 1][m]) : "v"(V[b][4 * m + kp]), "a"(W[n][kp]));
        else asm volatile("v_mfma_f32_16x16x32_bf16 %0, %1, %2, %0" : "+v"(c[n & 1][m]) : "v"(V[b][4 * m + kp]), "a"(W[n][kp]));
        SB0;
        if (q >= 2 && q < 6) { const f32x4 x = c[~n & 1][(q - 2) >> 1]; o[~n & 1][q - 2] = pk2v(x[2 * (q & 1)], x[2 * (q & 1) + 1]); }
        if (q == 6) st_u((n - 1) & 15, n ? ub : up);
        if (q == 5) asm volatile("" :: "v"(o[n & 1]));
        if (n == U_LD && q == 0) {
          if (__builtin_expect(bad != 0, 0)) {
            if (s <= U_NOW) {
              asm volatile("s_waitcnt vmcnt(%1)" : "+v"(cw) : "n"(U_LD - 2) : "memory");
              __builtin_amdgcn_s_barrier();
              signal(s - 1);
              bad = (int)__builtin_amdgcn_readfirstlane(cw) <= s;
            }
            if (bad & 1) ctr_wait<2>(ctr(a, CT_V + ct_near(~sel) + h), min(s + 2, NSEG), abortw, lane, dead);
            if (bad & 2) {
              ctr_wait<U_SLP>(ctr(a, CT_DONE + ct_near(~sel) + 8 * b + (h & 7)), max(s + 3 - URING, 0), abortw, lane, dead);
              for (int i = 0; i < U_STG * (h & 7) + U_ST0; ++i) __builtin_amdgcn_s_sleep(1);
            }
          }
          ld_v(min(s + 1u, NSEG - 1u));
        }
        if (n == 1 && q == 7) asm volatile("buffer_load_dword %0, %1, %2, 0 offen sc1" : "=&v"(cw) : "v"(s <= U_NOW ? lk[0] : 0x7fffffffu), "s"(ru) : "memory");
        if (n == U_LD && q == 7) asm volatile("buffer_load_dword %0, %1, %2, 0 offen sc1" : "=&v"(cv) : "v"(lk[b ^ 1]), "s"(ru) : "memory");
        if (n == U_SIG2 && q == 0) asm volatile("s_waitcnt vmcnt(%4)" : "+v"(pc[0]), "+v"(pc[1]), "+v"(cv), "+v"(cw) : "n"(U_SIG2 - U_LD - 1) : "memory");
        if (n == U_SIG2 && (q == 1 || q == 7)) *(u32x4*)(vl + (b ^ 1) * 8192 + w * 2048 + (q >> 2) * 1024) = pc[q >> 2];
        if (n == U_SIG2 + 1 && q == 0) { bad = __builtin_amdgcn_ballot_w64(s >= 0 && (int)cv < (s < U_NOW && lane > 1 ? NSEG : min(s + (int)nd, NSEG - 1))); asm volatile("" : "+s"(bad)); }
        if (n == U_SIG2 + 1 && q == 7) lds_barrier();
        if (n == U_SIG2 + 2 && q == 0) signal(s - 1);
        if ((n == U_SIG2 + 2 || n == U_SIG2 + 3) && (q & 1)) V[b ^ 1][4 * (n - U_SIG2 - 2) + (q >> 1)] = *(const u32x4*)(vl + (b ^ 1) * 8192 + (4 * (n - U_SIG2 - 2) + (q >> 1)) * 1024);
        SB0;
      }
    up = ub;
  };
  for (int s = -U_DRY; s < NSEG; s += 2) {
    if (__builtin_expect(s == 0, 0)) fetch(0);
    seg(s, 0);
    if (__builtin_expect(s == 0, 0)) {
      o[1] = pk8v(c[1][0], c[1][1]);
      st_u(15, up);
      fetch(1);
    }
    seg(s + 1, 1);
  }
  o[1] = pk8v(c[1][0], c[1][1]);
  st_u(15, up);
  bg_drain();
  signal(NSEG - 1);
}

__device__ __forceinline__ void task_a3(const ChainArgs& a, int dg, int t0, char* T, Sel sel) {
  const int lane = opaque_tid(0);
  const int d = dg * 64 + 8 * (lane & 7);
  const int tb = t0 + 8 * (lane >> 3);
  const __amdgpu_buffer_rsrc_t rz = rsrc_u(a.sync);
  u32x4 qq[16], zz[8][2];
#pragma unroll
  for (int i = 0; i < 16; ++i) qq[i] = LDNT((const u32x4*)(a.Aq + (long)(tb + 64 * (i >> 3) + (i & 7)) * D_A + d));
  {
    u32x4 zr[16];
    bool dead = false;
    ctr_wait<A3_SLP>(ctr(a, CT_A2 + ct_near(~sel) + dg), 64u, (GAS unsigned*)(a.sync + SY_ABORT), lane, dead);
  #pragma unroll
    for (int i = 0; i < 16; ++i) zr[i] = ld16(rz, kv_row(dg * 64, sel) + (2 * i + (lane >> 5)) * CHK + ((lane >> 4) & 1) * 4096 + (t0 + (lane & 15) * 8) * 2);
#pragma unroll
    for (int i = 0; i < 16; ++i) {
      const int row = 4 * i + (lane >> 4);
      *(u32x4*)(T + row * 256 + (((lane & 15) ^ (row >> 3)) * 16)) = zr[i];
    }
  }
#pragma unroll
  for (int c = 0; c < 8; ++c)
#pragma unroll
    for (int gq = 0; gq < 2; ++gq) zz[c][gq] = *(const u32x4*)(T + (8 * (lane & 7) + c) * 256 + (((gq * 8 + (lane >> 3)) ^ (lane & 7)) * 16));
#pragma unroll
  for (int i = 0; i < 16; ++i) {
    u32x4 o;
#pragma unroll
    for (int c = 0; c < 8; c += 2) {
      const unsigned z0 = zz[c][i >> 3][(i >> 1) & 3], z1 = zz[c + 1][i >> 3][(i >> 1) & 3];
      o[c >> 1] = pk2(blo(qq[i][c >> 1]) * ((i & 1) ? bhi(z0) : blo(z0)), bhi(qq[i][c >> 1]) * ((i & 1) ? bhi(z1) : blo(z1)));
    }
    st16g(a.out + (long)(tb + 64 * (i >> 3) + (i & 7)) * OUTW + d, o);
  }
}

__device__ __forceinline__ void part_a(const ChainArgs& a, char* smem, int x, int wi, int w, Sel sel) {
  char* T = smem + 1024 + w * A2_WLDS;
  if (wi >= 0) a2_wave(a, 256 * x + wi, 256 * x + 256, smem, w, sel);
  while (true) {
    unsigned t = 0;
    if (opaque_tid(0) == 0) t = __hip_atomic_fetch_add(ctr(a, CT_TK + ct_near(~sel) + x), 1u, __ATOMIC_RELAXED, __HIP_MEMORY_SCOPE_AGENT) + 1u;
    t = __builtin_amdgcn_readfirstlane(t);
    if (t >= 64) break;
    task_a3(a, 4 * x + (t >> 4), (t & 15) * 128, T, sel);
  }
}

__global__ __launch_bounds__(256, 1) void chain_kernel(ChainArgs a) {
  extern __shared__ __attribute__((aligned(16))) char smem[];
  const int b = blockIdx.x;
  {
    u32x4 rc;
    asm volatile("s_getpc_b64 s[60:61]\n\t.Lchain_here:\n\ts_sub_u32 s60, s60, .Lchain_here-_Z12chain_kernel9ChainArgs\n\ts_subb_u32 s61, s61, 0\n\ts_and_b32 s61, s61, 0xffff\n\t"
                 "s_mov_b32 s62, .Lchain_end-_Z12chain_kernel9ChainArgs\n\ts_mov_b32 s63, 0x20000\n\t"
                 "buffer_load_dwordx4 a[252:255], %1, s[60:63], 0 offen\n\tbuffer_load_dwordx4 a[252:255], %1, s[60:63], s63 offen"
                 : "={s[60:63]}"(rc) : "v"((b >> 3) * 4096 + threadIdx.x * 16) : "scc", "a252", "a253", "a254", "a255");
  }
  int w = __builtin_amdgcn_readfirstlane(threadIdx.x >> 6);
  Gate G;
  G.entry = __builtin_amdgcn_s_memrealtime(); G.open = false; G.arr = w == 0 ? b & 7 : -1;
  G.rot = ((unsigned)__builtin_amdgcn_s_getreg(20 | (3 << 11)) - b + WRONG) & 7u;
  {
    volatile LAS unsigned* t = (volatile LAS unsigned*)smem;
    if (w == 0) { t[0] = (unsigned)G.entry; t[1] = (unsigned)(G.entry >> 32); }
    __syncthreads();
    G.entry = (u64)(unsigned)__builtin_amdgcn_readfirstlane(t[0]) | ((u64)(unsigned)__builtin_amdgcn_readfirstlane(t[1]) << 32);
    __syncthreads();
  }
  G.tx = G.entry;
  if (((b + G.rot) & 4) == 0 && b < 128) {
    const int c = (b >> 6) * 32 + 8 * ((b + G.rot) & 3) + ((b >> 3) & 7);
    if (w < 2) calibrate(a, w ? 32 + (0x3200 >> (4 * (c & 3)) & 3) : c, 2 * c + w, G.entry);
    __builtin_amdgcn_s_barrier();
  }
  if (b == 0) {
    const int lane = opaque_tid(0);
    const __amdgpu_buffer_rsrc_t rs = mk_rsrc(a.sync + SY_SET, 3 * CHK);
    st16(rs, CHK + w * 8192 + lane * 128, (u32x4){SENT, SENT, SENT, SENT});
    if (w == 0 && lane >= 2) st16(rs, lane * 128, (u32x4){SENT, SENT, SENT, SENT});
    if (w == 3) {
      u32x4 v;
      bool ok;
      do {
        v = ld16(mk_rsrc(a.sync + SY_CT + CT_CAL * 128, 8192), lane * 128);
        ok = ((u64)v[1] << 32 | v[0]) - G.entry + 2000u < (1ull << 31) && ((u64)v[3] << 32 | v[2]) - G.entry + 2000u < (1ull << 31);
      } while (!__all(ok) && __builtin_amdgcn_s_memrealtime() - G.entry < 2 * CALM);
      const u64 k = lane == 32 ? 0x3333333333333333ull : (lane == 34 || lane == 35 ? 0x1111111111111111ull << (lane & 3) : 0ull);
      const int n1 = __builtin_popcountll(k & __builtin_amdgcn_ballot_w64(ok && (v[2] & 3u) == 3u)), n0 = __builtin_popcountll(k & __builtin_amdgcn_ballot_w64(ok && (v[2] & 3u) == 2u));
      if (!ok) v[0] = 0;
      if (n0 != n1) v[0] = n1 > n0 ? 3u : 2u;
      const unsigned t = (v[0] ^ lane) & 1u;
      const bool most = __builtin_popcountll(__builtin_amdgcn_ballot_w64((v[0] & 2u) && t)) > __builtin_popcountll(__builtin_amdgcn_ballot_w64((v[0] & 2u) && !t));
      const u64 m = __builtin_amdgcn_ballot_w64((v[0] & 2u) ? v[0] & 1u : (lane ^ most) & 1u);
      if (lane == 0) __hip_atomic_store((GAS u64*)(a.sync + SY_SEL), m, __ATOMIC_RELAXED, __HIP_MEMORY_SCOPE_AGENT);
    }
    bg_drain();
    if (w == 0 && lane == 0) __hip_atomic_store((GAS u64*)(a.sync + SY_EPOCH), (__builtin_amdgcn_s_memrealtime() & ~7ull) | G.rot, __ATOMIC_RELAXED, __HIP_MEMORY_SCOPE_AGENT);
  }
  while (true) {
    const unsigned rot = G.rot;
    asm volatile("" : "+s"(w));
    const int x = (b + rot) & 7, j = b >> 3, h = 8 * (x & 3) + (j & 7), role = (x & 4) + (j >> 3);
    if ((role == 0 || role == 7) && w >= 2) {
      const int n = opaque_tid(w - 2);
      const unsigned x0 = __builtin_amdgcn_raw_buffer_load_b32(mk_rsrc(a.sync, 106u << 20), n * PAGE + 4096 + (j & 31) * 128, 0, 2);
      const unsigned x1 = __builtin_amdgcn_raw_buffer_load_b32(mk_rsrc(a.out, 8u << 20), n * PAGE + (j & 31) * 128, 0, 2);
      asm volatile("" :: "v"(x0), "v"(x1));
    }
    if (role < 2) role_cb(a, 32 * role + h, smem, w, G);
    else if (role == 2) role_qk(a, h, smem, w, G);
    else if (role == 3) role_vo(a, h, smem, w, G);
    else if (role == 4) task_u(a, h, smem, w, G);
    else task_w(a, 7 - role, h, smem, w, G);
    if (G.rot == rot && role >= 4 && !NOA) part_a(a, smem, x & 3, 4 * (j - 8) + w, w, G.sel);
    if (G.rot == rot) break;
    __syncthreads();
  }
  asm volatile(".Lchain_end:");
}

extern "C" void kernel_launch(void* const* d_in, const int* in_sizes, int n_in,
                              void* d_out, int out_size, void* d_ws, size_t ws_size,
                              hipStream_t stream) {
  const u16* A_q = (const u16*)d_in[0];
  const u16* A_k = (const u16*)d_in[1];
  const u16* A_v = (const u16*)d_in[2];
  const u16* A_shift = (const u16*)d_in[3];
  const u16* A_ssmk = (const u16*)d_in[4];
  const u16* A_D = (const u16*)d_in[5];
  const u16* B_x = (const u16*)d_in[6];
  const u16* B_mem0 = (const u16*)d_in[7];
  const u16* B_Wq = (const u16*)d_in[8];
  const u16* B_Wk = (const u16*)d_in[9];
  const u16* B_Wv = (const u16*)d_in[10];
  const u16* B_Wo = (const u16*)d_in[11];
  u16* out = (u16*)d_out;
  char* ws = (char*)(((uintptr_t)d_ws + (2u << 20) - 1) & ~(uintptr_t)((2u << 20) - 1)) + WSOFF * MB;
  hipFuncSetAttribute((const void*)chain_kernel, hipFuncAttributeMaxDynamicSharedMemorySize, CHAIN_LDS);
  ChainArgs ca;
  ca.prof = (u64*)((char*)d_ws + PROF_AT);
  ca.mem0 = B_mem0; ca.Wq = B_Wq; ca.Wk = B_Wk; ca.Wv = B_Wv; ca.Wo = B_Wo; ca.X = B_x;
  ca.out = out; ca.sync = ws;
  ca.Aq = A_q; ca.Ak = A_k; ca.Av = A_v; ca.Ashift = A_shift; ca.Assm = A_ssmk; ca.AD = A_D;
  chain_kernel<<<256, 256, CHAIN_LDS, stream>>>(ca);
}
```

```cpp
#include <hip/hip_runtime.h>
#include <hip/hip_bf16.h>
#include <stdint.h>
#include <type_traits>

constexpr int S_LEN = 2048;
constexpr int D_A   = 1024;
constexpr int DM    = 1024;
constexpr int NH    = 32;
constexpr int DH    = 128;
constexpr int HD    = NH * DH;
constexpr int MEMR  = 8;
constexpr int SEG   = 32;
constexpr int NSEG  = S_LEN / SEG;
constexpr int OUTW  = 2048;

typedef unsigned short u16;
typedef unsigned long long u64;
typedef short bf16x8 __attribute__((ext_vector_type(8)));
typedef short bf16x4 __attribute__((ext_vector_type(4)));
typedef float f32x4 __attribute__((ext_vector_type(4)));
typedef float f32x16 __attribute__((ext_vector_type(16)));
typedef unsigned u32x4 __attribute__((ext_vector_type(4)));
typedef unsigned u32x2 __attribute__((ext_vector_type(2)));
#define LAS __attribute__((address_space(3)))
#define GAS __attribute__((address_space(1)))

constexpr size_t MB = 1u << 20;
constexpr size_t PROF_AT = 0;

__device__ __forceinline__ float bf2f(u16 b) { return __uint_as_float(((unsigned)b) << 16); }
__device__ __forceinline__ u16 f2bf(float f) { __bf16 b = (__bf16)f; return __builtin_bit_cast(u16, b); }
__device__ __forceinline__ float rbf(float f) { return bf2f(f2bf(f)); }
__device__ __forceinline__ unsigned pk2(float a, float b) {
  typedef float f2 __attribute__((ext_vector_type(2)));
  typedef __bf16 b2 __attribute__((ext_vector_type(2)));
  f2 v = {a, b}; b2 r = __builtin_convertvector(v, b2); return __builtin_bit_cast(unsigned, r);
}

__device__ __forceinline__ int opaque_tid(int wv) {
  int l;
  asm volatile("v_mbcnt_lo_u32_b32 %0, -1, 0\n\tv_mbcnt_hi_u32_b32 %0, -1, %0" : "=v"(l));
  return wv * 64 + l;
}
__device__ __forceinline__ int swz_b(int row) { return ((row & 3) << 2) | ((row >> 2) & 3); }

#define KNOB(name, v) constexpr int name = v
#define KDEF 70
KNOB(K_DEF, KDEF);
KNOB(W_STEP, 33); KNOB(W_OFF, 56);
#define RHOV 3
KNOB(RHO, RHOV);
#define CDUR 1510
#define U_SLP 8
#define U_STG 0
#define U_ST0 0
#define WSOFF 122
#define START_SLP 3
#define QK_D 0
#define A1_0 20
#define A1_D 4
#define PSEG 1
#define KQ_NOW 2
#define NOA 0
#define V_PF 3
#define V_PF0 3
#define CALN 3
#define CALM 500u
#define CALT 680u
#define KQ_D 0
#define W_NOW 4
#define U_SIG2 11
#define U_LD 5
#define U_NOW 6
#define WRONG 0
#define OUTP "sc1"
#define LDNT(p) __builtin_nontemporal_load(p)
constexpr int CHK      = 16384;
constexpr int PAGE     = 2 << 20;
constexpr int SY_UM    = 34 * PAGE;
constexpr int SY_HOT   = 32 * PAGE;
constexpr int H_MEM    = 0;
constexpr int H_P      = 16 * CHK;
constexpr int H_ANN    = 28 * CHK;
constexpr int H_DLT    = 32 * CHK;
constexpr int H_TR     = 33 * CHK;
constexpr int H_PX     = 64 * CHK;
constexpr int H_BYTES  = 128 * CHK;
constexpr int URING    = 16;
constexpr int SY_SET   = 52 * PAGE;
constexpr int SY_EPOCH = SY_SET + 128;
constexpr int SY_ABORT = SY_SET + 256;
constexpr int SY_SEL   = SY_EPOCH + 8;
constexpr int SY_CT    = SY_SET + CHK;
constexpr int CT_Q = 0, CT_K = 32, CT_U = 128;
constexpr int CT_V = 0, CT_DONE = 32, CT_A2 = 48, CT_A1 = 128, CT_TK = 132;
constexpr int CT_ARR = -124, CT_INIT = -116, CT_CAL = 256;
constexpr int SY_KV    = 48 * PAGE;
constexpr int SY_BYTES = SY_CT + (CT_CAL + 64) * 128;
constexpr unsigned SENT = 0xFFFFFFFFu;
constexpr unsigned SPIN_CAP = 1u << 19;
constexpr int NLF = 16;
constexpr int L_WTS = 0;
constexpr int L_MEM = L_WTS + 4 * NLF * 2048;
constexpr int L_XCH = L_MEM + 16384;
constexpr int L_XK  = L_XCH + 2048;
constexpr int L_KX  = L_XCH + 4096;
constexpr int L_PRF = L_KX + 8192;
constexpr int XRS = 40;
constexpr int CHAIN_LDS = 160 * 1024;
static_assert(L_XCH + 128 * XRS <= L_PRF && L_PRF + 4096 <= CHAIN_LDS, "lds");

typedef __bf16 bfv8 __attribute__((ext_vector_type(8)));
typedef __bf16 bfv16 __attribute__((ext_vector_type(16)));
typedef unsigned u32x8 __attribute__((ext_vector_type(8)));
typedef unsigned u32x12 __attribute__((ext_vector_type(12)));
typedef unsigned u32x16 __attribute__((ext_vector_type(16)));
typedef unsigned u32x32 __attribute__((ext_vector_type(32)));
struct Wts { u32x8 a[32]; u32x32 v[4]; };

#define STAMPL(i) do {} while (0)
#define PVAL(i, v) do {} while (0)
#define PDECL(base, str) do {} while (0)
#define PZERO do {} while (0)
#define PDUMP(mask) do {} while (0)
#define TA(x) do {} while (0)
#define TB(x) TS(x)
#define TS(x) do {} while (0)
#define SSTAMP(i) do {} while (0)
#define WST(c, i) do {} while (0)
#define AST(i) do {} while (0)
struct ChainArgs {
  u64* prof;
  const u16* mem0; const u16* Wq; const u16* Wk; const u16* Wv; const u16* Wo; const u16* X;
  u16* out; char* sync;
  const u16* Aq; const u16* Ak; const u16* Av; const u16* Ashift; const u16* Assm; const u16* AD;
};
__device__ __forceinline__ GAS unsigned* ctr(const ChainArgs& a, int i) { return (GAS unsigned*)(a.sync + SY_CT + i * 128); }
__device__ __forceinline__ void ctr_add(const ChainArgs& a, int i, unsigned v) {
  __hip_atomic_fetch_add(ctr(a, i), v, __ATOMIC_RELAXED, __HIP_MEMORY_SCOPE_AGENT);
}

__device__ __forceinline__ __amdgpu_buffer_rsrc_t mk_rsrc(const void* p, unsigned bytes) {
  const u64 v = (u64)p;
  const unsigned lo = (unsigned)__builtin_amdgcn_readfirstlane((unsigned)v), hi = (unsigned)__builtin_amdgcn_readfirstlane((unsigned)(v >> 32));
  const u64 u = (u64)lo | ((u64)hi << 32);
  return __builtin_amdgcn_make_buffer_rsrc((void*)u, 0, bytes, 0x00020000);
}
__device__ __forceinline__ u32x4 own_rsrc(const void* p, unsigned bytes) {
  u32x4 r = {(unsigned)__builtin_amdgcn_readfirstlane((unsigned)(u64)p), (unsigned)__builtin_amdgcn_readfirstlane((unsigned)((u64)p >> 32)) & 0xffffu, bytes, 0x00020000u};
  asm volatile("s_nop 4" : "+s"(r));
  return r;
}
constexpr int LD_AUX = 16, ST_AUX = 16;
__device__ __forceinline__ u32x4 ld16(__amdgpu_buffer_rsrc_t rs, unsigned off) {
  return __builtin_amdgcn_raw_buffer_load_b128(rs, off, 0, LD_AUX);
}
__device__ __forceinline__ void st16(__amdgpu_buffer_rsrc_t rs, unsigned off, u32x4 v) {
  __builtin_amdgcn_raw_buffer_store_b128(v, rs, off, 0, ST_AUX);
}
__device__ __forceinline__ void st16s(u32x4 v, __amdgpu_buffer_rsrc_t rs, unsigned voff, unsigned soff) {
  __builtin_amdgcn_raw_buffer_store_b128(v, rs, voff, soff, ST_AUX);
  asm volatile("s_nop 1" :: "v"(v) : "memory");
}
__device__ __forceinline__ void st8(__amdgpu_buffer_rsrc_t rs, unsigned off, u32x2 v) {
  __builtin_amdgcn_raw_buffer_store_b64(v, rs, off, 0, ST_AUX);
}
__device__ __forceinline__ void st4(__amdgpu_buffer_rsrc_t rs, unsigned off, unsigned v) {
  __builtin_amdgcn_raw_buffer_store_b32(v, rs, off, 0, ST_AUX);
}
__device__ __forceinline__ unsigned ld4(__amdgpu_buffer_rsrc_t rs, unsigned off) {
  return __builtin_amdgcn_raw_buffer_load_b32(rs, off, 0, LD_AUX);
}
constexpr int TU = 16;
__device__ __forceinline__ unsigned cyc32() { return (unsigned)__builtin_amdgcn_s_memtime(); }
__device__ __forceinline__ unsigned to_sgpr(unsigned x) {
  unsigned r;
  asm volatile("s_nop 0\n\tv_readfirstlane_b32 %0, %1\n\ts_nop 4" : "=s"(r) : "v"(x));
  return r;
}
struct Clk {
  unsigned ce, te;
  unsigned c2t, t2c;
  __device__ __forceinline__ void edge(unsigned& c, unsigned& t) {
    u64 c0, r0, r1, r2, r3, r4, r5;
    asm volatile("s_memtime %0\n\ts_memrealtime %1\n\ts_nop 4\n\ts_memrealtime %2\n\ts_nop 4\n\ts_memrealtime %3\n\ts_nop 4\n\t"
                 "s_memrealtime %4\n\ts_nop 4\n\ts_memrealtime %5\n\ts_nop 4\n\ts_memrealtime %6\n\ts_waitcnt lgkmcnt(0)"
                 : "=&s"(c0), "=&s"(r0), "=&s"(r1), "=&s"(r2), "=&s"(r3), "=&s"(r4), "=&s"(r5) :: "memory");
    const unsigned x = (unsigned)r0;
    const unsigned n = ((unsigned)r1 == x) + ((unsigned)r2 == x) + ((unsigned)r3 == x) + ((unsigned)r4 == x) + ((unsigned)r5 == x);
    c = to_sgpr((unsigned)c0 + 24u * n); t = (x + 4u) * TU;
  }
  __device__ __forceinline__ void init() { edge(ce, te); c2t = 44000u; t2c = 97600u; }
  __device__ __forceinline__ void sync() {
    unsigned c, t;
    edge(c, t);
    set(c, t);
  }
  __device__ __forceinline__ void set(unsigned c, unsigned t) {
    if (c == ce) return;
    const float q = (float)(t - te) * __builtin_amdgcn_rcpf((float)(c - ce));
    const float qc = fminf(fmaxf(q, 0.51f), 1.99f);
    c2t = to_sgpr((unsigned)(qc * 65536.f));
    t2c = to_sgpr((unsigned)(__builtin_amdgcn_rcpf(qc) * 65536.f));
    ce = c; te = t;
  }
  __device__ __forceinline__ unsigned time(unsigned c) const { return te + ((min(c - ce, 30000u) * c2t) >> 16); }
  __device__ __forceinline__ unsigned now() const { return time(cyc32()); }
  __device__ __forceinline__ unsigned target(unsigned t) const {
    return to_sgpr(ce + (((unsigned)min(max((int)(t - te), 0), 20000) * t2c) >> 16));
  }
  __device__ __forceinline__ void wait(unsigned t) const { wait_c(target(t)); }
  __device__ __forceinline__ void wait_c(unsigned c) const {
    unsigned y;
    asm volatile("1:\n\ts_memtime vcc\n\ts_waitcnt lgkmcnt(0)\n\ts_sub_i32 %0, %1, vcc_lo\n\ts_cmp_gt_i32 %0, 128\n\ts_cbranch_scc1 1b\n\t"
                 "2:\n\ts_sub_i32 %0, %0, %2\n\ts_cmp_gt_i32 %0, %3\n\ts_cbranch_scc1 2b"
                 : "=&s"(y) : "s"(c), "n"(W_STEP), "n"(W_OFF) : "memory", "scc", "vcc");
  }
};
__device__ __forceinline__ unsigned nosent(unsigned d) { return min(d, SENT - 1u); }
__device__ __forceinline__ int arow(int row, int half) { return 4 * (row >> 1) + 2 * half + (row & 1); }
__device__ __forceinline__ unsigned umax4(u32x4 v) { return max(max(v[0], v[1]), max(v[2], v[3])); }
__device__ __forceinline__ void stamp_again(GAS u64* p) {
  __hip_atomic_store(p, (__builtin_amdgcn_s_memrealtime() & ~7ull) | (__hip_atomic_load(p, __ATOMIC_RELAXED, __HIP_MEMORY_SCOPE_AGENT) & 7ull),
                     __ATOMIC_RELAXED, __HIP_MEMORY_SCOPE_AGENT);
}
__device__ __forceinline__ bool poll_fail(unsigned& spins, GAS unsigned* abortw, int lane) {
  ++spins;
  if (spins > SPIN_CAP) {
    if (lane == 0) __hip_atomic_store(abortw, 1u, __ATOMIC_RELAXED, __HIP_MEMORY_SCOPE_AGENT);
    return true;
  }
  if ((spins & 511u) == 0u && lane == 0) {
    unsigned n = 0;
    for (int i = 0; i < 8; ++i) n += __hip_atomic_load(abortw + (SY_CT - SY_ABORT + (CT_ARR + i) * 128) / 4, __ATOMIC_RELAXED, __HIP_MEMORY_SCOPE_AGENT) + 1u;
    if (n < 256u) stamp_again((GAS u64*)(abortw - 32));
  }
  if ((spins & 63u) == 0u)
    return __builtin_amdgcn_readfirstlane(__hip_atomic_load(abortw, __ATOMIC_RELAXED, __HIP_MEMORY_SCOPE_AGENT)) != SENT;
  return false;
}
#define TP(k) do {} while (0)
struct Gate {
  u64 tx; u64 entry, sel; int arr; unsigned rot; bool open;
  __device__ __forceinline__ bool look(char* sync, u32x4 v) {
    if (open) return true;
    for (unsigned spins = 1;; ++spins) {
#pragma unroll
      for (int i = 0; i < 4; ++i) v[i] = __builtin_amdgcn_readfirstlane(v[i]);
      if (((u64)v[1] << 32 | v[0]) - entry + 2000u < (1ull << 31) || spins > 4 * SPIN_CAP) break;
      if (spins > 1) __builtin_amdgcn_s_sleep(8);
      v = ld16(mk_rsrc(sync + SY_EPOCH, 16), 0);
    }
    asm volatile("" ::: "memory");
    sel = (u64)v[3] << 32 | v[2];
    const bool same = rot == (v[0] & 7u);
    rot = v[0] & 7u; open = true;
    return same;
  }
  __device__ __forceinline__ void ask(char* sync, u32x4& v) { asm volatile("s_nop 4\n\tbuffer_load_dwordx4 %0, off, %1, 0 sc1" : "=v"(v) : "s"(own_rsrc(sync + SY_EPOCH, 16)) : "memory"); }
  __device__ __forceinline__ void arrive(char* sync) {
    if (arr >= 0 && opaque_tid(0) == 0) __hip_atomic_fetch_add((GAS unsigned*)(sync + SY_CT + (CT_ARR + arr) * 128), 1u, __ATOMIC_RELAXED, __HIP_MEMORY_SCOPE_AGENT);
    arr = -1;
  }
  __device__ __forceinline__ bool pass(char* sync, u32x4 v = {0u, 0u, 0u, 0u}) { const bool r = look(sync, v); arrive(sync); return r; }
};
__device__ __forceinline__ unsigned ann_val(unsigned t, int s) { return (t << 8) | (unsigned)s; }
__device__ __forceinline__ unsigned die_half() { return ((unsigned)__builtin_amdgcn_s_getreg(20 | (3 << 11)) >> 2) & 1u; }
typedef u64 Sel;
__device__ __forceinline__ unsigned start_wait(char* sync, GAS unsigned* abortw, int lane, bool& dead, int ci, unsigned n) {
  unsigned spins = 0, v;
  const bool m = lane < 4 || ci < 0;
  while (true) {
    v = ld4(mk_rsrc(sync + SY_SET, SY_BYTES - SY_SET), lane < 6 ? CHK + (m ? CT_INIT + (lane & 3) : ci + 32 * (lane & 1)) * 128 : 0x7fffffffu) + 1u;
    if (__all(lane > 5 || v >= (m ? 16u : n))) break;
    __builtin_amdgcn_s_sleep(START_SLP);
    if (dead || poll_fail(spins, abortw, lane)) { dead = true; break; }
  }
  asm volatile("" ::: "memory");
  return min((unsigned)__builtin_amdgcn_readlane(v, 4), (unsigned)__builtin_amdgcn_readlane(v, 5)) - 1u;
}
__device__ __forceinline__ __amdgpu_buffer_rsrc_t rsrc_u(char* sync) { return mk_rsrc((void*)sync, SY_SET); }
__device__ __forceinline__ unsigned near_half(unsigned off, u64 sel) { return off + ((unsigned)(sel >> ((off >> 21) & 63)) & 1u) * 8192; }
__device__ __forceinline__ unsigned x_slot(int kind, int s, int h, Sel sel) {
  return kind == 2 ? near_half(32 * PAGE + (s * 32 + h) * CHK, ~sel) : near_half((s * 64 + 2 * h + 1 - kind) * CHK, sel);
}
__device__ __forceinline__ int ct_near(Sel sel) { return 64 * ((unsigned)(sel >> (SY_SET / PAGE)) & 1u); }
__device__ __forceinline__ unsigned kv_row(int d, Sel sel) { return near_half(SY_KV + (d >> 1) * CHK, ~sel) + (d & 1) * 4096; }
__device__ __forceinline__ int hpos(int h) { return (h & 3) * 8 + (h >> 2); }
constexpr unsigned UXH = 8 * CHK;
__device__ __forceinline__ unsigned ux_base(int s, int h, Sel sel) { return near_half(((s & (URING - 1)) * 32 + h) * UXH, ~sel); }
__device__ __forceinline__ constexpr unsigned ux_tile(int t) { return (t >> 3) * CHK + (t & 7) * 1024; }
__device__ __forceinline__ __amdgpu_buffer_rsrc_t rsrc_hot(char* sync, Sel sel) {
  return mk_rsrc((void*)(sync + near_half(SY_HOT, sel)), H_BYTES - 8192);
}
template <int SLP = 4>
__device__ __forceinline__ void ctr_wait(GAS unsigned* c, unsigned need, GAS unsigned* abortw, int lane, bool& dead) {
  unsigned spins = 0;
  while (true) {
    unsigned v = __builtin_amdgcn_readfirstlane(__hip_atomic_load(c, __ATOMIC_RELAXED, __HIP_MEMORY_SCOPE_AGENT)) + 1u;
    if (v >= need) break;
    __builtin_amdgcn_s_sleep(SLP);
    if (dead || poll_fail(spins, abortw, lane)) { dead = true; break; }
  }
}
template <int N> __device__ __forceinline__ void wait_vm() { asm volatile("s_waitcnt vmcnt(%0)" :: "n"(N) : "memory"); }
__device__ __forceinline__ void lds_barrier() {
  asm volatile("s_waitcnt lgkmcnt(0)" ::: "memory");
  __builtin_amdgcn_s_barrier();
  asm volatile("" ::: "memory");
}
__device__ __forceinline__ f32x4 vzero() { float z; asm volatile("v_mov_b32 %0, 0" : "=v"(z)); f32x4 r = {z, z, z, z}; asm volatile("" : "+v"(r)); return r; }
__device__ __forceinline__ f32x4 smf(u32x4 a, u32x8 b, f32x4 c, int idx) {
  return __builtin_amdgcn_smfmac_f32_16x16x64_bf16(__builtin_bit_cast(bfv8, a), __builtin_bit_cast(bfv16, b), c, idx, 0, 0);
}
__device__ __forceinline__ float red16max(float x) { auto r = __builtin_amdgcn_permlane16_swap(__float_as_uint(x), __float_as_uint(x), false, false); return fmaxf(__uint_as_float(r[0]), __uint_as_float(r[1])); }
__device__ __forceinline__ float red32max(float x) { auto r = __builtin_amdgcn_permlane32_swap(__float_as_uint(x), __float_as_uint(x), false, false); return fmaxf(__uint_as_float(r[0]), __uint_as_float(r[1])); }
__device__ __forceinline__ float red16add(float x) { auto r = __builtin_amdgcn_permlane16_swap(__float_as_uint(x), __float_as_uint(x), false, false); return __uint_as_float(r[0]) + __uint_as_float(r[1]); }
__device__ __forceinline__ float red32add(float x) { auto r = __builtin_amdgcn_permlane32_swap(__float_as_uint(x), __float_as_uint(x), false, false); return __uint_as_float(r[0]) + __uint_as_float(r[1]); }
constexpr bool blk_in_lds(int R, int b) {
  return R == 0 ? ((b & 7) == 1 || (b & 7) == 4) : (b < 16 ? (b & 3) == 0 : (b & 3) == 3);
}
constexpr int lds_index(int R, int b) { int n = 0; for (int i = 0; i < b; ++i) if (blk_in_lds(R, i)) ++n; return n; }
constexpr bool frag_in_lds(int R, int f) { return blk_in_lds(R, f >> 1); }
constexpr int frag_reg_slot(int R, int f) { int n = 0; for (int i = 0; i < f; ++i) if (!frag_in_lds(R, i)) ++n; return n; }

constexpr int m_block(int R, int m, int i) {
  return R == 0 ? 2 * (i < 12 ? (m ? i + i / 3 + 1 : i + (i + 1) / 3) : 4 * (i - 12) + 2 - 2 * m) + m : m ? 16 + 4 * (i & 3) + (i >> 2) : (i < 12 ? i + i / 3 + 1 : 4 * (i - 12));
}
#define QK_REV 0
#define W_BV 0xba9876543210fedcull
#define VO_ORD 3
constexpr int w_block(int R, int i) {
  if (R && VO_ORD == 2) return i < 12 ? m_block(1, 0, 11 - i) : m_block(1, 1, i - 12);
  if (R && VO_ORD == 3) return i & 1 ? m_block(1, 1, i >> 1) : m_block(1, 0, 11 - (i >> 1));
  if (!R && QK_REV) return m_block(0, i & 1, 11 - (i >> 1));
  return R && VO_ORD ? m_block(R, i < 12, i % 12) : m_block(R, i & 1, i >> 1);
}
#define W_BG 0xfedcba9876543210ull
#define W_NT 0
#define U_ORD 0x3210
constexpr u64 bg_order(int m) { u64 r = 0; for (int n = 0; n < 16; ++n) r |= (u64)(m_block(0, m > 0, ((m == 2 ? W_BV : W_BG) >> (4 * n)) & 15) >> 1) << (4 * n); return r; }
struct WSched { int ord[32]; int slot[32]; int issue[32]; int out[32]; int reg[32]; };
constexpr WSched make_wsched(int R) {
  WSched S{};
  int n = 0;
  for (int i = 0; i < 24; ++i) S.ord[n++] = w_block(R, i);
  for (int i = 0; i < n; ++i) S.reg[i] = frag_reg_slot(R, 2 * S.ord[i]);
  const int npure = n;
  for (int b = 0; b < 32; ++b) if (blk_in_lds(R, b)) S.ord[n++] = b;
  int seq[32] = {};
  int nissued = 0, next = 0;
  for (int i = 0; i < 10; ++i) { S.slot[i] = (i + 10 - npure % 10) % 10; S.issue[i] = -1; seq[i] = nissued++; }
  next = 10;
  S.out[0] = nissued - 1;
  for (int c = 0; c <= npure; ++c) {
    if (c >= 1) {
      const int k = S.slot[c - 1];
      if (next < npure) { S.slot[next] = k; S.issue[next] = c; seq[next] = nissued++; ++next; }
      else if (k < NLF / 2) { S.slot[npure + k] = k; S.issue[npure + k] = c; seq[npure + k] = nissued++; }
    }
    if (c + 1 < npure) S.out[c + 1] = nissued - 1 - seq[c + 1];
  }
  return S;
}
__device__ __forceinline__ int kcol(int k) { return 8 * ((k >> 1) & 1) + ((k & 1) ? 7 - (k >> 2) : k >> 2); }
template <bool PERM = false>
__device__ __forceinline__ u32x8 tr_frag(const char* sb, int tile, int lane) {
  u32x8 f;
#pragma unroll
  for (int q = 0; q < 4; ++q) {
    int row = (q >> 1) * 32 + 8 * (lane >> 4) + 4 * (q & 1) + ((lane & 15) >> 2);
    if (PERM) row = (row & ~15) + kcol(row & 15);
    int p = lane & 3;
    int lc = 2 * tile + (p >> 1);
    int addr = row * 256 + ((lc ^ swz_b(row)) * 16) + 8 * (p & 1);
    bf16x4 v = __builtin_amdgcn_ds_read_tr16_b64_v4i16((LAS bf16x4*)(sb + addr));
    u32x2 u = __builtin_bit_cast(u32x2, v);
    f[2 * q] = u[0]; f[2 * q + 1] = u[1];
  }
  return f;
}
__device__ __forceinline__ void dma16(__amdgpu_buffer_rsrc_t rs, unsigned voff, unsigned soff, unsigned lds, bool nt = false, bool pad = true) {
  if (nt) asm volatile("s_mov_b32 m0, %0\n\ts_nop 3\n\tbuffer_load_dwordx4 %1, %2, %3 offen nt lds" :: "s"(lds), "v"(voff), "s"(rs), "s"(soff) : "m0", "memory");
  else if (pad) asm volatile("s_mov_b32 m0, %0\n\ts_nop 3\n\tbuffer_load_dwordx4 %1, %2, %3 offen lds" :: "s"(lds), "v"(voff), "s"(rs), "s"(soff) : "m0", "memory");
  else asm volatile("s_mov_b32 m0, %0\n\ts_nop 0\n\tbuffer_load_dwordx4 %1, %2, %3 offen lds" :: "s"(lds), "v"(voff), "s"(rs), "s"(soff) : "m0", "memory");
}
__device__ __forceinline__ u32x8 tr_frag2(const char* sb, int tile, int f, int lane) {
  u32x8 r;
#pragma unroll
  for (int c = 0; c < 2; ++c)
#pragma unroll
    for (int t = 0; t < 2; ++t) {
      const int row = 32 * c + 16 * f + 4 * (lane >> 4) + ((lane & 15) >> 2);
      const int p = lane & 3, lc = 2 * (tile + t) + (p >> 1);
      const u32x2 u = __builtin_bit_cast(u32x2, __builtin_amdgcn_ds_read_tr16_b64_v4i16((LAS bf16x4*)(sb + row * 256 + ((lc ^ swz_b(row)) * 16) + 8 * (p & 1))));
      r[4 * c + t] = u[0]; r[4 * c + 2 + t] = u[1];
    }
  return r;
}
template <int ROLE>
__device__ __forceinline__ void load_weights(const ChainArgs& a, int h, int w, int lane, char* smem, Wts& W, Gate& G, u32x4& gv) {
#pragma unroll
  for (int i = 0; i < 4; ++i) asm volatile("" : "=v"(W.v[i]));
#pragma unroll
  for (int i = 0; i < 32; ++i) asm volatile("" : "=a"(W.a[i]));
  constexpr WSched S = make_wsched(ROLE);
  constexpr int NP = 32 - NLF / 2;
  const __amdgpu_buffer_rsrc_t r0 = mk_rsrc(ROLE == 0 ? a.Wq : a.Wv, 8u << 20), r1 = mk_rsrc(ROLE == 0 ? a.Wk : a.Wo, 8u << 20);
  const unsigned lds0 = (unsigned)(uintptr_t)(LAS char*)smem;
  unsigned vo[2][4];
#pragma unroll
  for (int j = 0; j < 4; ++j) {
    const int row = (w * 4 + j) * 4 + (lane >> 4), lc = (lane & 15) ^ swz_b(row);
    vo[0][j] = row * HD * 2 + lc * 16; vo[1][j] = row * DM * 2 + lc * 16;
  }
  asm volatile("s_nop 4");
  auto stage = [&](int i) {
    const int blk = S.ord[i];
    const bool second = ROLE == 0 ? (blk & 1) : blk >= 16, wo = ROLE == 1 && second;
    const unsigned so = wo ? (unsigned)(((128 * h + 64 * (blk & 1)) * DM + 128 * (((blk - 16) >> 1) ^ (2 * (h & 3)))) * 2)
                           : (unsigned)((64 * ((ROLE == 0 ? blk >> 1 : blk) ^ (h & RHO)) * HD + 128 * h) * 2);
#pragma unroll
    for (int j = 0; j < 4; ++j) dma16(second ? r1 : r0, vo[wo][j], so, lds0 + 16384 * S.slot[i] + (w * 4 + j) * 1024, W_NT & (ROLE ? 8 : 1));
  };
#pragma unroll
  for (int i = 0; i < 32; ++i) if (S.issue[i] == -1) stage(i);
#pragma unroll
  for (int c = 0; c < NP; ++c) {
    asm volatile("s_waitcnt vmcnt(%0)" :: "n"(4 * S.out[c]) : "memory");
    lds_barrier();
#pragma unroll
    for (int i = 0; i < 32; ++i) if (S.issue[i] == c) stage(i);
    const char* sb = smem + 16384 * S.slot[c];
#pragma unroll
    for (int t = 0; t < 2; ++t) {
      u32x8 f = (ROLE == 1 && S.ord[c] >= 16) ? tr_frag2(sb, 2 * w, t, lane) : tr_frag<true>(sb, 2 * w + t, lane);
      if (S.reg[c] < 32) {
#pragma unroll
        for (int i = 0; i < 8; ++i) asm volatile("v_accvgpr_write_b32 a[%1], %0" :: "v"(f[i]), "n"(8 * ((S.reg[c] + t) & 31) + i));
      }
      else {
#pragma unroll
        for (int i = 0; i < 8; ++i) W.v[(S.reg[c] >> 2) & 3][8 * ((S.reg[c] + t) & 3) + i] = f[i];
      }
    }
    if (S.reg[c] >= 32) asm volatile("" : "+v"(W.v[(S.reg[c] >> 2) & 3]));
  }
  static_assert(S.issue[31] < NP && S.issue[NP] < S.issue[31], "resident blocks are issued in slot order inside the loop");
  G.ask(a.sync, gv);
#pragma unroll
  for (int j = 0; j < NLF / 2; ++j) {
    asm volatile("s_waitcnt vmcnt(%0)" :: "n"(4 * (NLF / 2 - 1 - j) + 1) : "memory");
    lds_barrier();
    const bool two = ROLE == 1 && S.ord[NP + j] >= 16;
    u32x8 f0 = two ? tr_frag2(smem + 16384 * j, 2 * w, 0, lane) : tr_frag<true>(smem + 16384 * j, 2 * w, lane);
    u32x8 f1 = two ? tr_frag2(smem + 16384 * j, 2 * w, 1, lane) : tr_frag<true>(smem + 16384 * j, 2 * w + 1, lane);
    lds_barrier();
    char* d = smem + 16384 * j + w * 4096 + lane * 16;
    *(u32x4*)d = (u32x4){f0[0], f0[1], f0[2], f0[3]};
    *(u32x4*)(d + 1024) = (u32x4){f0[4], f0[5], f0[6], f0[7]};
    *(u32x4*)(d + 2048) = (u32x4){f1[0], f1[1], f1[2], f1[3]};
    *(u32x4*)(d + 3072) = (u32x4){f1[4], f1[5], f1[6], f1[7]};
  }
  asm volatile("s_waitcnt vmcnt(0)" : "+v"(gv) :: "memory");
  lds_barrier();
}
template <int... Is> struct iseq {};
template <int N, int... Is> struct mkseq : mkseq<N - 1, N - 1, Is...> {};
template <int... Is> struct mkseq<0, Is...> { typedef iseq<Is...> type; };

#define SB0 __builtin_amdgcn_sched_barrier(0)
#define CAT8(x, y) ((u32x8){x[0], x[1], x[2], x[3], y[0], y[1], y[2], y[3]})
template <class F, int... I>
__device__ __forceinline__ void for_slots(iseq<I...>, F f) { (f(std::integral_constant<int, I>{}), ...); }
#define QK_BODY \
  "s_cmp_eq_u32 %[wv], 1\n\t" \
  "s_cbranch_scc1 91f\n\t" \
  "s_cmp_eq_u32 %[wv], 2\n\t" \
  "s_cbranch_scc1 92f\n\t" \
  "s_cmp_eq_u32 %[wv], 3\n\t" \
  "s_cbranch_scc1 93f\n\t" \
  "90:\n\t" \
  "s_mov_b32 s58, s42\n\t" \
  "20:\n\t" \
  "s_and_b32 s76, s44, 7\n\t" \
  "s_lshl_b32 s76, s76, 15\n\t" \
  "s_add_i32 s40, s54, s76\n\t" \
  "s_and_b32 s76, s44, 3\n\t" \
  "s_lshl_b32 s45, s76, 14\n\t" \
  "s_mul_i32 s47, s76, 0xc000\n\t" \
  "s_add_i32 s47, s47, 0x40000\n\t" \
  "s_add_i32 s49, s44, -1\n\t" \
  "s_and_b32 s48, s49, 2\n\t" \
  "s_lshl_b32 s48, s48, 13\n\t" \
  "s_and_b32 s76, s49, 1\n\t" \
  "s_lshl_b32 s76, s76, 12\n\t" \
  "s_add_i32 s48, s48, s76\n\t" \
  "s_lshl_b32 s50, s53, 8\n\t" \
  "s_mov_b32 s59, 0\n\t" \
  "s_mov_b32 s92, s51\n\t" \
  "s_mov_b32 s93, s53\n\t" \
  "s_xor_b32 s76, s40, 0x400\n\t" \
  "s_xor_b32 s77, s40, 0x800\n\t" \
  "s_xor_b32 s78, s40, 0xc00\n\t" \
  "s_cmp_lg_u32 s58, 0\n\t" \
  "s_cbranch_scc1 11f\n\t" \
  "10:\n\t" \
  "ds_read_b128 v[48:51], v102 offset:0\n\t" \
  "ds_read_b128 v[52:55], v102 offset:1024\n\t" \
  "ds_read_b128 v[56:59], v102 offset:2048\n\t" \
  "ds_read_b128 v[60:63], v102 offset:3072\n\t" \
  "ds_read_b128 v[16:19], v102 offset:16384\n\t" \
  "ds_read_b128 v[20:23], v102 offset:17408\n\t" \
  "ds_read_b128 v[24:27], v102 offset:18432\n\t" \
  "ds_read_b128 v[28:31], v102 offset:19456\n\t" \
  "ds_read_b128 v[84:87], v102 offset:49152\n\t" \
  "ds_read_b128 v[88:91], v102 offset:50176\n\t" \
  "ds_read_b128 v[92:95], v102 offset:51200\n\t" \
  "ds_read_b128 v[96:99], v102 offset:52224\n\t" \
  "s_waitcnt lgkmcnt(4)\n\t" \
  "ds_read_b128 v[0:3], v103 offset:16384\n\t" \
  "ds_read_b128 v[4:7], v103 offset:17408\n\t" \
  "ds_read_b128 v[8:11], v103 offset:18432\n\t" \
  "ds_read_b128 v[12:15], v103 offset:19456\n\t" \
  "46:\n\t" \
  "s_bitcmp1_b32 s58, 1\n\t" \
  "s_cselect_b64 s[80:81], -1, 0\n\t" \
  "s_cmp_lg_u32 s58, 0\n\t" \
  "s_cbranch_scc1 17f\n\t" \
  "s_mov_b64 exec, 0xff\n\t" \
  "16:\n\t" \
  "s_memtime vcc\n\t" \
  "s_waitcnt lgkmcnt(0)\n\t" \
  "s_sub_i32 s79, s41, vcc_lo\n\t" \
  "s_cmp_gt_i32 s79, 64\n\t" \
  "s_cbranch_scc1 16b\n\t" \
  "13:\n\t" \
  "buffer_load_dword v116, v112, %[rh], s48 offen sc1\n\t" \
  "s_memtime vcc\n\t" \
  "s_memrealtime s[82:83]\n\t" \
  "s_nop 4\n\t" \
  "s_memrealtime s[84:85]\n\t" \
  "s_nop 4\n\t" \
  "s_memrealtime s[56:57]\n\t" \
  "s_nop 4\n\t" \
  "s_memrealtime s[86:87]\n\t" \
  "s_nop 4\n\t" \
  "s_memrealtime s[88:89]\n\t" \
  "s_nop 4\n\t" \
  "s_memrealtime s[90:91]\n\t" \
  "s_mov_b32 s92, 0\n\t" \
  "s_waitcnt lgkmcnt(0)\n\t" \
  "s_cmp_eq_u32 s82, s84\n\t" \
  "s_addc_u32 s92, s92, 0\n\t" \
  "s_cmp_eq_u32 s82, s56\n\t" \
  "s_addc_u32 s92, s92, 0\n\t" \
  "s_cmp_eq_u32 s82, s86\n\t" \
  "s_addc_u32 s92, s92, 0\n\t" \
  "s_cmp_eq_u32 s82, s88\n\t" \
  "s_addc_u32 s92, s92, 0\n\t" \
  "s_cmp_eq_u32 s82, s90\n\t" \
  "s_addc_u32 s92, s92, 0\n\t" \
  "s_mul_i32 s92, s92, 24\n\t" \
  "s_add_i32 s92, s92, vcc_lo\n\t" \
  "s_lshl4_add_u32 s93, s82, 64\n\t" \
  "s_lshl_b32 s50, s93, 8\n\t" \
  "s_waitcnt vmcnt(0)\n\t" \
  "15:\n\t" \
  "v_mov_b32 v117, 20000\n\t" \
  "v_cmp_ne_u32_sdwa vcc, v116, s49 src0_sel:BYTE_0 src1_sel:DWORD\n\t" \
  "v_subrev_u32 v116, s50, v116\n\t" \
  "v_ashrrev_i32 v116, 8, v116\n\t" \
  "s_nop 1\n\t" \
  "v_max_i32_dpp v116, v116, v116 quad_perm:[1,0,3,2] row_mask:0xf bank_mask:0xf\n\t" \
  "s_cbranch_vccnz 14f\n\t" \
  "s_nop 0\n\t" \
  "v_max_i32_dpp v116, v116, v116 quad_perm:[2,3,0,1] row_mask:0xf bank_mask:0xf\n\t" \
  "s_nop 1\n\t" \
  "v_max_i32_dpp v116, v116, v116 row_half_mirror row_mask:0xf bank_mask:0xf\n\t" \
  "v_med3_i32 v116, v116, 0, v117\n\t" \
  "v_mul_u32_u24 v116, s52, v116\n\t" \
  "v_lshrrev_b32 v116, 16, v116\n\t" \
  "s_mov_b64 exec, -1\n\t" \
  "v_readfirstlane_b32 s84, v116\n\t" \
  "s_add_i32 s84, s84, s92\n\t" \
  ".p2align 6\n\t" \
  "s_nop 0\n\t" \
  "s_nop 0\n\t" \
  "s_nop 0\n\t" \
  "s_nop 0\n\t" \
  "s_nop 0\n\t" \
  "s_nop 0\n\t" \
  "s_nop 0\n\t" \
  "s_nop 0\n\t" \
  "1:\n\t" \
  "s_memtime vcc\n\t" \
  "s_waitcnt lgkmcnt(0)\n\t" \
  "s_sub_i32 s79, s84, vcc_lo\n\t" \
  "s_cmp_gt_i32 s79, 128\n\t" \
  "s_cbranch_scc1 1b\n\t" \
  ".p2align 4\n\t" \
  "2:\n\t" \
  "s_sub_i32 s79, s79, 33\n\t" \
  "s_cmp_gt_i32 s79, 56\n\t" \
  "s_cbranch_scc1 2b\n\t" \
  "buffer_load_dwordx4 v[32:35], v100, %[rh], s40 offen sc1\n\t" \
  "s_nop 12\n\t" \
  "buffer_load_dwordx4 v[36:39], v100, %[rh], s76 offen sc1\n\t" \
  "s_nop 12\n\t" \
  "buffer_load_dwordx4 v[40:43], v100, %[rh], s77 offen sc1\n\t" \
  "s_nop 12\n\t" \
  "buffer_load_dwordx4 v[44:47], v100, %[rh], s78 offen sc1\n\t" \
  "s_memtime vcc\n\t" \
  "s_memrealtime s[82:83]\n\t" \
  "s_nop 4\n\t" \
  "s_memrealtime s[84:85]\n\t" \
  "s_nop 4\n\t" \
  "s_memrealtime s[56:57]\n\t" \
  "s_nop 4\n\t" \
  "s_memrealtime s[86:87]\n\t" \
  "s_nop 4\n\t" \
  "s_memrealtime s[88:89]\n\t" \
  "s_nop 4\n\t" \
  "s_memrealtime s[90:91]\n\t" \
  "s_mov_b32 s92, 0\n\t" \
  "s_waitcnt lgkmcnt(0)\n\t" \
  "s_cmp_eq_u32 s82, s84\n\t" \
  "s_addc_u32 s92, s92, 0\n\t" \
  "s_cmp_eq_u32 s82, s56\n\t" \
  "s_addc_u32 s92, s92, 0\n\t" \
  "s_cmp_eq_u32 s82, s86\n\t" \
  "s_addc_u32 s92, s92, 0\n\t" \
  "s_cmp_eq_u32 s82, s88\n\t" \
  "s_addc_u32 s92, s92, 0\n\t" \
  "s_cmp_eq_u32 s82, s90\n\t" \
  "s_addc_u32 s92, s92, 0\n\t" \
  "s_mul_i32 s92, s92, 24\n\t" \
  "s_add_i32 s92, s92, vcc_lo\n\t" \
  "s_lshl4_add_u32 s93, s82, 64\n\t" \
  "3:\n\t" \
  "s_sub_i32 s84, s92, s43\n\t" \
  "s_lshl_b32 s85, s93, 8\n\t" \
  "s_or_b32 s85, s85, s44\n\t" \
  "v_mov_b32 v118, s85\n\t" \
  "v_mov_b64 v[68:69], 0\n\t" \
  "v_mov_b64 v[70:71], 0\n\t" \
  "v_mov_b64 v[72:73], 0\n\t" \
  "v_mov_b64 v[74:75], 0\n\t" \
  "v_mov_b64 v[76:77], 0\n\t" \
  "v_mov_b64 v[78:79], 0\n\t" \
  "v_mov_b64 v[80:81], 0\n\t" \
  "v_mov_b64 v[82:83], 0\n\t" \
  "s_waitcnt lgkmcnt(0)\n\t" \
  "s_waitcnt vmcnt(3)\n\t" \
  "v_smfmac_f32_16x16x64_bf16 v[68:71], v[32:35], a[0:7], v104\n\t" \
  "ds_write_b128 v101, v[32:35] offset:0\n\t" \
  "v_smfmac_f32_16x16x64_bf16 v[72:75], v[32:35], a[8:15], v104\n\t" \
  "v_smfmac_f32_16x16x64_bf16 v[76:79], v[32:35], v[48:55], v104\n\t" \
  "v_smfmac_f32_16x16x64_bf16 v[80:83], v[32:35], v[56:63], v104\n\t" \
  "s_waitcnt vmcnt(2)\n\t" \
  "v_smfmac_f32_16x16x64_bf16 v[68:71], v[36:39], a[16:23], v104\n\t" \
  "ds_write_b128 v101, v[36:39] offset:1024\n\t" \
  "v_smfmac_f32_16x16x64_bf16 v[72:75], v[36:39], a[24:31], v104\n\t" \
  "v_smfmac_f32_16x16x64_bf16 v[76:79], v[36:39], a[32:39], v104\n\t" \
  "v_cmp_u_f32 vcc, v68, v69\n\t" \
  "v_smfmac_f32_16x16x64_bf16 v[80:83], v[36:39], a[40:47], v104\n\t" \
  "v_cmp_u_f32 s[82:83], v70, v71\n\t" \
  "s_waitcnt vmcnt(1)\n\t" \
  "v_smfmac_f32_16x16x64_bf16 v[68:71], v[40:43], v[16:23], v104\n\t" \
  "ds_write_b128 v101, v[40:43] offset:2048\n\t" \
  "v_smfmac_f32_16x16x64_bf16 v[72:75], v[40:43], v[24:31], v104\n\t" \
  "s_or_b64 vcc, vcc, s[82:83]\n\t" \
  "s_or_b64 vcc, vcc, s[80:81]\n\t" \
  "s_waitcnt lgkmcnt(1)\n\t" \
  "v_smfmac_f32_16x16x64_bf16 v[76:79], v[40:43], a[48:55], v104\n\t" \
  "s_cbranch_scc1 8f\n\t" \
  "5:\n\t" \
  "s_barrier\n\t" \
  "4:\n\t" \
  "s_memtime s[56:57]\n\t" \
  "ds_read_b128 v[48:51], v101 offset:4096\n\t" \
  "v_smfmac_f32_16x16x64_bf16 v[80:83], v[40:43], a[56:63], v104\n\t" \
  "ds_read_b128 v[52:55], v101 offset:5120\n\t" \
  "ds_read_b128 v[56:59], v101 offset:8192\n\t" \
  "s_waitcnt vmcnt(0)\n\t" \
  "v_smfmac_f32_16x16x64_bf16 v[68:71], v[44:47], a[64:71], v104\n\t" \
  "ds_write_b128 v101, v[44:47] offset:3072\n\t" \
  "v_smfmac_f32_16x16x64_bf16 v[72:75], v[44:47], a[72:79], v104\n\t" \
  "v_smfmac_f32_16x16x64_bf16 v[76:79], v[44:47], a[80:87], v104\n\t" \
  "v_cmp_u_f32 vcc, v68, v69\n\t" \
  "v_smfmac_f32_16x16x64_bf16 v[80:83], v[44:47], a[88:95], v104\n\t" \
  "v_cmp_u_f32 s[82:83], v70, v71\n\t" \
  "s_waitcnt lgkmcnt(3)\n\t" \
  "v_smfmac_f32_16x16x64_bf16 v[68:71], v[48:51], a[96:103], v104\n\t" \
  "v_smfmac_f32_16x16x64_bf16 v[72:75], v[48:51], a[104:111], v104\n\t" \
  "s_or_b64 vcc, vcc, s[82:83]\n\t" \
  "s_waitcnt lgkmcnt(0)\n\t" \
  "v_smfmac_f32_16x16x64_bf16 v[68:71], v[52:55], a[112:119], v104\n\t" \
  "s_cbranch_scc1 9f\n\t" \
  "6:\n\t" \
  "s_barrier\n\t" \
  "ds_read_b128 v[60:63], v101 offset:9216\n\t" \
  "v_smfmac_f32_16x16x64_bf16 v[72:75], v[52:55], a[120:127], v104\n\t" \
  "ds_read_b128 v[64:67], v101 offset:12288\n\t" \
  "s_sub_i32 s79, s56, s84\n\t" \
  "s_mul_hi_u32 s79, s79, s46\n\t" \
  "v_smfmac_f32_16x16x64_bf16 v[68:71], v[56:59], a[192:199], v104\n\t" \
  "v_lshl_add_u32 v116, s79, 9, v118\n\t" \
  "buffer_store_dword v116, v109, %[rh], s45 offen sc1\n\t" \
  "v_smfmac_f32_16x16x64_bf16 v[72:75], v[56:59], a[200:207], v104\n\t" \
  "ds_read_b128 v[48:51], v101 offset:13312\n\t" \
  "s_waitcnt lgkmcnt(2)\n\t" \
  "v_smfmac_f32_16x16x64_bf16 v[68:71], v[60:63], a[208:215], v104\n\t" \
  "ds_read_b128 v[52:55], v101 offset:6144\n\t" \
  "v_smfmac_f32_16x16x64_bf16 v[72:75], v[60:63], a[216:223], v104\n\t" \
  "ds_read_b128 v[56:59], v101 offset:7168\n\t" \
  "s_waitcnt lgkmcnt(3)\n\t" \
  "v_smfmac_f32_16x16x64_bf16 v[68:71], v[64:67], v[152:159], v104\n\t" \
  "ds_read_b128 v[60:63], v101 offset:10240\n\t" \
  "v_smfmac_f32_16x16x64_bf16 v[72:75], v[64:67], v[160:167], v104\n\t" \
  "ds_read_b128 v[32:35], v101 offset:20480\n\t" \
  "s_waitcnt lgkmcnt(4)\n\t" \
  "v_smfmac_f32_16x16x64_bf16 v[68:71], v[48:51], v[168:175], v104\n\t" \
  "ds_read_b128 v[64:67], v101 offset:11264\n\t" \
  "ds_read_b128 v[16:19], v103 offset:49152\n\t" \
  "v_smfmac_f32_16x16x64_bf16 v[72:75], v[48:51], v[176:183], v104\n\t" \
  "ds_read_b128 v[36:39], v101 offset:21504\n\t" \
  "s_waitcnt lgkmcnt(6)\n\t" \
  "v_smfmac_f32_16x16x64_bf16 v[68:71], v[52:55], v[84:91], v104\n\t" \
  "ds_read_b128 v[48:51], v101 offset:15360\n\t" \
  "ds_read_b128 v[20:23], v103 offset:50176\n\t" \
  "v_smfmac_f32_16x16x64_bf16 v[72:75], v[52:55], v[92:99], v104\n\t" \
  "ds_read_b128 v[40:43], v101 offset:22528\n\t" \
  "s_waitcnt lgkmcnt(8)\n\t" \
  "v_smfmac_f32_16x16x64_bf16 v[68:71], v[56:59], a[160:167], v104\n\t" \
  "ds_read_b128 v[52:55], v101 offset:14336\n\t" \
  "ds_read_b128 v[24:27], v103 offset:51200\n\t" \
  "v_smfmac_f32_16x16x64_bf16 v[72:75], v[56:59], a[168:175], v104\n\t" \
  "ds_read_b128 v[28:31], v103 offset:52224\n\t" \
  "s_waitcnt lgkmcnt(10)\n\t" \
  "v_smfmac_f32_16x16x64_bf16 v[68:71], v[60:63], v[0:7], v104\n\t" \
  "ds_read_b128 v[44:47], v101 offset:23552\n\t" \
  "v_smfmac_f32_16x16x64_bf16 v[72:75], v[60:63], v[8:15], v104\n\t" \
  "v_mov_b64 v[92:93], 0\n\t" \
  "s_waitcnt lgkmcnt(9)\n\t" \
  "v_smfmac_f32_16x16x64_bf16 v[68:71], v[64:67], v[120:127], v104\n\t" \
  "ds_read_b128 v[0:3], v101 offset:24576\n\t" \
  "v_smfmac_f32_16x16x64_bf16 v[72:75], v[64:67], v[128:135], v104\n\t" \
  "ds_read_b128 v[4:7], v101 offset:25600\n\t" \
  "s_waitcnt lgkmcnt(8)\n\t" \
  "v_smfmac_f32_16x16x64_bf16 v[68:71], v[48:51], v[216:223], v104\n\t" \
  "v_mov_b64 v[94:95], 0\n\t" \
  "s_waitcnt lgkmcnt(5)\n\t" \
  "v_smfmac_f32_16x16x64_bf16 v[68:71], v[52:55], v[16:23], v104\n\t" \
  "ds_read_b128 v[8:11], v101 offset:26624\n\t" \
  "v_smfmac_f32_16x16x64_bf16 v[72:75], v[48:51], v[224:231], v104\n\t" \
  "ds_read_b128 v[12:15], v101 offset:27648\n\t" \
  "s_waitcnt lgkmcnt(5)\n\t" \
  "v_smfmac_f32_16x16x64_bf16 v[72:75], v[52:55], v[24:31], v104\n\t" \
  "v_add_f32 v96, v68, v70\n\t" \
  "v_smfmac_f32_16x16x64_bf16 v[76:79], v[56:59], a[176:183], v104\n\t" \
  "v_add_f32 v97, v69, v71\n\t" \
  "v_cvt_pk_bf16_f32 v96, v96, v97\n\t" \
  "v_smfmac_f32_16x16x64_bf16 v[80:83], v[56:59], a[184:191], v104\n\t" \
  "v_add_f32 v98, v72, v74\n\t" \
  "v_add_f32 v99, v73, v75\n\t" \
  "v_smfmac_f32_16x16x64_bf16 v[76:79], v[60:63], a[240:247], v104\n\t" \
  "v_cvt_pk_bf16_f32 v98, v98, v99\n\t" \
  "ds_write2st64_b32 v105, v96, v98 offset1:1\n\t" \
  "v_smfmac_f32_16x16x64_bf16 v[80:83], v[60:63], a[248:255], v104\n\t" \
  "ds_read_b128 v[56:59], v101 offset:9216\n\t" \
  "v_mov_b64 v[68:69], 0\n\t" \
  "v_smfmac_f32_16x16x64_bf16 v[76:79], v[64:67], v[136:143], v104\n\t" \
  "ds_read_b128 v[60:63], v101 offset:13312\n\t" \
  "v_mov_b64 v[70:71], 0\n\t" \
  "v_smfmac_f32_16x16x64_bf16 v[80:83], v[64:67], v[144:151], v104\n\t" \
  "v_smfmac_f32_16x16x64_bf16 v[76:79], v[48:51], v[232:239], v104\n\t" \
  "s_waitcnt lgkmcnt(2)\n\t" \
  "s_barrier\n\t" \
  "ds_read_b64_tr_b16 v[84:85], v107 offset:0\n\t" \
  "v_smfmac_f32_16x16x64_bf16 v[80:83], v[48:51], v[240:247], v104\n\t" \
  "ds_read_b64_tr_b16 v[86:87], v107 offset:128\n\t" \
  "ds_read_b64_tr_b16 v[88:89], v107 offset:1024\n\t" \
  "v_smfmac_f32_16x16x64_bf16 v[76:79], v[52:55], v[200:207], v104\n\t" \
  "ds_read_b64_tr_b16 v[90:91], v107 offset:1152\n\t" \
  "v_smfmac_f32_16x16x64_bf16 v[80:83], v[52:55], v[208:215], v104\n\t" \
  "ds_read_b128 v[64:67], v101 offset:6144\n\t" \
  "s_waitcnt lgkmcnt(5)\n\t" \
  "v_smfmac_f32_16x16x64_bf16 v[76:79], v[60:63], v[184:191], v104\n\t" \
  "ds_read_b128 v[52:55], v101 offset:5120\n\t" \
  "s_waitcnt lgkmcnt(4)\n\t" \
  "v_smfmac_f32_16x16x64_bf16 v[92:95], v[84:87], v[32:39], v104\n\t" \
  "s_waitcnt lgkmcnt(2)\n\t" \
  "v_smfmac_f32_16x16x64_bf16 v[92:95], v[88:91], v[40:47], v104\n\t" \
  "v_smfmac_f32_16x16x64_bf16 v[68:71], v[84:87], v[0:7], v104\n\t" \
  "v_smfmac_f32_16x16x64_bf16 v[68:71], v[88:91], v[8:15], v104\n\t" \
  "v_add_f32 v92, v92, v94\n\t" \
  "v_add_f32 v94, v93, v95\n\t" \
  "v_smfmac_f32_16x16x64_bf16 v[80:83], v[60:63], v[192:199], v104\n\t" \
  "v_mov_b32 v97, v94\n\t" \
  "v_mov_b32_dpp v94, v92 row_ror:8 row_mask:0xf bank_mask:0x3\n\t" \
  "s_nop 0\n\t" \
  "v_mov_b32_dpp v92, v97 row_ror:8 row_mask:0xf bank_mask:0xc\n\t" \
  "s_waitcnt lgkmcnt(1)\n\t" \
  "v_smfmac_f32_16x16x64_bf16 v[76:79], v[64:67], a[144:151], v104\n\t" \
  "v_add_f32 v93, v68, v70\n\t" \
  "v_add_f32 v95, v69, v71\n\t" \
  "v_max_f32 v98, v92, v94\n\t" \
  "v_mov_b32 v97, v95\n\t" \
  "v_mov_b32_dpp v95, v93 row_ror:8 row_mask:0xf bank_mask:0x3\n\t" \
  "s_nop 0\n\t" \
  "v_mov_b32_dpp v93, v97 row_ror:8 row_mask:0xf bank_mask:0xc\n\t" \
  "v_max3_f32 v98, v98, v93, v95\n\t" \
  "v_smfmac_f32_16x16x64_bf16 v[80:83], v[64:67], a[152:159], v104\n\t" \
  "s_nop 0\n\t" \
  "v_max_f32_dpp v98, v98, v98 quad_perm:[1,0,3,2] row_mask:0xf bank_mask:0xf\n\t" \
  "s_waitcnt lgkmcnt(0)\n\t" \
  "v_smfmac_f32_16x16x64_bf16 v[76:79], v[52:55], a[128:135], v104\n\t" \
  "v_max_f32_dpp v98, v98, v98 quad_perm:[2,3,0,1] row_mask:0xf bank_mask:0xf\n\t" \
  "v_smfmac_f32_16x16x64_bf16 v[76:79], v[56:59], a[224:231], v104\n\t" \
  "s_nop 0\n\t" \
  "v_max_f32_dpp v98, v98, v98 row_half_mirror row_mask:0xf bank_mask:0xf\n\t" \
  "v_mul_f32 v98, %[cs], v98\n\t" \
  "v_fma_f32 v92, v92, %[cs], -v98\n\t" \
  "v_fma_f32 v93, v93, %[cs], -v98\n\t" \
  "v_fma_f32 v94, v94, %[cs], -v98\n\t" \
  "v_fma_f32 v95, v95, %[cs], -v98\n\t" \
  "v_exp_f32 v72, v92\n\t" \
  "v_exp_f32 v73, v93\n\t" \
  "v_exp_f32 v74, v94\n\t" \
  "v_exp_f32 v75, v95\n\t" \
  "v_cvt_pk_bf16_f32 v68, v72, v73\n\t" \
  "v_cvt_pk_bf16_f32 v69, v74, v75\n\t" \
  "buffer_store_dwordx2 v[68:69], v110, %[rh], s47 offen sc1\n\t" \
  "v_smfmac_f32_16x16x64_bf16 v[80:83], v[52:55], a[136:143], v104\n\t" \
  "v_smfmac_f32_16x16x64_bf16 v[80:83], v[56:59], a[232:239], v104\n\t" \
  "v_add_f32 v20, v76, v78\n\t" \
  "v_add_f32 v21, v77, v79\n\t" \
  "v_cvt_pk_bf16_f32 v20, v20, v21\n\t" \
  "ds_write_b32 v106, v20\n\t" \
  "s_nop 0\n\t" \
  "s_nop 0\n\t" \
  "s_nop 0\n\t" \
  "s_nop 0\n\t" \
  "v_add_f32 v23, v80, v82\n\t" \
  "v_add_f32 v21, v81, v83\n\t" \
  "v_cvt_pk_bf16_f32 v21, v23, v21\n\t" \
  "ds_write_b32 v106, v21 offset:256\n\t" \
  "v_mov_b64 v[0:1], 0\n\t" \
  "v_mov_b64 v[2:3], 0\n\t" \
  "v_mov_b64 v[36:37], 0\n\t" \
  "v_mov_b64 v[38:39], 0\n\t" \
  "s_waitcnt lgkmcnt(0)\n\t" \
  "s_barrier\n\t" \
  "ds_read_b64_tr_b16 v[32:33], v108 offset:12288\n\t" \
  "ds_read_b64_tr_b16 v[34:35], v108 offset:12352\n\t" \
  "ds_read_b64_tr_b16 v[4:5], v108\n\t" \
  "ds_read_b64_tr_b16 v[6:7], v108 offset:64\n\t" \
  "ds_read_b64_tr_b16 v[8:9], v108 offset:512\n\t" \
  "ds_read_b64_tr_b16 v[10:11], v108 offset:576\n\t" \
  "ds_read_b64_tr_b16 v[12:13], v108 offset:1024\n\t" \
  "ds_read_b64_tr_b16 v[14:15], v108 offset:1088\n\t" \
  "ds_read_b64_tr_b16 v[16:17], v108 offset:1536\n\t" \
  "ds_read_b64_tr_b16 v[18:19], v108 offset:1600\n\t" \
  "v_add_f32 v29, v72, v73\n\t" \
  "v_add_f32 v29, v29, v74\n\t" \
  "s_waitcnt lgkmcnt(8)\n\t" \
  "v_smfmac_f32_16x16x64_bf16 v[0:3], v[84:87], v[32:39], v104\n\t" \
  "s_waitcnt lgkmcnt(4)\n\t" \
  "v_smfmac_f32_16x16x64_bf16 v[0:3], v[84:87], v[4:11], v104\n\t" \
  "s_waitcnt lgkmcnt(0)\n\t" \
  "v_smfmac_f32_16x16x64_bf16 v[0:3], v[88:91], v[12:19], v104\n\t" \
  "v_add_f32 v29, v29, v75\n\t" \
  "s_mov_b32 s76, 0xaaaaaaaa\n\t" \
  "s_mov_b32 s77, s76\n\t" \
  "s_nop 4\n\t" \
  "v_add_f32 v96, v0, v2\n\t" \
  "v_add_f32_dpp v96, v1, v3 quad_perm:[0,1,2,3] row_mask:0xf bank_mask:0xc\n\t" \
  "v_fma_f32 v96, v96, %[cs], -v98\n\t" \
  "v_exp_f32 v24, v96\n\t" \
  "s_nop 0\n\t" \
  "v_add_f32 v29, v29, v24\n\t" \
  "s_nop 0\n\t" \
  "v_mov_b32_dpp v25, v24 quad_perm:[1,0,3,2] row_mask:0xf bank_mask:0xf\n\t" \
  "v_add_f32_dpp v29, v29, v29 quad_perm:[1,0,3,2] row_mask:0xf bank_mask:0xf\n\t" \
  "v_cndmask_b32_e64 v24, v24, v114, s[76:77]\n\t" \
  "v_cndmask_b32_e64 v25, v25, v115, s[76:77]\n\t" \
  "v_add_f32_dpp v29, v29, v29 quad_perm:[2,3,0,1] row_mask:0xf bank_mask:0xf\n\t" \
  "s_nop 1\n\t" \
  "v_add_f32_dpp v29, v29, v29 row_half_mirror row_mask:0xf bank_mask:0xf\n\t" \
  "v_rcp_f32 v26, v29\n\t" \
  "v_cmpx_gt_f32 vcc, 0x5d5e0b6b, v29\n\t" \
  "v_mul_f32 v24, v24, v26\n\t" \
  "v_mul_f32 v25, v25, v26\n\t" \
  "v_cvt_pk_bf16_f32 v24, v24, v25\n\t" \
  "buffer_store_dword v24, v111, %[rh], s47 offen sc1\n\t" \
  "s_memtime s[76:77]\n\t" \
  "s_mov_b64 exec, -1\n\t" \
  "v_mov_b32 v97, v29\n\t" \
  "s_cmp_eq_u64 vcc, -1\n\t" \
  "s_cselect_b32 s42, 0, 8\n\t" \
  "s_waitcnt lgkmcnt(0)\n\t" \
  "s_mov_b32 s57, s76\n\t" \
  "s_barrier\n\t" \
  "s_add_i32 s41, s56, 1950\n\t" \
  "s_sub_i32 s76, s57, s56\n\t" \
  "s_add_i32 s77, s43, 8\n\t" \
  "s_min_u32 s76, s76, s77\n\t" \
  "s_sub_i32 s43, s43, 4\n\t" \
  "s_max_u32 s43, s43, s76\n\t" \
  "s_add_i32 s76, s45, 0x4000\n\t" \
  "s_and_b32 s76, s76, 0xc000\n\t" \
  "v_mov_b32 v116, -1\n\t" \
  "buffer_store_dword v116, v109, %[rh], s76 offen sc1\n\t" \
  "s_sub_i32 s76, s92, s51\n\t" \
  "s_sub_i32 s77, s93, s53\n\t" \
  "s_cmp_eq_u32 s76, 0\n\t" \
  "s_cbranch_scc1 21f\n\t" \
  "v_cvt_f32_u32 v117, s76\n\t" \
  "v_cvt_f32_u32 v116, s77\n\t" \
  "v_rcp_f32 v117, v117\n\t" \
  "s_mov_b32 s51, s92\n\t" \
  "v_mul_f32 v117, v117, v116\n\t" \
  "v_max_f32 v117, 0x3f028f5c, v117\n\t" \
  "v_min_f32 v117, 0x3ffeb852, v117\n\t" \
  "v_rcp_f32 v116, v117\n\t" \
  "v_mul_f32 v117, 0x4f000000, v117\n\t" \
  "v_mul_f32 v116, 0x47800000, v116\n\t" \
  "v_cvt_u32_f32 v117, v117\n\t" \
  "v_cvt_u32_f32 v116, v116\n\t" \
  "s_mov_b32 s53, s93\n\t" \
  "v_readfirstlane_b32 s46, v117\n\t" \
  "v_readfirstlane_b32 s52, v116\n\t" \
  "21:\n\t" \
  "s_add_i32 s44, s44, 1\n\t" \
  "s_and_b32 s58, s58, 4\n\t" \
  "s_and_b32 s76, s44, 64\n\t" \
  "s_or_b32 s42, s42, s76\n\t" \
  "s_cbranch_scc0 20b\n\t" \
  "s_branch 99f\n\t" \
  "14:\n\t" \
  "s_sleep 3\n\t" \
  "s_add_u32 s59, s59, 0x10000\n\t" \
  "s_cmp_lt_u32 s59, 0x400000\n\t" \
  "s_cbranch_scc1 13b\n\t" \
  "s_mov_b64 exec, -1\n\t" \
  "s_mov_b32 s42, 16\n\t" \
  "s_branch 99f\n\t" \
  "8:\n\t" \
  "s_bitcmp1_b32 s58, 1\n\t" \
  "s_cbranch_scc1 4b\n\t" \
  "s_bitcmp1_b32 s58, 2\n\t" \
  "s_cbranch_scc1 5b\n\t" \
  "v_max3_u32 v116, v32, v33, v34\n\t" \
  "v_max3_u32 v117, v35, v36, v37\n\t" \
  "v_max3_u32 v116, v116, v38, v39\n\t" \
  "v_max_u32 v116, v116, v117\n\t" \
  "v_cmp_eq_u32 vcc, -1, v116\n\t" \
  "s_cbranch_vccz 5b\n\t" \
  "s_branch 0f\n\t" \
  "9:\n\t" \
  "s_bitcmp1_b32 s58, 2\n\t" \
  "s_cbranch_scc1 6b\n\t" \
  "v_max3_u32 v116, v40, v41, v42\n\t" \
  "v_max3_u32 v117, v43, v44, v45\n\t" \
  "v_max3_u32 v116, v116, v46, v47\n\t" \
  "v_max_u32 v116, v116, v117\n\t" \
  "v_cmp_eq_u32 vcc, -1, v116\n\t" \
  "s_cbranch_vccz 6b\n\t" \
  "s_or_b32 s58, s58, 2\n\t" \
  "0:\n\t" \
  "s_or_b32 s58, s58, 1\n\t" \
  "11:\n\t" \
  "buffer_load_dwordx4 v[32:35], v100, %[rh], s40 offen sc1\n\t" \
  "buffer_load_dwordx4 v[36:39], v100, %[rh], s76 offen sc1\n\t" \
  "buffer_load_dwordx4 v[40:43], v100, %[rh], s77 offen sc1\n\t" \
  "buffer_load_dwordx4 v[44:47], v100, %[rh], s78 offen sc1\n\t" \
  "s_waitcnt lgkmcnt(0)\n\t" \
  "s_branch 10b\n\t" \
  "17:\n\t" \
  "s_add_u32 s59, s59, 1\n\t" \
  "s_bitcmp1_b32 s58, 2\n\t" \
  "s_cbranch_scc1 3b\n\t" \
  "s_waitcnt vmcnt(0)\n\t" \
  "v_max_u32 v116, v32, v33\n\t" \
  "v_max3_u32 v116, v116, v34, v35\n\t" \
  "v_max3_u32 v116, v116, v36, v37\n\t" \
  "v_max3_u32 v116, v116, v38, v39\n\t" \
  "v_max3_u32 v116, v116, v40, v41\n\t" \
  "v_max3_u32 v116, v116, v42, v43\n\t" \
  "v_max3_u32 v116, v116, v44, v45\n\t" \
  "v_max3_u32 v116, v116, v46, v47\n\t" \
  "v_cmp_eq_u32 vcc, -1, v116\n\t" \
  "s_cbranch_vccz 3b\n\t" \
  "s_bitcmp1_b32 s59, 6\n\t" \
  "s_cbranch_scc1 12f\n\t" \
  "buffer_load_dwordx4 v[32:35], v100, %[rh], s40 offen sc1\n\t" \
  "buffer_load_dwordx4 v[36:39], v100, %[rh], s76 offen sc1\n\t" \
  "buffer_load_dwordx4 v[40:43], v100, %[rh], s77 offen sc1\n\t" \
  "buffer_load_dwordx4 v[44:47], v100, %[rh], s78 offen sc1\n\t" \
  "s_branch 17b\n\t" \
  "12:\n\t" \
  "s_mov_b32 s42, s58\n\t" \
  "s_branch 99f\n\t" \
  "91:\n\t" \
  "s_mov_b32 s58, s42\n\t" \
  "s_mov_b32 s95, 0\n\t" \
  "20:\n\t" \
  "s_and_b32 s76, s44, 7\n\t" \
  "s_lshl_b32 s76, s76, 15\n\t" \
  "s_add_i32 s40, s54, s76\n\t" \
  "s_and_b32 s76, s44, 3\n\t" \
  "s_lshl_b32 s45, s76, 14\n\t" \
  "s_add_i32 s77, s44, 1\n\t" \
  "s_min_u32 s77, s77, 63\n\t" \
  "s_lshr_b32 s76, s77, 1\n\t" \
  "s_lshl_b32 s47, s77, 20\n\t" \
  "s_add_i32 s47, s47, s55\n\t" \
  "s_bitcmp1_b64 %[sel], s76\n\t" \
  "s_cselect_b32 s76, 0x2000, 0\n\t" \
  "s_add_i32 s97, s47, s76\n\t" \
  "s_cmp_le_i32 s77, s43\n\t" \
  "s_cselect_b32 s47, s97, 0x70000000\n\t" \
  "s_lshr_b32 s76, s44, 1\n\t" \
  "s_lshl_b32 s94, s44, 20\n\t" \
  "s_bitcmp1_b64 %[sel], s76\n\t" \
  "s_cselect_b32 s76, 0x2000, 0\n\t" \
  "s_add_i32 s94, s94, s76\n\t" \
  "s_add_i32 s49, s44, -1\n\t" \
  "s_and_b32 s48, s49, 2\n\t" \
  "s_lshl_b32 s48, s48, 13\n\t" \
  "s_and_b32 s76, s49, 1\n\t" \
  "s_lshl_b32 s76, s76, 12\n\t" \
  "s_add_i32 s48, s48, s76\n\t" \
  "s_lshl_b32 s50, s53, 8\n\t" \
  "s_mov_b32 s59, 0\n\t" \
  "s_mov_b32 s92, s51\n\t" \
  "s_mov_b32 s93, s53\n\t" \
  "s_xor_b32 s76, s40, 0x400\n\t" \
  "s_xor_b32 s77, s40, 0x800\n\t" \
  "s_xor_b32 s78, s40, 0xc00\n\t" \
  "s_cmp_lg_u32 s58, 0\n\t" \
  "s_cbranch_scc1 11f\n\t" \
  "s_cmp_lg_u32 s95, 0\n\t" \
  "s_cbranch_scc1 46f\n\t" \
  "10:\n\t" \
  "ds_read_b128 v[48:51], v102 offset:32768\n\t" \
  "ds_read_b128 v[52:55], v102 offset:33792\n\t" \
  "ds_read_b128 v[56:59], v102 offset:34816\n\t" \
  "ds_read_b128 v[60:63], v102 offset:35840\n\t" \
  "ds_read_b128 v[16:19], v102 offset:49152\n\t" \
  "ds_read_b128 v[20:23], v102 offset:50176\n\t" \
  "ds_read_b128 v[24:27], v102 offset:51200\n\t" \
  "ds_read_b128 v[28:31], v102 offset:52224\n\t" \
  "ds_read_b128 v[84:87], v103 offset:16384\n\t" \
  "ds_read_b128 v[88:91], v103 offset:17408\n\t" \
  "ds_read_b128 v[92:95], v103 offset:18432\n\t" \
  "ds_read_b128 v[96:99], v103 offset:19456\n\t" \
  "s_waitcnt lgkmcnt(4)\n\t" \
  "ds_read_b128 v[0:3], v103 offset:49152\n\t" \
  "ds_read_b128 v[4:7], v103 offset:50176\n\t" \
  "ds_read_b128 v[8:11], v103 offset:51200\n\t" \
  "ds_read_b128 v[12:15], v103 offset:52224\n\t" \
  "46:\n\t" \
  "s_bitcmp1_b32 s58, 1\n\t" \
  "s_cselect_b64 s[80:81], -1, 0\n\t" \
  "s_cmp_lg_u32 s58, 0\n\t" \
  "s_cbranch_scc1 17f\n\t" \
  "s_mov_b64 exec, 0xff\n\t" \
  "s_cmp_eq_u32 s95, 0\n\t" \
  "s_cbranch_scc1 16f\n\t" \
  "s_memtime vcc\n\t" \
  "s_memrealtime s[82:83]\n\t" \
  "s_nop 4\n\t" \
  "s_memrealtime s[84:85]\n\t" \
  "s_nop 4\n\t" \
  "s_memrealtime s[56:57]\n\t" \
  "s_nop 4\n\t" \
  "s_memrealtime s[86:87]\n\t" \
  "s_nop 4\n\t" \
  "s_memrealtime s[88:89]\n\t" \
  "s_nop 4\n\t" \
  "s_memrealtime s[90:91]\n\t" \
  "s_mov_b32 s92, 0\n\t" \
  "s_waitcnt lgkmcnt(0)\n\t" \
  "s_cmp_eq_u32 s82, s84\n\t" \
  "s_addc_u32 s92, s92, 0\n\t" \
  "s_cmp_eq_u32 s82, s56\n\t" \
  "s_addc_u32 s92, s92, 0\n\t" \
  "s_cmp_eq_u32 s82, s86\n\t" \
  "s_addc_u32 s92, s92, 0\n\t" \
  "s_cmp_eq_u32 s82, s88\n\t" \
  "s_addc_u32 s92, s92, 0\n\t" \
  "s_cmp_eq_u32 s82, s90\n\t" \
  "s_addc_u32 s92, s92, 0\n\t" \
  "s_mul_i32 s92, s92, 24\n\t" \
  "s_add_i32 s92, s92, vcc_lo\n\t" \
  "s_lshl4_add_u32 s93, s82, 64\n\t" \
  "s_lshl_b32 s50, s93, 8\n\t" \
  "s_waitcnt vmcnt(2)\n\t" \
  "v_mov_b32 v116, v118\n\t" \
  "s_branch 15f\n\t" \
  "16:\n\t" \
  "s_memtime vcc\n\t" \
  "s_waitcnt lgkmcnt(0)\n\t" \
  "s_sub_i32 s79, s41, vcc_lo\n\t" \
  "s_cmp_gt_i32 s79, 64\n\t" \
  "s_cbranch_scc1 16b\n\t" \
  "13:\n\t" \
  "buffer_load_dword v116, v112, %[rh], s48 offen sc1\n\t" \
  "s_memtime vcc\n\t" \
  "s_memrealtime s[82:83]\n\t" \
  "s_nop 4\n\t" \
  "s_memrealtime s[84:85]\n\t" \
  "s_nop 4\n\t" \
  "s_memrealtime s[56:57]\n\t" \
  "s_nop 4\n\t" \
  "s_memrealtime s[86:87]\n\t" \
  "s_nop 4\n\t" \
  "s_memrealtime s[88:89]\n\t" \
  "s_nop 4\n\t" \
  "s_memrealtime s[90:91]\n\t" \
  "s_mov_b32 s92, 0\n\t" \
  "s_waitcnt lgkmcnt(0)\n\t" \
  "s_cmp_eq_u32 s82, s84\n\t" \
  "s_addc_u32 s92, s92, 0\n\t" \
  "s_cmp_eq_u32 s82, s56\n\t" \
  "s_addc_u32 s92, s92, 0\n\t" \
  "s_cmp_eq_u32 s82, s86\n\t" \
  "s_addc_u32 s92, s92, 0\n\t" \
  "s_cmp_eq_u32 s82, s88\n\t" \
  "s_addc_u32 s92, s92, 0\n\t" \
  "s_cmp_eq_u32 s82, s90\n\t" \
  "s_addc_u32 s92, s92, 0\n\t" \
  "s_mul_i32 s92, s92, 24\n\t" \
  "s_add_i32 s92, s92, vcc_lo\n\t" \
  "s_lshl4_add_u32 s93, s82, 64\n\t" \
  "s_lshl_b32 s50, s93, 8\n\t" \
  "s_waitcnt vmcnt(0)\n\t" \
  "15:\n\t" \
  "v_mov_b32 v117, 20000\n\t" \
  "v_cmp_ne_u32_sdwa vcc, v116, s49 src0_sel:BYTE_0 src1_sel:DWORD\n\t" \
  "v_subrev_u32 v116, s50, v116\n\t" \
  "v_ashrrev_i32 v116, 8, v116\n\t" \
  "s_nop 1\n\t" \
  "v_max_i32_dpp v116, v116, v116 quad_perm:[1,0,3,2] row_mask:0xf bank_mask:0xf\n\t" \
  "s_cbranch_vccnz 14f\n\t" \
  "s_nop 0\n\t" \
  "v_max_i32_dpp v116, v116, v116 quad_perm:[2,3,0,1] row_mask:0xf bank_mask:0xf\n\t" \
  "s_nop 1\n\t" \
  "v_max_i32_dpp v116, v116, v116 row_half_mirror row_mask:0xf bank_mask:0xf\n\t" \
  "v_med3_i32 v116, v116, 0, v117\n\t" \
  "v_mul_u32_u24 v116, s52, v116\n\t" \
  "v_lshrrev_b32 v116, 16, v116\n\t" \
  "s_mov_b64 exec, -1\n\t" \
  "v_readfirstlane_b32 s84, v116\n\t" \
  "s_add_i32 s84, s84, s92\n\t" \
  ".p2align 6\n\t" \
  "s_nop 0\n\t" \
  "s_nop 0\n\t" \
  "s_nop 0\n\t" \
  "s_nop 0\n\t" \
  "s_nop 0\n\t" \
  "s_nop 0\n\t" \
  "s_nop 0\n\t" \
  "s_nop 0\n\t" \
  "1:\n\t" \
  "s_memtime vcc\n\t" \
  "s_waitcnt lgkmcnt(0)\n\t" \
  "s_sub_i32 s79, s84, vcc_lo\n\t" \
  "s_cmp_gt_i32 s79, 128\n\t" \
  "s_cbranch_scc1 1b\n\t" \
  ".p2align 4\n\t" \
  "2:\n\t" \
  "s_sub_i32 s79, s79, 33\n\t" \
  "s_cmp_gt_i32 s79, 56\n\t" \
  "s_cbranch_scc1 2b\n\t" \
  "buffer_load_dwordx4 v[32:35], v100, %[rh], s40 offen sc1\n\t" \
  "s_nop 12\n\t" \
  "buffer_load_dwordx4 v[36:39], v100, %[rh], s76 offen sc1\n\t" \
  "s_nop 12\n\t" \
  "buffer_load_dwordx4 v[40:43], v100, %[rh], s77 offen sc1\n\t" \
  "s_nop 12\n\t" \
  "buffer_load_dwordx4 v[44:47], v100, %[rh], s78 offen sc1\n\t" \
  "3:\n\t" \
  "v_mov_b64 v[68:69], 0\n\t" \
  "v_mov_b64 v[70:71], 0\n\t" \
  "v_mov_b64 v[72:73], 0\n\t" \
  "v_mov_b64 v[74:75], 0\n\t" \
  "v_mov_b64 v[76:77], 0\n\t" \
  "v_mov_b64 v[78:79], 0\n\t" \
  "v_mov_b64 v[80:81], 0\n\t" \
  "v_mov_b64 v[82:83], 0\n\t" \
  "s_waitcnt lgkmcnt(0)\n\t" \
  "s_waitcnt vmcnt(3)\n\t" \
  "v_smfmac_f32_16x16x64_bf16 v[68:71], v[32:35], a[96:103], v104\n\t" \
  "ds_write_b128 v101, v[32:35] offset:4096\n\t" \
  "v_smfmac_f32_16x16x64_bf16 v[72:75], v[32:35], a[104:111], v104\n\t" \
  "v_smfmac_f32_16x16x64_bf16 v[76:79], v[32:35], v[48:55], v104\n\t" \
  "v_smfmac_f32_16x16x64_bf16 v[80:83], v[32:35], v[56:63], v104\n\t" \
  "s_waitcnt vmcnt(2)\n\t" \
  "v_smfmac_f32_16x16x64_bf16 v[68:71], v[36:39], a[112:119], v104\n\t" \
  "ds_write_b128 v101, v[36:39] offset:5120\n\t" \
  "v_smfmac_f32_16x16x64_bf16 v[72:75], v[36:39], a[120:127], v104\n\t" \
  "v_smfmac_f32_16x16x64_bf16 v[76:79], v[36:39], a[128:135], v104\n\t" \
  "v_cmp_u_f32 vcc, v68, v69\n\t" \
  "v_smfmac_f32_16x16x64_bf16 v[80:83], v[36:39], a[136:143], v104\n\t" \
  "v_cmp_u_f32 s[82:83], v70, v71\n\t" \
  "s_waitcnt vmcnt(1)\n\t" \
  "v_smfmac_f32_16x16x64_bf16 v[68:71], v[40:43], v[16:23], v104\n\t" \
  "ds_write_b128 v101, v[40:43] offset:6144\n\t" \
  "v_smfmac_f32_16x16x64_bf16 v[72:75], v[40:43], v[24:31], v104\n\t" \
  "s_or_b64 vcc, vcc, s[82:83]\n\t" \
  "s_or_b64 vcc, vcc, s[80:81]\n\t" \
  "s_waitcnt lgkmcnt(1)\n\t" \
  "v_smfmac_f32_16x16x64_bf16 v[76:79], v[40:43], a[144:151], v104\n\t" \
  "s_cbranch_scc1 8f\n\t" \
  "5:\n\t" \
  "s_barrier\n\t" \
  "4:\n\t" \
  "s_memtime s[56:57]\n\t" \
  "ds_read_b128 v[48:51], v101 offset:8192\n\t" \
  "v_smfmac_f32_16x16x64_bf16 v[80:83], v[40:43], a[152:159], v104\n\t" \
  "ds_read_b128 v[52:55], v101 offset:9216\n\t" \
  "ds_read_b128 v[56:59], v101 offset:12288\n\t" \
  "s_waitcnt vmcnt(0)\n\t" \
  "v_smfmac_f32_16x16x64_bf16 v[68:71], v[44:47], a[160:167], v104\n\t" \
  "ds_write_b128 v101, v[44:47] offset:7168\n\t" \
  "v_smfmac_f32_16x16x64_bf16 v[72:75], v[44:47], a[168:175], v104\n\t" \
  "v_smfmac_f32_16x16x64_bf16 v[76:79], v[44:47], a[176:183], v104\n\t" \
  "v_cmp_u_f32 vcc, v68, v69\n\t" \
  "v_smfmac_f32_16x16x64_bf16 v[80:83], v[44:47], a[184:191], v104\n\t" \
  "v_cmp_u_f32 s[82:83], v70, v71\n\t" \
  "s_waitcnt lgkmcnt(3)\n\t" \
  "v_smfmac_f32_16x16x64_bf16 v[68:71], v[48:51], a[192:199], v104\n\t" \
  "v_smfmac_f32_16x16x64_bf16 v[72:75], v[48:51], a[200:207], v104\n\t" \
  "s_or_b64 vcc, vcc, s[82:83]\n\t" \
  "s_waitcnt lgkmcnt(0)\n\t" \
  "v_smfmac_f32_16x16x64_bf16 v[68:71], v[52:55], a[208:215], v104\n\t" \
  "s_cbranch_scc1 9f\n\t" \
  "6:\n\t" \
  "s_barrier\n\t" \
  "ds_read_b128 v[60:63], v101 offset:13312\n\t" \
  "v_smfmac_f32_16x16x64_bf16 v[72:75], v[52:55], a[216:223], v104\n\t" \
  "ds_read_b128 v[64:67], v101 offset:0\n\t" \
  "v_smfmac_f32_16x16x64_bf16 v[68:71], v[56:59], v[152:159], v104\n\t" \
  "v_smfmac_f32_16x16x64_bf16 v[72:75], v[56:59], v[160:167], v104\n\t" \
  "ds_read_b128 v[48:51], v101 offset:1024\n\t" \
  "s_waitcnt lgkmcnt(2)\n\t" \
  "v_smfmac_f32_16x16x64_bf16 v[68:71], v[60:63], v[168:175], v104\n\t" \
  "ds_read_b128 v[52:55], v101 offset:10240\n\t" \
  "v_smfmac_f32_16x16x64_bf16 v[72:75], v[60:63], v[176:183], v104\n\t" \
  "ds_read_b128 v[56:59], v101 offset:11264\n\t" \
  "s_waitcnt lgkmcnt(3)\n\t" \
  "v_smfmac_f32_16x16x64_bf16 v[68:71], v[64:67], a[0:7], v104\n\t" \
  "ds_read_b128 v[60:63], v101 offset:14336\n\t" \
  "v_smfmac_f32_16x16x64_bf16 v[72:75], v[64:67], a[8:15], v104\n\t" \
  "s_waitcnt lgkmcnt(3)\n\t" \
  "v_smfmac_f32_16x16x64_bf16 v[68:71], v[48:51], a[16:23], v104\n\t" \
  "ds_read_b128 v[64:67], v101 offset:15360\n\t" \
  "ds_read_b128 v[16:19], v102 offset:16384\n\t" \
  "v_smfmac_f32_16x16x64_bf16 v[72:75], v[48:51], a[24:31], v104\n\t" \
  "s_waitcnt lgkmcnt(4)\n\t" \
  "v_smfmac_f32_16x16x64_bf16 v[68:71], v[52:55], v[84:91], v104\n\t" \
  "ds_read_b128 v[48:51], v101 offset:3072\n\t" \
  "ds_read_b128 v[20:23], v102 offset:17408\n\t" \
  "v_smfmac_f32_16x16x64_bf16 v[72:75], v[52:55], v[92:99], v104\n\t" \
  "s_waitcnt lgkmcnt(5)\n\t" \
  "v_smfmac_f32_16x16x64_bf16 v[68:71], v[56:59], v[120:127], v104\n\t" \
  "ds_read_b128 v[52:55], v101 offset:2048\n\t" \
  "ds_read_b128 v[24:27], v102 offset:18432\n\t" \
  "v_smfmac_f32_16x16x64_bf16 v[72:75], v[56:59], v[128:135], v104\n\t" \
  "ds_read_b128 v[28:31], v102 offset:19456\n\t" \
  "s_waitcnt lgkmcnt(7)\n\t" \
  "v_smfmac_f32_16x16x64_bf16 v[68:71], v[60:63], v[0:7], v104\n\t" \
  "ds_read_b128 v[36:39], v102 offset:28672\n\t" \
  "v_smfmac_f32_16x16x64_bf16 v[72:75], v[60:63], v[8:15], v104\n\t" \
  "buffer_load_dwordx4 v[84:87], v107, %[ru], s94 offen offset:0 sc1\n\t" \
  "s_waitcnt lgkmcnt(7)\n\t" \
  "v_smfmac_f32_16x16x64_bf16 v[68:71], v[64:67], v[216:223], v104\n\t" \
  "ds_read_b128 v[0:3], v103 offset:0\n\t" \
  "v_smfmac_f32_16x16x64_bf16 v[72:75], v[64:67], v[224:231], v104\n\t" \
  "ds_read_b128 v[4:7], v103 offset:1024\n\t" \
  "s_waitcnt lgkmcnt(7)\n\t" \
  "v_smfmac_f32_16x16x64_bf16 v[68:71], v[48:51], a[64:71], v104\n\t" \
  "buffer_load_dwordx4 v[88:91], v107, %[ru], s94 offen offset:1024 sc1\n\t" \
  "s_waitcnt lgkmcnt(5)\n\t" \
  "v_smfmac_f32_16x16x64_bf16 v[68:71], v[52:55], v[16:23], v104\n\t" \
  "ds_read_b128 v[8:11], v103 offset:2048\n\t" \
  "v_smfmac_f32_16x16x64_bf16 v[72:75], v[48:51], a[72:79], v104\n\t" \
  "ds_read_b128 v[12:15], v103 offset:3072\n\t" \
  "s_waitcnt lgkmcnt(5)\n\t" \
  "v_smfmac_f32_16x16x64_bf16 v[72:75], v[52:55], v[24:31], v104\n\t" \
  "v_add_f32 v44, v68, v70\n\t" \
  "ds_read_b128 v[40:43], v102 offset:29696\n\t" \
  "v_smfmac_f32_16x16x64_bf16 v[76:79], v[56:59], v[136:143], v104\n\t" \
  "v_add_f32 v45, v69, v71\n\t" \
  "v_cvt_pk_bf16_f32 v44, v44, v45\n\t" \
  "v_smfmac_f32_16x16x64_bf16 v[80:83], v[56:59], v[144:151], v104\n\t" \
  "v_add_f32 v46, v72, v74\n\t" \
  "v_add_f32 v47, v73, v75\n\t" \
  "v_smfmac_f32_16x16x64_bf16 v[76:79], v[60:63], v[200:207], v104\n\t" \
  "v_cvt_pk_bf16_f32 v46, v46, v47\n\t" \
  "ds_write2st64_b32 v105, v44, v46 offset1:1\n\t" \
  "v_smfmac_f32_16x16x64_bf16 v[80:83], v[60:63], v[208:215], v104\n\t" \
  "ds_read_b128 v[56:59], v101 offset:8192\n\t" \
  "v_mov_b64 v[68:69], 0\n\t" \
  "v_smfmac_f32_16x16x64_bf16 v[76:79], v[64:67], v[232:239], v104\n\t" \
  "ds_read_b128 v[16:19], v103 offset:32768\n\t" \
  "v_mov_b64 v[70:71], 0\n\t" \
  "v_smfmac_f32_16x16x64_bf16 v[80:83], v[64:67], v[240:247], v104\n\t" \
  "ds_read_b128 v[20:23], v103 offset:33792\n\t" \
  "v_smfmac_f32_16x16x64_bf16 v[76:79], v[48:51], a[80:87], v104\n\t" \
  "s_waitcnt lgkmcnt(3)\n\t" \
  "s_barrier\n\t" \
  "v_smfmac_f32_16x16x64_bf16 v[80:83], v[48:51], a[88:95], v104\n\t" \
  "buffer_load_dwordx4 v[92:95], v107, %[ru], s94 offen offset:2048 sc1\n\t" \
  "v_smfmac_f32_16x16x64_bf16 v[68:71], v[32:35], v[36:43], v104\n\t" \
  "ds_read_b128 v[36:39], v102 offset:61440\n\t" \
  "ds_read_b128 v[40:43], v102 offset:62464\n\t" \
  "v_smfmac_f32_16x16x64_bf16 v[76:79], v[52:55], a[48:55], v104\n\t" \
  "ds_read_b128 v[60:63], v101 offset:1024\n\t" \
  "ds_read_b128 v[24:27], v103 offset:34816\n\t" \
  "v_smfmac_f32_16x16x64_bf16 v[80:83], v[52:55], a[56:63], v104\n\t" \
  "ds_read_b128 v[64:67], v101 offset:10240\n\t" \
  "buffer_load_dwordx4 v[96:99], v107, %[ru], s94 offen offset:3072 sc1\n\t" \
  "s_waitcnt lgkmcnt(7)\n\t" \
  "v_smfmac_f32_16x16x64_bf16 v[76:79], v[56:59], v[0:7], v104\n\t" \
  "ds_read_b128 v[48:51], v101 offset:12288\n\t" \
  "ds_read_b128 v[28:31], v103 offset:35840\n\t" \
  "global_load_dword v119, v109, %[kc] sc1\n\t" \
  "v_smfmac_f32_16x16x64_bf16 v[80:83], v[56:59], v[8:15], v104\n\t" \
  "s_waitcnt lgkmcnt(4)\n\t" \
  "v_smfmac_f32_16x16x64_bf16 v[76:79], v[60:63], a[32:39], v104\n\t" \
  "ds_read_b128 v[52:55], v101 offset:9216\n\t" \
  "buffer_load_dwordx4 v[32:35], v100, %[ru], s47 offen offset:0 sc1\n\t" \
  "v_smfmac_f32_16x16x64_bf16 v[80:83], v[60:63], a[40:47], v104\n\t" \
  "ds_read_b128 v[0:3], v102 offset:0\n\t" \
  "v_smfmac_f32_16x16x64_bf16 v[68:71], v[56:59], v[36:43], v104\n\t" \
  "ds_read_b128 v[36:39], v103 offset:28672\n\t" \
  "ds_read_b128 v[40:43], v103 offset:29696\n\t" \
  "s_waitcnt lgkmcnt(6)\n\t" \
  "v_smfmac_f32_16x16x64_bf16 v[76:79], v[64:67], a[240:247], v104\n\t" \
  "ds_read_b128 v[56:59], v101 offset:13312\n\t" \
  "v_smfmac_f32_16x16x64_bf16 v[80:83], v[64:67], a[248:255], v104\n\t" \
  "ds_read_b128 v[60:63], v101 offset:0\n\t" \
  "ds_read_b128 v[4:7], v102 offset:1024\n\t" \
  "buffer_load_dwordx4 v[44:47], v100, %[ru], s47 offen offset:1024 sc1\n\t" \
  "s_waitcnt lgkmcnt(8)\n\t" \
  "v_smfmac_f32_16x16x64_bf16 v[76:79], v[48:51], v[16:23], v104\n\t" \
  "ds_read_b128 v[8:11], v102 offset:2048\n\t" \
  "s_waitcnt lgkmcnt(8)\n\t" \
  "v_smfmac_f32_16x16x64_bf16 v[80:83], v[48:51], v[24:31], v104\n\t" \
  "ds_read_b128 v[12:15], v102 offset:3072\n\t" \
  "s_waitcnt lgkmcnt(8)\n\t" \
  "v_smfmac_f32_16x16x64_bf16 v[76:79], v[52:55], a[224:231], v104\n\t" \
  "s_waitcnt lgkmcnt(5)\n\t" \
  "v_smfmac_f32_16x16x64_bf16 v[68:71], v[48:51], v[36:43], v104\n\t" \
  "v_smfmac_f32_16x16x64_bf16 v[80:83], v[52:55], a[232:239], v104\n\t" \
  "s_waitcnt lgkmcnt(4)\n\t" \
  "v_smfmac_f32_16x16x64_bf16 v[76:79], v[56:59], v[184:191], v104\n\t" \
  "v_add_f32 v72, v68, v70\n\t" \
  "s_waitcnt lgkmcnt(2)\n\t" \
  "v_smfmac_f32_16x16x64_bf16 v[76:79], v[60:63], v[0:7], v104\n\t" \
  "v_add_f32 v73, v69, v71\n\t" \
  "v_smfmac_f32_16x16x64_bf16 v[80:83], v[56:59], v[192:199], v104\n\t" \
  "v_cvt_pk_bf16_f32 v72, v72, v73\n\t" \
  "s_waitcnt lgkmcnt(0)\n\t" \
  "v_smfmac_f32_16x16x64_bf16 v[80:83], v[60:63], v[8:15], v104\n\t" \
  "ds_write_b32 v106, v72 offset:11776\n\t" \
  "v_add_f32 v20, v76, v78\n\t" \
  "v_add_f32 v21, v77, v79\n\t" \
  "v_cvt_pk_bf16_f32 v20, v20, v21\n\t" \
  "ds_write_b32 v106, v20\n\t" \
  "s_nop 3\n\t" \
  "v_add_f32 v23, v80, v82\n\t" \
  "v_add_f32 v21, v81, v83\n\t" \
  "v_cvt_pk_bf16_f32 v21, v23, v21\n\t" \
  "ds_write_b32 v106, v21 offset:256\n\t" \
  "s_waitcnt lgkmcnt(0)\n\t" \
  "s_barrier\n\t" \
  "v_bfe_i32 v64, v114, 0, 1\n\t" \
  "v_and_b32 v64, 0xff800000, v64\n\t" \
  "v_bfe_i32 v65, v114, 1, 1\n\t" \
  "v_and_b32 v65, 0xff800000, v65\n\t" \
  "v_bfe_i32 v66, v114, 2, 1\n\t" \
  "v_and_b32 v66, 0xff800000, v66\n\t" \
  "v_bfe_i32 v67, v114, 3, 1\n\t" \
  "v_and_b32 v67, 0xff800000, v67\n\t" \
  "v_bfe_i32 v68, v114, 4, 1\n\t" \
  "v_and_b32 v68, 0xff800000, v68\n\t" \
  "v_bfe_i32 v69, v114, 5, 1\n\t" \
  "v_and_b32 v69, 0xff800000, v69\n\t" \
  "v_bfe_i32 v70, v114, 6, 1\n\t" \
  "v_and_b32 v70, 0xff800000, v70\n\t" \
  "v_bfe_i32 v71, v114, 7, 1\n\t" \
  "v_and_b32 v71, 0xff800000, v71\n\t" \
  "s_lshl_b32 s94, s45, 4\n\t" \
  "s_and_b32 s96, s44, 2\n\t" \
  "s_lshl_b32 s96, s96, 13\n\t" \
  "s_and_b32 s76, s44, 1\n\t" \
  "s_lshl_b32 s76, s76, 12\n\t" \
  "s_add_i32 s96, s96, s76\n\t" \
  "s_mov_b32 s95, 1\n\t" \
  "s_add_i32 s41, s56, 1950\n\t" \
  "s_sub_i32 s76, s92, s51\n\t" \
  "s_sub_i32 s77, s93, s53\n\t" \
  "s_cmp_eq_u32 s76, 0\n\t" \
  "s_cbranch_scc1 21f\n\t" \
  "v_cvt_f32_u32 v117, s76\n\t" \
  "v_cvt_f32_u32 v116, s77\n\t" \
  "v_rcp_f32 v117, v117\n\t" \
  "s_mov_b32 s51, s92\n\t" \
  "v_mul_f32 v117, v117, v116\n\t" \
  "v_max_f32 v117, 0x3f028f5c, v117\n\t" \
  "v_min_f32 v117, 0x3ffeb852, v117\n\t" \
  "v_rcp_f32 v116, v117\n\t" \
  "v_mul_f32 v117, 0x4f000000, v117\n\t" \
  "v_mul_f32 v116, 0x47800000, v116\n\t" \
  "v_cvt_u32_f32 v117, v117\n\t" \
  "v_cvt_u32_f32 v116, v116\n\t" \
  "s_mov_b32 s53, s93\n\t" \
  "v_readfirstlane_b32 s46, v117\n\t" \
  "v_readfirstlane_b32 s52, v116\n\t" \
  "21:\n\t" \
  "ds_read_b128 v[0:3], v101 offset:20480\n\t" \
  "ds_read_b128 v[4:7], v101 offset:21504\n\t" \
  "ds_read_b128 v[8:11], v101 offset:22528\n\t" \
  "ds_read_b128 v[12:15], v101 offset:23552\n\t" \
  "ds_read_b128 v[16:19], v101 offset:24576\n\t" \
  "ds_read_b128 v[20:23], v101 offset:25600\n\t" \
  "ds_read_b128 v[24:27], v101 offset:26624\n\t" \
  "ds_read_b128 v[28:31], v101 offset:27648\n\t" \
  "ds_read_b64_tr_b16 v[48:49], v108 offset:0\n\t" \
  "ds_read_b64_tr_b16 v[50:51], v108 offset:64\n\t" \
  "ds_read_b64_tr_b16 v[52:53], v108 offset:512\n\t" \
  "ds_read_b64_tr_b16 v[54:55], v108 offset:576\n\t" \
  "ds_read_b64_tr_b16 v[56:57], v108 offset:1024\n\t" \
  "ds_read_b64_tr_b16 v[58:59], v108 offset:1088\n\t" \
  "ds_read_b64_tr_b16 v[60:61], v108 offset:1536\n\t" \
  "ds_read_b64_tr_b16 v[62:63], v108 offset:1600\n\t" \
  "ds_read_b64_tr_b16 v[76:77], v108 offset:12288\n\t" \
  "ds_read_b64_tr_b16 v[78:79], v108 offset:12352\n\t" \
  "s_waitcnt vmcnt(3) lgkmcnt(10)\n\t" \
  "v_mfma_f32_16x16x32_bf16 v[64:67], v[0:3], v[84:87], v[64:67]\n\t" \
  "v_mfma_f32_16x16x32_bf16 v[68:71], v[16:19], v[84:87], v[68:71]\n\t" \
  "v_mfma_f32_16x16x32_bf16 v[64:67], v[4:7], v[88:91], v[64:67]\n\t" \
  "v_mfma_f32_16x16x32_bf16 v[68:71], v[20:23], v[88:91], v[68:71]\n\t" \
  "v_mfma_f32_16x16x32_bf16 v[64:67], v[8:11], v[92:95], v[64:67]\n\t" \
  "v_mfma_f32_16x16x32_bf16 v[68:71], v[24:27], v[92:95], v[68:71]\n\t" \
  "v_mfma_f32_16x16x32_bf16 v[64:67], v[12:15], v[96:99], v[64:67]\n\t" \
  "v_mfma_f32_16x16x32_bf16 v[68:71], v[28:31], v[96:99], v[68:71]\n\t" \
  "s_waitcnt lgkmcnt(0)\n\t" \
  "s_barrier\n\t" \
  "v_mfma_f32_16x16x32_bf16 v[72:75], v[48:51], v[84:87], 0\n\t" \
  "v_mfma_f32_16x16x32_bf16 v[72:75], v[52:55], v[88:91], v[72:75]\n\t" \
  "v_mfma_f32_16x16x32_bf16 v[72:75], v[56:59], v[92:95], v[72:75]\n\t" \
  "v_mfma_f32_16x16x32_bf16 v[72:75], v[60:63], v[96:99], v[72:75]\n\t" \
  "v_mfma_f32_16x16x32_bf16 v[72:75], v[76:79], v[84:87], v[72:75]\n\t" \
  "s_waitcnt vmcnt(0)\n\t" \
  "ds_write_b128 v101, v[32:35] offset:20480\n\t" \
  "ds_write_b128 v101, v[44:47] offset:21504\n\t" \
  "ds_read_b128 v[48:51], v102 offset:32768\n\t" \
  "ds_read_b128 v[52:55], v102 offset:33792\n\t" \
  "ds_read_b128 v[56:59], v102 offset:34816\n\t" \
  "ds_read_b128 v[60:63], v102 offset:35840\n\t" \
  "ds_read_b128 v[16:19], v102 offset:49152\n\t" \
  "ds_read_b128 v[20:23], v102 offset:50176\n\t" \
  "ds_read_b128 v[24:27], v102 offset:51200\n\t" \
  "ds_read_b128 v[28:31], v102 offset:52224\n\t" \
  "ds_read_b128 v[84:87], v103 offset:16384\n\t" \
  "ds_read_b128 v[88:91], v103 offset:17408\n\t" \
  "ds_read_b128 v[92:95], v103 offset:18432\n\t" \
  "ds_read_b128 v[96:99], v103 offset:19456\n\t" \
  "ds_read_b128 v[0:3], v103 offset:49152\n\t" \
  "ds_read_b128 v[4:7], v103 offset:50176\n\t" \
  "ds_read_b128 v[8:11], v103 offset:51200\n\t" \
  "ds_read_b128 v[12:15], v103 offset:52224\n\t" \
  "v_max3_f32 v76, v64, v65, v66\n\t" \
  "v_max3_f32 v77, v67, v68, v69\n\t" \
  "v_max3_f32 v76, v76, v70, v71\n\t" \
  "v_max3_f32 v77, v77, v72, v73\n\t" \
  "v_max3_f32 v76, v76, v74, v75\n\t" \
  "v_max_f32 v76, v76, v77\n\t" \
  "v_mov_b32 v77, v76\n\t" \
  "s_nop 1\n\t" \
  "v_permlane16_swap_b32 v76, v77\n\t" \
  "v_max_f32 v76, v76, v77\n\t" \
  "v_mov_b32 v77, v76\n\t" \
  "s_nop 1\n\t" \
  "v_permlane32_swap_b32 v76, v77\n\t" \
  "v_max_f32 v76, v76, v77\n\t" \
  "v_mul_f32 v76, %[cs], v76\n\t" \
  "v_fma_f32 v64, v64, %[cs], -v76\n\t" \
  "v_fma_f32 v65, v65, %[cs], -v76\n\t" \
  "v_fma_f32 v66, v66, %[cs], -v76\n\t" \
  "v_fma_f32 v67, v67, %[cs], -v76\n\t" \
  "v_fma_f32 v68, v68, %[cs], -v76\n\t" \
  "v_fma_f32 v69, v69, %[cs], -v76\n\t" \
  "v_fma_f32 v70, v70, %[cs], -v76\n\t" \
  "v_fma_f32 v71, v71, %[cs], -v76\n\t" \
  "v_fma_f32 v72, v72, %[cs], -v76\n\t" \
  "v_fma_f32 v73, v73, %[cs], -v76\n\t" \
  "v_fma_f32 v74, v74, %[cs], -v76\n\t" \
  "v_fma_f32 v75, v75, %[cs], -v76\n\t" \
  "v_exp_f32 v64, v64\n\t" \
  "v_exp_f32 v65, v65\n\t" \
  "v_exp_f32 v66, v66\n\t" \
  "v_exp_f32 v67, v67\n\t" \
  "v_exp_f32 v68, v68\n\t" \
  "v_exp_f32 v69, v69\n\t" \
  "v_exp_f32 v70, v70\n\t" \
  "v_exp_f32 v71, v71\n\t" \
  "v_exp_f32 v72, v72\n\t" \
  "v_exp_f32 v73, v73\n\t" \
  "v_exp_f32 v74, v74\n\t" \
  "v_exp_f32 v75, v75\n\t" \
  "v_add_f32 v76, v72, v73\n\t" \
  "v_add_f32 v77, v74, v75\n\t" \
  "v_add_f32 v78, v64, v65\n\t" \
  "v_add_f32 v79, v66, v67\n\t" \
  "v_add_f32 v76, v76, v77\n\t" \
  "v_add_f32 v78, v78, v68\n\t" \
  "v_add_f32 v79, v79, v69\n\t" \
  "v_add_f32 v78, v78, v70\n\t" \
  "v_add_f32 v79, v79, v71\n\t" \
  "v_fma_f32 v76, v76, 0.5, v78\n\t" \
  "v_add_f32 v76, v76, v79\n\t" \
  "v_mov_b32 v77, v76\n\t" \
  "s_nop 1\n\t" \
  "v_permlane16_swap_b32 v76, v77\n\t" \
  "v_add_f32 v76, v76, v77\n\t" \
  "v_mov_b32 v77, v76\n\t" \
  "s_nop 1\n\t" \
  "v_permlane32_swap_b32 v76, v77\n\t" \
  "v_add_f32 v76, v76, v77\n\t" \
  "v_rcp_f32 v76, v76\n\t" \
  "s_mov_b64 exec, 0xff\n\t" \
  "buffer_load_dword v118, v112, %[rh], s96 offen sc1\n\t" \
  "s_mov_b64 exec, -1\n\t" \
  "v_mul_f32 v64, v64, v76\n\t" \
  "v_mul_f32 v65, v65, v76\n\t" \
  "v_mul_f32 v66, v66, v76\n\t" \
  "v_mul_f32 v67, v67, v76\n\t" \
  "v_mul_f32 v68, v68, v76\n\t" \
  "v_mul_f32 v69, v69, v76\n\t" \
  "v_mul_f32 v70, v70, v76\n\t" \
  "v_mul_f32 v71, v71, v76\n\t" \
  "v_mul_f32 v72, v72, v76\n\t" \
  "v_mul_f32 v73, v73, v76\n\t" \
  "v_mul_f32 v74, v74, v76\n\t" \
  "v_mul_f32 v75, v75, v76\n\t" \
  "v_cvt_pk_bf16_f32 v76, v64, v68\n\t" \
  "v_cvt_pk_bf16_f32 v77, v65, v69\n\t" \
  "v_cvt_pk_bf16_f32 v78, v66, v70\n\t" \
  "v_cvt_pk_bf16_f32 v79, v67, v71\n\t" \
  "v_cvt_pk_bf16_f32 v80, v72, v73\n\t" \
  "v_cvt_pk_bf16_f32 v81, v74, v75\n\t" \
  "v_permlane32_swap_b32 v76, v78\n\t" \
  "v_permlane32_swap_b32 v77, v79\n\t" \
  "v_mov_b64 v[82:83], v[80:81]\n\t" \
  "buffer_store_dwordx4 v[76:79], v110, %[rh], s94 offen sc1\n\t" \
  "s_nop 0\n\t" \
  "v_permlane16_swap_b32 v80, v82\n\t" \
  "v_permlane16_swap_b32 v81, v83\n\t" \
  "buffer_store_dwordx4 v[80:83], v111, %[rh], s94 offen sc1\n\t" \
  "v_readlane_b32 s76, v119, 0\n\t" \
  "v_readlane_b32 s77, v119, 1\n\t" \
  "s_mov_b32 s42, 0\n\t" \
  "s_min_i32 s43, s76, s77\n\t" \
  "s_cmp_eq_u32 s47, s97\n\t" \
  "s_cbranch_scc1 48f\n\t" \
  "s_mov_b32 s42, 32\n\t" \
  "s_cmp_le_i32 s43, s44\n\t" \
  "s_cbranch_scc1 48f\n\t" \
  "s_mov_b32 s42, 0\n\t" \
  "s_waitcnt lgkmcnt(0)\n\t" \
  "buffer_load_dwordx4 v[32:35], v100, %[ru], s97 offen offset:0 sc1\n\t" \
  "buffer_load_dwordx4 v[44:47], v100, %[ru], s97 offen offset:1024 sc1\n\t" \
  "s_waitcnt vmcnt(0)\n\t" \
  "ds_write_b128 v101, v[32:35] offset:20480\n\t" \
  "ds_write_b128 v101, v[44:47] offset:21504\n\t" \
  "48:\n\t" \
  "s_add_i32 s44, s44, 1\n\t" \
  "s_and_b32 s58, s58, 4\n\t" \
  "s_and_b32 s76, s44, 64\n\t" \
  "s_or_b32 s42, s42, s76\n\t" \
  "s_cbranch_scc0 20b\n\t" \
  "s_branch 99f\n\t" \
  "14:\n\t" \
  "s_sleep 3\n\t" \
  "s_add_u32 s59, s59, 0x10000\n\t" \
  "s_cmp_lt_u32 s59, 0x400000\n\t" \
  "s_cbranch_scc1 13b\n\t" \
  "s_mov_b64 exec, -1\n\t" \
  "s_mov_b32 s42, 16\n\t" \
  "s_branch 99f\n\t" \
  "8:\n\t" \
  "s_bitcmp1_b32 s58, 1\n\t" \
  "s_cbranch_scc1 4b\n\t" \
  "s_bitcmp1_b32 s58, 2\n\t" \
  "s_cbranch_scc1 5b\n\t" \
  "v_max3_u32 v116, v32, v33, v34\n\t" \
  "v_max3_u32 v117, v35, v36, v37\n\t" \
  "v_max3_u32 v116, v116, v38, v39\n\t" \
  "v_max_u32 v116, v116, v117\n\t" \
  "v_cmp_eq_u32 vcc, -1, v116\n\t" \
  "s_cbranch_vccz 5b\n\t" \
  "s_branch 0f\n\t" \
  "9:\n\t" \
  "s_bitcmp1_b32 s58, 2\n\t" \
  "s_cbranch_scc1 6b\n\t" \
  "v_max3_u32 v116, v40, v41, v42\n\t" \
  "v_max3_u32 v117, v43, v44, v45\n\t" \
  "v_max3_u32 v116, v116, v46, v47\n\t" \
  "v_max_u32 v116, v116, v117\n\t" \
  "v_cmp_eq_u32 vcc, -1, v116\n\t" \
  "s_cbranch_vccz 6b\n\t" \
  "s_or_b32 s58, s58, 2\n\t" \
  "0:\n\t" \
  "s_or_b32 s58, s58, 1\n\t" \
  "11:\n\t" \
  "buffer_load_dwordx4 v[32:35], v100, %[rh], s40 offen sc1\n\t" \
  "buffer_load_dwordx4 v[36:39], v100, %[rh], s76 offen sc1\n\t" \
  "buffer_load_dwordx4 v[40:43], v100, %[rh], s77 offen sc1\n\t" \
  "buffer_load_dwordx4 v[44:47], v100, %[rh], s78 offen sc1\n\t" \
  "s_waitcnt lgkmcnt(0)\n\t" \
  "s_branch 10b\n\t" \
  "17:\n\t" \
  "s_add_u32 s59, s59, 1\n\t" \
  "s_bitcmp1_b32 s58, 2\n\t" \
  "s_cbranch_scc1 3b\n\t" \
  "s_waitcnt vmcnt(0)\n\t" \
  "v_max_u32 v116, v32, v33\n\t" \
  "v_max3_u32 v116, v116, v34, v35\n\t" \
  "v_max3_u32 v116, v116, v36, v37\n\t" \
  "v_max3_u32 v116, v116, v38, v39\n\t" \
  "v_max3_u32 v116, v116, v40, v41\n\t" \
  "v_max3_u32 v116, v116, v42, v43\n\t" \
  "v_max3_u32 v116, v116, v44, v45\n\t" \
  "v_max3_u32 v116, v116, v46, v47\n\t" \
  "v_cmp_eq_u32 vcc, -1, v116\n\t" \
  "s_cbranch_vccz 3b\n\t" \
  "s_bitcmp1_b32 s59, 6\n\t" \
  "s_cbranch_scc1 12f\n\t" \
  "buffer_load_dwordx4 v[32:35], v100, %[rh], s40 offen sc1\n\t" \
  "buffer_load_dwordx4 v[36:39], v100, %[rh], s76 offen sc1\n\t" \
  "buffer_load_dwordx4 v[40:43], v100, %[rh], s77 offen sc1\n\t" \
  "buffer_load_dwordx4 v[44:47], v100, %[rh], s78 offen sc1\n\t" \
  "s_branch 17b\n\t" \
  "12:\n\t" \
  "s_mov_b32 s42, s58\n\t" \
  "s_branch 99f\n\t" \
  "92:\n\t" \
  "s_mov_b32 s58, s42\n\t" \
  "s_mov_b32 s95, 0\n\t" \
  "20:\n\t" \
  "s_and_b32 s76, s44, 7\n\t" \
  "s_lshl_b32 s76, s76, 15\n\t" \
  "s_add_i32 s40, s54, s76\n\t" \
  "s_and_b32 s76, s44, 3\n\t" \
  "s_lshl_b32 s45, s76, 14\n\t" \
  "s_add_i32 s77, s44, 1\n\t" \
  "s_min_u32 s77, s77, 63\n\t" \
  "s_lshr_b32 s76, s77, 1\n\t" \
  "s_lshl_b32 s47, s77, 20\n\t" \
  "s_add_i32 s47, s47, s55\n\t" \
  "s_bitcmp1_b64 %[sel], s76\n\t" \
  "s_cselect_b32 s76, 0x2000, 0\n\t" \
  "s_add_i32 s97, s47, s76\n\t" \
  "s_cmp_le_i32 s77, s43\n\t" \
  "s_cselect_b32 s47, s97, 0x70000000\n\t" \
  "s_lshr_b32 s76, s44, 1\n\t" \
  "s_lshl_b32 s94, s44, 20\n\t" \
  "s_bitcmp1_b64 %[sel], s76\n\t" \
  "s_cselect_b32 s76, 0x2000, 0\n\t" \
  "s_add_i32 s94, s94, s76\n\t" \
  "s_add_i32 s49, s44, -1\n\t" \
  "s_and_b32 s48, s49, 2\n\t" \
  "s_lshl_b32 s48, s48, 13\n\t" \
  "s_and_b32 s76, s49, 1\n\t" \
  "s_lshl_b32 s76, s76, 12\n\t" \
  "s_add_i32 s48, s48, s76\n\t" \
  "s_lshl_b32 s50, s53, 8\n\t" \
  "s_mov_b32 s59, 0\n\t" \
  "s_mov_b32 s92, s51\n\t" \
  "s_mov_b32 s93, s53\n\t" \
  "s_xor_b32 s76, s40, 0x400\n\t" \
  "s_xor_b32 s77, s40, 0x800\n\t" \
  "s_xor_b32 s78, s40, 0xc00\n\t" \
  "s_cmp_lg_u32 s58, 0\n\t" \
  "s_cbranch_scc1 11f\n\t" \
  "s_cmp_lg_u32 s95, 0\n\t" \
  "s_cbranch_scc1 46f\n\t" \
  "10:\n\t" \
  "ds_read_b128 v[48:51], v103 offset:0\n\t" \
  "ds_read_b128 v[52:55], v103 offset:1024\n\t" \
  "ds_read_b128 v[56:59], v103 offset:2048\n\t" \
  "ds_read_b128 v[60:63], v103 offset:3072\n\t" \
  "ds_read_b128 v[16:19], v103 offset:16384\n\t" \
  "ds_read_b128 v[20:23], v103 offset:17408\n\t" \
  "ds_read_b128 v[24:27], v103 offset:18432\n\t" \
  "ds_read_b128 v[28:31], v103 offset:19456\n\t" \
  "ds_read_b128 v[84:87], v103 offset:49152\n\t" \
  "ds_read_b128 v[88:91], v103 offset:50176\n\t" \
  "ds_read_b128 v[92:95], v103 offset:51200\n\t" \
  "ds_read_b128 v[96:99], v103 offset:52224\n\t" \
  "s_waitcnt lgkmcnt(4)\n\t" \
  "ds_read_b128 v[0:3], v102 offset:16384\n\t" \
  "ds_read_b128 v[4:7], v102 offset:17408\n\t" \
  "ds_read_b128 v[8:11], v102 offset:18432\n\t" \
  "ds_read_b128 v[12:15], v102 offset:19456\n\t" \
  "46:\n\t" \
  "s_bitcmp1_b32 s58, 1\n\t" \
  "s_cselect_b64 s[80:81], -1, 0\n\t" \
  "s_cmp_lg_u32 s58, 0\n\t" \
  "s_cbranch_scc1 17f\n\t" \
  "s_mov_b64 exec, 0xff\n\t" \
  "s_cmp_eq_u32 s95, 0\n\t" \
  "s_cbranch_scc1 16f\n\t" \
  "s_memtime vcc\n\t" \
  "s_memrealtime s[82:83]\n\t" \
  "s_nop 4\n\t" \
  "s_memrealtime s[84:85]\n\t" \
  "s_nop 4\n\t" \
  "s_memrealtime s[56:57]\n\t" \
  "s_nop 4\n\t" \
  "s_memrealtime s[86:87]\n\t" \
  "s_nop 4\n\t" \
  "s_memrealtime s[88:89]\n\t" \
  "s_nop 4\n\t" \
  "s_memrealtime s[90:91]\n\t" \
  "s_mov_b32 s92, 0\n\t" \
  "s_waitcnt lgkmcnt(0)\n\t" \
  "s_cmp_eq_u32 s82, s84\n\t" \
  "s_addc_u32 s92, s92, 0\n\t" \
  "s_cmp_eq_u32 s82, s56\n\t" \
  "s_addc_u32 s92, s92, 0\n\t" \
  "s_cmp_eq_u32 s82, s86\n\t" \
  "s_addc_u32 s92, s92, 0\n\t" \
  "s_cmp_eq_u32 s82, s88\n\t" \
  "s_addc_u32 s92, s92, 0\n\t" \
  "s_cmp_eq_u32 s82, s90\n\t" \
  "s_addc_u32 s92, s92, 0\n\t" \
  "s_mul_i32 s92, s92, 24\n\t" \
  "s_add_i32 s92, s92, vcc_lo\n\t" \
  "s_lshl4_add_u32 s93, s82, 64\n\t" \
  "s_lshl_b32 s50, s93, 8\n\t" \
  "s_waitcnt vmcnt(2)\n\t" \
  "v_mov_b32 v116, v118\n\t" \
  "s_branch 15f\n\t" \
  "16:\n\t" \
  "s_memtime vcc\n\t" \
  "s_waitcnt lgkmcnt(0)\n\t" \
  "s_sub_i32 s79, s41, vcc_lo\n\t" \
  "s_cmp_gt_i32 s79, 64\n\t" \
  "s_cbranch_scc1 16b\n\t" \
  "13:\n\t" \
  "buffer_load_dword v116, v112, %[rh], s48 offen sc1\n\t" \
  "s_memtime vcc\n\t" \
  "s_memrealtime s[82:83]\n\t" \
  "s_nop 4\n\t" \
  "s_memrealtime s[84:85]\n\t" \
  "s_nop 4\n\t" \
  "s_memrealtime s[56:57]\n\t" \
  "s_nop 4\n\t" \
  "s_memrealtime s[86:87]\n\t" \
  "s_nop 4\n\t" \
  "s_memrealtime s[88:89]\n\t" \
  "s_nop 4\n\t" \
  "s_memrealtime s[90:91]\n\t" \
  "s_mov_b32 s92, 0\n\t" \
  "s_waitcnt lgkmcnt(0)\n\t" \
  "s_cmp_eq_u32 s82, s84\n\t" \
  "s_addc_u32 s92, s92, 0\n\t" \
  "s_cmp_eq_u32 s82, s56\n\t" \
  "s_addc_u32 s92, s92, 0\n\t" \
  "s_cmp_eq_u32 s82, s86\n\t" \
  "s_addc_u32 s92, s92, 0\n\t" \
  "s_cmp_eq_u32 s82, s88\n\t" \
  "s_addc_u32 s92, s92, 0\n\t" \
  "s_cmp_eq_u32 s82, s90\n\t" \
  "s_addc_u32 s92, s92, 0\n\t" \
  "s_mul_i32 s92, s92, 24\n\t" \
  "s_add_i32 s92, s92, vcc_lo\n\t" \
  "s_lshl4_add_u32 s93, s82, 64\n\t" \
  "s_lshl_b32 s50, s93, 8\n\t" \
  "s_waitcnt vmcnt(0)\n\t" \
  "15:\n\t" \
  "v_mov_b32 v117, 20000\n\t" \
  "v_cmp_ne_u32_sdwa vcc, v116, s49 src0_sel:BYTE_0 src1_sel:DWORD\n\t" \
  "v_subrev_u32 v116, s50, v116\n\t" \
  "v_ashrrev_i32 v116, 8, v116\n\t" \
  "s_nop 1\n\t" \
  "v_max_i32_dpp v116, v116, v116 quad_perm:[1,0,3,2] row_mask:0xf bank_mask:0xf\n\t" \
  "s_cbranch_vccnz 14f\n\t" \
  "s_nop 0\n\t" \
  "v_max_i32_dpp v116, v116, v116 quad_perm:[2,3,0,1] row_mask:0xf bank_mask:0xf\n\t" \
  "s_nop 1\n\t" \
  "v_max_i32_dpp v116, v116, v116 row_half_mirror row_mask:0xf bank_mask:0xf\n\t" \
  "v_med3_i32 v116, v116, 0, v117\n\t" \
  "v_mul_u32_u24 v116, s52, v116\n\t" \
  "v_lshrrev_b32 v116, 16, v116\n\t" \
  "s_mov_b64 exec, -1\n\t" \
  "v_readfirstlane_b32 s84, v116\n\t" \
  "s_add_i32 s84, s84, s92\n\t" \
  ".p2align 6\n\t" \
  "s_nop 0\n\t" \
  "s_nop 0\n\t" \
  "s_nop 0\n\t" \
  "s_nop 0\n\t" \
  "s_nop 0\n\t" \
  "s_nop 0\n\t" \
  "s_nop 0\n\t" \
  "s_nop 0\n\t" \
  "1:\n\t" \
  "s_memtime vcc\n\t" \
  "s_waitcnt lgkmcnt(0)\n\t" \
  "s_sub_i32 s79, s84, vcc_lo\n\t" \
  "s_cmp_gt_i32 s79, 128\n\t" \
  "s_cbranch_scc1 1b\n\t" \
  ".p2align 4\n\t" \
  "2:\n\t" \
  "s_sub_i32 s79, s79, 33\n\t" \
  "s_cmp_gt_i32 s79, 56\n\t" \
  "s_cbranch_scc1 2b\n\t" \
  "buffer_load_dwordx4 v[32:35], v100, %[rh], s40 offen sc1\n\t" \
  "s_nop 12\n\t" \
  "buffer_load_dwordx4 v[36:39], v100, %[rh], s76 offen sc1\n\t" \
  "s_nop 12\n\t" \
  "buffer_load_dwordx4 v[40:43], v100, %[rh], s77 offen sc1\n\t" \
  "s_nop 12\n\t" \
  "buffer_load_dwordx4 v[44:47], v100, %[rh], s78 offen sc1\n\t" \
  "3:\n\t" \
  "v_mov_b64 v[68:69], 0\n\t" \
  "v_mov_b64 v[70:71], 0\n\t" \
  "v_mov_b64 v[72:73], 0\n\t" \
  "v_mov_b64 v[74:75], 0\n\t" \
  "v_mov_b64 v[76:77], 0\n\t" \
  "v_mov_b64 v[78:79], 0\n\t" \
  "v_mov_b64 v[80:81], 0\n\t" \
  "v_mov_b64 v[82:83], 0\n\t" \
  "s_waitcnt lgkmcnt(0)\n\t" \
  "s_waitcnt vmcnt(3)\n\t" \
  "v_smfmac_f32_16x16x64_bf16 v[68:71], v[32:35], a[192:199], v104\n\t" \
  "ds_write_b128 v101, v[32:35] offset:8192\n\t" \
  "v_smfmac_f32_16x16x64_bf16 v[72:75], v[32:35], a[200:207], v104\n\t" \
  "v_smfmac_f32_16x16x64_bf16 v[76:79], v[32:35], v[48:55], v104\n\t" \
  "v_smfmac_f32_16x16x64_bf16 v[80:83], v[32:35], v[56:63], v104\n\t" \
  "s_waitcnt vmcnt(2)\n\t" \
  "v_smfmac_f32_16x16x64_bf16 v[68:71], v[36:39], a[208:215], v104\n\t" \
  "ds_write_b128 v101, v[36:39] offset:9216\n\t" \
  "v_smfmac_f32_16x16x64_bf16 v[72:75], v[36:39], a[216:223], v104\n\t" \
  "v_smfmac_f32_16x16x64_bf16 v[76:79], v[36:39], a[224:231], v104\n\t" \
  "v_cmp_u_f32 vcc, v68, v69\n\t" \
  "v_smfmac_f32_16x16x64_bf16 v[80:83], v[36:39], a[232:239], v104\n\t" \
  "v_cmp_u_f32 s[82:83], v70, v71\n\t" \
  "s_waitcnt vmcnt(1)\n\t" \
  "v_smfmac_f32_16x16x64_bf16 v[68:71], v[40:43], v[16:23], v104\n\t" \
  "ds_write_b128 v101, v[40:43] offset:10240\n\t" \
  "v_smfmac_f32_16x16x64_bf16 v[72:75], v[40:43], v[24:31], v104\n\t" \
  "s_or_b64 vcc, vcc, s[82:83]\n\t" \
  "s_or_b64 vcc, vcc, s[80:81]\n\t" \
  "s_waitcnt lgkmcnt(1)\n\t" \
  "v_smfmac_f32_16x16x64_bf16 v[76:79], v[40:43], a[240:247], v104\n\t" \
  "s_cbranch_scc1 8f\n\t" \
  "5:\n\t" \
  "s_barrier\n\t" \
  "4:\n\t" \
  "s_memtime s[56:57]\n\t" \
  "ds_read_b128 v[48:51], v101 offset:12288\n\t" \
  "v_smfmac_f32_16x16x64_bf16 v[80:83], v[40:43], a[248:255], v104\n\t" \
  "ds_read_b128 v[52:55], v101 offset:13312\n\t" \
  "ds_read_b128 v[56:59], v101 offset:0\n\t" \
  "s_waitcnt vmcnt(0)\n\t" \
  "v_smfmac_f32_16x16x64_bf16 v[68:71], v[44:47], v[120:127], v104\n\t" \
  "v_smfmac_f32_16x16x64_bf16 v[72:75], v[44:47], v[128:135], v104\n\t" \
  "ds_write_b128 v101, v[44:47] offset:11264\n\t" \
  "v_smfmac_f32_16x16x64_bf16 v[76:79], v[44:47], v[136:143], v104\n\t" \
  "v_cmp_u_f32 vcc, v68, v69\n\t" \
  "v_smfmac_f32_16x16x64_bf16 v[80:83], v[44:47], v[144:151], v104\n\t" \
  "v_cmp_u_f32 s[82:83], v70, v71\n\t" \
  "s_waitcnt lgkmcnt(3)\n\t" \
  "v_smfmac_f32_16x16x64_bf16 v[68:71], v[48:51], v[152:159], v104\n\t" \
  "v_smfmac_f32_16x16x64_bf16 v[72:75], v[48:51], v[160:167], v104\n\t" \
  "s_or_b64 vcc, vcc, s[82:83]\n\t" \
  "s_waitcnt lgkmcnt(0)\n\t" \
  "v_smfmac_f32_16x16x64_bf16 v[68:71], v[52:55], v[168:175], v104\n\t" \
  "s_cbranch_scc1 9f\n\t" \
  "6:\n\t" \
  "s_barrier\n\t" \
  "ds_read_b128 v[60:63], v101 offset:1024\n\t" \
  "v_smfmac_f32_16x16x64_bf16 v[72:75], v[52:55], v[176:183], v104\n\t" \
  "ds_read_b128 v[64:67], v101 offset:4096\n\t" \
  "v_smfmac_f32_16x16x64_bf16 v[68:71], v[56:59], a[0:7], v104\n\t" \
  "v_smfmac_f32_16x16x64_bf16 v[72:75], v[56:59], a[8:15], v104\n\t" \
  "ds_read_b128 v[48:51], v101 offset:5120\n\t" \
  "s_waitcnt lgkmcnt(2)\n\t" \
  "v_smfmac_f32_16x16x64_bf16 v[68:71], v[60:63], a[16:23], v104\n\t" \
  "ds_read_b128 v[52:55], v101 offset:14336\n\t" \
  "v_smfmac_f32_16x16x64_bf16 v[72:75], v[60:63], a[24:31], v104\n\t" \
  "ds_read_b128 v[56:59], v101 offset:15360\n\t" \
  "s_waitcnt lgkmcnt(3)\n\t" \
  "v_smfmac_f32_16x16x64_bf16 v[68:71], v[64:67], a[96:103], v104\n\t" \
  "ds_read_b128 v[60:63], v101 offset:2048\n\t" \
  "v_smfmac_f32_16x16x64_bf16 v[72:75], v[64:67], a[104:111], v104\n\t" \
  "s_waitcnt lgkmcnt(3)\n\t" \
  "v_smfmac_f32_16x16x64_bf16 v[68:71], v[48:51], a[112:119], v104\n\t" \
  "ds_read_b128 v[64:67], v101 offset:3072\n\t" \
  "ds_read_b128 v[16:19], v102 offset:49152\n\t" \
  "v_smfmac_f32_16x16x64_bf16 v[72:75], v[48:51], a[120:127], v104\n\t" \
  "s_waitcnt lgkmcnt(4)\n\t" \
  "v_smfmac_f32_16x16x64_bf16 v[68:71], v[52:55], v[84:91], v104\n\t" \
  "ds_read_b128 v[48:51], v101 offset:7168\n\t" \
  "ds_read_b128 v[20:23], v102 offset:50176\n\t" \
  "v_smfmac_f32_16x16x64_bf16 v[72:75], v[52:55], v[92:99], v104\n\t" \
  "s_waitcnt lgkmcnt(5)\n\t" \
  "v_smfmac_f32_16x16x64_bf16 v[68:71], v[56:59], v[216:223], v104\n\t" \
  "ds_read_b128 v[52:55], v101 offset:6144\n\t" \
  "ds_read_b128 v[24:27], v102 offset:51200\n\t" \
  "v_smfmac_f32_16x16x64_bf16 v[72:75], v[56:59], v[224:231], v104\n\t" \
  "ds_read_b128 v[28:31], v102 offset:52224\n\t" \
  "s_waitcnt lgkmcnt(7)\n\t" \
  "v_smfmac_f32_16x16x64_bf16 v[68:71], v[60:63], v[0:7], v104\n\t" \
  "v_smfmac_f32_16x16x64_bf16 v[72:75], v[60:63], v[8:15], v104\n\t" \
  "buffer_load_dwordx4 v[84:87], v107, %[ru], s94 offen offset:0 sc1\n\t" \
  "s_waitcnt lgkmcnt(6)\n\t" \
  "v_smfmac_f32_16x16x64_bf16 v[68:71], v[64:67], a[64:71], v104\n\t" \
  "ds_read_b128 v[0:3], v103 offset:32768\n\t" \
  "v_smfmac_f32_16x16x64_bf16 v[72:75], v[64:67], a[72:79], v104\n\t" \
  "ds_read_b128 v[4:7], v103 offset:33792\n\t" \
  "s_waitcnt lgkmcnt(6)\n\t" \
  "v_smfmac_f32_16x16x64_bf16 v[68:71], v[48:51], a[160:167], v104\n\t" \
  "buffer_load_dwordx4 v[88:91], v107, %[ru], s94 offen offset:1024 sc1\n\t" \
  "s_waitcnt lgkmcnt(4)\n\t" \
  "v_smfmac_f32_16x16x64_bf16 v[68:71], v[52:55], v[16:23], v104\n\t" \
  "ds_read_b128 v[8:11], v103 offset:34816\n\t" \
  "v_smfmac_f32_16x16x64_bf16 v[72:75], v[48:51], a[168:175], v104\n\t" \
  "ds_read_b128 v[12:15], v103 offset:35840\n\t" \
  "s_waitcnt lgkmcnt(4)\n\t" \
  "v_smfmac_f32_16x16x64_bf16 v[72:75], v[52:55], v[24:31], v104\n\t" \
  "v_add_f32 v44, v68, v70\n\t" \
  "v_smfmac_f32_16x16x64_bf16 v[76:79], v[56:59], v[232:239], v104\n\t" \
  "v_add_f32 v45, v69, v71\n\t" \
  "v_cvt_pk_bf16_f32 v44, v44, v45\n\t" \
  "v_smfmac_f32_16x16x64_bf16 v[80:83], v[56:59], v[240:247], v104\n\t" \
  "v_add_f32 v46, v72, v74\n\t" \
  "v_add_f32 v47, v73, v75\n\t" \
  "v_smfmac_f32_16x16x64_bf16 v[76:79], v[60:63], a[48:55], v104\n\t" \
  "v_cvt_pk_bf16_f32 v46, v46, v47\n\t" \
  "ds_write2st64_b32 v105, v44, v46 offset1:1\n\t" \
  "v_smfmac_f32_16x16x64_bf16 v[80:83], v[60:63], a[56:63], v104\n\t" \
  "ds_read_b128 v[56:59], v101 offset:12288\n\t" \
  "v_smfmac_f32_16x16x64_bf16 v[76:79], v[64:67], a[80:87], v104\n\t" \
  "ds_read_b128 v[16:19], v102 offset:0\n\t" \
  "v_smfmac_f32_16x16x64_bf16 v[80:83], v[64:67], a[88:95], v104\n\t" \
  "ds_read_b128 v[20:23], v102 offset:1024\n\t" \
  "v_smfmac_f32_16x16x64_bf16 v[76:79], v[48:51], a[176:183], v104\n\t" \
  "s_waitcnt lgkmcnt(3)\n\t" \
  "s_barrier\n\t" \
  "v_smfmac_f32_16x16x64_bf16 v[80:83], v[48:51], a[184:191], v104\n\t" \
  "buffer_load_dwordx4 v[92:95], v107, %[ru], s94 offen offset:2048 sc1\n\t" \
  "v_smfmac_f32_16x16x64_bf16 v[76:79], v[52:55], a[144:151], v104\n\t" \
  "ds_read_b128 v[60:63], v101 offset:5120\n\t" \
  "ds_read_b128 v[24:27], v102 offset:2048\n\t" \
  "v_smfmac_f32_16x16x64_bf16 v[80:83], v[52:55], a[152:159], v104\n\t" \
  "ds_read_b128 v[64:67], v101 offset:14336\n\t" \
  "buffer_load_dwordx4 v[96:99], v107, %[ru], s94 offen offset:3072 sc1\n\t" \
  "s_waitcnt lgkmcnt(5)\n\t" \
  "v_smfmac_f32_16x16x64_bf16 v[76:79], v[56:59], v[0:7], v104\n\t" \
  "ds_read_b128 v[48:51], v101 offset:0\n\t" \
  "ds_read_b128 v[28:31], v102 offset:3072\n\t" \
  "v_smfmac_f32_16x16x64_bf16 v[80:83], v[56:59], v[8:15], v104\n\t" \
  "global_load_dword v119, v109, %[kc] sc1\n\t" \
  "s_waitcnt lgkmcnt(4)\n\t" \
  "v_smfmac_f32_16x16x64_bf16 v[76:79], v[60:63], a[128:135], v104\n\t" \
  "ds_read_b128 v[52:55], v101 offset:13312\n\t" \
  "v_smfmac_f32_16x16x64_bf16 v[80:83], v[60:63], a[136:143], v104\n\t" \
  "ds_read_b128 v[0:3], v102 offset:32768\n\t" \
  "buffer_load_dwordx4 v[32:35], v100, %[ru], s47 offen offset:0 sc1\n\t" \
  "s_waitcnt lgkmcnt(4)\n\t" \
  "v_smfmac_f32_16x16x64_bf16 v[76:79], v[64:67], v[200:207], v104\n\t" \
  "ds_read_b128 v[56:59], v101 offset:1024\n\t" \
  "v_smfmac_f32_16x16x64_bf16 v[80:83], v[64:67], v[208:215], v104\n\t" \
  "ds_read_b128 v[60:63], v101 offset:4096\n\t" \
  "ds_read_b128 v[4:7], v102 offset:33792\n\t" \
  "s_waitcnt lgkmcnt(6)\n\t" \
  "v_smfmac_f32_16x16x64_bf16 v[76:79], v[48:51], v[16:23], v104\n\t" \
  "ds_read_b128 v[8:11], v102 offset:34816\n\t" \
  "buffer_load_dwordx4 v[36:39], v100, %[ru], s47 offen offset:1024 sc1\n\t" \
  "s_waitcnt lgkmcnt(6)\n\t" \
  "v_smfmac_f32_16x16x64_bf16 v[80:83], v[48:51], v[24:31], v104\n\t" \
  "ds_read_b128 v[12:15], v102 offset:35840\n\t" \
  "s_waitcnt lgkmcnt(6)\n\t" \
  "v_smfmac_f32_16x16x64_bf16 v[76:79], v[52:55], v[184:191], v104\n\t" \
  "v_smfmac_f32_16x16x64_bf16 v[80:83], v[52:55], v[192:199], v104\n\t" \
  "buffer_load_dwordx4 v[40:43], v100, %[ru], s47 offen offset:2048 sc1\n\t" \
  "s_waitcnt lgkmcnt(4)\n\t" \
  "v_smfmac_f32_16x16x64_bf16 v[76:79], v[56:59], a[32:39], v104\n\t" \
  "s_waitcnt lgkmcnt(2)\n\t" \
  "v_smfmac_f32_16x16x64_bf16 v[76:79], v[60:63], v[0:7], v104\n\t" \
  "v_smfmac_f32_16x16x64_bf16 v[80:83], v[56:59], a[40:47], v104\n\t" \
  "s_waitcnt lgkmcnt(0)\n\t" \
  "v_smfmac_f32_16x16x64_bf16 v[80:83], v[60:63], v[8:15], v104\n\t" \
  "v_add_f32 v20, v76, v78\n\t" \
  "v_add_f32 v21, v77, v79\n\t" \
  "v_cvt_pk_bf16_f32 v20, v20, v21\n\t" \
  "ds_write_b32 v106, v20\n\t" \
  "s_nop 3\n\t" \
  "v_add_f32 v23, v80, v82\n\t" \
  "v_add_f32 v21, v81, v83\n\t" \
  "v_cvt_pk_bf16_f32 v21, v23, v21\n\t" \
  "ds_write_b32 v106, v21 offset:256\n\t" \
  "s_waitcnt lgkmcnt(0)\n\t" \
  "s_barrier\n\t" \
  "v_bfe_i32 v64, v114, 0, 1\n\t" \
  "v_and_b32 v64, 0xff800000, v64\n\t" \
  "v_bfe_i32 v65, v114, 1, 1\n\t" \
  "v_and_b32 v65, 0xff800000, v65\n\t" \
  "v_bfe_i32 v66, v114, 2, 1\n\t" \
  "v_and_b32 v66, 0xff800000, v66\n\t" \
  "v_bfe_i32 v67, v114, 3, 1\n\t" \
  "v_and_b32 v67, 0xff800000, v67\n\t" \
  "v_bfe_i32 v68, v114, 4, 1\n\t" \
  "v_and_b32 v68, 0xff800000, v68\n\t" \
  "v_bfe_i32 v69, v114, 5, 1\n\t" \
  "v_and_b32 v69, 0xff800000, v69\n\t" \
  "v_bfe_i32 v70, v114, 6, 1\n\t" \
  "v_and_b32 v70, 0xff800000, v70\n\t" \
  "v_bfe_i32 v71, v114, 7, 1\n\t" \
  "v_and_b32 v71, 0xff800000, v71\n\t" \
  "s_lshl_b32 s94, s45, 4\n\t" \
  "s_and_b32 s96, s44, 2\n\t" \
  "s_lshl_b32 s96, s96, 13\n\t" \
  "s_and_b32 s76, s44, 1\n\t" \
  "s_lshl_b32 s76, s76, 12\n\t" \
  "s_add_i32 s96, s96, s76\n\t" \
  "s_mov_b32 s95, 1\n\t" \
  "s_add_i32 s41, s56, 1950\n\t" \
  "s_sub_i32 s76, s92, s51\n\t" \
  "s_sub_i32 s77, s93, s53\n\t" \
  "s_cmp_eq_u32 s76, 0\n\t" \
  "s_cbranch_scc1 21f\n\t" \
  "v_cvt_f32_u32 v117, s76\n\t" \
  "v_cvt_f32_u32 v116, s77\n\t" \
  "v_rcp_f32 v117, v117\n\t" \
  "s_mov_b32 s51, s92\n\t" \
  "v_mul_f32 v117, v117, v116\n\t" \
  "v_max_f32 v117, 0x3f028f5c, v117\n\t" \
  "v_min_f32 v117, 0x3ffeb852, v117\n\t" \
  "v_rcp_f32 v116, v117\n\t" \
  "v_mul_f32 v117, 0x4f000000, v117\n\t" \
  "v_mul_f32 v116, 0x47800000, v116\n\t" \
  "v_cvt_u32_f32 v117, v117\n\t" \
  "v_cvt_u32_f32 v116, v116\n\t" \
  "s_mov_b32 s53, s93\n\t" \
  "v_readfirstlane_b32 s46, v117\n\t" \
  "v_readfirstlane_b32 s52, v116\n\t" \
  "21:\n\t" \
  "ds_read_b128 v[0:3], v101 offset:20480\n\t" \
  "ds_read_b128 v[4:7], v101 offset:21504\n\t" \
  "ds_read_b128 v[8:11], v101 offset:22528\n\t" \
  "ds_read_b128 v[12:15], v101 offset:23552\n\t" \
  "ds_read_b128 v[16:19], v101 offset:24576\n\t" \
  "ds_read_b128 v[20:23], v101 offset:25600\n\t" \
  "ds_read_b128 v[24:27], v101 offset:26624\n\t" \
  "ds_read_b128 v[28:31], v101 offset:27648\n\t" \
  "ds_read_b64_tr_b16 v[48:49], v108 offset:0\n\t" \
  "ds_read_b64_tr_b16 v[50:51], v108 offset:64\n\t" \
  "ds_read_b64_tr_b16 v[52:53], v108 offset:512\n\t" \
  "ds_read_b64_tr_b16 v[54:55], v108 offset:576\n\t" \
  "ds_read_b64_tr_b16 v[56:57], v108 offset:1024\n\t" \
  "ds_read_b64_tr_b16 v[58:59], v108 offset:1088\n\t" \
  "ds_read_b64_tr_b16 v[60:61], v108 offset:1536\n\t" \
  "ds_read_b64_tr_b16 v[62:63], v108 offset:1600\n\t" \
  "ds_read_b64_tr_b16 v[76:77], v108 offset:12288\n\t" \
  "ds_read_b64_tr_b16 v[78:79], v108 offset:12352\n\t" \
  "s_waitcnt vmcnt(4) lgkmcnt(10)\n\t" \
  "v_mfma_f32_16x16x32_bf16 v[64:67], v[0:3], v[84:87], v[64:67]\n\t" \
  "v_mfma_f32_16x16x32_bf16 v[68:71], v[16:19], v[84:87], v[68:71]\n\t" \
  "v_mfma_f32_16x16x32_bf16 v[64:67], v[4:7], v[88:91], v[64:67]\n\t" \
  "v_mfma_f32_16x16x32_bf16 v[68:71], v[20:23], v[88:91], v[68:71]\n\t" \
  "v_mfma_f32_16x16x32_bf16 v[64:67], v[8:11], v[92:95], v[64:67]\n\t" \
  "v_mfma_f32_16x16x32_bf16 v[68:71], v[24:27], v[92:95], v[68:71]\n\t" \
  "v_mfma_f32_16x16x32_bf16 v[64:67], v[12:15], v[96:99], v[64:67]\n\t" \
  "v_mfma_f32_16x16x32_bf16 v[68:71], v[28:31], v[96:99], v[68:71]\n\t" \
  "s_waitcnt lgkmcnt(0)\n\t" \
  "s_barrier\n\t" \
  "v_mfma_f32_16x16x32_bf16 v[72:75], v[48:51], v[84:87], 0\n\t" \
  "v_mfma_f32_16x16x32_bf16 v[72:75], v[52:55], v[88:91], v[72:75]\n\t" \
  "v_mfma_f32_16x16x32_bf16 v[72:75], v[56:59], v[92:95], v[72:75]\n\t" \
  "v_mfma_f32_16x16x32_bf16 v[72:75], v[60:63], v[96:99], v[72:75]\n\t" \
  "v_mfma_f32_16x16x32_bf16 v[72:75], v[76:79], v[84:87], v[72:75]\n\t" \
  "s_waitcnt vmcnt(0)\n\t" \
  "ds_write_b128 v101, v[32:35] offset:22528\n\t" \
  "ds_write_b128 v101, v[36:39] offset:23552\n\t" \
  "ds_write_b128 v101, v[40:43] offset:24576\n\t" \
  "ds_read_b128 v[48:51], v103 offset:0\n\t" \
  "ds_read_b128 v[52:55], v103 offset:1024\n\t" \
  "ds_read_b128 v[56:59], v103 offset:2048\n\t" \
  "ds_read_b128 v[60:63], v103 offset:3072\n\t" \
  "ds_read_b128 v[16:19], v103 offset:16384\n\t" \
  "ds_read_b128 v[20:23], v103 offset:17408\n\t" \
  "ds_read_b128 v[24:27], v103 offset:18432\n\t" \
  "ds_read_b128 v[28:31], v103 offset:19456\n\t" \
  "ds_read_b128 v[84:87], v103 offset:49152\n\t" \
  "ds_read_b128 v[88:91], v103 offset:50176\n\t" \
  "ds_read_b128 v[92:95], v103 offset:51200\n\t" \
  "ds_read_b128 v[96:99], v103 offset:52224\n\t" \
  "ds_read_b128 v[0:3], v102 offset:16384\n\t" \
  "ds_read_b128 v[4:7], v102 offset:17408\n\t" \
  "ds_read_b128 v[8:11], v102 offset:18432\n\t" \
  "ds_read_b128 v[12:15], v102 offset:19456\n\t" \
  "v_max3_f32 v76, v64, v65, v66\n\t" \
  "v_max3_f32 v77, v67, v68, v69\n\t" \
  "v_max3_f32 v76, v76, v70, v71\n\t" \
  "v_max3_f32 v77, v77, v72, v73\n\t" \
  "v_max3_f32 v76, v76, v74, v75\n\t" \
  "v_max_f32 v76, v76, v77\n\t" \
  "v_mov_b32 v77, v76\n\t" \
  "s_nop 1\n\t" \
  "v_permlane16_swap_b32 v76, v77\n\t" \
  "v_max_f32 v76, v76, v77\n\t" \
  "v_mov_b32 v77, v76\n\t" \
  "s_nop 1\n\t" \
  "v_permlane32_swap_b32 v76, v77\n\t" \
  "v_max_f32 v76, v76, v77\n\t" \
  "v_mul_f32 v76, %[cs], v76\n\t" \
  "v_fma_f32 v64, v64, %[cs], -v76\n\t" \
  "v_fma_f32 v65, v65, %[cs], -v76\n\t" \
  "v_fma_f32 v66, v66, %[cs], -v76\n\t" \
  "v_fma_f32 v67, v67, %[cs], -v76\n\t" \
  "v_fma_f32 v68, v68, %[cs], -v76\n\t" \
  "v_fma_f32 v69, v69, %[cs], -v76\n\t" \
  "v_fma_f32 v70, v70, %[cs], -v76\n\t" \
  "v_fma_f32 v71, v71, %[cs], -v76\n\t" \
  "v_fma_f32 v72, v72, %[cs], -v76\n\t" \
  "v_fma_f32 v73, v73, %[cs], -v76\n\t" \
  "v_fma_f32 v74, v74, %[cs], -v76\n\t" \
  "v_fma_f32 v75, v75, %[cs], -v76\n\t" \
  "v_exp_f32 v64, v64\n\t" \
  "v_exp_f32 v65, v65\n\t" \
  "v_exp_f32 v66, v66\n\t" \
  "v_exp_f32 v67, v67\n\t" \
  "v_exp_f32 v68, v68\n\t" \
  "v_exp_f32 v69, v69\n\t" \
  "v_exp_f32 v70, v70\n\t" \
  "v_exp_f32 v71, v71\n\t" \
  "v_exp_f32 v72, v72\n\t" \
  "v_exp_f32 v73, v73\n\t" \
  "v_exp_f32 v74, v74\n\t" \
  "v_exp_f32 v75, v75\n\t" \
  "v_add_f32 v76, v72, v73\n\t" \
  "v_add_f32 v77, v74, v75\n\t" \
  "v_add_f32 v78, v64, v65\n\t" \
  "v_add_f32 v79, v66, v67\n\t" \
  "v_add_f32 v76, v76, v77\n\t" \
  "v_add_f32 v78, v78, v68\n\t" \
  "v_add_f32 v79, v79, v69\n\t" \
  "v_add_f32 v78, v78, v70\n\t" \
  "v_add_f32 v79, v79, v71\n\t" \
  "v_fma_f32 v76, v76, 0.5, v78\n\t" \
  "v_add_f32 v76, v76, v79\n\t" \
  "v_mov_b32 v77, v76\n\t" \
  "s_nop 1\n\t" \
  "v_permlane16_swap_b32 v76, v77\n\t" \
  "v_add_f32 v76, v76, v77\n\t" \
  "v_mov_b32 v77, v76\n\t" \
  "s_nop 1\n\t" \
  "v_permlane32_swap_b32 v76, v77\n\t" \
  "v_add_f32 v76, v76, v77\n\t" \
  "v_rcp_f32 v76, v76\n\t" \
  "s_mov_b64 exec, 0xff\n\t" \
  "buffer_load_dword v118, v112, %[rh], s96 offen sc1\n\t" \
  "s_mov_b64 exec, -1\n\t" \
  "v_mul_f32 v64, v64, v76\n\t" \
  "v_mul_f32 v65, v65, v76\n\t" \
  "v_mul_f32 v66, v66, v76\n\t" \
  "v_mul_f32 v67, v67, v76\n\t" \
  "v_mul_f32 v68, v68, v76\n\t" \
  "v_mul_f32 v69, v69, v76\n\t" \
  "v_mul_f32 v70, v70, v76\n\t" \
  "v_mul_f32 v71, v71, v76\n\t" \
  "v_mul_f32 v72, v72, v76\n\t" \
  "v_mul_f32 v73, v73, v76\n\t" \
  "v_mul_f32 v74, v74, v76\n\t" \
  "v_mul_f32 v75, v75, v76\n\t" \
  "v_cvt_pk_bf16_f32 v76, v64, v68\n\t" \
  "v_cvt_pk_bf16_f32 v77, v65, v69\n\t" \
  "v_cvt_pk_bf16_f32 v78, v66, v70\n\t" \
  "v_cvt_pk_bf16_f32 v79, v67, v71\n\t" \
  "v_cvt_pk_bf16_f32 v80, v72, v73\n\t" \
  "v_cvt_pk_bf16_f32 v81, v74, v75\n\t" \
  "v_permlane32_swap_b32 v76, v78\n\t" \
  "v_permlane32_swap_b32 v77, v79\n\t" \
  "v_mov_b64 v[82:83], v[80:81]\n\t" \
  "buffer_store_dwordx4 v[76:79], v110, %[rh], s94 offen sc1\n\t" \
  "s_nop 0\n\t" \
  "v_permlane16_swap_b32 v80, v82\n\t" \
  "v_permlane16_swap_b32 v81, v83\n\t" \
  "buffer_store_dwordx4 v[80:83], v111, %[rh], s94 offen sc1\n\t" \
  "v_readlane_b32 s76, v119, 0\n\t" \
  "v_readlane_b32 s77, v119, 1\n\t" \
  "s_mov_b32 s42, 0\n\t" \
  "s_min_i32 s43, s76, s77\n\t" \
  "s_cmp_eq_u32 s47, s97\n\t" \
  "s_cbranch_scc1 48f\n\t" \
  "s_mov_b32 s42, 32\n\t" \
  "s_cmp_le_i32 s43, s44\n\t" \
  "s_cbranch_scc1 48f\n\t" \
  "s_mov_b32 s42, 0\n\t" \
  "s_waitcnt lgkmcnt(0)\n\t" \
  "buffer_load_dwordx4 v[32:35], v100, %[ru], s97 offen offset:0 sc1\n\t" \
  "buffer_load_dwordx4 v[36:39], v100, %[ru], s97 offen offset:1024 sc1\n\t" \
  "buffer_load_dwordx4 v[40:43], v100, %[ru], s97 offen offset:2048 sc1\n\t" \
  "s_waitcnt vmcnt(0)\n\t" \
  "ds_write_b128 v101, v[32:35] offset:22528\n\t" \
  "ds_write_b128 v101, v[36:39] offset:23552\n\t" \
  "ds_write_b128 v101, v[40:43] offset:24576\n\t" \
  "48:\n\t" \
  "s_add_i32 s44, s44, 1\n\t" \
  "s_and_b32 s58, s58, 4\n\t" \
  "s_and_b32 s76, s44, 64\n\t" \
  "s_or_b32 s42, s42, s76\n\t" \
  "s_cbranch_scc0 20b\n\t" \
  "s_branch 99f\n\t" \
  "14:\n\t" \
  "s_sleep 3\n\t" \
  "s_add_u32 s59, s59, 0x10000\n\t" \
  "s_cmp_lt_u32 s59, 0x400000\n\t" \
  "s_cbranch_scc1 13b\n\t" \
  "s_mov_b64 exec, -1\n\t" \
  "s_mov_b32 s42, 16\n\t" \
  "s_branch 99f\n\t" \
  "8:\n\t" \
  "s_bitcmp1_b32 s58, 1\n\t" \
  "s_cbranch_scc1 4b\n\t" \
  "s_bitcmp1_b32 s58, 2\n\t" \
  "s_cbranch_scc1 5b\n\t" \
  "v_max3_u32 v116, v32, v33, v34\n\t" \
  "v_max3_u32 v117, v35, v36, v37\n\t" \
  "v_max3_u32 v116, v116, v38, v39\n\t" \
  "v_max_u32 v116, v116, v117\n\t" \
  "v_cmp_eq_u32 vcc, -1, v116\n\t" \
  "s_cbranch_vccz 5b\n\t" \
  "s_branch 0f\n\t" \
  "9:\n\t" \
  "s_bitcmp1_b32 s58, 2\n\t" \
  "s_cbranch_scc1 6b\n\t" \
  "v_max3_u32 v116, v40, v41, v42\n\t" \
  "v_max3_u32 v117, v43, v44, v45\n\t" \
  "v_max3_u32 v116, v116, v46, v47\n\t" \
  "v_max_u32 v116, v116, v117\n\t" \
  "v_cmp_eq_u32 vcc, -1, v116\n\t" \
  "s_cbranch_vccz 6b\n\t" \
  "s_or_b32 s58, s58, 2\n\t" \
  "0:\n\t" \
  "s_or_b32 s58, s58, 1\n\t" \
  "11:\n\t" \
  "buffer_load_dwordx4 v[32:35], v100, %[rh], s40 offen sc1\n\t" \
  "buffer_load_dwordx4 v[36:39], v100, %[rh], s76 offen sc1\n\t" \
  "buffer_load_dwordx4 v[40:43], v100, %[rh], s77 offen sc1\n\t" \
  "buffer_load_dwordx4 v[44:47], v100, %[rh], s78 offen sc1\n\t" \
  "s_waitcnt lgkmcnt(0)\n\t" \
  "s_branch 10b\n\t" \
  "17:\n\t" \
  "s_add_u32 s59, s59, 1\n\t" \
  "s_bitcmp1_b32 s58, 2\n\t" \
  "s_cbranch_scc1 3b\n\t" \
  "s_waitcnt vmcnt(0)\n\t" \
  "v_max_u32 v116, v32, v33\n\t" \
  "v_max3_u32 v116, v116, v34, v35\n\t" \
  "v_max3_u32 v116, v116, v36, v37\n\t" \
  "v_max3_u32 v116, v116, v38, v39\n\t" \
  "v_max3_u32 v116, v116, v40, v41\n\t" \
  "v_max3_u32 v116, v116, v42, v43\n\t" \
  "v_max3_u32 v116, v116, v44, v45\n\t" \
  "v_max3_u32 v116, v116, v46, v47\n\t" \
  "v_cmp_eq_u32 vcc, -1, v116\n\t" \
  "s_cbranch_vccz 3b\n\t" \
  "s_bitcmp1_b32 s59, 6\n\t" \
  "s_cbranch_scc1 12f\n\t" \
  "buffer_load_dwordx4 v[32:35], v100, %[rh], s40 offen sc1\n\t" \
  "buffer_load_dwordx4 v[36:39], v100, %[rh], s76 offen sc1\n\t" \
  "buffer_load_dwordx4 v[40:43], v100, %[rh], s77 offen sc1\n\t" \
  "buffer_load_dwordx4 v[44:47], v100, %[rh], s78 offen sc1\n\t" \
  "s_branch 17b\n\t" \
  "12:\n\t" \
  "s_mov_b32 s42, s58\n\t" \
  "s_branch 99f\n\t" \
  "93:\n\t" \
  "s_mov_b32 s58, s42\n\t" \
  "20:\n\t" \
  "s_and_b32 s76, s44, 7\n\t" \
  "s_lshl_b32 s76, s76, 15\n\t" \
  "s_add_i32 s40, s54, s76\n\t" \
  "s_and_b32 s76, s44, 3\n\t" \
  "s_lshl_b32 s45, s76, 14\n\t" \
  "s_add_i32 s77, s44, 1\n\t" \
  "s_min_u32 s77, s77, 63\n\t" \
  "s_lshr_b32 s76, s77, 1\n\t" \
  "s_lshl_b32 s47, s77, 20\n\t" \
  "s_add_i32 s47, s47, s55\n\t" \
  "s_bitcmp1_b64 %[sel], s76\n\t" \
  "s_cselect_b32 s76, 0x2000, 0\n\t" \
  "s_add_i32 s97, s47, s76\n\t" \
  "s_cmp_le_i32 s77, s43\n\t" \
  "s_cselect_b32 s47, s97, 0x70000000\n\t" \
  "s_add_i32 s49, s44, -1\n\t" \
  "s_and_b32 s48, s49, 2\n\t" \
  "s_lshl_b32 s48, s48, 13\n\t" \
  "s_and_b32 s76, s49, 1\n\t" \
  "s_lshl_b32 s76, s76, 12\n\t" \
  "s_add_i32 s48, s48, s76\n\t" \
  "s_lshl_b32 s50, s53, 8\n\t" \
  "s_mov_b32 s59, 0\n\t" \
  "s_mov_b32 s92, s51\n\t" \
  "s_mov_b32 s93, s53\n\t" \
  "s_xor_b32 s76, s40, 0x400\n\t" \
  "s_xor_b32 s77, s40, 0x800\n\t" \
  "s_xor_b32 s78, s40, 0xc00\n\t" \
  "s_cmp_lg_u32 s58, 0\n\t" \
  "s_cbranch_scc1 11f\n\t" \
  "10:\n\t" \
  "ds_read_b128 v[48:51], v103 offset:32768\n\t" \
  "ds_read_b128 v[52:55], v103 offset:33792\n\t" \
  "ds_read_b128 v[56:59], v103 offset:34816\n\t" \
  "ds_read_b128 v[60:63], v103 offset:35840\n\t" \
  "ds_read_b128 v[16:19], v103 offset:49152\n\t" \
  "ds_read_b128 v[20:23], v103 offset:50176\n\t" \
  "ds_read_b128 v[24:27], v103 offset:51200\n\t" \
  "ds_read_b128 v[28:31], v103 offset:52224\n\t" \
  "ds_read_b128 v[84:87], v102 offset:16384\n\t" \
  "ds_read_b128 v[88:91], v102 offset:17408\n\t" \
  "ds_read_b128 v[92:95], v102 offset:18432\n\t" \
  "ds_read_b128 v[96:99], v102 offset:19456\n\t" \
  "s_waitcnt lgkmcnt(4)\n\t" \
  "ds_read_b128 v[0:3], v102 offset:49152\n\t" \
  "ds_read_b128 v[4:7], v102 offset:50176\n\t" \
  "ds_read_b128 v[8:11], v102 offset:51200\n\t" \
  "ds_read_b128 v[12:15], v102 offset:52224\n\t" \
  "46:\n\t" \
  "s_bitcmp1_b32 s58, 1\n\t" \
  "s_cselect_b64 s[80:81], -1, 0\n\t" \
  "s_cmp_lg_u32 s58, 0\n\t" \
  "s_cbranch_scc1 17f\n\t" \
  "s_mov_b64 exec, 0xff\n\t" \
  "16:\n\t" \
  "s_memtime vcc\n\t" \
  "s_waitcnt lgkmcnt(0)\n\t" \
  "s_sub_i32 s79, s41, vcc_lo\n\t" \
  "s_cmp_gt_i32 s79, 64\n\t" \
  "s_cbranch_scc1 16b\n\t" \
  "13:\n\t" \
  "buffer_load_dword v116, v112, %[rh], s48 offen sc1\n\t" \
  "s_memtime vcc\n\t" \
  "s_memrealtime s[82:83]\n\t" \
  "s_nop 4\n\t" \
  "s_memrealtime s[84:85]\n\t" \
  "s_nop 4\n\t" \
  "s_memrealtime s[56:57]\n\t" \
  "s_nop 4\n\t" \
  "s_memrealtime s[86:87]\n\t" \
  "s_nop 4\n\t" \
  "s_memrealtime s[88:89]\n\t" \
  "s_nop 4\n\t" \
  "s_memrealtime s[90:91]\n\t" \
  "s_mov_b32 s92, 0\n\t" \
  "s_waitcnt lgkmcnt(0)\n\t" \
  "s_cmp_eq_u32 s82, s84\n\t" \
  "s_addc_u32 s92, s92, 0\n\t" \
  "s_cmp_eq_u32 s82, s56\n\t" \
  "s_addc_u32 s92, s92, 0\n\t" \
  "s_cmp_eq_u32 s82, s86\n\t" \
  "s_addc_u32 s92, s92, 0\n\t" \
  "s_cmp_eq_u32 s82, s88\n\t" \
  "s_addc_u32 s92, s92, 0\n\t" \
  "s_cmp_eq_u32 s82, s90\n\t" \
  "s_addc_u32 s92, s92, 0\n\t" \
  "s_mul_i32 s92, s92, 24\n\t" \
  "s_add_i32 s92, s92, vcc_lo\n\t" \
  "s_lshl4_add_u32 s93, s82, 64\n\t" \
  "s_lshl_b32 s50, s93, 8\n\t" \
  "s_waitcnt vmcnt(0)\n\t" \
  "15:\n\t" \
  "v_mov_b32 v117, 20000\n\t" \
  "v_cmp_ne_u32_sdwa vcc, v116, s49 src0_sel:BYTE_0 src1_sel:DWORD\n\t" \
  "v_subrev_u32 v116, s50, v116\n\t" \
  "v_ashrrev_i32 v116, 8, v116\n\t" \
  "s_nop 1\n\t" \
  "v_max_i32_dpp v116, v116, v116 quad_perm:[1,0,3,2] row_mask:0xf bank_mask:0xf\n\t" \
  "s_cbranch_vccnz 14f\n\t" \
  "s_nop 0\n\t" \
  "v_max_i32_dpp v116, v116, v116 quad_perm:[2,3,0,1] row_mask:0xf bank_mask:0xf\n\t" \
  "s_nop 1\n\t" \
  "v_max_i32_dpp v116, v116, v116 row_half_mirror row_mask:0xf bank_mask:0xf\n\t" \
  "v_med3_i32 v116, v116, 0, v117\n\t" \
  "v_mul_u32_u24 v116, s52, v116\n\t" \
  "v_lshrrev_b32 v116, 16, v116\n\t" \
  "s_mov_b64 exec, -1\n\t" \
  "v_readfirstlane_b32 s84, v116\n\t" \
  "s_add_i32 s84, s84, s92\n\t" \
  ".p2align 6\n\t" \
  "s_nop 0\n\t" \
  "s_nop 0\n\t" \
  "s_nop 0\n\t" \
  "s_nop 0\n\t" \
  "s_nop 0\n\t" \
  "s_nop 0\n\t" \
  "s_nop 0\n\t" \
  "s_nop 0\n\t" \
  "1:\n\t" \
  "s_memtime vcc\n\t" \
  "s_waitcnt lgkmcnt(0)\n\t" \
  "s_sub_i32 s79, s84, vcc_lo\n\t" \
  "s_cmp_gt_i32 s79, 128\n\t" \
  "s_cbranch_scc1 1b\n\t" \
  ".p2align 4\n\t" \
  "2:\n\t" \
  "s_sub_i32 s79, s79, 33\n\t" \
  "s_cmp_gt_i32 s79, 56\n\t" \
  "s_cbranch_scc1 2b\n\t" \
  "buffer_load_dwordx4 v[32:35], v100, %[rh], s40 offen sc1\n\t" \
  "s_nop 12\n\t" \
  "buffer_load_dwordx4 v[36:39], v100, %[rh], s76 offen sc1\n\t" \
  "s_nop 12\n\t" \
  "buffer_load_dwordx4 v[40:43], v100, %[rh], s77 offen sc1\n\t" \
  "s_nop 12\n\t" \
  "buffer_load_dwordx4 v[44:47], v100, %[rh], s78 offen sc1\n\t" \
  "3:\n\t" \
  "v_mov_b64 v[68:69], 0\n\t" \
  "v_mov_b64 v[70:71], 0\n\t" \
  "v_mov_b64 v[72:73], 0\n\t" \
  "v_mov_b64 v[74:75], 0\n\t" \
  "v_mov_b64 v[76:77], 0\n\t" \
  "v_mov_b64 v[78:79], 0\n\t" \
  "v_mov_b64 v[80:81], 0\n\t" \
  "v_mov_b64 v[82:83], 0\n\t" \
  "s_waitcnt lgkmcnt(0)\n\t" \
  "s_waitcnt vmcnt(3)\n\t" \
  "v_smfmac_f32_16x16x64_bf16 v[68:71], v[32:35], v[152:159], v104\n\t" \
  "ds_write_b128 v101, v[32:35] offset:12288\n\t" \
  "v_smfmac_f32_16x16x64_bf16 v[72:75], v[32:35], v[160:167], v104\n\t" \
  "v_smfmac_f32_16x16x64_bf16 v[76:79], v[32:35], v[48:55], v104\n\t" \
  "v_smfmac_f32_16x16x64_bf16 v[80:83], v[32:35], v[56:63], v104\n\t" \
  "s_waitcnt vmcnt(2)\n\t" \
  "v_smfmac_f32_16x16x64_bf16 v[68:71], v[36:39], v[168:175], v104\n\t" \
  "ds_write_b128 v101, v[36:39] offset:13312\n\t" \
  "v_smfmac_f32_16x16x64_bf16 v[72:75], v[36:39], v[176:183], v104\n\t" \
  "v_smfmac_f32_16x16x64_bf16 v[76:79], v[36:39], v[184:191], v104\n\t" \
  "v_cmp_u_f32 vcc, v68, v69\n\t" \
  "v_smfmac_f32_16x16x64_bf16 v[80:83], v[36:39], v[192:199], v104\n\t" \
  "v_cmp_u_f32 s[82:83], v70, v71\n\t" \
  "s_waitcnt vmcnt(1)\n\t" \
  "v_smfmac_f32_16x16x64_bf16 v[68:71], v[40:43], v[16:23], v104\n\t" \
  "ds_write_b128 v101, v[40:43] offset:14336\n\t" \
  "v_smfmac_f32_16x16x64_bf16 v[72:75], v[40:43], v[24:31], v104\n\t" \
  "s_or_b64 vcc, vcc, s[82:83]\n\t" \
  "s_or_b64 vcc, vcc, s[80:81]\n\t" \
  "s_waitcnt lgkmcnt(1)\n\t" \
  "v_smfmac_f32_16x16x64_bf16 v[76:79], v[40:43], v[200:207], v104\n\t" \
  "s_cbranch_scc1 8f\n\t" \
  "5:\n\t" \
  "s_barrier\n\t" \
  "4:\n\t" \
  "s_memtime s[56:57]\n\t" \
  "ds_read_b128 v[48:51], v101 offset:0\n\t" \
  "v_smfmac_f32_16x16x64_bf16 v[80:83], v[40:43], v[208:215], v104\n\t" \
  "ds_read_b128 v[52:55], v101 offset:1024\n\t" \
  "ds_read_b128 v[56:59], v101 offset:4096\n\t" \
  "s_waitcnt vmcnt(0)\n\t" \
  "v_smfmac_f32_16x16x64_bf16 v[68:71], v[44:47], v[216:223], v104\n\t" \
  "v_smfmac_f32_16x16x64_bf16 v[72:75], v[44:47], v[224:231], v104\n\t" \
  "ds_write_b128 v101, v[44:47] offset:15360\n\t" \
  "v_smfmac_f32_16x16x64_bf16 v[76:79], v[44:47], v[232:239], v104\n\t" \
  "v_cmp_u_f32 vcc, v68, v69\n\t" \
  "v_smfmac_f32_16x16x64_bf16 v[80:83], v[44:47], v[240:247], v104\n\t" \
  "v_cmp_u_f32 s[82:83], v70, v71\n\t" \
  "s_waitcnt lgkmcnt(3)\n\t" \
  "v_smfmac_f32_16x16x64_bf16 v[68:71], v[48:51], a[0:7], v104\n\t" \
  "v_smfmac_f32_16x16x64_bf16 v[72:75], v[48:51], a[8:15], v104\n\t" \
  "s_or_b64 vcc, vcc, s[82:83]\n\t" \
  "s_waitcnt lgkmcnt(0)\n\t" \
  "v_smfmac_f32_16x16x64_bf16 v[68:71], v[52:55], a[16:23], v104\n\t" \
  "s_cbranch_scc1 9f\n\t" \
  "6:\n\t" \
  "s_barrier\n\t" \
  "ds_read_b128 v[60:63], v101 offset:5120\n\t" \
  "v_smfmac_f32_16x16x64_bf16 v[72:75], v[52:55], a[24:31], v104\n\t" \
  "ds_read_b128 v[64:67], v101 offset:8192\n\t" \
  "v_smfmac_f32_16x16x64_bf16 v[68:71], v[56:59], a[96:103], v104\n\t" \
  "v_smfmac_f32_16x16x64_bf16 v[72:75], v[56:59], a[104:111], v104\n\t" \
  "ds_read_b128 v[48:51], v101 offset:9216\n\t" \
  "s_waitcnt lgkmcnt(2)\n\t" \
  "v_smfmac_f32_16x16x64_bf16 v[68:71], v[60:63], a[112:119], v104\n\t" \
  "ds_read_b128 v[52:55], v101 offset:2048\n\t" \
  "v_smfmac_f32_16x16x64_bf16 v[72:75], v[60:63], a[120:127], v104\n\t" \
  "ds_read_b128 v[56:59], v101 offset:3072\n\t" \
  "s_waitcnt lgkmcnt(3)\n\t" \
  "v_smfmac_f32_16x16x64_bf16 v[68:71], v[64:67], a[192:199], v104\n\t" \
  "ds_read_b128 v[60:63], v101 offset:6144\n\t" \
  "v_smfmac_f32_16x16x64_bf16 v[72:75], v[64:67], a[200:207], v104\n\t" \
  "s_waitcnt lgkmcnt(3)\n\t" \
  "v_smfmac_f32_16x16x64_bf16 v[68:71], v[48:51], a[208:215], v104\n\t" \
  "ds_read_b128 v[64:67], v101 offset:7168\n\t" \
  "ds_read_b128 v[16:19], v103 offset:16384\n\t" \
  "v_smfmac_f32_16x16x64_bf16 v[72:75], v[48:51], a[216:223], v104\n\t" \
  "s_waitcnt lgkmcnt(4)\n\t" \
  "v_smfmac_f32_16x16x64_bf16 v[68:71], v[52:55], v[84:91], v104\n\t" \
  "ds_read_b128 v[48:51], v101 offset:11264\n\t" \
  "ds_read_b128 v[20:23], v103 offset:17408\n\t" \
  "v_smfmac_f32_16x16x64_bf16 v[72:75], v[52:55], v[92:99], v104\n\t" \
  "s_waitcnt lgkmcnt(5)\n\t" \
  "v_smfmac_f32_16x16x64_bf16 v[68:71], v[56:59], a[64:71], v104\n\t" \
  "ds_read_b128 v[52:55], v101 offset:10240\n\t" \
  "ds_read_b128 v[24:27], v103 offset:18432\n\t" \
  "v_smfmac_f32_16x16x64_bf16 v[72:75], v[56:59], a[72:79], v104\n\t" \
  "ds_read_b128 v[28:31], v103 offset:19456\n\t" \
  "s_waitcnt lgkmcnt(7)\n\t" \
  "v_smfmac_f32_16x16x64_bf16 v[68:71], v[60:63], v[0:7], v104\n\t" \
  "ds_read_b128 v[84:87], v103 offset:22528\n\t" \
  "v_smfmac_f32_16x16x64_bf16 v[72:75], v[60:63], v[8:15], v104\n\t" \
  "ds_read_b128 v[88:91], v103 offset:23552\n\t" \
  "s_waitcnt lgkmcnt(8)\n\t" \
  "v_smfmac_f32_16x16x64_bf16 v[68:71], v[64:67], a[160:167], v104\n\t" \
  "ds_read_b128 v[0:3], v102 offset:0\n\t" \
  "v_smfmac_f32_16x16x64_bf16 v[72:75], v[64:67], a[168:175], v104\n\t" \
  "ds_read_b128 v[4:7], v102 offset:1024\n\t" \
  "s_waitcnt lgkmcnt(8)\n\t" \
  "v_smfmac_f32_16x16x64_bf16 v[68:71], v[48:51], v[120:127], v104\n\t" \
  "ds_read_b128 v[92:95], v102 offset:22528\n\t" \
  "s_waitcnt lgkmcnt(7)\n\t" \
  "v_smfmac_f32_16x16x64_bf16 v[68:71], v[52:55], v[16:23], v104\n\t" \
  "ds_read_b128 v[8:11], v102 offset:2048\n\t" \
  "v_smfmac_f32_16x16x64_bf16 v[72:75], v[48:51], v[128:135], v104\n\t" \
  "ds_read_b128 v[12:15], v102 offset:3072\n\t" \
  "s_waitcnt lgkmcnt(7)\n\t" \
  "v_smfmac_f32_16x16x64_bf16 v[72:75], v[52:55], v[24:31], v104\n\t" \
  "v_add_f32 v44, v68, v70\n\t" \
  "ds_read_b128 v[96:99], v102 offset:23552\n\t" \
  "v_smfmac_f32_16x16x64_bf16 v[76:79], v[56:59], a[80:87], v104\n\t" \
  "v_add_f32 v45, v69, v71\n\t" \
  "v_cvt_pk_bf16_f32 v44, v44, v45\n\t" \
  "v_smfmac_f32_16x16x64_bf16 v[80:83], v[56:59], a[88:95], v104\n\t" \
  "v_add_f32 v46, v72, v74\n\t" \
  "v_add_f32 v47, v73, v75\n\t" \
  "v_smfmac_f32_16x16x64_bf16 v[76:79], v[60:63], a[144:151], v104\n\t" \
  "v_cvt_pk_bf16_f32 v46, v46, v47\n\t" \
  "ds_write2st64_b32 v105, v44, v46 offset1:1\n\t" \
  "v_smfmac_f32_16x16x64_bf16 v[80:83], v[60:63], a[152:159], v104\n\t" \
  "ds_read_b128 v[56:59], v101 offset:0\n\t" \
  "v_mov_b64 v[68:69], 0\n\t" \
  "v_smfmac_f32_16x16x64_bf16 v[76:79], v[64:67], a[176:183], v104\n\t" \
  "ds_read_b128 v[16:19], v102 offset:32768\n\t" \
  "v_mov_b64 v[70:71], 0\n\t" \
  "v_smfmac_f32_16x16x64_bf16 v[80:83], v[64:67], a[184:191], v104\n\t" \
  "ds_read_b128 v[20:23], v102 offset:33792\n\t" \
  "v_smfmac_f32_16x16x64_bf16 v[76:79], v[48:51], v[136:143], v104\n\t" \
  "s_waitcnt lgkmcnt(3)\n\t" \
  "s_barrier\n\t" \
  "v_smfmac_f32_16x16x64_bf16 v[80:83], v[48:51], v[144:151], v104\n\t" \
  "v_smfmac_f32_16x16x64_bf16 v[68:71], v[32:35], v[84:91], v104\n\t" \
  "v_smfmac_f32_16x16x64_bf16 v[76:79], v[52:55], a[240:247], v104\n\t" \
  "ds_read_b128 v[60:63], v101 offset:9216\n\t" \
  "ds_read_b128 v[24:27], v102 offset:34816\n\t" \
  "v_smfmac_f32_16x16x64_bf16 v[80:83], v[52:55], a[248:255], v104\n\t" \
  "ds_read_b128 v[64:67], v101 offset:2048\n\t" \
  "s_waitcnt lgkmcnt(5)\n\t" \
  "v_smfmac_f32_16x16x64_bf16 v[76:79], v[56:59], v[0:7], v104\n\t" \
  "ds_read_b128 v[48:51], v101 offset:4096\n\t" \
  "ds_read_b128 v[28:31], v102 offset:35840\n\t" \
  "v_smfmac_f32_16x16x64_bf16 v[80:83], v[56:59], v[8:15], v104\n\t" \
  "ds_read_b128 v[84:87], v102 offset:55296\n\t" \
  "global_load_dword v119, v109, %[kc] sc1\n\t" \
  "s_waitcnt lgkmcnt(5)\n\t" \
  "v_smfmac_f32_16x16x64_bf16 v[76:79], v[60:63], a[224:231], v104\n\t" \
  "ds_read_b128 v[52:55], v101 offset:1024\n\t" \
  "ds_read_b128 v[88:91], v102 offset:56320\n\t" \
  "v_smfmac_f32_16x16x64_bf16 v[80:83], v[60:63], a[232:239], v104\n\t" \
  "ds_read_b128 v[0:3], v103 offset:0\n\t" \
  "s_waitcnt lgkmcnt(6)\n\t" \
  "v_smfmac_f32_16x16x64_bf16 v[76:79], v[64:67], a[48:55], v104\n\t" \
  "ds_read_b128 v[56:59], v101 offset:5120\n\t" \
  "buffer_load_dwordx4 v[32:35], v100, %[ru], s47 offen offset:0 sc1\n\t" \
  "v_smfmac_f32_16x16x64_bf16 v[80:83], v[64:67], a[56:63], v104\n\t" \
  "ds_read_b128 v[60:63], v101 offset:8192\n\t" \
  "ds_read_b128 v[4:7], v103 offset:1024\n\t" \
  "s_waitcnt lgkmcnt(8)\n\t" \
  "v_smfmac_f32_16x16x64_bf16 v[76:79], v[48:51], v[16:23], v104\n\t" \
  "ds_read_b128 v[8:11], v103 offset:2048\n\t" \
  "s_waitcnt lgkmcnt(8)\n\t" \
  "v_smfmac_f32_16x16x64_bf16 v[80:83], v[48:51], v[24:31], v104\n\t" \
  "ds_read_b128 v[12:15], v103 offset:3072\n\t" \
  "buffer_load_dwordx4 v[36:39], v100, %[ru], s47 offen offset:1024 sc1\n\t" \
  "v_smfmac_f32_16x16x64_bf16 v[68:71], v[48:51], v[92:99], v104\n\t" \
  "s_waitcnt lgkmcnt(7)\n\t" \
  "v_smfmac_f32_16x16x64_bf16 v[76:79], v[52:55], a[32:39], v104\n\t" \
  "v_smfmac_f32_16x16x64_bf16 v[80:83], v[52:55], a[40:47], v104\n\t" \
  "s_waitcnt lgkmcnt(4)\n\t" \
  "v_smfmac_f32_16x16x64_bf16 v[76:79], v[56:59], a[128:135], v104\n\t" \
  "buffer_load_dwordx4 v[40:43], v100, %[ru], s47 offen offset:2048 sc1\n\t" \
  "s_waitcnt lgkmcnt(2)\n\t" \
  "v_smfmac_f32_16x16x64_bf16 v[76:79], v[60:63], v[0:7], v104\n\t" \
  "v_smfmac_f32_16x16x64_bf16 v[68:71], v[60:63], v[84:91], v104\n\t" \
  "v_smfmac_f32_16x16x64_bf16 v[80:83], v[56:59], a[136:143], v104\n\t" \
  "s_waitcnt lgkmcnt(0)\n\t" \
  "v_smfmac_f32_16x16x64_bf16 v[80:83], v[60:63], v[8:15], v104\n\t" \
  "v_add_f32 v20, v76, v78\n\t" \
  "v_add_f32 v21, v77, v79\n\t" \
  "v_cvt_pk_bf16_f32 v20, v20, v21\n\t" \
  "ds_write_b32 v106, v20\n\t" \
  "v_add_f32 v72, v68, v70\n\t" \
  "v_add_f32 v73, v69, v71\n\t" \
  "v_cvt_pk_bf16_f32 v72, v72, v73\n\t" \
  "ds_write_b32 v106, v72 offset:11008\n\t" \
  "v_add_f32 v23, v80, v82\n\t" \
  "v_add_f32 v21, v81, v83\n\t" \
  "v_cvt_pk_bf16_f32 v21, v23, v21\n\t" \
  "ds_write_b32 v106, v21 offset:256\n\t" \
  "s_waitcnt lgkmcnt(0)\n\t" \
  "s_barrier\n\t" \
  "s_waitcnt vmcnt(0)\n\t" \
  "s_barrier\n\t" \
  "ds_write_b128 v101, v[32:35] offset:25600\n\t" \
  "ds_write_b128 v101, v[36:39] offset:26624\n\t" \
  "ds_write_b128 v101, v[40:43] offset:27648\n\t" \
  "v_readlane_b32 s76, v119, 0\n\t" \
  "v_readlane_b32 s77, v119, 1\n\t" \
  "s_mov_b32 s42, 0\n\t" \
  "s_min_i32 s43, s76, s77\n\t" \
  "s_cmp_eq_u32 s47, s97\n\t" \
  "s_cbranch_scc1 48f\n\t" \
  "s_mov_b32 s42, 32\n\t" \
  "s_cmp_le_i32 s43, s44\n\t" \
  "s_cbranch_scc1 48f\n\t" \
  "s_mov_b32 s42, 0\n\t" \
  "s_waitcnt lgkmcnt(0)\n\t" \
  "buffer_load_dwordx4 v[32:35], v100, %[ru], s97 offen offset:0 sc1\n\t" \
  "buffer_load_dwordx4 v[36:39], v100, %[ru], s97 offen offset:1024 sc1\n\t" \
  "buffer_load_dwordx4 v[40:43], v100, %[ru], s97 offen offset:2048 sc1\n\t" \
  "s_waitcnt vmcnt(0)\n\t" \
  "ds_write_b128 v101, v[32:35] offset:25600\n\t" \
  "ds_write_b128 v101, v[36:39] offset:26624\n\t" \
  "ds_write_b128 v101, v[40:43] offset:27648\n\t" \
  "48:\n\t" \
  "s_add_i32 s41, s56, 1950\n\t" \
  "s_sub_i32 s76, s92, s51\n\t" \
  "s_sub_i32 s77, s93, s53\n\t" \
  "s_cmp_eq_u32 s76, 0\n\t" \
  "s_cbranch_scc1 21f\n\t" \
  "v_cvt_f32_u32 v117, s76\n\t" \
  "v_cvt_f32_u32 v116, s77\n\t" \
  "v_rcp_f32 v117, v117\n\t" \
  "s_mov_b32 s51, s92\n\t" \
  "v_mul_f32 v117, v117, v116\n\t" \
  "v_max_f32 v117, 0x3f028f5c, v117\n\t" \
  "v_min_f32 v117, 0x3ffeb852, v117\n\t" \
  "v_rcp_f32 v116, v117\n\t" \
  "v_mul_f32 v117, 0x4f000000, v117\n\t" \
  "v_mul_f32 v116, 0x47800000, v116\n\t" \
  "v_cvt_u32_f32 v117, v117\n\t" \
  "v_cvt_u32_f32 v116, v116\n\t" \
  "s_mov_b32 s53, s93\n\t" \
  "v_readfirstlane_b32 s46, v117\n\t" \
  "v_readfirstlane_b32 s52, v116\n\t" \
  "21:\n\t" \
  "v_mov_b32 v0, -1\n\t" \
  "v_mov_b32 v1, -1\n\t" \
  "v_mov_b32 v2, -1\n\t" \
  "v_mov_b32 v3, -1\n\t" \
  "s_and_b32 s76, s49, 3\n\t" \
  "s_mul_i32 s76, s76, 0xc000\n\t" \
  "s_add_i32 s76, s76, 0x40000\n\t" \
  "buffer_store_dwordx4 v[0:3], v110, %[rh], s76 offen sc1\n\t" \
  "s_add_i32 s76, s44, 1\n\t" \
  "s_and_b32 s76, s76, 3\n\t" \
  "s_lshl_b32 s76, s76, 18\n\t" \
  "s_add_i32 s77, s76, 0x10000\n\t" \
  "buffer_store_dwordx4 v[0:3], v107, %[rh], s76 offen sc1\n\t" \
  "buffer_store_dwordx4 v[0:3], v107, %[rh], s77 offen sc1\n\t" \
  "buffer_store_dwordx4 v[0:3], v111, %[rh], s76 offen sc1\n\t" \
  "s_add_i32 s44, s44, 1\n\t" \
  "s_and_b32 s58, s58, 4\n\t" \
  "s_and_b32 s76, s44, 64\n\t" \
  "s_or_b32 s42, s42, s76\n\t" \
  "s_cbranch_scc0 20b\n\t" \
  "s_branch 99f\n\t" \
  "14:\n\t" \
  "s_sleep 3\n\t" \
  "s_add_u32 s59, s59, 0x10000\n\t" \
  "s_cmp_lt_u32 s59, 0x400000\n\t" \
  "s_cbranch_scc1 13b\n\t" \
  "s_mov_b64 exec, -1\n\t" \
  "s_mov_b32 s42, 16\n\t" \
  "s_branch 99f\n\t" \
  "8:\n\t" \
  "s_bitcmp1_b32 s58, 1\n\t" \
  "s_cbranch_scc1 4b\n\t" \
  "s_bitcmp1_b32 s58, 2\n\t" \
  "s_cbranch_scc1 5b\n\t" \
  "v_max3_u32 v116, v32, v33, v34\n\t" \
  "v_max3_u32 v117, v35, v36, v37\n\t" \
  "v_max3_u32 v116, v116, v38, v39\n\t" \
  "v_max_u32 v116, v116, v117\n\t" \
  "v_cmp_eq_u32 vcc, -1, v116\n\t" \
  "s_cbranch_vccz 5b\n\t" \
  "s_branch 0f\n\t" \
  "9:\n\t" \
  "s_bitcmp1_b32 s58, 2\n\t" \
  "s_cbranch_scc1 6b\n\t" \
  "v_max3_u32 v116, v40, v41, v42\n\t" \
  "v_max3_u32 v117, v43, v44, v45\n\t" \
  "v_max3_u32 v116, v116, v46, v47\n\t" \
  "v_max_u32 v116, v116, v117\n\t" \
  "v_cmp_eq_u32 vcc, -1, v116\n\t" \
  "s_cbranch_vccz 6b\n\t" \
  "s_or_b32 s58, s58, 2\n\t" \
  "0:\n\t" \
  "s_or_b32 s58, s58, 1\n\t" \
  "11:\n\t" \
  "buffer_load_dwordx4 v[32:35], v100, %[rh], s40 offen sc1\n\t" \
  "buffer_load_dwordx4 v[36:39], v100, %[rh], s76 offen sc1\n\t" \
  "buffer_load_dwordx4 v[40:43], v100, %[rh], s77 offen sc1\n\t" \
  "buffer_load_dwordx4 v[44:47], v100, %[rh], s78 offen sc1\n\t" \
  "s_waitcnt lgkmcnt(0)\n\t" \
  "s_branch 10b\n\t" \
  "17:\n\t" \
  "s_add_u32 s59, s59, 1\n\t" \
  "s_bitcmp1_b32 s58, 2\n\t" \
  "s_cbranch_scc1 3b\n\t" \
  "s_waitcnt vmcnt(0)\n\t" \
  "v_max_u32 v116, v32, v33\n\t" \
  "v_max3_u32 v116, v116, v34, v35\n\t" \
  "v_max3_u32 v116, v116, v36, v37\n\t" \
  "v_max3_u32 v116, v116, v38, v39\n\t" \
  "v_max3_u32 v116, v116, v40, v41\n\t" \
  "v_max3_u32 v116, v116, v42, v43\n\t" \
  "v_max3_u32 v116, v116, v44, v45\n\t" \
  "v_max3_u32 v116, v116, v46, v47\n\t" \
  "v_cmp_eq_u32 vcc, -1, v116\n\t" \
  "s_cbranch_vccz 3b\n\t" \
  "s_bitcmp1_b32 s59, 6\n\t" \
  "s_cbranch_scc1 12f\n\t" \
  "buffer_load_dwordx4 v[32:35], v100, %[rh], s40 offen sc1\n\t" \
  "buffer_load_dwordx4 v[36:39], v100, %[rh], s76 offen sc1\n\t" \
  "buffer_load_dwordx4 v[40:43], v100, %[rh], s77 offen sc1\n\t" \
  "buffer_load_dwordx4 v[44:47], v100, %[rh], s78 offen sc1\n\t" \
  "s_branch 17b\n\t" \
  "12:\n\t" \
  "s_mov_b32 s42, s58\n\t" \
  "s_branch 99f\n\t" \
  "99:\n\t" \
  "s_waitcnt vmcnt(0) lgkmcnt(0)\n\t" \
  ""
#define VO_BODY \
  "s_cmp_eq_u32 %[wv], 1\n\t" \
  "s_cbranch_scc1 91f\n\t" \
  "s_cmp_eq_u32 %[wv], 2\n\t" \
  "s_cbranch_scc1 92f\n\t" \
  "s_cmp_eq_u32 %[wv], 3\n\t" \
  "s_cbranch_scc1 93f\n\t" \
  "90:\n\t" \
  "s_mov_b32 s58, s42\n\t" \
  "20:\n\t" \
  "s_and_b32 s76, s44, 7\n\t" \
  "s_lshl_b32 s76, s76, 15\n\t" \
  "s_add_i32 s40, s54, s76\n\t" \
  "s_and_b32 s76, s44, 3\n\t" \
  "s_lshl_b32 s45, s76, 14\n\t" \
  "s_lshl_b32 s47, s76, 20\n\t" \
  "s_add_i32 s47, s47, s55\n\t" \
  "s_lshr_b32 s76, s47, 21\n\t" \
  "s_bitcmp1_b64 %[sel], s76\n\t" \
  "s_cselect_b32 s76, 0x2000, 0\n\t" \
  "s_add_i32 s47, s47, s76\n\t" \
  "s_add_i32 s49, s44, -1\n\t" \
  "s_and_b32 s48, s49, 2\n\t" \
  "s_lshl_b32 s48, s48, 13\n\t" \
  "s_and_b32 s76, s49, 1\n\t" \
  "s_lshl_b32 s76, s76, 12\n\t" \
  "s_add_i32 s48, s48, s76\n\t" \
  "s_lshl_b32 s50, s53, 8\n\t" \
  "s_mov_b32 s59, 0\n\t" \
  "s_mov_b32 s92, s51\n\t" \
  "s_mov_b32 s93, s53\n\t" \
  "s_xor_b32 s76, s40, 0x400\n\t" \
  "s_xor_b32 s77, s40, 0x800\n\t" \
  "s_xor_b32 s78, s40, 0xc00\n\t" \
  "s_cmp_lg_u32 s58, 0\n\t" \
  "s_cbranch_scc1 11f\n\t" \
  "10:\n\t" \
  "ds_read_b128 v[48:51], v102 offset:0\n\t" \
  "ds_read_b128 v[52:55], v102 offset:1024\n\t" \
  "ds_read_b128 v[56:59], v102 offset:2048\n\t" \
  "ds_read_b128 v[60:63], v102 offset:3072\n\t" \
  "ds_read_b128 v[0:3], v102 offset:16384\n\t" \
  "ds_read_b128 v[4:7], v102 offset:17408\n\t" \
  "ds_read_b128 v[8:11], v102 offset:18432\n\t" \
  "ds_read_b128 v[12:15], v102 offset:19456\n\t" \
  "ds_read_b128 v[16:19], v102 offset:32768\n\t" \
  "ds_read_b128 v[20:23], v102 offset:33792\n\t" \
  "ds_read_b128 v[24:27], v102 offset:34816\n\t" \
  "ds_read_b128 v[28:31], v102 offset:35840\n\t" \
  "s_waitcnt lgkmcnt(4)\n\t" \
  "ds_read_b128 v[80:83], v102 offset:49152\n\t" \
  "ds_read_b128 v[84:87], v102 offset:50176\n\t" \
  "ds_read_b128 v[88:91], v102 offset:51200\n\t" \
  "ds_read_b128 v[92:95], v102 offset:52224\n\t" \
  "46:\n\t" \
  "s_bitcmp1_b32 s58, 1\n\t" \
  "s_cselect_b64 s[80:81], -1, 0\n\t" \
  "s_cmp_lg_u32 s58, 0\n\t" \
  "s_cbranch_scc1 17f\n\t" \
  "s_mov_b64 exec, 0xff\n\t" \
  "16:\n\t" \
  "s_memtime vcc\n\t" \
  "s_waitcnt lgkmcnt(0)\n\t" \
  "s_sub_i32 s79, s41, vcc_lo\n\t" \
  "s_cmp_gt_i32 s79, 64\n\t" \
  "s_cbranch_scc1 16b\n\t" \
  "13:\n\t" \
  "buffer_load_dword v116, v112, %[rh], s48 offen sc1\n\t" \
  "s_memtime vcc\n\t" \
  "s_memrealtime s[82:83]\n\t" \
  "s_nop 4\n\t" \
  "s_memrealtime s[84:85]\n\t" \
  "s_nop 4\n\t" \
  "s_memrealtime s[56:57]\n\t" \
  "s_nop 4\n\t" \
  "s_memrealtime s[86:87]\n\t" \
  "s_nop 4\n\t" \
  "s_memrealtime s[88:89]\n\t" \
  "s_nop 4\n\t" \
  "s_memrealtime s[90:91]\n\t" \
  "s_mov_b32 s92, 0\n\t" \
  "s_waitcnt lgkmcnt(0)\n\t" \
  "s_cmp_eq_u32 s82, s84\n\t" \
  "s_addc_u32 s92, s92, 0\n\t" \
  "s_cmp_eq_u32 s82, s56\n\t" \
  "s_addc_u32 s92, s92, 0\n\t" \
  "s_cmp_eq_u32 s82, s86\n\t" \
  "s_addc_u32 s92, s92, 0\n\t" \
  "s_cmp_eq_u32 s82, s88\n\t" \
  "s_addc_u32 s92, s92, 0\n\t" \
  "s_cmp_eq_u32 s82, s90\n\t" \
  "s_addc_u32 s92, s92, 0\n\t" \
  "s_mul_i32 s92, s92, 24\n\t" \
  "s_add_i32 s92, s92, vcc_lo\n\t" \
  "s_lshl4_add_u32 s93, s82, 64\n\t" \
  "s_lshl_b32 s50, s93, 8\n\t" \
  "s_waitcnt vmcnt(0)\n\t" \
  "15:\n\t" \
  "v_mov_b32 v117, 20000\n\t" \
  "v_cmp_ne_u32_sdwa vcc, v116, s49 src0_sel:BYTE_0 src1_sel:DWORD\n\t" \
  "v_subrev_u32 v116, s50, v116\n\t" \
  "v_ashrrev_i32 v116, 8, v116\n\t" \
  "s_nop 1\n\t" \
  "v_max_i32_dpp v116, v116, v116 quad_perm:[1,0,3,2] row_mask:0xf bank_mask:0xf\n\t" \
  "s_cbranch_vccnz 14f\n\t" \
  "s_nop 0\n\t" \
  "v_max_i32_dpp v116, v116, v116 quad_perm:[2,3,0,1] row_mask:0xf bank_mask:0xf\n\t" \
  "s_nop 1\n\t" \
  "v_max_i32_dpp v116, v116, v116 row_half_mirror row_mask:0xf bank_mask:0xf\n\t" \
  "v_med3_i32 v116, v116, 0, v117\n\t" \
  "v_mul_u32_u24 v116, s52, v116\n\t" \
  "v_lshrrev_b32 v116, 16, v116\n\t" \
  "s_mov_b64 exec, -1\n\t" \
  "v_readfirstlane_b32 s84, v116\n\t" \
  "s_add_i32 s84, s84, s92\n\t" \
  ".p2align 6\n\t" \
  "s_nop 0\n\t" \
  "s_nop 0\n\t" \
  "s_nop 0\n\t" \
  "s_nop 0\n\t" \
  "s_nop 0\n\t" \
  "s_nop 0\n\t" \
  "s_nop 0\n\t" \
  "s_nop 0\n\t" \
  "1:\n\t" \
  "s_memtime vcc\n\t" \
  "s_waitcnt lgkmcnt(0)\n\t" \
  "s_sub_i32 s79, s84, vcc_lo\n\t" \
  "s_cmp_gt_i32 s79, 128\n\t" \
  "s_cbranch_scc1 1b\n\t" \
  ".p2align 4\n\t" \
  "2:\n\t" \
  "s_sub_i32 s79, s79, 33\n\t" \
  "s_cmp_gt_i32 s79, 56\n\t" \
  "s_cbranch_scc1 2b\n\t" \
  "buffer_load_dwordx4 v[32:35], v100, %[rh], s40 offen sc1\n\t" \
  "s_nop 12\n\t" \
  "buffer_load_dwordx4 v[36:39], v100, %[rh], s76 offen sc1\n\t" \
  "s_nop 12\n\t" \
  "buffer_load_dwordx4 v[40:43], v100, %[rh], s77 offen sc1\n\t" \
  "s_nop 12\n\t" \
  "buffer_load_dwordx4 v[44:47], v100, %[rh], s78 offen sc1\n\t" \
  "3:\n\t" \
  "v_mov_b64 v[72:73], 0\n\t" \
  "v_mov_b64 v[74:75], 0\n\t" \
  "v_mov_b64 v[76:77], 0\n\t" \
  "v_mov_b64 v[78:79], 0\n\t" \
  "s_waitcnt lgkmcnt(0)\n\t" \
  "s_waitcnt vmcnt(3)\n\t" \
  "v_smfmac_f32_16x16x64_bf16 v[72:75], v[32:35], v[48:55], v104\n\t" \
  "ds_write_b128 v101, v[32:35] offset:0\n\t" \
  "v_smfmac_f32_16x16x64_bf16 v[76:79], v[32:35], v[56:63], v104\n\t" \
  "s_waitcnt vmcnt(2)\n\t" \
  "ds_write_b128 v101, v[36:39] offset:1024\n\t" \
  "v_smfmac_f32_16x16x64_bf16 v[72:75], v[36:39], a[0:7], v104\n\t" \
  "v_smfmac_f32_16x16x64_bf16 v[76:79], v[36:39], a[8:15], v104\n\t" \
  "s_nop 3\n\t" \
  "v_cmp_u_f32 vcc, v72, v73\n\t" \
  "v_cmp_u_f32 s[82:83], v74, v75\n\t" \
  "s_waitcnt vmcnt(1)\n\t" \
  "v_smfmac_f32_16x16x64_bf16 v[72:75], v[40:43], a[16:23], v104\n\t" \
  "s_or_b64 vcc, vcc, s[82:83]\n\t" \
  "s_or_b64 vcc, vcc, s[80:81]\n\t" \
  "v_smfmac_f32_16x16x64_bf16 v[76:79], v[40:43], a[24:31], v104\n\t" \
  "s_waitcnt lgkmcnt(0)\n\t" \
  "s_cbranch_scc1 8f\n\t" \
  "5:\n\t" \
  "s_barrier\n\t" \
  "4:\n\t" \
  "s_memtime s[56:57]\n\t" \
  "ds_read_b128 v[48:51], v101 offset:4096\n\t" \
  "ds_read_b128 v[52:55], v101 offset:5120\n\t" \
  "ds_read_b128 v[56:59], v101 offset:9216\n\t" \
  "s_waitcnt vmcnt(0)\n\t" \
  "v_smfmac_f32_16x16x64_bf16 v[72:75], v[44:47], a[32:39], v104\n\t" \
  "ds_read_b128 v[60:63], v101 offset:8192\n\t" \
  "ds_read_b128 v[64:67], v101 offset:13312\n\t" \
  "v_smfmac_f32_16x16x64_bf16 v[76:79], v[44:47], a[40:47], v104\n\t" \
  "ds_read_b128 v[68:71], v101 offset:12288\n\t" \
  "ds_write_b128 v101, v[40:43] offset:2048\n\t" \
  "s_nop 1\n\t" \
  "v_cmp_u_f32 vcc, v72, v73\n\t" \
  "v_cmp_u_f32 s[82:83], v74, v75\n\t" \
  "s_waitcnt lgkmcnt(6)\n\t" \
  "v_smfmac_f32_16x16x64_bf16 v[72:75], v[48:51], v[0:7], v104\n\t" \
  "s_or_b64 vcc, vcc, s[82:83]\n\t" \
  "v_smfmac_f32_16x16x64_bf16 v[76:79], v[48:51], v[8:15], v104\n\t" \
  "s_cbranch_scc1 9f\n\t" \
  "6:\n\t" \
  "s_waitcnt lgkmcnt(5)\n\t" \
  "v_smfmac_f32_16x16x64_bf16 v[72:75], v[52:55], a[48:55], v104\n\t" \
  "ds_write_b128 v101, v[44:47] offset:3072\n\t" \
  "v_smfmac_f32_16x16x64_bf16 v[76:79], v[52:55], a[56:63], v104\n\t" \
  "v_mov_b64 v[40:41], 0\n\t" \
  "s_waitcnt lgkmcnt(5)\n\t" \
  "v_smfmac_f32_16x16x64_bf16 v[72:75], v[56:59], a[96:103], v104\n\t" \
  "v_mov_b64 v[42:43], 0\n\t" \
  "v_smfmac_f32_16x16x64_bf16 v[76:79], v[56:59], a[104:111], v104\n\t" \
  "s_waitcnt lgkmcnt(4)\n\t" \
  "v_smfmac_f32_16x16x64_bf16 v[72:75], v[60:63], v[16:23], v104\n\t" \
  "s_waitcnt lgkmcnt(0)\n\t" \
  "s_barrier\n\t" \
  "v_smfmac_f32_16x16x64_bf16 v[76:79], v[60:63], v[24:31], v104\n\t" \
  "ds_read_b128 v[48:51], v101 offset:6144\n\t" \
  "ds_read_b128 v[52:55], v101 offset:10240\n\t" \
  "v_smfmac_f32_16x16x64_bf16 v[72:75], v[64:67], a[144:151], v104\n\t" \
  "ds_read_b128 v[56:59], v101 offset:14336\n\t" \
  "ds_read_b128 v[60:63], v101 offset:7168\n\t" \
  "v_smfmac_f32_16x16x64_bf16 v[76:79], v[64:67], a[152:159], v104\n\t" \
  "ds_read_b128 v[32:35], v101 offset:11264\n\t" \
  "ds_read_b128 v[36:39], v101 offset:15360\n\t" \
  "v_smfmac_f32_16x16x64_bf16 v[72:75], v[68:71], v[80:87], v104\n\t" \
  "ds_read_b128 v[0:3], v103 offset:0\n\t" \
  "v_mov_b64 v[44:45], 0\n\t" \
  "v_smfmac_f32_16x16x64_bf16 v[76:79], v[68:71], v[88:95], v104\n\t" \
  "v_mov_b64 v[46:47], 0\n\t" \
  "s_waitcnt lgkmcnt(5)\n\t" \
  "v_smfmac_f32_16x16x64_bf16 v[72:75], v[48:51], a[64:71], v104\n\t" \
  "ds_read_b128 v[4:7], v103 offset:1024\n\t" \
  "v_mov_b64 v[80:81], 0\n\t" \
  "v_smfmac_f32_16x16x64_bf16 v[72:75], v[52:55], a[112:119], v104\n\t" \
  "v_mov_b64 v[82:83], 0\n\t" \
  "s_waitcnt lgkmcnt(2)\n\t" \
  "v_smfmac_f32_16x16x64_bf16 v[72:75], v[56:59], a[160:167], v104\n\t" \
  "ds_read_b128 v[8:11], v103 offset:2048\n\t" \
  "v_mov_b64 v[84:85], 0\n\t" \
  "v_smfmac_f32_16x16x64_bf16 v[72:75], v[60:63], a[80:87], v104\n\t" \
  "ds_read_b128 v[12:15], v103 offset:3072\n\t" \
  "v_mov_b64 v[86:87], 0\n\t" \
  "v_smfmac_f32_16x16x64_bf16 v[72:75], v[32:35], a[128:135], v104\n\t" \
  "ds_read_b128 v[16:19], v103 offset:16384\n\t" \
  "v_mov_b64 v[88:89], 0\n\t" \
  "v_smfmac_f32_16x16x64_bf16 v[72:75], v[36:39], a[176:183], v104\n\t" \
  "ds_read_b128 v[20:23], v103 offset:17408\n\t" \
  "v_mov_b64 v[90:91], 0\n\t" \
  "v_smfmac_f32_16x16x64_bf16 v[76:79], v[48:51], a[72:79], v104\n\t" \
  "ds_read_b128 v[24:27], v103 offset:18432\n\t" \
  "v_mov_b64 v[92:93], 0\n\t" \
  "v_smfmac_f32_16x16x64_bf16 v[76:79], v[52:55], a[120:127], v104\n\t" \
  "v_add_f32 v96, v72, v74\n\t" \
  "v_add_f32 v97, v73, v75\n\t" \
  "v_cvt_pk_bf16_f32 v96, v96, v97\n\t" \
  "ds_write_b32 v105, v96\n\t" \
  "v_smfmac_f32_16x16x64_bf16 v[76:79], v[56:59], a[168:175], v104\n\t" \
  "ds_read_b128 v[28:31], v103 offset:19456\n\t" \
  "v_mov_b64 v[94:95], 0\n\t" \
  "v_smfmac_f32_16x16x64_bf16 v[76:79], v[60:63], a[88:95], v104\n\t" \
  "v_smfmac_f32_16x16x64_bf16 v[76:79], v[32:35], a[136:143], v104\n\t" \
  "v_smfmac_f32_16x16x64_bf16 v[76:79], v[36:39], a[184:191], v104\n\t" \
  "s_waitcnt lgkmcnt(1)\n\t" \
  "s_barrier\n\t" \
  "ds_read_b64_tr_b16 v[96:97], v107 offset:0\n\t" \
  "ds_read_b64_tr_b16 v[98:99], v107 offset:64\n\t" \
  "ds_read_b64_tr_b16 v[56:57], v107 offset:1024\n\t" \
  "ds_read_b64_tr_b16 v[58:59], v107 offset:1088\n\t" \
  "s_nop 1\n\t" \
  "v_add_f32 v116, v76, v78\n\t" \
  "v_add_f32 v117, v77, v79\n\t" \
  "v_cvt_pk_bf16_f32 v116, v116, v117\n\t" \
  "ds_write_b32 v105, v116 offset:256\n\t" \
  "s_waitcnt lgkmcnt(3)\n\t" \
  "v_smfmac_f32_16x16x64_bf16 v[40:43], v[96:99], a[192:199], v106\n\t" \
  "v_smfmac_f32_16x16x64_bf16 v[44:47], v[96:99], a[224:231], v106\n\t" \
  "s_waitcnt lgkmcnt(0)\n\t" \
  "s_barrier\n\t" \
  "ds_read_b64_tr_b16 v[52:53], v107 offset:256\n\t" \
  "ds_read_b64_tr_b16 v[54:55], v107 offset:320\n\t" \
  "v_smfmac_f32_16x16x64_bf16 v[40:43], v[56:59], a[208:215], v106\n\t" \
  "ds_read_b64_tr_b16 v[60:61], v107 offset:1280\n\t" \
  "ds_read_b64_tr_b16 v[62:63], v107 offset:1344\n\t" \
  "v_smfmac_f32_16x16x64_bf16 v[44:47], v[56:59], v[0:7], v106\n\t" \
  "v_smfmac_f32_16x16x64_bf16 v[80:83], v[96:99], a[240:247], v106\n\t" \
  "v_mov_b64 v[32:33], 0\n\t" \
  "v_smfmac_f32_16x16x64_bf16 v[84:87], v[96:99], v[136:143], v106\n\t" \
  "v_mov_b64 v[34:35], 0\n\t" \
  "v_smfmac_f32_16x16x64_bf16 v[80:83], v[56:59], v[120:127], v106\n\t" \
  "v_mov_b64 v[36:37], 0\n\t" \
  "v_smfmac_f32_16x16x64_bf16 v[84:87], v[56:59], v[16:23], v106\n\t" \
  "s_waitcnt lgkmcnt(2)\n\t" \
  "v_smfmac_f32_16x16x64_bf16 v[40:43], v[52:55], a[200:207], v106\n\t" \
  "v_smfmac_f32_16x16x64_bf16 v[44:47], v[52:55], a[232:239], v106\n\t" \
  "s_xor_b32 s76, s47, 0x40000\n\t" \
  "s_waitcnt lgkmcnt(0)\n\t" \
  "v_smfmac_f32_16x16x64_bf16 v[40:43], v[60:63], a[216:223], v106\n\t" \
  "v_mov_b64 v[38:39], 0\n\t" \
  "s_xor_b32 s77, s47, 0x80000\n\t" \
  "v_smfmac_f32_16x16x64_bf16 v[44:47], v[60:63], v[8:15], v106\n\t" \
  "s_xor_b32 s78, s47, 0xc0000\n\t" \
  "v_smfmac_f32_16x16x64_bf16 v[80:83], v[52:55], a[248:255], v106\n\t" \
  "ds_read_b128 v[0:3], v103 offset:32768\n\t" \
  "v_cvt_pk_bf16_f32 v64, v40, v41\n\t" \
  "v_smfmac_f32_16x16x64_bf16 v[84:87], v[52:55], v[144:151], v106\n\t" \
  "ds_read_b128 v[4:7], v103 offset:33792\n\t" \
  "v_cvt_pk_bf16_f32 v65, v42, v43\n\t" \
  "v_smfmac_f32_16x16x64_bf16 v[80:83], v[60:63], v[128:135], v106\n\t" \
  "ds_read_b128 v[8:11], v103 offset:34816\n\t" \
  "v_cvt_pk_bf16_f32 v66, v44, v45\n\t" \
  "v_smfmac_f32_16x16x64_bf16 v[84:87], v[60:63], v[24:31], v106\n\t" \
  "ds_read_b128 v[12:15], v103 offset:35840\n\t" \
  "v_cvt_pk_bf16_f32 v67, v46, v47\n\t" \
  "v_smfmac_f32_16x16x64_bf16 v[88:91], v[96:99], v[152:159], v106\n\t" \
  "v_permlane16_swap_b32 v64, v66\n\t" \
  "v_cvt_pk_bf16_f32 v68, v80, v81\n\t" \
  "v_smfmac_f32_16x16x64_bf16 v[92:95], v[96:99], v[184:191], v106\n\t" \
  "v_permlane16_swap_b32 v65, v67\n\t" \
  "buffer_store_dwordx4 v[64:67], v108, %[ru], s47 offen sc1\n\t" \
  "v_cvt_pk_bf16_f32 v69, v82, v83\n\t" \
  "v_smfmac_f32_16x16x64_bf16 v[88:91], v[52:55], v[160:167], v106\n\t" \
  "v_cvt_pk_bf16_f32 v70, v84, v85\n\t" \
  "v_smfmac_f32_16x16x64_bf16 v[92:95], v[52:55], v[192:199], v106\n\t" \
  "v_cvt_pk_bf16_f32 v71, v86, v87\n\t" \
  "v_smfmac_f32_16x16x64_bf16 v[88:91], v[56:59], v[168:175], v106\n\t" \
  "ds_read_b128 v[16:19], v103 offset:49152\n\t" \
  "v_permlane16_swap_b32 v68, v70\n\t" \
  "s_waitcnt lgkmcnt(3)\n\t" \
  "v_smfmac_f32_16x16x64_bf16 v[92:95], v[56:59], v[0:7], v106\n\t" \
  "ds_read_b128 v[20:23], v103 offset:50176\n\t" \
  "v_permlane16_swap_b32 v69, v71\n\t" \
  "buffer_store_dwordx4 v[68:71], v108, %[ru], s76 offen sc1\n\t" \
  "v_smfmac_f32_16x16x64_bf16 v[88:91], v[60:63], v[176:183], v106\n\t" \
  "ds_read_b128 v[24:27], v103 offset:51200\n\t" \
  "s_waitcnt lgkmcnt(3)\n\t" \
  "v_smfmac_f32_16x16x64_bf16 v[92:95], v[60:63], v[8:15], v106\n\t" \
  "ds_read_b128 v[28:31], v103 offset:52224\n\t" \
  "v_smfmac_f32_16x16x64_bf16 v[32:35], v[96:99], v[200:207], v106\n\t" \
  "v_cvt_pk_bf16_f32 v72, v88, v89\n\t" \
  "v_smfmac_f32_16x16x64_bf16 v[36:39], v[96:99], v[232:239], v106\n\t" \
  "v_cvt_pk_bf16_f32 v73, v90, v91\n\t" \
  "v_smfmac_f32_16x16x64_bf16 v[32:35], v[52:55], v[208:215], v106\n\t" \
  "v_cvt_pk_bf16_f32 v74, v92, v93\n\t" \
  "v_smfmac_f32_16x16x64_bf16 v[36:39], v[52:55], v[240:247], v106\n\t" \
  "v_cvt_pk_bf16_f32 v75, v94, v95\n\t" \
  "v_smfmac_f32_16x16x64_bf16 v[32:35], v[56:59], v[216:223], v106\n\t" \
  "v_permlane16_swap_b32 v72, v74\n\t" \
  "v_smfmac_f32_16x16x64_bf16 v[32:35], v[60:63], v[224:231], v106\n\t" \
  "v_permlane16_swap_b32 v73, v75\n\t" \
  "buffer_store_dwordx4 v[72:75], v108, %[ru], s77 offen sc1\n\t" \
  "s_waitcnt lgkmcnt(2)\n\t" \
  "v_smfmac_f32_16x16x64_bf16 v[36:39], v[56:59], v[16:23], v106\n\t" \
  "s_waitcnt lgkmcnt(0)\n\t" \
  "v_smfmac_f32_16x16x64_bf16 v[36:39], v[60:63], v[24:31], v106\n\t" \
  "v_cvt_pk_bf16_f32 v76, v32, v33\n\t" \
  "v_cvt_pk_bf16_f32 v77, v34, v35\n\t" \
  "s_nop 5\n\t" \
  "v_cvt_pk_bf16_f32 v78, v36, v37\n\t" \
  "v_cvt_pk_bf16_f32 v79, v38, v39\n\t" \
  "s_nop 0\n\t" \
  "v_permlane16_swap_b32 v76, v78\n\t" \
  "v_permlane16_swap_b32 v77, v79\n\t" \
  "buffer_store_dwordx4 v[76:79], v108, %[ru], s78 offen sc1\n\t" \
  "s_memtime s[76:77]\n\t" \
  "s_mov_b32 s42, 0\n\t" \
  "s_waitcnt lgkmcnt(0)\n\t" \
  "s_mov_b32 s57, s76\n\t" \
  "s_add_i32 s41, s56, 1950\n\t" \
  "s_sub_i32 s76, s92, s51\n\t" \
  "s_sub_i32 s77, s93, s53\n\t" \
  "s_cmp_eq_u32 s76, 0\n\t" \
  "s_cbranch_scc1 21f\n\t" \
  "v_cvt_f32_u32 v117, s76\n\t" \
  "v_cvt_f32_u32 v116, s77\n\t" \
  "v_rcp_f32 v117, v117\n\t" \
  "s_mov_b32 s51, s92\n\t" \
  "v_mul_f32 v117, v117, v116\n\t" \
  "v_max_f32 v117, 0x3f028f5c, v117\n\t" \
  "v_min_f32 v117, 0x3ffeb852, v117\n\t" \
  "v_rcp_f32 v116, v117\n\t" \
  "v_mul_f32 v117, 0x4f000000, v117\n\t" \
  "v_mul_f32 v116, 0x47800000, v116\n\t" \
  "v_cvt_u32_f32 v117, v117\n\t" \
  "v_cvt_u32_f32 v116, v116\n\t" \
  "s_mov_b32 s53, s93\n\t" \
  "v_readfirstlane_b32 s46, v117\n\t" \
  "v_readfirstlane_b32 s52, v116\n\t" \
  "21:\n\t" \
  "s_add_i32 s44, s44, 1\n\t" \
  "s_and_b32 s58, s58, 4\n\t" \
  "s_and_b32 s76, s44, 64\n\t" \
  "s_or_b32 s42, s42, s76\n\t" \
  "s_cbranch_scc0 20b\n\t" \
  "s_branch 99f\n\t" \
  "14:\n\t" \
  "s_sleep 3\n\t" \
  "s_add_u32 s59, s59, 0x10000\n\t" \
  "s_cmp_lt_u32 s59, 0x400000\n\t" \
  "s_cbranch_scc1 13b\n\t" \
  "s_mov_b64 exec, -1\n\t" \
  "s_mov_b32 s42, 16\n\t" \
  "s_branch 99f\n\t" \
  "8:\n\t" \
  "s_bitcmp1_b32 s58, 1\n\t" \
  "s_cbranch_scc1 4b\n\t" \
  "s_bitcmp1_b32 s58, 2\n\t" \
  "s_cbranch_scc1 5b\n\t" \
  "v_max3_u32 v116, v32, v33, v34\n\t" \
  "v_max3_u32 v117, v35, v36, v37\n\t" \
  "v_max3_u32 v116, v116, v38, v39\n\t" \
  "v_max_u32 v116, v116, v117\n\t" \
  "v_cmp_eq_u32 vcc, -1, v116\n\t" \
  "s_cbranch_vccz 5b\n\t" \
  "s_branch 0f\n\t" \
  "9:\n\t" \
  "s_bitcmp1_b32 s58, 2\n\t" \
  "s_cbranch_scc1 6b\n\t" \
  "v_max3_u32 v116, v40, v41, v42\n\t" \
  "v_max3_u32 v117, v43, v44, v45\n\t" \
  "v_max3_u32 v116, v116, v46, v47\n\t" \
  "v_max_u32 v116, v116, v117\n\t" \
  "v_cmp_eq_u32 vcc, -1, v116\n\t" \
  "s_cbranch_vccz 6b\n\t" \
  "s_or_b32 s58, s58, 2\n\t" \
  "0:\n\t" \
  "s_or_b32 s58, s58, 1\n\t" \
  "11:\n\t" \
  "buffer_load_dwordx4 v[32:35], v100, %[rh], s40 offen sc1\n\t" \
  "buffer_load_dwordx4 v[36:39], v100, %[rh], s76 offen sc1\n\t" \
  "buffer_load_dwordx4 v[40:43], v100, %[rh], s77 offen sc1\n\t" \
  "buffer_load_dwordx4 v[44:47], v100, %[rh], s78 offen sc1\n\t" \
  "s_waitcnt lgkmcnt(0)\n\t" \
  "s_branch 10b\n\t" \
  "17:\n\t" \
  "s_add_u32 s59, s59, 1\n\t" \
  "s_bitcmp1_b32 s58, 2\n\t" \
  "s_cbranch_scc1 3b\n\t" \
  "s_waitcnt vmcnt(0)\n\t" \
  "v_max_u32 v116, v32, v33\n\t" \
  "v_max3_u32 v116, v116, v34, v35\n\t" \
  "v_max3_u32 v116, v116, v36, v37\n\t" \
  "v_max3_u32 v116, v116, v38, v39\n\t" \
  "v_max3_u32 v116, v116, v40, v41\n\t" \
  "v_max3_u32 v116, v116, v42, v43\n\t" \
  "v_max3_u32 v116, v116, v44, v45\n\t" \
  "v_max3_u32 v116, v116, v46, v47\n\t" \
  "v_cmp_eq_u32 vcc, -1, v116\n\t" \
  "s_cbranch_vccz 3b\n\t" \
  "s_bitcmp1_b32 s59, 6\n\t" \
  "s_cbranch_scc1 12f\n\t" \
  "buffer_load_dwordx4 v[32:35], v100, %[rh], s40 offen sc1\n\t" \
  "buffer_load_dwordx4 v[36:39], v100, %[rh], s76 offen sc1\n\t" \
  "buffer_load_dwordx4 v[40:43], v100, %[rh], s77 offen sc1\n\t" \
  "buffer_load_dwordx4 v[44:47], v100, %[rh], s78 offen sc1\n\t" \
  "s_branch 17b\n\t" \
  "12:\n\t" \
  "s_mov_b32 s42, s58\n\t" \
  "s_branch 99f\n\t" \
  "91:\n\t" \
  "s_mov_b32 s58, s42\n\t" \
  "20:\n\t" \
  "s_and_b32 s76, s44, 7\n\t" \
  "s_lshl_b32 s76, s76, 15\n\t" \
  "s_add_i32 s40, s54, s76\n\t" \
  "s_and_b32 s76, s44, 3\n\t" \
  "s_lshl_b32 s45, s76, 14\n\t" \
  "s_lshl_b32 s47, s76, 20\n\t" \
  "s_add_i32 s47, s47, s55\n\t" \
  "s_lshr_b32 s76, s47, 21\n\t" \
  "s_bitcmp1_b64 %[sel], s76\n\t" \
  "s_cselect_b32 s76, 0x2000, 0\n\t" \
  "s_add_i32 s47, s47, s76\n\t" \
  "s_add_i32 s49, s44, -1\n\t" \
  "s_and_b32 s48, s49, 2\n\t" \
  "s_lshl_b32 s48, s48, 13\n\t" \
  "s_and_b32 s76, s49, 1\n\t" \
  "s_lshl_b32 s76, s76, 12\n\t" \
  "s_add_i32 s48, s48, s76\n\t" \
  "s_lshl_b32 s50, s53, 8\n\t" \
  "s_mov_b32 s59, 0\n\t" \
  "s_mov_b32 s92, s51\n\t" \
  "s_mov_b32 s93, s53\n\t" \
  "s_xor_b32 s76, s40, 0x400\n\t" \
  "s_xor_b32 s77, s40, 0x800\n\t" \
  "s_xor_b32 s78, s40, 0xc00\n\t" \
  "s_cmp_lg_u32 s58, 0\n\t" \
  "s_cbranch_scc1 11f\n\t" \
  "10:\n\t" \
  "ds_read_b128 v[48:51], v102 offset:16384\n\t" \
  "ds_read_b128 v[52:55], v102 offset:17408\n\t" \
  "ds_read_b128 v[56:59], v102 offset:18432\n\t" \
  "ds_read_b128 v[60:63], v102 offset:19456\n\t" \
  "ds_read_b128 v[0:3], v102 offset:32768\n\t" \
  "ds_read_b128 v[4:7], v102 offset:33792\n\t" \
  "ds_read_b128 v[8:11], v102 offset:34816\n\t" \
  "ds_read_b128 v[12:15], v102 offset:35840\n\t" \
  "ds_read_b128 v[16:19], v102 offset:49152\n\t" \
  "ds_read_b128 v[20:23], v102 offset:50176\n\t" \
  "ds_read_b128 v[24:27], v102 offset:51200\n\t" \
  "ds_read_b128 v[28:31], v102 offset:52224\n\t" \
  "s_waitcnt lgkmcnt(4)\n\t" \
  "ds_read_b128 v[80:83], v102 offset:0\n\t" \
  "ds_read_b128 v[84:87], v102 offset:1024\n\t" \
  "ds_read_b128 v[88:91], v102 offset:2048\n\t" \
  "ds_read_b128 v[92:95], v102 offset:3072\n\t" \
  "46:\n\t" \
  "s_bitcmp1_b32 s58, 1\n\t" \
  "s_cselect_b64 s[80:81], -1, 0\n\t" \
  "s_cmp_lg_u32 s58, 0\n\t" \
  "s_cbranch_scc1 17f\n\t" \
  "s_mov_b64 exec, 0xff\n\t" \
  "16:\n\t" \
  "s_memtime vcc\n\t" \
  "s_waitcnt lgkmcnt(0)\n\t" \
  "s_sub_i32 s79, s41, vcc_lo\n\t" \
  "s_cmp_gt_i32 s79, 64\n\t" \
  "s_cbranch_scc1 16b\n\t" \
  "13:\n\t" \
  "buffer_load_dword v116, v112, %[rh], s48 offen sc1\n\t" \
  "s_memtime vcc\n\t" \
  "s_memrealtime s[82:83]\n\t" \
  "s_nop 4\n\t" \
  "s_memrealtime s[84:85]\n\t" \
  "s_nop 4\n\t" \
  "s_memrealtime s[56:57]\n\t" \
  "s_nop 4\n\t" \
  "s_memrealtime s[86:87]\n\t" \
  "s_nop 4\n\t" \
  "s_memrealtime s[88:89]\n\t" \
  "s_nop 4\n\t" \
  "s_memrealtime s[90:91]\n\t" \
  "s_mov_b32 s92, 0\n\t" \
  "s_waitcnt lgkmcnt(0)\n\t" \
  "s_cmp_eq_u32 s82, s84\n\t" \
  "s_addc_u32 s92, s92, 0\n\t" \
  "s_cmp_eq_u32 s82, s56\n\t" \
  "s_addc_u32 s92, s92, 0\n\t" \
  "s_cmp_eq_u32 s82, s86\n\t" \
  "s_addc_u32 s92, s92, 0\n\t" \
  "s_cmp_eq_u32 s82, s88\n\t" \
  "s_addc_u32 s92, s92, 0\n\t" \
  "s_cmp_eq_u32 s82, s90\n\t" \
  "s_addc_u32 s92, s92, 0\n\t" \
  "s_mul_i32 s92, s92, 24\n\t" \
  "s_add_i32 s92, s92, vcc_lo\n\t" \
  "s_lshl4_add_u32 s93, s82, 64\n\t" \
  "s_lshl_b32 s50, s93, 8\n\t" \
  "s_waitcnt vmcnt(0)\n\t" \
  "15:\n\t" \
  "v_mov_b32 v117, 20000\n\t" \
  "v_cmp_ne_u32_sdwa vcc, v116, s49 src0_sel:BYTE_0 src1_sel:DWORD\n\t" \
  "v_subrev_u32 v116, s50, v116\n\t" \
  "v_ashrrev_i32 v116, 8, v116\n\t" \
  "s_nop 1\n\t" \
  "v_max_i32_dpp v116, v116, v116 quad_perm:[1,0,3,2] row_mask:0xf bank_mask:0xf\n\t" \
  "s_cbranch_vccnz 14f\n\t" \
  "s_nop 0\n\t" \
  "v_max_i32_dpp v116, v116, v116 quad_perm:[2,3,0,1] row_mask:0xf bank_mask:0xf\n\t" \
  "s_nop 1\n\t" \
  "v_max_i32_dpp v116, v116, v116 row_half_mirror row_mask:0xf bank_mask:0xf\n\t" \
  "v_med3_i32 v116, v116, 0, v117\n\t" \
  "v_mul_u32_u24 v116, s52, v116\n\t" \
  "v_lshrrev_b32 v116, 16, v116\n\t" \
  "s_mov_b64 exec, -1\n\t" \
  "v_readfirstlane_b32 s84, v116\n\t" \
  "s_add_i32 s84, s84, s92\n\t" \
  ".p2align 6\n\t" \
  "s_nop 0\n\t" \
  "s_nop 0\n\t" \
  "s_nop 0\n\t" \
  "s_nop 0\n\t" \
  "s_nop 0\n\t" \
  "s_nop 0\n\t" \
  "s_nop 0\n\t" \
  "s_nop 0\n\t" \
  "1:\n\t" \
  "s_memtime vcc\n\t" \
  "s_waitcnt lgkmcnt(0)\n\t" \
  "s_sub_i32 s79, s84, vcc_lo\n\t" \
  "s_cmp_gt_i32 s79, 128\n\t" \
  "s_cbranch_scc1 1b\n\t" \
  ".p2align 4\n\t" \
  "2:\n\t" \
  "s_sub_i32 s79, s79, 33\n\t" \
  "s_cmp_gt_i32 s79, 56\n\t" \
  "s_cbranch_scc1 2b\n\t" \
  "buffer_load_dwordx4 v[32:35], v100, %[rh], s40 offen sc1\n\t" \
  "s_nop 12\n\t" \
  "buffer_load_dwordx4 v[36:39], v100, %[rh], s76 offen sc1\n\t" \
  "s_nop 12\n\t" \
  "buffer_load_dwordx4 v[40:43], v100, %[rh], s77 offen sc1\n\t" \
  "s_nop 12\n\t" \
  "buffer_load_dwordx4 v[44:47], v100, %[rh], s78 offen sc1\n\t" \
  "3:\n\t" \
  "v_mov_b64 v[72:73], 0\n\t" \
  "v_mov_b64 v[74:75], 0\n\t" \
  "v_mov_b64 v[76:77], 0\n\t" \
  "v_mov_b64 v[78:79], 0\n\t" \
  "s_waitcnt lgkmcnt(0)\n\t" \
  "s_waitcnt vmcnt(3)\n\t" \
  "v_smfmac_f32_16x16x64_bf16 v[72:75], v[32:35], v[48:55], v104\n\t" \
  "ds_write_b128 v101, v[32:35] offset:4096\n\t" \
  "v_smfmac_f32_16x16x64_bf16 v[76:79], v[32:35], v[56:63], v104\n\t" \
  "s_waitcnt vmcnt(2)\n\t" \
  "ds_write_b128 v101, v[36:39] offset:5120\n\t" \
  "v_smfmac_f32_16x16x64_bf16 v[72:75], v[36:39], a[48:55], v104\n\t" \
  "v_smfmac_f32_16x16x64_bf16 v[76:79], v[36:39], a[56:63], v104\n\t" \
  "s_nop 3\n\t" \
  "v_cmp_u_f32 vcc, v72, v73\n\t" \
  "v_cmp_u_f32 s[82:83], v74, v75\n\t" \
  "s_waitcnt vmcnt(1)\n\t" \
  "v_smfmac_f32_16x16x64_bf16 v[72:75], v[40:43], a[64:71], v104\n\t" \
  "s_or_b64 vcc, vcc, s[82:83]\n\t" \
  "s_or_b64 vcc, vcc, s[80:81]\n\t" \
  "v_smfmac_f32_16x16x64_bf16 v[76:79], v[40:43], a[72:79], v104\n\t" \
  "s_waitcnt lgkmcnt(0)\n\t" \
  "s_cbranch_scc1 8f\n\t" \
  "5:\n\t" \
  "s_barrier\n\t" \
  "4:\n\t" \
  "s_memtime s[56:57]\n\t" \
  "ds_read_b128 v[48:51], v101 offset:8192\n\t" \
  "ds_read_b128 v[52:55], v101 offset:9216\n\t" \
  "ds_read_b128 v[56:59], v101 offset:13312\n\t" \
  "s_waitcnt vmcnt(0)\n\t" \
  "v_smfmac_f32_16x16x64_bf16 v[72:75], v[44:47], a[80:87], v104\n\t" \
  "ds_read_b128 v[60:63], v101 offset:12288\n\t" \
  "ds_read_b128 v[64:67], v101 offset:1024\n\t" \
  "v_smfmac_f32_16x16x64_bf16 v[76:79], v[44:47], a[88:95], v104\n\t" \
  "ds_read_b128 v[68:71], v101 offset:0\n\t" \
  "ds_write_b128 v101, v[40:43] offset:6144\n\t" \
  "s_nop 1\n\t" \
  "v_cmp_u_f32 vcc, v72, v73\n\t" \
  "v_cmp_u_f32 s[82:83], v74, v75\n\t" \
  "s_waitcnt lgkmcnt(6)\n\t" \
  "v_smfmac_f32_16x16x64_bf16 v[72:75], v[48:51], v[0:7], v104\n\t" \
  "s_or_b64 vcc, vcc, s[82:83]\n\t" \
  "v_smfmac_f32_16x16x64_bf16 v[76:79], v[48:51], v[8:15], v104\n\t" \
  "s_cbranch_scc1 9f\n\t" \
  "6:\n\t" \
  "s_waitcnt lgkmcnt(5)\n\t" \
  "v_smfmac_f32_16x16x64_bf16 v[72:75], v[52:55], a[96:103], v104\n\t" \
  "ds_write_b128 v101, v[44:47] offset:7168\n\t" \
  "v_smfmac_f32_16x16x64_bf16 v[76:79], v[52:55], a[104:111], v104\n\t" \
  "v_mov_b64 v[40:41], 0\n\t" \
  "s_waitcnt lgkmcnt(5)\n\t" \
  "v_smfmac_f32_16x16x64_bf16 v[72:75], v[56:59], a[144:151], v104\n\t" \
  "v_mov_b64 v[42:43], 0\n\t" \
  "v_smfmac_f32_16x16x64_bf16 v[76:79], v[56:59], a[152:159], v104\n\t" \
  "s_waitcnt lgkmcnt(4)\n\t" \
  "v_smfmac_f32_16x16x64_bf16 v[72:75], v[60:63], v[16:23], v104\n\t" \
  "s_waitcnt lgkmcnt(0)\n\t" \
  "s_barrier\n\t" \
  "v_smfmac_f32_16x16x64_bf16 v[76:79], v[60:63], v[24:31], v104\n\t" \
  "ds_read_b128 v[48:51], v101 offset:10240\n\t" \
  "ds_read_b128 v[52:55], v101 offset:14336\n\t" \
  "v_smfmac_f32_16x16x64_bf16 v[72:75], v[64:67], a[0:7], v104\n\t" \
  "ds_read_b128 v[56:59], v101 offset:2048\n\t" \
  "ds_read_b128 v[60:63], v101 offset:11264\n\t" \
  "v_smfmac_f32_16x16x64_bf16 v[76:79], v[64:67], a[8:15], v104\n\t" \
  "ds_read_b128 v[32:35], v101 offset:15360\n\t" \
  "ds_read_b128 v[36:39], v101 offset:3072\n\t" \
  "v_smfmac_f32_16x16x64_bf16 v[72:75], v[68:71], v[80:87], v104\n\t" \
  "ds_read_b128 v[0:3], v103 offset:0\n\t" \
  "v_mov_b64 v[44:45], 0\n\t" \
  "v_smfmac_f32_16x16x64_bf16 v[76:79], v[68:71], v[88:95], v104\n\t" \
  "v_mov_b64 v[46:47], 0\n\t" \
  "s_waitcnt lgkmcnt(5)\n\t" \
  "v_smfmac_f32_16x16x64_bf16 v[72:75], v[48:51], a[112:119], v104\n\t" \
  "ds_read_b128 v[4:7], v103 offset:1024\n\t" \
  "v_mov_b64 v[80:81], 0\n\t" \
  "v_smfmac_f32_16x16x64_bf16 v[72:75], v[52:55], a[160:167], v104\n\t" \
  "v_mov_b64 v[82:83], 0\n\t" \
  "s_waitcnt lgkmcnt(2)\n\t" \
  "v_smfmac_f32_16x16x64_bf16 v[72:75], v[56:59], a[16:23], v104\n\t" \
  "ds_read_b128 v[8:11], v103 offset:2048\n\t" \
  "v_mov_b64 v[84:85], 0\n\t" \
  "v_smfmac_f32_16x16x64_bf16 v[72:75], v[60:63], a[128:135], v104\n\t" \
  "ds_read_b128 v[12:15], v103 offset:3072\n\t" \
  "v_mov_b64 v[86:87], 0\n\t" \
  "v_smfmac_f32_16x16x64_bf16 v[72:75], v[32:35], a[176:183], v104\n\t" \
  "ds_read_b128 v[16:19], v103 offset:16384\n\t" \
  "v_mov_b64 v[88:89], 0\n\t" \
  "v_smfmac_f32_16x16x64_bf16 v[72:75], v[36:39], a[32:39], v104\n\t" \
  "ds_read_b128 v[20:23], v103 offset:17408\n\t" \
  "v_mov_b64 v[90:91], 0\n\t" \
  "v_smfmac_f32_16x16x64_bf16 v[76:79], v[48:51], a[120:127], v104\n\t" \
  "ds_read_b128 v[24:27], v103 offset:18432\n\t" \
  "v_mov_b64 v[92:93], 0\n\t" \
  "v_smfmac_f32_16x16x64_bf16 v[76:79], v[52:55], a[168:175], v104\n\t" \
  "v_add_f32 v96, v72, v74\n\t" \
  "v_add_f32 v97, v73, v75\n\t" \
  "v_cvt_pk_bf16_f32 v96, v96, v97\n\t" \
  "ds_write_b32 v105, v96\n\t" \
  "v_smfmac_f32_16x16x64_bf16 v[76:79], v[56:59], a[24:31], v104\n\t" \
  "ds_read_b128 v[28:31], v103 offset:19456\n\t" \
  "v_mov_b64 v[94:95], 0\n\t" \
  "v_smfmac_f32_16x16x64_bf16 v[76:79], v[60:63], a[136:143], v104\n\t" \
  "v_smfmac_f32_16x16x64_bf16 v[76:79], v[32:35], a[184:191], v104\n\t" \
  "v_smfmac_f32_16x16x64_bf16 v[76:79], v[36:39], a[40:47], v104\n\t" \
  "s_waitcnt lgkmcnt(1)\n\t" \
  "s_barrier\n\t" \
  "ds_read_b64_tr_b16 v[96:97], v107 offset:0\n\t" \
  "ds_read_b64_tr_b16 v[98:99], v107 offset:64\n\t" \
  "ds_read_b64_tr_b16 v[56:57], v107 offset:1024\n\t" \
  "ds_read_b64_tr_b16 v[58:59], v107 offset:1088\n\t" \
  "s_nop 1\n\t" \
  "v_add_f32 v116, v76, v78\n\t" \
  "v_add_f32 v117, v77, v79\n\t" \
  "v_cvt_pk_bf16_f32 v116, v116, v117\n\t" \
  "ds_write_b32 v105, v116 offset:256\n\t" \
  "s_waitcnt lgkmcnt(3)\n\t" \
  "v_smfmac_f32_16x16x64_bf16 v[40:43], v[96:99], a[192:199], v106\n\t" \
  "v_smfmac_f32_16x16x64_bf16 v[44:47], v[96:99], a[224:231], v106\n\t" \
  "s_waitcnt lgkmcnt(0)\n\t" \
  "s_barrier\n\t" \
  "ds_read_b64_tr_b16 v[52:53], v107 offset:256\n\t" \
  "ds_read_b64_tr_b16 v[54:55], v107 offset:320\n\t" \
  "v_smfmac_f32_16x16x64_bf16 v[40:43], v[56:59], a[208:215], v106\n\t" \
  "ds_read_b64_tr_b16 v[60:61], v107 offset:1280\n\t" \
  "ds_read_b64_tr_b16 v[62:63], v107 offset:1344\n\t" \
  "v_smfmac_f32_16x16x64_bf16 v[44:47], v[56:59], v[0:7], v106\n\t" \
  "v_smfmac_f32_16x16x64_bf16 v[80:83], v[96:99], a[240:247], v106\n\t" \
  "v_mov_b64 v[32:33], 0\n\t" \
  "v_smfmac_f32_16x16x64_bf16 v[84:87], v[96:99], v[136:143], v106\n\t" \
  "v_mov_b64 v[34:35], 0\n\t" \
  "v_smfmac_f32_16x16x64_bf16 v[80:83], v[56:59], v[120:127], v106\n\t" \
  "v_mov_b64 v[36:37], 0\n\t" \
  "v_smfmac_f32_16x16x64_bf16 v[84:87], v[56:59], v[16:23], v106\n\t" \
  "s_waitcnt lgkmcnt(2)\n\t" \
  "v_smfmac_f32_16x16x64_bf16 v[40:43], v[52:55], a[200:207], v106\n\t" \
  "v_smfmac_f32_16x16x64_bf16 v[44:47], v[52:55], a[232:239], v106\n\t" \
  "s_xor_b32 s76, s47, 0x40000\n\t" \
  "s_waitcnt lgkmcnt(0)\n\t" \
  "v_smfmac_f32_16x16x64_bf16 v[40:43], v[60:63], a[216:223], v106\n\t" \
  "v_mov_b64 v[38:39], 0\n\t" \
  "s_xor_b32 s77, s47, 0x80000\n\t" \
  "v_smfmac_f32_16x16x64_bf16 v[44:47], v[60:63], v[8:15], v106\n\t" \
  "s_xor_b32 s78, s47, 0xc0000\n\t" \
  "v_smfmac_f32_16x16x64_bf16 v[80:83], v[52:55], a[248:255], v106\n\t" \
  "ds_read_b128 v[0:3], v103 offset:32768\n\t" \
  "v_cvt_pk_bf16_f32 v64, v40, v41\n\t" \
  "v_smfmac_f32_16x16x64_bf16 v[84:87], v[52:55], v[144:151], v106\n\t" \
  "ds_read_b128 v[4:7], v103 offset:33792\n\t" \
  "v_cvt_pk_bf16_f32 v65, v42, v43\n\t" \
  "v_smfmac_f32_16x16x64_bf16 v[80:83], v[60:63], v[128:135], v106\n\t" \
  "ds_read_b128 v[8:11], v103 offset:34816\n\t" \
  "v_cvt_pk_bf16_f32 v66, v44, v45\n\t" \
  "v_smfmac_f32_16x16x64_bf16 v[84:87], v[60:63], v[24:31], v106\n\t" \
  "ds_read_b128 v[12:15], v103 offset:35840\n\t" \
  "v_cvt_pk_bf16_f32 v67, v46, v47\n\t" \
  "v_smfmac_f32_16x16x64_bf16 v[88:91], v[96:99], v[152:159], v106\n\t" \
  "v_permlane16_swap_b32 v64, v66\n\t" \
  "v_cvt_pk_bf16_f32 v68, v80, v81\n\t" \
  "v_smfmac_f32_16x16x64_bf16 v[92:95], v[96:99], v[184:191], v106\n\t" \
  "v_permlane16_swap_b32 v65, v67\n\t" \
  "v_cvt_pk_bf16_f32 v69, v82, v83\n\t" \
  "v_smfmac_f32_16x16x64_bf16 v[88:91], v[52:55], v[160:167], v106\n\t" \
  "buffer_store_dwordx4 v[64:67], v108, %[ru], s47 offen sc1\n\t" \
  "v_cvt_pk_bf16_f32 v70, v84, v85\n\t" \
  "v_smfmac_f32_16x16x64_bf16 v[92:95], v[52:55], v[192:199], v106\n\t" \
  "v_cvt_pk_bf16_f32 v71, v86, v87\n\t" \
  "v_smfmac_f32_16x16x64_bf16 v[88:91], v[56:59], v[168:175], v106\n\t" \
  "ds_read_b128 v[16:19], v103 offset:49152\n\t" \
  "v_permlane16_swap_b32 v68, v70\n\t" \
  "s_waitcnt lgkmcnt(3)\n\t" \
  "v_smfmac_f32_16x16x64_bf16 v[92:95], v[56:59], v[0:7], v106\n\t" \
  "ds_read_b128 v[20:23], v103 offset:50176\n\t" \
  "v_permlane16_swap_b32 v69, v71\n\t" \
  "v_smfmac_f32_16x16x64_bf16 v[88:91], v[60:63], v[176:183], v106\n\t" \
  "ds_read_b128 v[24:27], v103 offset:51200\n\t" \
  "buffer_store_dwordx4 v[68:71], v108, %[ru], s76 offen sc1\n\t" \
  "s_waitcnt lgkmcnt(3)\n\t" \
  "v_smfmac_f32_16x16x64_bf16 v[92:95], v[60:63], v[8:15], v106\n\t" \
  "ds_read_b128 v[28:31], v103 offset:52224\n\t" \
  "v_smfmac_f32_16x16x64_bf16 v[32:35], v[96:99], v[200:207], v106\n\t" \
  "v_cvt_pk_bf16_f32 v72, v88, v89\n\t" \
  "v_smfmac_f32_16x16x64_bf16 v[36:39], v[96:99], v[232:239], v106\n\t" \
  "v_cvt_pk_bf16_f32 v73, v90, v91\n\t" \
  "v_smfmac_f32_16x16x64_bf16 v[32:35], v[52:55], v[208:215], v106\n\t" \
  "v_cvt_pk_bf16_f32 v74, v92, v93\n\t" \
  "v_smfmac_f32_16x16x64_bf16 v[36:39], v[52:55], v[240:247], v106\n\t" \
  "v_cvt_pk_bf16_f32 v75, v94, v95\n\t" \
  "v_smfmac_f32_16x16x64_bf16 v[32:35], v[56:59], v[216:223], v106\n\t" \
  "v_permlane16_swap_b32 v72, v74\n\t" \
  "v_smfmac_f32_16x16x64_bf16 v[32:35], v[60:63], v[224:231], v106\n\t" \
  "v_permlane16_swap_b32 v73, v75\n\t" \
  "s_waitcnt lgkmcnt(2)\n\t" \
  "v_smfmac_f32_16x16x64_bf16 v[36:39], v[56:59], v[16:23], v106\n\t" \
  "buffer_store_dwordx4 v[72:75], v108, %[ru], s77 offen sc1\n\t" \
  "s_waitcnt lgkmcnt(0)\n\t" \
  "v_smfmac_f32_16x16x64_bf16 v[36:39], v[60:63], v[24:31], v106\n\t" \
  "v_cvt_pk_bf16_f32 v76, v32, v33\n\t" \
  "v_cvt_pk_bf16_f32 v77, v34, v35\n\t" \
  "s_nop 5\n\t" \
  "v_cvt_pk_bf16_f32 v78, v36, v37\n\t" \
  "v_cvt_pk_bf16_f32 v79, v38, v39\n\t" \
  "s_nop 0\n\t" \
  "v_permlane16_swap_b32 v76, v78\n\t" \
  "v_permlane16_swap_b32 v77, v79\n\t" \
  "buffer_store_dwordx4 v[76:79], v108, %[ru], s78 offen sc1\n\t" \
  "s_memtime s[76:77]\n\t" \
  "s_mov_b32 s42, 0\n\t" \
  "s_waitcnt lgkmcnt(0)\n\t" \
  "s_mov_b32 s57, s76\n\t" \
  "s_add_i32 s41, s56, 1950\n\t" \
  "s_sub_i32 s76, s92, s51\n\t" \
  "s_sub_i32 s77, s93, s53\n\t" \
  "s_cmp_eq_u32 s76, 0\n\t" \
  "s_cbranch_scc1 21f\n\t" \
  "v_cvt_f32_u32 v117, s76\n\t" \
  "v_cvt_f32_u32 v116, s77\n\t" \
  "v_rcp_f32 v117, v117\n\t" \
  "s_mov_b32 s51, s92\n\t" \
  "v_mul_f32 v117, v117, v116\n\t" \
  "v_max_f32 v117, 0x3f028f5c, v117\n\t" \
  "v_min_f32 v117, 0x3ffeb852, v117\n\t" \
  "v_rcp_f32 v116, v117\n\t" \
  "v_mul_f32 v117, 0x4f000000, v117\n\t" \
  "v_mul_f32 v116, 0x47800000, v116\n\t" \
  "v_cvt_u32_f32 v117, v117\n\t" \
  "v_cvt_u32_f32 v116, v116\n\t" \
  "s_mov_b32 s53, s93\n\t" \
  "v_readfirstlane_b32 s46, v117\n\t" \
  "v_readfirstlane_b32 s52, v116\n\t" \
  "21:\n\t" \
  "s_add_i32 s44, s44, 1\n\t" \
  "s_and_b32 s58, s58, 4\n\t" \
  "s_and_b32 s76, s44, 64\n\t" \
  "s_or_b32 s42, s42, s76\n\t" \
  "s_cbranch_scc0 20b\n\t" \
  "s_branch 99f\n\t" \
  "14:\n\t" \
  "s_sleep 3\n\t" \
  "s_add_u32 s59, s59, 0x10000\n\t" \
  "s_cmp_lt_u32 s59, 0x400000\n\t" \
  "s_cbranch_scc1 13b\n\t" \
  "s_mov_b64 exec, -1\n\t" \
  "s_mov_b32 s42, 16\n\t" \
  "s_branch 99f\n\t" \
  "8:\n\t" \
  "s_bitcmp1_b32 s58, 1\n\t" \
  "s_cbranch_scc1 4b\n\t" \
  "s_bitcmp1_b32 s58, 2\n\t" \
  "s_cbranch_scc1 5b\n\t" \
  "v_max3_u32 v116, v32, v33, v34\n\t" \
  "v_max3_u32 v117, v35, v36, v37\n\t" \
  "v_max3_u32 v116, v116, v38, v39\n\t" \
  "v_max_u32 v116, v116, v117\n\t" \
  "v_cmp_eq_u32 vcc, -1, v116\n\t" \
  "s_cbranch_vccz 5b\n\t" \
  "s_branch 0f\n\t" \
  "9:\n\t" \
  "s_bitcmp1_b32 s58, 2\n\t" \
  "s_cbranch_scc1 6b\n\t" \
  "v_max3_u32 v116, v40, v41, v42\n\t" \
  "v_max3_u32 v117, v43, v44, v45\n\t" \
  "v_max3_u32 v116, v116, v46, v47\n\t" \
  "v_max_u32 v116, v116, v117\n\t" \
  "v_cmp_eq_u32 vcc, -1, v116\n\t" \
  "s_cbranch_vccz 6b\n\t" \
  "s_or_b32 s58, s58, 2\n\t" \
  "0:\n\t" \
  "s_or_b32 s58, s58, 1\n\t" \
  "11:\n\t" \
  "buffer_load_dwordx4 v[32:35], v100, %[rh], s40 offen sc1\n\t" \
  "buffer_load_dwordx4 v[36:39], v100, %[rh], s76 offen sc1\n\t" \
  "buffer_load_dwordx4 v[40:43], v100, %[rh], s77 offen sc1\n\t" \
  "buffer_load_dwordx4 v[44:47], v100, %[rh], s78 offen sc1\n\t" \
  "s_waitcnt lgkmcnt(0)\n\t" \
  "s_branch 10b\n\t" \
  "17:\n\t" \
  "s_add_u32 s59, s59, 1\n\t" \
  "s_bitcmp1_b32 s58, 2\n\t" \
  "s_cbranch_scc1 3b\n\t" \
  "s_waitcnt vmcnt(0)\n\t" \
  "v_max_u32 v116, v32, v33\n\t" \
  "v_max3_u32 v116, v116, v34, v35\n\t" \
  "v_max3_u32 v116, v116, v36, v37\n\t" \
  "v_max3_u32 v116, v116, v38, v39\n\t" \
  "v_max3_u32 v116, v116, v40, v41\n\t" \
  "v_max3_u32 v116, v116, v42, v43\n\t" \
  "v_max3_u32 v116, v116, v44, v45\n\t" \
  "v_max3_u32 v116, v116, v46, v47\n\t" \
  "v_cmp_eq_u32 vcc, -1, v116\n\t" \
  "s_cbranch_vccz 3b\n\t" \
  "s_bitcmp1_b32 s59, 6\n\t" \
  "s_cbranch_scc1 12f\n\t" \
  "buffer_load_dwordx4 v[32:35], v100, %[rh], s40 offen sc1\n\t" \
  "buffer_load_dwordx4 v[36:39], v100, %[rh], s76 offen sc1\n\t" \
  "buffer_load_dwordx4 v[40:43], v100, %[rh], s77 offen sc1\n\t" \
  "buffer_load_dwordx4 v[44:47], v100, %[rh], s78 offen sc1\n\t" \
  "s_branch 17b\n\t" \
  "12:\n\t" \
  "s_mov_b32 s42, s58\n\t" \
  "s_branch 99f\n\t" \
  "92:\n\t" \
  "s_mov_b32 s58, s42\n\t" \
  "20:\n\t" \
  "s_and_b32 s76, s44, 7\n\t" \
  "s_lshl_b32 s76, s76, 15\n\t" \
  "s_add_i32 s40, s54, s76\n\t" \
  "s_and_b32 s76, s44, 3\n\t" \
  "s_lshl_b32 s45, s76, 14\n\t" \
  "s_lshl_b32 s47, s76, 20\n\t" \
  "s_add_i32 s47, s47, s55\n\t" \
  "s_lshr_b32 s76, s47, 21\n\t" \
  "s_bitcmp1_b64 %[sel], s76\n\t" \
  "s_cselect_b32 s76, 0x2000, 0\n\t" \
  "s_add_i32 s47, s47, s76\n\t" \
  "s_add_i32 s49, s44, -1\n\t" \
  "s_and_b32 s48, s49, 2\n\t" \
  "s_lshl_b32 s48, s48, 13\n\t" \
  "s_and_b32 s76, s49, 1\n\t" \
  "s_lshl_b32 s76, s76, 12\n\t" \
  "s_add_i32 s48, s48, s76\n\t" \
  "s_lshl_b32 s50, s53, 8\n\t" \
  "s_mov_b32 s59, 0\n\t" \
  "s_mov_b32 s92, s51\n\t" \
  "s_mov_b32 s93, s53\n\t" \
  "s_xor_b32 s76, s40, 0x400\n\t" \
  "s_xor_b32 s77, s40, 0x800\n\t" \
  "s_xor_b32 s78, s40, 0xc00\n\t" \
  "s_cmp_lg_u32 s58, 0\n\t" \
  "s_cbranch_scc1 11f\n\t" \
  "10:\n\t" \
  "ds_read_b128 v[48:51], v102 offset:32768\n\t" \
  "ds_read_b128 v[52:55], v102 offset:33792\n\t" \
  "ds_read_b128 v[56:59], v102 offset:34816\n\t" \
  "ds_read_b128 v[60:63], v102 offset:35840\n\t" \
  "ds_read_b128 v[0:3], v102 offset:49152\n\t" \
  "ds_read_b128 v[4:7], v102 offset:50176\n\t" \
  "ds_read_b128 v[8:11], v102 offset:51200\n\t" \
  "ds_read_b128 v[12:15], v102 offset:52224\n\t" \
  "ds_read_b128 v[16:19], v102 offset:0\n\t" \
  "ds_read_b128 v[20:23], v102 offset:1024\n\t" \
  "ds_read_b128 v[24:27], v102 offset:2048\n\t" \
  "ds_read_b128 v[28:31], v102 offset:3072\n\t" \
  "s_waitcnt lgkmcnt(4)\n\t" \
  "ds_read_b128 v[80:83], v102 offset:16384\n\t" \
  "ds_read_b128 v[84:87], v102 offset:17408\n\t" \
  "ds_read_b128 v[88:91], v102 offset:18432\n\t" \
  "ds_read_b128 v[92:95], v102 offset:19456\n\t" \
  "46:\n\t" \
  "s_bitcmp1_b32 s58, 1\n\t" \
  "s_cselect_b64 s[80:81], -1, 0\n\t" \
  "s_cmp_lg_u32 s58, 0\n\t" \
  "s_cbranch_scc1 17f\n\t" \
  "s_mov_b64 exec, 0xff\n\t" \
  "16:\n\t" \
  "s_memtime vcc\n\t" \
  "s_waitcnt lgkmcnt(0)\n\t" \
  "s_sub_i32 s79, s41, vcc_lo\n\t" \
  "s_cmp_gt_i32 s79, 64\n\t" \
  "s_cbranch_scc1 16b\n\t" \
  "13:\n\t" \
  "buffer_load_dword v116, v112, %[rh], s48 offen sc1\n\t" \
  "s_memtime vcc\n\t" \
  "s_memrealtime s[82:83]\n\t" \
  "s_nop 4\n\t" \
  "s_memrealtime s[84:85]\n\t" \
  "s_nop 4\n\t" \
  "s_memrealtime s[56:57]\n\t" \
  "s_nop 4\n\t" \
  "s_memrealtime s[86:87]\n\t" \
  "s_nop 4\n\t" \
  "s_memrealtime s[88:89]\n\t" \
  "s_nop 4\n\t" \
  "s_memrealtime s[90:91]\n\t" \
  "s_mov_b32 s92, 0\n\t" \
  "s_waitcnt lgkmcnt(0)\n\t" \
  "s_cmp_eq_u32 s82, s84\n\t" \
  "s_addc_u32 s92, s92, 0\n\t" \
  "s_cmp_eq_u32 s82, s56\n\t" \
  "s_addc_u32 s92, s92, 0\n\t" \
  "s_cmp_eq_u32 s82, s86\n\t" \
  "s_addc_u32 s92, s92, 0\n\t" \
  "s_cmp_eq_u32 s82, s88\n\t" \
  "s_addc_u32 s92, s92, 0\n\t" \
  "s_cmp_eq_u32 s82, s90\n\t" \
  "s_addc_u32 s92, s92, 0\n\t" \
  "s_mul_i32 s92, s92, 24\n\t" \
  "s_add_i32 s92, s92, vcc_lo\n\t" \
  "s_lshl4_add_u32 s93, s82, 64\n\t" \
  "s_lshl_b32 s50, s93, 8\n\t" \
  "s_waitcnt vmcnt(0)\n\t" \
  "15:\n\t" \
  "v_mov_b32 v117, 20000\n\t" \
  "v_cmp_ne_u32_sdwa vcc, v116, s49 src0_sel:BYTE_0 src1_sel:DWORD\n\t" \
  "v_subrev_u32 v116, s50, v116\n\t" \
  "v_ashrrev_i32 v116, 8, v116\n\t" \
  "s_nop 1\n\t" \
  "v_max_i32_dpp v116, v116, v116 quad_perm:[1,0,3,2] row_mask:0xf bank_mask:0xf\n\t" \
  "s_cbranch_vccnz 14f\n\t" \
  "s_nop 0\n\t" \
  "v_max_i32_dpp v116, v116, v116 quad_perm:[2,3,0,1] row_mask:0xf bank_mask:0xf\n\t" \
  "s_nop 1\n\t" \
  "v_max_i32_dpp v116, v116, v116 row_half_mirror row_mask:0xf bank_mask:0xf\n\t" \
  "v_med3_i32 v116, v116, 0, v117\n\t" \
  "v_mul_u32_u24 v116, s52, v116\n\t" \
  "v_lshrrev_b32 v116, 16, v116\n\t" \
  "s_mov_b64 exec, -1\n\t" \
  "v_readfirstlane_b32 s84, v116\n\t" \
  "s_add_i32 s84, s84, s92\n\t" \
  ".p2align 6\n\t" \
  "s_nop 0\n\t" \
  "s_nop 0\n\t" \
  "s_nop 0\n\t" \
  "s_nop 0\n\t" \
  "s_nop 0\n\t" \
  "s_nop 0\n\t" \
  "s_nop 0\n\t" \
  "s_nop 0\n\t" \
  "1:\n\t" \
  "s_memtime vcc\n\t" \
  "s_waitcnt lgkmcnt(0)\n\t" \
  "s_sub_i32 s79, s84, vcc_lo\n\t" \
  "s_cmp_gt_i32 s79, 128\n\t" \
  "s_cbranch_scc1 1b\n\t" \
  ".p2align 4\n\t" \
  "2:\n\t" \
  "s_sub_i32 s79, s79, 33\n\t" \
  "s_cmp_gt_i32 s79, 56\n\t" \
  "s_cbranch_scc1 2b\n\t" \
  "buffer_load_dwordx4 v[32:35], v100, %[rh], s40 offen sc1\n\t" \
  "s_nop 12\n\t" \
  "buffer_load_dwordx4 v[36:39], v100, %[rh], s76 offen sc1\n\t" \
  "s_nop 12\n\t" \
  "buffer_load_dwordx4 v[40:43], v100, %[rh], s77 offen sc1\n\t" \
  "s_nop 12\n\t" \
  "buffer_load_dwordx4 v[44:47], v100, %[rh], s78 offen sc1\n\t" \
  "3:\n\t" \
  "v_mov_b64 v[72:73], 0\n\t" \
  "v_mov_b64 v[74:75], 0\n\t" \
  "v_mov_b64 v[76:77], 0\n\t" \
  "v_mov_b64 v[78:79], 0\n\t" \
  "s_waitcnt lgkmcnt(0)\n\t" \
  "s_waitcnt vmcnt(3)\n\t" \
  "v_smfmac_f32_16x16x64_bf16 v[72:75], v[32:35], v[48:55], v104\n\t" \
  "ds_write_b128 v101, v[32:35] offset:8192\n\t" \
  "v_smfmac_f32_16x16x64_bf16 v[76:79], v[32:35], v[56:63], v104\n\t" \
  "s_waitcnt vmcnt(2)\n\t" \
  "ds_write_b128 v101, v[36:39] offset:9216\n\t" \
  "v_smfmac_f32_16x16x64_bf16 v[72:75], v[36:39], a[96:103], v104\n\t" \
  "v_smfmac_f32_16x16x64_bf16 v[76:79], v[36:39], a[104:111], v104\n\t" \
  "s_nop 3\n\t" \
  "v_cmp_u_f32 vcc, v72, v73\n\t" \
  "v_cmp_u_f32 s[82:83], v74, v75\n\t" \
  "s_waitcnt vmcnt(1)\n\t" \
  "v_smfmac_f32_16x16x64_bf16 v[72:75], v[40:43], a[112:119], v104\n\t" \
  "s_or_b64 vcc, vcc, s[82:83]\n\t" \
  "s_or_b64 vcc, vcc, s[80:81]\n\t" \
  "v_smfmac_f32_16x16x64_bf16 v[76:79], v[40:43], a[120:127], v104\n\t" \
  "s_waitcnt lgkmcnt(0)\n\t" \
  "s_cbranch_scc1 8f\n\t" \
  "5:\n\t" \
  "s_barrier\n\t" \
  "4:\n\t" \
  "s_memtime s[56:57]\n\t" \
  "ds_read_b128 v[48:51], v101 offset:12288\n\t" \
  "ds_read_b128 v[52:55], v101 offset:13312\n\t" \
  "ds_read_b128 v[56:59], v101 offset:1024\n\t" \
  "s_waitcnt vmcnt(0)\n\t" \
  "v_smfmac_f32_16x16x64_bf16 v[72:75], v[44:47], a[128:135], v104\n\t" \
  "ds_read_b128 v[60:63], v101 offset:0\n\t" \
  "ds_read_b128 v[64:67], v101 offset:5120\n\t" \
  "v_smfmac_f32_16x16x64_bf16 v[76:79], v[44:47], a[136:143], v104\n\t" \
  "ds_read_b128 v[68:71], v101 offset:4096\n\t" \
  "ds_write_b128 v101, v[40:43] offset:10240\n\t" \
  "s_nop 1\n\t" \
  "v_cmp_u_f32 vcc, v72, v73\n\t" \
  "v_cmp_u_f32 s[82:83], v74, v75\n\t" \
  "s_waitcnt lgkmcnt(6)\n\t" \
  "v_smfmac_f32_16x16x64_bf16 v[72:75], v[48:51], v[0:7], v104\n\t" \
  "s_or_b64 vcc, vcc, s[82:83]\n\t" \
  "v_smfmac_f32_16x16x64_bf16 v[76:79], v[48:51], v[8:15], v104\n\t" \
  "s_cbranch_scc1 9f\n\t" \
  "6:\n\t" \
  "s_waitcnt lgkmcnt(5)\n\t" \
  "v_smfmac_f32_16x16x64_bf16 v[72:75], v[52:55], a[144:151], v104\n\t" \
  "ds_write_b128 v101, v[44:47] offset:11264\n\t" \
  "v_smfmac_f32_16x16x64_bf16 v[76:79], v[52:55], a[152:159], v104\n\t" \
  "v_mov_b64 v[40:41], 0\n\t" \
  "s_waitcnt lgkmcnt(5)\n\t" \
  "v_smfmac_f32_16x16x64_bf16 v[72:75], v[56:59], a[0:7], v104\n\t" \
  "v_mov_b64 v[42:43], 0\n\t" \
  "v_smfmac_f32_16x16x64_bf16 v[76:79], v[56:59], a[8:15], v104\n\t" \
  "s_waitcnt lgkmcnt(4)\n\t" \
  "v_smfmac_f32_16x16x64_bf16 v[72:75], v[60:63], v[16:23], v104\n\t" \
  "s_waitcnt lgkmcnt(0)\n\t" \
  "s_barrier\n\t" \
  "v_smfmac_f32_16x16x64_bf16 v[76:79], v[60:63], v[24:31], v104\n\t" \
  "ds_read_b128 v[48:51], v101 offset:14336\n\t" \
  "ds_read_b128 v[52:55], v101 offset:2048\n\t" \
  "v_smfmac_f32_16x16x64_bf16 v[72:75], v[64:67], a[48:55], v104\n\t" \
  "ds_read_b128 v[56:59], v101 offset:6144\n\t" \
  "ds_read_b128 v[60:63], v101 offset:15360\n\t" \
  "v_smfmac_f32_16x16x64_bf16 v[76:79], v[64:67], a[56:63], v104\n\t" \
  "ds_read_b128 v[32:35], v101 offset:3072\n\t" \
  "ds_read_b128 v[36:39], v101 offset:7168\n\t" \
  "v_smfmac_f32_16x16x64_bf16 v[72:75], v[68:71], v[80:87], v104\n\t" \
  "ds_read_b128 v[0:3], v103 offset:0\n\t" \
  "v_mov_b64 v[44:45], 0\n\t" \
  "v_smfmac_f32_16x16x64_bf16 v[76:79], v[68:71], v[88:95], v104\n\t" \
  "v_mov_b64 v[46:47], 0\n\t" \
  "s_waitcnt lgkmcnt(5)\n\t" \
  "v_smfmac_f32_16x16x64_bf16 v[72:75], v[48:51], a[160:167], v104\n\t" \
  "ds_read_b128 v[4:7], v103 offset:1024\n\t" \
  "v_mov_b64 v[80:81], 0\n\t" \
  "v_smfmac_f32_16x16x64_bf16 v[72:75], v[52:55], a[16:23], v104\n\t" \
  "v_mov_b64 v[82:83], 0\n\t" \
  "s_waitcnt lgkmcnt(2)\n\t" \
  "v_smfmac_f32_16x16x64_bf16 v[72:75], v[56:59], a[64:71], v104\n\t" \
  "ds_read_b128 v[8:11], v103 offset:2048\n\t" \
  "v_mov_b64 v[84:85], 0\n\t" \
  "v_smfmac_f32_16x16x64_bf16 v[72:75], v[60:63], a[176:183], v104\n\t" \
  "ds_read_b128 v[12:15], v103 offset:3072\n\t" \
  "v_mov_b64 v[86:87], 0\n\t" \
  "v_smfmac_f32_16x16x64_bf16 v[72:75], v[32:35], a[32:39], v104\n\t" \
  "ds_read_b128 v[16:19], v103 offset:16384\n\t" \
  "v_mov_b64 v[88:89], 0\n\t" \
  "v_smfmac_f32_16x16x64_bf16 v[72:75], v[36:39], a[80:87], v104\n\t" \
  "ds_read_b128 v[20:23], v103 offset:17408\n\t" \
  "v_mov_b64 v[90:91], 0\n\t" \
  "v_smfmac_f32_16x16x64_bf16 v[76:79], v[48:51], a[168:175], v104\n\t" \
  "ds_read_b128 v[24:27], v103 offset:18432\n\t" \
  "v_mov_b64 v[92:93], 0\n\t" \
  "v_smfmac_f32_16x16x64_bf16 v[76:79], v[52:55], a[24:31], v104\n\t" \
  "v_add_f32 v96, v72, v74\n\t" \
  "v_add_f32 v97, v73, v75\n\t" \
  "v_cvt_pk_bf16_f32 v96, v96, v97\n\t" \
  "ds_write_b32 v105, v96\n\t" \
  "v_smfmac_f32_16x16x64_bf16 v[76:79], v[56:59], a[72:79], v104\n\t" \
  "ds_read_b128 v[28:31], v103 offset:19456\n\t" \
  "v_mov_b64 v[94:95], 0\n\t" \
  "v_smfmac_f32_16x16x64_bf16 v[76:79], v[60:63], a[184:191], v104\n\t" \
  "v_smfmac_f32_16x16x64_bf16 v[76:79], v[32:35], a[40:47], v104\n\t" \
  "v_smfmac_f32_16x16x64_bf16 v[76:79], v[36:39], a[88:95], v104\n\t" \
  "s_waitcnt lgkmcnt(1)\n\t" \
  "s_barrier\n\t" \
  "ds_read_b64_tr_b16 v[96:97], v107 offset:0\n\t" \
  "ds_read_b64_tr_b16 v[98:99], v107 offset:64\n\t" \
  "ds_read_b64_tr_b16 v[56:57], v107 offset:1024\n\t" \
  "ds_read_b64_tr_b16 v[58:59], v107 offset:1088\n\t" \
  "s_nop 1\n\t" \
  "v_add_f32 v116, v76, v78\n\t" \
  "v_add_f32 v117, v77, v79\n\t" \
  "v_cvt_pk_bf16_f32 v116, v116, v117\n\t" \
  "ds_write_b32 v105, v116 offset:256\n\t" \
  "s_waitcnt lgkmcnt(3)\n\t" \
  "v_smfmac_f32_16x16x64_bf16 v[40:43], v[96:99], a[192:199], v106\n\t" \
  "v_smfmac_f32_16x16x64_bf16 v[44:47], v[96:99], a[224:231], v106\n\t" \
  "s_waitcnt lgkmcnt(0)\n\t" \
  "s_barrier\n\t" \
  "ds_read_b64_tr_b16 v[52:53], v107 offset:256\n\t" \
  "ds_read_b64_tr_b16 v[54:55], v107 offset:320\n\t" \
  "v_smfmac_f32_16x16x64_bf16 v[40:43], v[56:59], a[208:215], v106\n\t" \
  "ds_read_b64_tr_b16 v[60:61], v107 offset:1280\n\t" \
  "ds_read_b64_tr_b16 v[62:63], v107 offset:1344\n\t" \
  "v_smfmac_f32_16x16x64_bf16 v[44:47], v[56:59], v[0:7], v106\n\t" \
  "v_smfmac_f32_16x16x64_bf16 v[80:83], v[96:99], a[240:247], v106\n\t" \
  "v_mov_b64 v[32:33], 0\n\t" \
  "v_smfmac_f32_16x16x64_bf16 v[84:87], v[96:99], v[136:143], v106\n\t" \
  "v_mov_b64 v[34:35], 0\n\t" \
  "v_smfmac_f32_16x16x64_bf16 v[80:83], v[56:59], v[120:127], v106\n\t" \
  "v_mov_b64 v[36:37], 0\n\t" \
  "v_smfmac_f32_16x16x64_bf16 v[84:87], v[56:59], v[16:23], v106\n\t" \
  "s_waitcnt lgkmcnt(2)\n\t" \
  "v_smfmac_f32_16x16x64_bf16 v[40:43], v[52:55], a[200:207], v106\n\t" \
  "v_smfmac_f32_16x16x64_bf16 v[44:47], v[52:55], a[232:239], v106\n\t" \
  "s_xor_b32 s76, s47, 0x40000\n\t" \
  "s_waitcnt lgkmcnt(0)\n\t" \
  "v_smfmac_f32_16x16x64_bf16 v[40:43], v[60:63], a[216:223], v106\n\t" \
  "v_mov_b64 v[38:39], 0\n\t" \
  "s_xor_b32 s77, s47, 0x80000\n\t" \
  "v_smfmac_f32_16x16x64_bf16 v[44:47], v[60:63], v[8:15], v106\n\t" \
  "s_xor_b32 s78, s47, 0xc0000\n\t" \
  "v_smfmac_f32_16x16x64_bf16 v[80:83], v[52:55], a[248:255], v106\n\t" \
  "ds_read_b128 v[0:3], v103 offset:32768\n\t" \
  "v_cvt_pk_bf16_f32 v64, v40, v41\n\t" \
  "v_smfmac_f32_16x16x64_bf16 v[84:87], v[52:55], v[144:151], v106\n\t" \
  "ds_read_b128 v[4:7], v103 offset:33792\n\t" \
  "v_cvt_pk_bf16_f32 v65, v42, v43\n\t" \
  "v_smfmac_f32_16x16x64_bf16 v[80:83], v[60:63], v[128:135], v106\n\t" \
  "ds_read_b128 v[8:11], v103 offset:34816\n\t" \
  "v_cvt_pk_bf16_f32 v66, v44, v45\n\t" \
  "v_smfmac_f32_16x16x64_bf16 v[84:87], v[60:63], v[24:31], v106\n\t" \
  "ds_read_b128 v[12:15], v103 offset:35840\n\t" \
  "v_cvt_pk_bf16_f32 v67, v46, v47\n\t" \
  "v_smfmac_f32_16x16x64_bf16 v[88:91], v[96:99], v[152:159], v106\n\t" \
  "v_permlane16_swap_b32 v64, v66\n\t" \
  "v_cvt_pk_bf16_f32 v68, v80, v81\n\t" \
  "v_smfmac_f32_16x16x64_bf16 v[92:95], v[96:99], v[184:191], v106\n\t" \
  "v_permlane16_swap_b32 v65, v67\n\t" \
  "v_cvt_pk_bf16_f32 v69, v82, v83\n\t" \
  "v_smfmac_f32_16x16x64_bf16 v[88:91], v[52:55], v[160:167], v106\n\t" \
  "v_cvt_pk_bf16_f32 v70, v84, v85\n\t" \
  "v_smfmac_f32_16x16x64_bf16 v[92:95], v[52:55], v[192:199], v106\n\t" \
  "buffer_store_dwordx4 v[64:67], v108, %[ru], s47 offen sc1\n\t" \
  "v_cvt_pk_bf16_f32 v71, v86, v87\n\t" \
  "v_smfmac_f32_16x16x64_bf16 v[88:91], v[56:59], v[168:175], v106\n\t" \
  "ds_read_b128 v[16:19], v103 offset:49152\n\t" \
  "v_permlane16_swap_b32 v68, v70\n\t" \
  "s_waitcnt lgkmcnt(3)\n\t" \
  "v_smfmac_f32_16x16x64_bf16 v[92:95], v[56:59], v[0:7], v106\n\t" \
  "ds_read_b128 v[20:23], v103 offset:50176\n\t" \
  "v_permlane16_swap_b32 v69, v71\n\t" \
  "v_smfmac_f32_16x16x64_bf16 v[88:91], v[60:63], v[176:183], v106\n\t" \
  "ds_read_b128 v[24:27], v103 offset:51200\n\t" \
  "s_waitcnt lgkmcnt(3)\n\t" \
  "v_smfmac_f32_16x16x64_bf16 v[92:95], v[60:63], v[8:15], v106\n\t" \
  "ds_read_b128 v[28:31], v103 offset:52224\n\t" \
  "buffer_store_dwordx4 v[68:71], v108, %[ru], s76 offen sc1\n\t" \
  "v_smfmac_f32_16x16x64_bf16 v[32:35], v[96:99], v[200:207], v106\n\t" \
  "v_cvt_pk_bf16_f32 v72, v88, v89\n\t" \
  "v_smfmac_f32_16x16x64_bf16 v[36:39], v[96:99], v[232:239], v106\n\t" \
  "v_cvt_pk_bf16_f32 v73, v90, v91\n\t" \
  "v_smfmac_f32_16x16x64_bf16 v[32:35], v[52:55], v[208:215], v106\n\t" \
  "v_cvt_pk_bf16_f32 v74, v92, v93\n\t" \
  "v_smfmac_f32_16x16x64_bf16 v[36:39], v[52:55], v[240:247], v106\n\t" \
  "v_cvt_pk_bf16_f32 v75, v94, v95\n\t" \
  "v_smfmac_f32_16x16x64_bf16 v[32:35], v[56:59], v[216:223], v106\n\t" \
  "v_permlane16_swap_b32 v72, v74\n\t" \
  "v_smfmac_f32_16x16x64_bf16 v[32:35], v[60:63], v[224:231], v106\n\t" \
  "v_permlane16_swap_b32 v73, v75\n\t" \
  "s_waitcnt lgkmcnt(2)\n\t" \
  "v_smfmac_f32_16x16x64_bf16 v[36:39], v[56:59], v[16:23], v106\n\t" \
  "s_waitcnt lgkmcnt(0)\n\t" \
  "v_smfmac_f32_16x16x64_bf16 v[36:39], v[60:63], v[24:31], v106\n\t" \
  "buffer_store_dwordx4 v[72:75], v108, %[ru], s77 offen sc1\n\t" \
  "v_cvt_pk_bf16_f32 v76, v32, v33\n\t" \
  "v_cvt_pk_bf16_f32 v77, v34, v35\n\t" \
  "s_nop 5\n\t" \
  "v_cvt_pk_bf16_f32 v78, v36, v37\n\t" \
  "v_cvt_pk_bf16_f32 v79, v38, v39\n\t" \
  "s_nop 0\n\t" \
  "v_permlane16_swap_b32 v76, v78\n\t" \
  "v_permlane16_swap_b32 v77, v79\n\t" \
  "buffer_store_dwordx4 v[76:79], v108, %[ru], s78 offen sc1\n\t" \
  "s_memtime s[76:77]\n\t" \
  "s_mov_b32 s42, 0\n\t" \
  "s_waitcnt lgkmcnt(0)\n\t" \
  "s_mov_b32 s57, s76\n\t" \
  "s_add_i32 s41, s56, 1950\n\t" \
  "s_sub_i32 s76, s92, s51\n\t" \
  "s_sub_i32 s77, s93, s53\n\t" \
  "s_cmp_eq_u32 s76, 0\n\t" \
  "s_cbranch_scc1 21f\n\t" \
  "v_cvt_f32_u32 v117, s76\n\t" \
  "v_cvt_f32_u32 v116, s77\n\t" \
  "v_rcp_f32 v117, v117\n\t" \
  "s_mov_b32 s51, s92\n\t" \
  "v_mul_f32 v117, v117, v116\n\t" \
  "v_max_f32 v117, 0x3f028f5c, v117\n\t" \
  "v_min_f32 v117, 0x3ffeb852, v117\n\t" \
  "v_rcp_f32 v116, v117\n\t" \
  "v_mul_f32 v117, 0x4f000000, v117\n\t" \
  "v_mul_f32 v116, 0x47800000, v116\n\t" \
  "v_cvt_u32_f32 v117, v117\n\t" \
  "v_cvt_u32_f32 v116, v116\n\t" \
  "s_mov_b32 s53, s93\n\t" \
  "v_readfirstlane_b32 s46, v117\n\t" \
  "v_readfirstlane_b32 s52, v116\n\t" \
  "21:\n\t" \
  "s_add_i32 s44, s44, 1\n\t" \
  "s_and_b32 s58, s58, 4\n\t" \
  "s_and_b32 s76, s44, 64\n\t" \
  "s_or_b32 s42, s42, s76\n\t" \
  "s_cbranch_scc0 20b\n\t" \
  "s_branch 99f\n\t" \
  "14:\n\t" \
  "s_sleep 3\n\t" \
  "s_add_u32 s59, s59, 0x10000\n\t" \
  "s_cmp_lt_u32 s59, 0x400000\n\t" \
  "s_cbranch_scc1 13b\n\t" \
  "s_mov_b64 exec, -1\n\t" \
  "s_mov_b32 s42, 16\n\t" \
  "s_branch 99f\n\t" \
  "8:\n\t" \
  "s_bitcmp1_b32 s58, 1\n\t" \
  "s_cbranch_scc1 4b\n\t" \
  "s_bitcmp1_b32 s58, 2\n\t" \
  "s_cbranch_scc1 5b\n\t" \
  "v_max3_u32 v116, v32, v33, v34\n\t" \
  "v_max3_u32 v117, v35, v36, v37\n\t" \
  "v_max3_u32 v116, v116, v38, v39\n\t" \
  "v_max_u32 v116, v116, v117\n\t" \
  "v_cmp_eq_u32 vcc, -1, v116\n\t" \
  "s_cbranch_vccz 5b\n\t" \
  "s_branch 0f\n\t" \
  "9:\n\t" \
  "s_bitcmp1_b32 s58, 2\n\t" \
  "s_cbranch_scc1 6b\n\t" \
  "v_max3_u32 v116, v40, v41, v42\n\t" \
  "v_max3_u32 v117, v43, v44, v45\n\t" \
  "v_max3_u32 v116, v116, v46, v47\n\t" \
  "v_max_u32 v116, v116, v117\n\t" \
  "v_cmp_eq_u32 vcc, -1, v116\n\t" \
  "s_cbranch_vccz 6b\n\t" \
  "s_or_b32 s58, s58, 2\n\t" \
  "0:\n\t" \
  "s_or_b32 s58, s58, 1\n\t" \
  "11:\n\t" \
  "buffer_load_dwordx4 v[32:35], v100, %[rh], s40 offen sc1\n\t" \
  "buffer_load_dwordx4 v[36:39], v100, %[rh], s76 offen sc1\n\t" \
  "buffer_load_dwordx4 v[40:43], v100, %[rh], s77 offen sc1\n\t" \
  "buffer_load_dwordx4 v[44:47], v100, %[rh], s78 offen sc1\n\t" \
  "s_waitcnt lgkmcnt(0)\n\t" \
  "s_branch 10b\n\t" \
  "17:\n\t" \
  "s_add_u32 s59, s59, 1\n\t" \
  "s_bitcmp1_b32 s58, 2\n\t" \
  "s_cbranch_scc1 3b\n\t" \
  "s_waitcnt vmcnt(0)\n\t" \
  "v_max_u32 v116, v32, v33\n\t" \
  "v_max3_u32 v116, v116, v34, v35\n\t" \
  "v_max3_u32 v116, v116, v36, v37\n\t" \
  "v_max3_u32 v116, v116, v38, v39\n\t" \
  "v_max3_u32 v116, v116, v40, v41\n\t" \
  "v_max3_u32 v116, v116, v42, v43\n\t" \
  "v_max3_u32 v116, v116, v44, v45\n\t" \
  "v_max3_u32 v116, v116, v46, v47\n\t" \
  "v_cmp_eq_u32 vcc, -1, v116\n\t" \
  "s_cbranch_vccz 3b\n\t" \
  "s_bitcmp1_b32 s59, 6\n\t" \
  "s_cbranch_scc1 12f\n\t" \
  "buffer_load_dwordx4 v[32:35], v100, %[rh], s40 offen sc1\n\t" \
  "buffer_load_dwordx4 v[36:39], v100, %[rh], s76 offen sc1\n\t" \
  "buffer_load_dwordx4 v[40:43], v100, %[rh], s77 offen sc1\n\t" \
  "buffer_load_dwordx4 v[44:47], v100, %[rh], s78 offen sc1\n\t" \
  "s_branch 17b\n\t" \
  "12:\n\t" \
  "s_mov_b32 s42, s58\n\t" \
  "s_branch 99f\n\t" \
  "93:\n\t" \
  "s_mov_b32 s58, s42\n\t" \
  "20:\n\t" \
  "s_and_b32 s76, s44, 7\n\t" \
  "s_lshl_b32 s76, s76, 15\n\t" \
  "s_add_i32 s40, s54, s76\n\t" \
  "s_and_b32 s76, s44, 3\n\t" \
  "s_lshl_b32 s45, s76, 14\n\t" \
  "s_lshl_b32 s47, s76, 20\n\t" \
  "s_add_i32 s47, s47, s55\n\t" \
  "s_lshr_b32 s76, s47, 21\n\t" \
  "s_bitcmp1_b64 %[sel], s76\n\t" \
  "s_cselect_b32 s76, 0x2000, 0\n\t" \
  "s_add_i32 s47, s47, s76\n\t" \
  "s_add_i32 s49, s44, -1\n\t" \
  "s_and_b32 s48, s49, 2\n\t" \
  "s_lshl_b32 s48, s48, 13\n\t" \
  "s_and_b32 s76, s49, 1\n\t" \
  "s_lshl_b32 s76, s76, 12\n\t" \
  "s_add_i32 s48, s48, s76\n\t" \
  "s_lshl_b32 s50, s53, 8\n\t" \
  "s_mov_b32 s59, 0\n\t" \
  "s_mov_b32 s92, s51\n\t" \
  "s_mov_b32 s93, s53\n\t" \
  "s_xor_b32 s76, s40, 0x400\n\t" \
  "s_xor_b32 s77, s40, 0x800\n\t" \
  "s_xor_b32 s78, s40, 0xc00\n\t" \
  "s_cmp_lg_u32 s58, 0\n\t" \
  "s_cbranch_scc1 11f\n\t" \
  "10:\n\t" \
  "ds_read_b128 v[48:51], v102 offset:49152\n\t" \
  "ds_read_b128 v[52:55], v102 offset:50176\n\t" \
  "ds_read_b128 v[56:59], v102 offset:51200\n\t" \
  "ds_read_b128 v[60:63], v102 offset:52224\n\t" \
  "ds_read_b128 v[0:3], v102 offset:0\n\t" \
  "ds_read_b128 v[4:7], v102 offset:1024\n\t" \
  "ds_read_b128 v[8:11], v102 offset:2048\n\t" \
  "ds_read_b128 v[12:15], v102 offset:3072\n\t" \
  "ds_read_b128 v[16:19], v102 offset:16384\n\t" \
  "ds_read_b128 v[20:23], v102 offset:17408\n\t" \
  "ds_read_b128 v[24:27], v102 offset:18432\n\t" \
  "ds_read_b128 v[28:31], v102 offset:19456\n\t" \
  "s_waitcnt lgkmcnt(4)\n\t" \
  "ds_read_b128 v[80:83], v102 offset:32768\n\t" \
  "ds_read_b128 v[84:87], v102 offset:33792\n\t" \
  "ds_read_b128 v[88:91], v102 offset:34816\n\t" \
  "ds_read_b128 v[92:95], v102 offset:35840\n\t" \
  "46:\n\t" \
  "s_bitcmp1_b32 s58, 1\n\t" \
  "s_cselect_b64 s[80:81], -1, 0\n\t" \
  "s_cmp_lg_u32 s58, 0\n\t" \
  "s_cbranch_scc1 17f\n\t" \
  "s_mov_b64 exec, 0xff\n\t" \
  "16:\n\t" \
  "s_memtime vcc\n\t" \
  "s_waitcnt lgkmcnt(0)\n\t" \
  "s_sub_i32 s79, s41, vcc_lo\n\t" \
  "s_cmp_gt_i32 s79, 64\n\t" \
  "s_cbranch_scc1 16b\n\t" \
  "13:\n\t" \
  "buffer_load_dword v116, v112, %[rh], s48 offen sc1\n\t" \
  "s_memtime vcc\n\t" \
  "s_memrealtime s[82:83]\n\t" \
  "s_nop 4\n\t" \
  "s_memrealtime s[84:85]\n\t" \
  "s_nop 4\n\t" \
  "s_memrealtime s[56:57]\n\t" \
  "s_nop 4\n\t" \
  "s_memrealtime s[86:87]\n\t" \
  "s_nop 4\n\t" \
  "s_memrealtime s[88:89]\n\t" \
  "s_nop 4\n\t" \
  "s_memrealtime s[90:91]\n\t" \
  "s_mov_b32 s92, 0\n\t" \
  "s_waitcnt lgkmcnt(0)\n\t" \
  "s_cmp_eq_u32 s82, s84\n\t" \
  "s_addc_u32 s92, s92, 0\n\t" \
  "s_cmp_eq_u32 s82, s56\n\t" \
  "s_addc_u32 s92, s92, 0\n\t" \
  "s_cmp_eq_u32 s82, s86\n\t" \
  "s_addc_u32 s92, s92, 0\n\t" \
  "s_cmp_eq_u32 s82, s88\n\t" \
  "s_addc_u32 s92, s92, 0\n\t" \
  "s_cmp_eq_u32 s82, s90\n\t" \
  "s_addc_u32 s92, s92, 0\n\t" \
  "s_mul_i32 s92, s92, 24\n\t" \
  "s_add_i32 s92, s92, vcc_lo\n\t" \
  "s_lshl4_add_u32 s93, s82, 64\n\t" \
  "s_lshl_b32 s50, s93, 8\n\t" \
  "s_waitcnt vmcnt(0)\n\t" \
  "15:\n\t" \
  "v_mov_b32 v117, 20000\n\t" \
  "v_cmp_ne_u32_sdwa vcc, v116, s49 src0_sel:BYTE_0 src1_sel:DWORD\n\t" \
  "v_subrev_u32 v116, s50, v116\n\t" \
  "v_ashrrev_i32 v116, 8, v116\n\t" \
  "s_nop 1\n\t" \
  "v_max_i32_dpp v116, v116, v116 quad_perm:[1,0,3,2] row_mask:0xf bank_mask:0xf\n\t" \
  "s_cbranch_vccnz 14f\n\t" \
  "s_nop 0\n\t" \
  "v_max_i32_dpp v116, v116, v116 quad_perm:[2,3,0,1] row_mask:0xf bank_mask:0xf\n\t" \
  "s_nop 1\n\t" \
  "v_max_i32_dpp v116, v116, v116 row_half_mirror row_mask:0xf bank_mask:0xf\n\t" \
  "v_med3_i32 v116, v116, 0, v117\n\t" \
  "v_mul_u32_u24 v116, s52, v116\n\t" \
  "v_lshrrev_b32 v116, 16, v116\n\t" \
  "s_mov_b64 exec, -1\n\t" \
  "v_readfirstlane_b32 s84, v116\n\t" \
  "s_add_i32 s84, s84, s92\n\t" \
  ".p2align 6\n\t" \
  "s_nop 0\n\t" \
  "s_nop 0\n\t" \
  "s_nop 0\n\t" \
  "s_nop 0\n\t" \
  "s_nop 0\n\t" \
  "s_nop 0\n\t" \
  "s_nop 0\n\t" \
  "s_nop 0\n\t" \
  "1:\n\t" \
  "s_memtime vcc\n\t" \
  "s_waitcnt lgkmcnt(0)\n\t" \
  "s_sub_i32 s79, s84, vcc_lo\n\t" \
  "s_cmp_gt_i32 s79, 128\n\t" \
  "s_cbranch_scc1 1b\n\t" \
  ".p2align 4\n\t" \
  "2:\n\t" \
  "s_sub_i32 s79, s79, 33\n\t" \
  "s_cmp_gt_i32 s79, 56\n\t" \
  "s_cbranch_scc1 2b\n\t" \
  "buffer_load_dwordx4 v[32:35], v100, %[rh], s40 offen sc1\n\t" \
  "s_nop 12\n\t" \
  "buffer_load_dwordx4 v[36:39], v100, %[rh], s76 offen sc1\n\t" \
  "s_nop 12\n\t" \
  "buffer_load_dwordx4 v[40:43], v100, %[rh], s77 offen sc1\n\t" \
  "s_nop 12\n\t" \
  "buffer_load_dwordx4 v[44:47], v100, %[rh], s78 offen sc1\n\t" \
  "3:\n\t" \
  "v_mov_b64 v[72:73], 0\n\t" \
  "v_mov_b64 v[74:75], 0\n\t" \
  "v_mov_b64 v[76:77], 0\n\t" \
  "v_mov_b64 v[78:79], 0\n\t" \
  "s_waitcnt lgkmcnt(0)\n\t" \
  "s_waitcnt vmcnt(3)\n\t" \
  "v_smfmac_f32_16x16x64_bf16 v[72:75], v[32:35], v[48:55], v104\n\t" \
  "ds_write_b128 v101, v[32:35] offset:12288\n\t" \
  "v_smfmac_f32_16x16x64_bf16 v[76:79], v[32:35], v[56:63], v104\n\t" \
  "s_waitcnt vmcnt(2)\n\t" \
  "ds_write_b128 v101, v[36:39] offset:13312\n\t" \
  "v_smfmac_f32_16x16x64_bf16 v[72:75], v[36:39], a[144:151], v104\n\t" \
  "v_smfmac_f32_16x16x64_bf16 v[76:79], v[36:39], a[152:159], v104\n\t" \
  "s_nop 3\n\t" \
  "v_cmp_u_f32 vcc, v72, v73\n\t" \
  "v_cmp_u_f32 s[82:83], v74, v75\n\t" \
  "s_waitcnt vmcnt(1)\n\t" \
  "v_smfmac_f32_16x16x64_bf16 v[72:75], v[40:43], a[160:167], v104\n\t" \
  "s_or_b64 vcc, vcc, s[82:83]\n\t" \
  "s_or_b64 vcc, vcc, s[80:81]\n\t" \
  "v_smfmac_f32_16x16x64_bf16 v[76:79], v[40:43], a[168:175], v104\n\t" \
  "s_waitcnt lgkmcnt(0)\n\t" \
  "s_cbranch_scc1 8f\n\t" \
  "5:\n\t" \
  "s_barrier\n\t" \
  "4:\n\t" \
  "s_memtime s[56:57]\n\t" \
  "ds_read_b128 v[48:51], v101 offset:0\n\t" \
  "ds_read_b128 v[52:55], v101 offset:1024\n\t" \
  "ds_read_b128 v[56:59], v101 offset:5120\n\t" \
  "s_waitcnt vmcnt(0)\n\t" \
  "v_smfmac_f32_16x16x64_bf16 v[72:75], v[44:47], a[176:183], v104\n\t" \
  "ds_read_b128 v[60:63], v101 offset:4096\n\t" \
  "ds_read_b128 v[64:67], v101 offset:9216\n\t" \
  "v_smfmac_f32_16x16x64_bf16 v[76:79], v[44:47], a[184:191], v104\n\t" \
  "ds_read_b128 v[68:71], v101 offset:8192\n\t" \
  "ds_write_b128 v101, v[40:43] offset:14336\n\t" \
  "s_nop 1\n\t" \
  "v_cmp_u_f32 vcc, v72, v73\n\t" \
  "v_cmp_u_f32 s[82:83], v74, v75\n\t" \
  "s_waitcnt lgkmcnt(6)\n\t" \
  "v_smfmac_f32_16x16x64_bf16 v[72:75], v[48:51], v[0:7], v104\n\t" \
  "s_or_b64 vcc, vcc, s[82:83]\n\t" \
  "v_smfmac_f32_16x16x64_bf16 v[76:79], v[48:51], v[8:15], v104\n\t" \
  "s_cbranch_scc1 9f\n\t" \
  "6:\n\t" \
  "s_waitcnt lgkmcnt(5)\n\t" \
  "v_smfmac_f32_16x16x64_bf16 v[72:75], v[52:55], a[0:7], v104\n\t" \
  "ds_write_b128 v101, v[44:47] offset:15360\n\t" \
  "v_smfmac_f32_16x16x64_bf16 v[76:79], v[52:55], a[8:15], v104\n\t" \
  "v_mov_b64 v[40:41], 0\n\t" \
  "s_waitcnt lgkmcnt(5)\n\t" \
  "v_smfmac_f32_16x16x64_bf16 v[72:75], v[56:59], a[48:55], v104\n\t" \
  "v_mov_b64 v[42:43], 0\n\t" \
  "v_smfmac_f32_16x16x64_bf16 v[76:79], v[56:59], a[56:63], v104\n\t" \
  "s_waitcnt lgkmcnt(4)\n\t" \
  "v_smfmac_f32_16x16x64_bf16 v[72:75], v[60:63], v[16:23], v104\n\t" \
  "s_waitcnt lgkmcnt(0)\n\t" \
  "s_barrier\n\t" \
  "v_smfmac_f32_16x16x64_bf16 v[76:79], v[60:63], v[24:31], v104\n\t" \
  "ds_read_b128 v[48:51], v101 offset:2048\n\t" \
  "ds_read_b128 v[52:55], v101 offset:6144\n\t" \
  "v_smfmac_f32_16x16x64_bf16 v[72:75], v[64:67], a[96:103], v104\n\t" \
  "ds_read_b128 v[56:59], v101 offset:10240\n\t" \
  "ds_read_b128 v[60:63], v101 offset:3072\n\t" \
  "v_smfmac_f32_16x16x64_bf16 v[76:79], v[64:67], a[104:111], v104\n\t" \
  "ds_read_b128 v[32:35], v101 offset:7168\n\t" \
  "ds_read_b128 v[36:39], v101 offset:11264\n\t" \
  "v_smfmac_f32_16x16x64_bf16 v[72:75], v[68:71], v[80:87], v104\n\t" \
  "ds_read_b128 v[0:3], v103 offset:0\n\t" \
  "v_mov_b64 v[44:45], 0\n\t" \
  "v_smfmac_f32_16x16x64_bf16 v[76:79], v[68:71], v[88:95], v104\n\t" \
  "v_mov_b64 v[46:47], 0\n\t" \
  "s_waitcnt lgkmcnt(5)\n\t" \
  "v_smfmac_f32_16x16x64_bf16 v[72:75], v[48:51], a[16:23], v104\n\t" \
  "ds_read_b128 v[4:7], v103 offset:1024\n\t" \
  "v_mov_b64 v[80:81], 0\n\t" \
  "v_smfmac_f32_16x16x64_bf16 v[72:75], v[52:55], a[64:71], v104\n\t" \
  "v_mov_b64 v[82:83], 0\n\t" \
  "s_waitcnt lgkmcnt(2)\n\t" \
  "v_smfmac_f32_16x16x64_bf16 v[72:75], v[56:59], a[112:119], v104\n\t" \
  "ds_read_b128 v[8:11], v103 offset:2048\n\t" \
  "v_mov_b64 v[84:85], 0\n\t" \
  "v_smfmac_f32_16x16x64_bf16 v[72:75], v[60:63], a[32:39], v104\n\t" \
  "ds_read_b128 v[12:15], v103 offset:3072\n\t" \
  "v_mov_b64 v[86:87], 0\n\t" \
  "v_smfmac_f32_16x16x64_bf16 v[72:75], v[32:35], a[80:87], v104\n\t" \
  "ds_read_b128 v[16:19], v103 offset:16384\n\t" \
  "v_mov_b64 v[88:89], 0\n\t" \
  "v_smfmac_f32_16x16x64_bf16 v[72:75], v[36:39], a[128:135], v104\n\t" \
  "ds_read_b128 v[20:23], v103 offset:17408\n\t" \
  "v_mov_b64 v[90:91], 0\n\t" \
  "v_smfmac_f32_16x16x64_bf16 v[76:79], v[48:51], a[24:31], v104\n\t" \
  "ds_read_b128 v[24:27], v103 offset:18432\n\t" \
  "v_mov_b64 v[92:93], 0\n\t" \
  "v_smfmac_f32_16x16x64_bf16 v[76:79], v[52:55], a[72:79], v104\n\t" \
  "v_add_f32 v96, v72, v74\n\t" \
  "v_add_f32 v97, v73, v75\n\t" \
  "v_cvt_pk_bf16_f32 v96, v96, v97\n\t" \
  "ds_write_b32 v105, v96\n\t" \
  "v_smfmac_f32_16x16x64_bf16 v[76:79], v[56:59], a[120:127], v104\n\t" \
  "ds_read_b128 v[28:31], v103 offset:19456\n\t" \
  "v_mov_b64 v[94:95], 0\n\t" \
  "v_smfmac_f32_16x16x64_bf16 v[76:79], v[60:63], a[40:47], v104\n\t" \
  "v_smfmac_f32_16x16x64_bf16 v[76:79], v[32:35], a[88:95], v104\n\t" \
  "v_smfmac_f32_16x16x64_bf16 v[76:79], v[36:39], a[136:143], v104\n\t" \
  "s_waitcnt lgkmcnt(1)\n\t" \
  "s_barrier\n\t" \
  "ds_read_b64_tr_b16 v[96:97], v107 offset:0\n\t" \
  "ds_read_b64_tr_b16 v[98:99], v107 offset:64\n\t" \
  "ds_read_b64_tr_b16 v[56:57], v107 offset:1024\n\t" \
  "ds_read_b64_tr_b16 v[58:59], v107 offset:1088\n\t" \
  "s_nop 1\n\t" \
  "v_add_f32 v116, v76, v78\n\t" \
  "v_add_f32 v117, v77, v79\n\t" \
  "v_cvt_pk_bf16_f32 v116, v116, v117\n\t" \
  "ds_write_b32 v105, v116 offset:256\n\t" \
  "s_waitcnt lgkmcnt(3)\n\t" \
  "v_smfmac_f32_16x16x64_bf16 v[40:43], v[96:99], a[192:199], v106\n\t" \
  "v_smfmac_f32_16x16x64_bf16 v[44:47], v[96:99], a[224:231], v106\n\t" \
  "s_waitcnt lgkmcnt(0)\n\t" \
  "s_barrier\n\t" \
  "ds_read_b64_tr_b16 v[52:53], v107 offset:256\n\t" \
  "ds_read_b64_tr_b16 v[54:55], v107 offset:320\n\t" \
  "v_smfmac_f32_16x16x64_bf16 v[40:43], v[56:59], a[208:215], v106\n\t" \
  "ds_read_b64_tr_b16 v[60:61], v107 offset:1280\n\t" \
  "ds_read_b64_tr_b16 v[62:63], v107 offset:1344\n\t" \
  "v_smfmac_f32_16x16x64_bf16 v[44:47], v[56:59], v[0:7], v106\n\t" \
  "v_smfmac_f32_16x16x64_bf16 v[80:83], v[96:99], a[240:247], v106\n\t" \
  "v_mov_b64 v[32:33], 0\n\t" \
  "v_smfmac_f32_16x16x64_bf16 v[84:87], v[96:99], v[136:143], v106\n\t" \
  "v_mov_b64 v[34:35], 0\n\t" \
  "v_smfmac_f32_16x16x64_bf16 v[80:83], v[56:59], v[120:127], v106\n\t" \
  "v_mov_b64 v[36:37], 0\n\t" \
  "v_smfmac_f32_16x16x64_bf16 v[84:87], v[56:59], v[16:23], v106\n\t" \
  "s_waitcnt lgkmcnt(2)\n\t" \
  "v_smfmac_f32_16x16x64_bf16 v[40:43], v[52:55], a[200:207], v106\n\t" \
  "v_smfmac_f32_16x16x64_bf16 v[44:47], v[52:55], a[232:239], v106\n\t" \
  "s_xor_b32 s76, s47, 0x40000\n\t" \
  "s_waitcnt lgkmcnt(0)\n\t" \
  "v_smfmac_f32_16x16x64_bf16 v[40:43], v[60:63], a[216:223], v106\n\t" \
  "v_mov_b64 v[38:39], 0\n\t" \
  "s_xor_b32 s77, s47, 0x80000\n\t" \
  "v_smfmac_f32_16x16x64_bf16 v[44:47], v[60:63], v[8:15], v106\n\t" \
  "s_xor_b32 s78, s47, 0xc0000\n\t" \
  "v_smfmac_f32_16x16x64_bf16 v[80:83], v[52:55], a[248:255], v106\n\t" \
  "ds_read_b128 v[0:3], v103 offset:32768\n\t" \
  "v_cvt_pk_bf16_f32 v64, v40, v41\n\t" \
  "v_smfmac_f32_16x16x64_bf16 v[84:87], v[52:55], v[144:151], v106\n\t" \
  "ds_read_b128 v[4:7], v103 offset:33792\n\t" \
  "v_cvt_pk_bf16_f32 v65, v42, v43\n\t" \
  "v_smfmac_f32_16x16x64_bf16 v[80:83], v[60:63], v[128:135], v106\n\t" \
  "ds_read_b128 v[8:11], v103 offset:34816\n\t" \
  "v_cvt_pk_bf16_f32 v66, v44, v45\n\t" \
  "v_smfmac_f32_16x16x64_bf16 v[84:87], v[60:63], v[24:31], v106\n\t" \
  "ds_read_b128 v[12:15], v103 offset:35840\n\t" \
  "v_cvt_pk_bf16_f32 v67, v46, v47\n\t" \
  "v_smfmac_f32_16x16x64_bf16 v[88:91], v[96:99], v[152:159], v106\n\t" \
  "v_permlane16_swap_b32 v64, v66\n\t" \
  "v_cvt_pk_bf16_f32 v68, v80, v81\n\t" \
  "v_smfmac_f32_16x16x64_bf16 v[92:95], v[96:99], v[184:191], v106\n\t" \
  "v_permlane16_swap_b32 v65, v67\n\t" \
  "v_cvt_pk_bf16_f32 v69, v82, v83\n\t" \
  "v_smfmac_f32_16x16x64_bf16 v[88:91], v[52:55], v[160:167], v106\n\t" \
  "v_cvt_pk_bf16_f32 v70, v84, v85\n\t" \
  "v_smfmac_f32_16x16x64_bf16 v[92:95], v[52:55], v[192:199], v106\n\t" \
  "v_cvt_pk_bf16_f32 v71, v86, v87\n\t" \
  "v_smfmac_f32_16x16x64_bf16 v[88:91], v[56:59], v[168:175], v106\n\t" \
  "ds_read_b128 v[16:19], v103 offset:49152\n\t" \
  "buffer_store_dwordx4 v[64:67], v108, %[ru], s47 offen sc1\n\t" \
  "v_permlane16_swap_b32 v68, v70\n\t" \
  "s_waitcnt lgkmcnt(3)\n\t" \
  "v_smfmac_f32_16x16x64_bf16 v[92:95], v[56:59], v[0:7], v106\n\t" \
  "ds_read_b128 v[20:23], v103 offset:50176\n\t" \
  "v_permlane16_swap_b32 v69, v71\n\t" \
  "v_smfmac_f32_16x16x64_bf16 v[88:91], v[60:63], v[176:183], v106\n\t" \
  "ds_read_b128 v[24:27], v103 offset:51200\n\t" \
  "s_waitcnt lgkmcnt(3)\n\t" \
  "v_smfmac_f32_16x16x64_bf16 v[92:95], v[60:63], v[8:15], v106\n\t" \
  "ds_read_b128 v[28:31], v103 offset:52224\n\t" \
  "v_smfmac_f32_16x16x64_bf16 v[32:35], v[96:99], v[200:207], v106\n\t" \
  "buffer_store_dwordx4 v[68:71], v108, %[ru], s76 offen sc1\n\t" \
  "v_cvt_pk_bf16_f32 v72, v88, v89\n\t" \
  "v_smfmac_f32_16x16x64_bf16 v[36:39], v[96:99], v[232:239], v106\n\t" \
  "v_cvt_pk_bf16_f32 v73, v90, v91\n\t" \
  "v_smfmac_f32_16x16x64_bf16 v[32:35], v[52:55], v[208:215], v106\n\t" \
  "v_cvt_pk_bf16_f32 v74, v92, v93\n\t" \
  "v_smfmac_f32_16x16x64_bf16 v[36:39], v[52:55], v[240:247], v106\n\t" \
  "v_cvt_pk_bf16_f32 v75, v94, v95\n\t" \
  "v_smfmac_f32_16x16x64_bf16 v[32:35], v[56:59], v[216:223], v106\n\t" \
  "v_permlane16_swap_b32 v72, v74\n\t" \
  "v_smfmac_f32_16x16x64_bf16 v[32:35], v[60:63], v[224:231], v106\n\t" \
  "v_permlane16_swap_b32 v73, v75\n\t" \
  "s_waitcnt lgkmcnt(2)\n\t" \
  "v_smfmac_f32_16x16x64_bf16 v[36:39], v[56:59], v[16:23], v106\n\t" \
  "s_waitcnt lgkmcnt(0)\n\t" \
  "v_smfmac_f32_16x16x64_bf16 v[36:39], v[60:63], v[24:31], v106\n\t" \
  "buffer_store_dwordx4 v[72:75], v108, %[ru], s77 offen sc1\n\t" \
  "v_cvt_pk_bf16_f32 v76, v32, v33\n\t" \
  "v_cvt_pk_bf16_f32 v77, v34, v35\n\t" \
  "s_nop 5\n\t" \
  "v_cvt_pk_bf16_f32 v78, v36, v37\n\t" \
  "v_cvt_pk_bf16_f32 v79, v38, v39\n\t" \
  "s_nop 0\n\t" \
  "v_permlane16_swap_b32 v76, v78\n\t" \
  "v_permlane16_swap_b32 v77, v79\n\t" \
  "buffer_store_dwordx4 v[76:79], v108, %[ru], s78 offen sc1\n\t" \
  "s_memtime s[76:77]\n\t" \
  "s_mov_b32 s42, 0\n\t" \
  "s_waitcnt lgkmcnt(0)\n\t" \
  "s_mov_b32 s57, s76\n\t" \
  "s_add_i32 s41, s56, 1950\n\t" \
  "s_sub_i32 s76, s92, s51\n\t" \
  "s_sub_i32 s77, s93, s53\n\t" \
  "s_cmp_eq_u32 s76, 0\n\t" \
  "s_cbranch_scc1 21f\n\t" \
  "v_cvt_f32_u32 v117, s76\n\t" \
  "v_cvt_f32_u32 v116, s77\n\t" \
  "v_rcp_f32 v117, v117\n\t" \
  "s_mov_b32 s51, s92\n\t" \
  "v_mul_f32 v117, v117, v116\n\t" \
  "v_max_f32 v117, 0x3f028f5c, v117\n\t" \
  "v_min_f32 v117, 0x3ffeb852, v117\n\t" \
  "v_rcp_f32 v116, v117\n\t" \
  "v_mul_f32 v117, 0x4f000000, v117\n\t" \
  "v_mul_f32 v116, 0x47800000, v116\n\t" \
  "v_cvt_u32_f32 v117, v117\n\t" \
  "v_cvt_u32_f32 v116, v116\n\t" \
  "s_mov_b32 s53, s93\n\t" \
  "v_readfirstlane_b32 s46, v117\n\t" \
  "v_readfirstlane_b32 s52, v116\n\t" \
  "21:\n\t" \
  "s_add_i32 s44, s44, 1\n\t" \
  "s_and_b32 s58, s58, 4\n\t" \
  "s_and_b32 s76, s44, 64\n\t" \
  "s_or_b32 s42, s42, s76\n\t" \
  "s_cbranch_scc0 20b\n\t" \
  "s_branch 99f\n\t" \
  "14:\n\t" \
  "s_sleep 3\n\t" \
  "s_add_u32 s59, s59, 0x10000\n\t" \
  "s_cmp_lt_u32 s59, 0x400000\n\t" \
  "s_cbranch_scc1 13b\n\t" \
  "s_mov_b64 exec, -1\n\t" \
  "s_mov_b32 s42, 16\n\t" \
  "s_branch 99f\n\t" \
  "8:\n\t" \
  "s_bitcmp1_b32 s58, 1\n\t" \
  "s_cbranch_scc1 4b\n\t" \
  "s_bitcmp1_b32 s58, 2\n\t" \
  "s_cbranch_scc1 5b\n\t" \
  "v_max3_u32 v116, v32, v33, v34\n\t" \
  "v_max3_u32 v117, v35, v36, v37\n\t" \
  "v_max3_u32 v116, v116, v38, v39\n\t" \
  "v_max_u32 v116, v116, v117\n\t" \
  "v_cmp_eq_u32 vcc, -1, v116\n\t" \
  "s_cbranch_vccz 5b\n\t" \
  "s_branch 0f\n\t" \
  "9:\n\t" \
  "s_bitcmp1_b32 s58, 2\n\t" \
  "s_cbranch_scc1 6b\n\t" \
  "v_max3_u32 v116, v40, v41, v42\n\t" \
  "v_max3_u32 v117, v43, v44, v45\n\t" \
  "v_max3_u32 v116, v116, v46, v47\n\t" \
  "v_max_u32 v116, v116, v117\n\t" \
  "v_cmp_eq_u32 vcc, -1, v116\n\t" \
  "s_cbranch_vccz 6b\n\t" \
  "s_or_b32 s58, s58, 2\n\t" \
  "0:\n\t" \
  "s_or_b32 s58, s58, 1\n\t" \
  "11:\n\t" \
  "buffer_load_dwordx4 v[32:35], v100, %[rh], s40 offen sc1\n\t" \
  "buffer_load_dwordx4 v[36:39], v100, %[rh], s76 offen sc1\n\t" \
  "buffer_load_dwordx4 v[40:43], v100, %[rh], s77 offen sc1\n\t" \
  "buffer_load_dwordx4 v[44:47], v100, %[rh], s78 offen sc1\n\t" \
  "s_waitcnt lgkmcnt(0)\n\t" \
  "s_branch 10b\n\t" \
  "17:\n\t" \
  "s_add_u32 s59, s59, 1\n\t" \
  "s_bitcmp1_b32 s58, 2\n\t" \
  "s_cbranch_scc1 3b\n\t" \
  "s_waitcnt vmcnt(0)\n\t" \
  "v_max_u32 v116, v32, v33\n\t" \
  "v_max3_u32 v116, v116, v34, v35\n\t" \
  "v_max3_u32 v116, v116, v36, v37\n\t" \
  "v_max3_u32 v116, v116, v38, v39\n\t" \
  "v_max3_u32 v116, v116, v40, v41\n\t" \
  "v_max3_u32 v116, v116, v42, v43\n\t" \
  "v_max3_u32 v116, v116, v44, v45\n\t" \
  "v_max3_u32 v116, v116, v46, v47\n\t" \
  "v_cmp_eq_u32 vcc, -1, v116\n\t" \
  "s_cbranch_vccz 3b\n\t" \
  "s_bitcmp1_b32 s59, 6\n\t" \
  "s_cbranch_scc1 12f\n\t" \
  "buffer_load_dwordx4 v[32:35], v100, %[rh], s40 offen sc1\n\t" \
  "buffer_load_dwordx4 v[36:39], v100, %[rh], s76 offen sc1\n\t" \
  "buffer_load_dwordx4 v[40:43], v100, %[rh], s77 offen sc1\n\t" \
  "buffer_load_dwordx4 v[44:47], v100, %[rh], s78 offen sc1\n\t" \
  "s_branch 17b\n\t" \
  "12:\n\t" \
  "s_mov_b32 s42, s58\n\t" \
  "s_branch 99f\n\t" \
  "99:\n\t" \
  "s_waitcnt vmcnt(0) lgkmcnt(0)\n\t" \
  ".Lchain_last:\n\t" \
  ""
#define CB_BODY \
  "s_cmp_gt_u32 %[wv], 1\n\t" \
  "s_cbranch_scc1 91f\n\t" \
  "s_mov_b32 s58, s42\n\t" \
  "s_mov_b32 s59, 0\n\t" \
  "s_bitcmp1_b32 s58, 4\n\t" \
  "s_cbranch_scc1 26f\n\t" \
  "s_bitcmp1_b32 s58, 3\n\t" \
  "s_cbranch_scc1 34f\n\t" \
  "s_bitcmp1_b32 s58, 1\n\t" \
  "s_cbranch_scc1 18f\n\t" \
  "17:\n\t" \
  "19:\n\t" \
  "s_add_i32 s85, s40, -1\n\t" \
  "s_and_b32 s82, s85, 2\n\t" \
  "s_lshl_b32 s82, s82, 13\n\t" \
  "s_and_b32 s80, s85, 1\n\t" \
  "s_lshl_b32 s80, s80, 12\n\t" \
  "s_add_i32 s82, s82, s80\n\t" \
  "s_add_i32 s82, s82, 0x84000\n\t" \
  "s_mov_b32 exec_hi, 0\n\t" \
  "buffer_load_dword v184, v200, %[rh], s82 offen sc1\n\t" \
  "buffer_load_dword v186, v203, %[ru], 0 offen sc1\n\t" \
  "s_mov_b64 exec, -1\n\t" \
  "20:\n\t" \
  "s_and_b32 s80, s40, 3\n\t" \
  "s_mul_i32 s77, s80, 0xc000\n\t" \
  "s_add_i32 s77, s77, 0x40000\n\t" \
  "s_lshl_b32 s78, s80, 14\n\t" \
  "s_add_i32 s78, s78, 0x70000\n\t" \
  "s_mov_b32 s57, 0\n\t" \
  "s_add_i32 s96, s77, 0x1000\n\t" \
  "s_lshl_b32 s88, s80, 20\n\t" \
  "s_add_i32 s88, s88, s48\n\t" \
  "s_lshr_b32 s80, s88, 21\n\t" \
  "s_bitcmp1_b64 %[sel], s80\n\t" \
  "s_cselect_b32 s80, 0x2000, 0\n\t" \
  "s_add_i32 s88, s88, s80\n\t" \
  "s_xor_b32 s89, s88, 0x800\n\t" \
  "s_xor_b32 s90, s88, 0x1000\n\t" \
  "s_xor_b32 s91, s88, 0x1800\n\t" \
  "s_add_i32 s92, s77, s50\n\t" \
  "s_xor_b32 s93, s92, 0x100\n\t" \
  "s_xor_b32 s94, s92, 0x200\n\t" \
  "s_xor_b32 s95, s92, 0x300\n\t" \
  "s_add_i32 s97, s40, 1\n\t" \
  "s_and_b32 s97, s97, 7\n\t" \
  "s_lshl_b32 s97, s97, 15\n\t" \
  "s_add_i32 s97, s97, s49\n\t" \
  "s_memtime vcc\n\t" \
  "s_memrealtime s[86:87]\n\t" \
  "s_nop 4\n\t" \
  "s_memrealtime s[60:61]\n\t" \
  "s_nop 4\n\t" \
  "s_memrealtime s[62:63]\n\t" \
  "s_nop 4\n\t" \
  "s_memrealtime s[64:65]\n\t" \
  "s_nop 4\n\t" \
  "s_memrealtime s[66:67]\n\t" \
  "s_nop 4\n\t" \
  "s_memrealtime s[68:69]\n\t" \
  "s_mov_b32 s79, 0\n\t" \
  "s_waitcnt lgkmcnt(0)\n\t" \
  "s_cmp_eq_u32 s86, s60\n\t" \
  "s_addc_u32 s79, s79, 0\n\t" \
  "s_cmp_eq_u32 s86, s62\n\t" \
  "s_addc_u32 s79, s79, 0\n\t" \
  "s_cmp_eq_u32 s86, s64\n\t" \
  "s_addc_u32 s79, s79, 0\n\t" \
  "s_cmp_eq_u32 s86, s66\n\t" \
  "s_addc_u32 s79, s79, 0\n\t" \
  "s_cmp_eq_u32 s86, s68\n\t" \
  "s_addc_u32 s79, s79, 0\n\t" \
  "s_mul_i32 s79, s79, 24\n\t" \
  "s_add_i32 s79, s79, vcc_lo\n\t" \
  "s_lshl4_add_u32 s80, s86, 64\n\t" \
  "s_sub_i32 s86, s79, s41\n\t" \
  "s_sub_i32 s87, s80, s43\n\t" \
  "s_cmp_lt_u32 s86, 4000\n\t" \
  "s_cbranch_scc1 21f\n\t" \
  "v_cvt_f32_u32 v165, s86\n\t" \
  "v_cvt_f32_u32 v164, s87\n\t" \
  "v_rcp_f32 v165, v165\n\t" \
  "s_nop 0\n\t" \
  "v_mul_f32 v165, v165, v164\n\t" \
  "v_max_f32 v165, 0x3f028f5c, v165\n\t" \
  "v_min_f32 v165, 0x3ffeb852, v165\n\t" \
  "v_rcp_f32 v164, v165\n\t" \
  "v_mul_f32 v165, 0x4f000000, v165\n\t" \
  "v_mul_f32 v164, 0x47800000, v164\n\t" \
  "v_cvt_u32_f32 v165, v165\n\t" \
  "v_cvt_u32_f32 v164, v164\n\t" \
  "s_nop 0\n\t" \
  "v_readfirstlane_b32 s45, v165\n\t" \
  "v_readfirstlane_b32 s44, v164\n\t" \
  "21:\n\t" \
  "s_mov_b32 s41, s79\n\t" \
  "s_mov_b32 s43, s80\n\t" \
  "s_lshl_b32 s83, s43, 8\n\t" \
  "s_mov_b32 s84, 610\n\t" \
  "s_mov_b32 exec_hi, 0\n\t" \
  "s_waitcnt vmcnt(0)\n\t" \
  "v_cmp_ne_u32_sdwa vcc, v184, s85 src0_sel:BYTE_0 src1_sel:DWORD\n\t" \
  "v_subrev_u32 v184, s83, v184\n\t" \
  "v_ashrrev_i32 v184, 8, v184\n\t" \
  "s_cbranch_vccnz 22f\n\t" \
  "s_nop 0\n\t" \
  "v_max_i32_dpp v184, v184, v184 quad_perm:[1,0,3,2] row_mask:0xf bank_mask:0xf\n\t" \
  "s_nop 1\n\t" \
  "v_max_i32_dpp v184, v184, v184 quad_perm:[2,3,0,1] row_mask:0xf bank_mask:0xf\n\t" \
  "s_nop 1\n\t" \
  "v_max_i32_dpp v184, v184, v184 row_half_mirror row_mask:0xf bank_mask:0xf\n\t" \
  "s_nop 1\n\t" \
  "v_max_i32_dpp v184, v184, v184 row_mirror row_mask:0xf bank_mask:0xf\n\t" \
  "s_nop 0\n\t" \
  "v_readlane_b32 s81, v184, 0\n\t" \
  "v_readlane_b32 s82, v184, 16\n\t" \
  "s_max_i32 s81, s81, s82\n\t" \
  "s_add_i32 s76, s81, s43\n\t" \
  "s_add_i32 s81, s81, s53\n\t" \
  "s_max_i32 s81, s81, 0\n\t" \
  "s_min_i32 s81, s81, 20000\n\t" \
  "s_mul_i32 s81, s81, s44\n\t" \
  "s_lshr_b32 s81, s81, 16\n\t" \
  "s_add_i32 s81, s81, -900\n\t" \
  "24:\n\t" \
  "s_add_i32 s81, s81, s41\n\t" \
  ".p2align 6\n\t" \
  "s_nop 0\n\t" \
  "s_nop 0\n\t" \
  "s_nop 0\n\t" \
  "s_nop 0\n\t" \
  "s_nop 0\n\t" \
  "s_nop 0\n\t" \
  "s_nop 0\n\t" \
  "s_nop 0\n\t" \
  "1:\n\t" \
  "s_memtime vcc\n\t" \
  "s_waitcnt lgkmcnt(0)\n\t" \
  "s_sub_i32 s79, s81, vcc_lo\n\t" \
  "s_cmp_gt_i32 s79, 128\n\t" \
  "s_cbranch_scc1 1b\n\t" \
  ".p2align 4\n\t" \
  "2:\n\t" \
  "s_sub_i32 s79, s79, 33\n\t" \
  "s_cmp_gt_i32 s79, 56\n\t" \
  "s_cbranch_scc1 2b\n\t" \
  "buffer_load_dword v187, v200, %[rh], s78 offen sc1\n\t" \
  "s_memtime vcc\n\t" \
  "s_memrealtime s[86:87]\n\t" \
  "s_nop 4\n\t" \
  "s_memrealtime s[60:61]\n\t" \
  "s_nop 4\n\t" \
  "s_memrealtime s[62:63]\n\t" \
  "s_nop 4\n\t" \
  "s_memrealtime s[64:65]\n\t" \
  "s_nop 4\n\t" \
  "s_memrealtime s[66:67]\n\t" \
  "s_nop 4\n\t" \
  "s_memrealtime s[68:69]\n\t" \
  "s_mov_b32 s79, 0\n\t" \
  "s_waitcnt lgkmcnt(0)\n\t" \
  "s_cmp_eq_u32 s86, s60\n\t" \
  "s_addc_u32 s79, s79, 0\n\t" \
  "s_cmp_eq_u32 s86, s62\n\t" \
  "s_addc_u32 s79, s79, 0\n\t" \
  "s_cmp_eq_u32 s86, s64\n\t" \
  "s_addc_u32 s79, s79, 0\n\t" \
  "s_cmp_eq_u32 s86, s66\n\t" \
  "s_addc_u32 s79, s79, 0\n\t" \
  "s_cmp_eq_u32 s86, s68\n\t" \
  "s_addc_u32 s79, s79, 0\n\t" \
  "s_mul_i32 s79, s79, 24\n\t" \
  "s_add_i32 s79, s79, vcc_lo\n\t" \
  "s_lshl4_add_u32 s80, s86, 64\n\t" \
  "s_mov_b32 s41, s79\n\t" \
  "s_mov_b32 s43, s80\n\t" \
  "s_lshl_b32 s83, s43, 8\n\t" \
  "s_add_i32 s98, s40, 2\n\t" \
  "v_cmp_gt_i32 vcc, s98, v186\n\t" \
  "s_and_b32 s54, s98, 15\n\t" \
  "s_lshl_b32 s54, s54, 22\n\t" \
  "s_add_i32 s54, s54, s51\n\t" \
  "s_lshr_b32 s80, s54, 21\n\t" \
  "s_bitcmp0_b64 %[sel], s80\n\t" \
  "s_cselect_b32 s80, 0x2000, 0\n\t" \
  "s_add_i32 s54, s54, s80\n\t" \
  "s_cmp_eq_u64 vcc, 0\n\t" \
  "s_cselect_b32 s96, 0x70000000, 0\n\t" \
  "s_cselect_b32 s98, 0, s98\n\t" \
  "13:\n\t" \
  "s_waitcnt vmcnt(0)\n\t" \
  "s_memtime s[80:81]\n\t" \
  "v_cmp_ne_u32_sdwa vcc, v187, s40 src0_sel:BYTE_0 src1_sel:DWORD\n\t" \
  "v_subrev_u32 v184, s83, v187\n\t" \
  "v_ashrrev_i32 v184, 8, v184\n\t" \
  "s_nop 1\n\t" \
  "v_max_i32_dpp v184, v184, v184 quad_perm:[1,0,3,2] row_mask:0xf bank_mask:0xf\n\t" \
  "s_nop 1\n\t" \
  "v_max_i32_dpp v184, v184, v184 quad_perm:[2,3,0,1] row_mask:0xf bank_mask:0xf\n\t" \
  "s_cbranch_vccnz 14f\n\t" \
  "15:\n\t" \
  "s_nop 0\n\t" \
  "v_max_i32_dpp v184, v184, v184 row_half_mirror row_mask:0xf bank_mask:0xf\n\t" \
  "s_nop 1\n\t" \
  "v_max_i32_dpp v184, v184, v184 row_mirror row_mask:0xf bank_mask:0xf\n\t" \
  "s_mov_b64 exec, -1\n\t" \
  "v_readlane_b32 s57, v184, 0\n\t" \
  "v_readlane_b32 s82, v184, 16\n\t" \
  "s_waitcnt lgkmcnt(0)\n\t" \
  "s_sub_i32 s79, s41, s80\n\t" \
  "s_max_i32 s57, s57, s82\n\t" \
  "s_add_i32 s81, s57, 212\n\t" \
  "s_max_i32 s81, s81, 0\n\t" \
  "s_min_i32 s81, s81, 20000\n\t" \
  "s_mul_i32 s81, s81, s44\n\t" \
  "s_lshr_b32 s81, s81, 16\n\t" \
  "s_add_i32 s79, s79, s81\n\t" \
  "s_add_i32 s79, s79, -330\n\t" \
  ".p2align 4\n\t" \
  "3:\n\t" \
  "s_sub_i32 s79, s79, 33\n\t" \
  "s_cmp_gt_i32 s79, 56\n\t" \
  "s_cbranch_scc1 3b\n\t" \
  "buffer_load_dwordx4 v[72:75], v192, %[ru], s88 offen offset:0 sc1\n\t" \
  "s_nop 4\n\t" \
  "buffer_load_dwordx4 v[76:79], v192, %[ru], s88 offen offset:1024 sc1\n\t" \
  "s_nop 4\n\t" \
  "s_add_i32 s79, s79, 12\n\t" \
  ".p2align 4\n\t" \
  "4:\n\t" \
  "s_sub_i32 s79, s79, 33\n\t" \
  "s_cmp_gt_i32 s79, 56\n\t" \
  "s_cbranch_scc1 4b\n\t" \
  "buffer_load_dwordx4 v[80:83], v192, %[ru], s89 offen offset:0 sc1\n\t" \
  "s_nop 4\n\t" \
  "buffer_load_dwordx4 v[84:87], v192, %[ru], s89 offen offset:1024 sc1\n\t" \
  "s_nop 4\n\t" \
  "s_add_i32 s79, s79, 22\n\t" \
  ".p2align 4\n\t" \
  "5:\n\t" \
  "s_sub_i32 s79, s79, 33\n\t" \
  "s_cmp_gt_i32 s79, 56\n\t" \
  "s_cbranch_scc1 5b\n\t" \
  "buffer_load_dwordx4 v[88:91], v192, %[ru], s90 offen offset:0 sc1\n\t" \
  "buffer_load_dwordx4 v[92:95], v192, %[ru], s90 offen offset:1024 sc1\n\t" \
  "s_add_i32 s79, s79, 122\n\t" \
  ".p2align 4\n\t" \
  "9:\n\t" \
  "s_sub_i32 s79, s79, 33\n\t" \
  "s_cmp_gt_i32 s79, 56\n\t" \
  "s_cbranch_scc1 9b\n\t" \
  "buffer_load_dwordx4 v[104:107], v194, %[rh], s92 offen sc1\n\t" \
  "s_nop 4\n\t" \
  "buffer_load_dwordx4 v[108:111], v194, %[rh], s93 offen sc1\n\t" \
  "s_nop 4\n\t" \
  "buffer_load_dwordx4 v[112:115], v194, %[rh], s94 offen sc1\n\t" \
  "s_nop 4\n\t" \
  "buffer_load_dwordx4 v[116:119], v194, %[rh], s95 offen sc1\n\t" \
  "s_nop 4\n\t" \
  "buffer_load_dwordx4 v[96:99], v192, %[ru], s91 offen offset:0 sc1\n\t" \
  "s_nop 4\n\t" \
  "buffer_load_dwordx4 v[100:103], v192, %[ru], s91 offen offset:1024 sc1\n\t" \
  "s_nop 4\n\t" \
  "s_mov_b64 exec, 0xffff\n\t" \
  "buffer_load_dword v185, v203, %[ru], s96 offen sc1\n\t" \
  "s_mov_b64 exec, -1\n\t" \
  "10:\n\t" \
  "s_waitcnt vmcnt(6)\n\t" \
  "v_mfma_f32_16x16x32_bf16 v[136:139], v[104:107], v[72:75], 0\n\t" \
  "v_mfma_f32_16x16x32_bf16 v[140:143], v[104:107], v[76:79], 0\n\t" \
  "s_waitcnt vmcnt(5)\n\t" \
  "v_mfma_f32_16x16x32_bf16 v[136:139], v[108:111], v[80:83], v[136:139]\n\t" \
  "v_mfma_f32_16x16x32_bf16 v[140:143], v[108:111], v[84:87], v[140:143]\n\t" \
  "s_waitcnt vmcnt(4)\n\t" \
  "v_mfma_f32_16x16x32_bf16 v[136:139], v[112:115], v[88:91], v[136:139]\n\t" \
  "v_mfma_f32_16x16x32_bf16 v[140:143], v[112:115], v[92:95], v[140:143]\n\t" \
  "s_waitcnt vmcnt(2)\n\t" \
  "v_mfma_f32_16x16x32_bf16 v[136:139], v[116:119], v[96:99], v[136:139]\n\t" \
  "s_waitcnt vmcnt(1)\n\t" \
  "v_mfma_f32_16x16x32_bf16 v[140:143], v[116:119], v[100:103], v[140:143]\n\t" \
  "s_barrier\n\t" \
  "ds_read2st64_b64 v[144:147], v195 offset1:1\n\t" \
  "s_nop 1\n\t" \
  "v_permlane32_swap_b32 v136, v138\n\t" \
  "v_permlane32_swap_b32 v137, v139\n\t" \
  "s_nop 1\n\t" \
  "v_permlane32_swap_b32 v140, v142\n\t" \
  "v_permlane32_swap_b32 v141, v143\n\t" \
  "v_pk_add_f32 v[136:137], v[136:137], v[142:143]\n\t" \
  "s_waitcnt lgkmcnt(0)\n\t" \
  "v_pk_add_f32 v[136:137], v[136:137], v[144:145]\n\t" \
  "v_pk_add_f32 v[136:137], v[136:137], v[146:147]\n\t" \
  "v_cvt_pk_bf16_f32 v148, v136, v137\n\t" \
  "s_nop 1\n\t" \
  "v_mov_b32_dpp v149, v148 row_half_mirror row_mask:0xf bank_mask:0xf\n\t" \
  "v_perm_b32 v150, v148, v149, v205\n\t" \
  "v_cmpx_o_f16_sdwa vcc, v150, v150 src0_sel:WORD_0 src1_sel:WORD_1\n\t" \
  "6:\n\t" \
  "buffer_store_dword v150, v201, %[rh], s97 offen sc1\n\t" \
  "s_memtime s[82:83]\n\t" \
  "s_mov_b64 exec, -1\n\t" \
  "s_cmp_eq_u64 vcc, -1\n\t" \
  "s_cbranch_scc0 8f\n\t" \
  "v_mov_b32 v151, 0\n\t" \
  "5:\n\t" \
  "ds_write_b32 v206, v151\n\t" \
  "s_mov_b64 exec, 0xffff\n\t" \
  "s_waitcnt vmcnt(1)\n\t" \
  "v_cmp_gt_i32 vcc, s98, v185\n\t" \
  "s_mov_b64 exec, -1\n\t" \
  "s_cmp_eq_u64 vcc, 0\n\t" \
  "s_cselect_b32 s54, s54, 0x70000000\n\t" \
  "buffer_load_dwordx4 v[0:3], v210, %[ru], s54 offen offset:0 sc1\n\t" \
  "buffer_load_dwordx4 v[4:7], v210, %[ru], s54 offen offset:1024 sc1\n\t" \
  "s_add_i32 s54, s54, 0x20000\n\t" \
  "buffer_load_dwordx4 v[8:11], v210, %[ru], s54 offen offset:0 sc1\n\t" \
  "buffer_load_dwordx4 v[12:15], v210, %[ru], s54 offen offset:1024 sc1\n\t" \
  "s_add_i32 s54, s54, 0x20000\n\t" \
  "s_waitcnt lgkmcnt(0)\n\t" \
  "s_mov_b32 s56, s82\n\t" \
  "s_barrier\n\t" \
  "ds_read2_b32 v[148:149], v207 offset1:8\n\t" \
  "s_waitcnt lgkmcnt(0)\n\t" \
  "v_or_b32 v148, v148, v149\n\t" \
  "v_cmp_ne_u32 vcc, 0, v148\n\t" \
  "s_cbranch_vccnz 12f\n\t" \
  "s_and_b32 s58, s58, 4\n\t" \
  "s_add_i32 s80, s57, s43\n\t" \
  "s_sub_i32 s80, s80, s76\n\t" \
  "s_add_i32 s53, s53, -12\n\t" \
  "s_max_i32 s80, s80, s53\n\t" \
  "s_add_i32 s53, s53, 24\n\t" \
  "s_min_i32 s53, s53, s80\n\t" \
  "s_cmp_lt_u32 s54, 0x70000000\n\t" \
  "s_cbranch_scc1 48f\n\t" \
  "25:\n\t" \
  "s_add_i32 s82, s40, 2\n\t" \
  "s_min_i32 s83, s82, 63\n\t" \
  "s_mov_b64 exec, 0xffff\n\t" \
  "v_cmp_gt_i32 vcc, s83, v186\n\t" \
  "s_mov_b64 exec, -1\n\t" \
  "s_and_b32 s54, s82, 15\n\t" \
  "s_lshl_b32 s54, s54, 22\n\t" \
  "s_add_i32 s54, s54, s51\n\t" \
  "s_lshr_b32 s80, s54, 21\n\t" \
  "s_bitcmp0_b64 %[sel], s80\n\t" \
  "s_cselect_b32 s80, 0x2000, 0\n\t" \
  "s_add_i32 s54, s54, s80\n\t" \
  "s_cbranch_vccnz 27f\n\t" \
  "28:\n\t" \
  "s_cmp_ge_i32 s40, 62\n\t" \
  "s_cbranch_scc1 37f\n\t" \
  "buffer_load_dwordx4 v[0:3], v210, %[ru], s54 offen offset:0 sc1\n\t" \
  "buffer_load_dwordx4 v[4:7], v210, %[ru], s54 offen offset:1024 sc1\n\t" \
  "s_add_i32 s54, s54, 0x20000\n\t" \
  "buffer_load_dwordx4 v[8:11], v210, %[ru], s54 offen offset:0 sc1\n\t" \
  "buffer_load_dwordx4 v[12:15], v210, %[ru], s54 offen offset:1024 sc1\n\t" \
  "s_add_i32 s54, s54, 0x20000\n\t" \
  "48:\n\t" \
  "s_and_b32 s76, s40, 3\n\t" \
  "s_lshl_b32 s76, s76, 18\n\t" \
  "s_bitcmp1_b32 s58, 4\n\t" \
  "s_cbranch_scc1 29f\n\t" \
  "s_sub_i32 s80, s56, s41\n\t" \
  "s_mul_hi_u32 s80, s80, s45\n\t" \
  "s_lshl_b32 s80, s80, 1\n\t" \
  "s_sub_i32 s80, s80, s57\n\t" \
  "s_min_u32 s47, s47, s46\n\t" \
  "s_min_u32 s47, s47, s80\n\t" \
  "v_mov_b32 v188, s47\n\t" \
  "s_mov_b32 s47, s46\n\t" \
  "s_mov_b32 s46, s80\n\t" \
  "buffer_store_dword v188, v202, %[rh], s52 offen sc1\n\t" \
  "v_mov_b64 v[188:189], -1\n\t" \
  "v_mov_b64 v[190:191], -1\n\t" \
  "v_mov_b32 v187, s40\n\t" \
  "s_add_i32 s80, s40, -1\n\t" \
  "s_and_b32 s80, s80, 7\n\t" \
  "s_lshl_b32 s80, s80, 15\n\t" \
  "s_add_i32 s80, s80, s49\n\t" \
  "buffer_store_dwordx4 v[188:191], v208, %[rh], s80 offen sc1\n\t" \
  "buffer_store_dword v187, v209, %[ru], 0 offen sc1\n\t" \
  "ds_write_b128 v197, v[72:75] offset:16384\n\t" \
  "ds_write_b128 v197, v[76:79] offset:17408\n\t" \
  "ds_write_b128 v197, v[80:83] offset:18432\n\t" \
  "ds_write_b128 v197, v[84:87] offset:19456\n\t" \
  "ds_write_b128 v197, v[88:91] offset:20480\n\t" \
  "ds_write_b128 v197, v[92:95] offset:21504\n\t" \
  "ds_write_b128 v197, v[96:99] offset:22528\n\t" \
  "ds_write_b128 v197, v[100:103] offset:23552\n\t" \
  "29:\n\t" \
  "s_and_b32 s58, s58, 36\n\t" \
  "buffer_load_dwordx4 v[16:19], v210, %[ru], s54 offen offset:0 sc1\n\t" \
  "buffer_load_dwordx4 v[20:23], v210, %[ru], s54 offen offset:1024 sc1\n\t" \
  "s_add_i32 s54, s54, 0x20000\n\t" \
  "buffer_load_dwordx4 v[24:27], v210, %[ru], s54 offen offset:0 sc1\n\t" \
  "buffer_load_dwordx4 v[28:31], v210, %[ru], s54 offen offset:1024 sc1\n\t" \
  "s_add_i32 s54, s54, 0x20000\n\t" \
  "buffer_load_dwordx4 v[32:35], v210, %[ru], s54 offen offset:0 sc1\n\t" \
  "buffer_load_dwordx4 v[36:39], v210, %[ru], s54 offen offset:1024 sc1\n\t" \
  "s_add_i32 s54, s54, 0x20000\n\t" \
  "buffer_load_dwordx4 v[40:43], v210, %[ru], s54 offen offset:0 sc1\n\t" \
  "buffer_load_dwordx4 v[44:47], v210, %[ru], s54 offen offset:1024 sc1\n\t" \
  "s_add_i32 s54, s54, 0x20000\n\t" \
  "buffer_load_dwordx4 a[0:3], v211, %[rh], s76 offen offset:0 sc1\n\t" \
  "buffer_load_dwordx4 a[4:7], v211, %[rh], s76 offen offset:1024 sc1\n\t" \
  "buffer_load_dwordx4 v[48:51], v210, %[ru], s54 offen offset:0 sc1\n\t" \
  "buffer_load_dwordx4 v[52:55], v210, %[ru], s54 offen offset:1024 sc1\n\t" \
  "s_add_i32 s54, s54, 0x20000\n\t" \
  "buffer_load_dwordx4 v[56:59], v210, %[ru], s54 offen offset:0 sc1\n\t" \
  "buffer_load_dwordx4 v[60:63], v210, %[ru], s54 offen offset:1024 sc1\n\t" \
  "s_add_i32 s54, s54, 0x20000\n\t" \
  "buffer_load_dwordx4 a[8:11], v212, %[rh], s76 offen offset:0 sc1\n\t" \
  "buffer_load_dwordx4 a[12:15], v212, %[rh], s76 offen offset:1024 sc1\n\t" \
  "s_waitcnt lgkmcnt(0)\n\t" \
  "s_barrier\n\t" \
  "buffer_load_dwordx4 v[64:67], v210, %[ru], s54 offen offset:0 sc1\n\t" \
  "buffer_load_dwordx4 v[68:71], v210, %[ru], s54 offen offset:1024 sc1\n\t" \
  "s_add_i32 s54, s54, 0x20000\n\t" \
  "buffer_load_dwordx4 v[104:107], v210, %[ru], s54 offen offset:0 sc1\n\t" \
  "buffer_load_dwordx4 v[108:111], v210, %[ru], s54 offen offset:1024 sc1\n\t" \
  "s_add_i32 s54, s54, 0x20000\n\t" \
  "buffer_load_dwordx4 a[16:19], v213, %[rh], s76 offen offset:0 sc1\n\t" \
  "buffer_load_dwordx4 a[20:23], v213, %[rh], s76 offen offset:1024 sc1\n\t" \
  "buffer_load_dwordx4 v[112:115], v210, %[ru], s54 offen offset:0 sc1\n\t" \
  "buffer_load_dwordx4 v[116:119], v210, %[ru], s54 offen offset:1024 sc1\n\t" \
  "s_add_i32 s54, s54, 0x20000\n\t" \
  "buffer_load_dwordx4 v[120:123], v210, %[ru], s54 offen offset:0 sc1\n\t" \
  "buffer_load_dwordx4 v[124:127], v210, %[ru], s54 offen offset:1024 sc1\n\t" \
  "s_add_i32 s54, s54, 0x20000\n\t" \
  "buffer_load_dwordx4 a[24:27], v214, %[rh], s76 offen offset:0 sc1\n\t" \
  "buffer_load_dwordx4 a[28:31], v214, %[rh], s76 offen offset:1024 sc1\n\t" \
  "ds_read_b128 v[160:163], v193 offset:0\n\t" \
  "ds_read_b128 v[164:167], v193 offset:256\n\t" \
  "ds_read_b128 v[168:171], v193 offset:2048\n\t" \
  "ds_read_b128 v[172:175], v193 offset:2304\n\t" \
  "ds_read_b128 v[176:179], v193 offset:4096\n\t" \
  "ds_read_b128 v[180:183], v193 offset:4352\n\t" \
  "s_waitcnt lgkmcnt(2)\n\t" \
  "v_pk_add_f32 v[160:161], v[160:161], v[168:169]\n\t" \
  "v_pk_add_f32 v[162:163], v[162:163], v[170:171]\n\t" \
  "v_pk_add_f32 v[164:165], v[164:165], v[172:173]\n\t" \
  "v_pk_add_f32 v[166:167], v[166:167], v[174:175]\n\t" \
  "s_waitcnt lgkmcnt(0)\n\t" \
  "v_pk_add_f32 v[160:161], v[160:161], v[176:177]\n\t" \
  "v_pk_add_f32 v[162:163], v[162:163], v[178:179]\n\t" \
  "v_pk_add_f32 v[164:165], v[164:165], v[180:181]\n\t" \
  "v_pk_add_f32 v[166:167], v[166:167], v[182:183]\n\t" \
  "v_cvt_pk_bf16_f32 v160, v160, v161\n\t" \
  "v_cvt_pk_bf16_f32 v161, v162, v163\n\t" \
  "v_cvt_pk_bf16_f32 v162, v164, v165\n\t" \
  "v_cvt_pk_bf16_f32 v163, v166, v167\n\t" \
  "buffer_load_dwordx4 v[128:131], v210, %[ru], s54 offen offset:0 sc1\n\t" \
  "buffer_load_dwordx4 v[132:135], v210, %[ru], s54 offen offset:1024 sc1\n\t" \
  "s_add_i32 s54, s54, 0x20000\n\t" \
  "buffer_load_dwordx4 v[136:139], v210, %[ru], s54 offen offset:0 sc1\n\t" \
  "buffer_load_dwordx4 v[140:143], v210, %[ru], s54 offen offset:1024 sc1\n\t" \
  "s_add_i32 s54, s54, 0x20000\n\t" \
  "s_waitcnt vmcnt(32)\n\t" \
  "v_swap_b32 v1, v8\n\t" \
  "v_swap_b32 v3, v10\n\t" \
  "v_swap_b32 v5, v12\n\t" \
  "v_swap_b32 v7, v14\n\t" \
  "ds_write_b128 v204, v[0:3] offset:0\n\t" \
  "ds_write_b128 v204, v[8:11] offset:1024\n\t" \
  "ds_write_b128 v204, v[4:7] offset:2048\n\t" \
  "ds_write_b128 v204, v[12:15] offset:3072\n\t" \
  "buffer_load_dwordx4 v[144:147], v210, %[ru], s54 offen offset:0 sc1\n\t" \
  "buffer_load_dwordx4 v[148:151], v210, %[ru], s54 offen offset:1024 sc1\n\t" \
  "s_add_i32 s54, s54, 0x20000\n\t" \
  "buffer_load_dwordx4 v[152:155], v210, %[ru], s54 offen offset:0 sc1\n\t" \
  "buffer_load_dwordx4 v[156:159], v210, %[ru], s54 offen offset:1024 sc1\n\t" \
  "s_add_i32 s54, s54, 0x20000\n\t" \
  "s_waitcnt vmcnt(32)\n\t" \
  "v_swap_b32 v17, v24\n\t" \
  "v_swap_b32 v19, v26\n\t" \
  "v_swap_b32 v21, v28\n\t" \
  "v_swap_b32 v23, v30\n\t" \
  "ds_write_b128 v204, v[16:19] offset:4096\n\t" \
  "ds_write_b128 v204, v[24:27] offset:5120\n\t" \
  "ds_write_b128 v204, v[20:23] offset:6144\n\t" \
  "ds_write_b128 v204, v[28:31] offset:7168\n\t" \
  "s_waitcnt vmcnt(28)\n\t" \
  "v_swap_b32 v33, v40\n\t" \
  "v_swap_b32 v35, v42\n\t" \
  "v_swap_b32 v37, v44\n\t" \
  "v_swap_b32 v39, v46\n\t" \
  "ds_write_b128 v204, v[32:35] offset:8192\n\t" \
  "ds_write_b128 v204, v[40:43] offset:9216\n\t" \
  "ds_write_b128 v204, v[36:39] offset:10240\n\t" \
  "ds_write_b128 v204, v[44:47] offset:11264\n\t" \
  "s_waitcnt vmcnt(22)\n\t" \
  "v_swap_b32 v49, v56\n\t" \
  "v_swap_b32 v51, v58\n\t" \
  "v_swap_b32 v53, v60\n\t" \
  "v_swap_b32 v55, v62\n\t" \
  "ds_write_b128 v204, v[48:51] offset:12288\n\t" \
  "ds_write_b128 v204, v[56:59] offset:13312\n\t" \
  "ds_write_b128 v204, v[52:55] offset:14336\n\t" \
  "ds_write_b128 v204, v[60:63] offset:15360\n\t" \
  "s_waitcnt vmcnt(16)\n\t" \
  "v_swap_b32 v65, v104\n\t" \
  "v_swap_b32 v67, v106\n\t" \
  "v_swap_b32 v69, v108\n\t" \
  "v_swap_b32 v71, v110\n\t" \
  "ds_write_b128 v204, v[64:67] offset:16384\n\t" \
  "ds_write_b128 v204, v[104:107] offset:17408\n\t" \
  "ds_write_b128 v204, v[68:71] offset:18432\n\t" \
  "ds_write_b128 v204, v[108:111] offset:19456\n\t" \
  "s_waitcnt vmcnt(10)\n\t" \
  "v_swap_b32 v113, v120\n\t" \
  "v_swap_b32 v115, v122\n\t" \
  "v_swap_b32 v117, v124\n\t" \
  "v_swap_b32 v119, v126\n\t" \
  "ds_write_b128 v204, v[112:115] offset:20480\n\t" \
  "ds_write_b128 v204, v[120:123] offset:21504\n\t" \
  "ds_write_b128 v204, v[116:119] offset:22528\n\t" \
  "ds_write_b128 v204, v[124:127] offset:23552\n\t" \
  "s_waitcnt vmcnt(4)\n\t" \
  "v_swap_b32 v129, v136\n\t" \
  "v_swap_b32 v131, v138\n\t" \
  "v_swap_b32 v133, v140\n\t" \
  "v_swap_b32 v135, v142\n\t" \
  "ds_write_b128 v204, v[128:131] offset:24576\n\t" \
  "ds_write_b128 v204, v[136:139] offset:25600\n\t" \
  "ds_write_b128 v204, v[132:135] offset:26624\n\t" \
  "ds_write_b128 v204, v[140:143] offset:27648\n\t" \
  "s_waitcnt vmcnt(0)\n\t" \
  "v_swap_b32 v145, v152\n\t" \
  "v_swap_b32 v147, v154\n\t" \
  "v_swap_b32 v149, v156\n\t" \
  "v_swap_b32 v151, v158\n\t" \
  "ds_write_b128 v204, v[144:147] offset:28672\n\t" \
  "ds_write_b128 v204, v[152:155] offset:29696\n\t" \
  "ds_write_b128 v204, v[148:151] offset:30720\n\t" \
  "ds_write_b128 v204, v[156:159] offset:31744\n\t" \
  "s_waitcnt lgkmcnt(0)\n\t" \
  "s_cmp_lt_i32 s40, 0\n\t" \
  "s_cbranch_scc1 39f\n\t" \
  "38:\n\t" \
  "s_add_i32 s80, s40, -2\n\t" \
  "s_lshl_b32 s80, s80, 17\n\t" \
  "s_min_u32 s80, s80, 0x70000000\n\t" \
  "buffer_store_dwordx4 v[160:163], v196, %[ro], s80 offen sc1\n\t" \
  "ds_read_b128 a[32:35], v198 offset:0\n\t" \
  "ds_read_b128 a[36:39], v198 offset:1024\n\t" \
  "ds_read_b128 a[40:43], v198 offset:2048\n\t" \
  "ds_read_b128 a[44:47], v198 offset:3072\n\t" \
  "ds_read_b128 a[48:51], v198 offset:4096\n\t" \
  "ds_read_b128 a[52:55], v198 offset:5120\n\t" \
  "ds_read_b128 a[56:59], v198 offset:6144\n\t" \
  "ds_read_b128 a[60:63], v198 offset:7168\n\t" \
  "ds_read_b128 a[64:67], v198 offset:8192\n\t" \
  "ds_read_b128 a[68:71], v198 offset:9216\n\t" \
  "ds_read_b128 a[72:75], v198 offset:10240\n\t" \
  "ds_read_b128 a[76:79], v198 offset:11264\n\t" \
  "ds_read_b128 a[80:83], v198 offset:12288\n\t" \
  "ds_read_b128 a[84:87], v198 offset:13312\n\t" \
  "ds_read_b128 a[88:91], v198 offset:14336\n\t" \
  "ds_read_b128 a[92:95], v198 offset:15360\n\t" \
  "s_waitcnt vmcnt(1) lgkmcnt(0)\n\t" \
  "31:\n\t" \
  "v_mfma_f32_16x16x32_bf16 v[168:171], a[32:35], a[0:3], 0\n\t" \
  "v_mfma_f32_16x16x32_bf16 v[172:175], a[64:67], a[0:3], 0\n\t" \
  "v_mfma_f32_16x16x32_bf16 v[168:171], a[36:39], a[4:7], v[168:171]\n\t" \
  "v_mfma_f32_16x16x32_bf16 v[172:175], a[68:71], a[4:7], v[172:175]\n\t" \
  "v_mfma_f32_16x16x32_bf16 v[168:171], a[40:43], a[8:11], v[168:171]\n\t" \
  "v_mfma_f32_16x16x32_bf16 v[172:175], a[72:75], a[8:11], v[172:175]\n\t" \
  "v_mfma_f32_16x16x32_bf16 v[168:171], a[44:47], a[12:15], v[168:171]\n\t" \
  "v_mfma_f32_16x16x32_bf16 v[172:175], a[76:79], a[12:15], v[172:175]\n\t" \
  "v_mfma_f32_16x16x32_bf16 v[168:171], a[48:51], a[16:19], v[168:171]\n\t" \
  "v_mfma_f32_16x16x32_bf16 v[172:175], a[80:83], a[16:19], v[172:175]\n\t" \
  "v_mfma_f32_16x16x32_bf16 v[168:171], a[52:55], a[20:23], v[168:171]\n\t" \
  "v_mfma_f32_16x16x32_bf16 v[172:175], a[84:87], a[20:23], v[172:175]\n\t" \
  "v_mfma_f32_16x16x32_bf16 v[168:171], a[56:59], a[24:27], v[168:171]\n\t" \
  "v_mfma_f32_16x16x32_bf16 v[172:175], a[88:91], a[24:27], v[172:175]\n\t" \
  "v_mfma_f32_16x16x32_bf16 v[168:171], a[60:63], a[28:31], v[168:171]\n\t" \
  "v_mfma_f32_16x16x32_bf16 v[172:175], a[92:95], a[28:31], v[172:175]\n\t" \
  "s_nop 7\n\t" \
  "39:\n\t" \
  "s_add_i32 s40, s40, 2\n\t" \
  "v_cmp_u_f32 vcc, v168, v168\n\t" \
  "s_cbranch_vccnz 32f\n\t" \
  "33:\n\t" \
  "s_barrier\n\t" \
  "s_add_i32 s85, s40, -1\n\t" \
  "s_cmp_ge_i32 s40, 64\n\t" \
  "s_cbranch_scc1 47f\n\t" \
  "s_and_b32 s82, s85, 2\n\t" \
  "s_lshl_b32 s82, s82, 13\n\t" \
  "s_and_b32 s80, s85, 1\n\t" \
  "s_lshl_b32 s80, s80, 12\n\t" \
  "s_add_i32 s82, s82, s80\n\t" \
  "s_add_i32 s82, s82, 0x84000\n\t" \
  "s_mov_b32 exec_hi, 0\n\t" \
  "buffer_load_dword v184, v200, %[rh], s82 offen sc1\n\t" \
  "buffer_load_dword v186, v203, %[ru], 0 offen sc1\n\t" \
  "s_mov_b64 exec, -1\n\t" \
  "47:\n\t" \
  "ds_write_b128 v199, v[168:171] offset:0\n\t" \
  "ds_write_b128 v199, v[172:175] offset:1024\n\t" \
  "s_mov_b32 s59, 0\n\t" \
  "s_and_b32 s58, s58, 4\n\t" \
  "s_cmp_lt_i32 s40, 64\n\t" \
  "s_cbranch_scc1 20b\n\t" \
  "s_waitcnt lgkmcnt(0)\n\t" \
  "s_barrier\n\t" \
  "ds_read_b128 v[160:163], v193 offset:0\n\t" \
  "ds_read_b128 v[164:167], v193 offset:256\n\t" \
  "ds_read_b128 v[168:171], v193 offset:2048\n\t" \
  "ds_read_b128 v[172:175], v193 offset:2304\n\t" \
  "ds_read_b128 v[176:179], v193 offset:4096\n\t" \
  "ds_read_b128 v[180:183], v193 offset:4352\n\t" \
  "s_waitcnt lgkmcnt(2)\n\t" \
  "v_pk_add_f32 v[160:161], v[160:161], v[168:169]\n\t" \
  "v_pk_add_f32 v[162:163], v[162:163], v[170:171]\n\t" \
  "v_pk_add_f32 v[164:165], v[164:165], v[172:173]\n\t" \
  "v_pk_add_f32 v[166:167], v[166:167], v[174:175]\n\t" \
  "s_waitcnt lgkmcnt(0)\n\t" \
  "v_pk_add_f32 v[160:161], v[160:161], v[176:177]\n\t" \
  "v_pk_add_f32 v[162:163], v[162:163], v[178:179]\n\t" \
  "v_pk_add_f32 v[164:165], v[164:165], v[180:181]\n\t" \
  "v_pk_add_f32 v[166:167], v[166:167], v[182:183]\n\t" \
  "v_cvt_pk_bf16_f32 v160, v160, v161\n\t" \
  "v_cvt_pk_bf16_f32 v161, v162, v163\n\t" \
  "v_cvt_pk_bf16_f32 v162, v164, v165\n\t" \
  "v_cvt_pk_bf16_f32 v163, v166, v167\n\t" \
  "s_add_i32 s80, s40, -2\n\t" \
  "s_lshl_b32 s80, s80, 17\n\t" \
  "s_min_u32 s80, s80, 0x70000000\n\t" \
  "buffer_store_dwordx4 v[160:163], v196, %[ro], s80 offen sc1\n\t" \
  "s_mov_b32 s58, 64\n\t" \
  "s_branch 7f\n\t" \
  "22:\n\t" \
  "s_mov_b32 s81, 0\n\t" \
  "s_cmp_eq_u32 s40, 0\n\t" \
  "s_cbranch_scc1 24b\n\t" \
  "s_bitcmp1_b32 s58, 2\n\t" \
  "s_cbranch_scc1 24b\n\t" \
  "s_mov_b64 exec, -1\n\t" \
  "s_sleep 10\n\t" \
  "s_add_u32 s59, s59, 0x10000\n\t" \
  "s_cmp_lt_u32 s59, 0x400000\n\t" \
  "s_cbranch_scc1 19b\n\t" \
  "s_or_b32 s58, s58, 1\n\t" \
  "s_branch 7f\n\t" \
  "14:\n\t" \
  "v_mov_b32 v184, 0\n\t" \
  "s_bitcmp1_b32 s58, 2\n\t" \
  "s_nop 0\n\t" \
  "s_cbranch_scc1 15b\n\t" \
  "s_mov_b32 s84, 910\n\t" \
  "s_add_u32 s59, s59, 0x10000\n\t" \
  "s_cmp_lt_u32 s59, 0x400000\n\t" \
  "s_mov_b64 exec, vcc\n\t" \
  "buffer_load_dword v187, v200, %[rh], s78 offen sc1\n\t" \
  "s_mov_b32 exec_lo, -1\n\t" \
  "s_cbranch_scc1 13b\n\t" \
  "s_mov_b64 exec, -1\n\t" \
  "s_or_b32 s58, s58, 1\n\t" \
  "s_branch 7f\n\t" \
  "27:\n\t" \
  "s_bitcmp1_b32 s58, 2\n\t" \
  "s_cbranch_scc1 28b\n\t" \
  "s_bitcmp1_b32 s58, 4\n\t" \
  "s_cbranch_scc1 23f\n\t" \
  "s_sub_i32 s80, s56, s41\n\t" \
  "s_mul_hi_u32 s80, s80, s45\n\t" \
  "s_lshl_b32 s80, s80, 1\n\t" \
  "s_sub_i32 s80, s80, s57\n\t" \
  "s_min_u32 s47, s47, s46\n\t" \
  "s_min_u32 s47, s47, s80\n\t" \
  "v_mov_b32 v188, s47\n\t" \
  "s_mov_b32 s47, s46\n\t" \
  "s_mov_b32 s46, s80\n\t" \
  "buffer_store_dword v188, v202, %[rh], s52 offen sc1\n\t" \
  "v_mov_b64 v[188:189], -1\n\t" \
  "v_mov_b64 v[190:191], -1\n\t" \
  "v_mov_b32 v187, s40\n\t" \
  "s_add_i32 s80, s40, -1\n\t" \
  "s_and_b32 s80, s80, 7\n\t" \
  "s_lshl_b32 s80, s80, 15\n\t" \
  "s_add_i32 s80, s80, s49\n\t" \
  "buffer_store_dwordx4 v[188:191], v208, %[rh], s80 offen sc1\n\t" \
  "buffer_store_dword v187, v209, %[ru], 0 offen sc1\n\t" \
  "ds_write_b128 v197, v[72:75] offset:16384\n\t" \
  "ds_write_b128 v197, v[76:79] offset:17408\n\t" \
  "ds_write_b128 v197, v[80:83] offset:18432\n\t" \
  "ds_write_b128 v197, v[84:87] offset:19456\n\t" \
  "ds_write_b128 v197, v[88:91] offset:20480\n\t" \
  "ds_write_b128 v197, v[92:95] offset:21504\n\t" \
  "ds_write_b128 v197, v[96:99] offset:22528\n\t" \
  "ds_write_b128 v197, v[100:103] offset:23552\n\t" \
  "s_or_b32 s58, s58, 16\n\t" \
  "23:\n\t" \
  "s_add_u32 s59, s59, 0x1000000\n\t" \
  "s_cmp_lt_u32 s59, 0x40000000\n\t" \
  "s_cbranch_scc1 26f\n\t" \
  "s_or_b32 s58, s58, 1\n\t" \
  "s_branch 7f\n\t" \
  "26:\n\t" \
  "buffer_load_dword v186, v203, %[ru], 0 offen sc1\n\t" \
  "s_waitcnt vmcnt(0)\n\t" \
  "s_branch 25b\n\t" \
  "37:\n\t" \
  "s_and_b32 s76, s40, 3\n\t" \
  "s_lshl_b32 s76, s76, 18\n\t" \
  "buffer_load_dwordx4 a[0:3], v211, %[rh], s76 offen offset:0 sc1\n\t" \
  "buffer_load_dwordx4 a[4:7], v211, %[rh], s76 offen offset:1024 sc1\n\t" \
  "buffer_load_dwordx4 a[8:11], v212, %[rh], s76 offen offset:0 sc1\n\t" \
  "buffer_load_dwordx4 a[12:15], v212, %[rh], s76 offen offset:1024 sc1\n\t" \
  "buffer_load_dwordx4 a[16:19], v213, %[rh], s76 offen offset:0 sc1\n\t" \
  "buffer_load_dwordx4 a[20:23], v213, %[rh], s76 offen offset:1024 sc1\n\t" \
  "buffer_load_dwordx4 a[24:27], v214, %[rh], s76 offen offset:0 sc1\n\t" \
  "buffer_load_dwordx4 a[28:31], v214, %[rh], s76 offen offset:1024 sc1\n\t" \
  "s_bitcmp1_b32 s58, 4\n\t" \
  "s_cbranch_scc1 29f\n\t" \
  "s_sub_i32 s80, s56, s41\n\t" \
  "s_mul_hi_u32 s80, s80, s45\n\t" \
  "s_lshl_b32 s80, s80, 1\n\t" \
  "s_sub_i32 s80, s80, s57\n\t" \
  "s_min_u32 s47, s47, s46\n\t" \
  "s_min_u32 s47, s47, s80\n\t" \
  "v_mov_b32 v188, s47\n\t" \
  "s_mov_b32 s47, s46\n\t" \
  "s_mov_b32 s46, s80\n\t" \
  "buffer_store_dword v188, v202, %[rh], s52 offen sc1\n\t" \
  "v_mov_b64 v[188:189], -1\n\t" \
  "v_mov_b64 v[190:191], -1\n\t" \
  "v_mov_b32 v187, s40\n\t" \
  "s_add_i32 s80, s40, -1\n\t" \
  "s_and_b32 s80, s80, 7\n\t" \
  "s_lshl_b32 s80, s80, 15\n\t" \
  "s_add_i32 s80, s80, s49\n\t" \
  "buffer_store_dwordx4 v[188:191], v208, %[rh], s80 offen sc1\n\t" \
  "buffer_store_dword v187, v209, %[ru], 0 offen sc1\n\t" \
  "ds_write_b128 v197, v[72:75] offset:16384\n\t" \
  "ds_write_b128 v197, v[76:79] offset:17408\n\t" \
  "ds_write_b128 v197, v[80:83] offset:18432\n\t" \
  "ds_write_b128 v197, v[84:87] offset:19456\n\t" \
  "ds_write_b128 v197, v[88:91] offset:20480\n\t" \
  "ds_write_b128 v197, v[92:95] offset:21504\n\t" \
  "ds_write_b128 v197, v[96:99] offset:22528\n\t" \
  "ds_write_b128 v197, v[100:103] offset:23552\n\t" \
  "29:\n\t" \
  "s_and_b32 s58, s58, 36\n\t" \
  "s_waitcnt lgkmcnt(0)\n\t" \
  "s_barrier\n\t" \
  "ds_read_b128 v[160:163], v193 offset:0\n\t" \
  "ds_read_b128 v[164:167], v193 offset:256\n\t" \
  "ds_read_b128 v[168:171], v193 offset:2048\n\t" \
  "ds_read_b128 v[172:175], v193 offset:2304\n\t" \
  "ds_read_b128 v[176:179], v193 offset:4096\n\t" \
  "ds_read_b128 v[180:183], v193 offset:4352\n\t" \
  "s_waitcnt lgkmcnt(2)\n\t" \
  "v_pk_add_f32 v[160:161], v[160:161], v[168:169]\n\t" \
  "v_pk_add_f32 v[162:163], v[162:163], v[170:171]\n\t" \
  "v_pk_add_f32 v[164:165], v[164:165], v[172:173]\n\t" \
  "v_pk_add_f32 v[166:167], v[166:167], v[174:175]\n\t" \
  "s_waitcnt lgkmcnt(0)\n\t" \
  "v_pk_add_f32 v[160:161], v[160:161], v[176:177]\n\t" \
  "v_pk_add_f32 v[162:163], v[162:163], v[178:179]\n\t" \
  "v_pk_add_f32 v[164:165], v[164:165], v[180:181]\n\t" \
  "v_pk_add_f32 v[166:167], v[166:167], v[182:183]\n\t" \
  "v_cvt_pk_bf16_f32 v160, v160, v161\n\t" \
  "v_cvt_pk_bf16_f32 v161, v162, v163\n\t" \
  "v_cvt_pk_bf16_f32 v162, v164, v165\n\t" \
  "v_cvt_pk_bf16_f32 v163, v166, v167\n\t" \
  "s_branch 38b\n\t" \
  "8:\n\t" \
  "s_mov_b64 vcc, -1\n\t" \
  "s_and_b32 s80, s58, 6\n\t" \
  "s_cbranch_scc1 6b\n\t" \
  "v_mov_b32 v151, 1\n\t" \
  "s_branch 5b\n\t" \
  "32:\n\t" \
  "s_and_b32 s80, s58, 36\n\t" \
  "s_cbranch_scc1 33b\n\t" \
  "s_add_i32 s40, s40, -2\n\t" \
  "34:\n\t" \
  "s_or_b32 s59, s59, 128\n\t" \
  "s_and_b32 s76, s40, 3\n\t" \
  "s_lshl_b32 s76, s76, 18\n\t" \
  "35:\n\t" \
  "buffer_load_dwordx4 a[0:3], v211, %[rh], s76 offen offset:0 sc1\n\t" \
  "buffer_load_dwordx4 a[4:7], v211, %[rh], s76 offen offset:1024 sc1\n\t" \
  "buffer_load_dwordx4 a[8:11], v212, %[rh], s76 offen offset:0 sc1\n\t" \
  "buffer_load_dwordx4 a[12:15], v212, %[rh], s76 offen offset:1024 sc1\n\t" \
  "buffer_load_dwordx4 a[16:19], v213, %[rh], s76 offen offset:0 sc1\n\t" \
  "buffer_load_dwordx4 a[20:23], v213, %[rh], s76 offen offset:1024 sc1\n\t" \
  "buffer_load_dwordx4 a[24:27], v214, %[rh], s76 offen offset:0 sc1\n\t" \
  "buffer_load_dwordx4 a[28:31], v214, %[rh], s76 offen offset:1024 sc1\n\t" \
  "s_waitcnt vmcnt(0)\n\t" \
  "s_bitcmp1_b32 s58, 2\n\t" \
  "s_cbranch_scc1 36f\n\t" \
  "v_accvgpr_read_b32 v0, a0\n\t" \
  "v_accvgpr_read_b32 v1, a1\n\t" \
  "v_accvgpr_read_b32 v2, a2\n\t" \
  "v_accvgpr_read_b32 v3, a3\n\t" \
  "v_accvgpr_read_b32 v4, a4\n\t" \
  "v_accvgpr_read_b32 v5, a5\n\t" \
  "v_accvgpr_read_b32 v6, a6\n\t" \
  "v_accvgpr_read_b32 v7, a7\n\t" \
  "v_accvgpr_read_b32 v8, a8\n\t" \
  "v_accvgpr_read_b32 v9, a9\n\t" \
  "v_accvgpr_read_b32 v10, a10\n\t" \
  "v_accvgpr_read_b32 v11, a11\n\t" \
  "v_accvgpr_read_b32 v12, a12\n\t" \
  "v_accvgpr_read_b32 v13, a13\n\t" \
  "v_accvgpr_read_b32 v14, a14\n\t" \
  "v_accvgpr_read_b32 v15, a15\n\t" \
  "v_accvgpr_read_b32 v16, a16\n\t" \
  "v_accvgpr_read_b32 v17, a17\n\t" \
  "v_accvgpr_read_b32 v18, a18\n\t" \
  "v_accvgpr_read_b32 v19, a19\n\t" \
  "v_accvgpr_read_b32 v20, a20\n\t" \
  "v_accvgpr_read_b32 v21, a21\n\t" \
  "v_accvgpr_read_b32 v22, a22\n\t" \
  "v_accvgpr_read_b32 v23, a23\n\t" \
  "v_accvgpr_read_b32 v24, a24\n\t" \
  "v_accvgpr_read_b32 v25, a25\n\t" \
  "v_accvgpr_read_b32 v26, a26\n\t" \
  "v_accvgpr_read_b32 v27, a27\n\t" \
  "v_accvgpr_read_b32 v28, a28\n\t" \
  "v_accvgpr_read_b32 v29, a29\n\t" \
  "v_accvgpr_read_b32 v30, a30\n\t" \
  "v_accvgpr_read_b32 v31, a31\n\t" \
  "v_max_u32 v148, v0, v1\n\t" \
  "v_max3_u32 v148, v148, v2, v3\n\t" \
  "v_max3_u32 v148, v148, v4, v5\n\t" \
  "v_max3_u32 v148, v148, v6, v7\n\t" \
  "v_max3_u32 v148, v148, v8, v9\n\t" \
  "v_max3_u32 v148, v148, v10, v11\n\t" \
  "v_max3_u32 v148, v148, v12, v13\n\t" \
  "v_max3_u32 v148, v148, v14, v15\n\t" \
  "v_max3_u32 v148, v148, v16, v17\n\t" \
  "v_max3_u32 v148, v148, v18, v19\n\t" \
  "v_max3_u32 v148, v148, v20, v21\n\t" \
  "v_max3_u32 v148, v148, v22, v23\n\t" \
  "v_max3_u32 v148, v148, v24, v25\n\t" \
  "v_max3_u32 v148, v148, v26, v27\n\t" \
  "v_max3_u32 v148, v148, v28, v29\n\t" \
  "v_max3_u32 v148, v148, v30, v31\n\t" \
  "v_cmp_eq_u32 vcc, -1, v148\n\t" \
  "s_cbranch_vccz 36f\n\t" \
  "s_add_u32 s59, s59, 0x1000000\n\t" \
  "s_cmp_lt_u32 s59, 0x10000000\n\t" \
  "s_cbranch_scc1 35b\n\t" \
  "s_or_b32 s58, s58, 9\n\t" \
  "s_branch 7f\n\t" \
  "36:\n\t" \
  "s_and_b32 s58, s58, 4\n\t" \
  "s_or_b32 s58, s58, 32\n\t" \
  "s_branch 31b\n\t" \
  "18:\n\t" \
  "v_mov_b32 v186, -1\n\t" \
  "s_and_b32 s80, s40, 3\n\t" \
  "s_mul_i32 s77, s80, 0xc000\n\t" \
  "s_add_i32 s77, s77, 0x40000\n\t" \
  "s_lshl_b32 s78, s80, 14\n\t" \
  "s_add_i32 s78, s78, 0x70000\n\t" \
  "s_mov_b32 s57, 0\n\t" \
  "s_add_i32 s96, s77, 0x1000\n\t" \
  "s_lshl_b32 s88, s80, 20\n\t" \
  "s_add_i32 s88, s88, s48\n\t" \
  "s_lshr_b32 s80, s88, 21\n\t" \
  "s_bitcmp1_b64 %[sel], s80\n\t" \
  "s_cselect_b32 s80, 0x2000, 0\n\t" \
  "s_add_i32 s88, s88, s80\n\t" \
  "s_xor_b32 s89, s88, 0x800\n\t" \
  "s_xor_b32 s90, s88, 0x1000\n\t" \
  "s_xor_b32 s91, s88, 0x1800\n\t" \
  "s_add_i32 s92, s77, s50\n\t" \
  "s_xor_b32 s93, s92, 0x100\n\t" \
  "s_xor_b32 s94, s92, 0x200\n\t" \
  "s_xor_b32 s95, s92, 0x300\n\t" \
  "s_add_i32 s97, s40, 1\n\t" \
  "s_and_b32 s97, s97, 7\n\t" \
  "s_lshl_b32 s97, s97, 15\n\t" \
  "s_add_i32 s97, s97, s49\n\t" \
  "12:\n\t" \
  "s_or_b32 s58, s58, 3\n\t" \
  "s_mov_b32 s54, 0x70000000\n\t" \
  "11:\n\t" \
  "buffer_load_dwordx4 v[72:75], v192, %[ru], s88 offen offset:0 sc1\n\t" \
  "buffer_load_dwordx4 v[76:79], v192, %[ru], s88 offen offset:1024 sc1\n\t" \
  "buffer_load_dwordx4 v[80:83], v192, %[ru], s89 offen offset:0 sc1\n\t" \
  "buffer_load_dwordx4 v[84:87], v192, %[ru], s89 offen offset:1024 sc1\n\t" \
  "buffer_load_dwordx4 v[88:91], v192, %[ru], s90 offen offset:0 sc1\n\t" \
  "buffer_load_dwordx4 v[92:95], v192, %[ru], s90 offen offset:1024 sc1\n\t" \
  "buffer_load_dwordx4 v[96:99], v192, %[ru], s91 offen offset:0 sc1\n\t" \
  "buffer_load_dwordx4 v[100:103], v192, %[ru], s91 offen offset:1024 sc1\n\t" \
  "buffer_load_dwordx4 v[104:107], v194, %[rh], s92 offen sc1\n\t" \
  "buffer_load_dwordx4 v[108:111], v194, %[rh], s93 offen sc1\n\t" \
  "buffer_load_dwordx4 v[112:115], v194, %[rh], s94 offen sc1\n\t" \
  "buffer_load_dwordx4 v[116:119], v194, %[rh], s95 offen sc1\n\t" \
  "s_waitcnt vmcnt(0)\n\t" \
  "s_add_u32 s59, s59, 1\n\t" \
  "s_bitcmp1_b32 s58, 2\n\t" \
  "s_cbranch_scc1 10b\n\t" \
  "v_max_u32 v148, v72, v73\n\t" \
  "v_max3_u32 v148, v148, v74, v75\n\t" \
  "v_max3_u32 v148, v148, v76, v77\n\t" \
  "v_max3_u32 v148, v148, v78, v79\n\t" \
  "v_max3_u32 v148, v148, v80, v81\n\t" \
  "v_max3_u32 v148, v148, v82, v83\n\t" \
  "v_max3_u32 v148, v148, v84, v85\n\t" \
  "v_max3_u32 v148, v148, v86, v87\n\t" \
  "v_max3_u32 v148, v148, v88, v89\n\t" \
  "v_max3_u32 v148, v148, v90, v91\n\t" \
  "v_max3_u32 v148, v148, v92, v93\n\t" \
  "v_max3_u32 v148, v148, v94, v95\n\t" \
  "v_max3_u32 v148, v148, v96, v97\n\t" \
  "v_max3_u32 v148, v148, v98, v99\n\t" \
  "v_max3_u32 v148, v148, v100, v101\n\t" \
  "v_max3_u32 v148, v148, v102, v103\n\t" \
  "v_max3_u32 v148, v148, v104, v105\n\t" \
  "v_max3_u32 v148, v148, v106, v107\n\t" \
  "v_max3_u32 v148, v148, v108, v109\n\t" \
  "v_max3_u32 v148, v148, v110, v111\n\t" \
  "v_max3_u32 v148, v148, v112, v113\n\t" \
  "v_max3_u32 v148, v148, v114, v115\n\t" \
  "v_max3_u32 v148, v148, v116, v117\n\t" \
  "v_max3_u32 v148, v148, v118, v119\n\t" \
  "v_cmp_eq_u32 vcc, -1, v148\n\t" \
  "s_cbranch_vccz 10b\n\t" \
  "s_bitcmp1_b32 s59, 6\n\t" \
  "s_cbranch_scc0 11b\n\t" \
  "7:\n\t" \
  "s_branch 92f\n\t" \
  "91:\n\t" \
  "s_mov_b32 s90, s41\n\t" \
  "s_mov_b32 s91, s43\n\t" \
  "s_mov_b32 s58, s42\n\t" \
  "s_mov_b32 s59, 0\n\t" \
  "s_bitcmp1_b32 s58, 4\n\t" \
  "s_cbranch_scc1 37f\n\t" \
  "s_bitcmp1_b32 s58, 3\n\t" \
  "s_cbranch_scc1 34f\n\t" \
  "s_bitcmp1_b32 s58, 1\n\t" \
  "s_cbranch_scc1 18f\n\t" \
  "17:\n\t" \
  "v_mov_b64 v[40:41], 0\n\t" \
  "v_mov_b64 v[42:43], 0\n\t" \
  "v_mov_b64 v[44:45], 0\n\t" \
  "v_mov_b64 v[46:47], 0\n\t" \
  "v_mov_b64 v[48:49], 0\n\t" \
  "v_mov_b64 v[50:51], 0\n\t" \
  "v_mov_b64 v[52:53], 0\n\t" \
  "v_mov_b64 v[54:55], 0\n\t" \
  "v_mov_b64 v[56:57], 0\n\t" \
  "v_mov_b64 v[58:59], 0\n\t" \
  "v_mov_b64 v[60:61], 0\n\t" \
  "v_mov_b64 v[62:63], 0\n\t" \
  "v_mov_b64 v[64:65], 0\n\t" \
  "v_mov_b64 v[66:67], 0\n\t" \
  "v_mov_b64 v[68:69], 0\n\t" \
  "v_mov_b64 v[70:71], 0\n\t" \
  "v_mov_b64 v[72:73], 0\n\t" \
  "v_mov_b64 v[74:75], 0\n\t" \
  "v_mov_b64 v[76:77], 0\n\t" \
  "v_mov_b64 v[78:79], 0\n\t" \
  "v_mov_b64 v[80:81], 0\n\t" \
  "v_mov_b64 v[82:83], 0\n\t" \
  "v_mov_b64 v[84:85], 0\n\t" \
  "v_mov_b64 v[86:87], 0\n\t" \
  "v_mov_b64 v[88:89], 0\n\t" \
  "v_mov_b64 v[90:91], 0\n\t" \
  "v_mov_b64 v[92:93], 0\n\t" \
  "v_mov_b64 v[94:95], 0\n\t" \
  "v_mov_b64 v[96:97], 0\n\t" \
  "v_mov_b64 v[98:99], 0\n\t" \
  "v_mov_b64 v[100:101], 0\n\t" \
  "v_mov_b64 v[102:103], 0\n\t" \
  "v_mov_b64 v[136:137], 0\n\t" \
  "v_mov_b64 v[138:139], 0\n\t" \
  "v_mov_b64 v[140:141], 0\n\t" \
  "v_mov_b64 v[142:143], 0\n\t" \
  "19:\n\t" \
  "s_add_i32 s85, s40, -1\n\t" \
  "s_and_b32 s82, s85, 2\n\t" \
  "s_lshl_b32 s82, s82, 13\n\t" \
  "s_and_b32 s80, s85, 1\n\t" \
  "s_lshl_b32 s80, s80, 12\n\t" \
  "s_add_i32 s82, s82, s80\n\t" \
  "s_add_i32 s82, s82, 0x84000\n\t" \
  "s_mov_b32 exec_hi, 0\n\t" \
  "buffer_load_dword v184, v200, %[rh], s82 offen sc1\n\t" \
  "buffer_load_dword v185, v201, %[rh], s52 offen sc1\n\t" \
  "s_mov_b64 exec, -1\n\t" \
  "20:\n\t" \
  "s_and_b32 s80, s40, 3\n\t" \
  "s_mul_i32 s77, s80, 0xc000\n\t" \
  "s_add_i32 s77, s77, 0x40000\n\t" \
  "s_lshl_b32 s78, s80, 14\n\t" \
  "s_add_i32 s78, s78, 0x70000\n\t" \
  "s_mov_b32 s57, 0\n\t" \
  "s_add_i32 s96, s77, 0x1000\n\t" \
  "s_and_b32 s98, s40, 2\n\t" \
  "s_lshl_b32 s98, s98, 13\n\t" \
  "s_and_b32 s80, s40, 1\n\t" \
  "s_lshl_b32 s80, s80, 12\n\t" \
  "s_add_i32 s98, s98, s80\n\t" \
  "s_add_i32 s98, s98, 0x84000\n\t" \
  "s_memtime vcc\n\t" \
  "s_memrealtime s[86:87]\n\t" \
  "s_nop 4\n\t" \
  "s_memrealtime s[60:61]\n\t" \
  "s_nop 4\n\t" \
  "s_memrealtime s[62:63]\n\t" \
  "s_nop 4\n\t" \
  "s_memrealtime s[64:65]\n\t" \
  "s_nop 4\n\t" \
  "s_memrealtime s[66:67]\n\t" \
  "s_nop 4\n\t" \
  "s_memrealtime s[68:69]\n\t" \
  "s_mov_b32 s79, 0\n\t" \
  "s_waitcnt lgkmcnt(0)\n\t" \
  "s_cmp_eq_u32 s86, s60\n\t" \
  "s_addc_u32 s79, s79, 0\n\t" \
  "s_cmp_eq_u32 s86, s62\n\t" \
  "s_addc_u32 s79, s79, 0\n\t" \
  "s_cmp_eq_u32 s86, s64\n\t" \
  "s_addc_u32 s79, s79, 0\n\t" \
  "s_cmp_eq_u32 s86, s66\n\t" \
  "s_addc_u32 s79, s79, 0\n\t" \
  "s_cmp_eq_u32 s86, s68\n\t" \
  "s_addc_u32 s79, s79, 0\n\t" \
  "s_mul_i32 s79, s79, 24\n\t" \
  "s_add_i32 s79, s79, vcc_lo\n\t" \
  "s_lshl4_add_u32 s80, s86, 64\n\t" \
  "s_mov_b32 s41, s79\n\t" \
  "s_mov_b32 s43, s80\n\t" \
  "s_lshl_b32 s83, s43, 8\n\t" \
  "s_mov_b32 s84, 610\n\t" \
  "s_mov_b32 exec_hi, 0\n\t" \
  "s_waitcnt vmcnt(0)\n\t" \
  "v_cmp_ne_u32_sdwa vcc, v184, s85 src0_sel:BYTE_0 src1_sel:DWORD\n\t" \
  "v_subrev_u32 v184, s83, v184\n\t" \
  "v_ashrrev_i32 v184, 8, v184\n\t" \
  "s_cbranch_vccnz 22f\n\t" \
  "v_max_i32_dpp v185, v185, v185 quad_perm:[1,0,3,2] row_mask:0xf bank_mask:0xf\n\t" \
  "v_max_i32_dpp v184, v184, v184 quad_perm:[1,0,3,2] row_mask:0xf bank_mask:0xf\n\t" \
  "s_nop 0\n\t" \
  "v_max_i32_dpp v185, v185, v185 quad_perm:[2,3,0,1] row_mask:0xf bank_mask:0xf\n\t" \
  "v_max_i32_dpp v184, v184, v184 quad_perm:[2,3,0,1] row_mask:0xf bank_mask:0xf\n\t" \
  "s_nop 0\n\t" \
  "v_max_i32_dpp v185, v185, v185 row_half_mirror row_mask:0xf bank_mask:0xf\n\t" \
  "v_max_i32_dpp v184, v184, v184 row_half_mirror row_mask:0xf bank_mask:0xf\n\t" \
  "v_readlane_b32 s54, v185, 0\n\t" \
  "s_nop 0\n\t" \
  "v_max_i32_dpp v184, v184, v184 row_mirror row_mask:0xf bank_mask:0xf\n\t" \
  "s_nop 0\n\t" \
  "v_readlane_b32 s81, v184, 0\n\t" \
  "v_readlane_b32 s82, v184, 16\n\t" \
  "s_max_i32 s81, s81, s82\n\t" \
  "s_add_i32 s76, s81, s43\n\t" \
  "s_add_i32 s81, s81, s53\n\t" \
  "s_max_i32 s81, s81, 0\n\t" \
  "s_min_i32 s81, s81, 20000\n\t" \
  "s_mul_i32 s81, s81, s44\n\t" \
  "s_lshr_b32 s81, s81, 16\n\t" \
  "s_add_i32 s81, s81, -1020\n\t" \
  "24:\n\t" \
  "s_add_i32 s81, s81, s41\n\t" \
  "s_min_u32 s54, s54, 1760\n\t" \
  "s_add_i32 s54, s54, 240\n\t" \
  ".p2align 6\n\t" \
  "s_nop 0\n\t" \
  "s_nop 0\n\t" \
  "s_nop 0\n\t" \
  "s_nop 0\n\t" \
  "s_nop 0\n\t" \
  "s_nop 0\n\t" \
  "s_nop 0\n\t" \
  "s_nop 0\n\t" \
  "1:\n\t" \
  "s_memtime vcc\n\t" \
  "s_waitcnt lgkmcnt(0)\n\t" \
  "s_sub_i32 s79, s81, vcc_lo\n\t" \
  "s_cmp_gt_i32 s79, 128\n\t" \
  "s_cbranch_scc1 1b\n\t" \
  ".p2align 4\n\t" \
  "2:\n\t" \
  "s_sub_i32 s79, s79, 33\n\t" \
  "s_cmp_gt_i32 s79, 56\n\t" \
  "s_cbranch_scc1 2b\n\t" \
  "buffer_load_dword v187, v200, %[rh], s78 offen sc1\n\t" \
  "s_memtime vcc\n\t" \
  "s_memrealtime s[86:87]\n\t" \
  "s_nop 4\n\t" \
  "s_memrealtime s[60:61]\n\t" \
  "s_nop 4\n\t" \
  "s_memrealtime s[62:63]\n\t" \
  "s_nop 4\n\t" \
  "s_memrealtime s[64:65]\n\t" \
  "s_nop 4\n\t" \
  "s_memrealtime s[66:67]\n\t" \
  "s_nop 4\n\t" \
  "s_memrealtime s[68:69]\n\t" \
  "s_mov_b32 s79, 0\n\t" \
  "s_waitcnt lgkmcnt(0)\n\t" \
  "s_mov_b64 exec, -1\n\t" \
  "ds_read_b128 a[0:3], v194 offset:0\n\t" \
  "ds_read_b128 a[4:7], v194 offset:1024\n\t" \
  "ds_read_b128 a[8:11], v194 offset:2048\n\t" \
  "ds_read_b128 a[12:15], v194 offset:3072\n\t" \
  "ds_read_b128 a[16:19], v194 offset:4096\n\t" \
  "ds_read_b128 a[20:23], v194 offset:5120\n\t" \
  "ds_read_b128 a[24:27], v194 offset:6144\n\t" \
  "ds_read_b128 a[28:31], v194 offset:7168\n\t" \
  "ds_read_b128 a[32:35], v194 offset:8192\n\t" \
  "ds_read_b128 a[36:39], v194 offset:9216\n\t" \
  "ds_read_b128 a[40:43], v194 offset:10240\n\t" \
  "ds_read_b128 a[44:47], v194 offset:11264\n\t" \
  "ds_read_b128 a[48:51], v194 offset:12288\n\t" \
  "ds_read_b128 a[52:55], v194 offset:13312\n\t" \
  "ds_read_b128 a[56:59], v194 offset:14336\n\t" \
  "ds_read_b128 a[60:63], v194 offset:15360\n\t" \
  "ds_read_b128 a[64:67], v194 offset:16384\n\t" \
  "ds_read_b128 a[68:71], v194 offset:17408\n\t" \
  "ds_read_b128 a[72:75], v194 offset:18432\n\t" \
  "ds_read_b128 a[76:79], v194 offset:19456\n\t" \
  "ds_read_b128 a[80:83], v194 offset:20480\n\t" \
  "ds_read_b128 a[84:87], v194 offset:21504\n\t" \
  "ds_read_b128 a[88:91], v194 offset:22528\n\t" \
  "ds_read_b128 a[92:95], v194 offset:23552\n\t" \
  "ds_read_b128 a[96:99], v194 offset:24576\n\t" \
  "ds_read_b128 a[100:103], v194 offset:25600\n\t" \
  "ds_read_b128 a[104:107], v194 offset:26624\n\t" \
  "ds_read_b128 a[108:111], v194 offset:27648\n\t" \
  "ds_read_b128 a[112:115], v194 offset:28672\n\t" \
  "ds_read_b128 a[116:119], v194 offset:29696\n\t" \
  "ds_read_b128 a[120:123], v194 offset:30720\n\t" \
  "ds_read_b128 a[124:127], v194 offset:31744\n\t" \
  "s_mov_b32 exec_hi, 0\n\t" \
  "s_cmp_eq_u32 s86, s60\n\t" \
  "s_addc_u32 s79, s79, 0\n\t" \
  "s_cmp_eq_u32 s86, s62\n\t" \
  "s_addc_u32 s79, s79, 0\n\t" \
  "s_cmp_eq_u32 s86, s64\n\t" \
  "s_addc_u32 s79, s79, 0\n\t" \
  "s_cmp_eq_u32 s86, s66\n\t" \
  "s_addc_u32 s79, s79, 0\n\t" \
  "s_cmp_eq_u32 s86, s68\n\t" \
  "s_addc_u32 s79, s79, 0\n\t" \
  "s_mul_i32 s79, s79, 24\n\t" \
  "s_add_i32 s79, s79, vcc_lo\n\t" \
  "s_lshl4_add_u32 s80, s86, 64\n\t" \
  "s_mov_b32 s41, s79\n\t" \
  "s_mov_b32 s43, s80\n\t" \
  "s_lshl_b32 s83, s43, 8\n\t" \
  "s_add_i32 s54, s54, s43\n\t" \
  "13:\n\t" \
  "s_waitcnt vmcnt(0)\n\t" \
  "s_memtime s[80:81]\n\t" \
  "v_cmp_ne_u32_sdwa vcc, v187, s40 src0_sel:BYTE_0 src1_sel:DWORD\n\t" \
  "v_subrev_u32 v184, s83, v187\n\t" \
  "v_ashrrev_i32 v184, 8, v184\n\t" \
  "s_nop 1\n\t" \
  "v_max_i32_dpp v184, v184, v184 quad_perm:[1,0,3,2] row_mask:0xf bank_mask:0xf\n\t" \
  "s_nop 1\n\t" \
  "v_max_i32_dpp v184, v184, v184 quad_perm:[2,3,0,1] row_mask:0xf bank_mask:0xf\n\t" \
  "s_cbranch_vccnz 14f\n\t" \
  "15:\n\t" \
  "s_nop 0\n\t" \
  "v_max_i32_dpp v184, v184, v184 row_half_mirror row_mask:0xf bank_mask:0xf\n\t" \
  "s_nop 1\n\t" \
  "v_max_i32_dpp v184, v184, v184 row_mirror row_mask:0xf bank_mask:0xf\n\t" \
  "s_mov_b64 exec, -1\n\t" \
  "v_readlane_b32 s57, v184, 0\n\t" \
  "v_readlane_b32 s82, v184, 16\n\t" \
  "s_waitcnt lgkmcnt(0)\n\t" \
  "s_sub_i32 s79, s41, s80\n\t" \
  "s_max_i32 s57, s57, s82\n\t" \
  "s_add_i32 s81, s57, 212\n\t" \
  "s_max_i32 s81, s81, 0\n\t" \
  "s_min_i32 s81, s81, 20000\n\t" \
  "s_mul_i32 s81, s81, s44\n\t" \
  "s_lshr_b32 s81, s81, 16\n\t" \
  "s_add_i32 s79, s79, s81\n\t" \
  "s_sub_i32 s85, s84, s79\n\t" \
  "s_max_i32 s85, s85, 0\n\t" \
  "s_mul_hi_u32 s85, s85, s45\n\t" \
  "s_lshl1_add_u32 s85, s85, s57\n\t" \
  "s_add_i32 s85, s85, s54\n\t" \
  "s_lshl_b32 s85, s85, 8\n\t" \
  "s_or_b32 s85, s85, s40\n\t" \
  "v_mov_b32 v184, s85\n\t" \
  "buffer_store_dword v184, v202, %[rh], s98 offen sc1\n\t" \
  "s_add_i32 s79, s79, -620\n\t" \
  ".p2align 4\n\t" \
  "3:\n\t" \
  "s_sub_i32 s79, s79, 33\n\t" \
  "s_cmp_gt_i32 s79, 56\n\t" \
  "s_cbranch_scc1 3b\n\t" \
  "buffer_load_dwordx4 v[0:3], v193, %[rh], s77 offen offset:0 sc1\n\t" \
  "s_nop 4\n\t" \
  "buffer_load_dwordx4 v[4:7], v193, %[rh], s77 offen offset:1024 sc1\n\t" \
  "s_nop 4\n\t" \
  "buffer_load_dwordx4 v[8:11], v193, %[rh], s77 offen offset:2048 sc1\n\t" \
  "s_nop 4\n\t" \
  "buffer_load_dwordx4 v[12:15], v193, %[rh], s77 offen offset:3072 sc1\n\t" \
  "s_nop 4\n\t" \
  "buffer_load_dwordx4 v[16:19], v193, %[rh], s96 offen offset:0 sc1\n\t" \
  "s_nop 4\n\t" \
  "buffer_load_dwordx4 v[20:23], v193, %[rh], s96 offen offset:1024 sc1\n\t" \
  "s_nop 4\n\t" \
  "buffer_load_dwordx4 v[24:27], v193, %[rh], s96 offen offset:2048 sc1\n\t" \
  "s_nop 4\n\t" \
  "buffer_load_dwordx4 v[28:31], v193, %[rh], s96 offen offset:3072 sc1\n\t" \
  "s_add_i32 s79, s79, 267\n\t" \
  ".p2align 4\n\t" \
  "4:\n\t" \
  "s_sub_i32 s79, s79, 33\n\t" \
  "s_cmp_gt_i32 s79, 56\n\t" \
  "s_cbranch_scc1 4b\n\t" \
  "buffer_load_dwordx4 v[32:35], v195, %[rh], s77 offen offset:0 sc1\n\t" \
  "buffer_load_dwordx4 v[36:39], v195, %[rh], s77 offen offset:2048 sc1\n\t" \
  "10:\n\t" \
  "s_waitcnt vmcnt(9)\n\t" \
  "v_smfmac_f32_16x16x64_bf16 v[40:43], v[0:3], a[0:7], v203\n\t" \
  "v_smfmac_f32_16x16x64_bf16 v[56:59], v[0:3], a[8:15], v203\n\t" \
  "s_waitcnt vmcnt(8)\n\t" \
  "v_smfmac_f32_16x16x64_bf16 v[44:47], v[4:7], a[16:23], v203\n\t" \
  "v_cvt_pk_bf16_f32 v40, v40, v41\n\t" \
  "v_cvt_pk_bf16_f32 v41, v42, v43\n\t" \
  "v_smfmac_f32_16x16x64_bf16 v[60:63], v[4:7], a[24:31], v203\n\t" \
  "v_cvt_pk_bf16_f32 v56, v56, v57\n\t" \
  "v_cvt_pk_bf16_f32 v57, v58, v59\n\t" \
  "s_waitcnt vmcnt(7)\n\t" \
  "v_smfmac_f32_16x16x64_bf16 v[48:51], v[8:11], a[32:39], v203\n\t" \
  "v_cvt_pk_bf16_f32 v42, v44, v45\n\t" \
  "v_cvt_pk_bf16_f32 v43, v46, v47\n\t" \
  "v_smfmac_f32_16x16x64_bf16 v[64:67], v[8:11], a[40:47], v203\n\t" \
  "v_cvt_pk_bf16_f32 v58, v60, v61\n\t" \
  "v_cvt_pk_bf16_f32 v59, v62, v63\n\t" \
  "s_waitcnt vmcnt(6)\n\t" \
  "v_smfmac_f32_16x16x64_bf16 v[52:55], v[12:15], a[48:55], v203\n\t" \
  "v_cvt_pk_bf16_f32 v44, v48, v49\n\t" \
  "v_cvt_pk_bf16_f32 v45, v50, v51\n\t" \
  "v_smfmac_f32_16x16x64_bf16 v[68:71], v[12:15], a[56:63], v203\n\t" \
  "v_cvt_pk_bf16_f32 v60, v64, v65\n\t" \
  "v_cvt_pk_bf16_f32 v61, v66, v67\n\t" \
  "s_waitcnt vmcnt(5)\n\t" \
  "v_smfmac_f32_16x16x64_bf16 v[72:75], v[16:19], a[64:71], v203\n\t" \
  "v_cvt_pk_bf16_f32 v46, v52, v53\n\t" \
  "v_cvt_pk_bf16_f32 v47, v54, v55\n\t" \
  "v_smfmac_f32_16x16x64_bf16 v[88:91], v[16:19], a[72:79], v203\n\t" \
  "v_cvt_pk_bf16_f32 v62, v68, v69\n\t" \
  "v_cvt_pk_bf16_f32 v63, v70, v71\n\t" \
  "s_waitcnt vmcnt(4)\n\t" \
  "v_smfmac_f32_16x16x64_bf16 v[76:79], v[20:23], a[80:87], v203\n\t" \
  "v_cvt_pk_bf16_f32 v72, v72, v73\n\t" \
  "v_cvt_pk_bf16_f32 v73, v74, v75\n\t" \
  "v_smfmac_f32_16x16x64_bf16 v[92:95], v[20:23], a[88:95], v203\n\t" \
  "v_cvt_pk_bf16_f32 v88, v88, v89\n\t" \
  "v_cvt_pk_bf16_f32 v89, v90, v91\n\t" \
  "s_waitcnt vmcnt(3)\n\t" \
  "v_smfmac_f32_16x16x64_bf16 v[80:83], v[24:27], a[96:103], v203\n\t" \
  "v_cvt_pk_bf16_f32 v74, v76, v77\n\t" \
  "v_cvt_pk_bf16_f32 v75, v78, v79\n\t" \
  "v_smfmac_f32_16x16x64_bf16 v[96:99], v[24:27], a[104:111], v203\n\t" \
  "v_cvt_pk_bf16_f32 v90, v92, v93\n\t" \
  "v_cvt_pk_bf16_f32 v91, v94, v95\n\t" \
  "s_waitcnt vmcnt(2)\n\t" \
  "v_smfmac_f32_16x16x64_bf16 v[84:87], v[28:31], a[112:119], v203\n\t" \
  "v_cvt_pk_bf16_f32 v76, v80, v81\n\t" \
  "v_cvt_pk_bf16_f32 v77, v82, v83\n\t" \
  "v_smfmac_f32_16x16x64_bf16 v[100:103], v[28:31], a[120:127], v203\n\t" \
  "v_cvt_pk_bf16_f32 v92, v96, v97\n\t" \
  "v_cvt_pk_bf16_f32 v93, v98, v99\n\t" \
  "s_nop 1\n\t" \
  "v_cvt_pk_bf16_f32 v78, v84, v85\n\t" \
  "v_cvt_pk_bf16_f32 v79, v86, v87\n\t" \
  "s_nop 1\n\t" \
  "v_cvt_pk_bf16_f32 v94, v100, v101\n\t" \
  "v_cvt_pk_bf16_f32 v95, v102, v103\n\t" \
  "s_waitcnt vmcnt(1)\n\t" \
  "v_smfmac_f32_16x16x64_bf16 v[136:139], v[32:35], v[40:47], v204\n\t" \
  "v_smfmac_f32_16x16x64_bf16 v[140:143], v[32:35], v[56:63], v204\n\t" \
  "s_waitcnt vmcnt(0)\n\t" \
  "v_smfmac_f32_16x16x64_bf16 v[136:139], v[36:39], v[72:79], v204\n\t" \
  "v_smfmac_f32_16x16x64_bf16 v[140:143], v[36:39], v[88:95], v204\n\t" \
  "s_nop 3\n\t" \
  "v_permlane32_swap_b32 v136, v138\n\t" \
  "v_permlane32_swap_b32 v137, v139\n\t" \
  "v_pk_add_f32 v[136:137], v[136:137], v[138:139]\n\t" \
  "s_nop 0\n\t" \
  "v_permlane32_swap_b32 v140, v142\n\t" \
  "v_permlane32_swap_b32 v141, v143\n\t" \
  "v_pk_add_f32 v[138:139], v[140:141], v[142:143]\n\t" \
  "ds_write2st64_b64 v197, v[136:137], v[138:139] offset1:2\n\t" \
  "s_waitcnt lgkmcnt(0)\n\t" \
  "s_barrier\n\t" \
  "s_sub_i32 s86, s41, s90\n\t" \
  "s_sub_i32 s87, s43, s91\n\t" \
  "s_mov_b32 s90, s41\n\t" \
  "s_mov_b32 s91, s43\n\t" \
  "s_cmp_lt_u32 s86, 4000\n\t" \
  "s_cbranch_scc1 21f\n\t" \
  "v_cvt_f32_u32 v165, s86\n\t" \
  "v_cvt_f32_u32 v164, s87\n\t" \
  "v_rcp_f32 v165, v165\n\t" \
  "s_nop 0\n\t" \
  "v_mul_f32 v165, v165, v164\n\t" \
  "v_max_f32 v165, 0x3f028f5c, v165\n\t" \
  "v_min_f32 v165, 0x3ffeb852, v165\n\t" \
  "v_rcp_f32 v164, v165\n\t" \
  "v_mul_f32 v165, 0x4f000000, v165\n\t" \
  "v_mul_f32 v164, 0x47800000, v164\n\t" \
  "v_cvt_u32_f32 v165, v165\n\t" \
  "v_cvt_u32_f32 v164, v164\n\t" \
  "s_nop 0\n\t" \
  "v_readfirstlane_b32 s45, v165\n\t" \
  "v_readfirstlane_b32 s44, v164\n\t" \
  "21:\n\t" \
  "s_barrier\n\t" \
  "ds_read2_b32 v[148:149], v207 offset1:8\n\t" \
  "s_waitcnt lgkmcnt(0)\n\t" \
  "v_or_b32 v148, v148, v149\n\t" \
  "v_cmp_ne_u32 vcc, 0, v148\n\t" \
  "s_cbranch_vccnz 12f\n\t" \
  "s_and_b32 s58, s58, 4\n\t" \
  "s_add_i32 s80, s57, s43\n\t" \
  "s_sub_i32 s80, s80, s76\n\t" \
  "s_add_i32 s53, s53, -12\n\t" \
  "s_max_i32 s80, s80, s53\n\t" \
  "s_add_i32 s53, s53, 24\n\t" \
  "s_min_i32 s53, s53, s80\n\t" \
  "s_and_b32 s76, s40, 3\n\t" \
  "s_lshl_b32 s76, s76, 18\n\t" \
  "s_add_i32 s77, s76, 0x10000\n\t" \
  "s_add_i32 s78, s76, 0x1000\n\t" \
  "s_add_i32 s79, s76, 0x11000\n\t" \
  "s_mov_b32 s97, 1\n\t" \
  "s_cmp_ge_i32 s40, 62\n\t" \
  "s_cbranch_scc1 31f\n\t" \
  "s_and_b32 s82, s40, 3\n\t" \
  "s_lshl_b32 s82, s82, 20\n\t" \
  "s_add_i32 s82, s82, s48\n\t" \
  "s_andn2_b32 s82, s82, 0x1800\n\t" \
  "s_lshr_b32 s80, s82, 21\n\t" \
  "s_bitcmp1_b64 %[sel], s80\n\t" \
  "s_cselect_b32 s80, 0x2000, 0\n\t" \
  "s_add_i32 s82, s82, s80\n\t" \
  "s_add_i32 s83, s82, 0x1000\n\t" \
  "v_mov_b64 v[188:189], -1\n\t" \
  "v_mov_b64 v[190:191], -1\n\t" \
  "s_sleep 0\n\t" \
  "buffer_store_dwordx4 v[188:191], v192, %[ru], s82 offen offset:0 sc1\n\t" \
  "buffer_store_dwordx4 v[188:191], v192, %[ru], s82 offen offset:1024 sc1\n\t" \
  "buffer_store_dwordx4 v[188:191], v192, %[ru], s82 offen offset:2048 sc1\n\t" \
  "buffer_store_dwordx4 v[188:191], v192, %[ru], s82 offen offset:3072 sc1\n\t" \
  "buffer_store_dwordx4 v[188:191], v192, %[ru], s83 offen offset:0 sc1\n\t" \
  "buffer_store_dwordx4 v[188:191], v192, %[ru], s83 offen offset:1024 sc1\n\t" \
  "buffer_store_dwordx4 v[188:191], v192, %[ru], s83 offen offset:2048 sc1\n\t" \
  "buffer_store_dwordx4 v[188:191], v192, %[ru], s83 offen offset:3072 sc1\n\t" \
  "31:\n\t" \
  "buffer_load_dwordx4 v[0:3], v210, %[rh], s76 offen offset:0 sc1\n\t" \
  "s_nop 10\n\t" \
  "buffer_load_dwordx4 v[4:7], v210, %[rh], s76 offen offset:1024 sc1\n\t" \
  "s_nop 10\n\t" \
  "buffer_load_dwordx4 v[8:11], v210, %[rh], s77 offen offset:0 sc1\n\t" \
  "s_nop 10\n\t" \
  "buffer_load_dwordx4 v[12:15], v210, %[rh], s77 offen offset:1024 sc1\n\t" \
  "s_cmp_eq_u32 s97, 0\n\t" \
  "s_cbranch_scc1 26f\n\t" \
  "s_barrier\n\t" \
  "s_mov_b32 s97, 0\n\t" \
  "26:\n\t" \
  "buffer_load_dwordx4 v[16:19], v210, %[rh], s76 offen offset:2048 sc1\n\t" \
  "s_nop 10\n\t" \
  "buffer_load_dwordx4 v[20:23], v210, %[rh], s76 offen offset:3072 sc1\n\t" \
  "s_nop 10\n\t" \
  "buffer_load_dwordx4 v[24:27], v210, %[rh], s77 offen offset:2048 sc1\n\t" \
  "s_nop 10\n\t" \
  "buffer_load_dwordx4 v[28:31], v210, %[rh], s77 offen offset:3072 sc1\n\t" \
  "s_nop 10\n\t" \
  "buffer_load_dwordx4 v[32:35], v210, %[rh], s78 offen offset:0 sc1\n\t" \
  "s_nop 10\n\t" \
  "buffer_load_dwordx4 v[36:39], v210, %[rh], s78 offen offset:1024 sc1\n\t" \
  "s_nop 10\n\t" \
  "buffer_load_dwordx4 v[40:43], v210, %[rh], s79 offen offset:0 sc1\n\t" \
  "s_nop 10\n\t" \
  "buffer_load_dwordx4 v[44:47], v210, %[rh], s79 offen offset:1024 sc1\n\t" \
  "s_nop 10\n\t" \
  "buffer_load_dwordx4 v[48:51], v210, %[rh], s78 offen offset:2048 sc1\n\t" \
  "s_nop 10\n\t" \
  "buffer_load_dwordx4 v[52:55], v210, %[rh], s78 offen offset:3072 sc1\n\t" \
  "s_nop 10\n\t" \
  "buffer_load_dwordx4 v[56:59], v210, %[rh], s79 offen offset:2048 sc1\n\t" \
  "s_nop 10\n\t" \
  "buffer_load_dwordx4 v[60:63], v210, %[rh], s79 offen offset:3072 sc1\n\t" \
  "s_nop 10\n\t" \
  "buffer_load_dwordx4 v[64:67], v211, %[rh], s76 offen offset:0 sc1\n\t" \
  "s_nop 10\n\t" \
  "buffer_load_dwordx4 v[68:71], v211, %[rh], s76 offen offset:1024 sc1\n\t" \
  "s_nop 10\n\t" \
  "buffer_load_dwordx4 v[72:75], v211, %[rh], s77 offen offset:0 sc1\n\t" \
  "s_nop 10\n\t" \
  "buffer_load_dwordx4 v[76:79], v211, %[rh], s77 offen offset:1024 sc1\n\t" \
  "s_nop 10\n\t" \
  "buffer_load_dwordx4 v[80:83], v211, %[rh], s76 offen offset:2048 sc1\n\t" \
  "s_nop 10\n\t" \
  "buffer_load_dwordx4 v[84:87], v211, %[rh], s76 offen offset:3072 sc1\n\t" \
  "s_nop 10\n\t" \
  "buffer_load_dwordx4 v[88:91], v211, %[rh], s77 offen offset:2048 sc1\n\t" \
  "s_nop 10\n\t" \
  "buffer_load_dwordx4 v[92:95], v211, %[rh], s77 offen offset:3072 sc1\n\t" \
  "s_nop 10\n\t" \
  "v_mov_b64 v[136:137], 0\n\t" \
  "v_mov_b64 v[138:139], 0\n\t" \
  "v_mov_b64 v[140:141], 0\n\t" \
  "v_mov_b64 v[142:143], 0\n\t" \
  "s_waitcnt vmcnt(22)\n\t" \
  "v_swap_b32 v1, v4\n\t" \
  "v_swap_b32 v3, v6\n\t" \
  "s_nop 1\n\t" \
  "v_mfma_f32_16x16x32_bf16 v[168:171], a[0:3], v[0:3], 0\n\t" \
  "buffer_load_dwordx4 v[96:99], v211, %[rh], s78 offen offset:0 sc1\n\t" \
  "v_mfma_f32_16x16x32_bf16 v[172:175], a[8:11], v[0:3], 0\n\t" \
  "s_waitcnt vmcnt(21)\n\t" \
  "v_swap_b32 v9, v12\n\t" \
  "v_mfma_f32_16x16x32_bf16 v[168:171], a[4:7], v[4:7], v[168:171]\n\t" \
  "v_swap_b32 v11, v14\n\t" \
  "v_mfma_f32_16x16x32_bf16 v[172:175], a[12:15], v[4:7], v[172:175]\n\t" \
  "buffer_load_dwordx4 v[100:103], v211, %[rh], s78 offen offset:1024 sc1\n\t" \
  "v_mfma_f32_16x16x32_bf16 v[176:179], a[0:3], v[8:11], 0\n\t" \
  "buffer_load_dwordx4 v[104:107], v211, %[rh], s79 offen offset:0 sc1\n\t" \
  "v_mfma_f32_16x16x32_bf16 v[180:183], a[8:11], v[8:11], 0\n\t" \
  "s_waitcnt vmcnt(21)\n\t" \
  "v_swap_b32 v17, v20\n\t" \
  "v_mfma_f32_16x16x32_bf16 v[176:179], a[4:7], v[12:15], v[176:179]\n\t" \
  "v_swap_b32 v19, v22\n\t" \
  "v_mfma_f32_16x16x32_bf16 v[180:183], a[12:15], v[12:15], v[180:183]\n\t" \
  "buffer_load_dwordx4 v[108:111], v211, %[rh], s79 offen offset:1024 sc1\n\t" \
  "v_mfma_f32_16x16x32_bf16 v[168:171], a[16:19], v[16:19], v[168:171]\n\t" \
  "buffer_load_dwordx4 v[112:115], v211, %[rh], s78 offen offset:2048 sc1\n\t" \
  "v_mfma_f32_16x16x32_bf16 v[172:175], a[24:27], v[16:19], v[172:175]\n\t" \
  "s_waitcnt vmcnt(21)\n\t" \
  "v_swap_b32 v25, v28\n\t" \
  "v_mfma_f32_16x16x32_bf16 v[168:171], a[20:23], v[20:23], v[168:171]\n\t" \
  "v_swap_b32 v27, v30\n\t" \
  "v_mfma_f32_16x16x32_bf16 v[172:175], a[28:31], v[20:23], v[172:175]\n\t" \
  "buffer_load_dwordx4 v[116:119], v211, %[rh], s78 offen offset:3072 sc1\n\t" \
  "v_mfma_f32_16x16x32_bf16 v[176:179], a[16:19], v[24:27], v[176:179]\n\t" \
  "buffer_load_dwordx4 v[120:123], v211, %[rh], s79 offen offset:2048 sc1\n\t" \
  "v_mfma_f32_16x16x32_bf16 v[180:183], a[24:27], v[24:27], v[180:183]\n\t" \
  "s_waitcnt vmcnt(21)\n\t" \
  "v_swap_b32 v33, v36\n\t" \
  "v_mfma_f32_16x16x32_bf16 v[176:179], a[20:23], v[28:31], v[176:179]\n\t" \
  "v_swap_b32 v35, v38\n\t" \
  "v_mfma_f32_16x16x32_bf16 v[180:183], a[28:31], v[28:31], v[180:183]\n\t" \
  "buffer_load_dwordx4 v[124:127], v211, %[rh], s79 offen offset:3072 sc1\n\t" \
  "v_mfma_f32_16x16x32_bf16 v[168:171], a[32:35], v[32:35], v[168:171]\n\t" \
  "v_mfma_f32_16x16x32_bf16 v[172:175], a[40:43], v[32:35], v[172:175]\n\t" \
  "s_waitcnt vmcnt(20)\n\t" \
  "v_swap_b32 v41, v44\n\t" \
  "v_mfma_f32_16x16x32_bf16 v[168:171], a[36:39], v[36:39], v[168:171]\n\t" \
  "v_swap_b32 v43, v46\n\t" \
  "v_mfma_f32_16x16x32_bf16 v[172:175], a[44:47], v[36:39], v[172:175]\n\t" \
  "s_nop 0\n\t" \
  "v_mfma_f32_16x16x32_bf16 v[176:179], a[32:35], v[40:43], v[176:179]\n\t" \
  "v_mfma_f32_16x16x32_bf16 v[180:183], a[40:43], v[40:43], v[180:183]\n\t" \
  "s_waitcnt vmcnt(18)\n\t" \
  "v_swap_b32 v49, v52\n\t" \
  "v_mfma_f32_16x16x32_bf16 v[176:179], a[36:39], v[44:47], v[176:179]\n\t" \
  "v_swap_b32 v51, v54\n\t" \
  "v_mfma_f32_16x16x32_bf16 v[180:183], a[44:47], v[44:47], v[180:183]\n\t" \
  "v_mov_b64 v[40:41], 0\n\t" \
  "v_mov_b64 v[42:43], 0\n\t" \
  "v_mfma_f32_16x16x32_bf16 v[168:171], a[48:51], v[48:51], v[168:171]\n\t" \
  "v_mov_b64 v[44:45], 0\n\t" \
  "v_mov_b64 v[46:47], 0\n\t" \
  "v_mfma_f32_16x16x32_bf16 v[172:175], a[56:59], v[48:51], v[172:175]\n\t" \
  "s_waitcnt vmcnt(16)\n\t" \
  "v_swap_b32 v57, v60\n\t" \
  "v_mfma_f32_16x16x32_bf16 v[168:171], a[52:55], v[52:55], v[168:171]\n\t" \
  "v_swap_b32 v59, v62\n\t" \
  "v_mfma_f32_16x16x32_bf16 v[172:175], a[60:63], v[52:55], v[172:175]\n\t" \
  "v_mov_b64 v[48:49], 0\n\t" \
  "v_mov_b64 v[50:51], 0\n\t" \
  "v_mfma_f32_16x16x32_bf16 v[176:179], a[48:51], v[56:59], v[176:179]\n\t" \
  "v_mov_b64 v[52:53], 0\n\t" \
  "v_mov_b64 v[54:55], 0\n\t" \
  "v_mfma_f32_16x16x32_bf16 v[180:183], a[56:59], v[56:59], v[180:183]\n\t" \
  "s_waitcnt vmcnt(14)\n\t" \
  "v_swap_b32 v65, v68\n\t" \
  "v_mfma_f32_16x16x32_bf16 v[176:179], a[52:55], v[60:63], v[176:179]\n\t" \
  "v_swap_b32 v67, v70\n\t" \
  "v_mfma_f32_16x16x32_bf16 v[180:183], a[60:63], v[60:63], v[180:183]\n\t" \
  "v_mov_b64 v[56:57], 0\n\t" \
  "v_mov_b64 v[58:59], 0\n\t" \
  "v_mfma_f32_16x16x32_bf16 v[168:171], a[64:67], v[64:67], v[168:171]\n\t" \
  "v_mov_b64 v[60:61], 0\n\t" \
  "v_mov_b64 v[62:63], 0\n\t" \
  "v_mfma_f32_16x16x32_bf16 v[172:175], a[72:75], v[64:67], v[172:175]\n\t" \
  "s_waitcnt vmcnt(12)\n\t" \
  "v_swap_b32 v73, v76\n\t" \
  "v_mfma_f32_16x16x32_bf16 v[168:171], a[68:71], v[68:71], v[168:171]\n\t" \
  "v_swap_b32 v75, v78\n\t" \
  "v_mfma_f32_16x16x32_bf16 v[172:175], a[76:79], v[68:71], v[172:175]\n\t" \
  "v_mov_b64 v[64:65], 0\n\t" \
  "v_mov_b64 v[66:67], 0\n\t" \
  "v_mfma_f32_16x16x32_bf16 v[176:179], a[64:67], v[72:75], v[176:179]\n\t" \
  "v_mov_b64 v[68:69], 0\n\t" \
  "v_mov_b64 v[70:71], 0\n\t" \
  "v_mfma_f32_16x16x32_bf16 v[180:183], a[72:75], v[72:75], v[180:183]\n\t" \
  "s_waitcnt vmcnt(10)\n\t" \
  "v_swap_b32 v81, v84\n\t" \
  "v_mfma_f32_16x16x32_bf16 v[176:179], a[68:71], v[76:79], v[176:179]\n\t" \
  "v_swap_b32 v83, v86\n\t" \
  "v_mfma_f32_16x16x32_bf16 v[180:183], a[76:79], v[76:79], v[180:183]\n\t" \
  "v_mov_b64 v[72:73], 0\n\t" \
  "v_mov_b64 v[74:75], 0\n\t" \
  "v_mfma_f32_16x16x32_bf16 v[168:171], a[80:83], v[80:83], v[168:171]\n\t" \
  "v_mov_b64 v[76:77], 0\n\t" \
  "v_mov_b64 v[78:79], 0\n\t" \
  "v_mfma_f32_16x16x32_bf16 v[172:175], a[88:91], v[80:83], v[172:175]\n\t" \
  "s_waitcnt vmcnt(8)\n\t" \
  "v_swap_b32 v89, v92\n\t" \
  "v_mfma_f32_16x16x32_bf16 v[168:171], a[84:87], v[84:87], v[168:171]\n\t" \
  "v_swap_b32 v91, v94\n\t" \
  "v_mfma_f32_16x16x32_bf16 v[172:175], a[92:95], v[84:87], v[172:175]\n\t" \
  "v_mov_b64 v[80:81], 0\n\t" \
  "v_mov_b64 v[82:83], 0\n\t" \
  "v_mfma_f32_16x16x32_bf16 v[176:179], a[80:83], v[88:91], v[176:179]\n\t" \
  "v_mov_b64 v[84:85], 0\n\t" \
  "v_mov_b64 v[86:87], 0\n\t" \
  "v_mfma_f32_16x16x32_bf16 v[180:183], a[88:91], v[88:91], v[180:183]\n\t" \
  "s_waitcnt vmcnt(6)\n\t" \
  "v_swap_b32 v97, v100\n\t" \
  "v_mfma_f32_16x16x32_bf16 v[176:179], a[84:87], v[92:95], v[176:179]\n\t" \
  "v_swap_b32 v99, v102\n\t" \
  "v_mfma_f32_16x16x32_bf16 v[180:183], a[92:95], v[92:95], v[180:183]\n\t" \
  "v_mov_b64 v[88:89], 0\n\t" \
  "v_mov_b64 v[90:91], 0\n\t" \
  "v_mfma_f32_16x16x32_bf16 v[168:171], a[96:99], v[96:99], v[168:171]\n\t" \
  "v_mov_b64 v[92:93], 0\n\t" \
  "v_mov_b64 v[94:95], 0\n\t" \
  "v_mfma_f32_16x16x32_bf16 v[172:175], a[104:107], v[96:99], v[172:175]\n\t" \
  "s_waitcnt vmcnt(4)\n\t" \
  "v_swap_b32 v105, v108\n\t" \
  "v_mfma_f32_16x16x32_bf16 v[168:171], a[100:103], v[100:103], v[168:171]\n\t" \
  "v_swap_b32 v107, v110\n\t" \
  "v_mfma_f32_16x16x32_bf16 v[172:175], a[108:111], v[100:103], v[172:175]\n\t" \
  "v_mov_b64 v[96:97], 0\n\t" \
  "v_mov_b64 v[98:99], 0\n\t" \
  "v_mfma_f32_16x16x32_bf16 v[176:179], a[96:99], v[104:107], v[176:179]\n\t" \
  "v_mov_b64 v[100:101], 0\n\t" \
  "v_mov_b64 v[102:103], 0\n\t" \
  "v_mfma_f32_16x16x32_bf16 v[180:183], a[104:107], v[104:107], v[180:183]\n\t" \
  "s_waitcnt vmcnt(2)\n\t" \
  "v_swap_b32 v113, v116\n\t" \
  "v_mfma_f32_16x16x32_bf16 v[176:179], a[100:103], v[108:111], v[176:179]\n\t" \
  "v_swap_b32 v115, v118\n\t" \
  "v_mfma_f32_16x16x32_bf16 v[180:183], a[108:111], v[108:111], v[180:183]\n\t" \
  "s_nop 0\n\t" \
  "s_add_i32 s85, s40, 1\n\t" \
  "s_and_b32 s82, s85, 2\n\t" \
  "s_lshl_b32 s82, s82, 13\n\t" \
  "s_and_b32 s80, s85, 1\n\t" \
  "s_lshl_b32 s80, s80, 12\n\t" \
  "s_add_i32 s82, s82, s80\n\t" \
  "s_add_i32 s82, s82, 0x84000\n\t" \
  "s_mov_b32 exec_hi, 0\n\t" \
  "buffer_load_dword v184, v200, %[rh], s82 offen sc1\n\t" \
  "buffer_load_dword v185, v201, %[rh], s52 offen sc1\n\t" \
  "s_mov_b64 exec, -1\n\t" \
  "v_mfma_f32_16x16x32_bf16 v[168:171], a[112:115], v[112:115], v[168:171]\n\t" \
  "v_mfma_f32_16x16x32_bf16 v[172:175], a[120:123], v[112:115], v[172:175]\n\t" \
  "s_waitcnt vmcnt(2)\n\t" \
  "v_swap_b32 v121, v124\n\t" \
  "v_mfma_f32_16x16x32_bf16 v[168:171], a[116:119], v[116:119], v[168:171]\n\t" \
  "v_swap_b32 v123, v126\n\t" \
  "v_mfma_f32_16x16x32_bf16 v[172:175], a[124:127], v[116:119], v[172:175]\n\t" \
  "s_nop 0\n\t" \
  "v_mfma_f32_16x16x32_bf16 v[176:179], a[112:115], v[120:123], v[176:179]\n\t" \
  "v_mfma_f32_16x16x32_bf16 v[180:183], a[120:123], v[120:123], v[180:183]\n\t" \
  "v_mfma_f32_16x16x32_bf16 v[176:179], a[116:119], v[124:127], v[176:179]\n\t" \
  "v_mfma_f32_16x16x32_bf16 v[180:183], a[124:127], v[124:127], v[180:183]\n\t" \
  "s_nop 6\n\t" \
  "39:\n\t" \
  "s_add_i32 s40, s40, 2\n\t" \
  "v_cmp_u_f32 vcc, v168, v176\n\t" \
  "s_cbranch_vccnz 32f\n\t" \
  "33:\n\t" \
  "s_barrier\n\t" \
  "s_add_i32 s85, s40, -1\n\t" \
  "s_cmp_ge_i32 s40, 64\n\t" \
  "s_cbranch_scc1 47f\n\t" \
  "47:\n\t" \
  "ds_write_b128 v199, v[168:171] offset:0\n\t" \
  "ds_write_b128 v199, v[172:175] offset:1024\n\t" \
  "ds_write_b128 v199, v[176:179] offset:8192\n\t" \
  "ds_write_b128 v199, v[180:183] offset:9216\n\t" \
  "s_mov_b32 s59, 0\n\t" \
  "s_and_b32 s58, s58, 4\n\t" \
  "s_cmp_lt_i32 s40, 64\n\t" \
  "s_cbranch_scc1 20b\n\t" \
  "s_waitcnt lgkmcnt(0)\n\t" \
  "s_barrier\n\t" \
  "s_mov_b32 s58, 64\n\t" \
  "s_branch 7f\n\t" \
  "22:\n\t" \
  "s_mov_b32 s81, 0\n\t" \
  "s_mov_b32 s54, s46\n\t" \
  "s_cmp_eq_u32 s40, 0\n\t" \
  "s_cbranch_scc1 24b\n\t" \
  "s_bitcmp1_b32 s58, 2\n\t" \
  "s_cbranch_scc1 24b\n\t" \
  "s_mov_b64 exec, -1\n\t" \
  "s_sleep 10\n\t" \
  "s_add_u32 s59, s59, 0x10000\n\t" \
  "s_cmp_lt_u32 s59, 0x400000\n\t" \
  "s_cbranch_scc1 19b\n\t" \
  "s_or_b32 s58, s58, 1\n\t" \
  "s_branch 7f\n\t" \
  "14:\n\t" \
  "v_mov_b32 v184, 0\n\t" \
  "s_bitcmp1_b32 s58, 2\n\t" \
  "s_nop 0\n\t" \
  "s_cbranch_scc1 15b\n\t" \
  "s_mov_b32 s84, 910\n\t" \
  "s_add_u32 s59, s59, 0x10000\n\t" \
  "s_cmp_lt_u32 s59, 0x400000\n\t" \
  "s_mov_b64 exec, vcc\n\t" \
  "buffer_load_dword v187, v200, %[rh], s78 offen sc1\n\t" \
  "s_mov_b32 exec_lo, -1\n\t" \
  "s_cbranch_scc1 13b\n\t" \
  "s_mov_b64 exec, -1\n\t" \
  "s_or_b32 s58, s58, 1\n\t" \
  "s_branch 7f\n\t" \
  "37:\n\t" \
  "s_barrier\n\t" \
  "s_barrier\n\t" \
  "s_add_i32 s40, s40, 2\n\t" \
  "s_and_b32 s58, s58, 4\n\t" \
  "s_branch 17b\n\t" \
  "32:\n\t" \
  "s_and_b32 s80, s58, 36\n\t" \
  "s_cbranch_scc1 33b\n\t" \
  "s_add_i32 s40, s40, -2\n\t" \
  "34:\n\t" \
  "s_or_b32 s59, s59, 128\n\t" \
  "s_and_b32 s76, s40, 3\n\t" \
  "s_lshl_b32 s76, s76, 18\n\t" \
  "s_add_i32 s77, s76, 0x10000\n\t" \
  "s_add_i32 s78, s76, 0x1000\n\t" \
  "s_add_i32 s79, s76, 0x11000\n\t" \
  "35:\n\t" \
  "buffer_load_dwordx4 v[0:3], v210, %[rh], s76 offen offset:0 sc1\n\t" \
  "buffer_load_dwordx4 v[4:7], v210, %[rh], s76 offen offset:1024 sc1\n\t" \
  "buffer_load_dwordx4 v[8:11], v210, %[rh], s77 offen offset:0 sc1\n\t" \
  "buffer_load_dwordx4 v[12:15], v210, %[rh], s77 offen offset:1024 sc1\n\t" \
  "buffer_load_dwordx4 v[16:19], v210, %[rh], s76 offen offset:2048 sc1\n\t" \
  "buffer_load_dwordx4 v[20:23], v210, %[rh], s76 offen offset:3072 sc1\n\t" \
  "buffer_load_dwordx4 v[24:27], v210, %[rh], s77 offen offset:2048 sc1\n\t" \
  "buffer_load_dwordx4 v[28:31], v210, %[rh], s77 offen offset:3072 sc1\n\t" \
  "buffer_load_dwordx4 v[32:35], v210, %[rh], s78 offen offset:0 sc1\n\t" \
  "buffer_load_dwordx4 v[36:39], v210, %[rh], s78 offen offset:1024 sc1\n\t" \
  "buffer_load_dwordx4 v[40:43], v210, %[rh], s79 offen offset:0 sc1\n\t" \
  "buffer_load_dwordx4 v[44:47], v210, %[rh], s79 offen offset:1024 sc1\n\t" \
  "buffer_load_dwordx4 v[48:51], v210, %[rh], s78 offen offset:2048 sc1\n\t" \
  "buffer_load_dwordx4 v[52:55], v210, %[rh], s78 offen offset:3072 sc1\n\t" \
  "buffer_load_dwordx4 v[56:59], v210, %[rh], s79 offen offset:2048 sc1\n\t" \
  "buffer_load_dwordx4 v[60:63], v210, %[rh], s79 offen offset:3072 sc1\n\t" \
  "buffer_load_dwordx4 v[64:67], v211, %[rh], s76 offen offset:0 sc1\n\t" \
  "buffer_load_dwordx4 v[68:71], v211, %[rh], s76 offen offset:1024 sc1\n\t" \
  "buffer_load_dwordx4 v[72:75], v211, %[rh], s77 offen offset:0 sc1\n\t" \
  "buffer_load_dwordx4 v[76:79], v211, %[rh], s77 offen offset:1024 sc1\n\t" \
  "buffer_load_dwordx4 v[80:83], v211, %[rh], s76 offen offset:2048 sc1\n\t" \
  "buffer_load_dwordx4 v[84:87], v211, %[rh], s76 offen offset:3072 sc1\n\t" \
  "buffer_load_dwordx4 v[88:91], v211, %[rh], s77 offen offset:2048 sc1\n\t" \
  "buffer_load_dwordx4 v[92:95], v211, %[rh], s77 offen offset:3072 sc1\n\t" \
  "buffer_load_dwordx4 v[96:99], v211, %[rh], s78 offen offset:0 sc1\n\t" \
  "buffer_load_dwordx4 v[100:103], v211, %[rh], s78 offen offset:1024 sc1\n\t" \
  "buffer_load_dwordx4 v[104:107], v211, %[rh], s79 offen offset:0 sc1\n\t" \
  "buffer_load_dwordx4 v[108:111], v211, %[rh], s79 offen offset:1024 sc1\n\t" \
  "buffer_load_dwordx4 v[112:115], v211, %[rh], s78 offen offset:2048 sc1\n\t" \
  "buffer_load_dwordx4 v[116:119], v211, %[rh], s78 offen offset:3072 sc1\n\t" \
  "buffer_load_dwordx4 v[120:123], v211, %[rh], s79 offen offset:2048 sc1\n\t" \
  "buffer_load_dwordx4 v[124:127], v211, %[rh], s79 offen offset:3072 sc1\n\t" \
  "s_waitcnt vmcnt(0)\n\t" \
  "s_bitcmp1_b32 s58, 2\n\t" \
  "s_cbranch_scc1 36f\n\t" \
  "v_max_u32 v148, v0, v1\n\t" \
  "v_max3_u32 v148, v148, v2, v3\n\t" \
  "v_max3_u32 v148, v148, v4, v5\n\t" \
  "v_max3_u32 v148, v148, v6, v7\n\t" \
  "v_max3_u32 v148, v148, v8, v9\n\t" \
  "v_max3_u32 v148, v148, v10, v11\n\t" \
  "v_max3_u32 v148, v148, v12, v13\n\t" \
  "v_max3_u32 v148, v148, v14, v15\n\t" \
  "v_max3_u32 v148, v148, v16, v17\n\t" \
  "v_max3_u32 v148, v148, v18, v19\n\t" \
  "v_max3_u32 v148, v148, v20, v21\n\t" \
  "v_max3_u32 v148, v148, v22, v23\n\t" \
  "v_max3_u32 v148, v148, v24, v25\n\t" \
  "v_max3_u32 v148, v148, v26, v27\n\t" \
  "v_max3_u32 v148, v148, v28, v29\n\t" \
  "v_max3_u32 v148, v148, v30, v31\n\t" \
  "v_max3_u32 v148, v148, v32, v33\n\t" \
  "v_max3_u32 v148, v148, v34, v35\n\t" \
  "v_max3_u32 v148, v148, v36, v37\n\t" \
  "v_max3_u32 v148, v148, v38, v39\n\t" \
  "v_max3_u32 v148, v148, v40, v41\n\t" \
  "v_max3_u32 v148, v148, v42, v43\n\t" \
  "v_max3_u32 v148, v148, v44, v45\n\t" \
  "v_max3_u32 v148, v148, v46, v47\n\t" \
  "v_max3_u32 v148, v148, v48, v49\n\t" \
  "v_max3_u32 v148, v148, v50, v51\n\t" \
  "v_max3_u32 v148, v148, v52, v53\n\t" \
  "v_max3_u32 v148, v148, v54, v55\n\t" \
  "v_max3_u32 v148, v148, v56, v57\n\t" \
  "v_max3_u32 v148, v148, v58, v59\n\t" \
  "v_max3_u32 v148, v148, v60, v61\n\t" \
  "v_max3_u32 v148, v148, v62, v63\n\t" \
  "v_max3_u32 v148, v148, v64, v65\n\t" \
  "v_max3_u32 v148, v148, v66, v67\n\t" \
  "v_max3_u32 v148, v148, v68, v69\n\t" \
  "v_max3_u32 v148, v148, v70, v71\n\t" \
  "v_max3_u32 v148, v148, v72, v73\n\t" \
  "v_max3_u32 v148, v148, v74, v75\n\t" \
  "v_max3_u32 v148, v148, v76, v77\n\t" \
  "v_max3_u32 v148, v148, v78, v79\n\t" \
  "v_max3_u32 v148, v148, v80, v81\n\t" \
  "v_max3_u32 v148, v148, v82, v83\n\t" \
  "v_max3_u32 v148, v148, v84, v85\n\t" \
  "v_max3_u32 v148, v148, v86, v87\n\t" \
  "v_max3_u32 v148, v148, v88, v89\n\t" \
  "v_max3_u32 v148, v148, v90, v91\n\t" \
  "v_max3_u32 v148, v148, v92, v93\n\t" \
  "v_max3_u32 v148, v148, v94, v95\n\t" \
  "v_max3_u32 v148, v148, v96, v97\n\t" \
  "v_max3_u32 v148, v148, v98, v99\n\t" \
  "v_max3_u32 v148, v148, v100, v101\n\t" \
  "v_max3_u32 v148, v148, v102, v103\n\t" \
  "v_max3_u32 v148, v148, v104, v105\n\t" \
  "v_max3_u32 v148, v148, v106, v107\n\t" \
  "v_max3_u32 v148, v148, v108, v109\n\t" \
  "v_max3_u32 v148, v148, v110, v111\n\t" \
  "v_max3_u32 v148, v148, v112, v113\n\t" \
  "v_max3_u32 v148, v148, v114, v115\n\t" \
  "v_max3_u32 v148, v148, v116, v117\n\t" \
  "v_max3_u32 v148, v148, v118, v119\n\t" \
  "v_max3_u32 v148, v148, v120, v121\n\t" \
  "v_max3_u32 v148, v148, v122, v123\n\t" \
  "v_max3_u32 v148, v148, v124, v125\n\t" \
  "v_max3_u32 v148, v148, v126, v127\n\t" \
  "v_cmp_eq_u32 vcc, -1, v148\n\t" \
  "s_cbranch_vccz 36f\n\t" \
  "s_add_u32 s59, s59, 0x1000000\n\t" \
  "s_cmp_lt_u32 s59, 0x10000000\n\t" \
  "s_cbranch_scc1 35b\n\t" \
  "s_or_b32 s58, s58, 9\n\t" \
  "s_branch 7f\n\t" \
  "36:\n\t" \
  "s_mov_b32 s97, 0\n\t" \
  "s_and_b32 s58, s58, 4\n\t" \
  "s_or_b32 s58, s58, 32\n\t" \
  "s_branch 31b\n\t" \
  "18:\n\t" \
  "v_mov_b32 v186, -1\n\t" \
  "s_and_b32 s80, s40, 3\n\t" \
  "s_mul_i32 s77, s80, 0xc000\n\t" \
  "s_add_i32 s77, s77, 0x40000\n\t" \
  "s_lshl_b32 s78, s80, 14\n\t" \
  "s_add_i32 s78, s78, 0x70000\n\t" \
  "s_mov_b32 s57, 0\n\t" \
  "s_add_i32 s96, s77, 0x1000\n\t" \
  "s_and_b32 s98, s40, 2\n\t" \
  "s_lshl_b32 s98, s98, 13\n\t" \
  "s_and_b32 s80, s40, 1\n\t" \
  "s_lshl_b32 s80, s80, 12\n\t" \
  "s_add_i32 s98, s98, s80\n\t" \
  "s_add_i32 s98, s98, 0x84000\n\t" \
  "12:\n\t" \
  "s_or_b32 s58, s58, 3\n\t" \
  "11:\n\t" \
  "buffer_load_dwordx4 v[0:3], v193, %[rh], s77 offen offset:0 sc1\n\t" \
  "buffer_load_dwordx4 v[4:7], v193, %[rh], s77 offen offset:1024 sc1\n\t" \
  "buffer_load_dwordx4 v[8:11], v193, %[rh], s77 offen offset:2048 sc1\n\t" \
  "buffer_load_dwordx4 v[12:15], v193, %[rh], s77 offen offset:3072 sc1\n\t" \
  "buffer_load_dwordx4 v[16:19], v193, %[rh], s96 offen offset:0 sc1\n\t" \
  "buffer_load_dwordx4 v[20:23], v193, %[rh], s96 offen offset:1024 sc1\n\t" \
  "buffer_load_dwordx4 v[24:27], v193, %[rh], s96 offen offset:2048 sc1\n\t" \
  "buffer_load_dwordx4 v[28:31], v193, %[rh], s96 offen offset:3072 sc1\n\t" \
  "buffer_load_dwordx4 v[32:35], v195, %[rh], s77 offen offset:0 sc1\n\t" \
  "buffer_load_dwordx4 v[36:39], v195, %[rh], s77 offen offset:2048 sc1\n\t" \
  "v_mov_b64 v[40:41], 0\n\t" \
  "v_mov_b64 v[42:43], 0\n\t" \
  "v_mov_b64 v[44:45], 0\n\t" \
  "v_mov_b64 v[46:47], 0\n\t" \
  "v_mov_b64 v[48:49], 0\n\t" \
  "v_mov_b64 v[50:51], 0\n\t" \
  "v_mov_b64 v[52:53], 0\n\t" \
  "v_mov_b64 v[54:55], 0\n\t" \
  "v_mov_b64 v[56:57], 0\n\t" \
  "v_mov_b64 v[58:59], 0\n\t" \
  "v_mov_b64 v[60:61], 0\n\t" \
  "v_mov_b64 v[62:63], 0\n\t" \
  "v_mov_b64 v[64:65], 0\n\t" \
  "v_mov_b64 v[66:67], 0\n\t" \
  "v_mov_b64 v[68:69], 0\n\t" \
  "v_mov_b64 v[70:71], 0\n\t" \
  "v_mov_b64 v[72:73], 0\n\t" \
  "v_mov_b64 v[74:75], 0\n\t" \
  "v_mov_b64 v[76:77], 0\n\t" \
  "v_mov_b64 v[78:79], 0\n\t" \
  "v_mov_b64 v[80:81], 0\n\t" \
  "v_mov_b64 v[82:83], 0\n\t" \
  "v_mov_b64 v[84:85], 0\n\t" \
  "v_mov_b64 v[86:87], 0\n\t" \
  "v_mov_b64 v[88:89], 0\n\t" \
  "v_mov_b64 v[90:91], 0\n\t" \
  "v_mov_b64 v[92:93], 0\n\t" \
  "v_mov_b64 v[94:95], 0\n\t" \
  "v_mov_b64 v[96:97], 0\n\t" \
  "v_mov_b64 v[98:99], 0\n\t" \
  "v_mov_b64 v[100:101], 0\n\t" \
  "v_mov_b64 v[102:103], 0\n\t" \
  "v_mov_b64 v[136:137], 0\n\t" \
  "v_mov_b64 v[138:139], 0\n\t" \
  "v_mov_b64 v[140:141], 0\n\t" \
  "v_mov_b64 v[142:143], 0\n\t" \
  "s_waitcnt vmcnt(0)\n\t" \
  "s_add_u32 s59, s59, 1\n\t" \
  "s_bitcmp1_b32 s58, 2\n\t" \
  "s_cbranch_scc1 10b\n\t" \
  "v_max_u32 v148, v0, v1\n\t" \
  "v_max3_u32 v148, v148, v2, v3\n\t" \
  "v_max3_u32 v148, v148, v4, v5\n\t" \
  "v_max3_u32 v148, v148, v6, v7\n\t" \
  "v_max3_u32 v148, v148, v8, v9\n\t" \
  "v_max3_u32 v148, v148, v10, v11\n\t" \
  "v_max3_u32 v148, v148, v12, v13\n\t" \
  "v_max3_u32 v148, v148, v14, v15\n\t" \
  "v_max3_u32 v148, v148, v16, v17\n\t" \
  "v_max3_u32 v148, v148, v18, v19\n\t" \
  "v_max3_u32 v148, v148, v20, v21\n\t" \
  "v_max3_u32 v148, v148, v22, v23\n\t" \
  "v_max3_u32 v148, v148, v24, v25\n\t" \
  "v_max3_u32 v148, v148, v26, v27\n\t" \
  "v_max3_u32 v148, v148, v28, v29\n\t" \
  "v_max3_u32 v148, v148, v30, v31\n\t" \
  "v_max3_u32 v148, v148, v32, v33\n\t" \
  "v_max3_u32 v148, v148, v34, v35\n\t" \
  "v_max3_u32 v148, v148, v36, v37\n\t" \
  "v_max3_u32 v148, v148, v38, v39\n\t" \
  "v_cmp_eq_u32 vcc, -1, v148\n\t" \
  "s_cbranch_vccz 10b\n\t" \
  "s_bitcmp1_b32 s59, 6\n\t" \
  "s_cbranch_scc0 11b\n\t" \
  "7:\n\t" \
  "92:\n\t" \
  "s_mov_b32 s42, s58\n\t" \
  "s_waitcnt vmcnt(0) lgkmcnt(0)\n\t" \
  ""

#define V53 "v0", "v1", "v2", "v3", "v4", "v5", "v6", "v7", "v8", "v9", "v10", "v11", "v12", "v13", "v14", "v15", "v16", "v17", \
  "v18", "v19", "v20", "v21", "v22", "v23", "v24", "v25", "v26", "v27", "v28", "v29", "v30", "v31", "v32", "v33", "v34", "v35", \
  "v36", "v37", "v38", "v39", "v40", "v41", "v42", "v43", "v44", "v45", "v46", "v47", "v48", "v49", "v50", "v51", "v52"
#define V100 V53, "v53", "v54", "v55", "v56", "v57", "v58", "v59", "v60", "v61", "v62", "v63", "v64", "v65", "v66", "v67", "v68", "v69", "v70", \
  "v71", "v72", "v73", "v74", "v75", "v76", "v77", "v78", "v79", "v80", "v81", "v82", "v83", "v84", "v85", "v86", "v87", "v88", \
  "v89", "v90", "v91", "v116", "v117", "v118", "v119"
#define STEP_PV
#define CB_CL "s60", "s61", "s62", "s63", "s64", "s65", "s66", "s67", "s68", "s69",
#define STEPS(BODY, ...) asm volatile(BODY : "=&{v[92:99]}"(X), "+{s[40:55]}"(P) STEP_PV \
  : "{v[120:151]}"(W.v[0]), "{v[152:183]}"(W.v[1]), "{v[184:215]}"(W.v[2]), "{v[216:247]}"(W.v[3]), "{v[100:115]}"(C), [rh] "s"(rh), [wv] "s"(w), [ru] "s"(rum), [sel] "s"(sel) __VA_ARGS__ \
  : "memory", "vcc", "scc", "s56", "s57", "s58", "s59", "s76", "s77", "s78", "s79", "s80", "s81", "s82", "s83", "s84", "s85", "s86", "s87", "s88", "s89", \
    "s90", "s91", "s92", "s93", "s94", "s95", "s96", "s97", V100)
__device__ __forceinline__ void keep_a(const Wts& W) {
#pragma unroll
  for (int i = 0; i < 32; ++i) asm volatile("" :: "a"(W.a[i]));
}
__device__ __forceinline__ char* lds_base(char* smem, int off) { asm volatile("" : "+v"(off)); return smem + off; }
__device__ __forceinline__ u32x2 tr4(unsigned p) { return __builtin_bit_cast(u32x2, __builtin_amdgcn_ds_read_tr16_b64_v4i16((LAS bf16x4*)(uintptr_t)p)); }
__device__ __forceinline__ float rb1(float x) { return __uint_as_float(pk2(0.f, x)); }

__device__ __forceinline__ void steps_init(u32x16& P, int h, int w, unsigned place) {
#pragma unroll
  for (int i = 0; i < 16; ++i) P[i] = 0;
  P[2] = 1; P[3] = CDUR; P[14] = H_MEM + (w >> 1) * CHK + (w & 1) * 4096 + (h & RHO) * 1024; P[15] = place;
}
__device__ __forceinline__ void steps_enter(u32x16& P, Clk& ck, bool dead) {
  ck.sync();
  P[2] = (P[2] & 3u) | (dead ? 4u : 0u); P[6] = ck.c2t << 15; P[11] = ck.ce; P[12] = ck.t2c; P[13] = ck.te;
#pragma unroll
  for (int i = 0; i < 16; ++i) P[i] = __builtin_amdgcn_readfirstlane(P[i]);
}

__device__ __forceinline__ void role_qk(const ChainArgs& a, int h, char* smem, const int w, Gate& G) {
  int lane = opaque_tid(0);
  const int b = h; (void)b;
  u64* prof = a.prof; (void)prof;
  PDECL(L_PRF, 4);
  const float CS = 0.08838834764831845f * 1.4426950408889634f;
  GAS unsigned* abortw = (GAS unsigned*)(a.sync + SY_ABORT);
  bool dead = false;
  Wts W;
  Clk ck; ck.init();
  u32x4 gv;
  if (QK_D) while (__builtin_amdgcn_s_memrealtime() - G.entry < QK_D) __builtin_amdgcn_s_sleep(8);
  load_weights<0>(a, h, w, lane, smem, W, G, gv);
  TP(1);
  if (!G.look(a.sync, gv)) return;
  PZERO;
  const Sel sel = G.sel;
  const __amdgpu_buffer_rsrc_t rh = rsrc_hot(a.sync, sel), rum = rsrc_u(a.sync);
  auto pm_o = [&](int l) { return 2 * CHK + h * 256 + (l & 1) * 128 + (l >> 3) * 16 + (l & 6) * 2; };
  auto fac = [&](int l, int hf) { return ((l & 7) >> 1) == 2 * (l >> 5) + ((h >> 1) & 1) && ((l >> 3) & 1) == hf ? 1.f : 0.f; };
  u32x16 C, P;
  {
    const int r = lane & 15, g = lane >> 4;
    const unsigned l0 = (unsigned)(uintptr_t)(LAS char*)smem;
    C[0] = lane * 16; C[1] = l0 + L_MEM + lane * 16; C[2] = l0 + L_WTS + w * 4096 + lane * 16; C[3] = C[2] + 65536;
    C[4] = (lane & 2) ? 0xEEEE : 0x4444;
    C[5] = l0 + L_XCH + (16 * w + 2 * (r >> 2) + (r & 1)) * 32 + g * 8 + ((r >> 1) & 1) * 4;
    C[6] = l0 + L_XK + (32 * w + r) * 16 + g * 4;
    C[7] = w == 3 ? H_PX + (h >> 3) * CHK + (h & 7) * 1024 + lane * 16 : w ? (2 * h + 1) * CHK + (w - 1) * 4096 + lane * 16 : l0 + L_XCH + (8 * g + (r >> 2)) * 32 + (r & 3) * 8;
    C[8] = l0 + L_XK + (8 * g + (r >> 2)) * 16 + (r & 1) * 8;
    C[9] = w ? (lane & 1) * 4096 : (lane == 0 ? H_ANN + h * 128 : 0x7fffffffu);
    C[10] = w == 0 ? (h >> 4) * CHK + (h & 15) * 512 + ((lane >> 1) & 1) * 256 + (lane >> 5) * 128 + (lane & 1) * 64 + ((lane >> 3) & 3) * 16 + ((lane >> 2) & 1) * 8 :
            w < 3 ? H_PX + ((w - 1) * 4 + (h >> 3)) * CHK + (h & 7) * 1024 + lane * 16 :
            lane < 32 ? (h >> 4) * CHK + (h & 15) * 512 + lane * 16 : (lane < 48 ? 2 * CHK + h * 256 + (lane - 32) * 16 : 0x7fffffffu);
    C[11] = w == 0 ? pm_o(lane) : (lane < (w == 3 ? 32 : 16) ? H_PX + (w == 3 ? 8 + g : 7 + w) * CHK + hpos(h) * 256 + r * 16 : 0x7fffffffu);
    C[12] = H_TR + (8 * w + (lane & 7)) * 128; C[13] = l0 + L_PRF + w * 1024 + (lane >> 6);
    unsigned mk = 0;
#pragma unroll
    for (int i = 0; i < 8; ++i) mk |= (16 * ((i >> 1) & 1) + 8 * (g & 1) + 4 * (g >> 1) + 2 * (i & 1) + (i >> 2) > 16 * (w - 1) + r ? 1u : 0u) << i;
    C[14] = w ? mk : __float_as_uint(fac(lane, 0)); C[15] = __float_as_uint(fac(lane, 1));
    asm volatile("" : "+v"(C));
  }
  steps_init(P, h, w, 2 * h * CHK + max(3 * w - 4, 0) * 1024);
  u32x4 k0[2];
  const unsigned seen = start_wait(a.sync, abortw, lane, dead, CT_Q + ct_near(sel) + h, 1u);
  if (w) P[3] = seen;
#pragma unroll
  for (int i = 0; i < 2; ++i) k0[i] = ld16(rum, x_slot(1, 0, h, sel) + (2 * w + i) * 1024 + lane * 16);
  G.arrive(a.sync);
#pragma unroll
  for (int i = 0; i < 2; ++i) *(u32x4*)(smem + L_KX + (2 * w + i) * 1024 + lane * 16) = k0[i];
  SSTAMP(96 + h);
  TP(2);
  GAS unsigned* kcp = ctr(a, CT_Q + ct_near(sel) + h);
  unsigned spins = 0;
  while (true) {
    u32x8 X; u32x16 pv; (void)pv;
    steps_enter(P, ck, dead);
    STEPS(QK_BODY, , [cs] "s"(CS), [kc] "s"(kcp));
    keep_a(W);
    const unsigned res = __builtin_amdgcn_readfirstlane(P[2]), s = __builtin_amdgcn_readfirstlane(P[4]);
    lane = opaque_tid(0);
    if (res & 8u) {
      auto sw = [](float v, int k) { return __int_as_float(k == 0 ? __builtin_amdgcn_mov_dpp(__float_as_int(v), 0xB1, 0xf, 0xf, false) :
                                                           k == 1 ? __builtin_amdgcn_mov_dpp(__float_as_int(v), 0x4E, 0xf, 0xf, false) :
                                                                    __builtin_amdgcn_mov_dpp(__float_as_int(v), 0x141, 0xf, 0xf, false)); };
      float x[5], d = fmaxf(__uint_as_float(X[4]), 0.f);
      d = fmaxf(d, sw(d, 0)); d = fmaxf(d, sw(d, 1)); d = fmaxf(d, sw(d, 2));
#pragma unroll
      for (int i = 0; i < 5; ++i) x[i] = __builtin_amdgcn_exp2f(__uint_as_float(X[i]) - d);
      float z = ((x[0] + x[1]) + (x[2] + x[3])) + x[4];
      z += sw(z, 0); z += sw(z, 1); z += sw(z, 2);
      z = __builtin_amdgcn_rcpf(z);
      const float y = sw(x[4], 0), f = __builtin_amdgcn_exp2f(-d) * z;
      const unsigned pm = (lane & 1) ? pk2(fac(lane, 0) * f, fac(lane, 1) * f) : pk2(x[4] * z, y * z);
      if (!(__uint_as_float(X[5]) < 1.0e18f))
        __builtin_amdgcn_raw_buffer_store_b32(nosent(pm), rh, pm_o(lane), H_P + ((s - 1) & 3) * 3 * CHK, ST_AUX);
    }
    if (res & 64u) break;
    if (res & 32u) {
      u32x4 k[3];
      P[3] = start_wait(a.sync, abortw, lane, dead, CT_Q + ct_near(sel) + h, s + 1);
#pragma unroll
      for (int i = 0; i < 3; ++i) k[i] = ld16(rum, x_slot(1, s, h, sel) + max(3 * w - 4 + i, 0) * 1024 + lane * 16);
#pragma unroll
      for (int i = 0; i < 3; ++i) *(u32x4*)(smem + L_KX + max(3 * w - 4 + i, 0) * 1024 + lane * 16) = k[i];
    }
    if (res & 17u) {
      spins |= 63u;
      if (dead || poll_fail(spins, abortw, lane)) dead = true;
    }
  }
  PDUMP(0);
}

__device__ __forceinline__ void role_vo(const ChainArgs& a, int h, char* smem, const int w, Gate& G) {
  int lane = opaque_tid(0);
  const int b = 32 + h; (void)b;
  u64* prof = a.prof; (void)prof;
  PDECL(L_PRF, 4);
  GAS unsigned* abortw = (GAS unsigned*)(a.sync + SY_ABORT);
  bool dead = false;
  Wts W;
  Clk ck; ck.init();
  u32x4 gv;
  load_weights<1>(a, h, w, lane, smem, W, G, gv);
  if (!G.look(a.sync, gv)) return;
  PZERO;
  const Sel sel = G.sel;
  const __amdgpu_buffer_rsrc_t rh = rsrc_hot(a.sync, sel), rum = rsrc_u(a.sync);
  u32x16 C, P;
  {
    const int r = lane & 15, g = lane >> 4;
    const unsigned l0 = (unsigned)(uintptr_t)(LAS char*)smem;
#pragma unroll
    for (int i = 0; i < 16; ++i) C[i] = H_TR + (8 * w + (lane & 7)) * 128;
    C[0] = lane * 16; C[1] = l0 + L_MEM + lane * 16; C[2] = l0 + L_WTS + w * 4096 + lane * 16; C[3] = C[2] + 65536;
    C[4] = (lane & 2) ? 0xEEEE : 0x4444;
    C[5] = l0 + L_XCH + (32 * w + r) * 16 + g * 4;
    C[6] = (lane & 8) ? 0xEEEE : 0x4444;
    C[7] = l0 + L_XCH + (32 * (g >> 1) + 8 * (g & 1) + (r >> 2)) * 16 + (r & 1) * 8;
    C[8] = ((g & 1) * 8 + (g >> 1)) * CHK + r * 16;
    C[13] = l0 + L_PRF + w * 1024 + (lane >> 6);
    asm volatile("" : "+v"(C));
  }
  start_wait(a.sync, abortw, lane, dead, -1, 0u);
  G.arrive(a.sync);
  SSTAMP(128 + h);
  steps_init(P, h, w, SY_UM + (16 * (h & 3) + 2 * w) * CHK + hpos(h) * 256);
  unsigned spins = 0;
  while (true) {
    u32x8 X; u32x16 pv; (void)pv;
    steps_enter(P, ck, dead);
    STEPS(VO_BODY);
    keep_a(W);
    lane = opaque_tid(0);
    if (__builtin_amdgcn_readfirstlane(P[2]) & 64u) break;
    spins |= 63u;
    if (dead || poll_fail(spins, abortw, lane)) dead = true;
  }
  PDUMP(0);
}

__device__ __forceinline__ void calibrate(const ChainArgs& a, int cal, int slot, u64 entry) {
  const u32x4 rs = own_rsrc(a.sync, 64 * PAGE);
  const unsigned so = cal * PAGE + (slot >> 1) * CHK + (slot & 1) * 4096;
  unsigned best[2] = {1u << 30, 1u << 30};
  bool sure;
  {
    unsigned z = 0;
    asm volatile("s_mov_b64 exec, 0xffff\n\tbuffer_atomic_or %0, %1, %2, %3 offen sc0 sc1\n\ts_mov_b64 exec, -1\n\ts_waitcnt vmcnt(0)"
                 : "+v"(z) : "v"((opaque_tid(0) & 8) * 1024 + (opaque_tid(0) & 7) * 256), "s"(rs), "s"(so) : "memory");
  }
  for (int i = 0;; ++i) {
    sure = min(best[0], best[1]) < CALT && max(best[0], best[1]) - min(best[0], best[1]) > 110u;
    if (i >= CALN && (sure || __builtin_amdgcn_s_memrealtime() - entry > CALM)) break;
#pragma unroll
    for (int hf = 0; hf < 2; ++hf) {
      u64 c0, c1;
      unsigned z = 0;
      asm volatile("s_mov_b64 exec, 1\n\ts_memtime %0\n\ts_waitcnt lgkmcnt(0)\n\tbuffer_atomic_or %2, off, %3, %4 sc0 sc1\n\ts_waitcnt vmcnt(0)\n\ts_memtime %1\n\ts_mov_b64 exec, -1\n\ts_waitcnt lgkmcnt(0)"
                   : "=&s"(c0), "=&s"(c1), "+v"(z) : "s"(rs), "s"(so + hf * 8192 + (i & 7) * 256) : "memory");
      best[hf] = min(best[hf], (unsigned)(c1 - c0));
    }
  }
  const u64 hs = (__builtin_amdgcn_s_memrealtime() & ~3ull) | (sure ? 2u : 0u) | ((best[1] < best[0] ? 1u : 0u) ^ die_half());
  if (opaque_tid(0) == 0) __hip_atomic_store((GAS u64*)ctr(a, CT_CAL + (slot >> 1)) + (slot & 1), hs, __ATOMIC_RELAXED, __HIP_MEMORY_SCOPE_AGENT);
}
__device__ __forceinline__ u32x4 pk8(f32x4 a, f32x4 b) { return (u32x4){pk2(a[0], a[1]), pk2(a[2], a[3]), pk2(b[0], b[1]), pk2(b[2], b[3])}; }
__device__ __forceinline__ unsigned pk2v(float a, float b) { unsigned r; asm volatile("v_cvt_pk_bf16_f32 %0, %1, %2" : "=v"(r) : "v"(a), "v"(b)); return r; }
__device__ __forceinline__ u32x4 pk8v(f32x4 a, f32x4 b) { asm volatile("s_nop 7"); return (u32x4){pk2v(a[0], a[1]), pk2v(a[2], a[3]), pk2v(b[0], b[1]), pk2v(b[2], b[3])}; }
__device__ __forceinline__ void st16g(u16* p, u32x4 v) { asm volatile("global_store_dwordx4 %0, %1, off " OUTP "\n\ts_nop 1" :: "v"(p), "v"(v) : "memory"); }
__device__ __forceinline__ void role_cb(const ChainArgs& a, int cbi, char* smem, const int w, Gate& G) {
  int lane = opaque_tid(0);
  const int b = 64 + cbi; (void)b;
  const int par = cbi >> 5, ci = cbi & 31, J = ci >> 3;
  u64* prof = a.prof; (void)prof;
  PDECL(65536, 8); PZERO;
  GAS unsigned* abortw = (GAS unsigned*)(a.sync + SY_ABORT);
  bool dead = false;
  const int tw = 2 * ci + (w & 1);
  const unsigned mo = (tw >> 5) * CHK + (tw & 31) * 256;
  Sel sel = 0;
  auto start = [&]() {
    lane = opaque_tid(0);
    SSTAMP(0 + ci);
    unsigned m0 = 0;
    if (par == 0 && w < 2) {
      const u16* m = a.mem0 + (2 * (lane >> 4) + ((lane >> 2) & 1)) * DM + 16 * tw + (lane & 8);
      m0 = m[lane & 3] | (unsigned)m[7 - (lane & 3)] << 16;
    }
    if (!G.look(a.sync, (u32x4){0u, 0u, 0u, 0u})) return false;
    sel = G.sel;
    SSTAMP(64 + ci);
    const __amdgpu_buffer_rsrc_t rh = rsrc_hot(a.sync, sel), rum = rsrc_u(a.sync);
    const unsigned s1v = ~(unsigned)(lane >> 6);
    const u32x4 sv = {s1v, s1v, s1v, s1v};
    auto um = [&](int k) {
#pragma unroll
      for (int i = 0; i < 4; ++i) st16(rum, near_half(SY_UM + (64 * k + tw) * CHK, sel) + (w >> 1) * 4096 + i * 1024 + lane * 16, sv);
    };
    um(par);
    const unsigned dv = cbi == H_DLT / CHK ? K_DEF * TU : s1v;
#pragma unroll
    for (int i = 0; i < 2; ++i) st16(rh, cbi >= 2 && cbi < 45 ? (cbi < 35 ? cbi * CHK : H_PX + (cbi - 35) * CHK) + w * 2048 + i * 1024 + lane * 16 : 0x7fffffffu, (u32x4){dv, s1v, s1v, s1v});
    if (par == 0 && w < 2) st4(rh, H_MEM + mo + lane * 4, nosent(m0));
    asm volatile("s_waitcnt vmcnt(0)" ::: "memory");
    lds_barrier();
    if (w == 0 && lane == 0) ctr_add(a, CT_INIT + J, 1u);
    SSTAMP(256 + ci);
    TP(6);
    G.arrive(a.sync);
    um(par + 2);
    {
      const __amdgpu_buffer_rsrc_t rx = mk_rsrc((void*)a.X, 4u << 20);
      u32x4 v[16];
#pragma unroll
      for (int i = 0; i < 16; ++i) v[i] = __builtin_amdgcn_raw_buffer_load_b128(rx, (unsigned)(cbi * 4096 + w * 1024 + lane * 16), (unsigned)(i * 262144), 0);
#pragma unroll
      for (int i = 0; i < 16; ++i) asm volatile("" :: "v"(v[i]));
    }
    SSTAMP(224 + ci);
    start_wait(a.sync, abortw, lane, dead, -1, 0u);
    return true;
  };
  if (!start()) return;
  Wts W;
#pragma unroll
  for (int i = 0; i < 32; ++i) asm volatile("" : "=a"(W.a[i]));
  u32x16 C, P; u32x8 C2;
  {
    const unsigned l0 = (unsigned)(uintptr_t)(LAS char*)smem, oor = 0x7fffffffu;
    const int r = lane & 7, t = (lane >> 3) & 1, g = lane >> 4, e = w & 1;
    const bool L = w < 2;
    C[0] = lane * 16;
    C[1] = L ? l0 + 32768 + e * 8192 + (lane & 2) * 512 + ((lane & 1) * 32 + (lane >> 2)) * 16 : e * CHK + t * 512 + (g & 1) * 256 + (r >> 2) * 128 + (g >> 1) * 64 + (r & 3) * 16;
    C[2] = L ? (4 * t + g) * 1024 + r * 16 : l0 + 81920 + e * 32768 + lane * 16;
    C[3] = L ? l0 + e * 1024 + lane * 8 : 2 * CHK + e * 4096 + (4 * (g >> 1) + 2 * t + (g & 1)) * 256 + 128 + r * 16;
    C[4] = ((16 * e + (lane >> 2)) * OUTW + DM + 32 * ci + 8 * (lane & 3)) * 2;
    C[5] = l0 + (L ? e * 8192 + lane * 16 : e * 512 + lane * 8); C[6] = l0 + 16384 + lane * 16; C[7] = l0 + 32768 + (L ? e * 8192 + 4096 : e * 2048) + lane * 16;
    C[8] = (lane & 31) * 128;
    C[9] = L ? (8 * (g & 1) + 4 * (g >> 1) + 2 * t + (r >> 2)) * 16 + 4 * (r < 4 ? r : 7 - r) : (8 * J + r) * 128;
    C[10] = (lane | e) ? oor : ci * 128;
    C[11] = L ? SY_CT + (CT_U + ct_near(sel) + 16 * e + (lane & 15)) * 128 : (t ? 0xEEEE : 0x4444); C[12] = L ? l0 + 81920 + e * 32768 + lane * 16 : ((lane & 2) ? 0xEEEE : 0x4444); C[13] = r < 4 ? 0x01000504u : 0x07060302u;
    C[14] = l0 + 49152 + 32 * e + (lane >> 6); C[15] = l0 + 49152 + (lane >> 6);
    C2[0] = L ? (lane < 16 ? lane * 16 : oor) : ux_tile(2 * ci) + lane * 16;
    C2[1] = lane == 0 && e && ci < 8 ? SY_CT + (CT_DONE + ct_near(~sel) + 8 * par + ci) * 128 : oor;
    C2[2] = (L ? ux_tile(2 * ci) : H_PX + 2 * e * CHK) + lane * 16;
#pragma unroll
    for (int n = 0; n < 4; ++n) C2[3 + n] = L ? H_PX + (8 + e) * CHK + (J ^ n) * 2048 + lane * 16 : C2[2] + CHK;
    C2[7] = l0 + 65536 + w * 2048 - 64 + (lane >> 6);
    asm volatile("" : "+v"(C), "+v"(C2));
#pragma unroll
    for (int i = 0; i < 16; ++i) P[i] = par - 2;
    P[2] = 48; P[6] = P[7] = K_DEF * TU; P[8] = SY_UM + tw * CHK + J * 2048; P[9] = H_MEM + mo; P[10] = 2 * CHK + J * 256; P[11] = e * 16 * UXH; P[13] = 1780; P[12] = H_DLT + par * 4096;
  }
  Clk ck; ck.init();
  unsigned spins = 0;
  while (true) {
    u32x16 pv; (void)pv;
    unsigned res = __builtin_amdgcn_readfirstlane(P[2]);
    if (res & 64u) break;
    const u32x4 rh2 = own_rsrc(a.sync + near_half(SY_HOT, sel), H_BYTES - 8192), ra = own_rsrc(a.sync, SY_BYTES), ro = own_rsrc(a.out, S_LEN * OUTW * 2);
    if (res & 1u) {
      spins |= 63u;
      if (dead || poll_fail(spins, abortw, opaque_tid(0))) dead = true;
    }
    ck.sync();
    P[2] = (res & 58u) | (dead ? 4u : 0u); P[1] = ck.ce; P[3] = ck.te; P[4] = ck.t2c; P[5] = ck.c2t << 15;
#pragma unroll
    for (int i = 0; i < 16; ++i) P[i] = __builtin_amdgcn_readfirstlane(P[i]);
    asm volatile(CB_BODY : "+{s[40:55]}"(P) STEP_PV
                 : "{v[192:207]}"(C), "{v[208:215]}"(C2), [rh] "s"(rh2), [ru] "s"(ra), [ro] "s"(ro), [wv] "s"(w), [sel] "s"(sel)
                 : "memory", "vcc", "scc", CB_CL "s56", "s57", "s58", "s59", "s76", "s77", "s78", "s79", "s80", "s81", "s82", "s83", "s84", "s85", "s86", "s87", "s88", "s89", "s90", "s91", "s92", "s93", "s94", "s95", "s96", "s97", "s98", "s99", "s100", "s101", V53, "v53", "v54", "v55", "v56", "v57", "v58", "v59", "v60", "v61", "v62", "v63", "v64", "v65", "v66", "v67", "v68", "v69", "v70", "v71", "v72", "v73", "v74", "v75", "v76", "v77", "v78", "v79", "v80", "v81", "v82", "v83", "v84", "v85", "v86", "v87", "v88", "v89", "v90", "v91", "v92", "v93", "v94", "v95", "v96", "v97", "v98", "v99", "v100", "v101", "v102", "v103", "v104", "v105", "v106", "v107", "v108", "v109", "v110", "v111", "v112", "v113", "v114", "v115", "v116", "v117", "v118", "v119", "v120", "v121", "v122", "v123", "v124", "v125", "v126", "v127", "v128", "v129", "v130", "v131", "v132", "v133", "v134", "v135", "v136", "v137", "v138", "v139", "v140", "v141", "v142", "v143", "v144", "v145", "v146", "v147", "v148", "v149", "v150", "v151", "v152", "v153", "v154", "v155", "v156", "v157", "v158", "v159", "v160", "v161", "v162", "v163", "v164", "v165", "v166", "v167", "v168", "v169", "v170", "v171", "v172", "v173", "v174", "v175", "v176", "v177", "v178", "v179", "v180", "v181", "v182", "v183", "v184", "v185", "v186", "v187", "v188", "v189", "v190", "v191");
    keep_a(W);
  }
  PDUMP(2);
}

__device__ __forceinline__ void bg_drain() {
  asm volatile("s_waitcnt vmcnt(0)" ::: "memory");
  __builtin_amdgcn_s_barrier();
  asm volatile("" ::: "memory");
}
#define BG_SLP 16
#define A3_SLP 48
__device__ __forceinline__ float blo(unsigned d) { return __uint_as_float(d << 16); }
__device__ __forceinline__ float bhi(unsigned d) { return __uint_as_float(d & 0xffff0000u); }
struct A1 {
  u32x4 kk[11], vv[8], sh[4];
  __device__ __forceinline__ void load(const ChainArgs& a, int x, int n) {
    const int lane = opaque_tid(0);
    const int d = (4 * x + (n & 3)) * 64 + 8 * (lane & 7), tb = (n >> 2) * 64 + 8 * (lane >> 3);
    const __amdgpu_buffer_rsrc_t rk = mk_rsrc(a.Ak, 4u << 20), rv = mk_rsrc(a.Av, 4u << 20), rs = mk_rsrc(a.Ashift, 8192);
#pragma unroll
    for (int i = 0; i < 4; ++i) sh[i] = __builtin_amdgcn_raw_buffer_load_b128(rs, d * 8 + 16 * i, 0, 0);
#pragma unroll
    for (int i = 0; i < 11; ++i) kk[i] = __builtin_amdgcn_raw_buffer_load_b128(rk, tb - 3 + i < 0 ? 0x7fffffffu : ((tb - 3 + i) * D_A + d) * 2, 0, 2);
#pragma unroll
    for (int i = 0; i < 8; ++i) vv[i] = __builtin_amdgcn_raw_buffer_load_b128(rv, ((tb + i) * D_A + d) * 2, 0, 2);
  }
  __device__ __forceinline__ void finish(const ChainArgs& a, int x, int n, char* T, Sel sel) {
    const int lane = opaque_tid(0);
#pragma unroll
    for (int c = 0; c < 8; ++c) {
      const unsigned t01 = sh[c >> 1][2 * (c & 1)], t23 = sh[c >> 1][2 * (c & 1) + 1];
      const float s0 = blo(t01), s1 = bhi(t01), s2 = blo(t23), s3 = bhi(t23);
      float kf[11];
#pragma unroll
      for (int i = 0; i < 11; ++i) kf[i] = (c & 1) ? bhi(kk[i][c >> 1]) : blo(kk[i][c >> 1]);
      u32x4 o;
#pragma unroll
      for (int i = 0; i < 8; i += 2) {
        float r[2];
#pragma unroll
        for (int j = 0; j < 2; ++j)
          r[j] = fmaf(kf[i + j + 3], s0, fmaf(kf[i + j + 2], s1, fmaf(kf[i + j + 1], s2, kf[i + j] * s3))) * ((c & 1) ? bhi(vv[i + j][c >> 1]) : blo(vv[i + j][c >> 1]));
        o[i >> 1] = pk2(r[0], r[1]);
      }
      *(u32x4*)(T + (8 * (lane & 7) + c) * 128 + (((lane >> 3) ^ (lane & 7)) * 16)) = o;
    }
    const __amdgpu_buffer_rsrc_t rkv = rsrc_u(a.sync);
#pragma unroll
    for (int i = 0; i < 8; ++i)
      st16(rkv, kv_row((4 * x + (n & 3)) * 64, sel) + (4 * i + (lane >> 4)) * CHK + ((lane >> 3) & 1) * 4096 + (n >> 2) * 128 + (lane & 7) * 16,
           *(const u32x4*)(T + (8 * i + (lane >> 3)) * 128 + (((lane & 7) ^ i) * 16)));
  }
};
#define WAH 4
#define DSP 4
#define SIGQ 2
__device__ __forceinline__ void task_w(const ChainArgs& a, int kind, int h, char* smem, int wv, Gate& G) {
  const int lane = opaque_tid(0), w = wv, r = lane & 15, g = lane >> 4;
  const __amdgpu_buffer_rsrc_t rw = mk_rsrc(kind == 0 ? a.Wq : (kind == 1 ? a.Wk : a.Wv), 8u << 20), rx = mk_rsrc(a.X, 4u << 20);
  const unsigned lds0 = (unsigned)(uintptr_t)(LAS char*)smem + w * 4096;
  unsigned vx[2], vw[4];
#pragma unroll
  for (int j = 0; j < 2; ++j) {
    const int rr = 8 * j + (lane >> 3);
    const int tb = kind == 0 ? rr : (kind == 1 ? 16 * ((rr >> 1) & 1) + 8 * ((rr >> 2) & 1) + 4 * (rr >> 3) + 2 * (rr & 1) : 8 * (rr >> 2) + (rr & 3));
    vx[j] = tb * DM * 2 + ((lane & 7) ^ (rr >> 1)) * 16;
  }
#pragma unroll
  for (int j = 0; j < 4; ++j) { const int row = (w * 4 + j) * 4 + g; vw[j] = row * HD * 2 + (r ^ swz_b(row)) * 16; }
  const u64 wo = kind == 2 ? bg_order(2) : (kind ? bg_order(1) : bg_order(0));
  auto xo = [&](int q) { return ((int)((wo >> (16 * q + 4 * w)) & 15) ^ (h & RHO)) * 128; };
  unsigned dT = (kind == 0 ? 16 : (kind == 1 ? 1 : 4)) * DM * 2;
  asm volatile("s_nop 4" : "+s"(dT));
  const unsigned vx0 = ((w & 1) ? vx[1] : vx[0]) + (w >> 1) * dT;
  auto st_w = [&](int n) {
#pragma unroll
    for (int j = 0; j < 4; ++j) dma16(rw, vw[j], (64 * ((int)((wo >> (4 * n)) & 15) ^ (h & RHO)) * HD + 128 * h) * 2, lds0 + (n & 7) * 16384 + j * 1024, W_NT & 16);
    dma16(rx, vx0, ((int)((wo >> (4 * n)) & 15) ^ (h & RHO)) * 128, lds0 - 3072 * w + 131072 + (n & 7) * 4096);
  };
  auto st_x = [&](int s, int b, int q, int j, bool pad) { dma16(rx, vx[j & 1], s * 65536 + (j >> 1) * dT + xo(q), lds0 + (b * 4 + q) * 16384 + j * 1024, false, pad); };
  if (kind < 2) {
    const __amdgpu_buffer_rsrc_t rs = mk_rsrc(a.Wv, 8u << 20);
#pragma unroll
    for (int n = 0; n < 4 * V_PF; ++n) dma16(rs, vw[n & 3], (64 * ((int)((bg_order(2) >> (4 * (V_PF0 + 2 * (n >> 2) + kind))) & 15) ^ (h & RHO)) * HD + 128 * h) * 2, lds0);
  }
  if (KQ_D && kind < 2) while (__builtin_amdgcn_s_memrealtime() - G.entry < KQ_D) __builtin_amdgcn_s_sleep(8);
  __builtin_amdgcn_s_waitcnt(0x0F70);
  u32x4 W[32][2], gv;
  char* xb[3][2];
#pragma unroll
  for (int i = 0; i < 6; ++i) xb[i >> 1][i & 1] = lds_base(smem, (i >> 1) * 65536 + (r >> 3) * 1024 + (r & 7) * 128 + ((4 * (i & 1) + g) ^ (r >> 1)) * 16);
  f32x4 acc[2][4];
#pragma unroll
  for (int i = 0; i < 4; ++i) acc[1][i] = vzero();
#pragma unroll
  for (int n = 0; n < WAH; ++n) st_w(n);
#pragma unroll
  for (int n = 0; n < 16; ++n) {
    asm volatile("s_waitcnt vmcnt(%0)" :: "n"(4 * WAH - 4 + (n < 16 - WAH ? WAH - 1 : 15 - n)) : "memory");
    lds_barrier();
    u32x4 f[4], y[4];
#pragma unroll
    for (int i = 0; i < 4; ++i) {
#pragma unroll
      for (int q = 0; q < 2; ++q) {
        const int row = (i & 1) * 32 + 8 * g + 4 * q + (r >> 2);
        const u32x2 u = __builtin_bit_cast(u32x2, __builtin_amdgcn_ds_read_tr16_b64_v4i16((LAS bf16x4*)(smem + (n & 7) * 16384 + row * 256 + (((4 * w + (r & 3)) ^ swz_b(row)) * 16) + 8 * (i >> 1))));
        f[i][2 * q] = u[0]; f[i][2 * q + 1] = u[1];
      }
      y[i] = *(const u32x4*)(xb[2][i & 1] + (n & 7) * 4096 + (i >> 1) * 2048);
    }
    SB0;
    if (n == 16 - WAH) G.ask(a.sync, gv);
    if (n == 15) asm volatile("" : "+v"(gv));
    if (n + WAH < 16) st_w(n + WAH);
    else {
#pragma unroll
      for (int j = 0; j < 4; ++j) st_x(1 + ((n + WAH - 16) >> 2), (n + WAH - 16) >> 2, (n + WAH) & 3, j, true);
    }
#pragma unroll
    for (int i = 0; i < 8; ++i)
      asm volatile("s_nop 1\n\tv_mfma_f32_16x16x32_bf16 %0, %1, %2, %0" : "+v"(acc[1][i & 3]) : "v"(f[2 * (i & 1) + (i >> 2)]), "v"(y[(i & 2) + (i >> 2)]));
#pragma unroll
    for (int i = 0; i < 4; ++i) { asm volatile("" : "+a"(f[i])); W[2 * n + (i & 1)][i >> 1] = f[i]; }
  }
  if (!G.look(a.sync, gv)) { bg_drain(); return; }
  TP(4);
  SSTAMP(160 + h);
  const Sel sel = G.sel;
  const __amdgpu_buffer_rsrc_t ru = mk_rsrc(a.sync, SY_BYTES);
  const unsigned sgo = (w | lane) ? 0x7fffffffu : SY_CT + (kind == 2 ? CT_V + ct_near(~sel) : CT_Q + 32 * kind + ct_near(sel)) * 128 + h * 128;
#pragma unroll
  for (int T = 0; T < 2; ++T) st16s(pk8v(acc[1][2 * T], acc[1][2 * T + 1]), ru, lane * 16 + T * 4096, x_slot(kind, 0, h, sel) + w * 1024);
  SB0;
  const u32x4 rx2 = own_rsrc(a.X, 4u << 20);
  unsigned vq[4][4];
#pragma unroll
  for (int i = 0; i < 16; ++i) { vq[i >> 2][i & 3] = vx[i & 1] + ((i >> 1) & 1) * dT + xo(i >> 2); asm volatile("" : "+v"(vq[i >> 2][i & 3])); }
  u32x4 xf[4][2], o;
  auto rd = [&](int b, int k, int T) { xf[k & 3][T] = *(const u32x4*)(xb[b][k & 1] + (k >> 1) * 4096 + T * 2048); };
  auto signal = [&](int s) {
    st4(ru, sgo, s);
    if (w == 0 && lane == 0 && s >= 0 && (s < PSEG || (s & 7) == 0 || ((s & 15) == 2 && PSEG > 2) || s > 61)) {
    }
  };
  if (kind < KQ_NOW) { bg_drain(); signal(0); }
  G.arrive(a.sync);
#pragma unroll
  for (int i = 4 * WAH; i < 28; ++i) st_x(1 + (i >> 4), i >> 4, (i >> 2) & 3, i & 3, true);
  wait_vm<26>();
  lds_barrier();
#pragma unroll
  for (int i = 0; i < 6; ++i) rd(0, i >> 1, i & 1);
  WST(kind == 2, 18);
  auto seg = [&](int s, int b) {
    const unsigned so = x_slot(kind, s - 1, h, sel) + w * 1024;
    WST(kind == 2 && s > 0 && s < 4, 18 + s);
#pragma unroll
    for (int k = 0; k < 32; ++k)
#pragma unroll
      for (int i = 0; i < 4; ++i) {
        if (k == 0) asm volatile("v_mfma_f32_16x16x32_bf16 %0, %1, %2, 0" : "=&v"(acc[b][i]) : "a"(W[k][i & 1]), "v"(xf[k & 3][i >> 1]));
        else asm volatile("v_mfma_f32_16x16x32_bf16 %0, %1, %2, %0" : "+v"(acc[b][i]) : "a"(W[k][i & 1]), "v"(xf[k & 3][i >> 1]));
        SB0;
        const int q = k >> 3, n = 4 * k + i;
        if ((k & 7) == 5 && i == 0) {
          if (q == SIGQ) wait_vm<4 * SIGQ>(); else wait_vm<20>();
          if (q == 1 && __builtin_expect(s - 2u < W_NOW, 0)) { wait_vm<4>(); __builtin_amdgcn_s_barrier(); signal(s - 1); }
          else if (q == 0 && b == 0 && __builtin_expect(s == 1, 0)) { wait_vm<32 - 4 * WAH>(); __builtin_amdgcn_s_barrier(); signal(0); }
          else __builtin_amdgcn_s_barrier();
          asm volatile("" ::: "memory");
        }
        if (i == 1 || i == 2) rd(k + 3 < 32 ? b : b ^ 1, (k + 3) & 31, i - 1);
        {
          const int m = (n + 128 - 23) & 127, j = (m & 31) / DSP, q0 = m >> 5;
          if (j < 4 && (n == 0 || ((m & 31) % DSP == 0 && n != 127))) asm volatile("s_add_i32 m0, %0, %1" :: "s"(lds0), "n"(((b ^ (q0 == 0) ^ (n < 23)) * 4 + ((q0 + 3) & 3)) * 16384 + j * 1024) : "m0", "scc");
          if (n == 0) asm volatile("s_nop 0");
          if (j < 4 && (m & 31) % DSP == 1)
            asm volatile("buffer_load_dwordx4 %0, %1, %2 offen lds" :: "v"(vq[(q0 + 3) & 3][j & 3]), "s"(rx2), "s"((s + 2 - (q0 == 0) - (n < 23)) * 65536) : "memory");
        }
        if (k == 8 * SIGQ + 5 && i == 2) signal(s - 1);
        if (k >= 1 && k < 5 && (i == 0 || i == 3)) { const f32x4 x = acc[b ^ 1][k - 1]; o[2 * (~k & 1) + (i & 1)] = pk2v(x[2 * (i & 1)], x[2 * (i & 1) + 1]); }
        if ((k == 2 || k == 4) && i == 3) { __builtin_amdgcn_raw_buffer_store_b128(o, ru, (unsigned)(lane * 16 + (k >> 2) * 4096), so, ST_AUX); asm volatile("s_nop 0" :: "v"(o)); }
        SB0;
      }
  };
  A1 a1;
#pragma unroll
  for (int i = 0; i < 11; ++i) asm volatile("" : "=v"(a1.kk[i]), "=v"(a1.vv[i & 7]), "=v"(a1.sh[i & 3]));
  const int wi = 32 * (2 - kind) + 4 * (h & 7) + w;
  for (int s = 1, ph = 0;; ++ph) {
    const int n = ph == 1 ? wi : (ph == 2 && wi >= 64 ? 32 + wi : 256);
    for (const int e = s + (ph ? A1_D : A1_0); s < e || ph == 4; s += 2) {
      seg(s, 0);
      if (s == NSEG - 1) break;
      seg(s + 1, 1);
    }
    if (ph == 4) break;
    if (ph == 3 && lane == 0) ctr_add(a, CT_A1 + ct_near(~sel) + (h >> 3), 1u);
    if (n < 128) a1.finish(a, h >> 3, n, smem + 131072 + w * 8192, sel);
    a1.load(a, h >> 3, ph == 0 ? wi : (ph == 1 && wi >= 64 ? 32 + wi : 256));
    SB0;
  }
#pragma unroll
  for (int T = 0; T < 2; ++T) st16s(pk8v(acc[0][2 * T], acc[0][2 * T + 1]), ru, lane * 16 + T * 4096, x_slot(kind, NSEG - 1, h, sel) + w * 1024);
  bg_drain();
  signal(NSEG - 1);
}
constexpr int A2_CSTR = 4160;
constexpr int A2_UOFF = 4 * A2_CSTR + 16 * 80;
constexpr int A2_USTR = 80 * 80;
constexpr int A2_WLDS = 4 * A2_CSTR + 2 * A2_USTR;
__device__ __forceinline__ void a2_wave(const ChainArgs& a, int d0, int d1, char* smem, int w, Sel sel) {
  const int lane = opaque_tid(0);
  char* Wb = smem + 1024 + w * A2_WLDS;
  char* U = Wb + A2_UOFF;
  const __amdgpu_buffer_rsrc_t rf = mk_rsrc((void*)a.Assm, 4u << 20), rd_ = mk_rsrc((void*)a.AD, 2048);
  const __amdgpu_buffer_rsrc_t rkv = rsrc_u(a.sync);
  u32x4 hi[4], uv[2][4];
  u32x2 lo[4];
  unsigned dn;
  auto ld_h = [&](int d) {
#pragma unroll
    for (int i = 0; i < 4; ++i) {
      hi[i] = __builtin_amdgcn_raw_buffer_load_b128(rf, (unsigned)((255 - 64 * i - lane) * 16), (unsigned)(d * 4096), 2);
      lo[i] = __builtin_amdgcn_raw_buffer_load_b64(rf, i == 3 && lane == 63 ? 0x7fffffffu : (255 - 64 * i - lane) * 16 - 8, (unsigned)(d * 4096), 2);
    }
    dn = __builtin_amdgcn_raw_buffer_load_b16(rd_, 0, 2 * d, 0);
  };
  auto ld_u = [&](int d) {
#pragma unroll
    for (int i = 0; i < 8; ++i) uv[i & 1][i >> 1] = __builtin_amdgcn_raw_buffer_load_b128(rkv, (unsigned)max(((i >> 1) * 64 + lane) * 16 - 8 * (i & 1), 0), kv_row(d, sel), LD_AUX);
  };
  ld_h(d0);
  {
    bool dead = false;
    ctr_wait<BG_SLP>(ctr(a, CT_A1 + ct_near(~sel) + (d0 >> 8)), 96u, (GAS unsigned*)(a.sync + SY_ABORT), lane, dead);
  }
  ld_u(d0);
  if (lane < 12) *(f32x4*)(Wb + (lane / 3) * A2_CSTR + (256 + lane % 3) * 16) = vzero();
#pragma unroll
  for (int i = 0; i < 4; ++i) if (64 * (i & 1) + lane < 80) *(f32x4*)(U + (i >> 1) * A2_USTR - 16 * 80 + (64 * (i & 1) + lane) * 16) = vzero();
  const int r = lane & 15, g = lane >> 4;
#pragma unroll 1
  for (int d = d0; d < d1; d += 96) {
    u64 at[6]; (void)at;
    AST(0);
#pragma unroll
    for (int i = 0; i < 4; ++i) {
      const unsigned src[6] = {lo[i][0], lo[i][1], hi[i][0], hi[i][1], hi[i][2], hi[i][3]};
      unsigned f[10];
#pragma unroll
      for (int x = 0; x < 10; ++x) {
        if ((x & 1) == 0) f[x] = __builtin_amdgcn_alignbit(src[5 - x / 2], src[5 - x / 2], 16);
        else f[x] = __builtin_amdgcn_perm(src[5 - (x + 1) / 2], src[5 - (x - 1) / 2], 0x07060100u);
      }
#pragma unroll
      for (int m = 0; m < 4; ++m) *(u32x4*)(Wb + m * A2_CSTR + (64 * i + lane) * 16) = (u32x4){f[3 - m], f[5 - m], f[7 - m], f[9 - m]};
    }
    if (lane == 0) uv[1][0] = (u32x4){0u, 0u, uv[1][0][0], uv[1][0][1]};
#pragma unroll
    for (int i = 0; i < 8; ++i) {
      const int ch = (i >> 1) * 64 + lane;
      *(u32x4*)(U + (i & 1) * A2_USTR + (ch >> 2) * 80 + (ch & 3) * 16) = uv[i & 1][i >> 1];
    }
    const float Dd = blo(dn);
    asm volatile("" :: "v"(Dd) : "memory");
    AST(1);
    ld_h(d + 96 < d1 ? d + 96 : D_A); ld_u(d + 96 < d1 ? d + 96 : D_A);
    f32x4 acc[4][2];
#pragma unroll
    for (int i = 0; i < 8; ++i) acc[i >> 1][i & 1] = vzero();
    const char* pa = Wb + (r & 3) * A2_CSTR + 16 * (255 - (r >> 2) + g) - 3 * 1024 - 64;
    const char* pb = U + (r - 1) * 80 + 16 * g;
    bf16x8 fa[2][4], fb[2][8];
    auto rd = [&](int b, int n, int k) {
      if (n == 0 || n > 8) fa[b][n ? n - 8 : 0] = *(const bf16x8*)(pa + 1024 * (3 - (n ? n - 8 : 0)) + 64 * (1 - k));
      else fb[b][n - 1] = *(const bf16x8*)(pb + ((n - 1) & 1) * A2_USTR + 80 * (16 * ((n - 1) >> 1) + 1 - k));
    };
#pragma unroll
    for (int n = 0; n < 12; ++n) rd(0, n, 0);
    AST(2);
#pragma unroll 1
    for (int k = 0; k < 16; k += 2) {
#pragma unroll
      for (int b = 0; b < 2; ++b) {
        int q = 0;
#pragma unroll
        for (int j = 0; j < 4; ++j)
#pragma unroll
          for (int i = j; i < 4; ++i)
#pragma unroll
            for (int mt = 0; mt < 2; ++mt) {
              asm volatile("v_mfma_f32_16x16x32_bf16 %0, %1, %2, %0" : "+v"(acc[i][mt]) : "v"(fa[b][j]), "v"(fb[b][2 * (i - j) + mt]));
              SB0;
              if (q < 12) rd(b ^ 1, q, 1);
              if (q == 12) { pa -= 64; pb -= 80; }
              ++q;
              SB0;
            }
      }
    }
    AST(3);
    u32x4 kv[4];
#pragma unroll
    for (int i = 0; i < 4; ++i) kv[i] = *(const u32x4*)(U + (16 * i + r) * 80 + 16 * g);
    asm volatile("s_nop 7\n\ts_waitcnt vmcnt(0)" ::: "memory");
#pragma unroll
    for (int i = 0; i < 8; ++i) asm volatile("" : "+v"(acc[i >> 1][i & 1]));
    if (lane == 0 && d > d0) ctr_add(a, CT_A2 + ct_near(~sel) + ((d - 96) >> 6), 1u);
#pragma unroll
    for (int i = 0; i < 4; ++i) {
      const f32x4 x = acc[i][1], y = acc[i][0];
      st16s((u32x4){pk2(fmaf(blo(kv[i][0]), Dd, x[0]), fmaf(bhi(kv[i][0]), Dd, x[1])), pk2(fmaf(blo(kv[i][1]), Dd, x[2]), fmaf(bhi(kv[i][1]), Dd, x[3])),
                    pk2(fmaf(blo(kv[i][2]), Dd, y[0]), fmaf(bhi(kv[i][2]), Dd, y[1])), pk2(fmaf(blo(kv[i][3]), Dd, y[2]), fmaf(bhi(kv[i][3]), Dd, y[3]))},
            rkv, (unsigned)(i * 1024 + r * 64 + g * 16), kv_row(d, sel));
    }
    AST(4);
  }
  wait_vm<0>();
  if (lane == 0) ctr_add(a, CT_A2 + ct_near(~sel) + ((d0 + (d1 - 1 - d0) / 96 * 96) >> 6), 1u);
}
__device__ __forceinline__ void task_u(const ChainArgs& a, int h, char* smem, int wv, Gate& G) {
  const int lane = opaque_tid(0);
  const int w = wv;
  GAS unsigned* abortw = (GAS unsigned*)(a.sync + SY_ABORT);
  bool dead = false;
  char* sb = smem + w * 32768;
  const __amdgpu_buffer_rsrc_t rwo = mk_rsrc(a.Wo, 8u << 20);
  const unsigned lds0 = (unsigned)(uintptr_t)(LAS char*)sb;
  unsigned wo4[4];
#pragma unroll
  for (int i = 0; i < 4; ++i) { const int row = i * 4 + (lane >> 4); wo4[i] = row * DM * 2 + (((lane & 15) ^ swz_b(row)) * 16); }
  asm volatile("s_nop 4");
  auto stage = [&](int n) {
    const int p = (U_ORD >> (4 * n)) & 3;
#pragma unroll
    for (int i = 0; i < 16; ++i)
      dma16(rwo, wo4[i & 3], (unsigned)(((128 * h + 64 * (p & 1) + 16 * (i >> 2)) * DM + 256 * w + 128 * (p >> 1)) * 2), lds0 + (n & 1) * 16384 + i * 1024, W_NT & 2);
  };
  __builtin_amdgcn_s_waitcnt(0x0F70);
  u32x4 W[16][4];
  stage(0); stage(1);
#pragma unroll
  for (int n = 0; n < 4; ++n) {
    const int p = (U_ORD >> (4 * n)) & 3;
    if (n < 3) wait_vm<16>(); else wait_vm<0>();
#pragma unroll
    for (int t = 0; t < 8; ++t) {
      const u32x8 f = tr_frag(sb + (n & 1) * 16384, t, lane);
      u32x4 f0 = {f[0], f[1], f[2], f[3]}, f1 = {f[4], f[5], f[6], f[7]};
      asm volatile("" : "+a"(f0), "+a"(f1));
      W[8 * (p >> 1) + t][2 * (p & 1)] = f0; W[8 * (p >> 1) + t][2 * (p & 1) + 1] = f1;
    }
    asm volatile("s_waitcnt lgkmcnt(0)" ::: "memory");
    if (n + 2 < 4) stage(n + 2);
  }
  lds_barrier();
  if (!G.pass(a.sync)) return;
  SSTAMP(192 + h);
  const Sel sel = G.sel;
  const u32x4 ru = own_rsrc(a.sync, SY_BYTES);
  u32x4 V[2][8], pc[2], o[2];
  f32x4 c[2][2];
  unsigned cv, cw, cx, vo[8], lk[2], sgo = (w | lane) ? 0x7fffffffu : SY_CT + (CT_U + ct_near(sel) + h) * 128, nd = (lane & 1) ? 3 - URING : 2;
#pragma unroll
  for (int i = 0; i < 8; ++i) { vo[i] = lane * 16 + ux_tile(16 * w + 2 * i); asm volatile("" : "+v"(vo[i])); }
#pragma unroll
  for (int i = 0; i < 2; ++i) lk[i] = SY_CT + (ct_near(~sel) + ((lane & 1) ? CT_DONE + 8 * i + (h & 7) : CT_V + h)) * 128;
  char* vl = lds_base(smem, lane * 16);
  const unsigned vl2 = lane * 16 + w * 2048;
  auto ld_v = [&](int s, bool pad = false) {
    if (pad) asm volatile("s_nop 4\n\tbuffer_load_dwordx4 %0, %2, %3, %4 offen sc1\n\tbuffer_load_dwordx4 %1, %2, %3, %4 offen offset:1024 sc1" : "=&v"(pc[0]), "=&v"(pc[1]) : "v"(vl2), "s"(ru), "s"(x_slot(2, s, h, sel)) : "memory");
    else asm volatile("buffer_load_dwordx4 %0, %2, %3, %4 offen sc1\n\tbuffer_load_dwordx4 %1, %2, %3, %4 offen offset:1024 sc1" : "=&v"(pc[0]), "=&v"(pc[1]) : "v"(vl2), "s"(ru), "s"(x_slot(2, s, h, sel)) : "memory");
  };
  auto signal = [&](int s) {
    asm volatile("buffer_store_dword %0, %1, %2, 0 offen sc1" :: "v"(s), "v"(sgo), "s"(ru) : "memory");
  };
  auto st_u = [&](int n, unsigned ub) { asm volatile("buffer_store_dwordx4 %0, %1, %2, %3 offen offset:%4 sc1" :: "v"(o[n & 1]), "v"(vo[n >> 1]), "s"(ru), "s"(ub), "n"((n & 1) * 1024) : "memory"); };
  auto fetch = [&](int s, bool seen) {
    if (s && !seen) { bg_drain(); signal(0); }
    if (!seen) ctr_wait<1>(ctr(a, CT_V + ct_near(~sel) + h), s + 1, abortw, lane, dead);
    ld_v(s, true);
    asm volatile("s_waitcnt vmcnt(0)" : "+v"(pc[0]), "+v"(pc[1]) :: "memory");
    if (seen) { __builtin_amdgcn_s_barrier(); signal(0); }
    WST(s == 0, 24);
    if (s == 0) TP(3);
#pragma unroll
    for (int i = 0; i < 2; ++i) *(u32x4*)(vl + 16384 + w * 2048 + i * 1024) = pc[i];
    lds_barrier();
#pragma unroll
    for (int i = 0; i < 8; ++i) V[s][i] = *(const u32x4*)(vl + 16384 + i * 1024);
  };
#pragma unroll
  for (int i = 0; i < 8; ++i) asm volatile("" : "=v"(V[0][i]));
  asm volatile("" : "=v"(cx));
  u64 bad = 0;
  unsigned up = 0x7f000000u;
  c[1][0] = c[1][1] = vzero();
  auto seg = [&](int s, int b) {
    const unsigned ub = ux_base(s, h, sel);
    WST(s >= 0 && s < 4, 25 + s);
#pragma unroll
    for (int n = 0; n < 16; ++n)
#pragma unroll
      for (int q = 0; q < 8; ++q) {
        const int kp = q >> 1, m = q & 1;
        if (kp == 0) asm volatile("v_mfma_f32_16x16x32_bf16 %0, %1, %2, 0" : "=&v"(c[n & 1][m]) : "v"(V[b][4 * m + kp]), "a"(W[n][kp]));
        else asm volatile("v_mfma_f32_16x16x32_bf16 %0, %1, %2, %0" : "+v"(c[n & 1][m]) : "v"(V[b][4 * m + kp]), "a"(W[n][kp]));
        SB0;
        if (q >= 2 && q < 6) { const f32x4 x = c[~n & 1][(q - 2) >> 1]; o[~n & 1][q - 2] = pk2v(x[2 * (q & 1)], x[2 * (q & 1) + 1]); }
        if (q == 6) st_u((n - 1) & 15, n ? ub : up);
        if (q == 5) asm volatile("" :: "v"(o[n & 1]));
        if (n == U_LD && q == 0) {
          if (__builtin_expect(bad != 0, 0)) {
            if (s <= U_NOW) {
              asm volatile("s_waitcnt vmcnt(%1)" : "+v"(cw) : "n"(U_LD - 2) : "memory");
              __builtin_amdgcn_s_barrier();
              signal(s - 1);
              bad = (int)__builtin_amdgcn_readfirstlane(cw) <= s;
            }
            if (bad & 1) ctr_wait<2>(ctr(a, CT_V + ct_near(~sel) + h), min(s + 2, NSEG), abortw, lane, dead);
            if (bad & 2) {
              ctr_wait<U_SLP>(ctr(a, CT_DONE + ct_near(~sel) + 8 * b + (h & 7)), max(s + 3 - URING, 0), abortw, lane, dead);
              for (int i = 0; i < U_STG * (h & 7) + U_ST0; ++i) __builtin_amdgcn_s_sleep(1);
            }
          }
          ld_v(s > 0 ? min(s + 1, NSEG - 1) : 0);
        }
        if (n == 1 && q == 7) asm volatile("buffer_load_dword %0, %1, %2, 0 offen sc1" : "=&v"(cw) : "v"(s <= U_NOW ? lk[0] : 0x7fffffffu), "s"(ru) : "memory");
        if (n == 13 && q == 0 && b == 0) asm volatile("buffer_load_dword %0, %1, %2, 0 offen sc1" : "=&v"(cx) : "v"(s == 0 ? lk[0] : 0x7fffffffu), "s"(ru) : "memory");
        if (n == U_LD && q == 7) asm volatile("buffer_load_dword %0, %1, %2, 0 offen sc1" : "=&v"(cv) : "v"(lk[b ^ 1]), "s"(ru) : "memory");
        if (n == U_SIG2 && q == 0) asm volatile("s_waitcnt vmcnt(%5)" : "+v"(pc[0]), "+v"(pc[1]), "+v"(cv), "+v"(cw), "+v"(cx) : "n"(U_SIG2 - U_LD - 1) : "memory");
        if (n == U_SIG2 && (q == 1 || q == 7)) *(u32x4*)(vl + (b ^ 1) * 8192 + w * 2048 + (q >> 2) * 1024) = pc[q >> 2];
        if (n == U_SIG2 + 1 && q == 0) { bad = __builtin_amdgcn_ballot_w64((int)cv < (s < U_NOW && lane > 1 ? NSEG : min(s + (int)nd, NSEG - 1))); asm volatile("" : "+s"(bad)); }
        if (n == U_SIG2 + 1 && q == 7) lds_barrier();
        if (n == U_SIG2 + 2 && q == 0) signal(s - 1);
        if ((n == U_SIG2 + 2 || n == U_SIG2 + 3) && (q & 1)) V[b ^ 1][4 * (n - U_SIG2 - 2) + (q >> 1)] = *(const u32x4*)(vl + (b ^ 1) * 8192 + (4 * (n - U_SIG2 - 2) + (q >> 1)) * 1024);
        SB0;
      }
    up = ub;
  };
  for (int s = 0; s < NSEG; s += 2) {
    if (__builtin_expect(s == 0, 0)) fetch(0, false);
    seg(s, 0);
    if (__builtin_expect(s == 0, 0)) {
      o[1] = pk8v(c[1][0], c[1][1]);
      st_u(15, up);
      asm volatile("s_waitcnt vmcnt(4)" : "+v"(cx) :: "memory");
      fetch(1, (int)__builtin_amdgcn_readfirstlane(cx) >= 1);
    }
    seg(s + 1, 1);
  }
  o[1] = pk8v(c[1][0], c[1][1]);
  st_u(15, up);
  bg_drain();
  signal(NSEG - 1);
}

__device__ __forceinline__ void task_a3(const ChainArgs& a, int dg, int t0, char* T, Sel sel) {
  const int lane = opaque_tid(0);
  const int d = dg * 64 + 8 * (lane & 7);
  const int tb = t0 + 8 * (lane >> 3);
  const __amdgpu_buffer_rsrc_t rz = rsrc_u(a.sync);
  u32x4 qq[16], zz[8][2];
#pragma unroll
  for (int i = 0; i < 16; ++i) qq[i] = LDNT((const u32x4*)(a.Aq + (long)(tb + 64 * (i >> 3) + (i & 7)) * D_A + d));
  {
    u32x4 zr[16];
    bool dead = false;
    ctr_wait<A3_SLP>(ctr(a, CT_A2 + ct_near(~sel) + dg), 64u, (GAS unsigned*)(a.sync + SY_ABORT), lane, dead);
  #pragma unroll
    for (int i = 0; i < 16; ++i) zr[i] = ld16(rz, kv_row(dg * 64, sel) + (2 * i + (lane >> 5)) * CHK + ((lane >> 4) & 1) * 4096 + (t0 + (lane & 15) * 8) * 2);
#pragma unroll
    for (int i = 0; i < 16; ++i) {
      const int row = 4 * i + (lane >> 4);
      *(u32x4*)(T + row * 256 + (((lane & 15) ^ (row >> 3)) * 16)) = zr[i];
    }
  }
#pragma unroll
  for (int c = 0; c < 8; ++c)
#pragma unroll
    for (int gq = 0; gq < 2; ++gq) zz[c][gq] = *(const u32x4*)(T + (8 * (lane & 7) + c) * 256 + (((gq * 8 + (lane >> 3)) ^ (lane & 7)) * 16));
#pragma unroll
  for (int i = 0; i < 16; ++i) {
    u32x4 o;
#pragma unroll
    for (int c = 0; c < 8; c += 2) {
      const unsigned z0 = zz[c][i >> 3][(i >> 1) & 3], z1 = zz[c + 1][i >> 3][(i >> 1) & 3];
      o[c >> 1] = pk2(blo(qq[i][c >> 1]) * ((i & 1) ? bhi(z0) : blo(z0)), bhi(qq[i][c >> 1]) * ((i & 1) ? bhi(z1) : blo(z1)));
    }
    st16g(a.out + (long)(tb + 64 * (i >> 3) + (i & 7)) * OUTW + d, o);
  }
}

__device__ __forceinline__ void part_a(const ChainArgs& a, char* smem, int x, int wi, int w, Sel sel) {
  char* T = smem + 1024 + w * A2_WLDS;
  if (wi >= 0) a2_wave(a, 256 * x + wi, 256 * x + 256, smem, w, sel);
  while (true) {
    unsigned t = 0;
    if (opaque_tid(0) == 0) t = __hip_atomic_fetch_add(ctr(a, CT_TK + ct_near(~sel) + x), 1u, __ATOMIC_RELAXED, __HIP_MEMORY_SCOPE_AGENT) + 1u;
    t = __builtin_amdgcn_readfirstlane(t);
    if (t >= 64) break;
    task_a3(a, 4 * x + (t >> 4), (t & 15) * 128, T, sel);
  }
}

__global__ __launch_bounds__(256, 1) void chain_kernel(ChainArgs a) {
  extern __shared__ __attribute__((aligned(16))) char smem[];
  const int b = blockIdx.x;
  {
    u32x4 rc;
    asm volatile("s_getpc_b64 s[60:61]\n\t.Lchain_here:\n\ts_sub_u32 s60, s60, .Lchain_here-_Z12chain_kernel9ChainArgs\n\ts_subb_u32 s61, s61, 0\n\ts_and_b32 s61, s61, 0xffff\n\t"
                 "s_mov_b32 s62, .Lchain_end-_Z12chain_kernel9ChainArgs\n\ts_mov_b32 s63, 0x20000\n\t"
                 "buffer_load_dwordx4 a[252:255], %1, s[60:63], 0 offen\n\tbuffer_load_dwordx4 a[252:255], %1, s[60:63], s63 offen"
                 : "={s[60:63]}"(rc) : "v"((b >> 3) * 4096 + threadIdx.x * 16) : "scc", "a252", "a253", "a254", "a255");
  }
  int w = __builtin_amdgcn_readfirstlane(threadIdx.x >> 6);
  Gate G;
  G.entry = __builtin_amdgcn_s_memrealtime(); G.open = false; G.arr = w == 0 ? b & 7 : -1;
  G.rot = ((unsigned)__builtin_amdgcn_s_getreg(20 | (3 << 11)) - b + WRONG) & 7u;
  {
    volatile LAS unsigned* t = (volatile LAS unsigned*)smem;
    if (w == 0) { t[0] = (unsigned)G.entry; t[1] = (unsigned)(G.entry >> 32); }
    __syncthreads();
    G.entry = (u64)(unsigned)__builtin_amdgcn_readfirstlane(t[0]) | ((u64)(unsigned)__builtin_amdgcn_readfirstlane(t[1]) << 32);
    __syncthreads();
  }
  G.tx = G.entry;
  if (((b + G.rot) & 4) == 0 && b < 128) {
    const int c = (b >> 6) * 32 + 8 * ((b + G.rot) & 3) + ((b >> 3) & 7);
    if (w < 2) calibrate(a, w ? 32 + (0x3200 >> (4 * (c & 3)) & 3) : c, 2 * c + w, G.entry);
    __builtin_amdgcn_s_barrier();
  }
  if (b == 0) {
    const int lane = opaque_tid(0);
    const __amdgpu_buffer_rsrc_t rs = mk_rsrc(a.sync + SY_SET, 3 * CHK);
    st16(rs, CHK + w * 8192 + lane * 128, (u32x4){SENT, SENT, SENT, SENT});
    if (w == 0 && lane >= 2) st16(rs, lane * 128, (u32x4){SENT, SENT, SENT, SENT});
    if (w == 3) {
      u32x4 v;
      bool ok;
      do {
        v = ld16(mk_rsrc(a.sync + SY_CT + CT_CAL * 128, 8192), lane * 128);
        ok = ((u64)v[1] << 32 | v[0]) - G.entry + 2000u < (1ull << 31) && ((u64)v[3] << 32 | v[2]) - G.entry + 2000u < (1ull << 31);
      } while (!__all(ok) && __builtin_amdgcn_s_memrealtime() - G.entry < 2 * CALM);
      const u64 k = lane == 32 ? 0x3333333333333333ull : (lane == 34 || lane == 35 ? 0x1111111111111111ull << (lane & 3) : 0ull);
      const int n1 = __builtin_popcountll(k & __builtin_amdgcn_ballot_w64(ok && (v[2] & 3u) == 3u)), n0 = __builtin_popcountll(k & __builtin_amdgcn_ballot_w64(ok && (v[2] & 3u) == 2u));
      if (!ok) v[0] = 0;
      if (n0 != n1) v[0] = n1 > n0 ? 3u : 2u;
      const unsigned t = (v[0] ^ lane) & 1u;
      const bool most = __builtin_popcountll(__builtin_amdgcn_ballot_w64((v[0] & 2u) && t)) > __builtin_popcountll(__builtin_amdgcn_ballot_w64((v[0] & 2u) && !t));
      const u64 m = __builtin_amdgcn_ballot_w64((v[0] & 2u) ? v[0] & 1u : (lane ^ most) & 1u);
      if (lane == 0) __hip_atomic_store((GAS u64*)(a.sync + SY_SEL), m, __ATOMIC_RELAXED, __HIP_MEMORY_SCOPE_AGENT);
    }
    bg_drain();
    if (w == 0 && lane == 0) __hip_atomic_store((GAS u64*)(a.sync + SY_EPOCH), (__builtin_amdgcn_s_memrealtime() & ~7ull) | G.rot, __ATOMIC_RELAXED, __HIP_MEMORY_SCOPE_AGENT);
  }
  while (true) {
    const unsigned rot = G.rot;
    asm volatile("" : "+s"(w));
    const int x = (b + rot) & 7, j = b >> 3, h = 8 * (x & 3) + (j & 7), role = (x & 4) + (j >> 3);
    if ((role == 0 || role == 7) && w >= 2) {
      const int n = opaque_tid(w - 2);
      const unsigned x0 = __builtin_amdgcn_raw_buffer_load_b32(mk_rsrc(a.sync, 106u << 20), n * PAGE + 4096 + (j & 31) * 128, 0, 2);
      const unsigned x1 = __builtin_amdgcn_raw_buffer_load_b32(mk_rsrc(a.out, 8u << 20), n * PAGE + (j & 31) * 128, 0, 2);
      asm volatile("" :: "v"(x0), "v"(x1));
    }
    if (role < 2) role_cb(a, 32 * role + h, smem, w, G);
    else if (role == 2) role_qk(a, h, smem, w, G);
    else if (role == 3) role_vo(a, h, smem, w, G);
    else if (role == 4) task_u(a, h, smem, w, G);
    else task_w(a, 7 - role, h, smem, w, G);
    if (G.rot == rot && role >= 4 && !NOA) part_a(a, smem, x & 3, 4 * (j - 8) + w, w, G.sel);
    if (G.rot == rot) break;
    __syncthreads();
  }
  asm volatile(".Lchain_end:");
}

extern "C" void kernel_launch(void* const* d_in, const int* in_sizes, int n_in,
                              void* d_out, int out_size, void* d_ws, size_t ws_size,
                              hipStream_t stream) {
  const u16* A_q = (const u16*)d_in[0];
  const u16* A_k = (const u16*)d_in[1];
  const u16* A_v = (const u16*)d_in[2];
  const u16* A_shift = (const u16*)d_in[3];
  const u16* A_ssmk = (const u16*)d_in[4];
  const u16* A_D = (const u16*)d_in[5];
  const u16* B_x = (const u16*)d_in[6];
  const u16* B_mem0 = (const u16*)d_in[7];
  const u16* B_Wq = (const u16*)d_in[8];
  const u16* B_Wk = (const u16*)d_in[9];
  const u16* B_Wv = (const u16*)d_in[10];
  const u16* B_Wo = (const u16*)d_in[11];
  u16* out = (u16*)d_out;
  char* ws = (char*)(((uintptr_t)d_ws + (2u << 20) - 1) & ~(uintptr_t)((2u << 20) - 1)) + WSOFF * MB;
  hipFuncSetAttribute((const void*)chain_kernel, hipFuncAttributeMaxDynamicSharedMemorySize, CHAIN_LDS);
  ChainArgs ca;
  ca.prof = (u64*)((char*)d_ws + PROF_AT);
  ca.mem0 = B_mem0; ca.Wq = B_Wq; ca.Wk = B_Wk; ca.Wv = B_Wv; ca.Wo = B_Wo; ca.X = B_x;
  ca.out = out; ca.sync = ws;
  ca.Aq = A_q; ca.Ak = A_k; ca.Av = A_v; ca.Ashift = A_shift; ca.Assm = A_ssmk; ca.AD = A_D;
  chain_kernel<<<256, 256, CHAIN_LDS, stream>>>(ca);
}
```

```cpp
#include <hip/hip_runtime.h>
#include <hip/hip_bf16.h>
#include <stdint.h>
#include <type_traits>

constexpr int S_LEN = 2048;
constexpr int D_A   = 1024;
constexpr int DM    = 1024;
constexpr int NH    = 32;
constexpr int DH    = 128;
constexpr int HD    = NH * DH;
constexpr int MEMR  = 8;
constexpr int SEG   = 32;
constexpr int NSEG  = S_LEN / SEG;
constexpr int OUTW  = 2048;

typedef unsigned short u16;
typedef unsigned long long u64;
typedef short bf16x8 __attribute__((ext_vector_type(8)));
typedef short bf16x4 __attribute__((ext_vector_type(4)));
typedef float f32x4 __attribute__((ext_vector_type(4)));
typedef float f32x16 __attribute__((ext_vector_type(16)));
typedef unsigned u32x4 __attribute__((ext_vector_type(4)));
typedef unsigned u32x2 __attribute__((ext_vector_type(2)));
#define LAS __attribute__((address_space(3)))
#define GAS __attribute__((address_space(1)))

constexpr size_t MB = 1u << 20;
constexpr size_t PROF_AT = 0;

__device__ __forceinline__ float bf2f(u16 b) { return __uint_as_float(((unsigned)b) << 16); }
__device__ __forceinline__ u16 f2bf(float f) { __bf16 b = (__bf16)f; return __builtin_bit_cast(u16, b); }
__device__ __forceinline__ float rbf(float f) { return bf2f(f2bf(f)); }
__device__ __forceinline__ unsigned pk2(float a, float b) {
  typedef float f2 __attribute__((ext_vector_type(2)));
  typedef __bf16 b2 __attribute__((ext_vector_type(2)));
  f2 v = {a, b}; b2 r = __builtin_convertvector(v, b2); return __builtin_bit_cast(unsigned, r);
}

__device__ __forceinline__ int opaque_tid(int wv) {
  int l;
  asm volatile("v_mbcnt_lo_u32_b32 %0, -1, 0\n\tv_mbcnt_hi_u32_b32 %0, -1, %0" : "=v"(l));
  return wv * 64 + l;
}
__device__ __forceinline__ int swz_b(int row) { return ((row & 3) << 2) | ((row >> 2) & 3); }

#define KNOB(name, v) constexpr int name = v
#define KDEF 70
KNOB(K_DEF, KDEF);
KNOB(W_STEP, 33); KNOB(W_OFF, 56);
#define RHOV 3
KNOB(RHO, RHOV);
#define CDUR 1510
#define U_SLP 8
#define U_STG 0
#define U_ST0 0
#define WSOFF 122
#define START_SLP 3
#define QK_D 0
#define A1_0 20
#define A1_D 4
#define PSEG 1
#define KQ_NOW 2
#define NOA 0
#define V_PF 3
#define V_PF0 3
#define CALN 3
#define CALM 500u
#define CALT 680u
#define KQ_D 0
#define W_NOW 4
#define U_SIG2 11
#define U_LD 5
#define U_NOW 6
#define WRONG 0
#define OUTP "sc1"
#define LDNT(p) __builtin_nontemporal_load(p)
constexpr int CHK      = 16384;
constexpr int PAGE     = 2 << 20;
constexpr int SY_UM    = 34 * PAGE;
constexpr int SY_HOT   = 32 * PAGE;
constexpr int H_MEM    = 0;
constexpr int H_P      = 16 * CHK;
constexpr int H_ANN    = 28 * CHK;
constexpr int H_DLT    = 32 * CHK;
constexpr int H_TR     = 33 * CHK;
constexpr int H_PX     = 64 * CHK;
constexpr int H_BYTES  = 128 * CHK;
constexpr int URING    = 16;
constexpr int SY_SET   = 52 * PAGE;
constexpr int SY_EPOCH = SY_SET + 128;
constexpr int SY_ABORT = SY_SET + 256;
constexpr int SY_SEL   = SY_EPOCH + 8;
constexpr int SY_CT    = SY_SET + CHK;
constexpr int CT_Q = 0, CT_K = 32, CT_U = 128;
constexpr int CT_V = 0, CT_DONE = 32, CT_A2 = 48, CT_A1 = 128, CT_TK = 132;
constexpr int CT_ARR = -124, CT_INIT = -116, CT_CAL = 256;
constexpr int SY_KV    = 48 * PAGE;
constexpr int SY_BYTES = SY_CT + (CT_CAL + 64) * 128;
constexpr unsigned SENT = 0xFFFFFFFFu;
constexpr unsigned SPIN_CAP = 1u << 19;
constexpr int NLF = 16;
constexpr int L_WTS = 0;
constexpr int L_MEM = L_WTS + 4 * NLF * 2048;
constexpr int L_XCH = L_MEM + 16384;
constexpr int L_XK  = L_XCH + 2048;
constexpr int L_KX  = L_XCH + 4096;
constexpr int L_PRF = L_KX + 8192;
constexpr int XRS = 40;
constexpr int CHAIN_LDS = 160 * 1024;
static_assert(L_XCH + 128 * XRS <= L_PRF && L_PRF + 4096 <= CHAIN_LDS, "lds");

typedef __bf16 bfv8 __attribute__((ext_vector_type(8)));
typedef __bf16 bfv16 __attribute__((ext_vector_type(16)));
typedef unsigned u32x8 __attribute__((ext_vector_type(8)));
typedef unsigned u32x12 __attribute__((ext_vector_type(12)));
typedef unsigned u32x16 __attribute__((ext_vector_type(16)));
typedef unsigned u32x32 __attribute__((ext_vector_type(32)));
struct Wts { u32x8 a[32]; u32x32 v[4]; };

#define STAMPL(i) do {} while (0)
#define PVAL(i, v) do {} while (0)
#define PDECL(base, str) do {} while (0)
#define PZERO do {} while (0)
#define PDUMP(mask) do {} while (0)
#define TA(x) do {} while (0)
#define TB(x) TS(x)
#define TS(x) do {} while (0)
#define SSTAMP(i) do {} while (0)
#define WST(c, i) do {} while (0)
#define AST(i) do {} while (0)
struct ChainArgs {
  u64* prof;
  const u16* mem0; const u16* Wq; const u16* Wk; const u16* Wv; const u16* Wo; const u16* X;
  u16* out; char* sync;
  const u16* Aq; const u16* Ak; const u16* Av; const u16* Ashift; const u16* Assm; const u16* AD;
};
__device__ __forceinline__ GAS unsigned* ctr(const ChainArgs& a, int i) { return (GAS unsigned*)(a.sync + SY_CT + i * 128); }
__device__ __forceinline__ void ctr_add(const ChainArgs& a, int i, unsigned v) {
  __hip_atomic_fetch_add(ctr(a, i), v, __ATOMIC_RELAXED, __HIP_MEMORY_SCOPE_AGENT);
}

__device__ __forceinline__ __amdgpu_buffer_rsrc_t mk_rsrc(const void* p, unsigned bytes) {
  const u64 v = (u64)p;
  const unsigned lo = (unsigned)__builtin_amdgcn_readfirstlane((unsigned)v), hi = (unsigned)__builtin_amdgcn_readfirstlane((unsigned)(v >> 32));
  const u64 u = (u64)lo | ((u64)hi << 32);
  return __builtin_amdgcn_make_buffer_rsrc((void*)u, 0, bytes, 0x00020000);
}
__device__ __forceinline__ u32x4 own_rsrc(const void* p, unsigned bytes) {
  u32x4 r = {(unsigned)__builtin_amdgcn_readfirstlane((unsigned)(u64)p), (unsigned)__builtin_amdgcn_readfirstlane((unsigned)((u64)p >> 32)) & 0xffffu, bytes, 0x00020000u};
  asm volatile("s_nop 4" : "+s"(r));
  return r;
}
constexpr int LD_AUX = 16, ST_AUX = 16;
__device__ __forceinline__ u32x4 ld16(__amdgpu_buffer_rsrc_t rs, unsigned off) {
  return __builtin_amdgcn_raw_buffer_load_b128(rs, off, 0, LD_AUX);
}
__device__ __forceinline__ void st16(__amdgpu_buffer_rsrc_t rs, unsigned off, u32x4 v) {
  __builtin_amdgcn_raw_buffer_store_b128(v, rs, off, 0, ST_AUX);
}
__device__ __forceinline__ void st16s(u32x4 v, __amdgpu_buffer_rsrc_t rs, unsigned voff, unsigned soff) {
  __builtin_amdgcn_raw_buffer_store_b128(v, rs, voff, soff, ST_AUX);
  asm volatile("s_nop 1" :: "v"(v) : "memory");
}
__device__ __forceinline__ void st8(__amdgpu_buffer_rsrc_t rs, unsigned off, u32x2 v) {
  __builtin_amdgcn_raw_buffer_store_b64(v, rs, off, 0, ST_AUX);
}
__device__ __forceinline__ void st4(__amdgpu_buffer_rsrc_t rs, unsigned off, unsigned v) {
  __builtin_amdgcn_raw_buffer_store_b32(v, rs, off, 0, ST_AUX);
}
__device__ __forceinline__ unsigned ld4(__amdgpu_buffer_rsrc_t rs, unsigned off) {
  return __builtin_amdgcn_raw_buffer_load_b32(rs, off, 0, LD_AUX);
}
constexpr int TU = 16;
__device__ __forceinline__ unsigned cyc32() { return (unsigned)__builtin_amdgcn_s_memtime(); }
__device__ __forceinline__ unsigned to_sgpr(unsigned x) {
  unsigned r;
  asm volatile("s_nop 0\n\tv_readfirstlane_b32 %0, %1\n\ts_nop 4" : "=s"(r) : "v"(x));
  return r;
}
struct Clk {
  unsigned ce, te;
  unsigned c2t, t2c;
  __device__ __forceinline__ void edge(unsigned& c, unsigned& t) {
    u64 c0, r0, r1, r2, r3, r4, r5;
    asm volatile("s_memtime %0\n\ts_memrealtime %1\n\ts_nop 4\n\ts_memrealtime %2\n\ts_nop 4\n\ts_memrealtime %3\n\ts_nop 4\n\t"
                 "s_memrealtime %4\n\ts_nop 4\n\ts_memrealtime %5\n\ts_nop 4\n\ts_memrealtime %6\n\ts_waitcnt lgkmcnt(0)"
                 : "=&s"(c0), "=&s"(r0), "=&s"(r1), "=&s"(r2), "=&s"(r3), "=&s"(r4), "=&s"(r5) :: "memory");
    const unsigned x = (unsigned)r0;
    const unsigned n = ((unsigned)r1 == x) + ((unsigned)r2 == x) + ((unsigned)r3 == x) + ((unsigned)r4 == x) + ((unsigned)r5 == x);
    c = to_sgpr((unsigned)c0 + 24u * n); t = (x + 4u) * TU;
  }
  __device__ __forceinline__ void init() { edge(ce, te); c2t = 44000u; t2c = 97600u; }
  __device__ __forceinline__ void sync() {
    unsigned c, t;
    edge(c, t);
    set(c, t);
  }
  __device__ __forceinline__ void set(unsigned c, unsigned t) {
    if (c == ce) return;
    const float q = (float)(t - te) * __builtin_amdgcn_rcpf((float)(c - ce));
    const float qc = fminf(fmaxf(q, 0.51f), 1.99f);
    c2t = to_sgpr((unsigned)(qc * 65536.f));
    t2c = to_sgpr((unsigned)(__builtin_amdgcn_rcpf(qc) * 65536.f));
    ce = c; te = t;
  }
  __device__ __forceinline__ unsigned time(unsigned c) const { return te + ((min(c - ce, 30000u) * c2t) >> 16); }
  __device__ __forceinline__ unsigned now() const { return time(cyc32()); }
  __device__ __forceinline__ unsigned target(unsigned t) const {
    return to_sgpr(ce + (((unsigned)min(max((int)(t - te), 0), 20000) * t2c) >> 16));
  }
  __device__ __forceinline__ void wait(unsigned t) const { wait_c(target(t)); }
  __device__ __forceinline__ void wait_c(unsigned c) const {
    unsigned y;
    asm volatile("1:\n\ts_memtime vcc\n\ts_waitcnt lgkmcnt(0)\n\ts_sub_i32 %0, %1, vcc_lo\n\ts_cmp_gt_i32 %0, 128\n\ts_cbranch_scc1 1b\n\t"
                 "2:\n\ts_sub_i32 %0, %0, %2\n\ts_cmp_gt_i32 %0, %3\n\ts_cbranch_scc1 2b"
                 : "=&s"(y) : "s"(c), "n"(W_STEP), "n"(W_OFF) : "memory", "scc", "vcc");
  }
};
__device__ __forceinline__ unsigned nosent(unsigned d) { return min(d, SENT - 1u); }
__device__ __forceinline__ int arow(int row, int half) { return 4 * (row >> 1) + 2 * half + (row & 1); }
__device__ __forceinline__ unsigned umax4(u32x4 v) { return max(max(v[0], v[1]), max(v[2], v[3])); }
__device__ __forceinline__ void stamp_again(GAS u64* p) {
  __hip_atomic_store(p, (__builtin_amdgcn_s_memrealtime() & ~7ull) | (__hip_atomic_load(p, __ATOMIC_RELAXED, __HIP_MEMORY_SCOPE_AGENT) & 7ull),
                     __ATOMIC_RELAXED, __HIP_MEMORY_SCOPE_AGENT);
}
__device__ __forceinline__ bool poll_fail(unsigned& spins, GAS unsigned* abortw, int lane) {
  ++spins;
  if (spins > SPIN_CAP) {
    if (lane == 0) __hip_atomic_store(abortw, 1u, __ATOMIC_RELAXED, __HIP_MEMORY_SCOPE_AGENT);
    return true;
  }
  if ((spins & 511u) == 0u && lane == 0) {
    unsigned n = 0;
    for (int i = 0; i < 8; ++i) n += __hip_atomic_load(abortw + (SY_CT - SY_ABORT + (CT_ARR + i) * 128) / 4, __ATOMIC_RELAXED, __HIP_MEMORY_SCOPE_AGENT) + 1u;
    if (n < 256u) stamp_again((GAS u64*)(abortw - 32));
  }
  if ((spins & 63u) == 0u)
    return __builtin_amdgcn_readfirstlane(__hip_atomic_load(abortw, __ATOMIC_RELAXED, __HIP_MEMORY_SCOPE_AGENT)) != SENT;
  return false;
}
#define TP(k) do {} while (0)
struct Gate {
  u64 tx; u64 entry, sel; int arr; unsigned rot; bool open;
  __device__ __forceinline__ bool look(char* sync, u32x4 v) {
    if (open) return true;
    for (unsigned spins = 1;; ++spins) {
#pragma unroll
      for (int i = 0; i < 4; ++i) v[i] = __builtin_amdgcn_readfirstlane(v[i]);
      if (((u64)v[1] << 32 | v[0]) - entry + 2000u < (1ull << 31) || spins > 4 * SPIN_CAP) break;
      if (spins > 1) __builtin_amdgcn_s_sleep(8);
      v = ld16(mk_rsrc(sync + SY_EPOCH, 16), 0);
    }
    asm volatile("" ::: "memory");
    sel = (u64)v[3] << 32 | v[2];
    const bool same = rot == (v[0] & 7u);
    rot = v[0] & 7u; open = true;
    return same;
  }
  __device__ __forceinline__ void ask(char* sync, u32x4& v) { asm volatile("s_nop 4\n\tbuffer_load_dwordx4 %0, off, %1, 0 sc1" : "=v"(v) : "s"(own_rsrc(sync + SY_EPOCH, 16)) : "memory"); }
  __device__ __forceinline__ void arrive(char* sync) {
    if (arr >= 0 && opaque_tid(0) == 0) __hip_atomic_fetch_add((GAS unsigned*)(sync + SY_CT + (CT_ARR + arr) * 128), 1u, __ATOMIC_RELAXED, __HIP_MEMORY_SCOPE_AGENT);
    arr = -1;
  }
  __device__ __forceinline__ bool pass(char* sync, u32x4 v = {0u, 0u, 0u, 0u}) { const bool r = look(sync, v); arrive(sync); return r; }
};
__device__ __forceinline__ unsigned ann_val(unsigned t, int s) { return (t << 8) | (unsigned)s; }
__device__ __forceinline__ unsigned die_half() { return ((unsigned)__builtin_amdgcn_s_getreg(20 | (3 << 11)) >> 2) & 1u; }
typedef u64 Sel;
__device__ __forceinline__ unsigned start_wait(char* sync, GAS unsigned* abortw, int lane, bool& dead, int ci, unsigned n) {
  unsigned spins = 0, v;
  const bool m = lane < 4 || ci < 0;
  while (true) {
    v = ld4(mk_rsrc(sync + SY_SET, SY_BYTES - SY_SET), lane < 6 ? CHK + (m ? CT_INIT + (lane & 3) : ci + 32 * (lane & 1)) * 128 : 0x7fffffffu) + 1u;
    if (__all(lane > 5 || v >= (m ? 16u : n))) break;
    __builtin_amdgcn_s_sleep(START_SLP);
    if (dead || poll_fail(spins, abortw, lane)) { dead = true; break; }
  }
  asm volatile("" ::: "memory");
  return min((unsigned)__builtin_amdgcn_readlane(v, 4), (unsigned)__builtin_amdgcn_readlane(v, 5)) - 1u;
}
__device__ __forceinline__ __amdgpu_buffer_rsrc_t rsrc_u(char* sync) { return mk_rsrc((void*)sync, SY_SET); }
__device__ __forceinline__ unsigned near_half(unsigned off, u64 sel) { return off + ((unsigned)(sel >> ((off >> 21) & 63)) & 1u) * 8192; }
__device__ __forceinline__ unsigned x_slot(int kind, int s, int h, Sel sel) {
  return kind == 2 ? near_half(32 * PAGE + (s * 32 + h) * CHK, ~sel) : near_half((s * 64 + 2 * h + 1 - kind) * CHK, sel);
}
__device__ __forceinline__ int ct_near(Sel sel) { return 64 * ((unsigned)(sel >> (SY_SET / PAGE)) & 1u); }
__device__ __forceinline__ unsigned kv_row(int d, Sel sel) { return near_half(SY_KV + (d >> 1) * CHK, ~sel) + (d & 1) * 4096; }
__device__ __forceinline__ int hpos(int h) { return (h & 3) * 8 + (h >> 2); }
constexpr unsigned UXH = 8 * CHK;
__device__ __forceinline__ unsigned ux_base(int s, int h, Sel sel) { return near_half(((s & (URING - 1)) * 32 + h) * UXH, ~sel); }
__device__ __forceinline__ constexpr unsigned ux_tile(int t) { return (t >> 3) * CHK + (t & 7) * 1024; }
__device__ __forceinline__ __amdgpu_buffer_rsrc_t rsrc_hot(char* sync, Sel sel) {
  return mk_rsrc((void*)(sync + near_half(SY_HOT, sel)), H_BYTES - 8192);
}
template <int SLP = 4>
__device__ __forceinline__ void ctr_wait(GAS unsigned* c, unsigned need, GAS unsigned* abortw, int lane, bool& dead) {
  unsigned spins = 0;
  while (true) {
    unsigned v = __builtin_amdgcn_readfirstlane(__hip_atomic_load(c, __ATOMIC_RELAXED, __HIP_MEMORY_SCOPE_AGENT)) + 1u;
    if (v >= need) break;
    __builtin_amdgcn_s_sleep(SLP);
    if (dead || poll_fail(spins, abortw, lane)) { dead = true; break; }
  }
}
template <int N> __device__ __forceinline__ void wait_vm() { asm volatile("s_waitcnt vmcnt(%0)" :: "n"(N) : "memory"); }
__device__ __forceinline__ void lds_barrier() {
  asm volatile("s_waitcnt lgkmcnt(0)" ::: "memory");
  __builtin_amdgcn_s_barrier();
  asm volatile("" ::: "memory");
}
__device__ __forceinline__ f32x4 vzero() { float z; asm volatile("v_mov_b32 %0, 0" : "=v"(z)); f32x4 r = {z, z, z, z}; asm volatile("" : "+v"(r)); return r; }
__device__ __forceinline__ f32x4 smf(u32x4 a, u32x8 b, f32x4 c, int idx) {
  return __builtin_amdgcn_smfmac_f32_16x16x64_bf16(__builtin_bit_cast(bfv8, a), __builtin_bit_cast(bfv16, b), c, idx, 0, 0);
}
__device__ __forceinline__ float red16max(float x) { auto r = __builtin_amdgcn_permlane16_swap(__float_as_uint(x), __float_as_uint(x), false, false); return fmaxf(__uint_as_float(r[0]), __uint_as_float(r[1])); }
__device__ __forceinline__ float red32max(float x) { auto r = __builtin_amdgcn_permlane32_swap(__float_as_uint(x), __float_as_uint(x), false, false); return fmaxf(__uint_as_float(r[0]), __uint_as_float(r[1])); }
__device__ __forceinline__ float red16add(float x) { auto r = __builtin_amdgcn_permlane16_swap(__float_as_uint(x), __float_as_uint(x), false, false); return __uint_as_float(r[0]) + __uint_as_float(r[1]); }
__device__ __forceinline__ float red32add(float x) { auto r = __builtin_amdgcn_permlane32_swap(__float_as_uint(x), __float_as_uint(x), false, false); return __uint_as_float(r[0]) + __uint_as_float(r[1]); }
constexpr bool blk_in_lds(int R, int b) {
  return R == 0 ? ((b & 7) == 1 || (b & 7) == 4) : (b < 16 ? (b & 3) == 0 : (b & 3) == 3);
}
constexpr int lds_index(int R, int b) { int n = 0; for (int i = 0; i < b; ++i) if (blk_in_lds(R, i)) ++n; return n; }
constexpr bool frag_in_lds(int R, int f) { return blk_in_lds(R, f >> 1); }
constexpr int frag_reg_slot(int R, int f) { int n = 0; for (int i = 0; i < f; ++i) if (!frag_in_lds(R, i)) ++n; return n; }

constexpr int m_block(int R, int m, int i) {
  return R == 0 ? 2 * (i < 12 ? (m ? i + i / 3 + 1 : i + (i + 1) / 3) : 4 * (i - 12) + 2 - 2 * m) + m : m ? 16 + 4 * (i & 3) + (i >> 2) : (i < 12 ? i + i / 3 + 1 : 4 * (i - 12));
}
#define QK_REV 0
#define W_BV 0xba9876543210fedcull
#define VO_ORD 3
constexpr int w_block(int R, int i) {
  if (R && VO_ORD == 2) return i < 12 ? m_block(1, 0, 11 - i) : m_block(1, 1, i - 12);
  if (R && VO_ORD == 3) return i & 1 ? m_block(1, 1, i >> 1) : m_block(1, 0, 11 - (i >> 1));
  if (!R && QK_REV) return m_block(0, i & 1, 11 - (i >> 1));
  return R && VO_ORD ? m_block(R, i < 12, i % 12) : m_block(R, i & 1, i >> 1);
}
#define W_BG 0xfedcba9876543210ull
#define W_NT 0
#define U_ORD 0x3210
constexpr u64 bg_order(int m) { u64 r = 0; for (int n = 0; n < 16; ++n) r |= (u64)(m_block(0, m > 0, ((m == 2 ? W_BV : W_BG) >> (4 * n)) & 15) >> 1) << (4 * n); return r; }
struct WSched { int ord[32]; int slot[32]; int issue[32]; int out[32]; int reg[32]; };
constexpr WSched make_wsched(int R) {
  WSched S{};
  int n = 0;
  for (int i = 0; i < 24; ++i) S.ord[n++] = w_block(R, i);
  for (int i = 0; i < n; ++i) S.reg[i] = frag_reg_slot(R, 2 * S.ord[i]);
  const int npure = n;
  for (int b = 0; b < 32; ++b) if (blk_in_lds(R, b)) S.ord[n++] = b;
  int seq[32] = {};
  int nissued = 0, next = 0;
  for (int i = 0; i < 10; ++i) { S.slot[i] = (i + 10 - npure % 10) % 10; S.issue[i] = -1; seq[i] = nissued++; }
  next = 10;
  S.out[0] = nissued - 1;
  for (int c = 0; c <= npure; ++c) {
    if (c >= 1) {
      const int k = S.slot[c - 1];
      if (next < npure) { S.slot[next] = k; S.issue[next] = c; seq[next] = nissued++; ++next; }
      else if (k < NLF / 2) { S.slot[npure + k] = k; S.issue[npure + k] = c; seq[npure + k] = nissued++; }
    }
    if (c + 1 < npure) S.out[c + 1] = nissued - 1 - seq[c + 1];
  }
  return S;
}
__device__ __forceinline__ int kcol(int k) { return 8 * ((k >> 1) & 1) + ((k & 1) ? 7 - (k >> 2) : k >> 2); }
template <bool PERM = false>
__device__ __forceinline__ u32x8 tr_frag(const char* sb, int tile, int lane) {
  u32x8 f;
#pragma unroll
  for (int q = 0; q < 4; ++q) {
    int row = (q >> 1) * 32 + 8 * (lane >> 4) + 4 * (q & 1) + ((lane & 15) >> 2);
    if (PERM) row = (row & ~15) + kcol(row & 15);
    int p = lane & 3;
    int lc = 2 * tile + (p >> 1);
    int addr = row * 256 + ((lc ^ swz_b(row)) * 16) + 8 * (p & 1);
    bf16x4 v = __builtin_amdgcn_ds_read_tr16_b64_v4i16((LAS bf16x4*)(sb + addr));
    u32x2 u = __builtin_bit_cast(u32x2, v);
    f[2 * q] = u[0]; f[2 * q + 1] = u[1];
  }
  return f;
}
__device__ __forceinline__ void dma16(__amdgpu_buffer_rsrc_t rs, unsigned voff, unsigned soff, unsigned lds, bool nt = false, bool pad = true) {
  if (nt) asm volatile("s_mov_b32 m0, %0\n\ts_nop 3\n\tbuffer_load_dwordx4 %1, %2, %3 offen nt lds" :: "s"(lds), "v"(voff), "s"(rs), "s"(soff) : "m0", "memory");
  else if (pad) asm volatile("s_mov_b32 m0, %0\n\ts_nop 3\n\tbuffer_load_dwordx4 %1, %2, %3 offen lds" :: "s"(lds), "v"(voff), "s"(rs), "s"(soff) : "m0", "memory");
  else asm volatile("s_mov_b32 m0, %0\n\ts_nop 0\n\tbuffer_load_dwordx4 %1, %2, %3 offen lds" :: "s"(lds), "v"(voff), "s"(rs), "s"(soff) : "m0", "memory");
}
__device__ __forceinline__ u32x8 tr_frag2(const char* sb, int tile, int f, int lane) {
  u32x8 r;
#pragma unroll
  for (int c = 0; c < 2; ++c)
#pragma unroll
    for (int t = 0; t < 2; ++t) {
      const int row = 32 * c + 16 * f + 4 * (lane >> 4) + ((lane & 15) >> 2);
      const int p = lane & 3, lc = 2 * (tile + t) + (p >> 1);
      const u32x2 u = __builtin_bit_cast(u32x2, __builtin_amdgcn_ds_read_tr16_b64_v4i16((LAS bf16x4*)(sb + row * 256 + ((lc ^ swz_b(row)) * 16) + 8 * (p & 1))));
      r[4 * c + t] = u[0]; r[4 * c + 2 + t] = u[1];
    }
  return r;
}
template <int ROLE>
__device__ __forceinline__ void load_weights(const ChainArgs& a, int h, int w, int lane, char* smem, Wts& W, Gate& G, u32x4& gv) {
#pragma unroll
  for (int i = 0; i < 4; ++i) asm volatile("" : "=v"(W.v[i]));
#pragma unroll
  for (int i = 0; i < 32; ++i) asm volatile("" : "=a"(W.a[i]));
  constexpr WSched S = make_wsched(ROLE);
  constexpr int NP = 32 - NLF / 2;
  const __amdgpu_buffer_rsrc_t r0 = mk_rsrc(ROLE == 0 ? a.Wq : a.Wv, 8u << 20), r1 = mk_rsrc(ROLE == 0 ? a.Wk : a.Wo, 8u << 20);
  const unsigned lds0 = (unsigned)(uintptr_t)(LAS char*)smem;
  unsigned vo[2][4];
#pragma unroll
  for (int j = 0; j < 4; ++j) {
    const int row = (w * 4 + j) * 4 + (lane >> 4), lc = (lane & 15) ^ swz_b(row);
    vo[0][j] = row * HD * 2 + lc * 16; vo[1][j] = row * DM * 2 + lc * 16;
  }
  asm volatile("s_nop 4");
  auto stage = [&](int i) {
    const int blk = S.ord[i];
    const bool second = ROLE == 0 ? (blk & 1) : blk >= 16, wo = ROLE == 1 && second;
    const unsigned so = wo ? (unsigned)(((128 * h + 64 * (blk & 1)) * DM + 128 * (((blk - 16) >> 1) ^ (2 * (h & 3)))) * 2)
                           : (unsigned)((64 * ((ROLE == 0 ? blk >> 1 : blk) ^ (h & RHO)) * HD + 128 * h) * 2);
#pragma unroll
    for (int j = 0; j < 4; ++j) dma16(second ? r1 : r0, vo[wo][j], so, lds0 + 16384 * S.slot[i] + (w * 4 + j) * 1024, W_NT & (ROLE ? 8 : 1));
  };
#pragma unroll
  for (int i = 0; i < 32; ++i) if (S.issue[i] == -1) stage(i);
#pragma unroll
  for (int c = 0; c < NP; ++c) {
    asm volatile("s_waitcnt vmcnt(%0)" :: "n"(4 * S.out[c]) : "memory");
    lds_barrier();
#pragma unroll
    for (int i = 0; i < 32; ++i) if (S.issue[i] == c) stage(i);
    const char* sb = smem + 16384 * S.slot[c];
#pragma unroll
    for (int t = 0; t < 2; ++t) {
      u32x8 f = (ROLE == 1 && S.ord[c] >= 16) ? tr_frag2(sb, 2 * w, t, lane) : tr_frag<true>(sb, 2 * w + t, lane);
      if (S.reg[c] < 32) {
#pragma unroll
        for (int i = 0; i < 8; ++i) asm volatile("v_accvgpr_write_b32 a[%1], %0" :: "v"(f[i]), "n"(8 * ((S.reg[c] + t) & 31) + i));
      }
      else {
#pragma unroll
        for (int i = 0; i < 8; ++i) W.v[(S.reg[c] >> 2) & 3][8 * ((S.reg[c] + t) & 3) + i] = f[i];
      }
    }
    if (S.reg[c] >= 32) asm volatile("" : "+v"(W.v[(S.reg[c] >> 2) & 3]));
  }
  static_assert(S.issue[31] < NP && S.issue[NP] < S.issue[31], "resident blocks are issued in slot order inside the loop");
  G.ask(a.sync, gv);
#pragma unroll
  for (int j = 0; j < NLF / 2; ++j) {
    asm volatile("s_waitcnt vmcnt(%0)" :: "n"(4 * (NLF / 2 - 1 - j) + 1) : "memory");
    lds_barrier();
    const bool two = ROLE == 1 && S.ord[NP + j] >= 16;
    u32x8 f0 = two ? tr_frag2(smem + 16384 * j, 2 * w, 0, lane) : tr_frag<true>(smem + 16384 * j, 2 * w, lane);
    u32x8 f1 = two ? tr_frag2(smem + 16384 * j, 2 * w, 1, lane) : tr_frag<true>(smem + 16384 * j, 2 * w + 1, lane);
    lds_barrier();
    char* d = smem + 16384 * j + w * 4096 + lane * 16;
    *(u32x4*)d = (u32x4){f0[0], f0[1], f0[2], f0[3]};
    *(u32x4*)(d + 1024) = (u32x4){f0[4], f0[5], f0[6], f0[7]};
    *(u32x4*)(d + 2048) = (u32x4){f1[0], f1[1], f1[2], f1[3]};
    *(u32x4*)(d + 3072) = (u32x4){f1[4], f1[5], f1[6], f1[7]};
  }
  asm volatile("s_waitcnt vmcnt(0)" : "+v"(gv) :: "memory");
  lds_barrier();
}
template <int... Is> struct iseq {};
template <int N, int... Is> struct mkseq : mkseq<N - 1, N - 1, Is...> {};
template <int... Is> struct mkseq<0, Is...> { typedef iseq<Is...> type; };

#define SB0 __builtin_amdgcn_sched_barrier(0)
#define CAT8(x, y) ((u32x8){x[0], x[1], x[2], x[3], y[0], y[1], y[2], y[3]})
template <class F, int... I>
__device__ __forceinline__ void for_slots(iseq<I...>, F f) { (f(std::integral_constant<int, I>{}), ...); }
#define QK_BODY \
  "s_cmp_eq_u32 %[wv], 1\n\t" \
  "s_cbranch_scc1 91f\n\t" \
  "s_cmp_eq_u32 %[wv], 2\n\t" \
  "s_cbranch_scc1 92f\n\t" \
  "s_cmp_eq_u32 %[wv], 3\n\t" \
  "s_cbranch_scc1 93f\n\t" \
  "90:\n\t" \
  "s_mov_b32 s58, s42\n\t" \
  "20:\n\t" \
  "s_and_b32 s76, s44, 7\n\t" \
  "s_lshl_b32 s76, s76, 15\n\t" \
  "s_add_i32 s40, s54, s76\n\t" \
  "s_and_b32 s76, s44, 3\n\t" \
  "s_lshl_b32 s45, s76, 14\n\t" \
  "s_mul_i32 s47, s76, 0xc000\n\t" \
  "s_add_i32 s47, s47, 0x40000\n\t" \
  "s_add_i32 s49, s44, -1\n\t" \
  "s_and_b32 s48, s49, 2\n\t" \
  "s_lshl_b32 s48, s48, 13\n\t" \
  "s_and_b32 s76, s49, 1\n\t" \
  "s_lshl_b32 s76, s76, 12\n\t" \
  "s_add_i32 s48, s48, s76\n\t" \
  "s_lshl_b32 s50, s53, 8\n\t" \
  "s_mov_b32 s59, 0\n\t" \
  "s_mov_b32 s92, s51\n\t" \
  "s_mov_b32 s93, s53\n\t" \
  "s_xor_b32 s76, s40, 0x400\n\t" \
  "s_xor_b32 s77, s40, 0x800\n\t" \
  "s_xor_b32 s78, s40, 0xc00\n\t" \
  "s_cmp_lg_u32 s58, 0\n\t" \
  "s_cbranch_scc1 11f\n\t" \
  "10:\n\t" \
  "ds_read_b128 v[48:51], v102 offset:0\n\t" \
  "ds_read_b128 v[52:55], v102 offset:1024\n\t" \
  "ds_read_b128 v[56:59], v102 offset:2048\n\t" \
  "ds_read_b128 v[60:63], v102 offset:3072\n\t" \
  "ds_read_b128 v[16:19], v102 offset:16384\n\t" \
  "ds_read_b128 v[20:23], v102 offset:17408\n\t" \
  "ds_read_b128 v[24:27], v102 offset:18432\n\t" \
  "ds_read_b128 v[28:31], v102 offset:19456\n\t" \
  "ds_read_b128 v[84:87], v102 offset:49152\n\t" \
  "ds_read_b128 v[88:91], v102 offset:50176\n\t" \
  "ds_read_b128 v[92:95], v102 offset:51200\n\t" \
  "ds_read_b128 v[96:99], v102 offset:52224\n\t" \
  "s_waitcnt lgkmcnt(4)\n\t" \
  "ds_read_b128 v[0:3], v103 offset:16384\n\t" \
  "ds_read_b128 v[4:7], v103 offset:17408\n\t" \
  "ds_read_b128 v[8:11], v103 offset:18432\n\t" \
  "ds_read_b128 v[12:15], v103 offset:19456\n\t" \
  "46:\n\t" \
  "s_bitcmp1_b32 s58, 1\n\t" \
  "s_cselect_b64 s[80:81], -1, 0\n\t" \
  "s_cmp_lg_u32 s58, 0\n\t" \
  "s_cbranch_scc1 17f\n\t" \
  "s_mov_b64 exec, 0xff\n\t" \
  "16:\n\t" \
  "s_memtime vcc\n\t" \
  "s_waitcnt lgkmcnt(0)\n\t" \
  "s_sub_i32 s79, s41, vcc_lo\n\t" \
  "s_cmp_gt_i32 s79, 64\n\t" \
  "s_cbranch_scc1 16b\n\t" \
  "13:\n\t" \
  "buffer_load_dword v116, v112, %[rh], s48 offen sc1\n\t" \
  "s_memtime vcc\n\t" \
  "s_memrealtime s[82:83]\n\t" \
  "s_nop 4\n\t" \
  "s_memrealtime s[84:85]\n\t" \
  "s_nop 4\n\t" \
  "s_memrealtime s[56:57]\n\t" \
  "s_nop 4\n\t" \
  "s_memrealtime s[86:87]\n\t" \
  "s_nop 4\n\t" \
  "s_memrealtime s[88:89]\n\t" \
  "s_nop 4\n\t" \
  "s_memrealtime s[90:91]\n\t" \
  "s_mov_b32 s92, 0\n\t" \
  "s_waitcnt lgkmcnt(0)\n\t" \
  "s_cmp_eq_u32 s82, s84\n\t" \
  "s_addc_u32 s92, s92, 0\n\t" \
  "s_cmp_eq_u32 s82, s56\n\t" \
  "s_addc_u32 s92, s92, 0\n\t" \
  "s_cmp_eq_u32 s82, s86\n\t" \
  "s_addc_u32 s92, s92, 0\n\t" \
  "s_cmp_eq_u32 s82, s88\n\t" \
  "s_addc_u32 s92, s92, 0\n\t" \
  "s_cmp_eq_u32 s82, s90\n\t" \
  "s_addc_u32 s92, s92, 0\n\t" \
  "s_mul_i32 s92, s92, 24\n\t" \
  "s_add_i32 s92, s92, vcc_lo\n\t" \
  "s_lshl4_add_u32 s93, s82, 64\n\t" \
  "s_lshl_b32 s50, s93, 8\n\t" \
  "s_waitcnt vmcnt(0)\n\t" \
  "15:\n\t" \
  "v_mov_b32 v117, 20000\n\t" \
  "v_cmp_ne_u32_sdwa vcc, v116, s49 src0_sel:BYTE_0 src1_sel:DWORD\n\t" \
  "v_subrev_u32 v116, s50, v116\n\t" \
  "v_ashrrev_i32 v116, 8, v116\n\t" \
  "s_nop 1\n\t" \
  "v_max_i32_dpp v116, v116, v116 quad_perm:[1,0,3,2] row_mask:0xf bank_mask:0xf\n\t" \
  "s_cbranch_vccnz 14f\n\t" \
  "s_nop 0\n\t" \
  "v_max_i32_dpp v116, v116, v116 quad_perm:[2,3,0,1] row_mask:0xf bank_mask:0xf\n\t" \
  "s_nop 1\n\t" \
  "v_max_i32_dpp v116, v116, v116 row_half_mirror row_mask:0xf bank_mask:0xf\n\t" \
  "v_med3_i32 v116, v116, 0, v117\n\t" \
  "v_mul_u32_u24 v116, s52, v116\n\t" \
  "v_lshrrev_b32 v116, 16, v116\n\t" \
  "s_mov_b64 exec, -1\n\t" \
  "v_readfirstlane_b32 s84, v116\n\t" \
  "s_add_i32 s84, s84, s92\n\t" \
  ".p2align 6\n\t" \
  "s_nop 0\n\t" \
  "s_nop 0\n\t" \
  "s_nop 0\n\t" \
  "s_nop 0\n\t" \
  "s_nop 0\n\t" \
  "s_nop 0\n\t" \
  "s_nop 0\n\t" \
  "s_nop 0\n\t" \
  "1:\n\t" \
  "s_memtime vcc\n\t" \
  "s_waitcnt lgkmcnt(0)\n\t" \
  "s_sub_i32 s79, s84, vcc_lo\n\t" \
  "s_cmp_gt_i32 s79, 128\n\t" \
  "s_cbranch_scc1 1b\n\t" \
  ".p2align 4\n\t" \
  "2:\n\t" \
  "s_sub_i32 s79, s79, 33\n\t" \
  "s_cmp_gt_i32 s79, 56\n\t" \
  "s_cbranch_scc1 2b\n\t" \
  "buffer_load_dwordx4 v[32:35], v100, %[rh], s40 offen sc1\n\t" \
  "s_nop 12\n\t" \
  "buffer_load_dwordx4 v[36:39], v100, %[rh], s76 offen sc1\n\t" \
  "s_nop 12\n\t" \
  "buffer_load_dwordx4 v[40:43], v100, %[rh], s77 offen sc1\n\t" \
  "s_nop 12\n\t" \
  "buffer_load_dwordx4 v[44:47], v100, %[rh], s78 offen sc1\n\t" \
  "s_memtime vcc\n\t" \
  "s_memrealtime s[82:83]\n\t" \
  "s_nop 4\n\t" \
  "s_memrealtime s[84:85]\n\t" \
  "s_nop 4\n\t" \
  "s_memrealtime s[56:57]\n\t" \
  "s_nop 4\n\t" \
  "s_memrealtime s[86:87]\n\t" \
  "s_nop 4\n\t" \
  "s_memrealtime s[88:89]\n\t" \
  "s_nop 4\n\t" \
  "s_memrealtime s[90:91]\n\t" \
  "s_mov_b32 s92, 0\n\t" \
  "s_waitcnt lgkmcnt(0)\n\t" \
  "s_cmp_eq_u32 s82, s84\n\t" \
  "s_addc_u32 s92, s92, 0\n\t" \
  "s_cmp_eq_u32 s82, s56\n\t" \
  "s_addc_u32 s92, s92, 0\n\t" \
  "s_cmp_eq_u32 s82, s86\n\t" \
  "s_addc_u32 s92, s92, 0\n\t" \
  "s_cmp_eq_u32 s82, s88\n\t" \
  "s_addc_u32 s92, s92, 0\n\t" \
  "s_cmp_eq_u32 s82, s90\n\t" \
  "s_addc_u32 s92, s92, 0\n\t" \
  "s_mul_i32 s92, s92, 24\n\t" \
  "s_add_i32 s92, s92, vcc_lo\n\t" \
  "s_lshl4_add_u32 s93, s82, 64\n\t" \
  "3:\n\t" \
  "s_sub_i32 s84, s92, s43\n\t" \
  "s_lshl_b32 s85, s93, 8\n\t" \
  "s_or_b32 s85, s85, s44\n\t" \
  "v_mov_b32 v118, s85\n\t" \
  "v_mov_b64 v[68:69], 0\n\t" \
  "v_mov_b64 v[70:71], 0\n\t" \
  "v_mov_b64 v[72:73], 0\n\t" \
  "v_mov_b64 v[74:75], 0\n\t" \
  "v_mov_b64 v[76:77], 0\n\t" \
  "v_mov_b64 v[78:79], 0\n\t" \
  "v_mov_b64 v[80:81], 0\n\t" \
  "v_mov_b64 v[82:83], 0\n\t" \
  "s_waitcnt lgkmcnt(0)\n\t" \
  "s_waitcnt vmcnt(3)\n\t" \
  "v_smfmac_f32_16x16x64_bf16 v[68:71], v[32:35], a[0:7], v104\n\t" \
  "ds_write_b128 v101, v[32:35] offset:0\n\t" \
  "v_smfmac_f32_16x16x64_bf16 v[72:75], v[32:35], a[8:15], v104\n\t" \
  "v_smfmac_f32_16x16x64_bf16 v[76:79], v[32:35], v[48:55], v104\n\t" \
  "v_smfmac_f32_16x16x64_bf16 v[80:83], v[32:35], v[56:63], v104\n\t" \
  "s_waitcnt vmcnt(2)\n\t" \
  "v_smfmac_f32_16x16x64_bf16 v[68:71], v[36:39], a[16:23], v104\n\t" \
  "ds_write_b128 v101, v[36:39] offset:1024\n\t" \
  "v_smfmac_f32_16x16x64_bf16 v[72:75], v[36:39], a[24:31], v104\n\t" \
  "v_smfmac_f32_16x16x64_bf16 v[76:79], v[36:39], a[32:39], v104\n\t" \
  "v_cmp_u_f32 vcc, v68, v69\n\t" \
  "v_smfmac_f32_16x16x64_bf16 v[80:83], v[36:39], a[40:47], v104\n\t" \
  "v_cmp_u_f32 s[82:83], v70, v71\n\t" \
  "s_waitcnt vmcnt(1)\n\t" \
  "v_smfmac_f32_16x16x64_bf16 v[68:71], v[40:43], v[16:23], v104\n\t" \
  "ds_write_b128 v101, v[40:43] offset:2048\n\t" \
  "v_smfmac_f32_16x16x64_bf16 v[72:75], v[40:43], v[24:31], v104\n\t" \
  "s_or_b64 vcc, vcc, s[82:83]\n\t" \
  "s_or_b64 vcc, vcc, s[80:81]\n\t" \
  "s_waitcnt lgkmcnt(1)\n\t" \
  "v_smfmac_f32_16x16x64_bf16 v[76:79], v[40:43], a[48:55], v104\n\t" \
  "s_cbranch_scc1 8f\n\t" \
  "5:\n\t" \
  "s_barrier\n\t" \
  "4:\n\t" \
  "s_memtime s[56:57]\n\t" \
  "ds_read_b128 v[48:51], v101 offset:4096\n\t" \
  "v_smfmac_f32_16x16x64_bf16 v[80:83], v[40:43], a[56:63], v104\n\t" \
  "ds_read_b128 v[52:55], v101 offset:5120\n\t" \
  "ds_read_b128 v[56:59], v101 offset:8192\n\t" \
  "s_waitcnt vmcnt(0)\n\t" \
  "v_smfmac_f32_16x16x64_bf16 v[68:71], v[44:47], a[64:71], v104\n\t" \
  "ds_write_b128 v101, v[44:47] offset:3072\n\t" \
  "v_smfmac_f32_16x16x64_bf16 v[72:75], v[44:47], a[72:79], v104\n\t" \
  "v_smfmac_f32_16x16x64_bf16 v[76:79], v[44:47], a[80:87], v104\n\t" \
  "v_cmp_u_f32 vcc, v68, v69\n\t" \
  "v_smfmac_f32_16x16x64_bf16 v[80:83], v[44:47], a[88:95], v104\n\t" \
  "v_cmp_u_f32 s[82:83], v70, v71\n\t" \
  "s_waitcnt lgkmcnt(3)\n\t" \
  "v_smfmac_f32_16x16x64_bf16 v[68:71], v[48:51], a[96:103], v104\n\t" \
  "v_smfmac_f32_16x16x64_bf16 v[72:75], v[48:51], a[104:111], v104\n\t" \
  "s_or_b64 vcc, vcc, s[82:83]\n\t" \
  "s_waitcnt lgkmcnt(0)\n\t" \
  "v_smfmac_f32_16x16x64_bf16 v[68:71], v[52:55], a[112:119], v104\n\t" \
  "s_cbranch_scc1 9f\n\t" \
  "6:\n\t" \
  "s_barrier\n\t" \
  "ds_read_b128 v[60:63], v101 offset:9216\n\t" \
  "v_smfmac_f32_16x16x64_bf16 v[72:75], v[52:55], a[120:127], v104\n\t" \
  "ds_read_b128 v[64:67], v101 offset:12288\n\t" \
  "s_sub_i32 s79, s56, s84\n\t" \
  "s_mul_hi_u32 s79, s79, s46\n\t" \
  "v_smfmac_f32_16x16x64_bf16 v[68:71], v[56:59], a[192:199], v104\n\t" \
  "v_lshl_add_u32 v116, s79, 9, v118\n\t" \
  "buffer_store_dword v116, v109, %[rh], s45 offen sc1\n\t" \
  "v_smfmac_f32_16x16x64_bf16 v[72:75], v[56:59], a[200:207], v104\n\t" \
  "ds_read_b128 v[48:51], v101 offset:13312\n\t" \
  "s_waitcnt lgkmcnt(2)\n\t" \
  "v_smfmac_f32_16x16x64_bf16 v[68:71], v[60:63], a[208:215], v104\n\t" \
  "ds_read_b128 v[52:55], v101 offset:6144\n\t" \
  "v_smfmac_f32_16x16x64_bf16 v[72:75], v[60:63], a[216:223], v104\n\t" \
  "ds_read_b128 v[56:59], v101 offset:7168\n\t" \
  "s_waitcnt lgkmcnt(3)\n\t" \
  "v_smfmac_f32_16x16x64_bf16 v[68:71], v[64:67], v[152:159], v104\n\t" \
  "ds_read_b128 v[60:63], v101 offset:10240\n\t" \
  "v_smfmac_f32_16x16x64_bf16 v[72:75], v[64:67], v[160:167], v104\n\t" \
  "ds_read_b128 v[32:35], v101 offset:20480\n\t" \
  "s_waitcnt lgkmcnt(4)\n\t" \
  "v_smfmac_f32_16x16x64_bf16 v[68:71], v[48:51], v[168:175], v104\n\t" \
  "ds_read_b128 v[64:67], v101 offset:11264\n\t" \
  "ds_read_b128 v[16:19], v103 offset:49152\n\t" \
  "v_smfmac_f32_16x16x64_bf16 v[72:75], v[48:51], v[176:183], v104\n\t" \
  "ds_read_b128 v[36:39], v101 offset:21504\n\t" \
  "s_waitcnt lgkmcnt(6)\n\t" \
  "v_smfmac_f32_16x16x64_bf16 v[68:71], v[52:55], v[84:91], v104\n\t" \
  "ds_read_b128 v[48:51], v101 offset:15360\n\t" \
  "ds_read_b128 v[20:23], v103 offset:50176\n\t" \
  "v_smfmac_f32_16x16x64_bf16 v[72:75], v[52:55], v[92:99], v104\n\t" \
  "ds_read_b128 v[40:43], v101 offset:22528\n\t" \
  "s_waitcnt lgkmcnt(8)\n\t" \
  "v_smfmac_f32_16x16x64_bf16 v[68:71], v[56:59], a[160:167], v104\n\t" \
  "ds_read_b128 v[52:55], v101 offset:14336\n\t" \
  "ds_read_b128 v[24:27], v103 offset:51200\n\t" \
  "v_smfmac_f32_16x16x64_bf16 v[72:75], v[56:59], a[168:175], v104\n\t" \
  "ds_read_b128 v[28:31], v103 offset:52224\n\t" \
  "s_waitcnt lgkmcnt(10)\n\t" \
  "v_smfmac_f32_16x16x64_bf16 v[68:71], v[60:63], v[0:7], v104\n\t" \
  "ds_read_b128 v[44:47], v101 offset:23552\n\t" \
  "v_smfmac_f32_16x16x64_bf16 v[72:75], v[60:63], v[8:15], v104\n\t" \
  "v_mov_b64 v[92:93], 0\n\t" \
  "s_waitcnt lgkmcnt(9)\n\t" \
  "v_smfmac_f32_16x16x64_bf16 v[68:71], v[64:67], v[120:127], v104\n\t" \
  "ds_read_b128 v[0:3], v101 offset:24576\n\t" \
  "v_smfmac_f32_16x16x64_bf16 v[72:75], v[64:67], v[128:135], v104\n\t" \
  "ds_read_b128 v[4:7], v101 offset:25600\n\t" \
  "s_waitcnt lgkmcnt(8)\n\t" \
  "v_smfmac_f32_16x16x64_bf16 v[68:71], v[48:51], v[216:223], v104\n\t" \
  "v_mov_b64 v[94:95], 0\n\t" \
  "s_waitcnt lgkmcnt(5)\n\t" \
  "v_smfmac_f32_16x16x64_bf16 v[68:71], v[52:55], v[16:23], v104\n\t" \
  "ds_read_b128 v[8:11], v101 offset:26624\n\t" \
  "v_smfmac_f32_16x16x64_bf16 v[72:75], v[48:51], v[224:231], v104\n\t" \
  "ds_read_b128 v[12:15], v101 offset:27648\n\t" \
  "s_waitcnt lgkmcnt(5)\n\t" \
  "v_smfmac_f32_16x16x64_bf16 v[72:75], v[52:55], v[24:31], v104\n\t" \
  "v_add_f32 v96, v68, v70\n\t" \
  "v_smfmac_f32_16x16x64_bf16 v[76:79], v[56:59], a[176:183], v104\n\t" \
  "v_add_f32 v97, v69, v71\n\t" \
  "v_cvt_pk_bf16_f32 v96, v96, v97\n\t" \
  "v_smfmac_f32_16x16x64_bf16 v[80:83], v[56:59], a[184:191], v104\n\t" \
  "v_add_f32 v98, v72, v74\n\t" \
  "v_add_f32 v99, v73, v75\n\t" \
  "v_smfmac_f32_16x16x64_bf16 v[76:79], v[60:63], a[240:247], v104\n\t" \
  "v_cvt_pk_bf16_f32 v98, v98, v99\n\t" \
  "ds_write2st64_b32 v105, v96, v98 offset1:1\n\t" \
  "v_smfmac_f32_16x16x64_bf16 v[80:83], v[60:63], a[248:255], v104\n\t" \
  "ds_read_b128 v[56:59], v101 offset:9216\n\t" \
  "v_mov_b64 v[68:69], 0\n\t" \
  "v_smfmac_f32_16x16x64_bf16 v[76:79], v[64:67], v[136:143], v104\n\t" \
  "ds_read_b128 v[60:63], v101 offset:13312\n\t" \
  "v_mov_b64 v[70:71], 0\n\t" \
  "v_smfmac_f32_16x16x64_bf16 v[80:83], v[64:67], v[144:151], v104\n\t" \
  "v_smfmac_f32_16x16x64_bf16 v[76:79], v[48:51], v[232:239], v104\n\t" \
  "s_waitcnt lgkmcnt(2)\n\t" \
  "s_barrier\n\t" \
  "ds_read_b64_tr_b16 v[84:85], v107 offset:0\n\t" \
  "v_smfmac_f32_16x16x64_bf16 v[80:83], v[48:51], v[240:247], v104\n\t" \
  "ds_read_b64_tr_b16 v[86:87], v107 offset:128\n\t" \
  "ds_read_b64_tr_b16 v[88:89], v107 offset:1024\n\t" \
  "v_smfmac_f32_16x16x64_bf16 v[76:79], v[52:55], v[200:207], v104\n\t" \
  "ds_read_b64_tr_b16 v[90:91], v107 offset:1152\n\t" \
  "v_smfmac_f32_16x16x64_bf16 v[80:83], v[52:55], v[208:215], v104\n\t" \
  "ds_read_b128 v[64:67], v101 offset:6144\n\t" \
  "s_waitcnt lgkmcnt(5)\n\t" \
  "v_smfmac_f32_16x16x64_bf16 v[76:79], v[60:63], v[184:191], v104\n\t" \
  "ds_read_b128 v[52:55], v101 offset:5120\n\t" \
  "s_waitcnt lgkmcnt(4)\n\t" \
  "v_smfmac_f32_16x16x64_bf16 v[92:95], v[84:87], v[32:39], v104\n\t" \
  "s_waitcnt lgkmcnt(2)\n\t" \
  "v_smfmac_f32_16x16x64_bf16 v[92:95], v[88:91], v[40:47], v104\n\t" \
  "v_smfmac_f32_16x16x64_bf16 v[68:71], v[84:87], v[0:7], v104\n\t" \
  "v_smfmac_f32_16x16x64_bf16 v[68:71], v[88:91], v[8:15], v104\n\t" \
  "v_add_f32 v92, v92, v94\n\t" \
  "v_add_f32 v94, v93, v95\n\t" \
  "v_smfmac_f32_16x16x64_bf16 v[80:83], v[60:63], v[192:199], v104\n\t" \
  "v_mov_b32 v97, v94\n\t" \
  "v_mov_b32_dpp v94, v92 row_ror:8 row_mask:0xf bank_mask:0x3\n\t" \
  "s_nop 0\n\t" \
  "v_mov_b32_dpp v92, v97 row_ror:8 row_mask:0xf bank_mask:0xc\n\t" \
  "s_waitcnt lgkmcnt(1)\n\t" \
  "v_smfmac_f32_16x16x64_bf16 v[76:79], v[64:67], a[144:151], v104\n\t" \
  "v_add_f32 v93, v68, v70\n\t" \
  "v_add_f32 v95, v69, v71\n\t" \
  "v_max_f32 v98, v92, v94\n\t" \
  "v_mov_b32 v97, v95\n\t" \
  "v_mov_b32_dpp v95, v93 row_ror:8 row_mask:0xf bank_mask:0x3\n\t" \
  "s_nop 0\n\t" \
  "v_mov_b32_dpp v93, v97 row_ror:8 row_mask:0xf bank_mask:0xc\n\t" \
  "v_max3_f32 v98, v98, v93, v95\n\t" \
  "v_smfmac_f32_16x16x64_bf16 v[80:83], v[64:67], a[152:159], v104\n\t" \
  "s_nop 0\n\t" \
  "v_max_f32_dpp v98, v98, v98 quad_perm:[1,0,3,2] row_mask:0xf bank_mask:0xf\n\t" \
  "s_waitcnt lgkmcnt(0)\n\t" \
  "v_smfmac_f32_16x16x64_bf16 v[76:79], v[52:55], a[128:135], v104\n\t" \
  "v_max_f32_dpp v98, v98, v98 quad_perm:[2,3,0,1] row_mask:0xf bank_mask:0xf\n\t" \
  "v_smfmac_f32_16x16x64_bf16 v[76:79], v[56:59], a[224:231], v104\n\t" \
  "s_nop 0\n\t" \
  "v_max_f32_dpp v98, v98, v98 row_half_mirror row_mask:0xf bank_mask:0xf\n\t" \
  "v_mul_f32 v98, %[cs], v98\n\t" \
  "v_fma_f32 v92, v92, %[cs], -v98\n\t" \
  "v_fma_f32 v93, v93, %[cs], -v98\n\t" \
  "v_fma_f32 v94, v94, %[cs], -v98\n\t" \
  "v_fma_f32 v95, v95, %[cs], -v98\n\t" \
  "v_exp_f32 v72, v92\n\t" \
  "v_exp_f32 v73, v93\n\t" \
  "v_exp_f32 v74, v94\n\t" \
  "v_exp_f32 v75, v95\n\t" \
  "v_cvt_pk_bf16_f32 v68, v72, v73\n\t" \
  "v_cvt_pk_bf16_f32 v69, v74, v75\n\t" \
  "buffer_store_dwordx2 v[68:69], v110, %[rh], s47 offen sc1\n\t" \
  "v_smfmac_f32_16x16x64_bf16 v[80:83], v[52:55], a[136:143], v104\n\t" \
  "v_smfmac_f32_16x16x64_bf16 v[80:83], v[56:59], a[232:239], v104\n\t" \
  "v_add_f32 v20, v76, v78\n\t" \
  "v_add_f32 v21, v77, v79\n\t" \
  "v_cvt_pk_bf16_f32 v20, v20, v21\n\t" \
  "ds_write_b32 v106, v20\n\t" \
  "s_nop 0\n\t" \
  "s_nop 0\n\t" \
  "s_nop 0\n\t" \
  "s_nop 0\n\t" \
  "v_add_f32 v23, v80, v82\n\t" \
  "v_add_f32 v21, v81, v83\n\t" \
  "v_cvt_pk_bf16_f32 v21, v23, v21\n\t" \
  "ds_write_b32 v106, v21 offset:256\n\t" \
  "v_mov_b64 v[0:1], 0\n\t" \
  "v_mov_b64 v[2:3], 0\n\t" \
  "v_mov_b64 v[36:37], 0\n\t" \
  "v_mov_b64 v[38:39], 0\n\t" \
  "s_waitcnt lgkmcnt(0)\n\t" \
  "s_barrier\n\t" \
  "ds_read_b64_tr_b16 v[32:33], v108 offset:12288\n\t" \
  "ds_read_b64_tr_b16 v[34:35], v108 offset:12352\n\t" \
  "ds_read_b64_tr_b16 v[4:5], v108\n\t" \
  "ds_read_b64_tr_b16 v[6:7], v108 offset:64\n\t" \
  "ds_read_b64_tr_b16 v[8:9], v108 offset:512\n\t" \
  "ds_read_b64_tr_b16 v[10:11], v108 offset:576\n\t" \
  "ds_read_b64_tr_b16 v[12:13], v108 offset:1024\n\t" \
  "ds_read_b64_tr_b16 v[14:15], v108 offset:1088\n\t" \
  "ds_read_b64_tr_b16 v[16:17], v108 offset:1536\n\t" \
  "ds_read_b64_tr_b16 v[18:19], v108 offset:1600\n\t" \
  "v_add_f32 v29, v72, v73\n\t" \
  "v_add_f32 v29, v29, v74\n\t" \
  "s_waitcnt lgkmcnt(8)\n\t" \
  "v_smfmac_f32_16x16x64_bf16 v[0:3], v[84:87], v[32:39], v104\n\t" \
  "s_waitcnt lgkmcnt(4)\n\t" \
  "v_smfmac_f32_16x16x64_bf16 v[0:3], v[84:87], v[4:11], v104\n\t" \
  "s_waitcnt lgkmcnt(0)\n\t" \
  "v_smfmac_f32_16x16x64_bf16 v[0:3], v[88:91], v[12:19], v104\n\t" \
  "v_add_f32 v29, v29, v75\n\t" \
  "s_mov_b32 s76, 0xaaaaaaaa\n\t" \
  "s_mov_b32 s77, s76\n\t" \
  "s_nop 4\n\t" \
  "v_add_f32 v96, v0, v2\n\t" \
  "v_add_f32_dpp v96, v1, v3 quad_perm:[0,1,2,3] row_mask:0xf bank_mask:0xc\n\t" \
  "v_fma_f32 v96, v96, %[cs], -v98\n\t" \
  "v_exp_f32 v24, v96\n\t" \
  "s_nop 0\n\t" \
  "v_add_f32 v29, v29, v24\n\t" \
  "s_nop 0\n\t" \
  "v_mov_b32_dpp v25, v24 quad_perm:[1,0,3,2] row_mask:0xf bank_mask:0xf\n\t" \
  "v_add_f32_dpp v29, v29, v29 quad_perm:[1,0,3,2] row_mask:0xf bank_mask:0xf\n\t" \
  "v_cndmask_b32_e64 v24, v24, v114, s[76:77]\n\t" \
  "v_cndmask_b32_e64 v25, v25, v115, s[76:77]\n\t" \
  "v_add_f32_dpp v29, v29, v29 quad_perm:[2,3,0,1] row_mask:0xf bank_mask:0xf\n\t" \
  "s_nop 1\n\t" \
  "v_add_f32_dpp v29, v29, v29 row_half_mirror row_mask:0xf bank_mask:0xf\n\t" \
  "v_rcp_f32 v26, v29\n\t" \
  "v_cmpx_gt_f32 vcc, 0x5d5e0b6b, v29\n\t" \
  "v_mul_f32 v24, v24, v26\n\t" \
  "v_mul_f32 v25, v25, v26\n\t" \
  "v_cvt_pk_bf16_f32 v24, v24, v25\n\t" \
  "buffer_store_dword v24, v111, %[rh], s47 offen sc1\n\t" \
  "s_memtime s[76:77]\n\t" \
  "s_mov_b64 exec, -1\n\t" \
  "v_mov_b32 v97, v29\n\t" \
  "s_cmp_eq_u64 vcc, -1\n\t" \
  "s_cselect_b32 s42, 0, 8\n\t" \
  "s_waitcnt lgkmcnt(0)\n\t" \
  "s_mov_b32 s57, s76\n\t" \
  "s_barrier\n\t" \
  "s_add_i32 s41, s56, 1950\n\t" \
  "s_sub_i32 s76, s57, s56\n\t" \
  "s_add_i32 s77, s43, 8\n\t" \
  "s_min_u32 s76, s76, s77\n\t" \
  "s_sub_i32 s43, s43, 4\n\t" \
  "s_max_u32 s43, s43, s76\n\t" \
  "s_add_i32 s76, s45, 0x4000\n\t" \
  "s_and_b32 s76, s76, 0xc000\n\t" \
  "v_mov_b32 v116, -1\n\t" \
  "buffer_store_dword v116, v109, %[rh], s76 offen sc1\n\t" \
  "s_sub_i32 s76, s92, s51\n\t" \
  "s_sub_i32 s77, s93, s53\n\t" \
  "s_cmp_eq_u32 s76, 0\n\t" \
  "s_cbranch_scc1 21f\n\t" \
  "v_cvt_f32_u32 v117, s76\n\t" \
  "v_cvt_f32_u32 v116, s77\n\t" \
  "v_rcp_f32 v117, v117\n\t" \
  "s_mov_b32 s51, s92\n\t" \
  "v_mul_f32 v117, v117, v116\n\t" \
  "v_max_f32 v117, 0x3f028f5c, v117\n\t" \
  "v_min_f32 v117, 0x3ffeb852, v117\n\t" \
  "v_rcp_f32 v116, v117\n\t" \
  "v_mul_f32 v117, 0x4f000000, v117\n\t" \
  "v_mul_f32 v116, 0x47800000, v116\n\t" \
  "v_cvt_u32_f32 v117, v117\n\t" \
  "v_cvt_u32_f32 v116, v116\n\t" \
  "s_mov_b32 s53, s93\n\t" \
  "v_readfirstlane_b32 s46, v117\n\t" \
  "v_readfirstlane_b32 s52, v116\n\t" \
  "21:\n\t" \
  "s_add_i32 s44, s44, 1\n\t" \
  "s_and_b32 s58, s58, 4\n\t" \
  "s_and_b32 s76, s44, 64\n\t" \
  "s_or_b32 s42, s42, s76\n\t" \
  "s_cbranch_scc0 20b\n\t" \
  "s_branch 99f\n\t" \
  "14:\n\t" \
  "s_sleep 3\n\t" \
  "s_add_u32 s59, s59, 0x10000\n\t" \
  "s_cmp_lt_u32 s59, 0x400000\n\t" \
  "s_cbranch_scc1 13b\n\t" \
  "s_mov_b64 exec, -1\n\t" \
  "s_mov_b32 s42, 16\n\t" \
  "s_branch 99f\n\t" \
  "8:\n\t" \
  "s_bitcmp1_b32 s58, 1\n\t" \
  "s_cbranch_scc1 4b\n\t" \
  "s_bitcmp1_b32 s58, 2\n\t" \
  "s_cbranch_scc1 5b\n\t" \
  "v_max3_u32 v116, v32, v33, v34\n\t" \
  "v_max3_u32 v117, v35, v36, v37\n\t" \
  "v_max3_u32 v116, v116, v38, v39\n\t" \
  "v_max_u32 v116, v116, v117\n\t" \
  "v_cmp_eq_u32 vcc, -1, v116\n\t" \
  "s_cbranch_vccz 5b\n\t" \
  "s_branch 0f\n\t" \
  "9:\n\t" \
  "s_bitcmp1_b32 s58, 2\n\t" \
  "s_cbranch_scc1 6b\n\t" \
  "v_max3_u32 v116, v40, v41, v42\n\t" \
  "v_max3_u32 v117, v43, v44, v45\n\t" \
  "v_max3_u32 v116, v116, v46, v47\n\t" \
  "v_max_u32 v116, v116, v117\n\t" \
  "v_cmp_eq_u32 vcc, -1, v116\n\t" \
  "s_cbranch_vccz 6b\n\t" \
  "s_or_b32 s58, s58, 2\n\t" \
  "0:\n\t" \
  "s_or_b32 s58, s58, 1\n\t" \
  "11:\n\t" \
  "buffer_load_dwordx4 v[32:35], v100, %[rh], s40 offen sc1\n\t" \
  "buffer_load_dwordx4 v[36:39], v100, %[rh], s76 offen sc1\n\t" \
  "buffer_load_dwordx4 v[40:43], v100, %[rh], s77 offen sc1\n\t" \
  "buffer_load_dwordx4 v[44:47], v100, %[rh], s78 offen sc1\n\t" \
  "s_waitcnt lgkmcnt(0)\n\t" \
  "s_branch 10b\n\t" \
  "17:\n\t" \
  "s_add_u32 s59, s59, 1\n\t" \
  "s_bitcmp1_b32 s58, 2\n\t" \
  "s_cbranch_scc1 3b\n\t" \
  "s_waitcnt vmcnt(0)\n\t" \
  "v_max_u32 v116, v32, v33\n\t" \
  "v_max3_u32 v116, v116, v34, v35\n\t" \
  "v_max3_u32 v116, v116, v36, v37\n\t" \
  "v_max3_u32 v116, v116, v38, v39\n\t" \
  "v_max3_u32 v116, v116, v40, v41\n\t" \
  "v_max3_u32 v116, v116, v42, v43\n\t" \
  "v_max3_u32 v116, v116, v44, v45\n\t" \
  "v_max3_u32 v116, v116, v46, v47\n\t" \
  "v_cmp_eq_u32 vcc, -1, v116\n\t" \
  "s_cbranch_vccz 3b\n\t" \
  "s_bitcmp1_b32 s59, 6\n\t" \
  "s_cbranch_scc1 12f\n\t" \
  "buffer_load_dwordx4 v[32:35], v100, %[rh], s40 offen sc1\n\t" \
  "buffer_load_dwordx4 v[36:39], v100, %[rh], s76 offen sc1\n\t" \
  "buffer_load_dwordx4 v[40:43], v100, %[rh], s77 offen sc1\n\t" \
  "buffer_load_dwordx4 v[44:47], v100, %[rh], s78 offen sc1\n\t" \
  "s_branch 17b\n\t" \
  "12:\n\t" \
  "s_mov_b32 s42, s58\n\t" \
  "s_branch 99f\n\t" \
  "91:\n\t" \
  "s_mov_b32 s58, s42\n\t" \
  "s_mov_b32 s95, 0\n\t" \
  "20:\n\t" \
  "s_and_b32 s76, s44, 7\n\t" \
  "s_lshl_b32 s76, s76, 15\n\t" \
  "s_add_i32 s40, s54, s76\n\t" \
  "s_and_b32 s76, s44, 3\n\t" \
  "s_lshl_b32 s45, s76, 14\n\t" \
  "s_add_i32 s77, s44, 1\n\t" \
  "s_min_u32 s77, s77, 63\n\t" \
  "s_lshr_b32 s76, s77, 1\n\t" \
  "s_lshl_b32 s47, s77, 20\n\t" \
  "s_add_i32 s47, s47, s55\n\t" \
  "s_bitcmp1_b64 %[sel], s76\n\t" \
  "s_cselect_b32 s76, 0x2000, 0\n\t" \
  "s_add_i32 s97, s47, s76\n\t" \
  "s_cmp_le_i32 s77, s43\n\t" \
  "s_cselect_b32 s47, s97, 0x70000000\n\t" \
  "s_lshr_b32 s76, s44, 1\n\t" \
  "s_lshl_b32 s94, s44, 20\n\t" \
  "s_bitcmp1_b64 %[sel], s76\n\t" \
  "s_cselect_b32 s76, 0x2000, 0\n\t" \
  "s_add_i32 s94, s94, s76\n\t" \
  "s_add_i32 s49, s44, -1\n\t" \
  "s_and_b32 s48, s49, 2\n\t" \
  "s_lshl_b32 s48, s48, 13\n\t" \
  "s_and_b32 s76, s49, 1\n\t" \
  "s_lshl_b32 s76, s76, 12\n\t" \
  "s_add_i32 s48, s48, s76\n\t" \
  "s_lshl_b32 s50, s53, 8\n\t" \
  "s_mov_b32 s59, 0\n\t" \
  "s_mov_b32 s92, s51\n\t" \
  "s_mov_b32 s93, s53\n\t" \
  "s_xor_b32 s76, s40, 0x400\n\t" \
  "s_xor_b32 s77, s40, 0x800\n\t" \
  "s_xor_b32 s78, s40, 0xc00\n\t" \
  "s_cmp_lg_u32 s58, 0\n\t" \
  "s_cbranch_scc1 11f\n\t" \
  "s_cmp_lg_u32 s95, 0\n\t" \
  "s_cbranch_scc1 46f\n\t" \
  "10:\n\t" \
  "ds_read_b128 v[48:51], v102 offset:32768\n\t" \
  "ds_read_b128 v[52:55], v102 offset:33792\n\t" \
  "ds_read_b128 v[56:59], v102 offset:34816\n\t" \
  "ds_read_b128 v[60:63], v102 offset:35840\n\t" \
  "ds_read_b128 v[16:19], v102 offset:49152\n\t" \
  "ds_read_b128 v[20:23], v102 offset:50176\n\t" \
  "ds_read_b128 v[24:27], v102 offset:51200\n\t" \
  "ds_read_b128 v[28:31], v102 offset:52224\n\t" \
  "ds_read_b128 v[84:87], v103 offset:16384\n\t" \
  "ds_read_b128 v[88:91], v103 offset:17408\n\t" \
  "ds_read_b128 v[92:95], v103 offset:18432\n\t" \
  "ds_read_b128 v[96:99], v103 offset:19456\n\t" \
  "s_waitcnt lgkmcnt(4)\n\t" \
  "ds_read_b128 v[0:3], v103 offset:49152\n\t" \
  "ds_read_b128 v[4:7], v103 offset:50176\n\t" \
  "ds_read_b128 v[8:11], v103 offset:51200\n\t" \
  "ds_read_b128 v[12:15], v103 offset:52224\n\t" \
  "46:\n\t" \
  "s_bitcmp1_b32 s58, 1\n\t" \
  "s_cselect_b64 s[80:81], -1, 0\n\t" \
  "s_cmp_lg_u32 s58, 0\n\t" \
  "s_cbranch_scc1 17f\n\t" \
  "s_mov_b64 exec, 0xff\n\t" \
  "s_cmp_eq_u32 s95, 0\n\t" \
  "s_cbranch_scc1 16f\n\t" \
  "s_memtime vcc\n\t" \
  "s_memrealtime s[82:83]\n\t" \
  "s_nop 4\n\t" \
  "s_memrealtime s[84:85]\n\t" \
  "s_nop 4\n\t" \
  "s_memrealtime s[56:57]\n\t" \
  "s_nop 4\n\t" \
  "s_memrealtime s[86:87]\n\t" \
  "s_nop 4\n\t" \
  "s_memrealtime s[88:89]\n\t" \
  "s_nop 4\n\t" \
  "s_memrealtime s[90:91]\n\t" \
  "s_mov_b32 s92, 0\n\t" \
  "s_waitcnt lgkmcnt(0)\n\t" \
  "s_cmp_eq_u32 s82, s84\n\t" \
  "s_addc_u32 s92, s92, 0\n\t" \
  "s_cmp_eq_u32 s82, s56\n\t" \
  "s_addc_u32 s92, s92, 0\n\t" \
  "s_cmp_eq_u32 s82, s86\n\t" \
  "s_addc_u32 s92, s92, 0\n\t" \
  "s_cmp_eq_u32 s82, s88\n\t" \
  "s_addc_u32 s92, s92, 0\n\t" \
  "s_cmp_eq_u32 s82, s90\n\t" \
  "s_addc_u32 s92, s92, 0\n\t" \
  "s_mul_i32 s92, s92, 24\n\t" \
  "s_add_i32 s92, s92, vcc_lo\n\t" \
  "s_lshl4_add_u32 s93, s82, 64\n\t" \
  "s_lshl_b32 s50, s93, 8\n\t" \
  "s_waitcnt vmcnt(2)\n\t" \
  "v_mov_b32 v116, v118\n\t" \
  "s_branch 15f\n\t" \
  "16:\n\t" \
  "s_memtime vcc\n\t" \
  "s_waitcnt lgkmcnt(0)\n\t" \
  "s_sub_i32 s79, s41, vcc_lo\n\t" \
  "s_cmp_gt_i32 s79, 64\n\t" \
  "s_cbranch_scc1 16b\n\t" \
  "13:\n\t" \
  "buffer_load_dword v116, v112, %[rh], s48 offen sc1\n\t" \
  "s_memtime vcc\n\t" \
  "s_memrealtime s[82:83]\n\t" \
  "s_nop 4\n\t" \
  "s_memrealtime s[84:85]\n\t" \
  "s_nop 4\n\t" \
  "s_memrealtime s[56:57]\n\t" \
  "s_nop 4\n\t" \
  "s_memrealtime s[86:87]\n\t" \
  "s_nop 4\n\t" \
  "s_memrealtime s[88:89]\n\t" \
  "s_nop 4\n\t" \
  "s_memrealtime s[90:91]\n\t" \
  "s_mov_b32 s92, 0\n\t" \
  "s_waitcnt lgkmcnt(0)\n\t" \
  "s_cmp_eq_u32 s82, s84\n\t" \
  "s_addc_u32 s92, s92, 0\n\t" \
  "s_cmp_eq_u32 s82, s56\n\t" \
  "s_addc_u32 s92, s92, 0\n\t" \
  "s_cmp_eq_u32 s82, s86\n\t" \
  "s_addc_u32 s92, s92, 0\n\t" \
  "s_cmp_eq_u32 s82, s88\n\t" \
  "s_addc_u32 s92, s92, 0\n\t" \
  "s_cmp_eq_u32 s82, s90\n\t" \
  "s_addc_u32 s92, s92, 0\n\t" \
  "s_mul_i32 s92, s92, 24\n\t" \
  "s_add_i32 s92, s92, vcc_lo\n\t" \
  "s_lshl4_add_u32 s93, s82, 64\n\t" \
  "s_lshl_b32 s50, s93, 8\n\t" \
  "s_waitcnt vmcnt(0)\n\t" \
  "15:\n\t" \
  "v_mov_b32 v117, 20000\n\t" \
  "v_cmp_ne_u32_sdwa vcc, v116, s49 src0_sel:BYTE_0 src1_sel:DWORD\n\t" \
  "v_subrev_u32 v116, s50, v116\n\t" \
  "v_ashrrev_i32 v116, 8, v116\n\t" \
  "s_nop 1\n\t" \
  "v_max_i32_dpp v116, v116, v116 quad_perm:[1,0,3,2] row_mask:0xf bank_mask:0xf\n\t" \
  "s_cbranch_vccnz 14f\n\t" \
  "s_nop 0\n\t" \
  "v_max_i32_dpp v116, v116, v116 quad_perm:[2,3,0,1] row_mask:0xf bank_mask:0xf\n\t" \
  "s_nop 1\n\t" \
  "v_max_i32_dpp v116, v116, v116 row_half_mirror row_mask:0xf bank_mask:0xf\n\t" \
  "v_med3_i32 v116, v116, 0, v117\n\t" \
  "v_mul_u32_u24 v116, s52, v116\n\t" \
  "v_lshrrev_b32 v116, 16, v116\n\t" \
  "s_mov_b64 exec, -1\n\t" \
  "v_readfirstlane_b32 s84, v116\n\t" \
  "s_add_i32 s84, s84, s92\n\t" \
  ".p2align 6\n\t" \
  "s_nop 0\n\t" \
  "s_nop 0\n\t" \
  "s_nop 0\n\t" \
  "s_nop 0\n\t" \
  "s_nop 0\n\t" \
  "s_nop 0\n\t" \
  "s_nop 0\n\t" \
  "s_nop 0\n\t" \
  "1:\n\t" \
  "s_memtime vcc\n\t" \
  "s_waitcnt lgkmcnt(0)\n\t" \
  "s_sub_i32 s79, s84, vcc_lo\n\t" \
  "s_cmp_gt_i32 s79, 128\n\t" \
  "s_cbranch_scc1 1b\n\t" \
  ".p2align 4\n\t" \
  "2:\n\t" \
  "s_sub_i32 s79, s79, 33\n\t" \
  "s_cmp_gt_i32 s79, 56\n\t" \
  "s_cbranch_scc1 2b\n\t" \
  "buffer_load_dwordx4 v[32:35], v100, %[rh], s40 offen sc1\n\t" \
  "s_nop 12\n\t" \
  "buffer_load_dwordx4 v[36:39], v100, %[rh], s76 offen sc1\n\t" \
  "s_nop 12\n\t" \
  "buffer_load_dwordx4 v[40:43], v100, %[rh], s77 offen sc1\n\t" \
  "s_nop 12\n\t" \
  "buffer_load_dwordx4 v[44:47], v100, %[rh], s78 offen sc1\n\t" \
  "3:\n\t" \
  "v_mov_b64 v[68:69], 0\n\t" \
  "v_mov_b64 v[70:71], 0\n\t" \
  "v_mov_b64 v[72:73], 0\n\t" \
  "v_mov_b64 v[74:75], 0\n\t" \
  "v_mov_b64 v[76:77], 0\n\t" \
  "v_mov_b64 v[78:79], 0\n\t" \
  "v_mov_b64 v[80:81], 0\n\t" \
  "v_mov_b64 v[82:83], 0\n\t" \
  "s_waitcnt lgkmcnt(0)\n\t" \
  "s_waitcnt vmcnt(3)\n\t" \
  "v_smfmac_f32_16x16x64_bf16 v[68:71], v[32:35], a[96:103], v104\n\t" \
  "ds_write_b128 v101, v[32:35] offset:4096\n\t" \
  "v_smfmac_f32_16x16x64_bf16 v[72:75], v[32:35], a[104:111], v104\n\t" \
  "v_smfmac_f32_16x16x64_bf16 v[76:79], v[32:35], v[48:55], v104\n\t" \
  "v_smfmac_f32_16x16x64_bf16 v[80:83], v[32:35], v[56:63], v104\n\t" \
  "s_waitcnt vmcnt(2)\n\t" \
  "v_smfmac_f32_16x16x64_bf16 v[68:71], v[36:39], a[112:119], v104\n\t" \
  "ds_write_b128 v101, v[36:39] offset:5120\n\t" \
  "v_smfmac_f32_16x16x64_bf16 v[72:75], v[36:39], a[120:127], v104\n\t" \
  "v_smfmac_f32_16x16x64_bf16 v[76:79], v[36:39], a[128:135], v104\n\t" \
  "v_cmp_u_f32 vcc, v68, v69\n\t" \
  "v_smfmac_f32_16x16x64_bf16 v[80:83], v[36:39], a[136:143], v104\n\t" \
  "v_cmp_u_f32 s[82:83], v70, v71\n\t" \
  "s_waitcnt vmcnt(1)\n\t" \
  "v_smfmac_f32_16x16x64_bf16 v[68:71], v[40:43], v[16:23], v104\n\t" \
  "ds_write_b128 v101, v[40:43] offset:6144\n\t" \
  "v_smfmac_f32_16x16x64_bf16 v[72:75], v[40:43], v[24:31], v104\n\t" \
  "s_or_b64 vcc, vcc, s[82:83]\n\t" \
  "s_or_b64 vcc, vcc, s[80:81]\n\t" \
  "s_waitcnt lgkmcnt(1)\n\t" \
  "v_smfmac_f32_16x16x64_bf16 v[76:79], v[40:43], a[144:151], v104\n\t" \
  "s_cbranch_scc1 8f\n\t" \
  "5:\n\t" \
  "s_barrier\n\t" \
  "4:\n\t" \
  "s_memtime s[56:57]\n\t" \
  "ds_read_b128 v[48:51], v101 offset:8192\n\t" \
  "v_smfmac_f32_16x16x64_bf16 v[80:83], v[40:43], a[152:159], v104\n\t" \
  "ds_read_b128 v[52:55], v101 offset:9216\n\t" \
  "ds_read_b128 v[56:59], v101 offset:12288\n\t" \
  "s_waitcnt vmcnt(0)\n\t" \
  "v_smfmac_f32_16x16x64_bf16 v[68:71], v[44:47], a[160:167], v104\n\t" \
  "ds_write_b128 v101, v[44:47] offset:7168\n\t" \
  "v_smfmac_f32_16x16x64_bf16 v[72:75], v[44:47], a[168:175], v104\n\t" \
  "v_smfmac_f32_16x16x64_bf16 v[76:79], v[44:47], a[176:183], v104\n\t" \
  "v_cmp_u_f32 vcc, v68, v69\n\t" \
  "v_smfmac_f32_16x16x64_bf16 v[80:83], v[44:47], a[184:191], v104\n\t" \
  "v_cmp_u_f32 s[82:83], v70, v71\n\t" \
  "s_waitcnt lgkmcnt(3)\n\t" \
  "v_smfmac_f32_16x16x64_bf16 v[68:71], v[48:51], a[192:199], v104\n\t" \
  "v_smfmac_f32_16x16x64_bf16 v[72:75], v[48:51], a[200:207], v104\n\t" \
  "s_or_b64 vcc, vcc, s[82:83]\n\t" \
  "s_waitcnt lgkmcnt(0)\n\t" \
  "v_smfmac_f32_16x16x64_bf16 v[68:71], v[52:55], a[208:215], v104\n\t" \
  "s_cbranch_scc1 9f\n\t" \
  "6:\n\t" \
  "s_barrier\n\t" \
  "ds_read_b128 v[60:63], v101 offset:13312\n\t" \
  "v_smfmac_f32_16x16x64_bf16 v[72:75], v[52:55], a[216:223], v104\n\t" \
  "ds_read_b128 v[64:67], v101 offset:0\n\t" \
  "v_smfmac_f32_16x16x64_bf16 v[68:71], v[56:59], v[152:159], v104\n\t" \
  "v_smfmac_f32_16x16x64_bf16 v[72:75], v[56:59], v[160:167], v104\n\t" \
  "ds_read_b128 v[48:51], v101 offset:1024\n\t" \
  "s_waitcnt lgkmcnt(2)\n\t" \
  "v_smfmac_f32_16x16x64_bf16 v[68:71], v[60:63], v[168:175], v104\n\t" \
  "ds_read_b128 v[52:55], v101 offset:10240\n\t" \
  "v_smfmac_f32_16x16x64_bf16 v[72:75], v[60:63], v[176:183], v104\n\t" \
  "ds_read_b128 v[56:59], v101 offset:11264\n\t" \
  "s_waitcnt lgkmcnt(3)\n\t" \
  "v_smfmac_f32_16x16x64_bf16 v[68:71], v[64:67], a[0:7], v104\n\t" \
  "ds_read_b128 v[60:63], v101 offset:14336\n\t" \
  "v_smfmac_f32_16x16x64_bf16 v[72:75], v[64:67], a[8:15], v104\n\t" \
  "s_waitcnt lgkmcnt(3)\n\t" \
  "v_smfmac_f32_16x16x64_bf16 v[68:71], v[48:51], a[16:23], v104\n\t" \
  "ds_read_b128 v[64:67], v101 offset:15360\n\t" \
  "ds_read_b128 v[16:19], v102 offset:16384\n\t" \
  "v_smfmac_f32_16x16x64_bf16 v[72:75], v[48:51], a[24:31], v104\n\t" \
  "s_waitcnt lgkmcnt(4)\n\t" \
  "v_smfmac_f32_16x16x64_bf16 v[68:71], v[52:55], v[84:91], v104\n\t" \
  "ds_read_b128 v[48:51], v101 offset:3072\n\t" \
  "ds_read_b128 v[20:23], v102 offset:17408\n\t" \
  "v_smfmac_f32_16x16x64_bf16 v[72:75], v[52:55], v[92:99], v104\n\t" \
  "s_waitcnt lgkmcnt(5)\n\t" \
  "v_smfmac_f32_16x16x64_bf16 v[68:71], v[56:59], v[120:127], v104\n\t" \
  "ds_read_b128 v[52:55], v101 offset:2048\n\t" \
  "ds_read_b128 v[24:27], v102 offset:18432\n\t" \
  "v_smfmac_f32_16x16x64_bf16 v[72:75], v[56:59], v[128:135], v104\n\t" \
  "ds_read_b128 v[28:31], v102 offset:19456\n\t" \
  "s_waitcnt lgkmcnt(7)\n\t" \
  "v_smfmac_f32_16x16x64_bf16 v[68:71], v[60:63], v[0:7], v104\n\t" \
  "ds_read_b128 v[36:39], v102 offset:28672\n\t" \
  "v_smfmac_f32_16x16x64_bf16 v[72:75], v[60:63], v[8:15], v104\n\t" \
  "buffer_load_dwordx4 v[84:87], v107, %[ru], s94 offen offset:0 sc1\n\t" \
  "s_waitcnt lgkmcnt(7)\n\t" \
  "v_smfmac_f32_16x16x64_bf16 v[68:71], v[64:67], v[216:223], v104\n\t" \
  "ds_read_b128 v[0:3], v103 offset:0\n\t" \
  "v_smfmac_f32_16x16x64_bf16 v[72:75], v[64:67], v[224:231], v104\n\t" \
  "ds_read_b128 v[4:7], v103 offset:1024\n\t" \
  "s_waitcnt lgkmcnt(7)\n\t" \
  "v_smfmac_f32_16x16x64_bf16 v[68:71], v[48:51], a[64:71], v104\n\t" \
  "buffer_load_dwordx4 v[88:91], v107, %[ru], s94 offen offset:1024 sc1\n\t" \
  "s_waitcnt lgkmcnt(5)\n\t" \
  "v_smfmac_f32_16x16x64_bf16 v[68:71], v[52:55], v[16:23], v104\n\t" \
  "ds_read_b128 v[8:11], v103 offset:2048\n\t" \
  "v_smfmac_f32_16x16x64_bf16 v[72:75], v[48:51], a[72:79], v104\n\t" \
  "ds_read_b128 v[12:15], v103 offset:3072\n\t" \
  "s_waitcnt lgkmcnt(5)\n\t" \
  "v_smfmac_f32_16x16x64_bf16 v[72:75], v[52:55], v[24:31], v104\n\t" \
  "v_add_f32 v44, v68, v70\n\t" \
  "ds_read_b128 v[40:43], v102 offset:29696\n\t" \
  "v_smfmac_f32_16x16x64_bf16 v[76:79], v[56:59], v[136:143], v104\n\t" \
  "v_add_f32 v45, v69, v71\n\t" \
  "v_cvt_pk_bf16_f32 v44, v44, v45\n\t" \
  "v_smfmac_f32_16x16x64_bf16 v[80:83], v[56:59], v[144:151], v104\n\t" \
  "v_add_f32 v46, v72, v74\n\t" \
  "v_add_f32 v47, v73, v75\n\t" \
  "v_smfmac_f32_16x16x64_bf16 v[76:79], v[60:63], v[200:207], v104\n\t" \
  "v_cvt_pk_bf16_f32 v46, v46, v47\n\t" \
  "ds_write2st64_b32 v105, v44, v46 offset1:1\n\t" \
  "v_smfmac_f32_16x16x64_bf16 v[80:83], v[60:63], v[208:215], v104\n\t" \
  "ds_read_b128 v[56:59], v101 offset:8192\n\t" \
  "v_mov_b64 v[68:69], 0\n\t" \
  "v_smfmac_f32_16x16x64_bf16 v[76:79], v[64:67], v[232:239], v104\n\t" \
  "ds_read_b128 v[16:19], v103 offset:32768\n\t" \
  "v_mov_b64 v[70:71], 0\n\t" \
  "v_smfmac_f32_16x16x64_bf16 v[80:83], v[64:67], v[240:247], v104\n\t" \
  "ds_read_b128 v[20:23], v103 offset:33792\n\t" \
  "v_smfmac_f32_16x16x64_bf16 v[76:79], v[48:51], a[80:87], v104\n\t" \
  "s_waitcnt lgkmcnt(3)\n\t" \
  "s_barrier\n\t" \
  "v_smfmac_f32_16x16x64_bf16 v[80:83], v[48:51], a[88:95], v104\n\t" \
  "buffer_load_dwordx4 v[92:95], v107, %[ru], s94 offen offset:2048 sc1\n\t" \
  "v_smfmac_f32_16x16x64_bf16 v[68:71], v[32:35], v[36:43], v104\n\t" \
  "ds_read_b128 v[36:39], v102 offset:61440\n\t" \
  "ds_read_b128 v[40:43], v102 offset:62464\n\t" \
  "v_smfmac_f32_16x16x64_bf16 v[76:79], v[52:55], a[48:55], v104\n\t" \
  "ds_read_b128 v[60:63], v101 offset:1024\n\t" \
  "ds_read_b128 v[24:27], v103 offset:34816\n\t" \
  "v_smfmac_f32_16x16x64_bf16 v[80:83], v[52:55], a[56:63], v104\n\t" \
  "ds_read_b128 v[64:67], v101 offset:10240\n\t" \
  "buffer_load_dwordx4 v[96:99], v107, %[ru], s94 offen offset:3072 sc1\n\t" \
  "s_waitcnt lgkmcnt(7)\n\t" \
  "v_smfmac_f32_16x16x64_bf16 v[76:79], v[56:59], v[0:7], v104\n\t" \
  "ds_read_b128 v[48:51], v101 offset:12288\n\t" \
  "ds_read_b128 v[28:31], v103 offset:35840\n\t" \
  "global_load_dword v119, v109, %[kc] sc1\n\t" \
  "v_smfmac_f32_16x16x64_bf16 v[80:83], v[56:59], v[8:15], v104\n\t" \
  "s_waitcnt lgkmcnt(4)\n\t" \
  "v_smfmac_f32_16x16x64_bf16 v[76:79], v[60:63], a[32:39], v104\n\t" \
  "ds_read_b128 v[52:55], v101 offset:9216\n\t" \
  "buffer_load_dwordx4 v[32:35], v100, %[ru], s47 offen offset:0 sc1\n\t" \
  "v_smfmac_f32_16x16x64_bf16 v[80:83], v[60:63], a[40:47], v104\n\t" \
  "ds_read_b128 v[0:3], v102 offset:0\n\t" \
  "v_smfmac_f32_16x16x64_bf16 v[68:71], v[56:59], v[36:43], v104\n\t" \
  "ds_read_b128 v[36:39], v103 offset:28672\n\t" \
  "ds_read_b128 v[40:43], v103 offset:29696\n\t" \
  "s_waitcnt lgkmcnt(6)\n\t" \
  "v_smfmac_f32_16x16x64_bf16 v[76:79], v[64:67], a[240:247], v104\n\t" \
  "ds_read_b128 v[56:59], v101 offset:13312\n\t" \
  "v_smfmac_f32_16x16x64_bf16 v[80:83], v[64:67], a[248:255], v104\n\t" \
  "ds_read_b128 v[60:63], v101 offset:0\n\t" \
  "ds_read_b128 v[4:7], v102 offset:1024\n\t" \
  "buffer_load_dwordx4 v[44:47], v100, %[ru], s47 offen offset:1024 sc1\n\t" \
  "s_waitcnt lgkmcnt(8)\n\t" \
  "v_smfmac_f32_16x16x64_bf16 v[76:79], v[48:51], v[16:23], v104\n\t" \
  "ds_read_b128 v[8:11], v102 offset:2048\n\t" \
  "s_waitcnt lgkmcnt(8)\n\t" \
  "v_smfmac_f32_16x16x64_bf16 v[80:83], v[48:51], v[24:31], v104\n\t" \
  "ds_read_b128 v[12:15], v102 offset:3072\n\t" \
  "s_waitcnt lgkmcnt(8)\n\t" \
  "v_smfmac_f32_16x16x64_bf16 v[76:79], v[52:55], a[224:231], v104\n\t" \
  "s_waitcnt lgkmcnt(5)\n\t" \
  "v_smfmac_f32_16x16x64_bf16 v[68:71], v[48:51], v[36:43], v104\n\t" \
  "v_smfmac_f32_16x16x64_bf16 v[80:83], v[52:55], a[232:239], v104\n\t" \
  "s_waitcnt lgkmcnt(4)\n\t" \
  "v_smfmac_f32_16x16x64_bf16 v[76:79], v[56:59], v[184:191], v104\n\t" \
  "v_add_f32 v72, v68, v70\n\t" \
  "s_waitcnt lgkmcnt(2)\n\t" \
  "v_smfmac_f32_16x16x64_bf16 v[76:79], v[60:63], v[0:7], v104\n\t" \
  "v_add_f32 v73, v69, v71\n\t" \
  "v_smfmac_f32_16x16x64_bf16 v[80:83], v[56:59], v[192:199], v104\n\t" \
  "v_cvt_pk_bf16_f32 v72, v72, v73\n\t" \
  "s_waitcnt lgkmcnt(0)\n\t" \
  "v_smfmac_f32_16x16x64_bf16 v[80:83], v[60:63], v[8:15], v104\n\t" \
  "ds_write_b32 v106, v72 offset:11776\n\t" \
  "v_add_f32 v20, v76, v78\n\t" \
  "v_add_f32 v21, v77, v79\n\t" \
  "v_cvt_pk_bf16_f32 v20, v20, v21\n\t" \
  "ds_write_b32 v106, v20\n\t" \
  "s_nop 3\n\t" \
  "v_add_f32 v23, v80, v82\n\t" \
  "v_add_f32 v21, v81, v83\n\t" \
  "v_cvt_pk_bf16_f32 v21, v23, v21\n\t" \
  "ds_write_b32 v106, v21 offset:256\n\t" \
  "s_waitcnt lgkmcnt(0)\n\t" \
  "s_barrier\n\t" \
  "v_bfe_i32 v64, v114, 0, 1\n\t" \
  "v_and_b32 v64, 0xff800000, v64\n\t" \
  "v_bfe_i32 v65, v114, 1, 1\n\t" \
  "v_and_b32 v65, 0xff800000, v65\n\t" \
  "v_bfe_i32 v66, v114, 2, 1\n\t" \
  "v_and_b32 v66, 0xff800000, v66\n\t" \
  "v_bfe_i32 v67, v114, 3, 1\n\t" \
  "v_and_b32 v67, 0xff800000, v67\n\t" \
  "v_bfe_i32 v68, v114, 4, 1\n\t" \
  "v_and_b32 v68, 0xff800000, v68\n\t" \
  "v_bfe_i32 v69, v114, 5, 1\n\t" \
  "v_and_b32 v69, 0xff800000, v69\n\t" \
  "v_bfe_i32 v70, v114, 6, 1\n\t" \
  "v_and_b32 v70, 0xff800000, v70\n\t" \
  "v_bfe_i32 v71, v114, 7, 1\n\t" \
  "v_and_b32 v71, 0xff800000, v71\n\t" \
  "s_lshl_b32 s94, s45, 4\n\t" \
  "s_and_b32 s96, s44, 2\n\t" \
  "s_lshl_b32 s96, s96, 13\n\t" \
  "s_and_b32 s76, s44, 1\n\t" \
  "s_lshl_b32 s76, s76, 12\n\t" \
  "s_add_i32 s96, s96, s76\n\t" \
  "s_mov_b32 s95, 1\n\t" \
  "s_add_i32 s41, s56, 1950\n\t" \
  "s_sub_i32 s76, s92, s51\n\t" \
  "s_sub_i32 s77, s93, s53\n\t" \
  "s_cmp_eq_u32 s76, 0\n\t" \
  "s_cbranch_scc1 21f\n\t" \
  "v_cvt_f32_u32 v117, s76\n\t" \
  "v_cvt_f32_u32 v116, s77\n\t" \
  "v_rcp_f32 v117, v117\n\t" \
  "s_mov_b32 s51, s92\n\t" \
  "v_mul_f32 v117, v117, v116\n\t" \
  "v_max_f32 v117, 0x3f028f5c, v117\n\t" \
  "v_min_f32 v117, 0x3ffeb852, v117\n\t" \
  "v_rcp_f32 v116, v117\n\t" \
  "v_mul_f32 v117, 0x4f000000, v117\n\t" \
  "v_mul_f32 v116, 0x47800000, v116\n\t" \
  "v_cvt_u32_f32 v117, v117\n\t" \
  "v_cvt_u32_f32 v116, v116\n\t" \
  "s_mov_b32 s53, s93\n\t" \
  "v_readfirstlane_b32 s46, v117\n\t" \
  "v_readfirstlane_b32 s52, v116\n\t" \
  "21:\n\t" \
  "ds_read_b128 v[0:3], v101 offset:20480\n\t" \
  "ds_read_b128 v[4:7], v101 offset:21504\n\t" \
  "ds_read_b128 v[8:11], v101 offset:22528\n\t" \
  "ds_read_b128 v[12:15], v101 offset:23552\n\t" \
  "ds_read_b128 v[16:19], v101 offset:24576\n\t" \
  "ds_read_b128 v[20:23], v101 offset:25600\n\t" \
  "ds_read_b128 v[24:27], v101 offset:26624\n\t" \
  "ds_read_b128 v[28:31], v101 offset:27648\n\t" \
  "ds_read_b64_tr_b16 v[48:49], v108 offset:0\n\t" \
  "ds_read_b64_tr_b16 v[50:51], v108 offset:64\n\t" \
  "ds_read_b64_tr_b16 v[52:53], v108 offset:512\n\t" \
  "ds_read_b64_tr_b16 v[54:55], v108 offset:576\n\t" \
  "ds_read_b64_tr_b16 v[56:57], v108 offset:1024\n\t" \
  "ds_read_b64_tr_b16 v[58:59], v108 offset:1088\n\t" \
  "ds_read_b64_tr_b16 v[60:61], v108 offset:1536\n\t" \
  "ds_read_b64_tr_b16 v[62:63], v108 offset:1600\n\t" \
  "ds_read_b64_tr_b16 v[76:77], v108 offset:12288\n\t" \
  "ds_read_b64_tr_b16 v[78:79], v108 offset:12352\n\t" \
  "s_waitcnt vmcnt(3) lgkmcnt(10)\n\t" \
  "v_mfma_f32_16x16x32_bf16 v[64:67], v[0:3], v[84:87], v[64:67]\n\t" \
  "v_mfma_f32_16x16x32_bf16 v[68:71], v[16:19], v[84:87], v[68:71]\n\t" \
  "v_mfma_f32_16x16x32_bf16 v[64:67], v[4:7], v[88:91], v[64:67]\n\t" \
  "v_mfma_f32_16x16x32_bf16 v[68:71], v[20:23], v[88:91], v[68:71]\n\t" \
  "v_mfma_f32_16x16x32_bf16 v[64:67], v[8:11], v[92:95], v[64:67]\n\t" \
  "v_mfma_f32_16x16x32_bf16 v[68:71], v[24:27], v[92:95], v[68:71]\n\t" \
  "v_mfma_f32_16x16x32_bf16 v[64:67], v[12:15], v[96:99], v[64:67]\n\t" \
  "v_mfma_f32_16x16x32_bf16 v[68:71], v[28:31], v[96:99], v[68:71]\n\t" \
  "s_waitcnt lgkmcnt(0)\n\t" \
  "s_barrier\n\t" \
  "v_mfma_f32_16x16x32_bf16 v[72:75], v[48:51], v[84:87], 0\n\t" \
  "v_mfma_f32_16x16x32_bf16 v[72:75], v[52:55], v[88:91], v[72:75]\n\t" \
  "v_mfma_f32_16x16x32_bf16 v[72:75], v[56:59], v[92:95], v[72:75]\n\t" \
  "v_mfma_f32_16x16x32_bf16 v[72:75], v[60:63], v[96:99], v[72:75]\n\t" \
  "v_mfma_f32_16x16x32_bf16 v[72:75], v[76:79], v[84:87], v[72:75]\n\t" \
  "s_waitcnt vmcnt(0)\n\t" \
  "ds_write_b128 v101, v[32:35] offset:20480\n\t" \
  "ds_write_b128 v101, v[44:47] offset:21504\n\t" \
  "ds_read_b128 v[48:51], v102 offset:32768\n\t" \
  "ds_read_b128 v[52:55], v102 offset:33792\n\t" \
  "ds_read_b128 v[56:59], v102 offset:34816\n\t" \
  "ds_read_b128 v[60:63], v102 offset:35840\n\t" \
  "ds_read_b128 v[16:19], v102 offset:49152\n\t" \
  "ds_read_b128 v[20:23], v102 offset:50176\n\t" \
  "ds_read_b128 v[24:27], v102 offset:51200\n\t" \
  "ds_read_b128 v[28:31], v102 offset:52224\n\t" \
  "ds_read_b128 v[84:87], v103 offset:16384\n\t" \
  "ds_read_b128 v[88:91], v103 offset:17408\n\t" \
  "ds_read_b128 v[92:95], v103 offset:18432\n\t" \
  "ds_read_b128 v[96:99], v103 offset:19456\n\t" \
  "ds_read_b128 v[0:3], v103 offset:49152\n\t" \
  "ds_read_b128 v[4:7], v103 offset:50176\n\t" \
  "ds_read_b128 v[8:11], v103 offset:51200\n\t" \
  "ds_read_b128 v[12:15], v103 offset:52224\n\t" \
  "v_max3_f32 v76, v64, v65, v66\n\t" \
  "v_max3_f32 v77, v67, v68, v69\n\t" \
  "v_max3_f32 v76, v76, v70, v71\n\t" \
  "v_max3_f32 v77, v77, v72, v73\n\t" \
  "v_max3_f32 v76, v76, v74, v75\n\t" \
  "v_max_f32 v76, v76, v77\n\t" \
  "v_mov_b32 v77, v76\n\t" \
  "s_nop 1\n\t" \
  "v_permlane16_swap_b32 v76, v77\n\t" \
  "v_max_f32 v76, v76, v77\n\t" \
  "v_mov_b32 v77, v76\n\t" \
  "s_nop 1\n\t" \
  "v_permlane32_swap_b32 v76, v77\n\t" \
  "v_max_f32 v76, v76, v77\n\t" \
  "v_mul_f32 v76, %[cs], v76\n\t" \
  "v_fma_f32 v64, v64, %[cs], -v76\n\t" \
  "v_fma_f32 v65, v65, %[cs], -v76\n\t" \
  "v_fma_f32 v66, v66, %[cs], -v76\n\t" \
  "v_fma_f32 v67, v67, %[cs], -v76\n\t" \
  "v_fma_f32 v68, v68, %[cs], -v76\n\t" \
  "v_fma_f32 v69, v69, %[cs], -v76\n\t" \
  "v_fma_f32 v70, v70, %[cs], -v76\n\t" \
  "v_fma_f32 v71, v71, %[cs], -v76\n\t" \
  "v_fma_f32 v72, v72, %[cs], -v76\n\t" \
  "v_fma_f32 v73, v73, %[cs], -v76\n\t" \
  "v_fma_f32 v74, v74, %[cs], -v76\n\t" \
  "v_fma_f32 v75, v75, %[cs], -v76\n\t" \
  "v_exp_f32 v64, v64\n\t" \
  "v_exp_f32 v65, v65\n\t" \
  "v_exp_f32 v66, v66\n\t" \
  "v_exp_f32 v67, v67\n\t" \
  "v_exp_f32 v68, v68\n\t" \
  "v_exp_f32 v69, v69\n\t" \
  "v_exp_f32 v70, v70\n\t" \
  "v_exp_f32 v71, v71\n\t" \
  "v_exp_f32 v72, v72\n\t" \
  "v_exp_f32 v73, v73\n\t" \
  "v_exp_f32 v74, v74\n\t" \
  "v_exp_f32 v75, v75\n\t" \
  "v_add_f32 v76, v72, v73\n\t" \
  "v_add_f32 v77, v74, v75\n\t" \
  "v_add_f32 v78, v64, v65\n\t" \
  "v_add_f32 v79, v66, v67\n\t" \
  "v_add_f32 v76, v76, v77\n\t" \
  "v_add_f32 v78, v78, v68\n\t" \
  "v_add_f32 v79, v79, v69\n\t" \
  "v_add_f32 v78, v78, v70\n\t" \
  "v_add_f32 v79, v79, v71\n\t" \
  "v_fma_f32 v76, v76, 0.5, v78\n\t" \
  "v_add_f32 v76, v76, v79\n\t" \
  "v_mov_b32 v77, v76\n\t" \
  "s_nop 1\n\t" \
  "v_permlane16_swap_b32 v76, v77\n\t" \
  "v_add_f32 v76, v76, v77\n\t" \
  "v_mov_b32 v77, v76\n\t" \
  "s_nop 1\n\t" \
  "v_permlane32_swap_b32 v76, v77\n\t" \
  "v_add_f32 v76, v76, v77\n\t" \
  "v_rcp_f32 v76, v76\n\t" \
  "s_mov_b64 exec, 0xff\n\t" \
  "buffer_load_dword v118, v112, %[rh], s96 offen sc1\n\t" \
  "s_mov_b64 exec, -1\n\t" \
  "v_mul_f32 v64, v64, v76\n\t" \
  "v_mul_f32 v65, v65, v76\n\t" \
  "v_mul_f32 v66, v66, v76\n\t" \
  "v_mul_f32 v67, v67, v76\n\t" \
  "v_mul_f32 v68, v68, v76\n\t" \
  "v_mul_f32 v69, v69, v76\n\t" \
  "v_mul_f32 v70, v70, v76\n\t" \
  "v_mul_f32 v71, v71, v76\n\t" \
  "v_mul_f32 v72, v72, v76\n\t" \
  "v_mul_f32 v73, v73, v76\n\t" \
  "v_mul_f32 v74, v74, v76\n\t" \
  "v_mul_f32 v75, v75, v76\n\t" \
  "v_cvt_pk_bf16_f32 v76, v64, v68\n\t" \
  "v_cvt_pk_bf16_f32 v77, v65, v69\n\t" \
  "v_cvt_pk_bf16_f32 v78, v66, v70\n\t" \
  "v_cvt_pk_bf16_f32 v79, v67, v71\n\t" \
  "v_cvt_pk_bf16_f32 v80, v72, v73\n\t" \
  "v_cvt_pk_bf16_f32 v81, v74, v75\n\t" \
  "v_permlane32_swap_b32 v76, v78\n\t" \
  "v_permlane32_swap_b32 v77, v79\n\t" \
  "v_mov_b64 v[82:83], v[80:81]\n\t" \
  "buffer_store_dwordx4 v[76:79], v110, %[rh], s94 offen sc1\n\t" \
  "s_nop 0\n\t" \
  "v_permlane16_swap_b32 v80, v82\n\t" \
  "v_permlane16_swap_b32 v81, v83\n\t" \
  "buffer_store_dwordx4 v[80:83], v111, %[rh], s94 offen sc1\n\t" \
  "v_readlane_b32 s76, v119, 0\n\t" \
  "v_readlane_b32 s77, v119, 1\n\t" \
  "s_mov_b32 s42, 0\n\t" \
  "s_min_i32 s43, s76, s77\n\t" \
  "s_cmp_eq_u32 s47, s97\n\t" \
  "s_cbranch_scc1 48f\n\t" \
  "s_mov_b32 s42, 32\n\t" \
  "s_cmp_le_i32 s43, s44\n\t" \
  "s_cbranch_scc1 48f\n\t" \
  "s_mov_b32 s42, 0\n\t" \
  "s_waitcnt lgkmcnt(0)\n\t" \
  "buffer_load_dwordx4 v[32:35], v100, %[ru], s97 offen offset:0 sc1\n\t" \
  "buffer_load_dwordx4 v[44:47], v100, %[ru], s97 offen offset:1024 sc1\n\t" \
  "s_waitcnt vmcnt(0)\n\t" \
  "ds_write_b128 v101, v[32:35] offset:20480\n\t" \
  "ds_write_b128 v101, v[44:47] offset:21504\n\t" \
  "48:\n\t" \
  "s_add_i32 s44, s44, 1\n\t" \
  "s_and_b32 s58, s58, 4\n\t" \
  "s_and_b32 s76, s44, 64\n\t" \
  "s_or_b32 s42, s42, s76\n\t" \
  "s_cbranch_scc0 20b\n\t" \
  "s_branch 99f\n\t" \
  "14:\n\t" \
  "s_sleep 3\n\t" \
  "s_add_u32 s59, s59, 0x10000\n\t" \
  "s_cmp_lt_u32 s59, 0x400000\n\t" \
  "s_cbranch_scc1 13b\n\t" \
  "s_mov_b64 exec, -1\n\t" \
  "s_mov_b32 s42, 16\n\t" \
  "s_branch 99f\n\t" \
  "8:\n\t" \
  "s_bitcmp1_b32 s58, 1\n\t" \
  "s_cbranch_scc1 4b\n\t" \
  "s_bitcmp1_b32 s58, 2\n\t" \
  "s_cbranch_scc1 5b\n\t" \
  "v_max3_u32 v116, v32, v33, v34\n\t" \
  "v_max3_u32 v117, v35, v36, v37\n\t" \
  "v_max3_u32 v116, v116, v38, v39\n\t" \
  "v_max_u32 v116, v116, v117\n\t" \
  "v_cmp_eq_u32 vcc, -1, v116\n\t" \
  "s_cbranch_vccz 5b\n\t" \
  "s_branch 0f\n\t" \
  "9:\n\t" \
  "s_bitcmp1_b32 s58, 2\n\t" \
  "s_cbranch_scc1 6b\n\t" \
  "v_max3_u32 v116, v40, v41, v42\n\t" \
  "v_max3_u32 v117, v43, v44, v45\n\t" \
  "v_max3_u32 v116, v116, v46, v47\n\t" \
  "v_max_u32 v116, v116, v117\n\t" \
  "v_cmp_eq_u32 vcc, -1, v116\n\t" \
  "s_cbranch_vccz 6b\n\t" \
  "s_or_b32 s58, s58, 2\n\t" \
  "0:\n\t" \
  "s_or_b32 s58, s58, 1\n\t" \
  "11:\n\t" \
  "buffer_load_dwordx4 v[32:35], v100, %[rh], s40 offen sc1\n\t" \
  "buffer_load_dwordx4 v[36:39], v100, %[rh], s76 offen sc1\n\t" \
  "buffer_load_dwordx4 v[40:43], v100, %[rh], s77 offen sc1\n\t" \
  "buffer_load_dwordx4 v[44:47], v100, %[rh], s78 offen sc1\n\t" \
  "s_waitcnt lgkmcnt(0)\n\t" \
  "s_branch 10b\n\t" \
  "17:\n\t" \
  "s_add_u32 s59, s59, 1\n\t" \
  "s_bitcmp1_b32 s58, 2\n\t" \
  "s_cbranch_scc1 3b\n\t" \
  "s_waitcnt vmcnt(0)\n\t" \
  "v_max_u32 v116, v32, v33\n\t" \
  "v_max3_u32 v116, v116, v34, v35\n\t" \
  "v_max3_u32 v116, v116, v36, v37\n\t" \
  "v_max3_u32 v116, v116, v38, v39\n\t" \
  "v_max3_u32 v116, v116, v40, v41\n\t" \
  "v_max3_u32 v116, v116, v42, v43\n\t" \
  "v_max3_u32 v116, v116, v44, v45\n\t" \
  "v_max3_u32 v116, v116, v46, v47\n\t" \
  "v_cmp_eq_u32 vcc, -1, v116\n\t" \
  "s_cbranch_vccz 3b\n\t" \
  "s_bitcmp1_b32 s59, 6\n\t" \
  "s_cbranch_scc1 12f\n\t" \
  "buffer_load_dwordx4 v[32:35], v100, %[rh], s40 offen sc1\n\t" \
  "buffer_load_dwordx4 v[36:39], v100, %[rh], s76 offen sc1\n\t" \
  "buffer_load_dwordx4 v[40:43], v100, %[rh], s77 offen sc1\n\t" \
  "buffer_load_dwordx4 v[44:47], v100, %[rh], s78 offen sc1\n\t" \
  "s_branch 17b\n\t" \
  "12:\n\t" \
  "s_mov_b32 s42, s58\n\t" \
  "s_branch 99f\n\t" \
  "92:\n\t" \
  "s_mov_b32 s58, s42\n\t" \
  "s_mov_b32 s95, 0\n\t" \
  "20:\n\t" \
  "s_and_b32 s76, s44, 7\n\t" \
  "s_lshl_b32 s76, s76, 15\n\t" \
  "s_add_i32 s40, s54, s76\n\t" \
  "s_and_b32 s76, s44, 3\n\t" \
  "s_lshl_b32 s45, s76, 14\n\t" \
  "s_add_i32 s77, s44, 1\n\t" \
  "s_min_u32 s77, s77, 63\n\t" \
  "s_lshr_b32 s76, s77, 1\n\t" \
  "s_lshl_b32 s47, s77, 20\n\t" \
  "s_add_i32 s47, s47, s55\n\t" \
  "s_bitcmp1_b64 %[sel], s76\n\t" \
  "s_cselect_b32 s76, 0x2000, 0\n\t" \
  "s_add_i32 s97, s47, s76\n\t" \
  "s_cmp_le_i32 s77, s43\n\t" \
  "s_cselect_b32 s47, s97, 0x70000000\n\t" \
  "s_lshr_b32 s76, s44, 1\n\t" \
  "s_lshl_b32 s94, s44, 20\n\t" \
  "s_bitcmp1_b64 %[sel], s76\n\t" \
  "s_cselect_b32 s76, 0x2000, 0\n\t" \
  "s_add_i32 s94, s94, s76\n\t" \
  "s_add_i32 s49, s44, -1\n\t" \
  "s_and_b32 s48, s49, 2\n\t" \
  "s_lshl_b32 s48, s48, 13\n\t" \
  "s_and_b32 s76, s49, 1\n\t" \
  "s_lshl_b32 s76, s76, 12\n\t" \
  "s_add_i32 s48, s48, s76\n\t" \
  "s_lshl_b32 s50, s53, 8\n\t" \
  "s_mov_b32 s59, 0\n\t" \
  "s_mov_b32 s92, s51\n\t" \
  "s_mov_b32 s93, s53\n\t" \
  "s_xor_b32 s76, s40, 0x400\n\t" \
  "s_xor_b32 s77, s40, 0x800\n\t" \
  "s_xor_b32 s78, s40, 0xc00\n\t" \
  "s_cmp_lg_u32 s58, 0\n\t" \
  "s_cbranch_scc1 11f\n\t" \
  "s_cmp_lg_u32 s95, 0\n\t" \
  "s_cbranch_scc1 46f\n\t" \
  "10:\n\t" \
  "ds_read_b128 v[48:51], v103 offset:0\n\t" \
  "ds_read_b128 v[52:55], v103 offset:1024\n\t" \
  "ds_read_b128 v[56:59], v103 offset:2048\n\t" \
  "ds_read_b128 v[60:63], v103 offset:3072\n\t" \
  "ds_read_b128 v[16:19], v103 offset:16384\n\t" \
  "ds_read_b128 v[20:23], v103 offset:17408\n\t" \
  "ds_read_b128 v[24:27], v103 offset:18432\n\t" \
  "ds_read_b128 v[28:31], v103 offset:19456\n\t" \
  "ds_read_b128 v[84:87], v103 offset:49152\n\t" \
  "ds_read_b128 v[88:91], v103 offset:50176\n\t" \
  "ds_read_b128 v[92:95], v103 offset:51200\n\t" \
  "ds_read_b128 v[96:99], v103 offset:52224\n\t" \
  "s_waitcnt lgkmcnt(4)\n\t" \
  "ds_read_b128 v[0:3], v102 offset:16384\n\t" \
  "ds_read_b128 v[4:7], v102 offset:17408\n\t" \
  "ds_read_b128 v[8:11], v102 offset:18432\n\t" \
  "ds_read_b128 v[12:15], v102 offset:19456\n\t" \
  "46:\n\t" \
  "s_bitcmp1_b32 s58, 1\n\t" \
  "s_cselect_b64 s[80:81], -1, 0\n\t" \
  "s_cmp_lg_u32 s58, 0\n\t" \
  "s_cbranch_scc1 17f\n\t" \
  "s_mov_b64 exec, 0xff\n\t" \
  "s_cmp_eq_u32 s95, 0\n\t" \
  "s_cbranch_scc1 16f\n\t" \
  "s_memtime vcc\n\t" \
  "s_memrealtime s[82:83]\n\t" \
  "s_nop 4\n\t" \
  "s_memrealtime s[84:85]\n\t" \
  "s_nop 4\n\t" \
  "s_memrealtime s[56:57]\n\t" \
  "s_nop 4\n\t" \
  "s_memrealtime s[86:87]\n\t" \
  "s_nop 4\n\t" \
  "s_memrealtime s[88:89]\n\t" \
  "s_nop 4\n\t" \
  "s_memrealtime s[90:91]\n\t" \
  "s_mov_b32 s92, 0\n\t" \
  "s_waitcnt lgkmcnt(0)\n\t" \
  "s_cmp_eq_u32 s82, s84\n\t" \
  "s_addc_u32 s92, s92, 0\n\t" \
  "s_cmp_eq_u32 s82, s56\n\t" \
  "s_addc_u32 s92, s92, 0\n\t" \
  "s_cmp_eq_u32 s82, s86\n\t" \
  "s_addc_u32 s92, s92, 0\n\t" \
  "s_cmp_eq_u32 s82, s88\n\t" \
  "s_addc_u32 s92, s92, 0\n\t" \
  "s_cmp_eq_u32 s82, s90\n\t" \
  "s_addc_u32 s92, s92, 0\n\t" \
  "s_mul_i32 s92, s92, 24\n\t" \
  "s_add_i32 s92, s92, vcc_lo\n\t" \
  "s_lshl4_add_u32 s93, s82, 64\n\t" \
  "s_lshl_b32 s50, s93, 8\n\t" \
  "s_waitcnt vmcnt(2)\n\t" \
  "v_mov_b32 v116, v118\n\t" \
  "s_branch 15f\n\t" \
  "16:\n\t" \
  "s_memtime vcc\n\t" \
  "s_waitcnt lgkmcnt(0)\n\t" \
  "s_sub_i32 s79, s41, vcc_lo\n\t" \
  "s_cmp_gt_i32 s79, 64\n\t" \
  "s_cbranch_scc1 16b\n\t" \
  "13:\n\t" \
  "buffer_load_dword v116, v112, %[rh], s48 offen sc1\n\t" \
  "s_memtime vcc\n\t" \
  "s_memrealtime s[82:83]\n\t" \
  "s_nop 4\n\t" \
  "s_memrealtime s[84:85]\n\t" \
  "s_nop 4\n\t" \
  "s_memrealtime s[56:57]\n\t" \
  "s_nop 4\n\t" \
  "s_memrealtime s[86:87]\n\t" \
  "s_nop 4\n\t" \
  "s_memrealtime s[88:89]\n\t" \
  "s_nop 4\n\t" \
  "s_memrealtime s[90:91]\n\t" \
  "s_mov_b32 s92, 0\n\t" \
  "s_waitcnt lgkmcnt(0)\n\t" \
  "s_cmp_eq_u32 s82, s84\n\t" \
  "s_addc_u32 s92, s92, 0\n\t" \
  "s_cmp_eq_u32 s82, s56\n\t" \
  "s_addc_u32 s92, s92, 0\n\t" \
  "s_cmp_eq_u32 s82, s86\n\t" \
  "s_addc_u32 s92, s92, 0\n\t" \
  "s_cmp_eq_u32 s82, s88\n\t" \
  "s_addc_u32 s92, s92, 0\n\t" \
  "s_cmp_eq_u32 s82, s90\n\t" \
  "s_addc_u32 s92, s92, 0\n\t" \
  "s_mul_i32 s92, s92, 24\n\t" \
  "s_add_i32 s92, s92, vcc_lo\n\t" \
  "s_lshl4_add_u32 s93, s82, 64\n\t" \
  "s_lshl_b32 s50, s93, 8\n\t" \
  "s_waitcnt vmcnt(0)\n\t" \
  "15:\n\t" \
  "v_mov_b32 v117, 20000\n\t" \
  "v_cmp_ne_u32_sdwa vcc, v116, s49 src0_sel:BYTE_0 src1_sel:DWORD\n\t" \
  "v_subrev_u32 v116, s50, v116\n\t" \
  "v_ashrrev_i32 v116, 8, v116\n\t" \
  "s_nop 1\n\t" \
  "v_max_i32_dpp v116, v116, v116 quad_perm:[1,0,3,2] row_mask:0xf bank_mask:0xf\n\t" \
  "s_cbranch_vccnz 14f\n\t" \
  "s_nop 0\n\t" \
  "v_max_i32_dpp v116, v116, v116 quad_perm:[2,3,0,1] row_mask:0xf bank_mask:0xf\n\t" \
  "s_nop 1\n\t" \
  "v_max_i32_dpp v116, v116, v116 row_half_mirror row_mask:0xf bank_mask:0xf\n\t" \
  "v_med3_i32 v116, v116, 0, v117\n\t" \
  "v_mul_u32_u24 v116, s52, v116\n\t" \
  "v_lshrrev_b32 v116, 16, v116\n\t" \
  "s_mov_b64 exec, -1\n\t" \
  "v_readfirstlane_b32 s84, v116\n\t" \
  "s_add_i32 s84, s84, s92\n\t" \
  ".p2align 6\n\t" \
  "s_nop 0\n\t" \
  "s_nop 0\n\t" \
  "s_nop 0\n\t" \
  "s_nop 0\n\t" \
  "s_nop 0\n\t" \
  "s_nop 0\n\t" \
  "s_nop 0\n\t" \
  "s_nop 0\n\t" \
  "1:\n\t" \
  "s_memtime vcc\n\t" \
  "s_waitcnt lgkmcnt(0)\n\t" \
  "s_sub_i32 s79, s84, vcc_lo\n\t" \
  "s_cmp_gt_i32 s79, 128\n\t" \
  "s_cbranch_scc1 1b\n\t" \
  ".p2align 4\n\t" \
  "2:\n\t" \
  "s_sub_i32 s79, s79, 33\n\t" \
  "s_cmp_gt_i32 s79, 56\n\t" \
  "s_cbranch_scc1 2b\n\t" \
  "buffer_load_dwordx4 v[32:35], v100, %[rh], s40 offen sc1\n\t" \
  "s_nop 12\n\t" \
  "buffer_load_dwordx4 v[36:39], v100, %[rh], s76 offen sc1\n\t" \
  "s_nop 12\n\t" \
  "buffer_load_dwordx4 v[40:43], v100, %[rh], s77 offen sc1\n\t" \
  "s_nop 12\n\t" \
  "buffer_load_dwordx4 v[44:47], v100, %[rh], s78 offen sc1\n\t" \
  "3:\n\t" \
  "v_mov_b64 v[68:69], 0\n\t" \
  "v_mov_b64 v[70:71], 0\n\t" \
  "v_mov_b64 v[72:73], 0\n\t" \
  "v_mov_b64 v[74:75], 0\n\t" \
  "v_mov_b64 v[76:77], 0\n\t" \
  "v_mov_b64 v[78:79], 0\n\t" \
  "v_mov_b64 v[80:81], 0\n\t" \
  "v_mov_b64 v[82:83], 0\n\t" \
  "s_waitcnt lgkmcnt(0)\n\t" \
  "s_waitcnt vmcnt(3)\n\t" \
  "v_smfmac_f32_16x16x64_bf16 v[68:71], v[32:35], a[192:199], v104\n\t" \
  "ds_write_b128 v101, v[32:35] offset:8192\n\t" \
  "v_smfmac_f32_16x16x64_bf16 v[72:75], v[32:35], a[200:207], v104\n\t" \
  "v_smfmac_f32_16x16x64_bf16 v[76:79], v[32:35], v[48:55], v104\n\t" \
  "v_smfmac_f32_16x16x64_bf16 v[80:83], v[32:35], v[56:63], v104\n\t" \
  "s_waitcnt vmcnt(2)\n\t" \
  "v_smfmac_f32_16x16x64_bf16 v[68:71], v[36:39], a[208:215], v104\n\t" \
  "ds_write_b128 v101, v[36:39] offset:9216\n\t" \
  "v_smfmac_f32_16x16x64_bf16 v[72:75], v[36:39], a[216:223], v104\n\t" \
  "v_smfmac_f32_16x16x64_bf16 v[76:79], v[36:39], a[224:231], v104\n\t" \
  "v_cmp_u_f32 vcc, v68, v69\n\t" \
  "v_smfmac_f32_16x16x64_bf16 v[80:83], v[36:39], a[232:239], v104\n\t" \
  "v_cmp_u_f32 s[82:83], v70, v71\n\t" \
  "s_waitcnt vmcnt(1)\n\t" \
  "v_smfmac_f32_16x16x64_bf16 v[68:71], v[40:43], v[16:23], v104\n\t" \
  "ds_write_b128 v101, v[40:43] offset:10240\n\t" \
  "v_smfmac_f32_16x16x64_bf16 v[72:75], v[40:43], v[24:31], v104\n\t" \
  "s_or_b64 vcc, vcc, s[82:83]\n\t" \
  "s_or_b64 vcc, vcc, s[80:81]\n\t" \
  "s_waitcnt lgkmcnt(1)\n\t" \
  "v_smfmac_f32_16x16x64_bf16 v[76:79], v[40:43], a[240:247], v104\n\t" \
  "s_cbranch_scc1 8f\n\t" \
  "5:\n\t" \
  "s_barrier\n\t" \
  "4:\n\t" \
  "s_memtime s[56:57]\n\t" \
  "ds_read_b128 v[48:51], v101 offset:12288\n\t" \
  "v_smfmac_f32_16x16x64_bf16 v[80:83], v[40:43], a[248:255], v104\n\t" \
  "ds_read_b128 v[52:55], v101 offset:13312\n\t" \
  "ds_read_b128 v[56:59], v101 offset:0\n\t" \
  "s_waitcnt vmcnt(0)\n\t" \
  "v_smfmac_f32_16x16x64_bf16 v[68:71], v[44:47], v[120:127], v104\n\t" \
  "v_smfmac_f32_16x16x64_bf16 v[72:75], v[44:47], v[128:135], v104\n\t" \
  "ds_write_b128 v101, v[44:47] offset:11264\n\t" \
  "v_smfmac_f32_16x16x64_bf16 v[76:79], v[44:47], v[136:143], v104\n\t" \
  "v_cmp_u_f32 vcc, v68, v69\n\t" \
  "v_smfmac_f32_16x16x64_bf16 v[80:83], v[44:47], v[144:151], v104\n\t" \
  "v_cmp_u_f32 s[82:83], v70, v71\n\t" \
  "s_waitcnt lgkmcnt(3)\n\t" \
  "v_smfmac_f32_16x16x64_bf16 v[68:71], v[48:51], v[152:159], v104\n\t" \
  "v_smfmac_f32_16x16x64_bf16 v[72:75], v[48:51], v[160:167], v104\n\t" \
  "s_or_b64 vcc, vcc, s[82:83]\n\t" \
  "s_waitcnt lgkmcnt(0)\n\t" \
  "v_smfmac_f32_16x16x64_bf16 v[68:71], v[52:55], v[168:175], v104\n\t" \
  "s_cbranch_scc1 9f\n\t" \
  "6:\n\t" \
  "s_barrier\n\t" \
  "ds_read_b128 v[60:63], v101 offset:1024\n\t" \
  "v_smfmac_f32_16x16x64_bf16 v[72:75], v[52:55], v[176:183], v104\n\t" \
  "ds_read_b128 v[64:67], v101 offset:4096\n\t" \
  "v_smfmac_f32_16x16x64_bf16 v[68:71], v[56:59], a[0:7], v104\n\t" \
  "v_smfmac_f32_16x16x64_bf16 v[72:75], v[56:59], a[8:15], v104\n\t" \
  "ds_read_b128 v[48:51], v101 offset:5120\n\t" \
  "s_waitcnt lgkmcnt(2)\n\t" \
  "v_smfmac_f32_16x16x64_bf16 v[68:71], v[60:63], a[16:23], v104\n\t" \
  "ds_read_b128 v[52:55], v101 offset:14336\n\t" \
  "v_smfmac_f32_16x16x64_bf16 v[72:75], v[60:63], a[24:31], v104\n\t" \
  "ds_read_b128 v[56:59], v101 offset:15360\n\t" \
  "s_waitcnt lgkmcnt(3)\n\t" \
  "v_smfmac_f32_16x16x64_bf16 v[68:71], v[64:67], a[96:103], v104\n\t" \
  "ds_read_b128 v[60:63], v101 offset:2048\n\t" \
  "v_smfmac_f32_16x16x64_bf16 v[72:75], v[64:67], a[104:111], v104\n\t" \
  "s_waitcnt lgkmcnt(3)\n\t" \
  "v_smfmac_f32_16x16x64_bf16 v[68:71], v[48:51], a[112:119], v104\n\t" \
  "ds_read_b128 v[64:67], v101 offset:3072\n\t" \
  "ds_read_b128 v[16:19], v102 offset:49152\n\t" \
  "v_smfmac_f32_16x16x64_bf16 v[72:75], v[48:51], a[120:127], v104\n\t" \
  "s_waitcnt lgkmcnt(4)\n\t" \
  "v_smfmac_f32_16x16x64_bf16 v[68:71], v[52:55], v[84:91], v104\n\t" \
  "ds_read_b128 v[48:51], v101 offset:7168\n\t" \
  "ds_read_b128 v[20:23], v102 offset:50176\n\t" \
  "v_smfmac_f32_16x16x64_bf16 v[72:75], v[52:55], v[92:99], v104\n\t" \
  "s_waitcnt lgkmcnt(5)\n\t" \
  "v_smfmac_f32_16x16x64_bf16 v[68:71], v[56:59], v[216:223], v104\n\t" \
  "ds_read_b128 v[52:55], v101 offset:6144\n\t" \
  "ds_read_b128 v[24:27], v102 offset:51200\n\t" \
  "v_smfmac_f32_16x16x64_bf16 v[72:75], v[56:59], v[224:231], v104\n\t" \
  "ds_read_b128 v[28:31], v102 offset:52224\n\t" \
  "s_waitcnt lgkmcnt(7)\n\t" \
  "v_smfmac_f32_16x16x64_bf16 v[68:71], v[60:63], v[0:7], v104\n\t" \
  "v_smfmac_f32_16x16x64_bf16 v[72:75], v[60:63], v[8:15], v104\n\t" \
  "buffer_load_dwordx4 v[84:87], v107, %[ru], s94 offen offset:0 sc1\n\t" \
  "s_waitcnt lgkmcnt(6)\n\t" \
  "v_smfmac_f32_16x16x64_bf16 v[68:71], v[64:67], a[64:71], v104\n\t" \
  "ds_read_b128 v[0:3], v103 offset:32768\n\t" \
  "v_smfmac_f32_16x16x64_bf16 v[72:75], v[64:67], a[72:79], v104\n\t" \
  "ds_read_b128 v[4:7], v103 offset:33792\n\t" \
  "s_waitcnt lgkmcnt(6)\n\t" \
  "v_smfmac_f32_16x16x64_bf16 v[68:71], v[48:51], a[160:167], v104\n\t" \
  "buffer_load_dwordx4 v[88:91], v107, %[ru], s94 offen offset:1024 sc1\n\t" \
  "s_waitcnt lgkmcnt(4)\n\t" \
  "v_smfmac_f32_16x16x64_bf16 v[68:71], v[52:55], v[16:23], v104\n\t" \
  "ds_read_b128 v[8:11], v103 offset:34816\n\t" \
  "v_smfmac_f32_16x16x64_bf16 v[72:75], v[48:51], a[168:175], v104\n\t" \
  "ds_read_b128 v[12:15], v103 offset:35840\n\t" \
  "s_waitcnt lgkmcnt(4)\n\t" \
  "v_smfmac_f32_16x16x64_bf16 v[72:75], v[52:55], v[24:31], v104\n\t" \
  "v_add_f32 v44, v68, v70\n\t" \
  "v_smfmac_f32_16x16x64_bf16 v[76:79], v[56:59], v[232:239], v104\n\t" \
  "v_add_f32 v45, v69, v71\n\t" \
  "v_cvt_pk_bf16_f32 v44, v44, v45\n\t" \
  "v_smfmac_f32_16x16x64_bf16 v[80:83], v[56:59], v[240:247], v104\n\t" \
  "v_add_f32 v46, v72, v74\n\t" \
  "v_add_f32 v47, v73, v75\n\t" \
  "v_smfmac_f32_16x16x64_bf16 v[76:79], v[60:63], a[48:55], v104\n\t" \
  "v_cvt_pk_bf16_f32 v46, v46, v47\n\t" \
  "ds_write2st64_b32 v105, v44, v46 offset1:1\n\t" \
  "v_smfmac_f32_16x16x64_bf16 v[80:83], v[60:63], a[56:63], v104\n\t" \
  "ds_read_b128 v[56:59], v101 offset:12288\n\t" \
  "v_smfmac_f32_16x16x64_bf16 v[76:79], v[64:67], a[80:87], v104\n\t" \
  "ds_read_b128 v[16:19], v102 offset:0\n\t" \
  "v_smfmac_f32_16x16x64_bf16 v[80:83], v[64:67], a[88:95], v104\n\t" \
  "ds_read_b128 v[20:23], v102 offset:1024\n\t" \
  "v_smfmac_f32_16x16x64_bf16 v[76:79], v[48:51], a[176:183], v104\n\t" \
  "s_waitcnt lgkmcnt(3)\n\t" \
  "s_barrier\n\t" \
  "v_smfmac_f32_16x16x64_bf16 v[80:83], v[48:51], a[184:191], v104\n\t" \
  "buffer_load_dwordx4 v[92:95], v107, %[ru], s94 offen offset:2048 sc1\n\t" \
  "v_smfmac_f32_16x16x64_bf16 v[76:79], v[52:55], a[144:151], v104\n\t" \
  "ds_read_b128 v[60:63], v101 offset:5120\n\t" \
  "ds_read_b128 v[24:27], v102 offset:2048\n\t" \
  "v_smfmac_f32_16x16x64_bf16 v[80:83], v[52:55], a[152:159], v104\n\t" \
  "ds_read_b128 v[64:67], v101 offset:14336\n\t" \
  "buffer_load_dwordx4 v[96:99], v107, %[ru], s94 offen offset:3072 sc1\n\t" \
  "s_waitcnt lgkmcnt(5)\n\t" \
  "v_smfmac_f32_16x16x64_bf16 v[76:79], v[56:59], v[0:7], v104\n\t" \
  "ds_read_b128 v[48:51], v101 offset:0\n\t" \
  "ds_read_b128 v[28:31], v102 offset:3072\n\t" \
  "v_smfmac_f32_16x16x64_bf16 v[80:83], v[56:59], v[8:15], v104\n\t" \
  "global_load_dword v119, v109, %[kc] sc1\n\t" \
  "s_waitcnt lgkmcnt(4)\n\t" \
  "v_smfmac_f32_16x16x64_bf16 v[76:79], v[60:63], a[128:135], v104\n\t" \
  "ds_read_b128 v[52:55], v101 offset:13312\n\t" \
  "v_smfmac_f32_16x16x64_bf16 v[80:83], v[60:63], a[136:143], v104\n\t" \
  "ds_read_b128 v[0:3], v102 offset:32768\n\t" \
  "buffer_load_dwordx4 v[32:35], v100, %[ru], s47 offen offset:0 sc1\n\t" \
  "s_waitcnt lgkmcnt(4)\n\t" \
  "v_smfmac_f32_16x16x64_bf16 v[76:79], v[64:67], v[200:207], v104\n\t" \
  "ds_read_b128 v[56:59], v101 offset:1024\n\t" \
  "v_smfmac_f32_16x16x64_bf16 v[80:83], v[64:67], v[208:215], v104\n\t" \
  "ds_read_b128 v[60:63], v101 offset:4096\n\t" \
  "ds_read_b128 v[4:7], v102 offset:33792\n\t" \
  "s_waitcnt lgkmcnt(6)\n\t" \
  "v_smfmac_f32_16x16x64_bf16 v[76:79], v[48:51], v[16:23], v104\n\t" \
  "ds_read_b128 v[8:11], v102 offset:34816\n\t" \
  "buffer_load_dwordx4 v[36:39], v100, %[ru], s47 offen offset:1024 sc1\n\t" \
  "s_waitcnt lgkmcnt(6)\n\t" \
  "v_smfmac_f32_16x16x64_bf16 v[80:83], v[48:51], v[24:31], v104\n\t" \
  "ds_read_b128 v[12:15], v102 offset:35840\n\t" \
  "s_waitcnt lgkmcnt(6)\n\t" \
  "v_smfmac_f32_16x16x64_bf16 v[76:79], v[52:55], v[184:191], v104\n\t" \
  "v_smfmac_f32_16x16x64_bf16 v[80:83], v[52:55], v[192:199], v104\n\t" \
  "buffer_load_dwordx4 v[40:43], v100, %[ru], s47 offen offset:2048 sc1\n\t" \
  "s_waitcnt lgkmcnt(4)\n\t" \
  "v_smfmac_f32_16x16x64_bf16 v[76:79], v[56:59], a[32:39], v104\n\t" \
  "s_waitcnt lgkmcnt(2)\n\t" \
  "v_smfmac_f32_16x16x64_bf16 v[76:79], v[60:63], v[0:7], v104\n\t" \
  "v_smfmac_f32_16x16x64_bf16 v[80:83], v[56:59], a[40:47], v104\n\t" \
  "s_waitcnt lgkmcnt(0)\n\t" \
  "v_smfmac_f32_16x16x64_bf16 v[80:83], v[60:63], v[8:15], v104\n\t" \
  "v_add_f32 v20, v76, v78\n\t" \
  "v_add_f32 v21, v77, v79\n\t" \
  "v_cvt_pk_bf16_f32 v20, v20, v21\n\t" \
  "ds_write_b32 v106, v20\n\t" \
  "s_nop 3\n\t" \
  "v_add_f32 v23, v80, v82\n\t" \
  "v_add_f32 v21, v81, v83\n\t" \
  "v_cvt_pk_bf16_f32 v21, v23, v21\n\t" \
  "ds_write_b32 v106, v21 offset:256\n\t" \
  "s_waitcnt lgkmcnt(0)\n\t" \
  "s_barrier\n\t" \
  "v_bfe_i32 v64, v114, 0, 1\n\t" \
  "v_and_b32 v64, 0xff800000, v64\n\t" \
  "v_bfe_i32 v65, v114, 1, 1\n\t" \
  "v_and_b32 v65, 0xff800000, v65\n\t" \
  "v_bfe_i32 v66, v114, 2, 1\n\t" \
  "v_and_b32 v66, 0xff800000, v66\n\t" \
  "v_bfe_i32 v67, v114, 3, 1\n\t" \
  "v_and_b32 v67, 0xff800000, v67\n\t" \
  "v_bfe_i32 v68, v114, 4, 1\n\t" \
  "v_and_b32 v68, 0xff800000, v68\n\t" \
  "v_bfe_i32 v69, v114, 5, 1\n\t" \
  "v_and_b32 v69, 0xff800000, v69\n\t" \
  "v_bfe_i32 v70, v114, 6, 1\n\t" \
  "v_and_b32 v70, 0xff800000, v70\n\t" \
  "v_bfe_i32 v71, v114, 7, 1\n\t" \
  "v_and_b32 v71, 0xff800000, v71\n\t" \
  "s_lshl_b32 s94, s45, 4\n\t" \
  "s_and_b32 s96, s44, 2\n\t" \
  "s_lshl_b32 s96, s96, 13\n\t" \
  "s_and_b32 s76, s44, 1\n\t" \
  "s_lshl_b32 s76, s76, 12\n\t" \
  "s_add_i32 s96, s96, s76\n\t" \
  "s_mov_b32 s95, 1\n\t" \
  "s_add_i32 s41, s56, 1950\n\t" \
  "s_sub_i32 s76, s92, s51\n\t" \
  "s_sub_i32 s77, s93, s53\n\t" \
  "s_cmp_eq_u32 s76, 0\n\t" \
  "s_cbranch_scc1 21f\n\t" \
  "v_cvt_f32_u32 v117, s76\n\t" \
  "v_cvt_f32_u32 v116, s77\n\t" \
  "v_rcp_f32 v117, v117\n\t" \
  "s_mov_b32 s51, s92\n\t" \
  "v_mul_f32 v117, v117, v116\n\t" \
  "v_max_f32 v117, 0x3f028f5c, v117\n\t" \
  "v_min_f32 v117, 0x3ffeb852, v117\n\t" \
  "v_rcp_f32 v116, v117\n\t" \
  "v_mul_f32 v117, 0x4f000000, v117\n\t" \
  "v_mul_f32 v116, 0x47800000, v116\n\t" \
  "v_cvt_u32_f32 v117, v117\n\t" \
  "v_cvt_u32_f32 v116, v116\n\t" \
  "s_mov_b32 s53, s93\n\t" \
  "v_readfirstlane_b32 s46, v117\n\t" \
  "v_readfirstlane_b32 s52, v116\n\t" \
  "21:\n\t" \
  "ds_read_b128 v[0:3], v101 offset:20480\n\t" \
  "ds_read_b128 v[4:7], v101 offset:21504\n\t" \
  "ds_read_b128 v[8:11], v101 offset:22528\n\t" \
  "ds_read_b128 v[12:15], v101 offset:23552\n\t" \
  "ds_read_b128 v[16:19], v101 offset:24576\n\t" \
  "ds_read_b128 v[20:23], v101 offset:25600\n\t" \
  "ds_read_b128 v[24:27], v101 offset:26624\n\t" \
  "ds_read_b128 v[28:31], v101 offset:27648\n\t" \
  "ds_read_b64_tr_b16 v[48:49], v108 offset:0\n\t" \
  "ds_read_b64_tr_b16 v[50:51], v108 offset:64\n\t" \
  "ds_read_b64_tr_b16 v[52:53], v108 offset:512\n\t" \
  "ds_read_b64_tr_b16 v[54:55], v108 offset:576\n\t" \
  "ds_read_b64_tr_b16 v[56:57], v108 offset:1024\n\t" \
  "ds_read_b64_tr_b16 v[58:59], v108 offset:1088\n\t" \
  "ds_read_b64_tr_b16 v[60:61], v108 offset:1536\n\t" \
  "ds_read_b64_tr_b16 v[62:63], v108 offset:1600\n\t" \
  "ds_read_b64_tr_b16 v[76:77], v108 offset:12288\n\t" \
  "ds_read_b64_tr_b16 v[78:79], v108 offset:12352\n\t" \
  "s_waitcnt vmcnt(4) lgkmcnt(10)\n\t" \
  "v_mfma_f32_16x16x32_bf16 v[64:67], v[0:3], v[84:87], v[64:67]\n\t" \
  "v_mfma_f32_16x16x32_bf16 v[68:71], v[16:19], v[84:87], v[68:71]\n\t" \
  "v_mfma_f32_16x16x32_bf16 v[64:67], v[4:7], v[88:91], v[64:67]\n\t" \
  "v_mfma_f32_16x16x32_bf16 v[68:71], v[20:23], v[88:91], v[68:71]\n\t" \
  "v_mfma_f32_16x16x32_bf16 v[64:67], v[8:11], v[92:95], v[64:67]\n\t" \
  "v_mfma_f32_16x16x32_bf16 v[68:71], v[24:27], v[92:95], v[68:71]\n\t" \
  "v_mfma_f32_16x16x32_bf16 v[64:67], v[12:15], v[96:99], v[64:67]\n\t" \
  "v_mfma_f32_16x16x32_bf16 v[68:71], v[28:31], v[96:99], v[68:71]\n\t" \
  "s_waitcnt lgkmcnt(0)\n\t" \
  "s_barrier\n\t" \
  "v_mfma_f32_16x16x32_bf16 v[72:75], v[48:51], v[84:87], 0\n\t" \
  "v_mfma_f32_16x16x32_bf16 v[72:75], v[52:55], v[88:91], v[72:75]\n\t" \
  "v_mfma_f32_16x16x32_bf16 v[72:75], v[56:59], v[92:95], v[72:75]\n\t" \
  "v_mfma_f32_16x16x32_bf16 v[72:75], v[60:63], v[96:99], v[72:75]\n\t" \
  "v_mfma_f32_16x16x32_bf16 v[72:75], v[76:79], v[84:87], v[72:75]\n\t" \
  "s_waitcnt vmcnt(0)\n\t" \
  "ds_write_b128 v101, v[32:35] offset:22528\n\t" \
  "ds_write_b128 v101, v[36:39] offset:23552\n\t" \
  "ds_write_b128 v101, v[40:43] offset:24576\n\t" \
  "ds_read_b128 v[48:51], v103 offset:0\n\t" \
  "ds_read_b128 v[52:55], v103 offset:1024\n\t" \
  "ds_read_b128 v[56:59], v103 offset:2048\n\t" \
  "ds_read_b128 v[60:63], v103 offset:3072\n\t" \
  "ds_read_b128 v[16:19], v103 offset:16384\n\t" \
  "ds_read_b128 v[20:23], v103 offset:17408\n\t" \
  "ds_read_b128 v[24:27], v103 offset:18432\n\t" \
  "ds_read_b128 v[28:31], v103 offset:19456\n\t" \
  "ds_read_b128 v[84:87], v103 offset:49152\n\t" \
  "ds_read_b128 v[88:91], v103 offset:50176\n\t" \
  "ds_read_b128 v[92:95], v103 offset:51200\n\t" \
  "ds_read_b128 v[96:99], v103 offset:52224\n\t" \
  "ds_read_b128 v[0:3], v102 offset:16384\n\t" \
  "ds_read_b128 v[4:7], v102 offset:17408\n\t" \
  "ds_read_b128 v[8:11], v102 offset:18432\n\t" \
  "ds_read_b128 v[12:15], v102 offset:19456\n\t" \
  "v_max3_f32 v76, v64, v65, v66\n\t" \
  "v_max3_f32 v77, v67, v68, v69\n\t" \
  "v_max3_f32 v76, v76, v70, v71\n\t" \
  "v_max3_f32 v77, v77, v72, v73\n\t" \
  "v_max3_f32 v76, v76, v74, v75\n\t" \
  "v_max_f32 v76, v76, v77\n\t" \
  "v_mov_b32 v77, v76\n\t" \
  "s_nop 1\n\t" \
  "v_permlane16_swap_b32 v76, v77\n\t" \
  "v_max_f32 v76, v76, v77\n\t" \
  "v_mov_b32 v77, v76\n\t" \
  "s_nop 1\n\t" \
  "v_permlane32_swap_b32 v76, v77\n\t" \
  "v_max_f32 v76, v76, v77\n\t" \
  "v_mul_f32 v76, %[cs], v76\n\t" \
  "v_fma_f32 v64, v64, %[cs], -v76\n\t" \
  "v_fma_f32 v65, v65, %[cs], -v76\n\t" \
  "v_fma_f32 v66, v66, %[cs], -v76\n\t" \
  "v_fma_f32 v67, v67, %[cs], -v76\n\t" \
  "v_fma_f32 v68, v68, %[cs], -v76\n\t" \
  "v_fma_f32 v69, v69, %[cs], -v76\n\t" \
  "v_fma_f32 v70, v70, %[cs], -v76\n\t" \
  "v_fma_f32 v71, v71, %[cs], -v76\n\t" \
  "v_fma_f32 v72, v72, %[cs], -v76\n\t" \
  "v_fma_f32 v73, v73, %[cs], -v76\n\t" \
  "v_fma_f32 v74, v74, %[cs], -v76\n\t" \
  "v_fma_f32 v75, v75, %[cs], -v76\n\t" \
  "v_exp_f32 v64, v64\n\t" \
  "v_exp_f32 v65, v65\n\t" \
  "v_exp_f32 v66, v66\n\t" \
  "v_exp_f32 v67, v67\n\t" \
  "v_exp_f32 v68, v68\n\t" \
  "v_exp_f32 v69, v69\n\t" \
  "v_exp_f32 v70, v70\n\t" \
  "v_exp_f32 v71, v71\n\t" \
  "v_exp_f32 v72, v72\n\t" \
  "v_exp_f32 v73, v73\n\t" \
  "v_exp_f32 v74, v74\n\t" \
  "v_exp_f32 v75, v75\n\t" \
  "v_add_f32 v76, v72, v73\n\t" \
  "v_add_f32 v77, v74, v75\n\t" \
  "v_add_f32 v78, v64, v65\n\t" \
  "v_add_f32 v79, v66, v67\n\t" \
  "v_add_f32 v76, v76, v77\n\t" \
  "v_add_f32 v78, v78, v68\n\t" \
  "v_add_f32 v79, v79, v69\n\t" \
  "v_add_f32 v78, v78, v70\n\t" \
  "v_add_f32 v79, v79, v71\n\t" \
  "v_fma_f32 v76, v76, 0.5, v78\n\t" \
  "v_add_f32 v76, v76, v79\n\t" \
  "v_mov_b32 v77, v76\n\t" \
  "s_nop 1\n\t" \
  "v_permlane16_swap_b32 v76, v77\n\t" \
  "v_add_f32 v76, v76, v77\n\t" \
  "v_mov_b32 v77, v76\n\t" \
  "s_nop 1\n\t" \
  "v_permlane32_swap_b32 v76, v77\n\t" \
  "v_add_f32 v76, v76, v77\n\t" \
  "v_rcp_f32 v76, v76\n\t" \
  "s_mov_b64 exec, 0xff\n\t" \
  "buffer_load_dword v118, v112, %[rh], s96 offen sc1\n\t" \
  "s_mov_b64 exec, -1\n\t" \
  "v_mul_f32 v64, v64, v76\n\t" \
  "v_mul_f32 v65, v65, v76\n\t" \
  "v_mul_f32 v66, v66, v76\n\t" \
  "v_mul_f32 v67, v67, v76\n\t" \
  "v_mul_f32 v68, v68, v76\n\t" \
  "v_mul_f32 v69, v69, v76\n\t" \
  "v_mul_f32 v70, v70, v76\n\t" \
  "v_mul_f32 v71, v71, v76\n\t" \
  "v_mul_f32 v72, v72, v76\n\t" \
  "v_mul_f32 v73, v73, v76\n\t" \
  "v_mul_f32 v74, v74, v76\n\t" \
  "v_mul_f32 v75, v75, v76\n\t" \
  "v_cvt_pk_bf16_f32 v76, v64, v68\n\t" \
  "v_cvt_pk_bf16_f32 v77, v65, v69\n\t" \
  "v_cvt_pk_bf16_f32 v78, v66, v70\n\t" \
  "v_cvt_pk_bf16_f32 v79, v67, v71\n\t" \
  "v_cvt_pk_bf16_f32 v80, v72, v73\n\t" \
  "v_cvt_pk_bf16_f32 v81, v74, v75\n\t" \
  "v_permlane32_swap_b32 v76, v78\n\t" \
  "v_permlane32_swap_b32 v77, v79\n\t" \
  "v_mov_b64 v[82:83], v[80:81]\n\t" \
  "buffer_store_dwordx4 v[76:79], v110, %[rh], s94 offen sc1\n\t" \
  "s_nop 0\n\t" \
  "v_permlane16_swap_b32 v80, v82\n\t" \
  "v_permlane16_swap_b32 v81, v83\n\t" \
  "buffer_store_dwordx4 v[80:83], v111, %[rh], s94 offen sc1\n\t" \
  "v_readlane_b32 s76, v119, 0\n\t" \
  "v_readlane_b32 s77, v119, 1\n\t" \
  "s_mov_b32 s42, 0\n\t" \
  "s_min_i32 s43, s76, s77\n\t" \
  "s_cmp_eq_u32 s47, s97\n\t" \
  "s_cbranch_scc1 48f\n\t" \
  "s_mov_b32 s42, 32\n\t" \
  "s_cmp_le_i32 s43, s44\n\t" \
  "s_cbranch_scc1 48f\n\t" \
  "s_mov_b32 s42, 0\n\t" \
  "s_waitcnt lgkmcnt(0)\n\t" \
  "buffer_load_dwordx4 v[32:35], v100, %[ru], s97 offen offset:0 sc1\n\t" \
  "buffer_load_dwordx4 v[36:39], v100, %[ru], s97 offen offset:1024 sc1\n\t" \
  "buffer_load_dwordx4 v[40:43], v100, %[ru], s97 offen offset:2048 sc1\n\t" \
  "s_waitcnt vmcnt(0)\n\t" \
  "ds_write_b128 v101, v[32:35] offset:22528\n\t" \
  "ds_write_b128 v101, v[36:39] offset:23552\n\t" \
  "ds_write_b128 v101, v[40:43] offset:24576\n\t" \
  "48:\n\t" \
  "s_add_i32 s44, s44, 1\n\t" \
  "s_and_b32 s58, s58, 4\n\t" \
  "s_and_b32 s76, s44, 64\n\t" \
  "s_or_b32 s42, s42, s76\n\t" \
  "s_cbranch_scc0 20b\n\t" \
  "s_branch 99f\n\t" \
  "14:\n\t" \
  "s_sleep 3\n\t" \
  "s_add_u32 s59, s59, 0x10000\n\t" \
  "s_cmp_lt_u32 s59, 0x400000\n\t" \
  "s_cbranch_scc1 13b\n\t" \
  "s_mov_b64 exec, -1\n\t" \
  "s_mov_b32 s42, 16\n\t" \
  "s_branch 99f\n\t" \
  "8:\n\t" \
  "s_bitcmp1_b32 s58, 1\n\t" \
  "s_cbranch_scc1 4b\n\t" \
  "s_bitcmp1_b32 s58, 2\n\t" \
  "s_cbranch_scc1 5b\n\t" \
  "v_max3_u32 v116, v32, v33, v34\n\t" \
  "v_max3_u32 v117, v35, v36, v37\n\t" \
  "v_max3_u32 v116, v116, v38, v39\n\t" \
  "v_max_u32 v116, v116, v117\n\t" \
  "v_cmp_eq_u32 vcc, -1, v116\n\t" \
  "s_cbranch_vccz 5b\n\t" \
  "s_branch 0f\n\t" \
  "9:\n\t" \
  "s_bitcmp1_b32 s58, 2\n\t" \
  "s_cbranch_scc1 6b\n\t" \
  "v_max3_u32 v116, v40, v41, v42\n\t" \
  "v_max3_u32 v117, v43, v44, v45\n\t" \
  "v_max3_u32 v116, v116, v46, v47\n\t" \
  "v_max_u32 v116, v116, v117\n\t" \
  "v_cmp_eq_u32 vcc, -1, v116\n\t" \
  "s_cbranch_vccz 6b\n\t" \
  "s_or_b32 s58, s58, 2\n\t" \
  "0:\n\t" \
  "s_or_b32 s58, s58, 1\n\t" \
  "11:\n\t" \
  "buffer_load_dwordx4 v[32:35], v100, %[rh], s40 offen sc1\n\t" \
  "buffer_load_dwordx4 v[36:39], v100, %[rh], s76 offen sc1\n\t" \
  "buffer_load_dwordx4 v[40:43], v100, %[rh], s77 offen sc1\n\t" \
  "buffer_load_dwordx4 v[44:47], v100, %[rh], s78 offen sc1\n\t" \
  "s_waitcnt lgkmcnt(0)\n\t" \
  "s_branch 10b\n\t" \
  "17:\n\t" \
  "s_add_u32 s59, s59, 1\n\t" \
  "s_bitcmp1_b32 s58, 2\n\t" \
  "s_cbranch_scc1 3b\n\t" \
  "s_waitcnt vmcnt(0)\n\t" \
  "v_max_u32 v116, v32, v33\n\t" \
  "v_max3_u32 v116, v116, v34, v35\n\t" \
  "v_max3_u32 v116, v116, v36, v37\n\t" \
  "v_max3_u32 v116, v116, v38, v39\n\t" \
  "v_max3_u32 v116, v116, v40, v41\n\t" \
  "v_max3_u32 v116, v116, v42, v43\n\t" \
  "v_max3_u32 v116, v116, v44, v45\n\t" \
  "v_max3_u32 v116, v116, v46, v47\n\t" \
  "v_cmp_eq_u32 vcc, -1, v116\n\t" \
  "s_cbranch_vccz 3b\n\t" \
  "s_bitcmp1_b32 s59, 6\n\t" \
  "s_cbranch_scc1 12f\n\t" \
  "buffer_load_dwordx4 v[32:35], v100, %[rh], s40 offen sc1\n\t" \
  "buffer_load_dwordx4 v[36:39], v100, %[rh], s76 offen sc1\n\t" \
  "buffer_load_dwordx4 v[40:43], v100, %[rh], s77 offen sc1\n\t" \
  "buffer_load_dwordx4 v[44:47], v100, %[rh], s78 offen sc1\n\t" \
  "s_branch 17b\n\t" \
  "12:\n\t" \
  "s_mov_b32 s42, s58\n\t" \
  "s_branch 99f\n\t" \
  "93:\n\t" \
  "s_mov_b32 s58, s42\n\t" \
  "20:\n\t" \
  "s_and_b32 s76, s44, 7\n\t" \
  "s_lshl_b32 s76, s76, 15\n\t" \
  "s_add_i32 s40, s54, s76\n\t" \
  "s_and_b32 s76, s44, 3\n\t" \
  "s_lshl_b32 s45, s76, 14\n\t" \
  "s_add_i32 s77, s44, 1\n\t" \
  "s_min_u32 s77, s77, 63\n\t" \
  "s_lshr_b32 s76, s77, 1\n\t" \
  "s_lshl_b32 s47, s77, 20\n\t" \
  "s_add_i32 s47, s47, s55\n\t" \
  "s_bitcmp1_b64 %[sel], s76\n\t" \
  "s_cselect_b32 s76, 0x2000, 0\n\t" \
  "s_add_i32 s97, s47, s76\n\t" \
  "s_cmp_le_i32 s77, s43\n\t" \
  "s_cselect_b32 s47, s97, 0x70000000\n\t" \
  "s_add_i32 s49, s44, -1\n\t" \
  "s_and_b32 s48, s49, 2\n\t" \
  "s_lshl_b32 s48, s48, 13\n\t" \
  "s_and_b32 s76, s49, 1\n\t" \
  "s_lshl_b32 s76, s76, 12\n\t" \
  "s_add_i32 s48, s48, s76\n\t" \
  "s_lshl_b32 s50, s53, 8\n\t" \
  "s_mov_b32 s59, 0\n\t" \
  "s_mov_b32 s92, s51\n\t" \
  "s_mov_b32 s93, s53\n\t" \
  "s_xor_b32 s76, s40, 0x400\n\t" \
  "s_xor_b32 s77, s40, 0x800\n\t" \
  "s_xor_b32 s78, s40, 0xc00\n\t" \
  "s_cmp_lg_u32 s58, 0\n\t" \
  "s_cbranch_scc1 11f\n\t" \
  "10:\n\t" \
  "ds_read_b128 v[48:51], v103 offset:32768\n\t" \
  "ds_read_b128 v[52:55], v103 offset:33792\n\t" \
  "ds_read_b128 v[56:59], v103 offset:34816\n\t" \
  "ds_read_b128 v[60:63], v103 offset:35840\n\t" \
  "ds_read_b128 v[16:19], v103 offset:49152\n\t" \
  "ds_read_b128 v[20:23], v103 offset:50176\n\t" \
  "ds_read_b128 v[24:27], v103 offset:51200\n\t" \
  "ds_read_b128 v[28:31], v103 offset:52224\n\t" \
  "ds_read_b128 v[84:87], v102 offset:16384\n\t" \
  "ds_read_b128 v[88:91], v102 offset:17408\n\t" \
  "ds_read_b128 v[92:95], v102 offset:18432\n\t" \
  "ds_read_b128 v[96:99], v102 offset:19456\n\t" \
  "s_waitcnt lgkmcnt(4)\n\t" \
  "ds_read_b128 v[0:3], v102 offset:49152\n\t" \
  "ds_read_b128 v[4:7], v102 offset:50176\n\t" \
  "ds_read_b128 v[8:11], v102 offset:51200\n\t" \
  "ds_read_b128 v[12:15], v102 offset:52224\n\t" \
  "46:\n\t" \
  "s_bitcmp1_b32 s58, 1\n\t" \
  "s_cselect_b64 s[80:81], -1, 0\n\t" \
  "s_cmp_lg_u32 s58, 0\n\t" \
  "s_cbranch_scc1 17f\n\t" \
  "s_mov_b64 exec, 0xff\n\t" \
  "16:\n\t" \
  "s_memtime vcc\n\t" \
  "s_waitcnt lgkmcnt(0)\n\t" \
  "s_sub_i32 s79, s41, vcc_lo\n\t" \
  "s_cmp_gt_i32 s79, 64\n\t" \
  "s_cbranch_scc1 16b\n\t" \
  "13:\n\t" \
  "buffer_load_dword v116, v112, %[rh], s48 offen sc1\n\t" \
  "s_memtime vcc\n\t" \
  "s_memrealtime s[82:83]\n\t" \
  "s_nop 4\n\t" \
  "s_memrealtime s[84:85]\n\t" \
  "s_nop 4\n\t" \
  "s_memrealtime s[56:57]\n\t" \
  "s_nop 4\n\t" \
  "s_memrealtime s[86:87]\n\t" \
  "s_nop 4\n\t" \
  "s_memrealtime s[88:89]\n\t" \
  "s_nop 4\n\t" \
  "s_memrealtime s[90:91]\n\t" \
  "s_mov_b32 s92, 0\n\t" \
  "s_waitcnt lgkmcnt(0)\n\t" \
  "s_cmp_eq_u32 s82, s84\n\t" \
  "s_addc_u32 s92, s92, 0\n\t" \
  "s_cmp_eq_u32 s82, s56\n\t" \
  "s_addc_u32 s92, s92, 0\n\t" \
  "s_cmp_eq_u32 s82, s86\n\t" \
  "s_addc_u32 s92, s92, 0\n\t" \
  "s_cmp_eq_u32 s82, s88\n\t" \
  "s_addc_u32 s92, s92, 0\n\t" \
  "s_cmp_eq_u32 s82, s90\n\t" \
  "s_addc_u32 s92, s92, 0\n\t" \
  "s_mul_i32 s92, s92, 24\n\t" \
  "s_add_i32 s92, s92, vcc_lo\n\t" \
  "s_lshl4_add_u32 s93, s82, 64\n\t" \
  "s_lshl_b32 s50, s93, 8\n\t" \
  "s_waitcnt vmcnt(0)\n\t" \
  "15:\n\t" \
  "v_mov_b32 v117, 20000\n\t" \
  "v_cmp_ne_u32_sdwa vcc, v116, s49 src0_sel:BYTE_0 src1_sel:DWORD\n\t" \
  "v_subrev_u32 v116, s50, v116\n\t" \
  "v_ashrrev_i32 v116, 8, v116\n\t" \
  "s_nop 1\n\t" \
  "v_max_i32_dpp v116, v116, v116 quad_perm:[1,0,3,2] row_mask:0xf bank_mask:0xf\n\t" \
  "s_cbranch_vccnz 14f\n\t" \
  "s_nop 0\n\t" \
  "v_max_i32_dpp v116, v116, v116 quad_perm:[2,3,0,1] row_mask:0xf bank_mask:0xf\n\t" \
  "s_nop 1\n\t" \
  "v_max_i32_dpp v116, v116, v116 row_half_mirror row_mask:0xf bank_mask:0xf\n\t" \
  "v_med3_i32 v116, v116, 0, v117\n\t" \
  "v_mul_u32_u24 v116, s52, v116\n\t" \
  "v_lshrrev_b32 v116, 16, v116\n\t" \
  "s_mov_b64 exec, -1\n\t" \
  "v_readfirstlane_b32 s84, v116\n\t" \
  "s_add_i32 s84, s84, s92\n\t" \
  ".p2align 6\n\t" \
  "s_nop 0\n\t" \
  "s_nop 0\n\t" \
  "s_nop 0\n\t" \
  "s_nop 0\n\t" \
  "s_nop 0\n\t" \
  "s_nop 0\n\t" \
  "s_nop 0\n\t" \
  "s_nop 0\n\t" \
  "1:\n\t" \
  "s_memtime vcc\n\t" \
  "s_waitcnt lgkmcnt(0)\n\t" \
  "s_sub_i32 s79, s84, vcc_lo\n\t" \
  "s_cmp_gt_i32 s79, 128\n\t" \
  "s_cbranch_scc1 1b\n\t" \
  ".p2align 4\n\t" \
  "2:\n\t" \
  "s_sub_i32 s79, s79, 33\n\t" \
  "s_cmp_gt_i32 s79, 56\n\t" \
  "s_cbranch_scc1 2b\n\t" \
  "buffer_load_dwordx4 v[32:35], v100, %[rh], s40 offen sc1\n\t" \
  "s_nop 12\n\t" \
  "buffer_load_dwordx4 v[36:39], v100, %[rh], s76 offen sc1\n\t" \
  "s_nop 12\n\t" \
  "buffer_load_dwordx4 v[40:43], v100, %[rh], s77 offen sc1\n\t" \
  "s_nop 12\n\t" \
  "buffer_load_dwordx4 v[44:47], v100, %[rh], s78 offen sc1\n\t" \
  "3:\n\t" \
  "v_mov_b64 v[68:69], 0\n\t" \
  "v_mov_b64 v[70:71], 0\n\t" \
  "v_mov_b64 v[72:73], 0\n\t" \
  "v_mov_b64 v[74:75], 0\n\t" \
  "v_mov_b64 v[76:77], 0\n\t" \
  "v_mov_b64 v[78:79], 0\n\t" \
  "v_mov_b64 v[80:81], 0\n\t" \
  "v_mov_b64 v[82:83], 0\n\t" \
  "s_waitcnt lgkmcnt(0)\n\t" \
  "s_waitcnt vmcnt(3)\n\t" \
  "v_smfmac_f32_16x16x64_bf16 v[68:71], v[32:35], v[152:159], v104\n\t" \
  "ds_write_b128 v101, v[32:35] offset:12288\n\t" \
  "v_smfmac_f32_16x16x64_bf16 v[72:75], v[32:35], v[160:167], v104\n\t" \
  "v_smfmac_f32_16x16x64_bf16 v[76:79], v[32:35], v[48:55], v104\n\t" \
  "v_smfmac_f32_16x16x64_bf16 v[80:83], v[32:35], v[56:63], v104\n\t" \
  "s_waitcnt vmcnt(2)\n\t" \
  "v_smfmac_f32_16x16x64_bf16 v[68:71], v[36:39], v[168:175], v104\n\t" \
  "ds_write_b128 v101, v[36:39] offset:13312\n\t" \
  "v_smfmac_f32_16x16x64_bf16 v[72:75], v[36:39], v[176:183], v104\n\t" \
  "v_smfmac_f32_16x16x64_bf16 v[76:79], v[36:39], v[184:191], v104\n\t" \
  "v_cmp_u_f32 vcc, v68, v69\n\t" \
  "v_smfmac_f32_16x16x64_bf16 v[80:83], v[36:39], v[192:199], v104\n\t" \
  "v_cmp_u_f32 s[82:83], v70, v71\n\t" \
  "s_waitcnt vmcnt(1)\n\t" \
  "v_smfmac_f32_16x16x64_bf16 v[68:71], v[40:43], v[16:23], v104\n\t" \
  "ds_write_b128 v101, v[40:43] offset:14336\n\t" \
  "v_smfmac_f32_16x16x64_bf16 v[72:75], v[40:43], v[24:31], v104\n\t" \
  "s_or_b64 vcc, vcc, s[82:83]\n\t" \
  "s_or_b64 vcc, vcc, s[80:81]\n\t" \
  "s_waitcnt lgkmcnt(1)\n\t" \
  "v_smfmac_f32_16x16x64_bf16 v[76:79], v[40:43], v[200:207], v104\n\t" \
  "s_cbranch_scc1 8f\n\t" \
  "5:\n\t" \
  "s_barrier\n\t" \
  "4:\n\t" \
  "s_memtime s[56:57]\n\t" \
  "ds_read_b128 v[48:51], v101 offset:0\n\t" \
  "v_smfmac_f32_16x16x64_bf16 v[80:83], v[40:43], v[208:215], v104\n\t" \
  "ds_read_b128 v[52:55], v101 offset:1024\n\t" \
  "ds_read_b128 v[56:59], v101 offset:4096\n\t" \
  "s_waitcnt vmcnt(0)\n\t" \
  "v_smfmac_f32_16x16x64_bf16 v[68:71], v[44:47], v[216:223], v104\n\t" \
  "v_smfmac_f32_16x16x64_bf16 v[72:75], v[44:47], v[224:231], v104\n\t" \
  "ds_write_b128 v101, v[44:47] offset:15360\n\t" \
  "v_smfmac_f32_16x16x64_bf16 v[76:79], v[44:47], v[232:239], v104\n\t" \
  "v_cmp_u_f32 vcc, v68, v69\n\t" \
  "v_smfmac_f32_16x16x64_bf16 v[80:83], v[44:47], v[240:247], v104\n\t" \
  "v_cmp_u_f32 s[82:83], v70, v71\n\t" \
  "s_waitcnt lgkmcnt(3)\n\t" \
  "v_smfmac_f32_16x16x64_bf16 v[68:71], v[48:51], a[0:7], v104\n\t" \
  "v_smfmac_f32_16x16x64_bf16 v[72:75], v[48:51], a[8:15], v104\n\t" \
  "s_or_b64 vcc, vcc, s[82:83]\n\t" \
  "s_waitcnt lgkmcnt(0)\n\t" \
  "v_smfmac_f32_16x16x64_bf16 v[68:71], v[52:55], a[16:23], v104\n\t" \
  "s_cbranch_scc1 9f\n\t" \
  "6:\n\t" \
  "s_barrier\n\t" \
  "ds_read_b128 v[60:63], v101 offset:5120\n\t" \
  "v_smfmac_f32_16x16x64_bf16 v[72:75], v[52:55], a[24:31], v104\n\t" \
  "ds_read_b128 v[64:67], v101 offset:8192\n\t" \
  "v_smfmac_f32_16x16x64_bf16 v[68:71], v[56:59], a[96:103], v104\n\t" \
  "v_smfmac_f32_16x16x64_bf16 v[72:75], v[56:59], a[104:111], v104\n\t" \
  "ds_read_b128 v[48:51], v101 offset:9216\n\t" \
  "s_waitcnt lgkmcnt(2)\n\t" \
  "v_smfmac_f32_16x16x64_bf16 v[68:71], v[60:63], a[112:119], v104\n\t" \
  "ds_read_b128 v[52:55], v101 offset:2048\n\t" \
  "v_smfmac_f32_16x16x64_bf16 v[72:75], v[60:63], a[120:127], v104\n\t" \
  "ds_read_b128 v[56:59], v101 offset:3072\n\t" \
  "s_waitcnt lgkmcnt(3)\n\t" \
  "v_smfmac_f32_16x16x64_bf16 v[68:71], v[64:67], a[192:199], v104\n\t" \
  "ds_read_b128 v[60:63], v101 offset:6144\n\t" \
  "v_smfmac_f32_16x16x64_bf16 v[72:75], v[64:67], a[200:207], v104\n\t" \
  "s_waitcnt lgkmcnt(3)\n\t" \
  "v_smfmac_f32_16x16x64_bf16 v[68:71], v[48:51], a[208:215], v104\n\t" \
  "ds_read_b128 v[64:67], v101 offset:7168\n\t" \
  "ds_read_b128 v[16:19], v103 offset:16384\n\t" \
  "v_smfmac_f32_16x16x64_bf16 v[72:75], v[48:51], a[216:223], v104\n\t" \
  "s_waitcnt lgkmcnt(4)\n\t" \
  "v_smfmac_f32_16x16x64_bf16 v[68:71], v[52:55], v[84:91], v104\n\t" \
  "ds_read_b128 v[48:51], v101 offset:11264\n\t" \
  "ds_read_b128 v[20:23], v103 offset:17408\n\t" \
  "v_smfmac_f32_16x16x64_bf16 v[72:75], v[52:55], v[92:99], v104\n\t" \
  "s_waitcnt lgkmcnt(5)\n\t" \
  "v_smfmac_f32_16x16x64_bf16 v[68:71], v[56:59], a[64:71], v104\n\t" \
  "ds_read_b128 v[52:55], v101 offset:10240\n\t" \
  "ds_read_b128 v[24:27], v103 offset:18432\n\t" \
  "v_smfmac_f32_16x16x64_bf16 v[72:75], v[56:59], a[72:79], v104\n\t" \
  "ds_read_b128 v[28:31], v103 offset:19456\n\t" \
  "s_waitcnt lgkmcnt(7)\n\t" \
  "v_smfmac_f32_16x16x64_bf16 v[68:71], v[60:63], v[0:7], v104\n\t" \
  "ds_read_b128 v[84:87], v103 offset:22528\n\t" \
  "v_smfmac_f32_16x16x64_bf16 v[72:75], v[60:63], v[8:15], v104\n\t" \
  "ds_read_b128 v[88:91], v103 offset:23552\n\t" \
  "s_waitcnt lgkmcnt(8)\n\t" \
  "v_smfmac_f32_16x16x64_bf16 v[68:71], v[64:67], a[160:167], v104\n\t" \
  "ds_read_b128 v[0:3], v102 offset:0\n\t" \
  "v_smfmac_f32_16x16x64_bf16 v[72:75], v[64:67], a[168:175], v104\n\t" \
  "ds_read_b128 v[4:7], v102 offset:1024\n\t" \
  "s_waitcnt lgkmcnt(8)\n\t" \
  "v_smfmac_f32_16x16x64_bf16 v[68:71], v[48:51], v[120:127], v104\n\t" \
  "ds_read_b128 v[92:95], v102 offset:22528\n\t" \
  "s_waitcnt lgkmcnt(7)\n\t" \
  "v_smfmac_f32_16x16x64_bf16 v[68:71], v[52:55], v[16:23], v104\n\t" \
  "ds_read_b128 v[8:11], v102 offset:2048\n\t" \
  "v_smfmac_f32_16x16x64_bf16 v[72:75], v[48:51], v[128:135], v104\n\t" \
  "ds_read_b128 v[12:15], v102 offset:3072\n\t" \
  "s_waitcnt lgkmcnt(7)\n\t" \
  "v_smfmac_f32_16x16x64_bf16 v[72:75], v[52:55], v[24:31], v104\n\t" \
  "v_add_f32 v44, v68, v70\n\t" \
  "ds_read_b128 v[96:99], v102 offset:23552\n\t" \
  "v_smfmac_f32_16x16x64_bf16 v[76:79], v[56:59], a[80:87], v104\n\t" \
  "v_add_f32 v45, v69, v71\n\t" \
  "v_cvt_pk_bf16_f32 v44, v44, v45\n\t" \
  "v_smfmac_f32_16x16x64_bf16 v[80:83], v[56:59], a[88:95], v104\n\t" \
  "v_add_f32 v46, v72, v74\n\t" \
  "v_add_f32 v47, v73, v75\n\t" \
  "v_smfmac_f32_16x16x64_bf16 v[76:79], v[60:63], a[144:151], v104\n\t" \
  "v_cvt_pk_bf16_f32 v46, v46, v47\n\t" \
  "ds_write2st64_b32 v105, v44, v46 offset1:1\n\t" \
  "v_smfmac_f32_16x16x64_bf16 v[80:83], v[60:63], a[152:159], v104\n\t" \
  "ds_read_b128 v[56:59], v101 offset:0\n\t" \
  "v_mov_b64 v[68:69], 0\n\t" \
  "v_smfmac_f32_16x16x64_bf16 v[76:79], v[64:67], a[176:183], v104\n\t" \
  "ds_read_b128 v[16:19], v102 offset:32768\n\t" \
  "v_mov_b64 v[70:71], 0\n\t" \
  "v_smfmac_f32_16x16x64_bf16 v[80:83], v[64:67], a[184:191], v104\n\t" \
  "ds_read_b128 v[20:23], v102 offset:33792\n\t" \
  "v_smfmac_f32_16x16x64_bf16 v[76:79], v[48:51], v[136:143], v104\n\t" \
  "s_waitcnt lgkmcnt(3)\n\t" \
  "s_barrier\n\t" \
  "v_smfmac_f32_16x16x64_bf16 v[80:83], v[48:51], v[144:151], v104\n\t" \
  "v_smfmac_f32_16x16x64_bf16 v[68:71], v[32:35], v[84:91], v104\n\t" \
  "v_smfmac_f32_16x16x64_bf16 v[76:79], v[52:55], a[240:247], v104\n\t" \
  "ds_read_b128 v[60:63], v101 offset:9216\n\t" \
  "ds_read_b128 v[24:27], v102 offset:34816\n\t" \
  "v_smfmac_f32_16x16x64_bf16 v[80:83], v[52:55], a[248:255], v104\n\t" \
  "ds_read_b128 v[64:67], v101 offset:2048\n\t" \
  "s_waitcnt lgkmcnt(5)\n\t" \
  "v_smfmac_f32_16x16x64_bf16 v[76:79], v[56:59], v[0:7], v104\n\t" \
  "ds_read_b128 v[48:51], v101 offset:4096\n\t" \
  "ds_read_b128 v[28:31], v102 offset:35840\n\t" \
  "v_smfmac_f32_16x16x64_bf16 v[80:83], v[56:59], v[8:15], v104\n\t" \
  "ds_read_b128 v[84:87], v102 offset:55296\n\t" \
  "global_load_dword v119, v109, %[kc] sc1\n\t" \
  "s_waitcnt lgkmcnt(5)\n\t" \
  "v_smfmac_f32_16x16x64_bf16 v[76:79], v[60:63], a[224:231], v104\n\t" \
  "ds_read_b128 v[52:55], v101 offset:1024\n\t" \
  "ds_read_b128 v[88:91], v102 offset:56320\n\t" \
  "v_smfmac_f32_16x16x64_bf16 v[80:83], v[60:63], a[232:239], v104\n\t" \
  "ds_read_b128 v[0:3], v103 offset:0\n\t" \
  "s_waitcnt lgkmcnt(6)\n\t" \
  "v_smfmac_f32_16x16x64_bf16 v[76:79], v[64:67], a[48:55], v104\n\t" \
  "ds_read_b128 v[56:59], v101 offset:5120\n\t" \
  "buffer_load_dwordx4 v[32:35], v100, %[ru], s47 offen offset:0 sc1\n\t" \
  "v_smfmac_f32_16x16x64_bf16 v[80:83], v[64:67], a[56:63], v104\n\t" \
  "ds_read_b128 v[60:63], v101 offset:8192\n\t" \
  "ds_read_b128 v[4:7], v103 offset:1024\n\t" \
  "s_waitcnt lgkmcnt(8)\n\t" \
  "v_smfmac_f32_16x16x64_bf16 v[76:79], v[48:51], v[16:23], v104\n\t" \
  "ds_read_b128 v[8:11], v103 offset:2048\n\t" \
  "s_waitcnt lgkmcnt(8)\n\t" \
  "v_smfmac_f32_16x16x64_bf16 v[80:83], v[48:51], v[24:31], v104\n\t" \
  "ds_read_b128 v[12:15], v103 offset:3072\n\t" \
  "buffer_load_dwordx4 v[36:39], v100, %[ru], s47 offen offset:1024 sc1\n\t" \
  "v_smfmac_f32_16x16x64_bf16 v[68:71], v[48:51], v[92:99], v104\n\t" \
  "s_waitcnt lgkmcnt(7)\n\t" \
  "v_smfmac_f32_16x16x64_bf16 v[76:79], v[52:55], a[32:39], v104\n\t" \
  "v_smfmac_f32_16x16x64_bf16 v[80:83], v[52:55], a[40:47], v104\n\t" \
  "s_waitcnt lgkmcnt(4)\n\t" \
  "v_smfmac_f32_16x16x64_bf16 v[76:79], v[56:59], a[128:135], v104\n\t" \
  "buffer_load_dwordx4 v[40:43], v100, %[ru], s47 offen offset:2048 sc1\n\t" \
  "s_waitcnt lgkmcnt(2)\n\t" \
  "v_smfmac_f32_16x16x64_bf16 v[76:79], v[60:63], v[0:7], v104\n\t" \
  "v_smfmac_f32_16x16x64_bf16 v[68:71], v[60:63], v[84:91], v104\n\t" \
  "v_smfmac_f32_16x16x64_bf16 v[80:83], v[56:59], a[136:143], v104\n\t" \
  "s_waitcnt lgkmcnt(0)\n\t" \
  "v_smfmac_f32_16x16x64_bf16 v[80:83], v[60:63], v[8:15], v104\n\t" \
  "v_add_f32 v20, v76, v78\n\t" \
  "v_add_f32 v21, v77, v79\n\t" \
  "v_cvt_pk_bf16_f32 v20, v20, v21\n\t" \
  "ds_write_b32 v106, v20\n\t" \
  "v_add_f32 v72, v68, v70\n\t" \
  "v_add_f32 v73, v69, v71\n\t" \
  "v_cvt_pk_bf16_f32 v72, v72, v73\n\t" \
  "ds_write_b32 v106, v72 offset:11008\n\t" \
  "v_add_f32 v23, v80, v82\n\t" \
  "v_add_f32 v21, v81, v83\n\t" \
  "v_cvt_pk_bf16_f32 v21, v23, v21\n\t" \
  "ds_write_b32 v106, v21 offset:256\n\t" \
  "s_waitcnt lgkmcnt(0)\n\t" \
  "s_barrier\n\t" \
  "s_waitcnt vmcnt(0)\n\t" \
  "s_barrier\n\t" \
  "ds_write_b128 v101, v[32:35] offset:25600\n\t" \
  "ds_write_b128 v101, v[36:39] offset:26624\n\t" \
  "ds_write_b128 v101, v[40:43] offset:27648\n\t" \
  "v_readlane_b32 s76, v119, 0\n\t" \
  "v_readlane_b32 s77, v119, 1\n\t" \
  "s_mov_b32 s42, 0\n\t" \
  "s_min_i32 s43, s76, s77\n\t" \
  "s_cmp_eq_u32 s47, s97\n\t" \
  "s_cbranch_scc1 48f\n\t" \
  "s_mov_b32 s42, 32\n\t" \
  "s_cmp_le_i32 s43, s44\n\t" \
  "s_cbranch_scc1 48f\n\t" \
  "s_mov_b32 s42, 0\n\t" \
  "s_waitcnt lgkmcnt(0)\n\t" \
  "buffer_load_dwordx4 v[32:35], v100, %[ru], s97 offen offset:0 sc1\n\t" \
  "buffer_load_dwordx4 v[36:39], v100, %[ru], s97 offen offset:1024 sc1\n\t" \
  "buffer_load_dwordx4 v[40:43], v100, %[ru], s97 offen offset:2048 sc1\n\t" \
  "s_waitcnt vmcnt(0)\n\t" \
  "ds_write_b128 v101, v[32:35] offset:25600\n\t" \
  "ds_write_b128 v101, v[36:39] offset:26624\n\t" \
  "ds_write_b128 v101, v[40:43] offset:27648\n\t" \
  "48:\n\t" \
  "s_add_i32 s41, s56, 1950\n\t" \
  "s_sub_i32 s76, s92, s51\n\t" \
  "s_sub_i32 s77, s93, s53\n\t" \
  "s_cmp_eq_u32 s76, 0\n\t" \
  "s_cbranch_scc1 21f\n\t" \
  "v_cvt_f32_u32 v117, s76\n\t" \
  "v_cvt_f32_u32 v116, s77\n\t" \
  "v_rcp_f32 v117, v117\n\t" \
  "s_mov_b32 s51, s92\n\t" \
  "v_mul_f32 v117, v117, v116\n\t" \
  "v_max_f32 v117, 0x3f028f5c, v117\n\t" \
  "v_min_f32 v117, 0x3ffeb852, v117\n\t" \
  "v_rcp_f32 v116, v117\n\t" \
  "v_mul_f32 v117, 0x4f000000, v117\n\t" \
  "v_mul_f32 v116, 0x47800000, v116\n\t" \
  "v_cvt_u32_f32 v117, v117\n\t" \
  "v_cvt_u32_f32 v116, v116\n\t" \
  "s_mov_b32 s53, s93\n\t" \
  "v_readfirstlane_b32 s46, v117\n\t" \
  "v_readfirstlane_b32 s52, v116\n\t" \
  "21:\n\t" \
  "v_mov_b32 v0, -1\n\t" \
  "v_mov_b32 v1, -1\n\t" \
  "v_mov_b32 v2, -1\n\t" \
  "v_mov_b32 v3, -1\n\t" \
  "s_and_b32 s76, s49, 3\n\t" \
  "s_mul_i32 s76, s76, 0xc000\n\t" \
  "s_add_i32 s76, s76, 0x40000\n\t" \
  "buffer_store_dwordx4 v[0:3], v110, %[rh], s76 offen sc1\n\t" \
  "s_add_i32 s76, s44, 1\n\t" \
  "s_and_b32 s76, s76, 3\n\t" \
  "s_lshl_b32 s76, s76, 18\n\t" \
  "s_add_i32 s77, s76, 0x10000\n\t" \
  "buffer_store_dwordx4 v[0:3], v107, %[rh], s76 offen sc1\n\t" \
  "buffer_store_dwordx4 v[0:3], v107, %[rh], s77 offen sc1\n\t" \
  "buffer_store_dwordx4 v[0:3], v111, %[rh], s76 offen sc1\n\t" \
  "s_add_i32 s44, s44, 1\n\t" \
  "s_and_b32 s58, s58, 4\n\t" \
  "s_and_b32 s76, s44, 64\n\t" \
  "s_or_b32 s42, s42, s76\n\t" \
  "s_cbranch_scc0 20b\n\t" \
  "s_branch 99f\n\t" \
  "14:\n\t" \
  "s_sleep 3\n\t" \
  "s_add_u32 s59, s59, 0x10000\n\t" \
  "s_cmp_lt_u32 s59, 0x400000\n\t" \
  "s_cbranch_scc1 13b\n\t" \
  "s_mov_b64 exec, -1\n\t" \
  "s_mov_b32 s42, 16\n\t" \
  "s_branch 99f\n\t" \
  "8:\n\t" \
  "s_bitcmp1_b32 s58, 1\n\t" \
  "s_cbranch_scc1 4b\n\t" \
  "s_bitcmp1_b32 s58, 2\n\t" \
  "s_cbranch_scc1 5b\n\t" \
  "v_max3_u32 v116, v32, v33, v34\n\t" \
  "v_max3_u32 v117, v35, v36, v37\n\t" \
  "v_max3_u32 v116, v116, v38, v39\n\t" \
  "v_max_u32 v116, v116, v117\n\t" \
  "v_cmp_eq_u32 vcc, -1, v116\n\t" \
  "s_cbranch_vccz 5b\n\t" \
  "s_branch 0f\n\t" \
  "9:\n\t" \
  "s_bitcmp1_b32 s58, 2\n\t" \
  "s_cbranch_scc1 6b\n\t" \
  "v_max3_u32 v116, v40, v41, v42\n\t" \
  "v_max3_u32 v117, v43, v44, v45\n\t" \
  "v_max3_u32 v116, v116, v46, v47\n\t" \
  "v_max_u32 v116, v116, v117\n\t" \
  "v_cmp_eq_u32 vcc, -1, v116\n\t" \
  "s_cbranch_vccz 6b\n\t" \
  "s_or_b32 s58, s58, 2\n\t" \
  "0:\n\t" \
  "s_or_b32 s58, s58, 1\n\t" \
  "11:\n\t" \
  "buffer_load_dwordx4 v[32:35], v100, %[rh], s40 offen sc1\n\t" \
  "buffer_load_dwordx4 v[36:39], v100, %[rh], s76 offen sc1\n\t" \
  "buffer_load_dwordx4 v[40:43], v100, %[rh], s77 offen sc1\n\t" \
  "buffer_load_dwordx4 v[44:47], v100, %[rh], s78 offen sc1\n\t" \
  "s_waitcnt lgkmcnt(0)\n\t" \
  "s_branch 10b\n\t" \
  "17:\n\t" \
  "s_add_u32 s59, s59, 1\n\t" \
  "s_bitcmp1_b32 s58, 2\n\t" \
  "s_cbranch_scc1 3b\n\t" \
  "s_waitcnt vmcnt(0)\n\t" \
  "v_max_u32 v116, v32, v33\n\t" \
  "v_max3_u32 v116, v116, v34, v35\n\t" \
  "v_max3_u32 v116, v116, v36, v37\n\t" \
  "v_max3_u32 v116, v116, v38, v39\n\t" \
  "v_max3_u32 v116, v116, v40, v41\n\t" \
  "v_max3_u32 v116, v116, v42, v43\n\t" \
  "v_max3_u32 v116, v116, v44, v45\n\t" \
  "v_max3_u32 v116, v116, v46, v47\n\t" \
  "v_cmp_eq_u32 vcc, -1, v116\n\t" \
  "s_cbranch_vccz 3b\n\t" \
  "s_bitcmp1_b32 s59, 6\n\t" \
  "s_cbranch_scc1 12f\n\t" \
  "buffer_load_dwordx4 v[32:35], v100, %[rh], s40 offen sc1\n\t" \
  "buffer_load_dwordx4 v[36:39], v100, %[rh], s76 offen sc1\n\t" \
  "buffer_load_dwordx4 v[40:43], v100, %[rh], s77 offen sc1\n\t" \
  "buffer_load_dwordx4 v[44:47], v100, %[rh], s78 offen sc1\n\t" \
  "s_branch 17b\n\t" \
  "12:\n\t" \
  "s_mov_b32 s42, s58\n\t" \
  "s_branch 99f\n\t" \
  "99:\n\t" \
  "s_waitcnt vmcnt(0) lgkmcnt(0)\n\t" \
  ""
#define VO_BODY \
  "s_cmp_eq_u32 %[wv], 1\n\t" \
  "s_cbranch_scc1 91f\n\t" \
  "s_cmp_eq_u32 %[wv], 2\n\t" \
  "s_cbranch_scc1 92f\n\t" \
  "s_cmp_eq_u32 %[wv], 3\n\t" \
  "s_cbranch_scc1 93f\n\t" \
  "90:\n\t" \
  "s_mov_b32 s58, s42\n\t" \
  "20:\n\t" \
  "s_and_b32 s76, s44, 7\n\t" \
  "s_lshl_b32 s76, s76, 15\n\t" \
  "s_add_i32 s40, s54, s76\n\t" \
  "s_and_b32 s76, s44, 3\n\t" \
  "s_lshl_b32 s45, s76, 14\n\t" \
  "s_lshl_b32 s47, s76, 20\n\t" \
  "s_add_i32 s47, s47, s55\n\t" \
  "s_lshr_b32 s76, s47, 21\n\t" \
  "s_bitcmp1_b64 %[sel], s76\n\t" \
  "s_cselect_b32 s76, 0x2000, 0\n\t" \
  "s_add_i32 s47, s47, s76\n\t" \
  "s_add_i32 s49, s44, -1\n\t" \
  "s_and_b32 s48, s49, 2\n\t" \
  "s_lshl_b32 s48, s48, 13\n\t" \
  "s_and_b32 s76, s49, 1\n\t" \
  "s_lshl_b32 s76, s76, 12\n\t" \
  "s_add_i32 s48, s48, s76\n\t" \
  "s_lshl_b32 s50, s53, 8\n\t" \
  "s_mov_b32 s59, 0\n\t" \
  "s_mov_b32 s92, s51\n\t" \
  "s_mov_b32 s93, s53\n\t" \
  "s_xor_b32 s76, s40, 0x400\n\t" \
  "s_xor_b32 s77, s40, 0x800\n\t" \
  "s_xor_b32 s78, s40, 0xc00\n\t" \
  "s_cmp_lg_u32 s58, 0\n\t" \
  "s_cbranch_scc1 11f\n\t" \
  "10:\n\t" \
  "ds_read_b128 v[48:51], v102 offset:0\n\t" \
  "ds_read_b128 v[52:55], v102 offset:1024\n\t" \
  "ds_read_b128 v[56:59], v102 offset:2048\n\t" \
  "ds_read_b128 v[60:63], v102 offset:3072\n\t" \
  "ds_read_b128 v[0:3], v102 offset:16384\n\t" \
  "ds_read_b128 v[4:7], v102 offset:17408\n\t" \
  "ds_read_b128 v[8:11], v102 offset:18432\n\t" \
  "ds_read_b128 v[12:15], v102 offset:19456\n\t" \
  "ds_read_b128 v[16:19], v102 offset:32768\n\t" \
  "ds_read_b128 v[20:23], v102 offset:33792\n\t" \
  "ds_read_b128 v[24:27], v102 offset:34816\n\t" \
  "ds_read_b128 v[28:31], v102 offset:35840\n\t" \
  "s_waitcnt lgkmcnt(4)\n\t" \
  "ds_read_b128 v[80:83], v102 offset:49152\n\t" \
  "ds_read_b128 v[84:87], v102 offset:50176\n\t" \
  "ds_read_b128 v[88:91], v102 offset:51200\n\t" \
  "ds_read_b128 v[92:95], v102 offset:52224\n\t" \
  "46:\n\t" \
  "s_bitcmp1_b32 s58, 1\n\t" \
  "s_cselect_b64 s[80:81], -1, 0\n\t" \
  "s_cmp_lg_u32 s58, 0\n\t" \
  "s_cbranch_scc1 17f\n\t" \
  "s_mov_b64 exec, 0xff\n\t" \
  "16:\n\t" \
  "s_memtime vcc\n\t" \
  "s_waitcnt lgkmcnt(0)\n\t" \
  "s_sub_i32 s79, s41, vcc_lo\n\t" \
  "s_cmp_gt_i32 s79, 64\n\t" \
  "s_cbranch_scc1 16b\n\t" \
  "13:\n\t" \
  "buffer_load_dword v116, v112, %[rh], s48 offen sc1\n\t" \
  "s_memtime vcc\n\t" \
  "s_memrealtime s[82:83]\n\t" \
  "s_nop 4\n\t" \
  "s_memrealtime s[84:85]\n\t" \
  "s_nop 4\n\t" \
  "s_memrealtime s[56:57]\n\t" \
  "s_nop 4\n\t" \
  "s_memrealtime s[86:87]\n\t" \
  "s_nop 4\n\t" \
  "s_memrealtime s[88:89]\n\t" \
  "s_nop 4\n\t" \
  "s_memrealtime s[90:91]\n\t" \
  "s_mov_b32 s92, 0\n\t" \
  "s_waitcnt lgkmcnt(0)\n\t" \
  "s_cmp_eq_u32 s82, s84\n\t" \
  "s_addc_u32 s92, s92, 0\n\t" \
  "s_cmp_eq_u32 s82, s56\n\t" \
  "s_addc_u32 s92, s92, 0\n\t" \
  "s_cmp_eq_u32 s82, s86\n\t" \
  "s_addc_u32 s92, s92, 0\n\t" \
  "s_cmp_eq_u32 s82, s88\n\t" \
  "s_addc_u32 s92, s92, 0\n\t" \
  "s_cmp_eq_u32 s82, s90\n\t" \
  "s_addc_u32 s92, s92, 0\n\t" \
  "s_mul_i32 s92, s92, 24\n\t" \
  "s_add_i32 s92, s92, vcc_lo\n\t" \
  "s_lshl4_add_u32 s93, s82, 64\n\t" \
  "s_lshl_b32 s50, s93, 8\n\t" \
  "s_waitcnt vmcnt(0)\n\t" \
  "15:\n\t" \
  "v_mov_b32 v117, 20000\n\t" \
  "v_cmp_ne_u32_sdwa vcc, v116, s49 src0_sel:BYTE_0 src1_sel:DWORD\n\t" \
  "v_subrev_u32 v116, s50, v116\n\t" \
  "v_ashrrev_i32 v116, 8, v116\n\t" \
  "s_nop 1\n\t" \
  "v_max_i32_dpp v116, v116, v116 quad_perm:[1,0,3,2] row_mask:0xf bank_mask:0xf\n\t" \
  "s_cbranch_vccnz 14f\n\t" \
  "s_nop 0\n\t" \
  "v_max_i32_dpp v116, v116, v116 quad_perm:[2,3,0,1] row_mask:0xf bank_mask:0xf\n\t" \
  "s_nop 1\n\t" \
  "v_max_i32_dpp v116, v116, v116 row_half_mirror row_mask:0xf bank_mask:0xf\n\t" \
  "v_med3_i32 v116, v116, 0, v117\n\t" \
  "v_mul_u32_u24 v116, s52, v116\n\t" \
  "v_lshrrev_b32 v116, 16, v116\n\t" \
  "s_mov_b64 exec, -1\n\t" \
  "v_readfirstlane_b32 s84, v116\n\t" \
  "s_add_i32 s84, s84, s92\n\t" \
  ".p2align 6\n\t" \
  "s_nop 0\n\t" \
  "s_nop 0\n\t" \
  "s_nop 0\n\t" \
  "s_nop 0\n\t" \
  "s_nop 0\n\t" \
  "s_nop 0\n\t" \
  "s_nop 0\n\t" \
  "s_nop 0\n\t" \
  "1:\n\t" \
  "s_memtime vcc\n\t" \
  "s_waitcnt lgkmcnt(0)\n\t" \
  "s_sub_i32 s79, s84, vcc_lo\n\t" \
  "s_cmp_gt_i32 s79, 128\n\t" \
  "s_cbranch_scc1 1b\n\t" \
  ".p2align 4\n\t" \
  "2:\n\t" \
  "s_sub_i32 s79, s79, 33\n\t" \
  "s_cmp_gt_i32 s79, 56\n\t" \
  "s_cbranch_scc1 2b\n\t" \
  "buffer_load_dwordx4 v[32:35], v100, %[rh], s40 offen sc1\n\t" \
  "s_nop 12\n\t" \
  "buffer_load_dwordx4 v[36:39], v100, %[rh], s76 offen sc1\n\t" \
  "s_nop 12\n\t" \
  "buffer_load_dwordx4 v[40:43], v100, %[rh], s77 offen sc1\n\t" \
  "s_nop 12\n\t" \
  "buffer_load_dwordx4 v[44:47], v100, %[rh], s78 offen sc1\n\t" \
  "3:\n\t" \
  "v_mov_b64 v[72:73], 0\n\t" \
  "v_mov_b64 v[74:75], 0\n\t" \
  "v_mov_b64 v[76:77], 0\n\t" \
  "v_mov_b64 v[78:79], 0\n\t" \
  "s_waitcnt lgkmcnt(0)\n\t" \
  "s_waitcnt vmcnt(3)\n\t" \
  "v_smfmac_f32_16x16x64_bf16 v[72:75], v[32:35], v[48:55], v104\n\t" \
  "ds_write_b128 v101, v[32:35] offset:0\n\t" \
  "v_smfmac_f32_16x16x64_bf16 v[76:79], v[32:35], v[56:63], v104\n\t" \
  "s_waitcnt vmcnt(2)\n\t" \
  "ds_write_b128 v101, v[36:39] offset:1024\n\t" \
  "v_smfmac_f32_16x16x64_bf16 v[72:75], v[36:39], a[0:7], v104\n\t" \
  "v_smfmac_f32_16x16x64_bf16 v[76:79], v[36:39], a[8:15], v104\n\t" \
  "s_nop 3\n\t" \
  "v_cmp_u_f32 vcc, v72, v73\n\t" \
  "v_cmp_u_f32 s[82:83], v74, v75\n\t" \
  "s_waitcnt vmcnt(1)\n\t" \
  "v_smfmac_f32_16x16x64_bf16 v[72:75], v[40:43], a[16:23], v104\n\t" \
  "s_or_b64 vcc, vcc, s[82:83]\n\t" \
  "s_or_b64 vcc, vcc, s[80:81]\n\t" \
  "v_smfmac_f32_16x16x64_bf16 v[76:79], v[40:43], a[24:31], v104\n\t" \
  "s_waitcnt lgkmcnt(0)\n\t" \
  "s_cbranch_scc1 8f\n\t" \
  "5:\n\t" \
  "s_barrier\n\t" \
  "4:\n\t" \
  "s_memtime s[56:57]\n\t" \
  "ds_read_b128 v[48:51], v101 offset:4096\n\t" \
  "ds_read_b128 v[52:55], v101 offset:5120\n\t" \
  "ds_read_b128 v[56:59], v101 offset:9216\n\t" \
  "s_waitcnt vmcnt(0)\n\t" \
  "v_smfmac_f32_16x16x64_bf16 v[72:75], v[44:47], a[32:39], v104\n\t" \
  "ds_read_b128 v[60:63], v101 offset:8192\n\t" \
  "ds_read_b128 v[64:67], v101 offset:13312\n\t" \
  "v_smfmac_f32_16x16x64_bf16 v[76:79], v[44:47], a[40:47], v104\n\t" \
  "ds_read_b128 v[68:71], v101 offset:12288\n\t" \
  "ds_write_b128 v101, v[40:43] offset:2048\n\t" \
  "s_nop 1\n\t" \
  "v_cmp_u_f32 vcc, v72, v73\n\t" \
  "v_cmp_u_f32 s[82:83], v74, v75\n\t" \
  "s_waitcnt lgkmcnt(6)\n\t" \
  "v_smfmac_f32_16x16x64_bf16 v[72:75], v[48:51], v[0:7], v104\n\t" \
  "s_or_b64 vcc, vcc, s[82:83]\n\t" \
  "v_smfmac_f32_16x16x64_bf16 v[76:79], v[48:51], v[8:15], v104\n\t" \
  "s_cbranch_scc1 9f\n\t" \
  "6:\n\t" \
  "s_waitcnt lgkmcnt(5)\n\t" \
  "v_smfmac_f32_16x16x64_bf16 v[72:75], v[52:55], a[48:55], v104\n\t" \
  "ds_write_b128 v101, v[44:47] offset:3072\n\t" \
  "v_smfmac_f32_16x16x64_bf16 v[76:79], v[52:55], a[56:63], v104\n\t" \
  "v_mov_b64 v[40:41], 0\n\t" \
  "s_waitcnt lgkmcnt(5)\n\t" \
  "v_smfmac_f32_16x16x64_bf16 v[72:75], v[56:59], a[96:103], v104\n\t" \
  "v_mov_b64 v[42:43], 0\n\t" \
  "v_smfmac_f32_16x16x64_bf16 v[76:79], v[56:59], a[104:111], v104\n\t" \
  "s_waitcnt lgkmcnt(4)\n\t" \
  "v_smfmac_f32_16x16x64_bf16 v[72:75], v[60:63], v[16:23], v104\n\t" \
  "s_waitcnt lgkmcnt(0)\n\t" \
  "s_barrier\n\t" \
  "v_smfmac_f32_16x16x64_bf16 v[76:79], v[60:63], v[24:31], v104\n\t" \
  "ds_read_b128 v[48:51], v101 offset:6144\n\t" \
  "ds_read_b128 v[52:55], v101 offset:10240\n\t" \
  "v_smfmac_f32_16x16x64_bf16 v[72:75], v[64:67], a[144:151], v104\n\t" \
  "ds_read_b128 v[56:59], v101 offset:14336\n\t" \
  "ds_read_b128 v[60:63], v101 offset:7168\n\t" \
  "v_smfmac_f32_16x16x64_bf16 v[76:79], v[64:67], a[152:159], v104\n\t" \
  "ds_read_b128 v[32:35], v101 offset:11264\n\t" \
  "ds_read_b128 v[36:39], v101 offset:15360\n\t" \
  "v_smfmac_f32_16x16x64_bf16 v[72:75], v[68:71], v[80:87], v104\n\t" \
  "ds_read_b128 v[0:3], v103 offset:0\n\t" \
  "v_mov_b64 v[44:45], 0\n\t" \
  "v_smfmac_f32_16x16x64_bf16 v[76:79], v[68:71], v[88:95], v104\n\t" \
  "v_mov_b64 v[46:47], 0\n\t" \
  "s_waitcnt lgkmcnt(5)\n\t" \
  "v_smfmac_f32_16x16x64_bf16 v[72:75], v[48:51], a[64:71], v104\n\t" \
  "ds_read_b128 v[4:7], v103 offset:1024\n\t" \
  "v_mov_b64 v[80:81], 0\n\t" \
  "v_smfmac_f32_16x16x64_bf16 v[72:75], v[52:55], a[112:119], v104\n\t" \
  "v_mov_b64 v[82:83], 0\n\t" \
  "s_waitcnt lgkmcnt(2)\n\t" \
  "v_smfmac_f32_16x16x64_bf16 v[72:75], v[56:59], a[160:167], v104\n\t" \
  "ds_read_b128 v[8:11], v103 offset:2048\n\t" \
  "v_mov_b64 v[84:85], 0\n\t" \
  "v_smfmac_f32_16x16x64_bf16 v[72:75], v[60:63], a[80:87], v104\n\t" \
  "ds_read_b128 v[12:15], v103 offset:3072\n\t" \
  "v_mov_b64 v[86:87], 0\n\t" \
  "v_smfmac_f32_16x16x64_bf16 v[72:75], v[32:35], a[128:135], v104\n\t" \
  "ds_read_b128 v[16:19], v103 offset:16384\n\t" \
  "v_mov_b64 v[88:89], 0\n\t" \
  "v_smfmac_f32_16x16x64_bf16 v[72:75], v[36:39], a[176:183], v104\n\t" \
  "ds_read_b128 v[20:23], v103 offset:17408\n\t" \
  "v_mov_b64 v[90:91], 0\n\t" \
  "v_smfmac_f32_16x16x64_bf16 v[76:79], v[48:51], a[72:79], v104\n\t" \
  "ds_read_b128 v[24:27], v103 offset:18432\n\t" \
  "v_mov_b64 v[92:93], 0\n\t" \
  "v_smfmac_f32_16x16x64_bf16 v[76:79], v[52:55], a[120:127], v104\n\t" \
  "v_add_f32 v96, v72, v74\n\t" \
  "v_add_f32 v97, v73, v75\n\t" \
  "v_cvt_pk_bf16_f32 v96, v96, v97\n\t" \
  "ds_write_b32 v105, v96\n\t" \
  "v_smfmac_f32_16x16x64_bf16 v[76:79], v[56:59], a[168:175], v104\n\t" \
  "ds_read_b128 v[28:31], v103 offset:19456\n\t" \
  "v_mov_b64 v[94:95], 0\n\t" \
  "v_smfmac_f32_16x16x64_bf16 v[76:79], v[60:63], a[88:95], v104\n\t" \
  "v_smfmac_f32_16x16x64_bf16 v[76:79], v[32:35], a[136:143], v104\n\t" \
  "v_smfmac_f32_16x16x64_bf16 v[76:79], v[36:39], a[184:191], v104\n\t" \
  "s_waitcnt lgkmcnt(1)\n\t" \
  "s_barrier\n\t" \
  "ds_read_b64_tr_b16 v[96:97], v107 offset:0\n\t" \
  "ds_read_b64_tr_b16 v[98:99], v107 offset:64\n\t" \
  "ds_read_b64_tr_b16 v[56:57], v107 offset:1024\n\t" \
  "ds_read_b64_tr_b16 v[58:59], v107 offset:1088\n\t" \
  "s_nop 1\n\t" \
  "v_add_f32 v116, v76, v78\n\t" \
  "v_add_f32 v117, v77, v79\n\t" \
  "v_cvt_pk_bf16_f32 v116, v116, v117\n\t" \
  "ds_write_b32 v105, v116 offset:256\n\t" \
  "s_waitcnt lgkmcnt(3)\n\t" \
  "v_smfmac_f32_16x16x64_bf16 v[40:43], v[96:99], a[192:199], v106\n\t" \
  "v_smfmac_f32_16x16x64_bf16 v[44:47], v[96:99], a[224:231], v106\n\t" \
  "s_waitcnt lgkmcnt(0)\n\t" \
  "s_barrier\n\t" \
  "ds_read_b64_tr_b16 v[52:53], v107 offset:256\n\t" \
  "ds_read_b64_tr_b16 v[54:55], v107 offset:320\n\t" \
  "v_smfmac_f32_16x16x64_bf16 v[40:43], v[56:59], a[208:215], v106\n\t" \
  "ds_read_b64_tr_b16 v[60:61], v107 offset:1280\n\t" \
  "ds_read_b64_tr_b16 v[62:63], v107 offset:1344\n\t" \
  "v_smfmac_f32_16x16x64_bf16 v[44:47], v[56:59], v[0:7], v106\n\t" \
  "v_smfmac_f32_16x16x64_bf16 v[80:83], v[96:99], a[240:247], v106\n\t" \
  "v_mov_b64 v[32:33], 0\n\t" \
  "v_smfmac_f32_16x16x64_bf16 v[84:87], v[96:99], v[136:143], v106\n\t" \
  "v_mov_b64 v[34:35], 0\n\t" \
  "v_smfmac_f32_16x16x64_bf16 v[80:83], v[56:59], v[120:127], v106\n\t" \
  "v_mov_b64 v[36:37], 0\n\t" \
  "v_smfmac_f32_16x16x64_bf16 v[84:87], v[56:59], v[16:23], v106\n\t" \
  "s_waitcnt lgkmcnt(2)\n\t" \
  "v_smfmac_f32_16x16x64_bf16 v[40:43], v[52:55], a[200:207], v106\n\t" \
  "v_smfmac_f32_16x16x64_bf16 v[44:47], v[52:55], a[232:239], v106\n\t" \
  "s_xor_b32 s76, s47, 0x40000\n\t" \
  "s_waitcnt lgkmcnt(0)\n\t" \
  "v_smfmac_f32_16x16x64_bf16 v[40:43], v[60:63], a[216:223], v106\n\t" \
  "v_mov_b64 v[38:39], 0\n\t" \
  "s_xor_b32 s77, s47, 0x80000\n\t" \
  "v_smfmac_f32_16x16x64_bf16 v[44:47], v[60:63], v[8:15], v106\n\t" \
  "s_xor_b32 s78, s47, 0xc0000\n\t" \
  "v_smfmac_f32_16x16x64_bf16 v[80:83], v[52:55], a[248:255], v106\n\t" \
  "ds_read_b128 v[0:3], v103 offset:32768\n\t" \
  "v_cvt_pk_bf16_f32 v64, v40, v41\n\t" \
  "v_smfmac_f32_16x16x64_bf16 v[84:87], v[52:55], v[144:151], v106\n\t" \
  "ds_read_b128 v[4:7], v103 offset:33792\n\t" \
  "v_cvt_pk_bf16_f32 v65, v42, v43\n\t" \
  "v_smfmac_f32_16x16x64_bf16 v[80:83], v[60:63], v[128:135], v106\n\t" \
  "ds_read_b128 v[8:11], v103 offset:34816\n\t" \
  "v_cvt_pk_bf16_f32 v66, v44, v45\n\t" \
  "v_smfmac_f32_16x16x64_bf16 v[84:87], v[60:63], v[24:31], v106\n\t" \
  "ds_read_b128 v[12:15], v103 offset:35840\n\t" \
  "v_cvt_pk_bf16_f32 v67, v46, v47\n\t" \
  "v_smfmac_f32_16x16x64_bf16 v[88:91], v[96:99], v[152:159], v106\n\t" \
  "v_permlane16_swap_b32 v64, v66\n\t" \
  "v_cvt_pk_bf16_f32 v68, v80, v81\n\t" \
  "v_smfmac_f32_16x16x64_bf16 v[92:95], v[96:99], v[184:191], v106\n\t" \
  "v_permlane16_swap_b32 v65, v67\n\t" \
  "buffer_store_dwordx4 v[64:67], v108, %[ru], s47 offen sc1\n\t" \
  "v_cvt_pk_bf16_f32 v69, v82, v83\n\t" \
  "v_smfmac_f32_16x16x64_bf16 v[88:91], v[52:55], v[160:167], v106\n\t" \
  "v_cvt_pk_bf16_f32 v70, v84, v85\n\t" \
  "v_smfmac_f32_16x16x64_bf16 v[92:95], v[52:55], v[192:199], v106\n\t" \
  "v_cvt_pk_bf16_f32 v71, v86, v87\n\t" \
  "v_smfmac_f32_16x16x64_bf16 v[88:91], v[56:59], v[168:175], v106\n\t" \
  "ds_read_b128 v[16:19], v103 offset:49152\n\t" \
  "v_permlane16_swap_b32 v68, v70\n\t" \
  "s_waitcnt lgkmcnt(3)\n\t" \
  "v_smfmac_f32_16x16x64_bf16 v[92:95], v[56:59], v[0:7], v106\n\t" \
  "ds_read_b128 v[20:23], v103 offset:50176\n\t" \
  "v_permlane16_swap_b32 v69, v71\n\t" \
  "buffer_store_dwordx4 v[68:71], v108, %[ru], s76 offen sc1\n\t" \
  "v_smfmac_f32_16x16x64_bf16 v[88:91], v[60:63], v[176:183], v106\n\t" \
  "ds_read_b128 v[24:27], v103 offset:51200\n\t" \
  "s_waitcnt lgkmcnt(3)\n\t" \
  "v_smfmac_f32_16x16x64_bf16 v[92:95], v[60:63], v[8:15], v106\n\t" \
  "ds_read_b128 v[28:31], v103 offset:52224\n\t" \
  "v_smfmac_f32_16x16x64_bf16 v[32:35], v[96:99], v[200:207], v106\n\t" \
  "v_cvt_pk_bf16_f32 v72, v88, v89\n\t" \
  "v_smfmac_f32_16x16x64_bf16 v[36:39], v[96:99], v[232:239], v106\n\t" \
  "v_cvt_pk_bf16_f32 v73, v90, v91\n\t" \
  "v_smfmac_f32_16x16x64_bf16 v[32:35], v[52:55], v[208:215], v106\n\t" \
  "v_cvt_pk_bf16_f32 v74, v92, v93\n\t" \
  "v_smfmac_f32_16x16x64_bf16 v[36:39], v[52:55], v[240:247], v106\n\t" \
  "v_cvt_pk_bf16_f32 v75, v94, v95\n\t" \
  "v_smfmac_f32_16x16x64_bf16 v[32:35], v[56:59], v[216:223], v106\n\t" \
  "v_permlane16_swap_b32 v72, v74\n\t" \
  "v_smfmac_f32_16x16x64_bf16 v[32:35], v[60:63], v[224:231], v106\n\t" \
  "v_permlane16_swap_b32 v73, v75\n\t" \
  "buffer_store_dwordx4 v[72:75], v108, %[ru], s77 offen sc1\n\t" \
  "s_waitcnt lgkmcnt(2)\n\t" \
  "v_smfmac_f32_16x16x64_bf16 v[36:39], v[56:59], v[16:23], v106\n\t" \
  "s_waitcnt lgkmcnt(0)\n\t" \
  "v_smfmac_f32_16x16x64_bf16 v[36:39], v[60:63], v[24:31], v106\n\t" \
  "v_cvt_pk_bf16_f32 v76, v32, v33\n\t" \
  "v_cvt_pk_bf16_f32 v77, v34, v35\n\t" \
  "s_nop 5\n\t" \
  "v_cvt_pk_bf16_f32 v78, v36, v37\n\t" \
  "v_cvt_pk_bf16_f32 v79, v38, v39\n\t" \
  "s_nop 0\n\t" \
  "v_permlane16_swap_b32 v76, v78\n\t" \
  "v_permlane16_swap_b32 v77, v79\n\t" \
  "buffer_store_dwordx4 v[76:79], v108, %[ru], s78 offen sc1\n\t" \
  "s_memtime s[76:77]\n\t" \
  "s_mov_b32 s42, 0\n\t" \
  "s_waitcnt lgkmcnt(0)\n\t" \
  "s_mov_b32 s57, s76\n\t" \
  "s_add_i32 s41, s56, 1950\n\t" \
  "s_sub_i32 s76, s92, s51\n\t" \
  "s_sub_i32 s77, s93, s53\n\t" \
  "s_cmp_eq_u32 s76, 0\n\t" \
  "s_cbranch_scc1 21f\n\t" \
  "v_cvt_f32_u32 v117, s76\n\t" \
  "v_cvt_f32_u32 v116, s77\n\t" \
  "v_rcp_f32 v117, v117\n\t" \
  "s_mov_b32 s51, s92\n\t" \
  "v_mul_f32 v117, v117, v116\n\t" \
  "v_max_f32 v117, 0x3f028f5c, v117\n\t" \
  "v_min_f32 v117, 0x3ffeb852, v117\n\t" \
  "v_rcp_f32 v116, v117\n\t" \
  "v_mul_f32 v117, 0x4f000000, v117\n\t" \
  "v_mul_f32 v116, 0x47800000, v116\n\t" \
  "v_cvt_u32_f32 v117, v117\n\t" \
  "v_cvt_u32_f32 v116, v116\n\t" \
  "s_mov_b32 s53, s93\n\t" \
  "v_readfirstlane_b32 s46, v117\n\t" \
  "v_readfirstlane_b32 s52, v116\n\t" \
  "21:\n\t" \
  "s_add_i32 s44, s44, 1\n\t" \
  "s_and_b32 s58, s58, 4\n\t" \
  "s_and_b32 s76, s44, 64\n\t" \
  "s_or_b32 s42, s42, s76\n\t" \
  "s_cbranch_scc0 20b\n\t" \
  "s_branch 99f\n\t" \
  "14:\n\t" \
  "s_sleep 3\n\t" \
  "s_add_u32 s59, s59, 0x10000\n\t" \
  "s_cmp_lt_u32 s59, 0x400000\n\t" \
  "s_cbranch_scc1 13b\n\t" \
  "s_mov_b64 exec, -1\n\t" \
  "s_mov_b32 s42, 16\n\t" \
  "s_branch 99f\n\t" \
  "8:\n\t" \
  "s_bitcmp1_b32 s58, 1\n\t" \
  "s_cbranch_scc1 4b\n\t" \
  "s_bitcmp1_b32 s58, 2\n\t" \
  "s_cbranch_scc1 5b\n\t" \
  "v_max3_u32 v116, v32, v33, v34\n\t" \
  "v_max3_u32 v117, v35, v36, v37\n\t" \
  "v_max3_u32 v116, v116, v38, v39\n\t" \
  "v_max_u32 v116, v116, v117\n\t" \
  "v_cmp_eq_u32 vcc, -1, v116\n\t" \
  "s_cbranch_vccz 5b\n\t" \
  "s_branch 0f\n\t" \
  "9:\n\t" \
  "s_bitcmp1_b32 s58, 2\n\t" \
  "s_cbranch_scc1 6b\n\t" \
  "v_max3_u32 v116, v40, v41, v42\n\t" \
  "v_max3_u32 v117, v43, v44, v45\n\t" \
  "v_max3_u32 v116, v116, v46, v47\n\t" \
  "v_max_u32 v116, v116, v117\n\t" \
  "v_cmp_eq_u32 vcc, -1, v116\n\t" \
  "s_cbranch_vccz 6b\n\t" \
  "s_or_b32 s58, s58, 2\n\t" \
  "0:\n\t" \
  "s_or_b32 s58, s58, 1\n\t" \
  "11:\n\t" \
  "buffer_load_dwordx4 v[32:35], v100, %[rh], s40 offen sc1\n\t" \
  "buffer_load_dwordx4 v[36:39], v100, %[rh], s76 offen sc1\n\t" \
  "buffer_load_dwordx4 v[40:43], v100, %[rh], s77 offen sc1\n\t" \
  "buffer_load_dwordx4 v[44:47], v100, %[rh], s78 offen sc1\n\t" \
  "s_waitcnt lgkmcnt(0)\n\t" \
  "s_branch 10b\n\t" \
  "17:\n\t" \
  "s_add_u32 s59, s59, 1\n\t" \
  "s_bitcmp1_b32 s58, 2\n\t" \
  "s_cbranch_scc1 3b\n\t" \
  "s_waitcnt vmcnt(0)\n\t" \
  "v_max_u32 v116, v32, v33\n\t" \
  "v_max3_u32 v116, v116, v34, v35\n\t" \
  "v_max3_u32 v116, v116, v36, v37\n\t" \
  "v_max3_u32 v116, v116, v38, v39\n\t" \
  "v_max3_u32 v116, v116, v40, v41\n\t" \
  "v_max3_u32 v116, v116, v42, v43\n\t" \
  "v_max3_u32 v116, v116, v44, v45\n\t" \
  "v_max3_u32 v116, v116, v46, v47\n\t" \
  "v_cmp_eq_u32 vcc, -1, v116\n\t" \
  "s_cbranch_vccz 3b\n\t" \
  "s_bitcmp1_b32 s59, 6\n\t" \
  "s_cbranch_scc1 12f\n\t" \
  "buffer_load_dwordx4 v[32:35], v100, %[rh], s40 offen sc1\n\t" \
  "buffer_load_dwordx4 v[36:39], v100, %[rh], s76 offen sc1\n\t" \
  "buffer_load_dwordx4 v[40:43], v100, %[rh], s77 offen sc1\n\t" \
  "buffer_load_dwordx4 v[44:47], v100, %[rh], s78 offen sc1\n\t" \
  "s_branch 17b\n\t" \
  "12:\n\t" \
  "s_mov_b32 s42, s58\n\t" \
  "s_branch 99f\n\t" \
  "91:\n\t" \
  "s_mov_b32 s58, s42\n\t" \
  "20:\n\t" \
  "s_and_b32 s76, s44, 7\n\t" \
  "s_lshl_b32 s76, s76, 15\n\t" \
  "s_add_i32 s40, s54, s76\n\t" \
  "s_and_b32 s76, s44, 3\n\t" \
  "s_lshl_b32 s45, s76, 14\n\t" \
  "s_lshl_b32 s47, s76, 20\n\t" \
  "s_add_i32 s47, s47, s55\n\t" \
  "s_lshr_b32 s76, s47, 21\n\t" \
  "s_bitcmp1_b64 %[sel], s76\n\t" \
  "s_cselect_b32 s76, 0x2000, 0\n\t" \
  "s_add_i32 s47, s47, s76\n\t" \
  "s_add_i32 s49, s44, -1\n\t" \
  "s_and_b32 s48, s49, 2\n\t" \
  "s_lshl_b32 s48, s48, 13\n\t" \
  "s_and_b32 s76, s49, 1\n\t" \
  "s_lshl_b32 s76, s76, 12\n\t" \
  "s_add_i32 s48, s48, s76\n\t" \
  "s_lshl_b32 s50, s53, 8\n\t" \
  "s_mov_b32 s59, 0\n\t" \
  "s_mov_b32 s92, s51\n\t" \
  "s_mov_b32 s93, s53\n\t" \
  "s_xor_b32 s76, s40, 0x400\n\t" \
  "s_xor_b32 s77, s40, 0x800\n\t" \
  "s_xor_b32 s78, s40, 0xc00\n\t" \
  "s_cmp_lg_u32 s58, 0\n\t" \
  "s_cbranch_scc1 11f\n\t" \
  "10:\n\t" \
  "ds_read_b128 v[48:51], v102 offset:16384\n\t" \
  "ds_read_b128 v[52:55], v102 offset:17408\n\t" \
  "ds_read_b128 v[56:59], v102 offset:18432\n\t" \
  "ds_read_b128 v[60:63], v102 offset:19456\n\t" \
  "ds_read_b128 v[0:3], v102 offset:32768\n\t" \
  "ds_read_b128 v[4:7], v102 offset:33792\n\t" \
  "ds_read_b128 v[8:11], v102 offset:34816\n\t" \
  "ds_read_b128 v[12:15], v102 offset:35840\n\t" \
  "ds_read_b128 v[16:19], v102 offset:49152\n\t" \
  "ds_read_b128 v[20:23], v102 offset:50176\n\t" \
  "ds_read_b128 v[24:27], v102 offset:51200\n\t" \
  "ds_read_b128 v[28:31], v102 offset:52224\n\t" \
  "s_waitcnt lgkmcnt(4)\n\t" \
  "ds_read_b128 v[80:83], v102 offset:0\n\t" \
  "ds_read_b128 v[84:87], v102 offset:1024\n\t" \
  "ds_read_b128 v[88:91], v102 offset:2048\n\t" \
  "ds_read_b128 v[92:95], v102 offset:3072\n\t" \
  "46:\n\t" \
  "s_bitcmp1_b32 s58, 1\n\t" \
  "s_cselect_b64 s[80:81], -1, 0\n\t" \
  "s_cmp_lg_u32 s58, 0\n\t" \
  "s_cbranch_scc1 17f\n\t" \
  "s_mov_b64 exec, 0xff\n\t" \
  "16:\n\t" \
  "s_memtime vcc\n\t" \
  "s_waitcnt lgkmcnt(0)\n\t" \
  "s_sub_i32 s79, s41, vcc_lo\n\t" \
  "s_cmp_gt_i32 s79, 64\n\t" \
  "s_cbranch_scc1 16b\n\t" \
  "13:\n\t" \
  "buffer_load_dword v116, v112, %[rh], s48 offen sc1\n\t" \
  "s_memtime vcc\n\t" \
  "s_memrealtime s[82:83]\n\t" \
  "s_nop 4\n\t" \
  "s_memrealtime s[84:85]\n\t" \
  "s_nop 4\n\t" \
  "s_memrealtime s[56:57]\n\t" \
  "s_nop 4\n\t" \
  "s_memrealtime s[86:87]\n\t" \
  "s_nop 4\n\t" \
  "s_memrealtime s[88:89]\n\t" \
  "s_nop 4\n\t" \
  "s_memrealtime s[90:91]\n\t" \
  "s_mov_b32 s92, 0\n\t" \
  "s_waitcnt lgkmcnt(0)\n\t" \
  "s_cmp_eq_u32 s82, s84\n\t" \
  "s_addc_u32 s92, s92, 0\n\t" \
  "s_cmp_eq_u32 s82, s56\n\t" \
  "s_addc_u32 s92, s92, 0\n\t" \
  "s_cmp_eq_u32 s82, s86\n\t" \
  "s_addc_u32 s92, s92, 0\n\t" \
  "s_cmp_eq_u32 s82, s88\n\t" \
  "s_addc_u32 s92, s92, 0\n\t" \
  "s_cmp_eq_u32 s82, s90\n\t" \
  "s_addc_u32 s92, s92, 0\n\t" \
  "s_mul_i32 s92, s92, 24\n\t" \
  "s_add_i32 s92, s92, vcc_lo\n\t" \
  "s_lshl4_add_u32 s93, s82, 64\n\t" \
  "s_lshl_b32 s50, s93, 8\n\t" \
  "s_waitcnt vmcnt(0)\n\t" \
  "15:\n\t" \
  "v_mov_b32 v117, 20000\n\t" \
  "v_cmp_ne_u32_sdwa vcc, v116, s49 src0_sel:BYTE_0 src1_sel:DWORD\n\t" \
  "v_subrev_u32 v116, s50, v116\n\t" \
  "v_ashrrev_i32 v116, 8, v116\n\t" \
  "s_nop 1\n\t" \
  "v_max_i32_dpp v116, v116, v116 quad_perm:[1,0,3,2] row_mask:0xf bank_mask:0xf\n\t" \
  "s_cbranch_vccnz 14f\n\t" \
  "s_nop 0\n\t" \
  "v_max_i32_dpp v116, v116, v116 quad_perm:[2,3,0,1] row_mask:0xf bank_mask:0xf\n\t" \
  "s_nop 1\n\t" \
  "v_max_i32_dpp v116, v116, v116 row_half_mirror row_mask:0xf bank_mask:0xf\n\t" \
  "v_med3_i32 v116, v116, 0, v117\n\t" \
  "v_mul_u32_u24 v116, s52, v116\n\t" \
  "v_lshrrev_b32 v116, 16, v116\n\t" \
  "s_mov_b64 exec, -1\n\t" \
  "v_readfirstlane_b32 s84, v116\n\t" \
  "s_add_i32 s84, s84, s92\n\t" \
  ".p2align 6\n\t" \
  "s_nop 0\n\t" \
  "s_nop 0\n\t" \
  "s_nop 0\n\t" \
  "s_nop 0\n\t" \
  "s_nop 0\n\t" \
  "s_nop 0\n\t" \
  "s_nop 0\n\t" \
  "s_nop 0\n\t" \
  "1:\n\t" \
  "s_memtime vcc\n\t" \
  "s_waitcnt lgkmcnt(0)\n\t" \
  "s_sub_i32 s79, s84, vcc_lo\n\t" \
  "s_cmp_gt_i32 s79, 128\n\t" \
  "s_cbranch_scc1 1b\n\t" \
  ".p2align 4\n\t" \
  "2:\n\t" \
  "s_sub_i32 s79, s79, 33\n\t" \
  "s_cmp_gt_i32 s79, 56\n\t" \
  "s_cbranch_scc1 2b\n\t" \
  "buffer_load_dwordx4 v[32:35], v100, %[rh], s40 offen sc1\n\t" \
  "s_nop 12\n\t" \
  "buffer_load_dwordx4 v[36:39], v100, %[rh], s76 offen sc1\n\t" \
  "s_nop 12\n\t" \
  "buffer_load_dwordx4 v[40:43], v100, %[rh], s77 offen sc1\n\t" \
  "s_nop 12\n\t" \
  "buffer_load_dwordx4 v[44:47], v100, %[rh], s78 offen sc1\n\t" \
  "3:\n\t" \
  "v_mov_b64 v[72:73], 0\n\t" \
  "v_mov_b64 v[74:75], 0\n\t" \
  "v_mov_b64 v[76:77], 0\n\t" \
  "v_mov_b64 v[78:79], 0\n\t" \
  "s_waitcnt lgkmcnt(0)\n\t" \
  "s_waitcnt vmcnt(3)\n\t" \
  "v_smfmac_f32_16x16x64_bf16 v[72:75], v[32:35], v[48:55], v104\n\t" \
  "ds_write_b128 v101, v[32:35] offset:4096\n\t" \
  "v_smfmac_f32_16x16x64_bf16 v[76:79], v[32:35], v[56:63], v104\n\t" \
  "s_waitcnt vmcnt(2)\n\t" \
  "ds_write_b128 v101, v[36:39] offset:5120\n\t" \
  "v_smfmac_f32_16x16x64_bf16 v[72:75], v[36:39], a[48:55], v104\n\t" \
  "v_smfmac_f32_16x16x64_bf16 v[76:79], v[36:39], a[56:63], v104\n\t" \
  "s_nop 3\n\t" \
  "v_cmp_u_f32 vcc, v72, v73\n\t" \
  "v_cmp_u_f32 s[82:83], v74, v75\n\t" \
  "s_waitcnt vmcnt(1)\n\t" \
  "v_smfmac_f32_16x16x64_bf16 v[72:75], v[40:43], a[64:71], v104\n\t" \
  "s_or_b64 vcc, vcc, s[82:83]\n\t" \
  "s_or_b64 vcc, vcc, s[80:81]\n\t" \
  "v_smfmac_f32_16x16x64_bf16 v[76:79], v[40:43], a[72:79], v104\n\t" \
  "s_waitcnt lgkmcnt(0)\n\t" \
  "s_cbranch_scc1 8f\n\t" \
  "5:\n\t" \
  "s_barrier\n\t" \
  "4:\n\t" \
  "s_memtime s[56:57]\n\t" \
  "ds_read_b128 v[48:51], v101 offset:8192\n\t" \
  "ds_read_b128 v[52:55], v101 offset:9216\n\t" \
  "ds_read_b128 v[56:59], v101 offset:13312\n\t" \
  "s_waitcnt vmcnt(0)\n\t" \
  "v_smfmac_f32_16x16x64_bf16 v[72:75], v[44:47], a[80:87], v104\n\t" \
  "ds_read_b128 v[60:63], v101 offset:12288\n\t" \
  "ds_read_b128 v[64:67], v101 offset:1024\n\t" \
  "v_smfmac_f32_16x16x64_bf16 v[76:79], v[44:47], a[88:95], v104\n\t" \
  "ds_read_b128 v[68:71], v101 offset:0\n\t" \
  "ds_write_b128 v101, v[40:43] offset:6144\n\t" \
  "s_nop 1\n\t" \
  "v_cmp_u_f32 vcc, v72, v73\n\t" \
  "v_cmp_u_f32 s[82:83], v74, v75\n\t" \
  "s_waitcnt lgkmcnt(6)\n\t" \
  "v_smfmac_f32_16x16x64_bf16 v[72:75], v[48:51], v[0:7], v104\n\t" \
  "s_or_b64 vcc, vcc, s[82:83]\n\t" \
  "v_smfmac_f32_16x16x64_bf16 v[76:79], v[48:51], v[8:15], v104\n\t" \
  "s_cbranch_scc1 9f\n\t" \
  "6:\n\t" \
  "s_waitcnt lgkmcnt(5)\n\t" \
  "v_smfmac_f32_16x16x64_bf16 v[72:75], v[52:55], a[96:103], v104\n\t" \
  "ds_write_b128 v101, v[44:47] offset:7168\n\t" \
  "v_smfmac_f32_16x16x64_bf16 v[76:79], v[52:55], a[104:111], v104\n\t" \
  "v_mov_b64 v[40:41], 0\n\t" \
  "s_waitcnt lgkmcnt(5)\n\t" \
  "v_smfmac_f32_16x16x64_bf16 v[72:75], v[56:59], a[144:151], v104\n\t" \
  "v_mov_b64 v[42:43], 0\n\t" \
  "v_smfmac_f32_16x16x64_bf16 v[76:79], v[56:59], a[152:159], v104\n\t" \
  "s_waitcnt lgkmcnt(4)\n\t" \
  "v_smfmac_f32_16x16x64_bf16 v[72:75], v[60:63], v[16:23], v104\n\t" \
  "s_waitcnt lgkmcnt(0)\n\t" \
  "s_barrier\n\t" \
  "v_smfmac_f32_16x16x64_bf16 v[76:79], v[60:63], v[24:31], v104\n\t" \
  "ds_read_b128 v[48:51], v101 offset:10240\n\t" \
  "ds_read_b128 v[52:55], v101 offset:14336\n\t" \
  "v_smfmac_f32_16x16x64_bf16 v[72:75], v[64:67], a[0:7], v104\n\t" \
  "ds_read_b128 v[56:59], v101 offset:2048\n\t" \
  "ds_read_b128 v[60:63], v101 offset:11264\n\t" \
  "v_smfmac_f32_16x16x64_bf16 v[76:79], v[64:67], a[8:15], v104\n\t" \
  "ds_read_b128 v[32:35], v101 offset:15360\n\t" \
  "ds_read_b128 v[36:39], v101 offset:3072\n\t" \
  "v_smfmac_f32_16x16x64_bf16 v[72:75], v[68:71], v[80:87], v104\n\t" \
  "ds_read_b128 v[0:3], v103 offset:0\n\t" \
  "v_mov_b64 v[44:45], 0\n\t" \
  "v_smfmac_f32_16x16x64_bf16 v[76:79], v[68:71], v[88:95], v104\n\t" \
  "v_mov_b64 v[46:47], 0\n\t" \
  "s_waitcnt lgkmcnt(5)\n\t" \
  "v_smfmac_f32_16x16x64_bf16 v[72:75], v[48:51], a[112:119], v104\n\t" \
  "ds_read_b128 v[4:7], v103 offset:1024\n\t" \
  "v_mov_b64 v[80:81], 0\n\t" \
  "v_smfmac_f32_16x16x64_bf16 v[72:75], v[52:55], a[160:167], v104\n\t" \
  "v_mov_b64 v[82:83], 0\n\t" \
  "s_waitcnt lgkmcnt(2)\n\t" \
  "v_smfmac_f32_16x16x64_bf16 v[72:75], v[56:59], a[16:23], v104\n\t" \
  "ds_read_b128 v[8:11], v103 offset:2048\n\t" \
  "v_mov_b64 v[84:85], 0\n\t" \
  "v_smfmac_f32_16x16x64_bf16 v[72:75], v[60:63], a[128:135], v104\n\t" \
  "ds_read_b128 v[12:15], v103 offset:3072\n\t" \
  "v_mov_b64 v[86:87], 0\n\t" \
  "v_smfmac_f32_16x16x64_bf16 v[72:75], v[32:35], a[176:183], v104\n\t" \
  "ds_read_b128 v[16:19], v103 offset:16384\n\t" \
  "v_mov_b64 v[88:89], 0\n\t" \
  "v_smfmac_f32_16x16x64_bf16 v[72:75], v[36:39], a[32:39], v104\n\t" \
  "ds_read_b128 v[20:23], v103 offset:17408\n\t" \
  "v_mov_b64 v[90:91], 0\n\t" \
  "v_smfmac_f32_16x16x64_bf16 v[76:79], v[48:51], a[120:127], v104\n\t" \
  "ds_read_b128 v[24:27], v103 offset:18432\n\t" \
  "v_mov_b64 v[92:93], 0\n\t" \
  "v_smfmac_f32_16x16x64_bf16 v[76:79], v[52:55], a[168:175], v104\n\t" \
  "v_add_f32 v96, v72, v74\n\t" \
  "v_add_f32 v97, v73, v75\n\t" \
  "v_cvt_pk_bf16_f32 v96, v96, v97\n\t" \
  "ds_write_b32 v105, v96\n\t" \
  "v_smfmac_f32_16x16x64_bf16 v[76:79], v[56:59], a[24:31], v104\n\t" \
  "ds_read_b128 v[28:31], v103 offset:19456\n\t" \
  "v_mov_b64 v[94:95], 0\n\t" \
  "v_smfmac_f32_16x16x64_bf16 v[76:79], v[60:63], a[136:143], v104\n\t" \
  "v_smfmac_f32_16x16x64_bf16 v[76:79], v[32:35], a[184:191], v104\n\t" \
  "v_smfmac_f32_16x16x64_bf16 v[76:79], v[36:39], a[40:47], v104\n\t" \
  "s_waitcnt lgkmcnt(1)\n\t" \
  "s_barrier\n\t" \
  "ds_read_b64_tr_b16 v[96:97], v107 offset:0\n\t" \
  "ds_read_b64_tr_b16 v[98:99], v107 offset:64\n\t" \
  "ds_read_b64_tr_b16 v[56:57], v107 offset:1024\n\t" \
  "ds_read_b64_tr_b16 v[58:59], v107 offset:1088\n\t" \
  "s_nop 1\n\t" \
  "v_add_f32 v116, v76, v78\n\t" \
  "v_add_f32 v117, v77, v79\n\t" \
  "v_cvt_pk_bf16_f32 v116, v116, v117\n\t" \
  "ds_write_b32 v105, v116 offset:256\n\t" \
  "s_waitcnt lgkmcnt(3)\n\t" \
  "v_smfmac_f32_16x16x64_bf16 v[40:43], v[96:99], a[192:199], v106\n\t" \
  "v_smfmac_f32_16x16x64_bf16 v[44:47], v[96:99], a[224:231], v106\n\t" \
  "s_waitcnt lgkmcnt(0)\n\t" \
  "s_barrier\n\t" \
  "ds_read_b64_tr_b16 v[52:53], v107 offset:256\n\t" \
  "ds_read_b64_tr_b16 v[54:55], v107 offset:320\n\t" \
  "v_smfmac_f32_16x16x64_bf16 v[40:43], v[56:59], a[208:215], v106\n\t" \
  "ds_read_b64_tr_b16 v[60:61], v107 offset:1280\n\t" \
  "ds_read_b64_tr_b16 v[62:63], v107 offset:1344\n\t" \
  "v_smfmac_f32_16x16x64_bf16 v[44:47], v[56:59], v[0:7], v106\n\t" \
  "v_smfmac_f32_16x16x64_bf16 v[80:83], v[96:99], a[240:247], v106\n\t" \
  "v_mov_b64 v[32:33], 0\n\t" \
  "v_smfmac_f32_16x16x64_bf16 v[84:87], v[96:99], v[136:143], v106\n\t" \
  "v_mov_b64 v[34:35], 0\n\t" \
  "v_smfmac_f32_16x16x64_bf16 v[80:83], v[56:59], v[120:127], v106\n\t" \
  "v_mov_b64 v[36:37], 0\n\t" \
  "v_smfmac_f32_16x16x64_bf16 v[84:87], v[56:59], v[16:23], v106\n\t" \
  "s_waitcnt lgkmcnt(2)\n\t" \
  "v_smfmac_f32_16x16x64_bf16 v[40:43], v[52:55], a[200:207], v106\n\t" \
  "v_smfmac_f32_16x16x64_bf16 v[44:47], v[52:55], a[232:239], v106\n\t" \
  "s_xor_b32 s76, s47, 0x40000\n\t" \
  "s_waitcnt lgkmcnt(0)\n\t" \
  "v_smfmac_f32_16x16x64_bf16 v[40:43], v[60:63], a[216:223], v106\n\t" \
  "v_mov_b64 v[38:39], 0\n\t" \
  "s_xor_b32 s77, s47, 0x80000\n\t" \
  "v_smfmac_f32_16x16x64_bf16 v[44:47], v[60:63], v[8:15], v106\n\t" \
  "s_xor_b32 s78, s47, 0xc0000\n\t" \
  "v_smfmac_f32_16x16x64_bf16 v[80:83], v[52:55], a[248:255], v106\n\t" \
  "ds_read_b128 v[0:3], v103 offset:32768\n\t" \
  "v_cvt_pk_bf16_f32 v64, v40, v41\n\t" \
  "v_smfmac_f32_16x16x64_bf16 v[84:87], v[52:55], v[144:151], v106\n\t" \
  "ds_read_b128 v[4:7], v103 offset:33792\n\t" \
  "v_cvt_pk_bf16_f32 v65, v42, v43\n\t" \
  "v_smfmac_f32_16x16x64_bf16 v[80:83], v[60:63], v[128:135], v106\n\t" \
  "ds_read_b128 v[8:11], v103 offset:34816\n\t" \
  "v_cvt_pk_bf16_f32 v66, v44, v45\n\t" \
  "v_smfmac_f32_16x16x64_bf16 v[84:87], v[60:63], v[24:31], v106\n\t" \
  "ds_read_b128 v[12:15], v103 offset:35840\n\t" \
  "v_cvt_pk_bf16_f32 v67, v46, v47\n\t" \
  "v_smfmac_f32_16x16x64_bf16 v[88:91], v[96:99], v[152:159], v106\n\t" \
  "v_permlane16_swap_b32 v64, v66\n\t" \
  "v_cvt_pk_bf16_f32 v68, v80, v81\n\t" \
  "v_smfmac_f32_16x16x64_bf16 v[92:95], v[96:99], v[184:191], v106\n\t" \
  "v_permlane16_swap_b32 v65, v67\n\t" \
  "v_cvt_pk_bf16_f32 v69, v82, v83\n\t" \
  "v_smfmac_f32_16x16x64_bf16 v[88:91], v[52:55], v[160:167], v106\n\t" \
  "buffer_store_dwordx4 v[64:67], v108, %[ru], s47 offen sc1\n\t" \
  "v_cvt_pk_bf16_f32 v70, v84, v85\n\t" \
  "v_smfmac_f32_16x16x64_bf16 v[92:95], v[52:55], v[192:199], v106\n\t" \
  "v_cvt_pk_bf16_f32 v71, v86, v87\n\t" \
  "v_smfmac_f32_16x16x64_bf16 v[88:91], v[56:59], v[168:175], v106\n\t" \
  "ds_read_b128 v[16:19], v103 offset:49152\n\t" \
  "v_permlane16_swap_b32 v68, v70\n\t" \
  "s_waitcnt lgkmcnt(3)\n\t" \
  "v_smfmac_f32_16x16x64_bf16 v[92:95], v[56:59], v[0:7], v106\n\t" \
  "ds_read_b128 v[20:23], v103 offset:50176\n\t" \
  "v_permlane16_swap_b32 v69, v71\n\t" \
  "v_smfmac_f32_16x16x64_bf16 v[88:91], v[60:63], v[176:183], v106\n\t" \
  "ds_read_b128 v[24:27], v103 offset:51200\n\t" \
  "buffer_store_dwordx4 v[68:71], v108, %[ru], s76 offen sc1\n\t" \
  "s_waitcnt lgkmcnt(3)\n\t" \
  "v_smfmac_f32_16x16x64_bf16 v[92:95], v[60:63], v[8:15], v106\n\t" \
  "ds_read_b128 v[28:31], v103 offset:52224\n\t" \
  "v_smfmac_f32_16x16x64_bf16 v[32:35], v[96:99], v[200:207], v106\n\t" \
  "v_cvt_pk_bf16_f32 v72, v88, v89\n\t" \
  "v_smfmac_f32_16x16x64_bf16 v[36:39], v[96:99], v[232:239], v106\n\t" \
  "v_cvt_pk_bf16_f32 v73, v90, v91\n\t" \
  "v_smfmac_f32_16x16x64_bf16 v[32:35], v[52:55], v[208:215], v106\n\t" \
  "v_cvt_pk_bf16_f32 v74, v92, v93\n\t" \
  "v_smfmac_f32_16x16x64_bf16 v[36:39], v[52:55], v[240:247], v106\n\t" \
  "v_cvt_pk_bf16_f32 v75, v94, v95\n\t" \
  "v_smfmac_f32_16x16x64_bf16 v[32:35], v[56:59], v[216:223], v106\n\t" \
  "v_permlane16_swap_b32 v72, v74\n\t" \
  "v_smfmac_f32_16x16x64_bf16 v[32:35], v[60:63], v[224:231], v106\n\t" \
  "v_permlane16_swap_b32 v73, v75\n\t" \
  "s_waitcnt lgkmcnt(2)\n\t" \
  "v_smfmac_f32_16x16x64_bf16 v[36:39], v[56:59], v[16:23], v106\n\t" \
  "buffer_store_dwordx4 v[72:75], v108, %[ru], s77 offen sc1\n\t" \
  "s_waitcnt lgkmcnt(0)\n\t" \
  "v_smfmac_f32_16x16x64_bf16 v[36:39], v[60:63], v[24:31], v106\n\t" \
  "v_cvt_pk_bf16_f32 v76, v32, v33\n\t" \
  "v_cvt_pk_bf16_f32 v77, v34, v35\n\t" \
  "s_nop 5\n\t" \
  "v_cvt_pk_bf16_f32 v78, v36, v37\n\t" \
  "v_cvt_pk_bf16_f32 v79, v38, v39\n\t" \
  "s_nop 0\n\t" \
  "v_permlane16_swap_b32 v76, v78\n\t" \
  "v_permlane16_swap_b32 v77, v79\n\t" \
  "buffer_store_dwordx4 v[76:79], v108, %[ru], s78 offen sc1\n\t" \
  "s_memtime s[76:77]\n\t" \
  "s_mov_b32 s42, 0\n\t" \
  "s_waitcnt lgkmcnt(0)\n\t" \
  "s_mov_b32 s57, s76\n\t" \
  "s_add_i32 s41, s56, 1950\n\t" \
  "s_sub_i32 s76, s92, s51\n\t" \
  "s_sub_i32 s77, s93, s53\n\t" \
  "s_cmp_eq_u32 s76, 0\n\t" \
  "s_cbranch_scc1 21f\n\t" \
  "v_cvt_f32_u32 v117, s76\n\t" \
  "v_cvt_f32_u32 v116, s77\n\t" \
  "v_rcp_f32 v117, v117\n\t" \
  "s_mov_b32 s51, s92\n\t" \
  "v_mul_f32 v117, v117, v116\n\t" \
  "v_max_f32 v117, 0x3f028f5c, v117\n\t" \
  "v_min_f32 v117, 0x3ffeb852, v117\n\t" \
  "v_rcp_f32 v116, v117\n\t" \
  "v_mul_f32 v117, 0x4f000000, v117\n\t" \
  "v_mul_f32 v116, 0x47800000, v116\n\t" \
  "v_cvt_u32_f32 v117, v117\n\t" \
  "v_cvt_u32_f32 v116, v116\n\t" \
  "s_mov_b32 s53, s93\n\t" \
  "v_readfirstlane_b32 s46, v117\n\t" \
  "v_readfirstlane_b32 s52, v116\n\t" \
  "21:\n\t" \
  "s_add_i32 s44, s44, 1\n\t" \
  "s_and_b32 s58, s58, 4\n\t" \
  "s_and_b32 s76, s44, 64\n\t" \
  "s_or_b32 s42, s42, s76\n\t" \
  "s_cbranch_scc0 20b\n\t" \
  "s_branch 99f\n\t" \
  "14:\n\t" \
  "s_sleep 3\n\t" \
  "s_add_u32 s59, s59, 0x10000\n\t" \
  "s_cmp_lt_u32 s59, 0x400000\n\t" \
  "s_cbranch_scc1 13b\n\t" \
  "s_mov_b64 exec, -1\n\t" \
  "s_mov_b32 s42, 16\n\t" \
  "s_branch 99f\n\t" \
  "8:\n\t" \
  "s_bitcmp1_b32 s58, 1\n\t" \
  "s_cbranch_scc1 4b\n\t" \
  "s_bitcmp1_b32 s58, 2\n\t" \
  "s_cbranch_scc1 5b\n\t" \
  "v_max3_u32 v116, v32, v33, v34\n\t" \
  "v_max3_u32 v117, v35, v36, v37\n\t" \
  "v_max3_u32 v116, v116, v38, v39\n\t" \
  "v_max_u32 v116, v116, v117\n\t" \
  "v_cmp_eq_u32 vcc, -1, v116\n\t" \
  "s_cbranch_vccz 5b\n\t" \
  "s_branch 0f\n\t" \
  "9:\n\t" \
  "s_bitcmp1_b32 s58, 2\n\t" \
  "s_cbranch_scc1 6b\n\t" \
  "v_max3_u32 v116, v40, v41, v42\n\t" \
  "v_max3_u32 v117, v43, v44, v45\n\t" \
  "v_max3_u32 v116, v116, v46, v47\n\t" \
  "v_max_u32 v116, v116, v117\n\t" \
  "v_cmp_eq_u32 vcc, -1, v116\n\t" \
  "s_cbranch_vccz 6b\n\t" \
  "s_or_b32 s58, s58, 2\n\t" \
  "0:\n\t" \
  "s_or_b32 s58, s58, 1\n\t" \
  "11:\n\t" \
  "buffer_load_dwordx4 v[32:35], v100, %[rh], s40 offen sc1\n\t" \
  "buffer_load_dwordx4 v[36:39], v100, %[rh], s76 offen sc1\n\t" \
  "buffer_load_dwordx4 v[40:43], v100, %[rh], s77 offen sc1\n\t" \
  "buffer_load_dwordx4 v[44:47], v100, %[rh], s78 offen sc1\n\t" \
  "s_waitcnt lgkmcnt(0)\n\t" \
  "s_branch 10b\n\t" \
  "17:\n\t" \
  "s_add_u32 s59, s59, 1\n\t" \
  "s_bitcmp1_b32 s58, 2\n\t" \
  "s_cbranch_scc1 3b\n\t" \
  "s_waitcnt vmcnt(0)\n\t" \
  "v_max_u32 v116, v32, v33\n\t" \
  "v_max3_u32 v116, v116, v34, v35\n\t" \
  "v_max3_u32 v116, v116, v36, v37\n\t" \
  "v_max3_u32 v116, v116, v38, v39\n\t" \
  "v_max3_u32 v116, v116, v40, v41\n\t" \
  "v_max3_u32 v116, v116, v42, v43\n\t" \
  "v_max3_u32 v116, v116, v44, v45\n\t" \
  "v_max3_u32 v116, v116, v46, v47\n\t" \
  "v_cmp_eq_u32 vcc, -1, v116\n\t" \
  "s_cbranch_vccz 3b\n\t" \
  "s_bitcmp1_b32 s59, 6\n\t" \
  "s_cbranch_scc1 12f\n\t" \
  "buffer_load_dwordx4 v[32:35], v100, %[rh], s40 offen sc1\n\t" \
  "buffer_load_dwordx4 v[36:39], v100, %[rh], s76 offen sc1\n\t" \
  "buffer_load_dwordx4 v[40:43], v100, %[rh], s77 offen sc1\n\t" \
  "buffer_load_dwordx4 v[44:47], v100, %[rh], s78 offen sc1\n\t" \
  "s_branch 17b\n\t" \
  "12:\n\t" \
  "s_mov_b32 s42, s58\n\t" \
  "s_branch 99f\n\t" \
  "92:\n\t" \
  "s_mov_b32 s58, s42\n\t" \
  "20:\n\t" \
  "s_and_b32 s76, s44, 7\n\t" \
  "s_lshl_b32 s76, s76, 15\n\t" \
  "s_add_i32 s40, s54, s76\n\t" \
  "s_and_b32 s76, s44, 3\n\t" \
  "s_lshl_b32 s45, s76, 14\n\t" \
  "s_lshl_b32 s47, s76, 20\n\t" \
  "s_add_i32 s47, s47, s55\n\t" \
  "s_lshr_b32 s76, s47, 21\n\t" \
  "s_bitcmp1_b64 %[sel], s76\n\t" \
  "s_cselect_b32 s76, 0x2000, 0\n\t" \
  "s_add_i32 s47, s47, s76\n\t" \
  "s_add_i32 s49, s44, -1\n\t" \
  "s_and_b32 s48, s49, 2\n\t" \
  "s_lshl_b32 s48, s48, 13\n\t" \
  "s_and_b32 s76, s49, 1\n\t" \
  "s_lshl_b32 s76, s76, 12\n\t" \
  "s_add_i32 s48, s48, s76\n\t" \
  "s_lshl_b32 s50, s53, 8\n\t" \
  "s_mov_b32 s59, 0\n\t" \
  "s_mov_b32 s92, s51\n\t" \
  "s_mov_b32 s93, s53\n\t" \
  "s_xor_b32 s76, s40, 0x400\n\t" \
  "s_xor_b32 s77, s40, 0x800\n\t" \
  "s_xor_b32 s78, s40, 0xc00\n\t" \
  "s_cmp_lg_u32 s58, 0\n\t" \
  "s_cbranch_scc1 11f\n\t" \
  "10:\n\t" \
  "ds_read_b128 v[48:51], v102 offset:32768\n\t" \
  "ds_read_b128 v[52:55], v102 offset:33792\n\t" \
  "ds_read_b128 v[56:59], v102 offset:34816\n\t" \
  "ds_read_b128 v[60:63], v102 offset:35840\n\t" \
  "ds_read_b128 v[0:3], v102 offset:49152\n\t" \
  "ds_read_b128 v[4:7], v102 offset:50176\n\t" \
  "ds_read_b128 v[8:11], v102 offset:51200\n\t" \
  "ds_read_b128 v[12:15], v102 offset:52224\n\t" \
  "ds_read_b128 v[16:19], v102 offset:0\n\t" \
  "ds_read_b128 v[20:23], v102 offset:1024\n\t" \
  "ds_read_b128 v[24:27], v102 offset:2048\n\t" \
  "ds_read_b128 v[28:31], v102 offset:3072\n\t" \
  "s_waitcnt lgkmcnt(4)\n\t" \
  "ds_read_b128 v[80:83], v102 offset:16384\n\t" \
  "ds_read_b128 v[84:87], v102 offset:17408\n\t" \
  "ds_read_b128 v[88:91], v102 offset:18432\n\t" \
  "ds_read_b128 v[92:95], v102 offset:19456\n\t" \
  "46:\n\t" \
  "s_bitcmp1_b32 s58, 1\n\t" \
  "s_cselect_b64 s[80:81], -1, 0\n\t" \
  "s_cmp_lg_u32 s58, 0\n\t" \
  "s_cbranch_scc1 17f\n\t" \
  "s_mov_b64 exec, 0xff\n\t" \
  "16:\n\t" \
  "s_memtime vcc\n\t" \
  "s_waitcnt lgkmcnt(0)\n\t" \
  "s_sub_i32 s79, s41, vcc_lo\n\t" \
  "s_cmp_gt_i32 s79, 64\n\t" \
  "s_cbranch_scc1 16b\n\t" \
  "13:\n\t" \
  "buffer_load_dword v116, v112, %[rh], s48 offen sc1\n\t" \
  "s_memtime vcc\n\t" \
  "s_memrealtime s[82:83]\n\t" \
  "s_nop 4\n\t" \
  "s_memrealtime s[84:85]\n\t" \
  "s_nop 4\n\t" \
  "s_memrealtime s[56:57]\n\t" \
  "s_nop 4\n\t" \
  "s_memrealtime s[86:87]\n\t" \
  "s_nop 4\n\t" \
  "s_memrealtime s[88:89]\n\t" \
  "s_nop 4\n\t" \
  "s_memrealtime s[90:91]\n\t" \
  "s_mov_b32 s92, 0\n\t" \
  "s_waitcnt lgkmcnt(0)\n\t" \
  "s_cmp_eq_u32 s82, s84\n\t" \
  "s_addc_u32 s92, s92, 0\n\t" \
  "s_cmp_eq_u32 s82, s56\n\t" \
  "s_addc_u32 s92, s92, 0\n\t" \
  "s_cmp_eq_u32 s82, s86\n\t" \
  "s_addc_u32 s92, s92, 0\n\t" \
  "s_cmp_eq_u32 s82, s88\n\t" \
  "s_addc_u32 s92, s92, 0\n\t" \
  "s_cmp_eq_u32 s82, s90\n\t" \
  "s_addc_u32 s92, s92, 0\n\t" \
  "s_mul_i32 s92, s92, 24\n\t" \
  "s_add_i32 s92, s92, vcc_lo\n\t" \
  "s_lshl4_add_u32 s93, s82, 64\n\t" \
  "s_lshl_b32 s50, s93, 8\n\t" \
  "s_waitcnt vmcnt(0)\n\t" \
  "15:\n\t" \
  "v_mov_b32 v117, 20000\n\t" \
  "v_cmp_ne_u32_sdwa vcc, v116, s49 src0_sel:BYTE_0 src1_sel:DWORD\n\t" \
  "v_subrev_u32 v116, s50, v116\n\t" \
  "v_ashrrev_i32 v116, 8, v116\n\t" \
  "s_nop 1\n\t" \
  "v_max_i32_dpp v116, v116, v116 quad_perm:[1,0,3,2] row_mask:0xf bank_mask:0xf\n\t" \
  "s_cbranch_vccnz 14f\n\t" \
  "s_nop 0\n\t" \
  "v_max_i32_dpp v116, v116, v116 quad_perm:[2,3,0,1] row_mask:0xf bank_mask:0xf\n\t" \
  "s_nop 1\n\t" \
  "v_max_i32_dpp v116, v116, v116 row_half_mirror row_mask:0xf bank_mask:0xf\n\t" \
  "v_med3_i32 v116, v116, 0, v117\n\t" \
  "v_mul_u32_u24 v116, s52, v116\n\t" \
  "v_lshrrev_b32 v116, 16, v116\n\t" \
  "s_mov_b64 exec, -1\n\t" \
  "v_readfirstlane_b32 s84, v116\n\t" \
  "s_add_i32 s84, s84, s92\n\t" \
  ".p2align 6\n\t" \
  "s_nop 0\n\t" \
  "s_nop 0\n\t" \
  "s_nop 0\n\t" \
  "s_nop 0\n\t" \
  "s_nop 0\n\t" \
  "s_nop 0\n\t" \
  "s_nop 0\n\t" \
  "s_nop 0\n\t" \
  "1:\n\t" \
  "s_memtime vcc\n\t" \
  "s_waitcnt lgkmcnt(0)\n\t" \
  "s_sub_i32 s79, s84, vcc_lo\n\t" \
  "s_cmp_gt_i32 s79, 128\n\t" \
  "s_cbranch_scc1 1b\n\t" \
  ".p2align 4\n\t" \
  "2:\n\t" \
  "s_sub_i32 s79, s79, 33\n\t" \
  "s_cmp_gt_i32 s79, 56\n\t" \
  "s_cbranch_scc1 2b\n\t" \
  "buffer_load_dwordx4 v[32:35], v100, %[rh], s40 offen sc1\n\t" \
  "s_nop 12\n\t" \
  "buffer_load_dwordx4 v[36:39], v100, %[rh], s76 offen sc1\n\t" \
  "s_nop 12\n\t" \
  "buffer_load_dwordx4 v[40:43], v100, %[rh], s77 offen sc1\n\t" \
  "s_nop 12\n\t" \
  "buffer_load_dwordx4 v[44:47], v100, %[rh], s78 offen sc1\n\t" \
  "3:\n\t" \
  "v_mov_b64 v[72:73], 0\n\t" \
  "v_mov_b64 v[74:75], 0\n\t" \
  "v_mov_b64 v[76:77], 0\n\t" \
  "v_mov_b64 v[78:79], 0\n\t" \
  "s_waitcnt lgkmcnt(0)\n\t" \
  "s_waitcnt vmcnt(3)\n\t" \
  "v_smfmac_f32_16x16x64_bf16 v[72:75], v[32:35], v[48:55], v104\n\t" \
  "ds_write_b128 v101, v[32:35] offset:8192\n\t" \
  "v_smfmac_f32_16x16x64_bf16 v[76:79], v[32:35], v[56:63], v104\n\t" \
  "s_waitcnt vmcnt(2)\n\t" \
  "ds_write_b128 v101, v[36:39] offset:9216\n\t" \
  "v_smfmac_f32_16x16x64_bf16 v[72:75], v[36:39], a[96:103], v104\n\t" \
  "v_smfmac_f32_16x16x64_bf16 v[76:79], v[36:39], a[104:111], v104\n\t" \
  "s_nop 3\n\t" \
  "v_cmp_u_f32 vcc, v72, v73\n\t" \
  "v_cmp_u_f32 s[82:83], v74, v75\n\t" \
  "s_waitcnt vmcnt(1)\n\t" \
  "v_smfmac_f32_16x16x64_bf16 v[72:75], v[40:43], a[112:119], v104\n\t" \
  "s_or_b64 vcc, vcc, s[82:83]\n\t" \
  "s_or_b64 vcc, vcc, s[80:81]\n\t" \
  "v_smfmac_f32_16x16x64_bf16 v[76:79], v[40:43], a[120:127], v104\n\t" \
  "s_waitcnt lgkmcnt(0)\n\t" \
  "s_cbranch_scc1 8f\n\t" \
  "5:\n\t" \
  "s_barrier\n\t" \
  "4:\n\t" \
  "s_memtime s[56:57]\n\t" \
  "ds_read_b128 v[48:51], v101 offset:12288\n\t" \
  "ds_read_b128 v[52:55], v101 offset:13312\n\t" \
  "ds_read_b128 v[56:59], v101 offset:1024\n\t" \
  "s_waitcnt vmcnt(0)\n\t" \
  "v_smfmac_f32_16x16x64_bf16 v[72:75], v[44:47], a[128:135], v104\n\t" \
  "ds_read_b128 v[60:63], v101 offset:0\n\t" \
  "ds_read_b128 v[64:67], v101 offset:5120\n\t" \
  "v_smfmac_f32_16x16x64_bf16 v[76:79], v[44:47], a[136:143], v104\n\t" \
  "ds_read_b128 v[68:71], v101 offset:4096\n\t" \
  "ds_write_b128 v101, v[40:43] offset:10240\n\t" \
  "s_nop 1\n\t" \
  "v_cmp_u_f32 vcc, v72, v73\n\t" \
  "v_cmp_u_f32 s[82:83], v74, v75\n\t" \
  "s_waitcnt lgkmcnt(6)\n\t" \
  "v_smfmac_f32_16x16x64_bf16 v[72:75], v[48:51], v[0:7], v104\n\t" \
  "s_or_b64 vcc, vcc, s[82:83]\n\t" \
  "v_smfmac_f32_16x16x64_bf16 v[76:79], v[48:51], v[8:15], v104\n\t" \
  "s_cbranch_scc1 9f\n\t" \
  "6:\n\t" \
  "s_waitcnt lgkmcnt(5)\n\t" \
  "v_smfmac_f32_16x16x64_bf16 v[72:75], v[52:55], a[144:151], v104\n\t" \
  "ds_write_b128 v101, v[44:47] offset:11264\n\t" \
  "v_smfmac_f32_16x16x64_bf16 v[76:79], v[52:55], a[152:159], v104\n\t" \
  "v_mov_b64 v[40:41], 0\n\t" \
  "s_waitcnt lgkmcnt(5)\n\t" \
  "v_smfmac_f32_16x16x64_bf16 v[72:75], v[56:59], a[0:7], v104\n\t" \
  "v_mov_b64 v[42:43], 0\n\t" \
  "v_smfmac_f32_16x16x64_bf16 v[76:79], v[56:59], a[8:15], v104\n\t" \
  "s_waitcnt lgkmcnt(4)\n\t" \
  "v_smfmac_f32_16x16x64_bf16 v[72:75], v[60:63], v[16:23], v104\n\t" \
  "s_waitcnt lgkmcnt(0)\n\t" \
  "s_barrier\n\t" \
  "v_smfmac_f32_16x16x64_bf16 v[76:79], v[60:63], v[24:31], v104\n\t" \
  "ds_read_b128 v[48:51], v101 offset:14336\n\t" \
  "ds_read_b128 v[52:55], v101 offset:2048\n\t" \
  "v_smfmac_f32_16x16x64_bf16 v[72:75], v[64:67], a[48:55], v104\n\t" \
  "ds_read_b128 v[56:59], v101 offset:6144\n\t" \
  "ds_read_b128 v[60:63], v101 offset:15360\n\t" \
  "v_smfmac_f32_16x16x64_bf16 v[76:79], v[64:67], a[56:63], v104\n\t" \
  "ds_read_b128 v[32:35], v101 offset:3072\n\t" \
  "ds_read_b128 v[36:39], v101 offset:7168\n\t" \
  "v_smfmac_f32_16x16x64_bf16 v[72:75], v[68:71], v[80:87], v104\n\t" \
  "ds_read_b128 v[0:3], v103 offset:0\n\t" \
  "v_mov_b64 v[44:45], 0\n\t" \
  "v_smfmac_f32_16x16x64_bf16 v[76:79], v[68:71], v[88:95], v104\n\t" \
  "v_mov_b64 v[46:47], 0\n\t" \
  "s_waitcnt lgkmcnt(5)\n\t" \
  "v_smfmac_f32_16x16x64_bf16 v[72:75], v[48:51], a[160:167], v104\n\t" \
  "ds_read_b128 v[4:7], v103 offset:1024\n\t" \
  "v_mov_b64 v[80:81], 0\n\t" \
  "v_smfmac_f32_16x16x64_bf16 v[72:75], v[52:55], a[16:23], v104\n\t" \
  "v_mov_b64 v[82:83], 0\n\t" \
  "s_waitcnt lgkmcnt(2)\n\t" \
  "v_smfmac_f32_16x16x64_bf16 v[72:75], v[56:59], a[64:71], v104\n\t" \
  "ds_read_b128 v[8:11], v103 offset:2048\n\t" \
  "v_mov_b64 v[84:85], 0\n\t" \
  "v_smfmac_f32_16x16x64_bf16 v[72:75], v[60:63], a[176:183], v104\n\t" \
  "ds_read_b128 v[12:15], v103 offset:3072\n\t" \
  "v_mov_b64 v[86:87], 0\n\t" \
  "v_smfmac_f32_16x16x64_bf16 v[72:75], v[32:35], a[32:39], v104\n\t" \
  "ds_read_b128 v[16:19], v103 offset:16384\n\t" \
  "v_mov_b64 v[88:89], 0\n\t" \
  "v_smfmac_f32_16x16x64_bf16 v[72:75], v[36:39], a[80:87], v104\n\t" \
  "ds_read_b128 v[20:23], v103 offset:17408\n\t" \
  "v_mov_b64 v[90:91], 0\n\t" \
  "v_smfmac_f32_16x16x64_bf16 v[76:79], v[48:51], a[168:175], v104\n\t" \
  "ds_read_b128 v[24:27], v103 offset:18432\n\t" \
  "v_mov_b64 v[92:93], 0\n\t" \
  "v_smfmac_f32_16x16x64_bf16 v[76:79], v[52:55], a[24:31], v104\n\t" \
  "v_add_f32 v96, v72, v74\n\t" \
  "v_add_f32 v97, v73, v75\n\t" \
  "v_cvt_pk_bf16_f32 v96, v96, v97\n\t" \
  "ds_write_b32 v105, v96\n\t" \
  "v_smfmac_f32_16x16x64_bf16 v[76:79], v[56:59], a[72:79], v104\n\t" \
  "ds_read_b128 v[28:31], v103 offset:19456\n\t" \
  "v_mov_b64 v[94:95], 0\n\t" \
  "v_smfmac_f32_16x16x64_bf16 v[76:79], v[60:63], a[184:191], v104\n\t" \
  "v_smfmac_f32_16x16x64_bf16 v[76:79], v[32:35], a[40:47], v104\n\t" \
  "v_smfmac_f32_16x16x64_bf16 v[76:79], v[36:39], a[88:95], v104\n\t" \
  "s_waitcnt lgkmcnt(1)\n\t" \
  "s_barrier\n\t" \
  "ds_read_b64_tr_b16 v[96:97], v107 offset:0\n\t" \
  "ds_read_b64_tr_b16 v[98:99], v107 offset:64\n\t" \
  "ds_read_b64_tr_b16 v[56:57], v107 offset:1024\n\t" \
  "ds_read_b64_tr_b16 v[58:59], v107 offset:1088\n\t" \
  "s_nop 1\n\t" \
  "v_add_f32 v116, v76, v78\n\t" \
  "v_add_f32 v117, v77, v79\n\t" \
  "v_cvt_pk_bf16_f32 v116, v116, v117\n\t" \
  "ds_write_b32 v105, v116 offset:256\n\t" \
  "s_waitcnt lgkmcnt(3)\n\t" \
  "v_smfmac_f32_16x16x64_bf16 v[40:43], v[96:99], a[192:199], v106\n\t" \
  "v_smfmac_f32_16x16x64_bf16 v[44:47], v[96:99], a[224:231], v106\n\t" \
  "s_waitcnt lgkmcnt(0)\n\t" \
  "s_barrier\n\t" \
  "ds_read_b64_tr_b16 v[52:53], v107 offset:256\n\t" \
  "ds_read_b64_tr_b16 v[54:55], v107 offset:320\n\t" \
  "v_smfmac_f32_16x16x64_bf16 v[40:43], v[56:59], a[208:215], v106\n\t" \
  "ds_read_b64_tr_b16 v[60:61], v107 offset:1280\n\t" \
  "ds_read_b64_tr_b16 v[62:63], v107 offset:1344\n\t" \
  "v_smfmac_f32_16x16x64_bf16 v[44:47], v[56:59], v[0:7], v106\n\t" \
  "v_smfmac_f32_16x16x64_bf16 v[80:83], v[96:99], a[240:247], v106\n\t" \
  "v_mov_b64 v[32:33], 0\n\t" \
  "v_smfmac_f32_16x16x64_bf16 v[84:87], v[96:99], v[136:143], v106\n\t" \
  "v_mov_b64 v[34:35], 0\n\t" \
  "v_smfmac_f32_16x16x64_bf16 v[80:83], v[56:59], v[120:127], v106\n\t" \
  "v_mov_b64 v[36:37], 0\n\t" \
  "v_smfmac_f32_16x16x64_bf16 v[84:87], v[56:59], v[16:23], v106\n\t" \
  "s_waitcnt lgkmcnt(2)\n\t" \
  "v_smfmac_f32_16x16x64_bf16 v[40:43], v[52:55], a[200:207], v106\n\t" \
  "v_smfmac_f32_16x16x64_bf16 v[44:47], v[52:55], a[232:239], v106\n\t" \
  "s_xor_b32 s76, s47, 0x40000\n\t" \
  "s_waitcnt lgkmcnt(0)\n\t" \
  "v_smfmac_f32_16x16x64_bf16 v[40:43], v[60:63], a[216:223], v106\n\t" \
  "v_mov_b64 v[38:39], 0\n\t" \
  "s_xor_b32 s77, s47, 0x80000\n\t" \
  "v_smfmac_f32_16x16x64_bf16 v[44:47], v[60:63], v[8:15], v106\n\t" \
  "s_xor_b32 s78, s47, 0xc0000\n\t" \
  "v_smfmac_f32_16x16x64_bf16 v[80:83], v[52:55], a[248:255], v106\n\t" \
  "ds_read_b128 v[0:3], v103 offset:32768\n\t" \
  "v_cvt_pk_bf16_f32 v64, v40, v41\n\t" \
  "v_smfmac_f32_16x16x64_bf16 v[84:87], v[52:55], v[144:151], v106\n\t" \
  "ds_read_b128 v[4:7], v103 offset:33792\n\t" \
  "v_cvt_pk_bf16_f32 v65, v42, v43\n\t" \
  "v_smfmac_f32_16x16x64_bf16 v[80:83], v[60:63], v[128:135], v106\n\t" \
  "ds_read_b128 v[8:11], v103 offset:34816\n\t" \
  "v_cvt_pk_bf16_f32 v66, v44, v45\n\t" \
  "v_smfmac_f32_16x16x64_bf16 v[84:87], v[60:63], v[24:31], v106\n\t" \
  "ds_read_b128 v[12:15], v103 offset:35840\n\t" \
  "v_cvt_pk_bf16_f32 v67, v46, v47\n\t" \
  "v_smfmac_f32_16x16x64_bf16 v[88:91], v[96:99], v[152:159], v106\n\t" \
  "v_permlane16_swap_b32 v64, v66\n\t" \
  "v_cvt_pk_bf16_f32 v68, v80, v81\n\t" \
  "v_smfmac_f32_16x16x64_bf16 v[92:95], v[96:99], v[184:191], v106\n\t" \
  "v_permlane16_swap_b32 v65, v67\n\t" \
  "v_cvt_pk_bf16_f32 v69, v82, v83\n\t" \
  "v_smfmac_f32_16x16x64_bf16 v[88:91], v[52:55], v[160:167], v106\n\t" \
  "v_cvt_pk_bf16_f32 v70, v84, v85\n\t" \
  "v_smfmac_f32_16x16x64_bf16 v[92:95], v[52:55], v[192:199], v106\n\t" \
  "buffer_store_dwordx4 v[64:67], v108, %[ru], s47 offen sc1\n\t" \
  "v_cvt_pk_bf16_f32 v71, v86, v87\n\t" \
  "v_smfmac_f32_16x16x64_bf16 v[88:91], v[56:59], v[168:175], v106\n\t" \
  "ds_read_b128 v[16:19], v103 offset:49152\n\t" \
  "v_permlane16_swap_b32 v68, v70\n\t" \
  "s_waitcnt lgkmcnt(3)\n\t" \
  "v_smfmac_f32_16x16x64_bf16 v[92:95], v[56:59], v[0:7], v106\n\t" \
  "ds_read_b128 v[20:23], v103 offset:50176\n\t" \
  "v_permlane16_swap_b32 v69, v71\n\t" \
  "v_smfmac_f32_16x16x64_bf16 v[88:91], v[60:63], v[176:183], v106\n\t" \
  "ds_read_b128 v[24:27], v103 offset:51200\n\t" \
  "s_waitcnt lgkmcnt(3)\n\t" \
  "v_smfmac_f32_16x16x64_bf16 v[92:95], v[60:63], v[8:15], v106\n\t" \
  "ds_read_b128 v[28:31], v103 offset:52224\n\t" \
  "buffer_store_dwordx4 v[68:71], v108, %[ru], s76 offen sc1\n\t" \
  "v_smfmac_f32_16x16x64_bf16 v[32:35], v[96:99], v[200:207], v106\n\t" \
  "v_cvt_pk_bf16_f32 v72, v88, v89\n\t" \
  "v_smfmac_f32_16x16x64_bf16 v[36:39], v[96:99], v[232:239], v106\n\t" \
  "v_cvt_pk_bf16_f32 v73, v90, v91\n\t" \
  "v_smfmac_f32_16x16x64_bf16 v[32:35], v[52:55], v[208:215], v106\n\t" \
  "v_cvt_pk_bf16_f32 v74, v92, v93\n\t" \
  "v_smfmac_f32_16x16x64_bf16 v[36:39], v[52:55], v[240:247], v106\n\t" \
  "v_cvt_pk_bf16_f32 v75, v94, v95\n\t" \
  "v_smfmac_f32_16x16x64_bf16 v[32:35], v[56:59], v[216:223], v106\n\t" \
  "v_permlane16_swap_b32 v72, v74\n\t" \
  "v_smfmac_f32_16x16x64_bf16 v[32:35], v[60:63], v[224:231], v106\n\t" \
  "v_permlane16_swap_b32 v73, v75\n\t" \
  "s_waitcnt lgkmcnt(2)\n\t" \
  "v_smfmac_f32_16x16x64_bf16 v[36:39], v[56:59], v[16:23], v106\n\t" \
  "s_waitcnt lgkmcnt(0)\n\t" \
  "v_smfmac_f32_16x16x64_bf16 v[36:39], v[60:63], v[24:31], v106\n\t" \
  "buffer_store_dwordx4 v[72:75], v108, %[ru], s77 offen sc1\n\t" \
  "v_cvt_pk_bf16_f32 v76, v32, v33\n\t" \
  "v_cvt_pk_bf16_f32 v77, v34, v35\n\t" \
  "s_nop 5\n\t" \
  "v_cvt_pk_bf16_f32 v78, v36, v37\n\t" \
  "v_cvt_pk_bf16_f32 v79, v38, v39\n\t" \
  "s_nop 0\n\t" \
  "v_permlane16_swap_b32 v76, v78\n\t" \
  "v_permlane16_swap_b32 v77, v79\n\t" \
  "buffer_store_dwordx4 v[76:79], v108, %[ru], s78 offen sc1\n\t" \
  "s_memtime s[76:77]\n\t" \
  "s_mov_b32 s42, 0\n\t" \
  "s_waitcnt lgkmcnt(0)\n\t" \
  "s_mov_b32 s57, s76\n\t" \
  "s_add_i32 s41, s56, 1950\n\t" \
  "s_sub_i32 s76, s92, s51\n\t" \
  "s_sub_i32 s77, s93, s53\n\t" \
  "s_cmp_eq_u32 s76, 0\n\t" \
  "s_cbranch_scc1 21f\n\t" \
  "v_cvt_f32_u32 v117, s76\n\t" \
  "v_cvt_f32_u32 v116, s77\n\t" \
  "v_rcp_f32 v117, v117\n\t" \
  "s_mov_b32 s51, s92\n\t" \
  "v_mul_f32 v117, v117, v116\n\t" \
  "v_max_f32 v117, 0x3f028f5c, v117\n\t" \
  "v_min_f32 v117, 0x3ffeb852, v117\n\t" \
  "v_rcp_f32 v116, v117\n\t" \
  "v_mul_f32 v117, 0x4f000000, v117\n\t" \
  "v_mul_f32 v116, 0x47800000, v116\n\t" \
  "v_cvt_u32_f32 v117, v117\n\t" \
  "v_cvt_u32_f32 v116, v116\n\t" \
  "s_mov_b32 s53, s93\n\t" \
  "v_readfirstlane_b32 s46, v117\n\t" \
  "v_readfirstlane_b32 s52, v116\n\t" \
  "21:\n\t" \
  "s_add_i32 s44, s44, 1\n\t" \
  "s_and_b32 s58, s58, 4\n\t" \
  "s_and_b32 s76, s44, 64\n\t" \
  "s_or_b32 s42, s42, s76\n\t" \
  "s_cbranch_scc0 20b\n\t" \
  "s_branch 99f\n\t" \
  "14:\n\t" \
  "s_sleep 3\n\t" \
  "s_add_u32 s59, s59, 0x10000\n\t" \
  "s_cmp_lt_u32 s59, 0x400000\n\t" \
  "s_cbranch_scc1 13b\n\t" \
  "s_mov_b64 exec, -1\n\t" \
  "s_mov_b32 s42, 16\n\t" \
  "s_branch 99f\n\t" \
  "8:\n\t" \
  "s_bitcmp1_b32 s58, 1\n\t" \
  "s_cbranch_scc1 4b\n\t" \
  "s_bitcmp1_b32 s58, 2\n\t" \
  "s_cbranch_scc1 5b\n\t" \
  "v_max3_u32 v116, v32, v33, v34\n\t" \
  "v_max3_u32 v117, v35, v36, v37\n\t" \
  "v_max3_u32 v116, v116, v38, v39\n\t" \
  "v_max_u32 v116, v116, v117\n\t" \
  "v_cmp_eq_u32 vcc, -1, v116\n\t" \
  "s_cbranch_vccz 5b\n\t" \
  "s_branch 0f\n\t" \
  "9:\n\t" \
  "s_bitcmp1_b32 s58, 2\n\t" \
  "s_cbranch_scc1 6b\n\t" \
  "v_max3_u32 v116, v40, v41, v42\n\t" \
  "v_max3_u32 v117, v43, v44, v45\n\t" \
  "v_max3_u32 v116, v116, v46, v47\n\t" \
  "v_max_u32 v116, v116, v117\n\t" \
  "v_cmp_eq_u32 vcc, -1, v116\n\t" \
  "s_cbranch_vccz 6b\n\t" \
  "s_or_b32 s58, s58, 2\n\t" \
  "0:\n\t" \
  "s_or_b32 s58, s58, 1\n\t" \
  "11:\n\t" \
  "buffer_load_dwordx4 v[32:35], v100, %[rh], s40 offen sc1\n\t" \
  "buffer_load_dwordx4 v[36:39], v100, %[rh], s76 offen sc1\n\t" \
  "buffer_load_dwordx4 v[40:43], v100, %[rh], s77 offen sc1\n\t" \
  "buffer_load_dwordx4 v[44:47], v100, %[rh], s78 offen sc1\n\t" \
  "s_waitcnt lgkmcnt(0)\n\t" \
  "s_branch 10b\n\t" \
  "17:\n\t" \
  "s_add_u32 s59, s59, 1\n\t" \
  "s_bitcmp1_b32 s58, 2\n\t" \
  "s_cbranch_scc1 3b\n\t" \
  "s_waitcnt vmcnt(0)\n\t" \
  "v_max_u32 v116, v32, v33\n\t" \
  "v_max3_u32 v116, v116, v34, v35\n\t" \
  "v_max3_u32 v116, v116, v36, v37\n\t" \
  "v_max3_u32 v116, v116, v38, v39\n\t" \
  "v_max3_u32 v116, v116, v40, v41\n\t" \
  "v_max3_u32 v116, v116, v42, v43\n\t" \
  "v_max3_u32 v116, v116, v44, v45\n\t" \
  "v_max3_u32 v116, v116, v46, v47\n\t" \
  "v_cmp_eq_u32 vcc, -1, v116\n\t" \
  "s_cbranch_vccz 3b\n\t" \
  "s_bitcmp1_b32 s59, 6\n\t" \
  "s_cbranch_scc1 12f\n\t" \
  "buffer_load_dwordx4 v[32:35], v100, %[rh], s40 offen sc1\n\t" \
  "buffer_load_dwordx4 v[36:39], v100, %[rh], s76 offen sc1\n\t" \
  "buffer_load_dwordx4 v[40:43], v100, %[rh], s77 offen sc1\n\t" \
  "buffer_load_dwordx4 v[44:47], v100, %[rh], s78 offen sc1\n\t" \
  "s_branch 17b\n\t" \
  "12:\n\t" \
  "s_mov_b32 s42, s58\n\t" \
  "s_branch 99f\n\t" \
  "93:\n\t" \
  "s_mov_b32 s58, s42\n\t" \
  "20:\n\t" \
  "s_and_b32 s76, s44, 7\n\t" \
  "s_lshl_b32 s76, s76, 15\n\t" \
  "s_add_i32 s40, s54, s76\n\t" \
  "s_and_b32 s76, s44, 3\n\t" \
  "s_lshl_b32 s45, s76, 14\n\t" \
  "s_lshl_b32 s47, s76, 20\n\t" \
  "s_add_i32 s47, s47, s55\n\t" \
  "s_lshr_b32 s76, s47, 21\n\t" \
  "s_bitcmp1_b64 %[sel], s76\n\t" \
  "s_cselect_b32 s76, 0x2000, 0\n\t" \
  "s_add_i32 s47, s47, s76\n\t" \
  "s_add_i32 s49, s44, -1\n\t" \
  "s_and_b32 s48, s49, 2\n\t" \
  "s_lshl_b32 s48, s48, 13\n\t" \
  "s_and_b32 s76, s49, 1\n\t" \
  "s_lshl_b32 s76, s76, 12\n\t" \
  "s_add_i32 s48, s48, s76\n\t" \
  "s_lshl_b32 s50, s53, 8\n\t" \
  "s_mov_b32 s59, 0\n\t" \
  "s_mov_b32 s92, s51\n\t" \
  "s_mov_b32 s93, s53\n\t" \
  "s_xor_b32 s76, s40, 0x400\n\t" \
  "s_xor_b32 s77, s40, 0x800\n\t" \
  "s_xor_b32 s78, s40, 0xc00\n\t" \
  "s_cmp_lg_u32 s58, 0\n\t" \
  "s_cbranch_scc1 11f\n\t" \
  "10:\n\t" \
  "ds_read_b128 v[48:51], v102 offset:49152\n\t" \
  "ds_read_b128 v[52:55], v102 offset:50176\n\t" \
  "ds_read_b128 v[56:59], v102 offset:51200\n\t" \
  "ds_read_b128 v[60:63], v102 offset:52224\n\t" \
  "ds_read_b128 v[0:3], v102 offset:0\n\t" \
  "ds_read_b128 v[4:7], v102 offset:1024\n\t" \
  "ds_read_b128 v[8:11], v102 offset:2048\n\t" \
  "ds_read_b128 v[12:15], v102 offset:3072\n\t" \
  "ds_read_b128 v[16:19], v102 offset:16384\n\t" \
  "ds_read_b128 v[20:23], v102 offset:17408\n\t" \
  "ds_read_b128 v[24:27], v102 offset:18432\n\t" \
  "ds_read_b128 v[28:31], v102 offset:19456\n\t" \
  "s_waitcnt lgkmcnt(4)\n\t" \
  "ds_read_b128 v[80:83], v102 offset:32768\n\t" \
  "ds_read_b128 v[84:87], v102 offset:33792\n\t" \
  "ds_read_b128 v[88:91], v102 offset:34816\n\t" \
  "ds_read_b128 v[92:95], v102 offset:35840\n\t" \
  "46:\n\t" \
  "s_bitcmp1_b32 s58, 1\n\t" \
  "s_cselect_b64 s[80:81], -1, 0\n\t" \
  "s_cmp_lg_u32 s58, 0\n\t" \
  "s_cbranch_scc1 17f\n\t" \
  "s_mov_b64 exec, 0xff\n\t" \
  "16:\n\t" \
  "s_memtime vcc\n\t" \
  "s_waitcnt lgkmcnt(0)\n\t" \
  "s_sub_i32 s79, s41, vcc_lo\n\t" \
  "s_cmp_gt_i32 s79, 64\n\t" \
  "s_cbranch_scc1 16b\n\t" \
  "13:\n\t" \
  "buffer_load_dword v116, v112, %[rh], s48 offen sc1\n\t" \
  "s_memtime vcc\n\t" \
  "s_memrealtime s[82:83]\n\t" \
  "s_nop 4\n\t" \
  "s_memrealtime s[84:85]\n\t" \
  "s_nop 4\n\t" \
  "s_memrealtime s[56:57]\n\t" \
  "s_nop 4\n\t" \
  "s_memrealtime s[86:87]\n\t" \
  "s_nop 4\n\t" \
  "s_memrealtime s[88:89]\n\t" \
  "s_nop 4\n\t" \
  "s_memrealtime s[90:91]\n\t" \
  "s_mov_b32 s92, 0\n\t" \
  "s_waitcnt lgkmcnt(0)\n\t" \
  "s_cmp_eq_u32 s82, s84\n\t" \
  "s_addc_u32 s92, s92, 0\n\t" \
  "s_cmp_eq_u32 s82, s56\n\t" \
  "s_addc_u32 s92, s92, 0\n\t" \
  "s_cmp_eq_u32 s82, s86\n\t" \
  "s_addc_u32 s92, s92, 0\n\t" \
  "s_cmp_eq_u32 s82, s88\n\t" \
  "s_addc_u32 s92, s92, 0\n\t" \
  "s_cmp_eq_u32 s82, s90\n\t" \
  "s_addc_u32 s92, s92, 0\n\t" \
  "s_mul_i32 s92, s92, 24\n\t" \
  "s_add_i32 s92, s92, vcc_lo\n\t" \
  "s_lshl4_add_u32 s93, s82, 64\n\t" \
  "s_lshl_b32 s50, s93, 8\n\t" \
  "s_waitcnt vmcnt(0)\n\t" \
  "15:\n\t" \
  "v_mov_b32 v117, 20000\n\t" \
  "v_cmp_ne_u32_sdwa vcc, v116, s49 src0_sel:BYTE_0 src1_sel:DWORD\n\t" \
  "v_subrev_u32 v116, s50, v116\n\t" \
  "v_ashrrev_i32 v116, 8, v116\n\t" \
  "s_nop 1\n\t" \
  "v_max_i32_dpp v116, v116, v116 quad_perm:[1,0,3,2] row_mask:0xf bank_mask:0xf\n\t" \
  "s_cbranch_vccnz 14f\n\t" \
  "s_nop 0\n\t" \
  "v_max_i32_dpp v116, v116, v116 quad_perm:[2,3,0,1] row_mask:0xf bank_mask:0xf\n\t" \
  "s_nop 1\n\t" \
  "v_max_i32_dpp v116, v116, v116 row_half_mirror row_mask:0xf bank_mask:0xf\n\t" \
  "v_med3_i32 v116, v116, 0, v117\n\t" \
  "v_mul_u32_u24 v116, s52, v116\n\t" \
  "v_lshrrev_b32 v116, 16, v116\n\t" \
  "s_mov_b64 exec, -1\n\t" \
  "v_readfirstlane_b32 s84, v116\n\t" \
  "s_add_i32 s84, s84, s92\n\t" \
  ".p2align 6\n\t" \
  "s_nop 0\n\t" \
  "s_nop 0\n\t" \
  "s_nop 0\n\t" \
  "s_nop 0\n\t" \
  "s_nop 0\n\t" \
  "s_nop 0\n\t" \
  "s_nop 0\n\t" \
  "s_nop 0\n\t" \
  "1:\n\t" \
  "s_memtime vcc\n\t" \
  "s_waitcnt lgkmcnt(0)\n\t" \
  "s_sub_i32 s79, s84, vcc_lo\n\t" \
  "s_cmp_gt_i32 s79, 128\n\t" \
  "s_cbranch_scc1 1b\n\t" \
  ".p2align 4\n\t" \
  "2:\n\t" \
  "s_sub_i32 s79, s79, 33\n\t" \
  "s_cmp_gt_i32 s79, 56\n\t" \
  "s_cbranch_scc1 2b\n\t" \
  "buffer_load_dwordx4 v[32:35], v100, %[rh], s40 offen sc1\n\t" \
  "s_nop 12\n\t" \
  "buffer_load_dwordx4 v[36:39], v100, %[rh], s76 offen sc1\n\t" \
  "s_nop 12\n\t" \
  "buffer_load_dwordx4 v[40:43], v100, %[rh], s77 offen sc1\n\t" \
  "s_nop 12\n\t" \
  "buffer_load_dwordx4 v[44:47], v100, %[rh], s78 offen sc1\n\t" \
  "3:\n\t" \
  "v_mov_b64 v[72:73], 0\n\t" \
  "v_mov_b64 v[74:75], 0\n\t" \
  "v_mov_b64 v[76:77], 0\n\t" \
  "v_mov_b64 v[78:79], 0\n\t" \
  "s_waitcnt lgkmcnt(0)\n\t" \
  "s_waitcnt vmcnt(3)\n\t" \
  "v_smfmac_f32_16x16x64_bf16 v[72:75], v[32:35], v[48:55], v104\n\t" \
  "ds_write_b128 v101, v[32:35] offset:12288\n\t" \
  "v_smfmac_f32_16x16x64_bf16 v[76:79], v[32:35], v[56:63], v104\n\t" \
  "s_waitcnt vmcnt(2)\n\t" \
  "ds_write_b128 v101, v[36:39] offset:13312\n\t" \
  "v_smfmac_f32_16x16x64_bf16 v[72:75], v[36:39], a[144:151], v104\n\t" \
  "v_smfmac_f32_16x16x64_bf16 v[76:79], v[36:39], a[152:159], v104\n\t" \
  "s_nop 3\n\t" \
  "v_cmp_u_f32 vcc, v72, v73\n\t" \
  "v_cmp_u_f32 s[82:83], v74, v75\n\t" \
  "s_waitcnt vmcnt(1)\n\t" \
  "v_smfmac_f32_16x16x64_bf16 v[72:75], v[40:43], a[160:167], v104\n\t" \
  "s_or_b64 vcc, vcc, s[82:83]\n\t" \
  "s_or_b64 vcc, vcc, s[80:81]\n\t" \
  "v_smfmac_f32_16x16x64_bf16 v[76:79], v[40:43], a[168:175], v104\n\t" \
  "s_waitcnt lgkmcnt(0)\n\t" \
  "s_cbranch_scc1 8f\n\t" \
  "5:\n\t" \
  "s_barrier\n\t" \
  "4:\n\t" \
  "s_memtime s[56:57]\n\t" \
  "ds_read_b128 v[48:51], v101 offset:0\n\t" \
  "ds_read_b128 v[52:55], v101 offset:1024\n\t" \
  "ds_read_b128 v[56:59], v101 offset:5120\n\t" \
  "s_waitcnt vmcnt(0)\n\t" \
  "v_smfmac_f32_16x16x64_bf16 v[72:75], v[44:47], a[176:183], v104\n\t" \
  "ds_read_b128 v[60:63], v101 offset:4096\n\t" \
  "ds_read_b128 v[64:67], v101 offset:9216\n\t" \
  "v_smfmac_f32_16x16x64_bf16 v[76:79], v[44:47], a[184:191], v104\n\t" \
  "ds_read_b128 v[68:71], v101 offset:8192\n\t" \
  "ds_write_b128 v101, v[40:43] offset:14336\n\t" \
  "s_nop 1\n\t" \
  "v_cmp_u_f32 vcc, v72, v73\n\t" \
  "v_cmp_u_f32 s[82:83], v74, v75\n\t" \
  "s_waitcnt lgkmcnt(6)\n\t" \
  "v_smfmac_f32_16x16x64_bf16 v[72:75], v[48:51], v[0:7], v104\n\t" \
  "s_or_b64 vcc, vcc, s[82:83]\n\t" \
  "v_smfmac_f32_16x16x64_bf16 v[76:79], v[48:51], v[8:15], v104\n\t" \
  "s_cbranch_scc1 9f\n\t" \
  "6:\n\t" \
  "s_waitcnt lgkmcnt(5)\n\t" \
  "v_smfmac_f32_16x16x64_bf16 v[72:75], v[52:55], a[0:7], v104\n\t" \
  "ds_write_b128 v101, v[44:47] offset:15360\n\t" \
  "v_smfmac_f32_16x16x64_bf16 v[76:79], v[52:55], a[8:15], v104\n\t" \
  "v_mov_b64 v[40:41], 0\n\t" \
  "s_waitcnt lgkmcnt(5)\n\t" \
  "v_smfmac_f32_16x16x64_bf16 v[72:75], v[56:59], a[48:55], v104\n\t" \
  "v_mov_b64 v[42:43], 0\n\t" \
  "v_smfmac_f32_16x16x64_bf16 v[76:79], v[56:59], a[56:63], v104\n\t" \
  "s_waitcnt lgkmcnt(4)\n\t" \
  "v_smfmac_f32_16x16x64_bf16 v[72:75], v[60:63], v[16:23], v104\n\t" \
  "s_waitcnt lgkmcnt(0)\n\t" \
  "s_barrier\n\t" \
  "v_smfmac_f32_16x16x64_bf16 v[76:79], v[60:63], v[24:31], v104\n\t" \
  "ds_read_b128 v[48:51], v101 offset:2048\n\t" \
  "ds_read_b128 v[52:55], v101 offset:6144\n\t" \
  "v_smfmac_f32_16x16x64_bf16 v[72:75], v[64:67], a[96:103], v104\n\t" \
  "ds_read_b128 v[56:59], v101 offset:10240\n\t" \
  "ds_read_b128 v[60:63], v101 offset:3072\n\t" \
  "v_smfmac_f32_16x16x64_bf16 v[76:79], v[64:67], a[104:111], v104\n\t" \
  "ds_read_b128 v[32:35], v101 offset:7168\n\t" \
  "ds_read_b128 v[36:39], v101 offset:11264\n\t" \
  "v_smfmac_f32_16x16x64_bf16 v[72:75], v[68:71], v[80:87], v104\n\t" \
  "ds_read_b128 v[0:3], v103 offset:0\n\t" \
  "v_mov_b64 v[44:45], 0\n\t" \
  "v_smfmac_f32_16x16x64_bf16 v[76:79], v[68:71], v[88:95], v104\n\t" \
  "v_mov_b64 v[46:47], 0\n\t" \
  "s_waitcnt lgkmcnt(5)\n\t" \
  "v_smfmac_f32_16x16x64_bf16 v[72:75], v[48:51], a[16:23], v104\n\t" \
  "ds_read_b128 v[4:7], v103 offset:1024\n\t" \
  "v_mov_b64 v[80:81], 0\n\t" \
  "v_smfmac_f32_16x16x64_bf16 v[72:75], v[52:55], a[64:71], v104\n\t" \
  "v_mov_b64 v[82:83], 0\n\t" \
  "s_waitcnt lgkmcnt(2)\n\t" \
  "v_smfmac_f32_16x16x64_bf16 v[72:75], v[56:59], a[112:119], v104\n\t" \
  "ds_read_b128 v[8:11], v103 offset:2048\n\t" \
  "v_mov_b64 v[84:85], 0\n\t" \
  "v_smfmac_f32_16x16x64_bf16 v[72:75], v[60:63], a[32:39], v104\n\t" \
  "ds_read_b128 v[12:15], v103 offset:3072\n\t" \
  "v_mov_b64 v[86:87], 0\n\t" \
  "v_smfmac_f32_16x16x64_bf16 v[72:75], v[32:35], a[80:87], v104\n\t" \
  "ds_read_b128 v[16:19], v103 offset:16384\n\t" \
  "v_mov_b64 v[88:89], 0\n\t" \
  "v_smfmac_f32_16x16x64_bf16 v[72:75], v[36:39], a[128:135], v104\n\t" \
  "ds_read_b128 v[20:23], v103 offset:17408\n\t" \
  "v_mov_b64 v[90:91], 0\n\t" \
  "v_smfmac_f32_16x16x64_bf16 v[76:79], v[48:51], a[24:31], v104\n\t" \
  "ds_read_b128 v[24:27], v103 offset:18432\n\t" \
  "v_mov_b64 v[92:93], 0\n\t" \
  "v_smfmac_f32_16x16x64_bf16 v[76:79], v[52:55], a[72:79], v104\n\t" \
  "v_add_f32 v96, v72, v74\n\t" \
  "v_add_f32 v97, v73, v75\n\t" \
  "v_cvt_pk_bf16_f32 v96, v96, v97\n\t" \
  "ds_write_b32 v105, v96\n\t" \
  "v_smfmac_f32_16x16x64_bf16 v[76:79], v[56:59], a[120:127], v104\n\t" \
  "ds_read_b128 v[28:31], v103 offset:19456\n\t" \
  "v_mov_b64 v[94:95], 0\n\t" \
  "v_smfmac_f32_16x16x64_bf16 v[76:79], v[60:63], a[40:47], v104\n\t" \
  "v_smfmac_f32_16x16x64_bf16 v[76:79], v[32:35], a[88:95], v104\n\t" \
  "v_smfmac_f32_16x16x64_bf16 v[76:79], v[36:39], a[136:143], v104\n\t" \
  "s_waitcnt lgkmcnt(1)\n\t" \
  "s_barrier\n\t" \
  "ds_read_b64_tr_b16 v[96:97], v107 offset:0\n\t" \
  "ds_read_b64_tr_b16 v[98:99], v107 offset:64\n\t" \
  "ds_read_b64_tr_b16 v[56:57], v107 offset:1024\n\t" \
  "ds_read_b64_tr_b16 v[58:59], v107 offset:1088\n\t" \
  "s_nop 1\n\t" \
  "v_add_f32 v116, v76, v78\n\t" \
  "v_add_f32 v117, v77, v79\n\t" \
  "v_cvt_pk_bf16_f32 v116, v116, v117\n\t" \
  "ds_write_b32 v105, v116 offset:256\n\t" \
  "s_waitcnt lgkmcnt(3)\n\t" \
  "v_smfmac_f32_16x16x64_bf16 v[40:43], v[96:99], a[192:199], v106\n\t" \
  "v_smfmac_f32_16x16x64_bf16 v[44:47], v[96:99], a[224:231], v106\n\t" \
  "s_waitcnt lgkmcnt(0)\n\t" \
  "s_barrier\n\t" \
  "ds_read_b64_tr_b16 v[52:53], v107 offset:256\n\t" \
  "ds_read_b64_tr_b16 v[54:55], v107 offset:320\n\t" \
  "v_smfmac_f32_16x16x64_bf16 v[40:43], v[56:59], a[208:215], v106\n\t" \
  "ds_read_b64_tr_b16 v[60:61], v107 offset:1280\n\t" \
  "ds_read_b64_tr_b16 v[62:63], v107 offset:1344\n\t" \
  "v_smfmac_f32_16x16x64_bf16 v[44:47], v[56:59], v[0:7], v106\n\t" \
  "v_smfmac_f32_16x16x64_bf16 v[80:83], v[96:99], a[240:247], v106\n\t" \
  "v_mov_b64 v[32:33], 0\n\t" \
  "v_smfmac_f32_16x16x64_bf16 v[84:87], v[96:99], v[136:143], v106\n\t" \
  "v_mov_b64 v[34:35], 0\n\t" \
  "v_smfmac_f32_16x16x64_bf16 v[80:83], v[56:59], v[120:127], v106\n\t" \
  "v_mov_b64 v[36:37], 0\n\t" \
  "v_smfmac_f32_16x16x64_bf16 v[84:87], v[56:59], v[16:23], v106\n\t" \
  "s_waitcnt lgkmcnt(2)\n\t" \
  "v_smfmac_f32_16x16x64_bf16 v[40:43], v[52:55], a[200:207], v106\n\t" \
  "v_smfmac_f32_16x16x64_bf16 v[44:47], v[52:55], a[232:239], v106\n\t" \
  "s_xor_b32 s76, s47, 0x40000\n\t" \
  "s_waitcnt lgkmcnt(0)\n\t" \
  "v_smfmac_f32_16x16x64_bf16 v[40:43], v[60:63], a[216:223], v106\n\t" \
  "v_mov_b64 v[38:39], 0\n\t" \
  "s_xor_b32 s77, s47, 0x80000\n\t" \
  "v_smfmac_f32_16x16x64_bf16 v[44:47], v[60:63], v[8:15], v106\n\t" \
  "s_xor_b32 s78, s47, 0xc0000\n\t" \
  "v_smfmac_f32_16x16x64_bf16 v[80:83], v[52:55], a[248:255], v106\n\t" \
  "ds_read_b128 v[0:3], v103 offset:32768\n\t" \
  "v_cvt_pk_bf16_f32 v64, v40, v41\n\t" \
  "v_smfmac_f32_16x16x64_bf16 v[84:87], v[52:55], v[144:151], v106\n\t" \
  "ds_read_b128 v[4:7], v103 offset:33792\n\t" \
  "v_cvt_pk_bf16_f32 v65, v42, v43\n\t" \
  "v_smfmac_f32_16x16x64_bf16 v[80:83], v[60:63], v[128:135], v106\n\t" \
  "ds_read_b128 v[8:11], v103 offset:34816\n\t" \
  "v_cvt_pk_bf16_f32 v66, v44, v45\n\t" \
  "v_smfmac_f32_16x16x64_bf16 v[84:87], v[60:63], v[24:31], v106\n\t" \
  "ds_read_b128 v[12:15], v103 offset:35840\n\t" \
  "v_cvt_pk_bf16_f32 v67, v46, v47\n\t" \
  "v_smfmac_f32_16x16x64_bf16 v[88:91], v[96:99], v[152:159], v106\n\t" \
  "v_permlane16_swap_b32 v64, v66\n\t" \
  "v_cvt_pk_bf16_f32 v68, v80, v81\n\t" \
  "v_smfmac_f32_16x16x64_bf16 v[92:95], v[96:99], v[184:191], v106\n\t" \
  "v_permlane16_swap_b32 v65, v67\n\t" \
  "v_cvt_pk_bf16_f32 v69, v82, v83\n\t" \
  "v_smfmac_f32_16x16x64_bf16 v[88:91], v[52:55], v[160:167], v106\n\t" \
  "v_cvt_pk_bf16_f32 v70, v84, v85\n\t" \
  "v_smfmac_f32_16x16x64_bf16 v[92:95], v[52:55], v[192:199], v106\n\t" \
  "v_cvt_pk_bf16_f32 v71, v86, v87\n\t" \
  "v_smfmac_f32_16x16x64_bf16 v[88:91], v[56:59], v[168:175], v106\n\t" \
  "ds_read_b128 v[16:19], v103 offset:49152\n\t" \
  "buffer_store_dwordx4 v[64:67], v108, %[ru], s47 offen sc1\n\t" \
  "v_permlane16_swap_b32 v68, v70\n\t" \
  "s_waitcnt lgkmcnt(3)\n\t" \
  "v_smfmac_f32_16x16x64_bf16 v[92:95], v[56:59], v[0:7], v106\n\t" \
  "ds_read_b128 v[20:23], v103 offset:50176\n\t" \
  "v_permlane16_swap_b32 v69, v71\n\t" \
  "v_smfmac_f32_16x16x64_bf16 v[88:91], v[60:63], v[176:183], v106\n\t" \
  "ds_read_b128 v[24:27], v103 offset:51200\n\t" \
  "s_waitcnt lgkmcnt(3)\n\t" \
  "v_smfmac_f32_16x16x64_bf16 v[92:95], v[60:63], v[8:15], v106\n\t" \
  "ds_read_b128 v[28:31], v103 offset:52224\n\t" \
  "v_smfmac_f32_16x16x64_bf16 v[32:35], v[96:99], v[200:207], v106\n\t" \
  "buffer_store_dwordx4 v[68:71], v108, %[ru], s76 offen sc1\n\t" \
  "v_cvt_pk_bf16_f32 v72, v88, v89\n\t" \
  "v_smfmac_f32_16x16x64_bf16 v[36:39], v[96:99], v[232:239], v106\n\t" \
  "v_cvt_pk_bf16_f32 v73, v90, v91\n\t" \
  "v_smfmac_f32_16x16x64_bf16 v[32:35], v[52:55], v[208:215], v106\n\t" \
  "v_cvt_pk_bf16_f32 v74, v92, v93\n\t" \
  "v_smfmac_f32_16x16x64_bf16 v[36:39], v[52:55], v[240:247], v106\n\t" \
  "v_cvt_pk_bf16_f32 v75, v94, v95\n\t" \
  "v_smfmac_f32_16x16x64_bf16 v[32:35], v[56:59], v[216:223], v106\n\t" \
  "v_permlane16_swap_b32 v72, v74\n\t" \
  "v_smfmac_f32_16x16x64_bf16 v[32:35], v[60:63], v[224:231], v106\n\t" \
  "v_permlane16_swap_b32 v73, v75\n\t" \
  "s_waitcnt lgkmcnt(2)\n\t" \
  "v_smfmac_f32_16x16x64_bf16 v[36:39], v[56:59], v[16:23], v106\n\t" \
  "s_waitcnt lgkmcnt(0)\n\t" \
  "v_smfmac_f32_16x16x64_bf16 v[36:39], v[60:63], v[24:31], v106\n\t" \
  "buffer_store_dwordx4 v[72:75], v108, %[ru], s77 offen sc1\n\t" \
  "v_cvt_pk_bf16_f32 v76, v32, v33\n\t" \
  "v_cvt_pk_bf16_f32 v77, v34, v35\n\t" \
  "s_nop 5\n\t" \
  "v_cvt_pk_bf16_f32 v78, v36, v37\n\t" \
  "v_cvt_pk_bf16_f32 v79, v38, v39\n\t" \
  "s_nop 0\n\t" \
  "v_permlane16_swap_b32 v76, v78\n\t" \
  "v_permlane16_swap_b32 v77, v79\n\t" \
  "buffer_store_dwordx4 v[76:79], v108, %[ru], s78 offen sc1\n\t" \
  "s_memtime s[76:77]\n\t" \
  "s_mov_b32 s42, 0\n\t" \
  "s_waitcnt lgkmcnt(0)\n\t" \
  "s_mov_b32 s57, s76\n\t" \
  "s_add_i32 s41, s56, 1950\n\t" \
  "s_sub_i32 s76, s92, s51\n\t" \
  "s_sub_i32 s77, s93, s53\n\t" \
  "s_cmp_eq_u32 s76, 0\n\t" \
  "s_cbranch_scc1 21f\n\t" \
  "v_cvt_f32_u32 v117, s76\n\t" \
  "v_cvt_f32_u32 v116, s77\n\t" \
  "v_rcp_f32 v117, v117\n\t" \
  "s_mov_b32 s51, s92\n\t" \
  "v_mul_f32 v117, v117, v116\n\t" \
  "v_max_f32 v117, 0x3f028f5c, v117\n\t" \
  "v_min_f32 v117, 0x3ffeb852, v117\n\t" \
  "v_rcp_f32 v116, v117\n\t" \
  "v_mul_f32 v117, 0x4f000000, v117\n\t" \
  "v_mul_f32 v116, 0x47800000, v116\n\t" \
  "v_cvt_u32_f32 v117, v117\n\t" \
  "v_cvt_u32_f32 v116, v116\n\t" \
  "s_mov_b32 s53, s93\n\t" \
  "v_readfirstlane_b32 s46, v117\n\t" \
  "v_readfirstlane_b32 s52, v116\n\t" \
  "21:\n\t" \
  "s_add_i32 s44, s44, 1\n\t" \
  "s_and_b32 s58, s58, 4\n\t" \
  "s_and_b32 s76, s44, 64\n\t" \
  "s_or_b32 s42, s42, s76\n\t" \
  "s_cbranch_scc0 20b\n\t" \
  "s_branch 99f\n\t" \
  "14:\n\t" \
  "s_sleep 3\n\t" \
  "s_add_u32 s59, s59, 0x10000\n\t" \
  "s_cmp_lt_u32 s59, 0x400000\n\t" \
  "s_cbranch_scc1 13b\n\t" \
  "s_mov_b64 exec, -1\n\t" \
  "s_mov_b32 s42, 16\n\t" \
  "s_branch 99f\n\t" \
  "8:\n\t" \
  "s_bitcmp1_b32 s58, 1\n\t" \
  "s_cbranch_scc1 4b\n\t" \
  "s_bitcmp1_b32 s58, 2\n\t" \
  "s_cbranch_scc1 5b\n\t" \
  "v_max3_u32 v116, v32, v33, v34\n\t" \
  "v_max3_u32 v117, v35, v36, v37\n\t" \
  "v_max3_u32 v116, v116, v38, v39\n\t" \
  "v_max_u32 v116, v116, v117\n\t" \
  "v_cmp_eq_u32 vcc, -1, v116\n\t" \
  "s_cbranch_vccz 5b\n\t" \
  "s_branch 0f\n\t" \
  "9:\n\t" \
  "s_bitcmp1_b32 s58, 2\n\t" \
  "s_cbranch_scc1 6b\n\t" \
  "v_max3_u32 v116, v40, v41, v42\n\t" \
  "v_max3_u32 v117, v43, v44, v45\n\t" \
  "v_max3_u32 v116, v116, v46, v47\n\t" \
  "v_max_u32 v116, v116, v117\n\t" \
  "v_cmp_eq_u32 vcc, -1, v116\n\t" \
  "s_cbranch_vccz 6b\n\t" \
  "s_or_b32 s58, s58, 2\n\t" \
  "0:\n\t" \
  "s_or_b32 s58, s58, 1\n\t" \
  "11:\n\t" \
  "buffer_load_dwordx4 v[32:35], v100, %[rh], s40 offen sc1\n\t" \
  "buffer_load_dwordx4 v[36:39], v100, %[rh], s76 offen sc1\n\t" \
  "buffer_load_dwordx4 v[40:43], v100, %[rh], s77 offen sc1\n\t" \
  "buffer_load_dwordx4 v[44:47], v100, %[rh], s78 offen sc1\n\t" \
  "s_waitcnt lgkmcnt(0)\n\t" \
  "s_branch 10b\n\t" \
  "17:\n\t" \
  "s_add_u32 s59, s59, 1\n\t" \
  "s_bitcmp1_b32 s58, 2\n\t" \
  "s_cbranch_scc1 3b\n\t" \
  "s_waitcnt vmcnt(0)\n\t" \
  "v_max_u32 v116, v32, v33\n\t" \
  "v_max3_u32 v116, v116, v34, v35\n\t" \
  "v_max3_u32 v116, v116, v36, v37\n\t" \
  "v_max3_u32 v116, v116, v38, v39\n\t" \
  "v_max3_u32 v116, v116, v40, v41\n\t" \
  "v_max3_u32 v116, v116, v42, v43\n\t" \
  "v_max3_u32 v116, v116, v44, v45\n\t" \
  "v_max3_u32 v116, v116, v46, v47\n\t" \
  "v_cmp_eq_u32 vcc, -1, v116\n\t" \
  "s_cbranch_vccz 3b\n\t" \
  "s_bitcmp1_b32 s59, 6\n\t" \
  "s_cbranch_scc1 12f\n\t" \
  "buffer_load_dwordx4 v[32:35], v100, %[rh], s40 offen sc1\n\t" \
  "buffer_load_dwordx4 v[36:39], v100, %[rh], s76 offen sc1\n\t" \
  "buffer_load_dwordx4 v[40:43], v100, %[rh], s77 offen sc1\n\t" \
  "buffer_load_dwordx4 v[44:47], v100, %[rh], s78 offen sc1\n\t" \
  "s_branch 17b\n\t" \
  "12:\n\t" \
  "s_mov_b32 s42, s58\n\t" \
  "s_branch 99f\n\t" \
  "99:\n\t" \
  "s_waitcnt vmcnt(0) lgkmcnt(0)\n\t" \
  ".Lchain_last:\n\t" \
  ""
#define CB_BODY \
  "s_cmp_gt_u32 %[wv], 1\n\t" \
  "s_cbranch_scc1 91f\n\t" \
  "s_mov_b32 s58, s42\n\t" \
  "s_mov_b32 s59, 0\n\t" \
  "s_bitcmp1_b32 s58, 4\n\t" \
  "s_cbranch_scc1 26f\n\t" \
  "s_bitcmp1_b32 s58, 3\n\t" \
  "s_cbranch_scc1 34f\n\t" \
  "s_bitcmp1_b32 s58, 1\n\t" \
  "s_cbranch_scc1 18f\n\t" \
  "17:\n\t" \
  "19:\n\t" \
  "s_add_i32 s85, s40, -1\n\t" \
  "s_and_b32 s82, s85, 2\n\t" \
  "s_lshl_b32 s82, s82, 13\n\t" \
  "s_and_b32 s80, s85, 1\n\t" \
  "s_lshl_b32 s80, s80, 12\n\t" \
  "s_add_i32 s82, s82, s80\n\t" \
  "s_add_i32 s82, s82, 0x84000\n\t" \
  "s_mov_b32 exec_hi, 0\n\t" \
  "buffer_load_dword v184, v200, %[rh], s82 offen sc1\n\t" \
  "buffer_load_dword v186, v203, %[ru], 0 offen sc1\n\t" \
  "s_mov_b64 exec, -1\n\t" \
  "20:\n\t" \
  "s_and_b32 s80, s40, 3\n\t" \
  "s_mul_i32 s77, s80, 0xc000\n\t" \
  "s_add_i32 s77, s77, 0x40000\n\t" \
  "s_lshl_b32 s78, s80, 14\n\t" \
  "s_add_i32 s78, s78, 0x70000\n\t" \
  "s_mov_b32 s57, 0\n\t" \
  "s_add_i32 s96, s77, 0x1000\n\t" \
  "s_lshl_b32 s88, s80, 20\n\t" \
  "s_add_i32 s88, s88, s48\n\t" \
  "s_lshr_b32 s80, s88, 21\n\t" \
  "s_bitcmp1_b64 %[sel], s80\n\t" \
  "s_cselect_b32 s80, 0x2000, 0\n\t" \
  "s_add_i32 s88, s88, s80\n\t" \
  "s_xor_b32 s89, s88, 0x800\n\t" \
  "s_xor_b32 s90, s88, 0x1000\n\t" \
  "s_xor_b32 s91, s88, 0x1800\n\t" \
  "s_add_i32 s92, s77, s50\n\t" \
  "s_xor_b32 s93, s92, 0x100\n\t" \
  "s_xor_b32 s94, s92, 0x200\n\t" \
  "s_xor_b32 s95, s92, 0x300\n\t" \
  "s_add_i32 s97, s40, 1\n\t" \
  "s_and_b32 s97, s97, 7\n\t" \
  "s_lshl_b32 s97, s97, 15\n\t" \
  "s_add_i32 s97, s97, s49\n\t" \
  "s_memtime vcc\n\t" \
  "s_memrealtime s[86:87]\n\t" \
  "s_nop 4\n\t" \
  "s_memrealtime s[60:61]\n\t" \
  "s_nop 4\n\t" \
  "s_memrealtime s[62:63]\n\t" \
  "s_nop 4\n\t" \
  "s_memrealtime s[64:65]\n\t" \
  "s_nop 4\n\t" \
  "s_memrealtime s[66:67]\n\t" \
  "s_nop 4\n\t" \
  "s_memrealtime s[68:69]\n\t" \
  "s_mov_b32 s79, 0\n\t" \
  "s_waitcnt lgkmcnt(0)\n\t" \
  "s_cmp_eq_u32 s86, s60\n\t" \
  "s_addc_u32 s79, s79, 0\n\t" \
  "s_cmp_eq_u32 s86, s62\n\t" \
  "s_addc_u32 s79, s79, 0\n\t" \
  "s_cmp_eq_u32 s86, s64\n\t" \
  "s_addc_u32 s79, s79, 0\n\t" \
  "s_cmp_eq_u32 s86, s66\n\t" \
  "s_addc_u32 s79, s79, 0\n\t" \
  "s_cmp_eq_u32 s86, s68\n\t" \
  "s_addc_u32 s79, s79, 0\n\t" \
  "s_mul_i32 s79, s79, 24\n\t" \
  "s_add_i32 s79, s79, vcc_lo\n\t" \
  "s_lshl4_add_u32 s80, s86, 64\n\t" \
  "s_sub_i32 s86, s79, s41\n\t" \
  "s_sub_i32 s87, s80, s43\n\t" \
  "s_cmp_lt_u32 s86, 4000\n\t" \
  "s_cbranch_scc1 21f\n\t" \
  "v_cvt_f32_u32 v165, s86\n\t" \
  "v_cvt_f32_u32 v164, s87\n\t" \
  "v_rcp_f32 v165, v165\n\t" \
  "s_nop 0\n\t" \
  "v_mul_f32 v165, v165, v164\n\t" \
  "v_max_f32 v165, 0x3f028f5c, v165\n\t" \
  "v_min_f32 v165, 0x3ffeb852, v165\n\t" \
  "v_rcp_f32 v164, v165\n\t" \
  "v_mul_f32 v165, 0x4f000000, v165\n\t" \
  "v_mul_f32 v164, 0x47800000, v164\n\t" \
  "v_cvt_u32_f32 v165, v165\n\t" \
  "v_cvt_u32_f32 v164, v164\n\t" \
  "s_nop 0\n\t" \
  "v_readfirstlane_b32 s45, v165\n\t" \
  "v_readfirstlane_b32 s44, v164\n\t" \
  "21:\n\t" \
  "s_mov_b32 s41, s79\n\t" \
  "s_mov_b32 s43, s80\n\t" \
  "s_lshl_b32 s83, s43, 8\n\t" \
  "s_mov_b32 s84, 610\n\t" \
  "s_mov_b32 exec_hi, 0\n\t" \
  "s_waitcnt vmcnt(0)\n\t" \
  "v_cmp_ne_u32_sdwa vcc, v184, s85 src0_sel:BYTE_0 src1_sel:DWORD\n\t" \
  "v_subrev_u32 v184, s83, v184\n\t" \
  "v_ashrrev_i32 v184, 8, v184\n\t" \
  "s_cbranch_vccnz 22f\n\t" \
  "s_nop 0\n\t" \
  "v_max_i32_dpp v184, v184, v184 quad_perm:[1,0,3,2] row_mask:0xf bank_mask:0xf\n\t" \
  "s_nop 1\n\t" \
  "v_max_i32_dpp v184, v184, v184 quad_perm:[2,3,0,1] row_mask:0xf bank_mask:0xf\n\t" \
  "s_nop 1\n\t" \
  "v_max_i32_dpp v184, v184, v184 row_half_mirror row_mask:0xf bank_mask:0xf\n\t" \
  "s_nop 1\n\t" \
  "v_max_i32_dpp v184, v184, v184 row_mirror row_mask:0xf bank_mask:0xf\n\t" \
  "s_nop 0\n\t" \
  "v_readlane_b32 s81, v184, 0\n\t" \
  "v_readlane_b32 s82, v184, 16\n\t" \
  "s_max_i32 s81, s81, s82\n\t" \
  "s_add_i32 s76, s81, s43\n\t" \
  "s_add_i32 s81, s81, s53\n\t" \
  "s_max_i32 s81, s81, 0\n\t" \
  "s_min_i32 s81, s81, 20000\n\t" \
  "s_mul_i32 s81, s81, s44\n\t" \
  "s_lshr_b32 s81, s81, 16\n\t" \
  "s_add_i32 s81, s81, -900\n\t" \
  "24:\n\t" \
  "s_add_i32 s81, s81, s41\n\t" \
  ".p2align 6\n\t" \
  "s_nop 0\n\t" \
  "s_nop 0\n\t" \
  "s_nop 0\n\t" \
  "s_nop 0\n\t" \
  "s_nop 0\n\t" \
  "s_nop 0\n\t" \
  "s_nop 0\n\t" \
  "s_nop 0\n\t" \
  "1:\n\t" \
  "s_memtime vcc\n\t" \
  "s_waitcnt lgkmcnt(0)\n\t" \
  "s_sub_i32 s79, s81, vcc_lo\n\t" \
  "s_cmp_gt_i32 s79, 128\n\t" \
  "s_cbranch_scc1 1b\n\t" \
  ".p2align 4\n\t" \
  "2:\n\t" \
  "s_sub_i32 s79, s79, 33\n\t" \
  "s_cmp_gt_i32 s79, 56\n\t" \
  "s_cbranch_scc1 2b\n\t" \
  "buffer_load_dword v187, v200, %[rh], s78 offen sc1\n\t" \
  "s_memtime vcc\n\t" \
  "s_memrealtime s[86:87]\n\t" \
  "s_nop 4\n\t" \
  "s_memrealtime s[60:61]\n\t" \
  "s_nop 4\n\t" \
  "s_memrealtime s[62:63]\n\t" \
  "s_nop 4\n\t" \
  "s_memrealtime s[64:65]\n\t" \
  "s_nop 4\n\t" \
  "s_memrealtime s[66:67]\n\t" \
  "s_nop 4\n\t" \
  "s_memrealtime s[68:69]\n\t" \
  "s_mov_b32 s79, 0\n\t" \
  "s_waitcnt lgkmcnt(0)\n\t" \
  "s_cmp_eq_u32 s86, s60\n\t" \
  "s_addc_u32 s79, s79, 0\n\t" \
  "s_cmp_eq_u32 s86, s62\n\t" \
  "s_addc_u32 s79, s79, 0\n\t" \
  "s_cmp_eq_u32 s86, s64\n\t" \
  "s_addc_u32 s79, s79, 0\n\t" \
  "s_cmp_eq_u32 s86, s66\n\t" \
  "s_addc_u32 s79, s79, 0\n\t" \
  "s_cmp_eq_u32 s86, s68\n\t" \
  "s_addc_u32 s79, s79, 0\n\t" \
  "s_mul_i32 s79, s79, 24\n\t" \
  "s_add_i32 s79, s79, vcc_lo\n\t" \
  "s_lshl4_add_u32 s80, s86, 64\n\t" \
  "s_mov_b32 s41, s79\n\t" \
  "s_mov_b32 s43, s80\n\t" \
  "s_lshl_b32 s83, s43, 8\n\t" \
  "s_add_i32 s98, s40, 2\n\t" \
  "v_cmp_gt_i32 vcc, s98, v186\n\t" \
  "s_and_b32 s54, s98, 15\n\t" \
  "s_lshl_b32 s54, s54, 22\n\t" \
  "s_add_i32 s54, s54, s51\n\t" \
  "s_lshr_b32 s80, s54, 21\n\t" \
  "s_bitcmp0_b64 %[sel], s80\n\t" \
  "s_cselect_b32 s80, 0x2000, 0\n\t" \
  "s_add_i32 s54, s54, s80\n\t" \
  "s_cmp_eq_u64 vcc, 0\n\t" \
  "s_cselect_b32 s96, 0x70000000, 0\n\t" \
  "s_cselect_b32 s98, 0, s98\n\t" \
  "13:\n\t" \
  "s_waitcnt vmcnt(0)\n\t" \
  "s_memtime s[80:81]\n\t" \
  "v_cmp_ne_u32_sdwa vcc, v187, s40 src0_sel:BYTE_0 src1_sel:DWORD\n\t" \
  "v_subrev_u32 v184, s83, v187\n\t" \
  "v_ashrrev_i32 v184, 8, v184\n\t" \
  "s_nop 1\n\t" \
  "v_max_i32_dpp v184, v184, v184 quad_perm:[1,0,3,2] row_mask:0xf bank_mask:0xf\n\t" \
  "s_nop 1\n\t" \
  "v_max_i32_dpp v184, v184, v184 quad_perm:[2,3,0,1] row_mask:0xf bank_mask:0xf\n\t" \
  "s_cbranch_vccnz 14f\n\t" \
  "15:\n\t" \
  "s_nop 0\n\t" \
  "v_max_i32_dpp v184, v184, v184 row_half_mirror row_mask:0xf bank_mask:0xf\n\t" \
  "s_nop 1\n\t" \
  "v_max_i32_dpp v184, v184, v184 row_mirror row_mask:0xf bank_mask:0xf\n\t" \
  "s_mov_b64 exec, -1\n\t" \
  "v_readlane_b32 s57, v184, 0\n\t" \
  "v_readlane_b32 s82, v184, 16\n\t" \
  "s_waitcnt lgkmcnt(0)\n\t" \
  "s_sub_i32 s79, s41, s80\n\t" \
  "s_max_i32 s57, s57, s82\n\t" \
  "s_add_i32 s81, s57, 212\n\t" \
  "s_max_i32 s81, s81, 0\n\t" \
  "s_min_i32 s81, s81, 20000\n\t" \
  "s_mul_i32 s81, s81, s44\n\t" \
  "s_lshr_b32 s81, s81, 16\n\t" \
  "s_add_i32 s79, s79, s81\n\t" \
  "s_add_i32 s79, s79, -330\n\t" \
  ".p2align 4\n\t" \
  "3:\n\t" \
  "s_sub_i32 s79, s79, 33\n\t" \
  "s_cmp_gt_i32 s79, 56\n\t" \
  "s_cbranch_scc1 3b\n\t" \
  "buffer_load_dwordx4 v[72:75], v192, %[ru], s88 offen offset:0 sc1\n\t" \
  "s_nop 4\n\t" \
  "buffer_load_dwordx4 v[76:79], v192, %[ru], s88 offen offset:1024 sc1\n\t" \
  "s_nop 4\n\t" \
  "s_add_i32 s79, s79, 12\n\t" \
  ".p2align 4\n\t" \
  "4:\n\t" \
  "s_sub_i32 s79, s79, 33\n\t" \
  "s_cmp_gt_i32 s79, 56\n\t" \
  "s_cbranch_scc1 4b\n\t" \
  "buffer_load_dwordx4 v[80:83], v192, %[ru], s89 offen offset:0 sc1\n\t" \
  "s_nop 4\n\t" \
  "buffer_load_dwordx4 v[84:87], v192, %[ru], s89 offen offset:1024 sc1\n\t" \
  "s_nop 4\n\t" \
  "s_add_i32 s79, s79, 22\n\t" \
  ".p2align 4\n\t" \
  "5:\n\t" \
  "s_sub_i32 s79, s79, 33\n\t" \
  "s_cmp_gt_i32 s79, 56\n\t" \
  "s_cbranch_scc1 5b\n\t" \
  "buffer_load_dwordx4 v[88:91], v192, %[ru], s90 offen offset:0 sc1\n\t" \
  "buffer_load_dwordx4 v[92:95], v192, %[ru], s90 offen offset:1024 sc1\n\t" \
  "s_add_i32 s79, s79, 122\n\t" \
  ".p2align 4\n\t" \
  "9:\n\t" \
  "s_sub_i32 s79, s79, 33\n\t" \
  "s_cmp_gt_i32 s79, 56\n\t" \
  "s_cbranch_scc1 9b\n\t" \
  "buffer_load_dwordx4 v[104:107], v194, %[rh], s92 offen sc1\n\t" \
  "s_nop 4\n\t" \
  "buffer_load_dwordx4 v[108:111], v194, %[rh], s93 offen sc1\n\t" \
  "s_nop 4\n\t" \
  "buffer_load_dwordx4 v[112:115], v194, %[rh], s94 offen sc1\n\t" \
  "s_nop 4\n\t" \
  "buffer_load_dwordx4 v[116:119], v194, %[rh], s95 offen sc1\n\t" \
  "s_nop 4\n\t" \
  "buffer_load_dwordx4 v[96:99], v192, %[ru], s91 offen offset:0 sc1\n\t" \
  "s_nop 4\n\t" \
  "buffer_load_dwordx4 v[100:103], v192, %[ru], s91 offen offset:1024 sc1\n\t" \
  "s_nop 4\n\t" \
  "s_mov_b64 exec, 0xffff\n\t" \
  "buffer_load_dword v185, v203, %[ru], s96 offen sc1\n\t" \
  "s_mov_b64 exec, -1\n\t" \
  "10:\n\t" \
  "s_waitcnt vmcnt(6)\n\t" \
  "v_mfma_f32_16x16x32_bf16 v[136:139], v[104:107], v[72:75], 0\n\t" \
  "v_mfma_f32_16x16x32_bf16 v[140:143], v[104:107], v[76:79], 0\n\t" \
  "s_waitcnt vmcnt(5)\n\t" \
  "v_mfma_f32_16x16x32_bf16 v[136:139], v[108:111], v[80:83], v[136:139]\n\t" \
  "v_mfma_f32_16x16x32_bf16 v[140:143], v[108:111], v[84:87], v[140:143]\n\t" \
  "s_waitcnt vmcnt(4)\n\t" \
  "v_mfma_f32_16x16x32_bf16 v[136:139], v[112:115], v[88:91], v[136:139]\n\t" \
  "v_mfma_f32_16x16x32_bf16 v[140:143], v[112:115], v[92:95], v[140:143]\n\t" \
  "s_waitcnt vmcnt(2)\n\t" \
  "v_mfma_f32_16x16x32_bf16 v[136:139], v[116:119], v[96:99], v[136:139]\n\t" \
  "s_waitcnt vmcnt(1)\n\t" \
  "v_mfma_f32_16x16x32_bf16 v[140:143], v[116:119], v[100:103], v[140:143]\n\t" \
  "s_barrier\n\t" \
  "ds_read2st64_b64 v[144:147], v195 offset1:1\n\t" \
  "s_nop 1\n\t" \
  "v_permlane32_swap_b32 v136, v138\n\t" \
  "v_permlane32_swap_b32 v137, v139\n\t" \
  "s_nop 1\n\t" \
  "v_permlane32_swap_b32 v140, v142\n\t" \
  "v_permlane32_swap_b32 v141, v143\n\t" \
  "v_pk_add_f32 v[136:137], v[136:137], v[142:143]\n\t" \
  "s_waitcnt lgkmcnt(0)\n\t" \
  "v_pk_add_f32 v[136:137], v[136:137], v[144:145]\n\t" \
  "v_pk_add_f32 v[136:137], v[136:137], v[146:147]\n\t" \
  "v_cvt_pk_bf16_f32 v148, v136, v137\n\t" \
  "s_nop 1\n\t" \
  "v_mov_b32_dpp v149, v148 row_half_mirror row_mask:0xf bank_mask:0xf\n\t" \
  "v_perm_b32 v150, v148, v149, v205\n\t" \
  "v_cmpx_o_f16_sdwa vcc, v150, v150 src0_sel:WORD_0 src1_sel:WORD_1\n\t" \
  "6:\n\t" \
  "buffer_store_dword v150, v201, %[rh], s97 offen sc1\n\t" \
  "s_memtime s[82:83]\n\t" \
  "s_mov_b64 exec, -1\n\t" \
  "s_cmp_eq_u64 vcc, -1\n\t" \
  "s_cbranch_scc0 8f\n\t" \
  "v_mov_b32 v151, 0\n\t" \
  "5:\n\t" \
  "ds_write_b32 v206, v151\n\t" \
  "s_mov_b64 exec, 0xffff\n\t" \
  "s_waitcnt vmcnt(1)\n\t" \
  "v_cmp_gt_i32 vcc, s98, v185\n\t" \
  "s_mov_b64 exec, -1\n\t" \
  "s_cmp_eq_u64 vcc, 0\n\t" \
  "s_cselect_b32 s54, s54, 0x70000000\n\t" \
  "buffer_load_dwordx4 v[0:3], v210, %[ru], s54 offen offset:0 sc1\n\t" \
  "buffer_load_dwordx4 v[4:7], v210, %[ru], s54 offen offset:1024 sc1\n\t" \
  "s_add_i32 s54, s54, 0x20000\n\t" \
  "buffer_load_dwordx4 v[8:11], v210, %[ru], s54 offen offset:0 sc1\n\t" \
  "buffer_load_dwordx4 v[12:15], v210, %[ru], s54 offen offset:1024 sc1\n\t" \
  "s_add_i32 s54, s54, 0x20000\n\t" \
  "s_waitcnt lgkmcnt(0)\n\t" \
  "s_mov_b32 s56, s82\n\t" \
  "s_barrier\n\t" \
  "ds_read2_b32 v[148:149], v207 offset1:8\n\t" \
  "s_waitcnt lgkmcnt(0)\n\t" \
  "v_or_b32 v148, v148, v149\n\t" \
  "v_cmp_ne_u32 vcc, 0, v148\n\t" \
  "s_cbranch_vccnz 12f\n\t" \
  "s_and_b32 s58, s58, 4\n\t" \
  "s_add_i32 s80, s57, s43\n\t" \
  "s_sub_i32 s80, s80, s76\n\t" \
  "s_add_i32 s53, s53, -12\n\t" \
  "s_max_i32 s80, s80, s53\n\t" \
  "s_add_i32 s53, s53, 24\n\t" \
  "s_min_i32 s53, s53, s80\n\t" \
  "s_cmp_lt_u32 s54, 0x70000000\n\t" \
  "s_cbranch_scc1 48f\n\t" \
  "25:\n\t" \
  "s_add_i32 s82, s40, 2\n\t" \
  "s_min_i32 s83, s82, 63\n\t" \
  "s_mov_b64 exec, 0xffff\n\t" \
  "v_cmp_gt_i32 vcc, s83, v186\n\t" \
  "s_mov_b64 exec, -1\n\t" \
  "s_and_b32 s54, s82, 15\n\t" \
  "s_lshl_b32 s54, s54, 22\n\t" \
  "s_add_i32 s54, s54, s51\n\t" \
  "s_lshr_b32 s80, s54, 21\n\t" \
  "s_bitcmp0_b64 %[sel], s80\n\t" \
  "s_cselect_b32 s80, 0x2000, 0\n\t" \
  "s_add_i32 s54, s54, s80\n\t" \
  "s_cbranch_vccnz 27f\n\t" \
  "28:\n\t" \
  "s_cmp_ge_i32 s40, 62\n\t" \
  "s_cbranch_scc1 37f\n\t" \
  "buffer_load_dwordx4 v[0:3], v210, %[ru], s54 offen offset:0 sc1\n\t" \
  "buffer_load_dwordx4 v[4:7], v210, %[ru], s54 offen offset:1024 sc1\n\t" \
  "s_add_i32 s54, s54, 0x20000\n\t" \
  "buffer_load_dwordx4 v[8:11], v210, %[ru], s54 offen offset:0 sc1\n\t" \
  "buffer_load_dwordx4 v[12:15], v210, %[ru], s54 offen offset:1024 sc1\n\t" \
  "s_add_i32 s54, s54, 0x20000\n\t" \
  "48:\n\t" \
  "s_and_b32 s76, s40, 3\n\t" \
  "s_lshl_b32 s76, s76, 18\n\t" \
  "s_bitcmp1_b32 s58, 4\n\t" \
  "s_cbranch_scc1 29f\n\t" \
  "s_sub_i32 s80, s56, s41\n\t" \
  "s_mul_hi_u32 s80, s80, s45\n\t" \
  "s_lshl_b32 s80, s80, 1\n\t" \
  "s_sub_i32 s80, s80, s57\n\t" \
  "s_max_u32 s82, s47, s46\n\t" \
  "s_min_u32 s47, s47, s46\n\t" \
  "s_max_u32 s82, s82, s80\n\t" \
  "s_min_u32 s47, s47, s80\n\t" \
  "s_add_i32 s47, s47, 80\n\t" \
  "s_min_u32 s47, s47, s82\n\t" \
  "v_mov_b32 v188, s47\n\t" \
  "s_mov_b32 s47, s46\n\t" \
  "s_mov_b32 s46, s80\n\t" \
  "buffer_store_dword v188, v202, %[rh], s52 offen sc1\n\t" \
  "v_mov_b64 v[188:189], -1\n\t" \
  "v_mov_b64 v[190:191], -1\n\t" \
  "v_mov_b32 v187, s40\n\t" \
  "s_add_i32 s80, s40, -1\n\t" \
  "s_and_b32 s80, s80, 7\n\t" \
  "s_lshl_b32 s80, s80, 15\n\t" \
  "s_add_i32 s80, s80, s49\n\t" \
  "buffer_store_dwordx4 v[188:191], v208, %[rh], s80 offen sc1\n\t" \
  "buffer_store_dword v187, v209, %[ru], 0 offen sc1\n\t" \
  "ds_write_b128 v197, v[72:75] offset:16384\n\t" \
  "ds_write_b128 v197, v[76:79] offset:17408\n\t" \
  "ds_write_b128 v197, v[80:83] offset:18432\n\t" \
  "ds_write_b128 v197, v[84:87] offset:19456\n\t" \
  "ds_write_b128 v197, v[88:91] offset:20480\n\t" \
  "ds_write_b128 v197, v[92:95] offset:21504\n\t" \
  "ds_write_b128 v197, v[96:99] offset:22528\n\t" \
  "ds_write_b128 v197, v[100:103] offset:23552\n\t" \
  "29:\n\t" \
  "s_and_b32 s58, s58, 36\n\t" \
  "buffer_load_dwordx4 v[16:19], v210, %[ru], s54 offen offset:0 sc1\n\t" \
  "buffer_load_dwordx4 v[20:23], v210, %[ru], s54 offen offset:1024 sc1\n\t" \
  "s_add_i32 s54, s54, 0x20000\n\t" \
  "buffer_load_dwordx4 v[24:27], v210, %[ru], s54 offen offset:0 sc1\n\t" \
  "buffer_load_dwordx4 v[28:31], v210, %[ru], s54 offen offset:1024 sc1\n\t" \
  "s_add_i32 s54, s54, 0x20000\n\t" \
  "buffer_load_dwordx4 v[32:35], v210, %[ru], s54 offen offset:0 sc1\n\t" \
  "buffer_load_dwordx4 v[36:39], v210, %[ru], s54 offen offset:1024 sc1\n\t" \
  "s_add_i32 s54, s54, 0x20000\n\t" \
  "buffer_load_dwordx4 v[40:43], v210, %[ru], s54 offen offset:0 sc1\n\t" \
  "buffer_load_dwordx4 v[44:47], v210, %[ru], s54 offen offset:1024 sc1\n\t" \
  "s_add_i32 s54, s54, 0x20000\n\t" \
  "buffer_load_dwordx4 a[0:3], v211, %[rh], s76 offen offset:0 sc1\n\t" \
  "buffer_load_dwordx4 a[4:7], v211, %[rh], s76 offen offset:1024 sc1\n\t" \
  "buffer_load_dwordx4 v[48:51], v210, %[ru], s54 offen offset:0 sc1\n\t" \
  "buffer_load_dwordx4 v[52:55], v210, %[ru], s54 offen offset:1024 sc1\n\t" \
  "s_add_i32 s54, s54, 0x20000\n\t" \
  "buffer_load_dwordx4 v[56:59], v210, %[ru], s54 offen offset:0 sc1\n\t" \
  "buffer_load_dwordx4 v[60:63], v210, %[ru], s54 offen offset:1024 sc1\n\t" \
  "s_add_i32 s54, s54, 0x20000\n\t" \
  "buffer_load_dwordx4 a[8:11], v212, %[rh], s76 offen offset:0 sc1\n\t" \
  "buffer_load_dwordx4 a[12:15], v212, %[rh], s76 offen offset:1024 sc1\n\t" \
  "s_waitcnt lgkmcnt(0)\n\t" \
  "s_barrier\n\t" \
  "buffer_load_dwordx4 v[64:67], v210, %[ru], s54 offen offset:0 sc1\n\t" \
  "buffer_load_dwordx4 v[68:71], v210, %[ru], s54 offen offset:1024 sc1\n\t" \
  "s_add_i32 s54, s54, 0x20000\n\t" \
  "buffer_load_dwordx4 v[104:107], v210, %[ru], s54 offen offset:0 sc1\n\t" \
  "buffer_load_dwordx4 v[108:111], v210, %[ru], s54 offen offset:1024 sc1\n\t" \
  "s_add_i32 s54, s54, 0x20000\n\t" \
  "buffer_load_dwordx4 a[16:19], v213, %[rh], s76 offen offset:0 sc1\n\t" \
  "buffer_load_dwordx4 a[20:23], v213, %[rh], s76 offen offset:1024 sc1\n\t" \
  "buffer_load_dwordx4 v[112:115], v210, %[ru], s54 offen offset:0 sc1\n\t" \
  "buffer_load_dwordx4 v[116:119], v210, %[ru], s54 offen offset:1024 sc1\n\t" \
  "s_add_i32 s54, s54, 0x20000\n\t" \
  "buffer_load_dwordx4 v[120:123], v210, %[ru], s54 offen offset:0 sc1\n\t" \
  "buffer_load_dwordx4 v[124:127], v210, %[ru], s54 offen offset:1024 sc1\n\t" \
  "s_add_i32 s54, s54, 0x20000\n\t" \
  "buffer_load_dwordx4 a[24:27], v214, %[rh], s76 offen offset:0 sc1\n\t" \
  "buffer_load_dwordx4 a[28:31], v214, %[rh], s76 offen offset:1024 sc1\n\t" \
  "ds_read_b128 v[160:163], v193 offset:0\n\t" \
  "ds_read_b128 v[164:167], v193 offset:256\n\t" \
  "ds_read_b128 v[168:171], v193 offset:2048\n\t" \
  "ds_read_b128 v[172:175], v193 offset:2304\n\t" \
  "ds_read_b128 v[176:179], v193 offset:4096\n\t" \
  "ds_read_b128 v[180:183], v193 offset:4352\n\t" \
  "s_waitcnt lgkmcnt(2)\n\t" \
  "v_pk_add_f32 v[160:161], v[160:161], v[168:169]\n\t" \
  "v_pk_add_f32 v[162:163], v[162:163], v[170:171]\n\t" \
  "v_pk_add_f32 v[164:165], v[164:165], v[172:173]\n\t" \
  "v_pk_add_f32 v[166:167], v[166:167], v[174:175]\n\t" \
  "s_waitcnt lgkmcnt(0)\n\t" \
  "v_pk_add_f32 v[160:161], v[160:161], v[176:177]\n\t" \
  "v_pk_add_f32 v[162:163], v[162:163], v[178:179]\n\t" \
  "v_pk_add_f32 v[164:165], v[164:165], v[180:181]\n\t" \
  "v_pk_add_f32 v[166:167], v[166:167], v[182:183]\n\t" \
  "v_cvt_pk_bf16_f32 v160, v160, v161\n\t" \
  "v_cvt_pk_bf16_f32 v161, v162, v163\n\t" \
  "v_cvt_pk_bf16_f32 v162, v164, v165\n\t" \
  "v_cvt_pk_bf16_f32 v163, v166, v167\n\t" \
  "buffer_load_dwordx4 v[128:131], v210, %[ru], s54 offen offset:0 sc1\n\t" \
  "buffer_load_dwordx4 v[132:135], v210, %[ru], s54 offen offset:1024 sc1\n\t" \
  "s_add_i32 s54, s54, 0x20000\n\t" \
  "buffer_load_dwordx4 v[136:139], v210, %[ru], s54 offen offset:0 sc1\n\t" \
  "buffer_load_dwordx4 v[140:143], v210, %[ru], s54 offen offset:1024 sc1\n\t" \
  "s_add_i32 s54, s54, 0x20000\n\t" \
  "s_waitcnt vmcnt(32)\n\t" \
  "v_swap_b32 v1, v8\n\t" \
  "v_swap_b32 v3, v10\n\t" \
  "v_swap_b32 v5, v12\n\t" \
  "v_swap_b32 v7, v14\n\t" \
  "ds_write_b128 v204, v[0:3] offset:0\n\t" \
  "ds_write_b128 v204, v[8:11] offset:1024\n\t" \
  "ds_write_b128 v204, v[4:7] offset:2048\n\t" \
  "ds_write_b128 v204, v[12:15] offset:3072\n\t" \
  "buffer_load_dwordx4 v[144:147], v210, %[ru], s54 offen offset:0 sc1\n\t" \
  "buffer_load_dwordx4 v[148:151], v210, %[ru], s54 offen offset:1024 sc1\n\t" \
  "s_add_i32 s54, s54, 0x20000\n\t" \
  "buffer_load_dwordx4 v[152:155], v210, %[ru], s54 offen offset:0 sc1\n\t" \
  "buffer_load_dwordx4 v[156:159], v210, %[ru], s54 offen offset:1024 sc1\n\t" \
  "s_add_i32 s54, s54, 0x20000\n\t" \
  "s_waitcnt vmcnt(32)\n\t" \
  "v_swap_b32 v17, v24\n\t" \
  "v_swap_b32 v19, v26\n\t" \
  "v_swap_b32 v21, v28\n\t" \
  "v_swap_b32 v23, v30\n\t" \
  "ds_write_b128 v204, v[16:19] offset:4096\n\t" \
  "ds_write_b128 v204, v[24:27] offset:5120\n\t" \
  "ds_write_b128 v204, v[20:23] offset:6144\n\t" \
  "ds_write_b128 v204, v[28:31] offset:7168\n\t" \
  "s_waitcnt vmcnt(28)\n\t" \
  "v_swap_b32 v33, v40\n\t" \
  "v_swap_b32 v35, v42\n\t" \
  "v_swap_b32 v37, v44\n\t" \
  "v_swap_b32 v39, v46\n\t" \
  "ds_write_b128 v204, v[32:35] offset:8192\n\t" \
  "ds_write_b128 v204, v[40:43] offset:9216\n\t" \
  "ds_write_b128 v204, v[36:39] offset:10240\n\t" \
  "ds_write_b128 v204, v[44:47] offset:11264\n\t" \
  "s_waitcnt vmcnt(22)\n\t" \
  "v_swap_b32 v49, v56\n\t" \
  "v_swap_b32 v51, v58\n\t" \
  "v_swap_b32 v53, v60\n\t" \
  "v_swap_b32 v55, v62\n\t" \
  "ds_write_b128 v204, v[48:51] offset:12288\n\t" \
  "ds_write_b128 v204, v[56:59] offset:13312\n\t" \
  "ds_write_b128 v204, v[52:55] offset:14336\n\t" \
  "ds_write_b128 v204, v[60:63] offset:15360\n\t" \
  "s_waitcnt vmcnt(16)\n\t" \
  "v_swap_b32 v65, v104\n\t" \
  "v_swap_b32 v67, v106\n\t" \
  "v_swap_b32 v69, v108\n\t" \
  "v_swap_b32 v71, v110\n\t" \
  "ds_write_b128 v204, v[64:67] offset:16384\n\t" \
  "ds_write_b128 v204, v[104:107] offset:17408\n\t" \
  "ds_write_b128 v204, v[68:71] offset:18432\n\t" \
  "ds_write_b128 v204, v[108:111] offset:19456\n\t" \
  "s_waitcnt vmcnt(10)\n\t" \
  "v_swap_b32 v113, v120\n\t" \
  "v_swap_b32 v115, v122\n\t" \
  "v_swap_b32 v117, v124\n\t" \
  "v_swap_b32 v119, v126\n\t" \
  "ds_write_b128 v204, v[112:115] offset:20480\n\t" \
  "ds_write_b128 v204, v[120:123] offset:21504\n\t" \
  "ds_write_b128 v204, v[116:119] offset:22528\n\t" \
  "ds_write_b128 v204, v[124:127] offset:23552\n\t" \
  "s_waitcnt vmcnt(4)\n\t" \
  "v_swap_b32 v129, v136\n\t" \
  "v_swap_b32 v131, v138\n\t" \
  "v_swap_b32 v133, v140\n\t" \
  "v_swap_b32 v135, v142\n\t" \
  "ds_write_b128 v204, v[128:131] offset:24576\n\t" \
  "ds_write_b128 v204, v[136:139] offset:25600\n\t" \
  "ds_write_b128 v204, v[132:135] offset:26624\n\t" \
  "ds_write_b128 v204, v[140:143] offset:27648\n\t" \
  "s_waitcnt vmcnt(0)\n\t" \
  "v_swap_b32 v145, v152\n\t" \
  "v_swap_b32 v147, v154\n\t" \
  "v_swap_b32 v149, v156\n\t" \
  "v_swap_b32 v151, v158\n\t" \
  "ds_write_b128 v204, v[144:147] offset:28672\n\t" \
  "ds_write_b128 v204, v[152:155] offset:29696\n\t" \
  "ds_write_b128 v204, v[148:151] offset:30720\n\t" \
  "ds_write_b128 v204, v[156:159] offset:31744\n\t" \
  "s_waitcnt lgkmcnt(0)\n\t" \
  "s_cmp_lt_i32 s40, 0\n\t" \
  "s_cbranch_scc1 39f\n\t" \
  "38:\n\t" \
  "s_add_i32 s80, s40, -2\n\t" \
  "s_lshl_b32 s80, s80, 17\n\t" \
  "s_min_u32 s80, s80, 0x70000000\n\t" \
  "buffer_store_dwordx4 v[160:163], v196, %[ro], s80 offen sc1\n\t" \
  "ds_read_b128 a[32:35], v198 offset:0\n\t" \
  "ds_read_b128 a[36:39], v198 offset:1024\n\t" \
  "ds_read_b128 a[40:43], v198 offset:2048\n\t" \
  "ds_read_b128 a[44:47], v198 offset:3072\n\t" \
  "ds_read_b128 a[48:51], v198 offset:4096\n\t" \
  "ds_read_b128 a[52:55], v198 offset:5120\n\t" \
  "ds_read_b128 a[56:59], v198 offset:6144\n\t" \
  "ds_read_b128 a[60:63], v198 offset:7168\n\t" \
  "ds_read_b128 a[64:67], v198 offset:8192\n\t" \
  "ds_read_b128 a[68:71], v198 offset:9216\n\t" \
  "ds_read_b128 a[72:75], v198 offset:10240\n\t" \
  "ds_read_b128 a[76:79], v198 offset:11264\n\t" \
  "ds_read_b128 a[80:83], v198 offset:12288\n\t" \
  "ds_read_b128 a[84:87], v198 offset:13312\n\t" \
  "ds_read_b128 a[88:91], v198 offset:14336\n\t" \
  "ds_read_b128 a[92:95], v198 offset:15360\n\t" \
  "s_waitcnt vmcnt(1) lgkmcnt(0)\n\t" \
  "31:\n\t" \
  "v_mfma_f32_16x16x32_bf16 v[168:171], a[32:35], a[0:3], 0\n\t" \
  "v_mfma_f32_16x16x32_bf16 v[172:175], a[64:67], a[0:3], 0\n\t" \
  "v_mfma_f32_16x16x32_bf16 v[168:171], a[36:39], a[4:7], v[168:171]\n\t" \
  "v_mfma_f32_16x16x32_bf16 v[172:175], a[68:71], a[4:7], v[172:175]\n\t" \
  "v_mfma_f32_16x16x32_bf16 v[168:171], a[40:43], a[8:11], v[168:171]\n\t" \
  "v_mfma_f32_16x16x32_bf16 v[172:175], a[72:75], a[8:11], v[172:175]\n\t" \
  "v_mfma_f32_16x16x32_bf16 v[168:171], a[44:47], a[12:15], v[168:171]\n\t" \
  "v_mfma_f32_16x16x32_bf16 v[172:175], a[76:79], a[12:15], v[172:175]\n\t" \
  "v_mfma_f32_16x16x32_bf16 v[168:171], a[48:51], a[16:19], v[168:171]\n\t" \
  "v_mfma_f32_16x16x32_bf16 v[172:175], a[80:83], a[16:19], v[172:175]\n\t" \
  "v_mfma_f32_16x16x32_bf16 v[168:171], a[52:55], a[20:23], v[168:171]\n\t" \
  "v_mfma_f32_16x16x32_bf16 v[172:175], a[84:87], a[20:23], v[172:175]\n\t" \
  "v_mfma_f32_16x16x32_bf16 v[168:171], a[56:59], a[24:27], v[168:171]\n\t" \
  "v_mfma_f32_16x16x32_bf16 v[172:175], a[88:91], a[24:27], v[172:175]\n\t" \
  "v_mfma_f32_16x16x32_bf16 v[168:171], a[60:63], a[28:31], v[168:171]\n\t" \
  "v_mfma_f32_16x16x32_bf16 v[172:175], a[92:95], a[28:31], v[172:175]\n\t" \
  "s_nop 7\n\t" \
  "39:\n\t" \
  "s_add_i32 s40, s40, 2\n\t" \
  "v_cmp_u_f32 vcc, v168, v168\n\t" \
  "s_cbranch_vccnz 32f\n\t" \
  "33:\n\t" \
  "s_barrier\n\t" \
  "s_add_i32 s85, s40, -1\n\t" \
  "s_cmp_ge_i32 s40, 64\n\t" \
  "s_cbranch_scc1 47f\n\t" \
  "s_and_b32 s82, s85, 2\n\t" \
  "s_lshl_b32 s82, s82, 13\n\t" \
  "s_and_b32 s80, s85, 1\n\t" \
  "s_lshl_b32 s80, s80, 12\n\t" \
  "s_add_i32 s82, s82, s80\n\t" \
  "s_add_i32 s82, s82, 0x84000\n\t" \
  "s_mov_b32 exec_hi, 0\n\t" \
  "buffer_load_dword v184, v200, %[rh], s82 offen sc1\n\t" \
  "buffer_load_dword v186, v203, %[ru], 0 offen sc1\n\t" \
  "s_mov_b64 exec, -1\n\t" \
  "47:\n\t" \
  "ds_write_b128 v199, v[168:171] offset:0\n\t" \
  "ds_write_b128 v199, v[172:175] offset:1024\n\t" \
  "s_mov_b32 s59, 0\n\t" \
  "s_and_b32 s58, s58, 4\n\t" \
  "s_cmp_lt_i32 s40, 64\n\t" \
  "s_cbranch_scc1 20b\n\t" \
  "s_waitcnt lgkmcnt(0)\n\t" \
  "s_barrier\n\t" \
  "ds_read_b128 v[160:163], v193 offset:0\n\t" \
  "ds_read_b128 v[164:167], v193 offset:256\n\t" \
  "ds_read_b128 v[168:171], v193 offset:2048\n\t" \
  "ds_read_b128 v[172:175], v193 offset:2304\n\t" \
  "ds_read_b128 v[176:179], v193 offset:4096\n\t" \
  "ds_read_b128 v[180:183], v193 offset:4352\n\t" \
  "s_waitcnt lgkmcnt(2)\n\t" \
  "v_pk_add_f32 v[160:161], v[160:161], v[168:169]\n\t" \
  "v_pk_add_f32 v[162:163], v[162:163], v[170:171]\n\t" \
  "v_pk_add_f32 v[164:165], v[164:165], v[172:173]\n\t" \
  "v_pk_add_f32 v[166:167], v[166:167], v[174:175]\n\t" \
  "s_waitcnt lgkmcnt(0)\n\t" \
  "v_pk_add_f32 v[160:161], v[160:161], v[176:177]\n\t" \
  "v_pk_add_f32 v[162:163], v[162:163], v[178:179]\n\t" \
  "v_pk_add_f32 v[164:165], v[164:165], v[180:181]\n\t" \
  "v_pk_add_f32 v[166:167], v[166:167], v[182:183]\n\t" \
  "v_cvt_pk_bf16_f32 v160, v160, v161\n\t" \
  "v_cvt_pk_bf16_f32 v161, v162, v163\n\t" \
  "v_cvt_pk_bf16_f32 v162, v164, v165\n\t" \
  "v_cvt_pk_bf16_f32 v163, v166, v167\n\t" \
  "s_add_i32 s80, s40, -2\n\t" \
  "s_lshl_b32 s80, s80, 17\n\t" \
  "s_min_u32 s80, s80, 0x70000000\n\t" \
  "buffer_store_dwordx4 v[160:163], v196, %[ro], s80 offen sc1\n\t" \
  "s_mov_b32 s58, 64\n\t" \
  "s_branch 7f\n\t" \
  "22:\n\t" \
  "s_mov_b32 s81, 0\n\t" \
  "s_cmp_eq_u32 s40, 0\n\t" \
  "s_cbranch_scc1 24b\n\t" \
  "s_bitcmp1_b32 s58, 2\n\t" \
  "s_cbranch_scc1 24b\n\t" \
  "s_mov_b64 exec, -1\n\t" \
  "s_sleep 10\n\t" \
  "s_add_u32 s59, s59, 0x10000\n\t" \
  "s_cmp_lt_u32 s59, 0x400000\n\t" \
  "s_cbranch_scc1 19b\n\t" \
  "s_or_b32 s58, s58, 1\n\t" \
  "s_branch 7f\n\t" \
  "14:\n\t" \
  "v_mov_b32 v184, 0\n\t" \
  "s_bitcmp1_b32 s58, 2\n\t" \
  "s_nop 0\n\t" \
  "s_cbranch_scc1 15b\n\t" \
  "s_mov_b32 s84, 910\n\t" \
  "s_add_u32 s59, s59, 0x10000\n\t" \
  "s_cmp_lt_u32 s59, 0x400000\n\t" \
  "s_mov_b64 exec, vcc\n\t" \
  "buffer_load_dword v187, v200, %[rh], s78 offen sc1\n\t" \
  "s_mov_b32 exec_lo, -1\n\t" \
  "s_cbranch_scc1 13b\n\t" \
  "s_mov_b64 exec, -1\n\t" \
  "s_or_b32 s58, s58, 1\n\t" \
  "s_branch 7f\n\t" \
  "27:\n\t" \
  "s_bitcmp1_b32 s58, 2\n\t" \
  "s_cbranch_scc1 28b\n\t" \
  "s_bitcmp1_b32 s58, 4\n\t" \
  "s_cbranch_scc1 23f\n\t" \
  "s_sub_i32 s80, s56, s41\n\t" \
  "s_mul_hi_u32 s80, s80, s45\n\t" \
  "s_lshl_b32 s80, s80, 1\n\t" \
  "s_sub_i32 s80, s80, s57\n\t" \
  "s_max_u32 s82, s47, s46\n\t" \
  "s_min_u32 s47, s47, s46\n\t" \
  "s_max_u32 s82, s82, s80\n\t" \
  "s_min_u32 s47, s47, s80\n\t" \
  "s_add_i32 s47, s47, 80\n\t" \
  "s_min_u32 s47, s47, s82\n\t" \
  "v_mov_b32 v188, s47\n\t" \
  "s_mov_b32 s47, s46\n\t" \
  "s_mov_b32 s46, s80\n\t" \
  "buffer_store_dword v188, v202, %[rh], s52 offen sc1\n\t" \
  "v_mov_b64 v[188:189], -1\n\t" \
  "v_mov_b64 v[190:191], -1\n\t" \
  "v_mov_b32 v187, s40\n\t" \
  "s_add_i32 s80, s40, -1\n\t" \
  "s_and_b32 s80, s80, 7\n\t" \
  "s_lshl_b32 s80, s80, 15\n\t" \
  "s_add_i32 s80, s80, s49\n\t" \
  "buffer_store_dwordx4 v[188:191], v208, %[rh], s80 offen sc1\n\t" \
  "buffer_store_dword v187, v209, %[ru], 0 offen sc1\n\t" \
  "ds_write_b128 v197, v[72:75] offset:16384\n\t" \
  "ds_write_b128 v197, v[76:79] offset:17408\n\t" \
  "ds_write_b128 v197, v[80:83] offset:18432\n\t" \
  "ds_write_b128 v197, v[84:87] offset:19456\n\t" \
  "ds_write_b128 v197, v[88:91] offset:20480\n\t" \
  "ds_write_b128 v197, v[92:95] offset:21504\n\t" \
  "ds_write_b128 v197, v[96:99] offset:22528\n\t" \
  "ds_write_b128 v197, v[100:103] offset:23552\n\t" \
  "s_or_b32 s58, s58, 16\n\t" \
  "23:\n\t" \
  "s_add_u32 s59, s59, 0x1000000\n\t" \
  "s_cmp_lt_u32 s59, 0x40000000\n\t" \
  "s_cbranch_scc1 26f\n\t" \
  "s_or_b32 s58, s58, 1\n\t" \
  "s_branch 7f\n\t" \
  "26:\n\t" \
  "buffer_load_dword v186, v203, %[ru], 0 offen sc1\n\t" \
  "s_waitcnt vmcnt(0)\n\t" \
  "s_branch 25b\n\t" \
  "37:\n\t" \
  "s_and_b32 s76, s40, 3\n\t" \
  "s_lshl_b32 s76, s76, 18\n\t" \
  "buffer_load_dwordx4 a[0:3], v211, %[rh], s76 offen offset:0 sc1\n\t" \
  "buffer_load_dwordx4 a[4:7], v211, %[rh], s76 offen offset:1024 sc1\n\t" \
  "buffer_load_dwordx4 a[8:11], v212, %[rh], s76 offen offset:0 sc1\n\t" \
  "buffer_load_dwordx4 a[12:15], v212, %[rh], s76 offen offset:1024 sc1\n\t" \
  "buffer_load_dwordx4 a[16:19], v213, %[rh], s76 offen offset:0 sc1\n\t" \
  "buffer_load_dwordx4 a[20:23], v213, %[rh], s76 offen offset:1024 sc1\n\t" \
  "buffer_load_dwordx4 a[24:27], v214, %[rh], s76 offen offset:0 sc1\n\t" \
  "buffer_load_dwordx4 a[28:31], v214, %[rh], s76 offen offset:1024 sc1\n\t" \
  "s_bitcmp1_b32 s58, 4\n\t" \
  "s_cbranch_scc1 29f\n\t" \
  "s_sub_i32 s80, s56, s41\n\t" \
  "s_mul_hi_u32 s80, s80, s45\n\t" \
  "s_lshl_b32 s80, s80, 1\n\t" \
  "s_sub_i32 s80, s80, s57\n\t" \
  "s_max_u32 s82, s47, s46\n\t" \
  "s_min_u32 s47, s47, s46\n\t" \
  "s_max_u32 s82, s82, s80\n\t" \
  "s_min_u32 s47, s47, s80\n\t" \
  "s_add_i32 s47, s47, 80\n\t" \
  "s_min_u32 s47, s47, s82\n\t" \
  "v_mov_b32 v188, s47\n\t" \
  "s_mov_b32 s47, s46\n\t" \
  "s_mov_b32 s46, s80\n\t" \
  "buffer_store_dword v188, v202, %[rh], s52 offen sc1\n\t" \
  "v_mov_b64 v[188:189], -1\n\t" \
  "v_mov_b64 v[190:191], -1\n\t" \
  "v_mov_b32 v187, s40\n\t" \
  "s_add_i32 s80, s40, -1\n\t" \
  "s_and_b32 s80, s80, 7\n\t" \
  "s_lshl_b32 s80, s80, 15\n\t" \
  "s_add_i32 s80, s80, s49\n\t" \
  "buffer_store_dwordx4 v[188:191], v208, %[rh], s80 offen sc1\n\t" \
  "buffer_store_dword v187, v209, %[ru], 0 offen sc1\n\t" \
  "ds_write_b128 v197, v[72:75] offset:16384\n\t" \
  "ds_write_b128 v197, v[76:79] offset:17408\n\t" \
  "ds_write_b128 v197, v[80:83] offset:18432\n\t" \
  "ds_write_b128 v197, v[84:87] offset:19456\n\t" \
  "ds_write_b128 v197, v[88:91] offset:20480\n\t" \
  "ds_write_b128 v197, v[92:95] offset:21504\n\t" \
  "ds_write_b128 v197, v[96:99] offset:22528\n\t" \
  "ds_write_b128 v197, v[100:103] offset:23552\n\t" \
  "29:\n\t" \
  "s_and_b32 s58, s58, 36\n\t" \
  "s_waitcnt lgkmcnt(0)\n\t" \
  "s_barrier\n\t" \
  "ds_read_b128 v[160:163], v193 offset:0\n\t" \
  "ds_read_b128 v[164:167], v193 offset:256\n\t" \
  "ds_read_b128 v[168:171], v193 offset:2048\n\t" \
  "ds_read_b128 v[172:175], v193 offset:2304\n\t" \
  "ds_read_b128 v[176:179], v193 offset:4096\n\t" \
  "ds_read_b128 v[180:183], v193 offset:4352\n\t" \
  "s_waitcnt lgkmcnt(2)\n\t" \
  "v_pk_add_f32 v[160:161], v[160:161], v[168:169]\n\t" \
  "v_pk_add_f32 v[162:163], v[162:163], v[170:171]\n\t" \
  "v_pk_add_f32 v[164:165], v[164:165], v[172:173]\n\t" \
  "v_pk_add_f32 v[166:167], v[166:167], v[174:175]\n\t" \
  "s_waitcnt lgkmcnt(0)\n\t" \
  "v_pk_add_f32 v[160:161], v[160:161], v[176:177]\n\t" \
  "v_pk_add_f32 v[162:163], v[162:163], v[178:179]\n\t" \
  "v_pk_add_f32 v[164:165], v[164:165], v[180:181]\n\t" \
  "v_pk_add_f32 v[166:167], v[166:167], v[182:183]\n\t" \
  "v_cvt_pk_bf16_f32 v160, v160, v161\n\t" \
  "v_cvt_pk_bf16_f32 v161, v162, v163\n\t" \
  "v_cvt_pk_bf16_f32 v162, v164, v165\n\t" \
  "v_cvt_pk_bf16_f32 v163, v166, v167\n\t" \
  "s_branch 38b\n\t" \
  "8:\n\t" \
  "s_mov_b64 vcc, -1\n\t" \
  "s_and_b32 s80, s58, 6\n\t" \
  "s_cbranch_scc1 6b\n\t" \
  "v_mov_b32 v151, 1\n\t" \
  "s_branch 5b\n\t" \
  "32:\n\t" \
  "s_and_b32 s80, s58, 36\n\t" \
  "s_cbranch_scc1 33b\n\t" \
  "s_add_i32 s40, s40, -2\n\t" \
  "34:\n\t" \
  "s_or_b32 s59, s59, 128\n\t" \
  "s_and_b32 s76, s40, 3\n\t" \
  "s_lshl_b32 s76, s76, 18\n\t" \
  "35:\n\t" \
  "buffer_load_dwordx4 a[0:3], v211, %[rh], s76 offen offset:0 sc1\n\t" \
  "buffer_load_dwordx4 a[4:7], v211, %[rh], s76 offen offset:1024 sc1\n\t" \
  "buffer_load_dwordx4 a[8:11], v212, %[rh], s76 offen offset:0 sc1\n\t" \
  "buffer_load_dwordx4 a[12:15], v212, %[rh], s76 offen offset:1024 sc1\n\t" \
  "buffer_load_dwordx4 a[16:19], v213, %[rh], s76 offen offset:0 sc1\n\t" \
  "buffer_load_dwordx4 a[20:23], v213, %[rh], s76 offen offset:1024 sc1\n\t" \
  "buffer_load_dwordx4 a[24:27], v214, %[rh], s76 offen offset:0 sc1\n\t" \
  "buffer_load_dwordx4 a[28:31], v214, %[rh], s76 offen offset:1024 sc1\n\t" \
  "s_waitcnt vmcnt(0)\n\t" \
  "s_bitcmp1_b32 s58, 2\n\t" \
  "s_cbranch_scc1 36f\n\t" \
  "v_accvgpr_read_b32 v0, a0\n\t" \
  "v_accvgpr_read_b32 v1, a1\n\t" \
  "v_accvgpr_read_b32 v2, a2\n\t" \
  "v_accvgpr_read_b32 v3, a3\n\t" \
  "v_accvgpr_read_b32 v4, a4\n\t" \
  "v_accvgpr_read_b32 v5, a5\n\t" \
  "v_accvgpr_read_b32 v6, a6\n\t" \
  "v_accvgpr_read_b32 v7, a7\n\t" \
  "v_accvgpr_read_b32 v8, a8\n\t" \
  "v_accvgpr_read_b32 v9, a9\n\t" \
  "v_accvgpr_read_b32 v10, a10\n\t" \
  "v_accvgpr_read_b32 v11, a11\n\t" \
  "v_accvgpr_read_b32 v12, a12\n\t" \
  "v_accvgpr_read_b32 v13, a13\n\t" \
  "v_accvgpr_read_b32 v14, a14\n\t" \
  "v_accvgpr_read_b32 v15, a15\n\t" \
  "v_accvgpr_read_b32 v16, a16\n\t" \
  "v_accvgpr_read_b32 v17, a17\n\t" \
  "v_accvgpr_read_b32 v18, a18\n\t" \
  "v_accvgpr_read_b32 v19, a19\n\t" \
  "v_accvgpr_read_b32 v20, a20\n\t" \
  "v_accvgpr_read_b32 v21, a21\n\t" \
  "v_accvgpr_read_b32 v22, a22\n\t" \
  "v_accvgpr_read_b32 v23, a23\n\t" \
  "v_accvgpr_read_b32 v24, a24\n\t" \
  "v_accvgpr_read_b32 v25, a25\n\t" \
  "v_accvgpr_read_b32 v26, a26\n\t" \
  "v_accvgpr_read_b32 v27, a27\n\t" \
  "v_accvgpr_read_b32 v28, a28\n\t" \
  "v_accvgpr_read_b32 v29, a29\n\t" \
  "v_accvgpr_read_b32 v30, a30\n\t" \
  "v_accvgpr_read_b32 v31, a31\n\t" \
  "v_max_u32 v148, v0, v1\n\t" \
  "v_max3_u32 v148, v148, v2, v3\n\t" \
  "v_max3_u32 v148, v148, v4, v5\n\t" \
  "v_max3_u32 v148, v148, v6, v7\n\t" \
  "v_max3_u32 v148, v148, v8, v9\n\t" \
  "v_max3_u32 v148, v148, v10, v11\n\t" \
  "v_max3_u32 v148, v148, v12, v13\n\t" \
  "v_max3_u32 v148, v148, v14, v15\n\t" \
  "v_max3_u32 v148, v148, v16, v17\n\t" \
  "v_max3_u32 v148, v148, v18, v19\n\t" \
  "v_max3_u32 v148, v148, v20, v21\n\t" \
  "v_max3_u32 v148, v148, v22, v23\n\t" \
  "v_max3_u32 v148, v148, v24, v25\n\t" \
  "v_max3_u32 v148, v148, v26, v27\n\t" \
  "v_max3_u32 v148, v148, v28, v29\n\t" \
  "v_max3_u32 v148, v148, v30, v31\n\t" \
  "v_cmp_eq_u32 vcc, -1, v148\n\t" \
  "s_cbranch_vccz 36f\n\t" \
  "s_add_u32 s59, s59, 0x1000000\n\t" \
  "s_cmp_lt_u32 s59, 0x10000000\n\t" \
  "s_cbranch_scc1 35b\n\t" \
  "s_or_b32 s58, s58, 9\n\t" \
  "s_branch 7f\n\t" \
  "36:\n\t" \
  "s_and_b32 s58, s58, 4\n\t" \
  "s_or_b32 s58, s58, 32\n\t" \
  "s_branch 31b\n\t" \
  "18:\n\t" \
  "v_mov_b32 v186, -1\n\t" \
  "s_and_b32 s80, s40, 3\n\t" \
  "s_mul_i32 s77, s80, 0xc000\n\t" \
  "s_add_i32 s77, s77, 0x40000\n\t" \
  "s_lshl_b32 s78, s80, 14\n\t" \
  "s_add_i32 s78, s78, 0x70000\n\t" \
  "s_mov_b32 s57, 0\n\t" \
  "s_add_i32 s96, s77, 0x1000\n\t" \
  "s_lshl_b32 s88, s80, 20\n\t" \
  "s_add_i32 s88, s88, s48\n\t" \
  "s_lshr_b32 s80, s88, 21\n\t" \
  "s_bitcmp1_b64 %[sel], s80\n\t" \
  "s_cselect_b32 s80, 0x2000, 0\n\t" \
  "s_add_i32 s88, s88, s80\n\t" \
  "s_xor_b32 s89, s88, 0x800\n\t" \
  "s_xor_b32 s90, s88, 0x1000\n\t" \
  "s_xor_b32 s91, s88, 0x1800\n\t" \
  "s_add_i32 s92, s77, s50\n\t" \
  "s_xor_b32 s93, s92, 0x100\n\t" \
  "s_xor_b32 s94, s92, 0x200\n\t" \
  "s_xor_b32 s95, s92, 0x300\n\t" \
  "s_add_i32 s97, s40, 1\n\t" \
  "s_and_b32 s97, s97, 7\n\t" \
  "s_lshl_b32 s97, s97, 15\n\t" \
  "s_add_i32 s97, s97, s49\n\t" \
  "12:\n\t" \
  "s_or_b32 s58, s58, 3\n\t" \
  "s_mov_b32 s54, 0x70000000\n\t" \
  "11:\n\t" \
  "buffer_load_dwordx4 v[72:75], v192, %[ru], s88 offen offset:0 sc1\n\t" \
  "buffer_load_dwordx4 v[76:79], v192, %[ru], s88 offen offset:1024 sc1\n\t" \
  "buffer_load_dwordx4 v[80:83], v192, %[ru], s89 offen offset:0 sc1\n\t" \
  "buffer_load_dwordx4 v[84:87], v192, %[ru], s89 offen offset:1024 sc1\n\t" \
  "buffer_load_dwordx4 v[88:91], v192, %[ru], s90 offen offset:0 sc1\n\t" \
  "buffer_load_dwordx4 v[92:95], v192, %[ru], s90 offen offset:1024 sc1\n\t" \
  "buffer_load_dwordx4 v[96:99], v192, %[ru], s91 offen offset:0 sc1\n\t" \
  "buffer_load_dwordx4 v[100:103], v192, %[ru], s91 offen offset:1024 sc1\n\t" \
  "buffer_load_dwordx4 v[104:107], v194, %[rh], s92 offen sc1\n\t" \
  "buffer_load_dwordx4 v[108:111], v194, %[rh], s93 offen sc1\n\t" \
  "buffer_load_dwordx4 v[112:115], v194, %[rh], s94 offen sc1\n\t" \
  "buffer_load_dwordx4 v[116:119], v194, %[rh], s95 offen sc1\n\t" \
  "s_waitcnt vmcnt(0)\n\t" \
  "s_add_u32 s59, s59, 1\n\t" \
  "s_bitcmp1_b32 s58, 2\n\t" \
  "s_cbranch_scc1 10b\n\t" \
  "v_max_u32 v148, v72, v73\n\t" \
  "v_max3_u32 v148, v148, v74, v75\n\t" \
  "v_max3_u32 v148, v148, v76, v77\n\t" \
  "v_max3_u32 v148, v148, v78, v79\n\t" \
  "v_max3_u32 v148, v148, v80, v81\n\t" \
  "v_max3_u32 v148, v148, v82, v83\n\t" \
  "v_max3_u32 v148, v148, v84, v85\n\t" \
  "v_max3_u32 v148, v148, v86, v87\n\t" \
  "v_max3_u32 v148, v148, v88, v89\n\t" \
  "v_max3_u32 v148, v148, v90, v91\n\t" \
  "v_max3_u32 v148, v148, v92, v93\n\t" \
  "v_max3_u32 v148, v148, v94, v95\n\t" \
  "v_max3_u32 v148, v148, v96, v97\n\t" \
  "v_max3_u32 v148, v148, v98, v99\n\t" \
  "v_max3_u32 v148, v148, v100, v101\n\t" \
  "v_max3_u32 v148, v148, v102, v103\n\t" \
  "v_max3_u32 v148, v148, v104, v105\n\t" \
  "v_max3_u32 v148, v148, v106, v107\n\t" \
  "v_max3_u32 v148, v148, v108, v109\n\t" \
  "v_max3_u32 v148, v148, v110, v111\n\t" \
  "v_max3_u32 v148, v148, v112, v113\n\t" \
  "v_max3_u32 v148, v148, v114, v115\n\t" \
  "v_max3_u32 v148, v148, v116, v117\n\t" \
  "v_max3_u32 v148, v148, v118, v119\n\t" \
  "v_cmp_eq_u32 vcc, -1, v148\n\t" \
  "s_cbranch_vccz 10b\n\t" \
  "s_bitcmp1_b32 s59, 6\n\t" \
  "s_cbranch_scc0 11b\n\t" \
  "7:\n\t" \
  "s_branch 92f\n\t" \
  "91:\n\t" \
  "s_mov_b32 s90, s41\n\t" \
  "s_mov_b32 s91, s43\n\t" \
  "s_mov_b32 s58, s42\n\t" \
  "s_mov_b32 s59, 0\n\t" \
  "s_bitcmp1_b32 s58, 4\n\t" \
  "s_cbranch_scc1 37f\n\t" \
  "s_bitcmp1_b32 s58, 3\n\t" \
  "s_cbranch_scc1 34f\n\t" \
  "s_bitcmp1_b32 s58, 1\n\t" \
  "s_cbranch_scc1 18f\n\t" \
  "17:\n\t" \
  "v_mov_b64 v[40:41], 0\n\t" \
  "v_mov_b64 v[42:43], 0\n\t" \
  "v_mov_b64 v[44:45], 0\n\t" \
  "v_mov_b64 v[46:47], 0\n\t" \
  "v_mov_b64 v[48:49], 0\n\t" \
  "v_mov_b64 v[50:51], 0\n\t" \
  "v_mov_b64 v[52:53], 0\n\t" \
  "v_mov_b64 v[54:55], 0\n\t" \
  "v_mov_b64 v[56:57], 0\n\t" \
  "v_mov_b64 v[58:59], 0\n\t" \
  "v_mov_b64 v[60:61], 0\n\t" \
  "v_mov_b64 v[62:63], 0\n\t" \
  "v_mov_b64 v[64:65], 0\n\t" \
  "v_mov_b64 v[66:67], 0\n\t" \
  "v_mov_b64 v[68:69], 0\n\t" \
  "v_mov_b64 v[70:71], 0\n\t" \
  "v_mov_b64 v[72:73], 0\n\t" \
  "v_mov_b64 v[74:75], 0\n\t" \
  "v_mov_b64 v[76:77], 0\n\t" \
  "v_mov_b64 v[78:79], 0\n\t" \
  "v_mov_b64 v[80:81], 0\n\t" \
  "v_mov_b64 v[82:83], 0\n\t" \
  "v_mov_b64 v[84:85], 0\n\t" \
  "v_mov_b64 v[86:87], 0\n\t" \
  "v_mov_b64 v[88:89], 0\n\t" \
  "v_mov_b64 v[90:91], 0\n\t" \
  "v_mov_b64 v[92:93], 0\n\t" \
  "v_mov_b64 v[94:95], 0\n\t" \
  "v_mov_b64 v[96:97], 0\n\t" \
  "v_mov_b64 v[98:99], 0\n\t" \
  "v_mov_b64 v[100:101], 0\n\t" \
  "v_mov_b64 v[102:103], 0\n\t" \
  "v_mov_b64 v[136:137], 0\n\t" \
  "v_mov_b64 v[138:139], 0\n\t" \
  "v_mov_b64 v[140:141], 0\n\t" \
  "v_mov_b64 v[142:143], 0\n\t" \
  "19:\n\t" \
  "s_add_i32 s85, s40, -1\n\t" \
  "s_and_b32 s82, s85, 2\n\t" \
  "s_lshl_b32 s82, s82, 13\n\t" \
  "s_and_b32 s80, s85, 1\n\t" \
  "s_lshl_b32 s80, s80, 12\n\t" \
  "s_add_i32 s82, s82, s80\n\t" \
  "s_add_i32 s82, s82, 0x84000\n\t" \
  "s_mov_b32 exec_hi, 0\n\t" \
  "buffer_load_dword v184, v200, %[rh], s82 offen sc1\n\t" \
  "buffer_load_dword v185, v201, %[rh], s52 offen sc1\n\t" \
  "s_mov_b64 exec, -1\n\t" \
  "20:\n\t" \
  "s_and_b32 s80, s40, 3\n\t" \
  "s_mul_i32 s77, s80, 0xc000\n\t" \
  "s_add_i32 s77, s77, 0x40000\n\t" \
  "s_lshl_b32 s78, s80, 14\n\t" \
  "s_add_i32 s78, s78, 0x70000\n\t" \
  "s_mov_b32 s57, 0\n\t" \
  "s_add_i32 s96, s77, 0x1000\n\t" \
  "s_and_b32 s98, s40, 2\n\t" \
  "s_lshl_b32 s98, s98, 13\n\t" \
  "s_and_b32 s80, s40, 1\n\t" \
  "s_lshl_b32 s80, s80, 12\n\t" \
  "s_add_i32 s98, s98, s80\n\t" \
  "s_add_i32 s98, s98, 0x84000\n\t" \
  "s_memtime vcc\n\t" \
  "s_memrealtime s[86:87]\n\t" \
  "s_nop 4\n\t" \
  "s_memrealtime s[60:61]\n\t" \
  "s_nop 4\n\t" \
  "s_memrealtime s[62:63]\n\t" \
  "s_nop 4\n\t" \
  "s_memrealtime s[64:65]\n\t" \
  "s_nop 4\n\t" \
  "s_memrealtime s[66:67]\n\t" \
  "s_nop 4\n\t" \
  "s_memrealtime s[68:69]\n\t" \
  "s_mov_b32 s79, 0\n\t" \
  "s_waitcnt lgkmcnt(0)\n\t" \
  "s_cmp_eq_u32 s86, s60\n\t" \
  "s_addc_u32 s79, s79, 0\n\t" \
  "s_cmp_eq_u32 s86, s62\n\t" \
  "s_addc_u32 s79, s79, 0\n\t" \
  "s_cmp_eq_u32 s86, s64\n\t" \
  "s_addc_u32 s79, s79, 0\n\t" \
  "s_cmp_eq_u32 s86, s66\n\t" \
  "s_addc_u32 s79, s79, 0\n\t" \
  "s_cmp_eq_u32 s86, s68\n\t" \
  "s_addc_u32 s79, s79, 0\n\t" \
  "s_mul_i32 s79, s79, 24\n\t" \
  "s_add_i32 s79, s79, vcc_lo\n\t" \
  "s_lshl4_add_u32 s80, s86, 64\n\t" \
  "s_mov_b32 s41, s79\n\t" \
  "s_mov_b32 s43, s80\n\t" \
  "s_lshl_b32 s83, s43, 8\n\t" \
  "s_mov_b32 s84, 610\n\t" \
  "s_mov_b32 exec_hi, 0\n\t" \
  "s_waitcnt vmcnt(0)\n\t" \
  "v_cmp_ne_u32_sdwa vcc, v184, s85 src0_sel:BYTE_0 src1_sel:DWORD\n\t" \
  "v_subrev_u32 v184, s83, v184\n\t" \
  "v_ashrrev_i32 v184, 8, v184\n\t" \
  "s_cbranch_vccnz 22f\n\t" \
  "v_max_i32_dpp v185, v185, v185 quad_perm:[1,0,3,2] row_mask:0xf bank_mask:0xf\n\t" \
  "v_max_i32_dpp v184, v184, v184 quad_perm:[1,0,3,2] row_mask:0xf bank_mask:0xf\n\t" \
  "s_nop 0\n\t" \
  "v_max_i32_dpp v185, v185, v185 quad_perm:[2,3,0,1] row_mask:0xf bank_mask:0xf\n\t" \
  "v_max_i32_dpp v184, v184, v184 quad_perm:[2,3,0,1] row_mask:0xf bank_mask:0xf\n\t" \
  "s_nop 0\n\t" \
  "v_max_i32_dpp v185, v185, v185 row_half_mirror row_mask:0xf bank_mask:0xf\n\t" \
  "v_max_i32_dpp v184, v184, v184 row_half_mirror row_mask:0xf bank_mask:0xf\n\t" \
  "v_readlane_b32 s54, v185, 0\n\t" \
  "s_nop 0\n\t" \
  "v_max_i32_dpp v184, v184, v184 row_mirror row_mask:0xf bank_mask:0xf\n\t" \
  "s_nop 0\n\t" \
  "v_readlane_b32 s81, v184, 0\n\t" \
  "v_readlane_b32 s82, v184, 16\n\t" \
  "s_max_i32 s81, s81, s82\n\t" \
  "s_add_i32 s76, s81, s43\n\t" \
  "s_add_i32 s81, s81, s53\n\t" \
  "s_max_i32 s81, s81, 0\n\t" \
  "s_min_i32 s81, s81, 20000\n\t" \
  "s_mul_i32 s81, s81, s44\n\t" \
  "s_lshr_b32 s81, s81, 16\n\t" \
  "s_add_i32 s81, s81, -1020\n\t" \
  "24:\n\t" \
  "s_add_i32 s81, s81, s41\n\t" \
  "s_min_u32 s54, s54, 1760\n\t" \
  "s_add_i32 s54, s54, 195\n\t" \
  ".p2align 6\n\t" \
  "s_nop 0\n\t" \
  "s_nop 0\n\t" \
  "s_nop 0\n\t" \
  "s_nop 0\n\t" \
  "s_nop 0\n\t" \
  "s_nop 0\n\t" \
  "s_nop 0\n\t" \
  "s_nop 0\n\t" \
  "1:\n\t" \
  "s_memtime vcc\n\t" \
  "s_waitcnt lgkmcnt(0)\n\t" \
  "s_sub_i32 s79, s81, vcc_lo\n\t" \
  "s_cmp_gt_i32 s79, 128\n\t" \
  "s_cbranch_scc1 1b\n\t" \
  ".p2align 4\n\t" \
  "2:\n\t" \
  "s_sub_i32 s79, s79, 33\n\t" \
  "s_cmp_gt_i32 s79, 56\n\t" \
  "s_cbranch_scc1 2b\n\t" \
  "buffer_load_dword v187, v200, %[rh], s78 offen sc1\n\t" \
  "s_memtime vcc\n\t" \
  "s_memrealtime s[86:87]\n\t" \
  "s_nop 4\n\t" \
  "s_memrealtime s[60:61]\n\t" \
  "s_nop 4\n\t" \
  "s_memrealtime s[62:63]\n\t" \
  "s_nop 4\n\t" \
  "s_memrealtime s[64:65]\n\t" \
  "s_nop 4\n\t" \
  "s_memrealtime s[66:67]\n\t" \
  "s_nop 4\n\t" \
  "s_memrealtime s[68:69]\n\t" \
  "s_mov_b32 s79, 0\n\t" \
  "s_waitcnt lgkmcnt(0)\n\t" \
  "s_mov_b64 exec, -1\n\t" \
  "ds_read_b128 a[0:3], v194 offset:0\n\t" \
  "ds_read_b128 a[4:7], v194 offset:1024\n\t" \
  "ds_read_b128 a[8:11], v194 offset:2048\n\t" \
  "ds_read_b128 a[12:15], v194 offset:3072\n\t" \
  "ds_read_b128 a[16:19], v194 offset:4096\n\t" \
  "ds_read_b128 a[20:23], v194 offset:5120\n\t" \
  "ds_read_b128 a[24:27], v194 offset:6144\n\t" \
  "ds_read_b128 a[28:31], v194 offset:7168\n\t" \
  "ds_read_b128 a[32:35], v194 offset:8192\n\t" \
  "ds_read_b128 a[36:39], v194 offset:9216\n\t" \
  "ds_read_b128 a[40:43], v194 offset:10240\n\t" \
  "ds_read_b128 a[44:47], v194 offset:11264\n\t" \
  "ds_read_b128 a[48:51], v194 offset:12288\n\t" \
  "ds_read_b128 a[52:55], v194 offset:13312\n\t" \
  "ds_read_b128 a[56:59], v194 offset:14336\n\t" \
  "ds_read_b128 a[60:63], v194 offset:15360\n\t" \
  "ds_read_b128 a[64:67], v194 offset:16384\n\t" \
  "ds_read_b128 a[68:71], v194 offset:17408\n\t" \
  "ds_read_b128 a[72:75], v194 offset:18432\n\t" \
  "ds_read_b128 a[76:79], v194 offset:19456\n\t" \
  "ds_read_b128 a[80:83], v194 offset:20480\n\t" \
  "ds_read_b128 a[84:87], v194 offset:21504\n\t" \
  "ds_read_b128 a[88:91], v194 offset:22528\n\t" \
  "ds_read_b128 a[92:95], v194 offset:23552\n\t" \
  "ds_read_b128 a[96:99], v194 offset:24576\n\t" \
  "ds_read_b128 a[100:103], v194 offset:25600\n\t" \
  "ds_read_b128 a[104:107], v194 offset:26624\n\t" \
  "ds_read_b128 a[108:111], v194 offset:27648\n\t" \
  "ds_read_b128 a[112:115], v194 offset:28672\n\t" \
  "ds_read_b128 a[116:119], v194 offset:29696\n\t" \
  "ds_read_b128 a[120:123], v194 offset:30720\n\t" \
  "ds_read_b128 a[124:127], v194 offset:31744\n\t" \
  "s_mov_b32 exec_hi, 0\n\t" \
  "s_cmp_eq_u32 s86, s60\n\t" \
  "s_addc_u32 s79, s79, 0\n\t" \
  "s_cmp_eq_u32 s86, s62\n\t" \
  "s_addc_u32 s79, s79, 0\n\t" \
  "s_cmp_eq_u32 s86, s64\n\t" \
  "s_addc_u32 s79, s79, 0\n\t" \
  "s_cmp_eq_u32 s86, s66\n\t" \
  "s_addc_u32 s79, s79, 0\n\t" \
  "s_cmp_eq_u32 s86, s68\n\t" \
  "s_addc_u32 s79, s79, 0\n\t" \
  "s_mul_i32 s79, s79, 24\n\t" \
  "s_add_i32 s79, s79, vcc_lo\n\t" \
  "s_lshl4_add_u32 s80, s86, 64\n\t" \
  "s_mov_b32 s41, s79\n\t" \
  "s_mov_b32 s43, s80\n\t" \
  "s_lshl_b32 s83, s43, 8\n\t" \
  "s_add_i32 s54, s54, s43\n\t" \
  "13:\n\t" \
  "s_waitcnt vmcnt(0)\n\t" \
  "s_memtime s[80:81]\n\t" \
  "v_cmp_ne_u32_sdwa vcc, v187, s40 src0_sel:BYTE_0 src1_sel:DWORD\n\t" \
  "v_subrev_u32 v184, s83, v187\n\t" \
  "v_ashrrev_i32 v184, 8, v184\n\t" \
  "s_nop 1\n\t" \
  "v_max_i32_dpp v184, v184, v184 quad_perm:[1,0,3,2] row_mask:0xf bank_mask:0xf\n\t" \
  "s_nop 1\n\t" \
  "v_max_i32_dpp v184, v184, v184 quad_perm:[2,3,0,1] row_mask:0xf bank_mask:0xf\n\t" \
  "s_cbranch_vccnz 14f\n\t" \
  "15:\n\t" \
  "s_nop 0\n\t" \
  "v_max_i32_dpp v184, v184, v184 row_half_mirror row_mask:0xf bank_mask:0xf\n\t" \
  "s_nop 1\n\t" \
  "v_max_i32_dpp v184, v184, v184 row_mirror row_mask:0xf bank_mask:0xf\n\t" \
  "s_mov_b64 exec, -1\n\t" \
  "v_readlane_b32 s57, v184, 0\n\t" \
  "v_readlane_b32 s82, v184, 16\n\t" \
  "s_waitcnt lgkmcnt(0)\n\t" \
  "s_sub_i32 s79, s41, s80\n\t" \
  "s_max_i32 s57, s57, s82\n\t" \
  "s_add_i32 s81, s57, 212\n\t" \
  "s_max_i32 s81, s81, 0\n\t" \
  "s_min_i32 s81, s81, 20000\n\t" \
  "s_mul_i32 s81, s81, s44\n\t" \
  "s_lshr_b32 s81, s81, 16\n\t" \
  "s_add_i32 s79, s79, s81\n\t" \
  "s_sub_i32 s85, s84, s79\n\t" \
  "s_max_i32 s85, s85, 0\n\t" \
  "s_mul_hi_u32 s85, s85, s45\n\t" \
  "s_lshl1_add_u32 s85, s85, s57\n\t" \
  "s_add_i32 s85, s85, s54\n\t" \
  "s_lshl_b32 s85, s85, 8\n\t" \
  "s_or_b32 s85, s85, s40\n\t" \
  "v_mov_b32 v184, s85\n\t" \
  "buffer_store_dword v184, v202, %[rh], s98 offen sc1\n\t" \
  "s_add_i32 s79, s79, -620\n\t" \
  ".p2align 4\n\t" \
  "3:\n\t" \
  "s_sub_i32 s79, s79, 33\n\t" \
  "s_cmp_gt_i32 s79, 56\n\t" \
  "s_cbranch_scc1 3b\n\t" \
  "buffer_load_dwordx4 v[0:3], v193, %[rh], s77 offen offset:0 sc1\n\t" \
  "s_nop 4\n\t" \
  "buffer_load_dwordx4 v[4:7], v193, %[rh], s77 offen offset:1024 sc1\n\t" \
  "s_nop 4\n\t" \
  "buffer_load_dwordx4 v[8:11], v193, %[rh], s77 offen offset:2048 sc1\n\t" \
  "s_nop 4\n\t" \
  "buffer_load_dwordx4 v[12:15], v193, %[rh], s77 offen offset:3072 sc1\n\t" \
  "s_nop 4\n\t" \
  "buffer_load_dwordx4 v[16:19], v193, %[rh], s96 offen offset:0 sc1\n\t" \
  "s_nop 4\n\t" \
  "buffer_load_dwordx4 v[20:23], v193, %[rh], s96 offen offset:1024 sc1\n\t" \
  "s_nop 4\n\t" \
  "buffer_load_dwordx4 v[24:27], v193, %[rh], s96 offen offset:2048 sc1\n\t" \
  "s_nop 4\n\t" \
  "buffer_load_dwordx4 v[28:31], v193, %[rh], s96 offen offset:3072 sc1\n\t" \
  "s_add_i32 s79, s79, 267\n\t" \
  ".p2align 4\n\t" \
  "4:\n\t" \
  "s_sub_i32 s79, s79, 33\n\t" \
  "s_cmp_gt_i32 s79, 56\n\t" \
  "s_cbranch_scc1 4b\n\t" \
  "buffer_load_dwordx4 v[32:35], v195, %[rh], s77 offen offset:0 sc1\n\t" \
  "buffer_load_dwordx4 v[36:39], v195, %[rh], s77 offen offset:2048 sc1\n\t" \
  "10:\n\t" \
  "s_waitcnt vmcnt(9)\n\t" \
  "v_smfmac_f32_16x16x64_bf16 v[40:43], v[0:3], a[0:7], v203\n\t" \
  "v_smfmac_f32_16x16x64_bf16 v[56:59], v[0:3], a[8:15], v203\n\t" \
  "s_waitcnt vmcnt(8)\n\t" \
  "v_smfmac_f32_16x16x64_bf16 v[44:47], v[4:7], a[16:23], v203\n\t" \
  "v_cvt_pk_bf16_f32 v40, v40, v41\n\t" \
  "v_cvt_pk_bf16_f32 v41, v42, v43\n\t" \
  "v_smfmac_f32_16x16x64_bf16 v[60:63], v[4:7], a[24:31], v203\n\t" \
  "v_cvt_pk_bf16_f32 v56, v56, v57\n\t" \
  "v_cvt_pk_bf16_f32 v57, v58, v59\n\t" \
  "s_waitcnt vmcnt(7)\n\t" \
  "v_smfmac_f32_16x16x64_bf16 v[48:51], v[8:11], a[32:39], v203\n\t" \
  "v_cvt_pk_bf16_f32 v42, v44, v45\n\t" \
  "v_cvt_pk_bf16_f32 v43, v46, v47\n\t" \
  "v_smfmac_f32_16x16x64_bf16 v[64:67], v[8:11], a[40:47], v203\n\t" \
  "v_cvt_pk_bf16_f32 v58, v60, v61\n\t" \
  "v_cvt_pk_bf16_f32 v59, v62, v63\n\t" \
  "s_waitcnt vmcnt(6)\n\t" \
  "v_smfmac_f32_16x16x64_bf16 v[52:55], v[12:15], a[48:55], v203\n\t" \
  "v_cvt_pk_bf16_f32 v44, v48, v49\n\t" \
  "v_cvt_pk_bf16_f32 v45, v50, v51\n\t" \
  "v_smfmac_f32_16x16x64_bf16 v[68:71], v[12:15], a[56:63], v203\n\t" \
  "v_cvt_pk_bf16_f32 v60, v64, v65\n\t" \
  "v_cvt_pk_bf16_f32 v61, v66, v67\n\t" \
  "s_waitcnt vmcnt(5)\n\t" \
  "v_smfmac_f32_16x16x64_bf16 v[72:75], v[16:19], a[64:71], v203\n\t" \
  "v_cvt_pk_bf16_f32 v46, v52, v53\n\t" \
  "v_cvt_pk_bf16_f32 v47, v54, v55\n\t" \
  "v_smfmac_f32_16x16x64_bf16 v[88:91], v[16:19], a[72:79], v203\n\t" \
  "v_cvt_pk_bf16_f32 v62, v68, v69\n\t" \
  "v_cvt_pk_bf16_f32 v63, v70, v71\n\t" \
  "s_waitcnt vmcnt(4)\n\t" \
  "v_smfmac_f32_16x16x64_bf16 v[76:79], v[20:23], a[80:87], v203\n\t" \
  "v_cvt_pk_bf16_f32 v72, v72, v73\n\t" \
  "v_cvt_pk_bf16_f32 v73, v74, v75\n\t" \
  "v_smfmac_f32_16x16x64_bf16 v[92:95], v[20:23], a[88:95], v203\n\t" \
  "v_cvt_pk_bf16_f32 v88, v88, v89\n\t" \
  "v_cvt_pk_bf16_f32 v89, v90, v91\n\t" \
  "s_waitcnt vmcnt(3)\n\t" \
  "v_smfmac_f32_16x16x64_bf16 v[80:83], v[24:27], a[96:103], v203\n\t" \
  "v_cvt_pk_bf16_f32 v74, v76, v77\n\t" \
  "v_cvt_pk_bf16_f32 v75, v78, v79\n\t" \
  "v_smfmac_f32_16x16x64_bf16 v[96:99], v[24:27], a[104:111], v203\n\t" \
  "v_cvt_pk_bf16_f32 v90, v92, v93\n\t" \
  "v_cvt_pk_bf16_f32 v91, v94, v95\n\t" \
  "s_waitcnt vmcnt(2)\n\t" \
  "v_smfmac_f32_16x16x64_bf16 v[84:87], v[28:31], a[112:119], v203\n\t" \
  "v_cvt_pk_bf16_f32 v76, v80, v81\n\t" \
  "v_cvt_pk_bf16_f32 v77, v82, v83\n\t" \
  "v_smfmac_f32_16x16x64_bf16 v[100:103], v[28:31], a[120:127], v203\n\t" \
  "v_cvt_pk_bf16_f32 v92, v96, v97\n\t" \
  "v_cvt_pk_bf16_f32 v93, v98, v99\n\t" \
  "s_nop 1\n\t" \
  "v_cvt_pk_bf16_f32 v78, v84, v85\n\t" \
  "v_cvt_pk_bf16_f32 v79, v86, v87\n\t" \
  "s_nop 1\n\t" \
  "v_cvt_pk_bf16_f32 v94, v100, v101\n\t" \
  "v_cvt_pk_bf16_f32 v95, v102, v103\n\t" \
  "s_waitcnt vmcnt(1)\n\t" \
  "v_smfmac_f32_16x16x64_bf16 v[136:139], v[32:35], v[40:47], v204\n\t" \
  "v_smfmac_f32_16x16x64_bf16 v[140:143], v[32:35], v[56:63], v204\n\t" \
  "s_waitcnt vmcnt(0)\n\t" \
  "v_smfmac_f32_16x16x64_bf16 v[136:139], v[36:39], v[72:79], v204\n\t" \
  "v_smfmac_f32_16x16x64_bf16 v[140:143], v[36:39], v[88:95], v204\n\t" \
  "s_nop 3\n\t" \
  "v_permlane32_swap_b32 v136, v138\n\t" \
  "v_permlane32_swap_b32 v137, v139\n\t" \
  "v_pk_add_f32 v[136:137], v[136:137], v[138:139]\n\t" \
  "s_nop 0\n\t" \
  "v_permlane32_swap_b32 v140, v142\n\t" \
  "v_permlane32_swap_b32 v141, v143\n\t" \
  "v_pk_add_f32 v[138:139], v[140:141], v[142:143]\n\t" \
  "ds_write2st64_b64 v197, v[136:137], v[138:139] offset1:2\n\t" \
  "s_waitcnt lgkmcnt(0)\n\t" \
  "s_barrier\n\t" \
  "s_sub_i32 s86, s41, s90\n\t" \
  "s_sub_i32 s87, s43, s91\n\t" \
  "s_mov_b32 s90, s41\n\t" \
  "s_mov_b32 s91, s43\n\t" \
  "s_cmp_lt_u32 s86, 4000\n\t" \
  "s_cbranch_scc1 21f\n\t" \
  "v_cvt_f32_u32 v165, s86\n\t" \
  "v_cvt_f32_u32 v164, s87\n\t" \
  "v_rcp_f32 v165, v165\n\t" \
  "s_nop 0\n\t" \
  "v_mul_f32 v165, v165, v164\n\t" \
  "v_max_f32 v165, 0x3f028f5c, v165\n\t" \
  "v_min_f32 v165, 0x3ffeb852, v165\n\t" \
  "v_rcp_f32 v164, v165\n\t" \
  "v_mul_f32 v165, 0x4f000000, v165\n\t" \
  "v_mul_f32 v164, 0x47800000, v164\n\t" \
  "v_cvt_u32_f32 v165, v165\n\t" \
  "v_cvt_u32_f32 v164, v164\n\t" \
  "s_nop 0\n\t" \
  "v_readfirstlane_b32 s45, v165\n\t" \
  "v_readfirstlane_b32 s44, v164\n\t" \
  "21:\n\t" \
  "s_barrier\n\t" \
  "ds_read2_b32 v[148:149], v207 offset1:8\n\t" \
  "s_waitcnt lgkmcnt(0)\n\t" \
  "v_or_b32 v148, v148, v149\n\t" \
  "v_cmp_ne_u32 vcc, 0, v148\n\t" \
  "s_cbranch_vccnz 12f\n\t" \
  "s_and_b32 s58, s58, 4\n\t" \
  "s_add_i32 s80, s57, s43\n\t" \
  "s_sub_i32 s80, s80, s76\n\t" \
  "s_add_i32 s53, s53, -12\n\t" \
  "s_max_i32 s80, s80, s53\n\t" \
  "s_add_i32 s53, s53, 24\n\t" \
  "s_min_i32 s53, s53, s80\n\t" \
  "s_and_b32 s76, s40, 3\n\t" \
  "s_lshl_b32 s76, s76, 18\n\t" \
  "s_add_i32 s77, s76, 0x10000\n\t" \
  "s_add_i32 s78, s76, 0x1000\n\t" \
  "s_add_i32 s79, s76, 0x11000\n\t" \
  "s_mov_b32 s97, 1\n\t" \
  "s_cmp_ge_i32 s40, 62\n\t" \
  "s_cbranch_scc1 31f\n\t" \
  "s_and_b32 s82, s40, 3\n\t" \
  "s_lshl_b32 s82, s82, 20\n\t" \
  "s_add_i32 s82, s82, s48\n\t" \
  "s_andn2_b32 s82, s82, 0x1800\n\t" \
  "s_lshr_b32 s80, s82, 21\n\t" \
  "s_bitcmp1_b64 %[sel], s80\n\t" \
  "s_cselect_b32 s80, 0x2000, 0\n\t" \
  "s_add_i32 s82, s82, s80\n\t" \
  "s_add_i32 s83, s82, 0x1000\n\t" \
  "v_mov_b64 v[188:189], -1\n\t" \
  "v_mov_b64 v[190:191], -1\n\t" \
  "s_sleep 0\n\t" \
  "buffer_store_dwordx4 v[188:191], v192, %[ru], s82 offen offset:0 sc1\n\t" \
  "buffer_store_dwordx4 v[188:191], v192, %[ru], s82 offen offset:1024 sc1\n\t" \
  "buffer_store_dwordx4 v[188:191], v192, %[ru], s82 offen offset:2048 sc1\n\t" \
  "buffer_store_dwordx4 v[188:191], v192, %[ru], s82 offen offset:3072 sc1\n\t" \
  "buffer_store_dwordx4 v[188:191], v192, %[ru], s83 offen offset:0 sc1\n\t" \
  "buffer_store_dwordx4 v[188:191], v192, %[ru], s83 offen offset:1024 sc1\n\t" \
  "buffer_store_dwordx4 v[188:191], v192, %[ru], s83 offen offset:2048 sc1\n\t" \
  "buffer_store_dwordx4 v[188:191], v192, %[ru], s83 offen offset:3072 sc1\n\t" \
  "31:\n\t" \
  "buffer_load_dwordx4 v[0:3], v210, %[rh], s76 offen offset:0 sc1\n\t" \
  "s_nop 10\n\t" \
  "buffer_load_dwordx4 v[4:7], v210, %[rh], s76 offen offset:1024 sc1\n\t" \
  "s_nop 10\n\t" \
  "buffer_load_dwordx4 v[8:11], v210, %[rh], s77 offen offset:0 sc1\n\t" \
  "s_nop 10\n\t" \
  "buffer_load_dwordx4 v[12:15], v210, %[rh], s77 offen offset:1024 sc1\n\t" \
  "s_cmp_eq_u32 s97, 0\n\t" \
  "s_cbranch_scc1 26f\n\t" \
  "s_barrier\n\t" \
  "s_mov_b32 s97, 0\n\t" \
  "26:\n\t" \
  "buffer_load_dwordx4 v[16:19], v210, %[rh], s76 offen offset:2048 sc1\n\t" \
  "s_nop 10\n\t" \
  "buffer_load_dwordx4 v[20:23], v210, %[rh], s76 offen offset:3072 sc1\n\t" \
  "s_nop 10\n\t" \
  "buffer_load_dwordx4 v[24:27], v210, %[rh], s77 offen offset:2048 sc1\n\t" \
  "s_nop 10\n\t" \
  "buffer_load_dwordx4 v[28:31], v210, %[rh], s77 offen offset:3072 sc1\n\t" \
  "s_nop 10\n\t" \
  "buffer_load_dwordx4 v[32:35], v210, %[rh], s78 offen offset:0 sc1\n\t" \
  "s_nop 10\n\t" \
  "buffer_load_dwordx4 v[36:39], v210, %[rh], s78 offen offset:1024 sc1\n\t" \
  "s_nop 10\n\t" \
  "buffer_load_dwordx4 v[40:43], v210, %[rh], s79 offen offset:0 sc1\n\t" \
  "s_nop 10\n\t" \
  "buffer_load_dwordx4 v[44:47], v210, %[rh], s79 offen offset:1024 sc1\n\t" \
  "s_nop 10\n\t" \
  "buffer_load_dwordx4 v[48:51], v210, %[rh], s78 offen offset:2048 sc1\n\t" \
  "s_nop 10\n\t" \
  "buffer_load_dwordx4 v[52:55], v210, %[rh], s78 offen offset:3072 sc1\n\t" \
  "s_nop 10\n\t" \
  "buffer_load_dwordx4 v[56:59], v210, %[rh], s79 offen offset:2048 sc1\n\t" \
  "s_nop 10\n\t" \
  "buffer_load_dwordx4 v[60:63], v210, %[rh], s79 offen offset:3072 sc1\n\t" \
  "s_nop 10\n\t" \
  "buffer_load_dwordx4 v[64:67], v211, %[rh], s76 offen offset:0 sc1\n\t" \
  "s_nop 10\n\t" \
  "buffer_load_dwordx4 v[68:71], v211, %[rh], s76 offen offset:1024 sc1\n\t" \
  "s_nop 10\n\t" \
  "buffer_load_dwordx4 v[72:75], v211, %[rh], s77 offen offset:0 sc1\n\t" \
  "s_nop 10\n\t" \
  "buffer_load_dwordx4 v[76:79], v211, %[rh], s77 offen offset:1024 sc1\n\t" \
  "s_nop 10\n\t" \
  "buffer_load_dwordx4 v[80:83], v211, %[rh], s76 offen offset:2048 sc1\n\t" \
  "s_nop 10\n\t" \
  "buffer_load_dwordx4 v[84:87], v211, %[rh], s76 offen offset:3072 sc1\n\t" \
  "s_nop 10\n\t" \
  "buffer_load_dwordx4 v[88:91], v211, %[rh], s77 offen offset:2048 sc1\n\t" \
  "s_nop 10\n\t" \
  "buffer_load_dwordx4 v[92:95], v211, %[rh], s77 offen offset:3072 sc1\n\t" \
  "s_nop 10\n\t" \
  "v_mov_b64 v[136:137], 0\n\t" \
  "v_mov_b64 v[138:139], 0\n\t" \
  "v_mov_b64 v[140:141], 0\n\t" \
  "v_mov_b64 v[142:143], 0\n\t" \
  "s_waitcnt vmcnt(22)\n\t" \
  "v_swap_b32 v1, v4\n\t" \
  "v_swap_b32 v3, v6\n\t" \
  "s_nop 1\n\t" \
  "v_mfma_f32_16x16x32_bf16 v[168:171], a[0:3], v[0:3], 0\n\t" \
  "buffer_load_dwordx4 v[96:99], v211, %[rh], s78 offen offset:0 sc1\n\t" \
  "v_mfma_f32_16x16x32_bf16 v[172:175], a[8:11], v[0:3], 0\n\t" \
  "s_waitcnt vmcnt(21)\n\t" \
  "v_swap_b32 v9, v12\n\t" \
  "v_mfma_f32_16x16x32_bf16 v[168:171], a[4:7], v[4:7], v[168:171]\n\t" \
  "v_swap_b32 v11, v14\n\t" \
  "v_mfma_f32_16x16x32_bf16 v[172:175], a[12:15], v[4:7], v[172:175]\n\t" \
  "buffer_load_dwordx4 v[100:103], v211, %[rh], s78 offen offset:1024 sc1\n\t" \
  "v_mfma_f32_16x16x32_bf16 v[176:179], a[0:3], v[8:11], 0\n\t" \
  "buffer_load_dwordx4 v[104:107], v211, %[rh], s79 offen offset:0 sc1\n\t" \
  "v_mfma_f32_16x16x32_bf16 v[180:183], a[8:11], v[8:11], 0\n\t" \
  "s_waitcnt vmcnt(21)\n\t" \
  "v_swap_b32 v17, v20\n\t" \
  "v_mfma_f32_16x16x32_bf16 v[176:179], a[4:7], v[12:15], v[176:179]\n\t" \
  "v_swap_b32 v19, v22\n\t" \
  "v_mfma_f32_16x16x32_bf16 v[180:183], a[12:15], v[12:15], v[180:183]\n\t" \
  "buffer_load_dwordx4 v[108:111], v211, %[rh], s79 offen offset:1024 sc1\n\t" \
  "v_mfma_f32_16x16x32_bf16 v[168:171], a[16:19], v[16:19], v[168:171]\n\t" \
  "buffer_load_dwordx4 v[112:115], v211, %[rh], s78 offen offset:2048 sc1\n\t" \
  "v_mfma_f32_16x16x32_bf16 v[172:175], a[24:27], v[16:19], v[172:175]\n\t" \
  "s_waitcnt vmcnt(21)\n\t" \
  "v_swap_b32 v25, v28\n\t" \
  "v_mfma_f32_16x16x32_bf16 v[168:171], a[20:23], v[20:23], v[168:171]\n\t" \
  "v_swap_b32 v27, v30\n\t" \
  "v_mfma_f32_16x16x32_bf16 v[172:175], a[28:31], v[20:23], v[172:175]\n\t" \
  "buffer_load_dwordx4 v[116:119], v211, %[rh], s78 offen offset:3072 sc1\n\t" \
  "v_mfma_f32_16x16x32_bf16 v[176:179], a[16:19], v[24:27], v[176:179]\n\t" \
  "buffer_load_dwordx4 v[120:123], v211, %[rh], s79 offen offset:2048 sc1\n\t" \
  "v_mfma_f32_16x16x32_bf16 v[180:183], a[24:27], v[24:27], v[180:183]\n\t" \
  "s_waitcnt vmcnt(21)\n\t" \
  "v_swap_b32 v33, v36\n\t" \
  "v_mfma_f32_16x16x32_bf16 v[176:179], a[20:23], v[28:31], v[176:179]\n\t" \
  "v_swap_b32 v35, v38\n\t" \
  "v_mfma_f32_16x16x32_bf16 v[180:183], a[28:31], v[28:31], v[180:183]\n\t" \
  "buffer_load_dwordx4 v[124:127], v211, %[rh], s79 offen offset:3072 sc1\n\t" \
  "v_mfma_f32_16x16x32_bf16 v[168:171], a[32:35], v[32:35], v[168:171]\n\t" \
  "v_mfma_f32_16x16x32_bf16 v[172:175], a[40:43], v[32:35], v[172:175]\n\t" \
  "s_waitcnt vmcnt(20)\n\t" \
  "v_swap_b32 v41, v44\n\t" \
  "v_mfma_f32_16x16x32_bf16 v[168:171], a[36:39], v[36:39], v[168:171]\n\t" \
  "v_swap_b32 v43, v46\n\t" \
  "v_mfma_f32_16x16x32_bf16 v[172:175], a[44:47], v[36:39], v[172:175]\n\t" \
  "s_nop 0\n\t" \
  "v_mfma_f32_16x16x32_bf16 v[176:179], a[32:35], v[40:43], v[176:179]\n\t" \
  "v_mfma_f32_16x16x32_bf16 v[180:183], a[40:43], v[40:43], v[180:183]\n\t" \
  "s_waitcnt vmcnt(18)\n\t" \
  "v_swap_b32 v49, v52\n\t" \
  "v_mfma_f32_16x16x32_bf16 v[176:179], a[36:39], v[44:47], v[176:179]\n\t" \
  "v_swap_b32 v51, v54\n\t" \
  "v_mfma_f32_16x16x32_bf16 v[180:183], a[44:47], v[44:47], v[180:183]\n\t" \
  "v_mov_b64 v[40:41], 0\n\t" \
  "v_mov_b64 v[42:43], 0\n\t" \
  "v_mfma_f32_16x16x32_bf16 v[168:171], a[48:51], v[48:51], v[168:171]\n\t" \
  "v_mov_b64 v[44:45], 0\n\t" \
  "v_mov_b64 v[46:47], 0\n\t" \
  "v_mfma_f32_16x16x32_bf16 v[172:175], a[56:59], v[48:51], v[172:175]\n\t" \
  "s_waitcnt vmcnt(16)\n\t" \
  "v_swap_b32 v57, v60\n\t" \
  "v_mfma_f32_16x16x32_bf16 v[168:171], a[52:55], v[52:55], v[168:171]\n\t" \
  "v_swap_b32 v59, v62\n\t" \
  "v_mfma_f32_16x16x32_bf16 v[172:175], a[60:63], v[52:55], v[172:175]\n\t" \
  "v_mov_b64 v[48:49], 0\n\t" \
  "v_mov_b64 v[50:51], 0\n\t" \
  "v_mfma_f32_16x16x32_bf16 v[176:179], a[48:51], v[56:59], v[176:179]\n\t" \
  "v_mov_b64 v[52:53], 0\n\t" \
  "v_mov_b64 v[54:55], 0\n\t" \
  "v_mfma_f32_16x16x32_bf16 v[180:183], a[56:59], v[56:59], v[180:183]\n\t" \
  "s_waitcnt vmcnt(14)\n\t" \
  "v_swap_b32 v65, v68\n\t" \
  "v_mfma_f32_16x16x32_bf16 v[176:179], a[52:55], v[60:63], v[176:179]\n\t" \
  "v_swap_b32 v67, v70\n\t" \
  "v_mfma_f32_16x16x32_bf16 v[180:183], a[60:63], v[60:63], v[180:183]\n\t" \
  "v_mov_b64 v[56:57], 0\n\t" \
  "v_mov_b64 v[58:59], 0\n\t" \
  "v_mfma_f32_16x16x32_bf16 v[168:171], a[64:67], v[64:67], v[168:171]\n\t" \
  "v_mov_b64 v[60:61], 0\n\t" \
  "v_mov_b64 v[62:63], 0\n\t" \
  "v_mfma_f32_16x16x32_bf16 v[172:175], a[72:75], v[64:67], v[172:175]\n\t" \
  "s_waitcnt vmcnt(12)\n\t" \
  "v_swap_b32 v73, v76\n\t" \
  "v_mfma_f32_16x16x32_bf16 v[168:171], a[68:71], v[68:71], v[168:171]\n\t" \
  "v_swap_b32 v75, v78\n\t" \
  "v_mfma_f32_16x16x32_bf16 v[172:175], a[76:79], v[68:71], v[172:175]\n\t" \
  "v_mov_b64 v[64:65], 0\n\t" \
  "v_mov_b64 v[66:67], 0\n\t" \
  "v_mfma_f32_16x16x32_bf16 v[176:179], a[64:67], v[72:75], v[176:179]\n\t" \
  "v_mov_b64 v[68:69], 0\n\t" \
  "v_mov_b64 v[70:71], 0\n\t" \
  "v_mfma_f32_16x16x32_bf16 v[180:183], a[72:75], v[72:75], v[180:183]\n\t" \
  "s_waitcnt vmcnt(10)\n\t" \
  "v_swap_b32 v81, v84\n\t" \
  "v_mfma_f32_16x16x32_bf16 v[176:179], a[68:71], v[76:79], v[176:179]\n\t" \
  "v_swap_b32 v83, v86\n\t" \
  "v_mfma_f32_16x16x32_bf16 v[180:183], a[76:79], v[76:79], v[180:183]\n\t" \
  "v_mov_b64 v[72:73], 0\n\t" \
  "v_mov_b64 v[74:75], 0\n\t" \
  "v_mfma_f32_16x16x32_bf16 v[168:171], a[80:83], v[80:83], v[168:171]\n\t" \
  "v_mov_b64 v[76:77], 0\n\t" \
  "v_mov_b64 v[78:79], 0\n\t" \
  "v_mfma_f32_16x16x32_bf16 v[172:175], a[88:91], v[80:83], v[172:175]\n\t" \
  "s_waitcnt vmcnt(8)\n\t" \
  "v_swap_b32 v89, v92\n\t" \
  "v_mfma_f32_16x16x32_bf16 v[168:171], a[84:87], v[84:87], v[168:171]\n\t" \
  "v_swap_b32 v91, v94\n\t" \
  "v_mfma_f32_16x16x32_bf16 v[172:175], a[92:95], v[84:87], v[172:175]\n\t" \
  "v_mov_b64 v[80:81], 0\n\t" \
  "v_mov_b64 v[82:83], 0\n\t" \
  "v_mfma_f32_16x16x32_bf16 v[176:179], a[80:83], v[88:91], v[176:179]\n\t" \
  "v_mov_b64 v[84:85], 0\n\t" \
  "v_mov_b64 v[86:87], 0\n\t" \
  "v_mfma_f32_16x16x32_bf16 v[180:183], a[88:91], v[88:91], v[180:183]\n\t" \
  "s_waitcnt vmcnt(6)\n\t" \
  "v_swap_b32 v97, v100\n\t" \
  "v_mfma_f32_16x16x32_bf16 v[176:179], a[84:87], v[92:95], v[176:179]\n\t" \
  "v_swap_b32 v99, v102\n\t" \
  "v_mfma_f32_16x16x32_bf16 v[180:183], a[92:95], v[92:95], v[180:183]\n\t" \
  "v_mov_b64 v[88:89], 0\n\t" \
  "v_mov_b64 v[90:91], 0\n\t" \
  "v_mfma_f32_16x16x32_bf16 v[168:171], a[96:99], v[96:99], v[168:171]\n\t" \
  "v_mov_b64 v[92:93], 0\n\t" \
  "v_mov_b64 v[94:95], 0\n\t" \
  "v_mfma_f32_16x16x32_bf16 v[172:175], a[104:107], v[96:99], v[172:175]\n\t" \
  "s_waitcnt vmcnt(4)\n\t" \
  "v_swap_b32 v105, v108\n\t" \
  "v_mfma_f32_16x16x32_bf16 v[168:171], a[100:103], v[100:103], v[168:171]\n\t" \
  "v_swap_b32 v107, v110\n\t" \
  "v_mfma_f32_16x16x32_bf16 v[172:175], a[108:111], v[100:103], v[172:175]\n\t" \
  "v_mov_b64 v[96:97], 0\n\t" \
  "v_mov_b64 v[98:99], 0\n\t" \
  "v_mfma_f32_16x16x32_bf16 v[176:179], a[96:99], v[104:107], v[176:179]\n\t" \
  "v_mov_b64 v[100:101], 0\n\t" \
  "v_mov_b64 v[102:103], 0\n\t" \
  "v_mfma_f32_16x16x32_bf16 v[180:183], a[104:107], v[104:107], v[180:183]\n\t" \
  "s_waitcnt vmcnt(2)\n\t" \
  "v_swap_b32 v113, v116\n\t" \
  "v_mfma_f32_16x16x32_bf16 v[176:179], a[100:103], v[108:111], v[176:179]\n\t" \
  "v_swap_b32 v115, v118\n\t" \
  "v_mfma_f32_16x16x32_bf16 v[180:183], a[108:111], v[108:111], v[180:183]\n\t" \
  "s_nop 0\n\t" \
  "s_add_i32 s85, s40, 1\n\t" \
  "s_and_b32 s82, s85, 2\n\t" \
  "s_lshl_b32 s82, s82, 13\n\t" \
  "s_and_b32 s80, s85, 1\n\t" \
  "s_lshl_b32 s80, s80, 12\n\t" \
  "s_add_i32 s82, s82, s80\n\t" \
  "s_add_i32 s82, s82, 0x84000\n\t" \
  "s_mov_b32 exec_hi, 0\n\t" \
  "buffer_load_dword v184, v200, %[rh], s82 offen sc1\n\t" \
  "buffer_load_dword v185, v201, %[rh], s52 offen sc1\n\t" \
  "s_mov_b64 exec, -1\n\t" \
  "v_mfma_f32_16x16x32_bf16 v[168:171], a[112:115], v[112:115], v[168:171]\n\t" \
  "v_mfma_f32_16x16x32_bf16 v[172:175], a[120:123], v[112:115], v[172:175]\n\t" \
  "s_waitcnt vmcnt(2)\n\t" \
  "v_swap_b32 v121, v124\n\t" \
  "v_mfma_f32_16x16x32_bf16 v[168:171], a[116:119], v[116:119], v[168:171]\n\t" \
  "v_swap_b32 v123, v126\n\t" \
  "v_mfma_f32_16x16x32_bf16 v[172:175], a[124:127], v[116:119], v[172:175]\n\t" \
  "s_nop 0\n\t" \
  "v_mfma_f32_16x16x32_bf16 v[176:179], a[112:115], v[120:123], v[176:179]\n\t" \
  "v_mfma_f32_16x16x32_bf16 v[180:183], a[120:123], v[120:123], v[180:183]\n\t" \
  "v_mfma_f32_16x16x32_bf16 v[176:179], a[116:119], v[124:127], v[176:179]\n\t" \
  "v_mfma_f32_16x16x32_bf16 v[180:183], a[124:127], v[124:127], v[180:183]\n\t" \
  "s_nop 6\n\t" \
  "39:\n\t" \
  "s_add_i32 s40, s40, 2\n\t" \
  "v_cmp_u_f32 vcc, v168, v176\n\t" \
  "s_cbranch_vccnz 32f\n\t" \
  "33:\n\t" \
  "s_barrier\n\t" \
  "s_add_i32 s85, s40, -1\n\t" \
  "s_cmp_ge_i32 s40, 64\n\t" \
  "s_cbranch_scc1 47f\n\t" \
  "47:\n\t" \
  "ds_write_b128 v199, v[168:171] offset:0\n\t" \
  "ds_write_b128 v199, v[172:175] offset:1024\n\t" \
  "ds_write_b128 v199, v[176:179] offset:8192\n\t" \
  "ds_write_b128 v199, v[180:183] offset:9216\n\t" \
  "s_mov_b32 s59, 0\n\t" \
  "s_and_b32 s58, s58, 4\n\t" \
  "s_cmp_lt_i32 s40, 64\n\t" \
  "s_cbranch_scc1 20b\n\t" \
  "s_waitcnt lgkmcnt(0)\n\t" \
  "s_barrier\n\t" \
  "s_mov_b32 s58, 64\n\t" \
  "s_branch 7f\n\t" \
  "22:\n\t" \
  "s_mov_b32 s81, 0\n\t" \
  "s_mov_b32 s54, s46\n\t" \
  "s_cmp_eq_u32 s40, 0\n\t" \
  "s_cbranch_scc1 24b\n\t" \
  "s_bitcmp1_b32 s58, 2\n\t" \
  "s_cbranch_scc1 24b\n\t" \
  "s_mov_b64 exec, -1\n\t" \
  "s_sleep 10\n\t" \
  "s_add_u32 s59, s59, 0x10000\n\t" \
  "s_cmp_lt_u32 s59, 0x400000\n\t" \
  "s_cbranch_scc1 19b\n\t" \
  "s_or_b32 s58, s58, 1\n\t" \
  "s_branch 7f\n\t" \
  "14:\n\t" \
  "v_mov_b32 v184, 0\n\t" \
  "s_bitcmp1_b32 s58, 2\n\t" \
  "s_nop 0\n\t" \
  "s_cbranch_scc1 15b\n\t" \
  "s_mov_b32 s84, 910\n\t" \
  "s_add_u32 s59, s59, 0x10000\n\t" \
  "s_cmp_lt_u32 s59, 0x400000\n\t" \
  "s_mov_b64 exec, vcc\n\t" \
  "buffer_load_dword v187, v200, %[rh], s78 offen sc1\n\t" \
  "s_mov_b32 exec_lo, -1\n\t" \
  "s_cbranch_scc1 13b\n\t" \
  "s_mov_b64 exec, -1\n\t" \
  "s_or_b32 s58, s58, 1\n\t" \
  "s_branch 7f\n\t" \
  "37:\n\t" \
  "s_barrier\n\t" \
  "s_barrier\n\t" \
  "s_add_i32 s40, s40, 2\n\t" \
  "s_and_b32 s58, s58, 4\n\t" \
  "s_branch 17b\n\t" \
  "32:\n\t" \
  "s_and_b32 s80, s58, 36\n\t" \
  "s_cbranch_scc1 33b\n\t" \
  "s_add_i32 s40, s40, -2\n\t" \
  "34:\n\t" \
  "s_or_b32 s59, s59, 128\n\t" \
  "s_and_b32 s76, s40, 3\n\t" \
  "s_lshl_b32 s76, s76, 18\n\t" \
  "s_add_i32 s77, s76, 0x10000\n\t" \
  "s_add_i32 s78, s76, 0x1000\n\t" \
  "s_add_i32 s79, s76, 0x11000\n\t" \
  "35:\n\t" \
  "buffer_load_dwordx4 v[0:3], v210, %[rh], s76 offen offset:0 sc1\n\t" \
  "buffer_load_dwordx4 v[4:7], v210, %[rh], s76 offen offset:1024 sc1\n\t" \
  "buffer_load_dwordx4 v[8:11], v210, %[rh], s77 offen offset:0 sc1\n\t" \
  "buffer_load_dwordx4 v[12:15], v210, %[rh], s77 offen offset:1024 sc1\n\t" \
  "buffer_load_dwordx4 v[16:19], v210, %[rh], s76 offen offset:2048 sc1\n\t" \
  "buffer_load_dwordx4 v[20:23], v210, %[rh], s76 offen offset:3072 sc1\n\t" \
  "buffer_load_dwordx4 v[24:27], v210, %[rh], s77 offen offset:2048 sc1\n\t" \
  "buffer_load_dwordx4 v[28:31], v210, %[rh], s77 offen offset:3072 sc1\n\t" \
  "buffer_load_dwordx4 v[32:35], v210, %[rh], s78 offen offset:0 sc1\n\t" \
  "buffer_load_dwordx4 v[36:39], v210, %[rh], s78 offen offset:1024 sc1\n\t" \
  "buffer_load_dwordx4 v[40:43], v210, %[rh], s79 offen offset:0 sc1\n\t" \
  "buffer_load_dwordx4 v[44:47], v210, %[rh], s79 offen offset:1024 sc1\n\t" \
  "buffer_load_dwordx4 v[48:51], v210, %[rh], s78 offen offset:2048 sc1\n\t" \
  "buffer_load_dwordx4 v[52:55], v210, %[rh], s78 offen offset:3072 sc1\n\t" \
  "buffer_load_dwordx4 v[56:59], v210, %[rh], s79 offen offset:2048 sc1\n\t" \
  "buffer_load_dwordx4 v[60:63], v210, %[rh], s79 offen offset:3072 sc1\n\t" \
  "buffer_load_dwordx4 v[64:67], v211, %[rh], s76 offen offset:0 sc1\n\t" \
  "buffer_load_dwordx4 v[68:71], v211, %[rh], s76 offen offset:1024 sc1\n\t" \
  "buffer_load_dwordx4 v[72:75], v211, %[rh], s77 offen offset:0 sc1\n\t" \
  "buffer_load_dwordx4 v[76:79], v211, %[rh], s77 offen offset:1024 sc1\n\t" \
  "buffer_load_dwordx4 v[80:83], v211, %[rh], s76 offen offset:2048 sc1\n\t" \
  "buffer_load_dwordx4 v[84:87], v211, %[rh], s76 offen offset:3072 sc1\n\t" \
  "buffer_load_dwordx4 v[88:91], v211, %[rh], s77 offen offset:2048 sc1\n\t" \
  "buffer_load_dwordx4 v[92:95], v211, %[rh], s77 offen offset:3072 sc1\n\t" \
  "buffer_load_dwordx4 v[96:99], v211, %[rh], s78 offen offset:0 sc1\n\t" \
  "buffer_load_dwordx4 v[100:103], v211, %[rh], s78 offen offset:1024 sc1\n\t" \
  "buffer_load_dwordx4 v[104:107], v211, %[rh], s79 offen offset:0 sc1\n\t" \
  "buffer_load_dwordx4 v[108:111], v211, %[rh], s79 offen offset:1024 sc1\n\t" \
  "buffer_load_dwordx4 v[112:115], v211, %[rh], s78 offen offset:2048 sc1\n\t" \
  "buffer_load_dwordx4 v[116:119], v211, %[rh], s78 offen offset:3072 sc1\n\t" \
  "buffer_load_dwordx4 v[120:123], v211, %[rh], s79 offen offset:2048 sc1\n\t" \
  "buffer_load_dwordx4 v[124:127], v211, %[rh], s79 offen offset:3072 sc1\n\t" \
  "s_waitcnt vmcnt(0)\n\t" \
  "s_bitcmp1_b32 s58, 2\n\t" \
  "s_cbranch_scc1 36f\n\t" \
  "v_max_u32 v148, v0, v1\n\t" \
  "v_max3_u32 v148, v148, v2, v3\n\t" \
  "v_max3_u32 v148, v148, v4, v5\n\t" \
  "v_max3_u32 v148, v148, v6, v7\n\t" \
  "v_max3_u32 v148, v148, v8, v9\n\t" \
  "v_max3_u32 v148, v148, v10, v11\n\t" \
  "v_max3_u32 v148, v148, v12, v13\n\t" \
  "v_max3_u32 v148, v148, v14, v15\n\t" \
  "v_max3_u32 v148, v148, v16, v17\n\t" \
  "v_max3_u32 v148, v148, v18, v19\n\t" \
  "v_max3_u32 v148, v148, v20, v21\n\t" \
  "v_max3_u32 v148, v148, v22, v23\n\t" \
  "v_max3_u32 v148, v148, v24, v25\n\t" \
  "v_max3_u32 v148, v148, v26, v27\n\t" \
  "v_max3_u32 v148, v148, v28, v29\n\t" \
  "v_max3_u32 v148, v148, v30, v31\n\t" \
  "v_max3_u32 v148, v148, v32, v33\n\t" \
  "v_max3_u32 v148, v148, v34, v35\n\t" \
  "v_max3_u32 v148, v148, v36, v37\n\t" \
  "v_max3_u32 v148, v148, v38, v39\n\t" \
  "v_max3_u32 v148, v148, v40, v41\n\t" \
  "v_max3_u32 v148, v148, v42, v43\n\t" \
  "v_max3_u32 v148, v148, v44, v45\n\t" \
  "v_max3_u32 v148, v148, v46, v47\n\t" \
  "v_max3_u32 v148, v148, v48, v49\n\t" \
  "v_max3_u32 v148, v148, v50, v51\n\t" \
  "v_max3_u32 v148, v148, v52, v53\n\t" \
  "v_max3_u32 v148, v148, v54, v55\n\t" \
  "v_max3_u32 v148, v148, v56, v57\n\t" \
  "v_max3_u32 v148, v148, v58, v59\n\t" \
  "v_max3_u32 v148, v148, v60, v61\n\t" \
  "v_max3_u32 v148, v148, v62, v63\n\t" \
  "v_max3_u32 v148, v148, v64, v65\n\t" \
  "v_max3_u32 v148, v148, v66, v67\n\t" \
  "v_max3_u32 v148, v148, v68, v69\n\t" \
  "v_max3_u32 v148, v148, v70, v71\n\t" \
  "v_max3_u32 v148, v148, v72, v73\n\t" \
  "v_max3_u32 v148, v148, v74, v75\n\t" \
  "v_max3_u32 v148, v148, v76, v77\n\t" \
  "v_max3_u32 v148, v148, v78, v79\n\t" \
  "v_max3_u32 v148, v148, v80, v81\n\t" \
  "v_max3_u32 v148, v148, v82, v83\n\t" \
  "v_max3_u32 v148, v148, v84, v85\n\t" \
  "v_max3_u32 v148, v148, v86, v87\n\t" \
  "v_max3_u32 v148, v148, v88, v89\n\t" \
  "v_max3_u32 v148, v148, v90, v91\n\t" \
  "v_max3_u32 v148, v148, v92, v93\n\t" \
  "v_max3_u32 v148, v148, v94, v95\n\t" \
  "v_max3_u32 v148, v148, v96, v97\n\t" \
  "v_max3_u32 v148, v148, v98, v99\n\t" \
  "v_max3_u32 v148, v148, v100, v101\n\t" \
  "v_max3_u32 v148, v148, v102, v103\n\t" \
  "v_max3_u32 v148, v148, v104, v105\n\t" \
  "v_max3_u32 v148, v148, v106, v107\n\t" \
  "v_max3_u32 v148, v148, v108, v109\n\t" \
  "v_max3_u32 v148, v148, v110, v111\n\t" \
  "v_max3_u32 v148, v148, v112, v113\n\t" \
  "v_max3_u32 v148, v148, v114, v115\n\t" \
  "v_max3_u32 v148, v148, v116, v117\n\t" \
  "v_max3_u32 v148, v148, v118, v119\n\t" \
  "v_max3_u32 v148, v148, v120, v121\n\t" \
  "v_max3_u32 v148, v148, v122, v123\n\t" \
  "v_max3_u32 v148, v148, v124, v125\n\t" \
  "v_max3_u32 v148, v148, v126, v127\n\t" \
  "v_cmp_eq_u32 vcc, -1, v148\n\t" \
  "s_cbranch_vccz 36f\n\t" \
  "s_add_u32 s59, s59, 0x1000000\n\t" \
  "s_cmp_lt_u32 s59, 0x10000000\n\t" \
  "s_cbranch_scc1 35b\n\t" \
  "s_or_b32 s58, s58, 9\n\t" \
  "s_branch 7f\n\t" \
  "36:\n\t" \
  "s_mov_b32 s97, 0\n\t" \
  "s_and_b32 s58, s58, 4\n\t" \
  "s_or_b32 s58, s58, 32\n\t" \
  "s_branch 31b\n\t" \
  "18:\n\t" \
  "v_mov_b32 v186, -1\n\t" \
  "s_and_b32 s80, s40, 3\n\t" \
  "s_mul_i32 s77, s80, 0xc000\n\t" \
  "s_add_i32 s77, s77, 0x40000\n\t" \
  "s_lshl_b32 s78, s80, 14\n\t" \
  "s_add_i32 s78, s78, 0x70000\n\t" \
  "s_mov_b32 s57, 0\n\t" \
  "s_add_i32 s96, s77, 0x1000\n\t" \
  "s_and_b32 s98, s40, 2\n\t" \
  "s_lshl_b32 s98, s98, 13\n\t" \
  "s_and_b32 s80, s40, 1\n\t" \
  "s_lshl_b32 s80, s80, 12\n\t" \
  "s_add_i32 s98, s98, s80\n\t" \
  "s_add_i32 s98, s98, 0x84000\n\t" \
  "12:\n\t" \
  "s_or_b32 s58, s58, 3\n\t" \
  "11:\n\t" \
  "buffer_load_dwordx4 v[0:3], v193, %[rh], s77 offen offset:0 sc1\n\t" \
  "buffer_load_dwordx4 v[4:7], v193, %[rh], s77 offen offset:1024 sc1\n\t" \
  "buffer_load_dwordx4 v[8:11], v193, %[rh], s77 offen offset:2048 sc1\n\t" \
  "buffer_load_dwordx4 v[12:15], v193, %[rh], s77 offen offset:3072 sc1\n\t" \
  "buffer_load_dwordx4 v[16:19], v193, %[rh], s96 offen offset:0 sc1\n\t" \
  "buffer_load_dwordx4 v[20:23], v193, %[rh], s96 offen offset:1024 sc1\n\t" \
  "buffer_load_dwordx4 v[24:27], v193, %[rh], s96 offen offset:2048 sc1\n\t" \
  "buffer_load_dwordx4 v[28:31], v193, %[rh], s96 offen offset:3072 sc1\n\t" \
  "buffer_load_dwordx4 v[32:35], v195, %[rh], s77 offen offset:0 sc1\n\t" \
  "buffer_load_dwordx4 v[36:39], v195, %[rh], s77 offen offset:2048 sc1\n\t" \
  "v_mov_b64 v[40:41], 0\n\t" \
  "v_mov_b64 v[42:43], 0\n\t" \
  "v_mov_b64 v[44:45], 0\n\t" \
  "v_mov_b64 v[46:47], 0\n\t" \
  "v_mov_b64 v[48:49], 0\n\t" \
  "v_mov_b64 v[50:51], 0\n\t" \
  "v_mov_b64 v[52:53], 0\n\t" \
  "v_mov_b64 v[54:55], 0\n\t" \
  "v_mov_b64 v[56:57], 0\n\t" \
  "v_mov_b64 v[58:59], 0\n\t" \
  "v_mov_b64 v[60:61], 0\n\t" \
  "v_mov_b64 v[62:63], 0\n\t" \
  "v_mov_b64 v[64:65], 0\n\t" \
  "v_mov_b64 v[66:67], 0\n\t" \
  "v_mov_b64 v[68:69], 0\n\t" \
  "v_mov_b64 v[70:71], 0\n\t" \
  "v_mov_b64 v[72:73], 0\n\t" \
  "v_mov_b64 v[74:75], 0\n\t" \
  "v_mov_b64 v[76:77], 0\n\t" \
  "v_mov_b64 v[78:79], 0\n\t" \
  "v_mov_b64 v[80:81], 0\n\t" \
  "v_mov_b64 v[82:83], 0\n\t" \
  "v_mov_b64 v[84:85], 0\n\t" \
  "v_mov_b64 v[86:87], 0\n\t" \
  "v_mov_b64 v[88:89], 0\n\t" \
  "v_mov_b64 v[90:91], 0\n\t" \
  "v_mov_b64 v[92:93], 0\n\t" \
  "v_mov_b64 v[94:95], 0\n\t" \
  "v_mov_b64 v[96:97], 0\n\t" \
  "v_mov_b64 v[98:99], 0\n\t" \
  "v_mov_b64 v[100:101], 0\n\t" \
  "v_mov_b64 v[102:103], 0\n\t" \
  "v_mov_b64 v[136:137], 0\n\t" \
  "v_mov_b64 v[138:139], 0\n\t" \
  "v_mov_b64 v[140:141], 0\n\t" \
  "v_mov_b64 v[142:143], 0\n\t" \
  "s_waitcnt vmcnt(0)\n\t" \
  "s_add_u32 s59, s59, 1\n\t" \
  "s_bitcmp1_b32 s58, 2\n\t" \
  "s_cbranch_scc1 10b\n\t" \
  "v_max_u32 v148, v0, v1\n\t" \
  "v_max3_u32 v148, v148, v2, v3\n\t" \
  "v_max3_u32 v148, v148, v4, v5\n\t" \
  "v_max3_u32 v148, v148, v6, v7\n\t" \
  "v_max3_u32 v148, v148, v8, v9\n\t" \
  "v_max3_u32 v148, v148, v10, v11\n\t" \
  "v_max3_u32 v148, v148, v12, v13\n\t" \
  "v_max3_u32 v148, v148, v14, v15\n\t" \
  "v_max3_u32 v148, v148, v16, v17\n\t" \
  "v_max3_u32 v148, v148, v18, v19\n\t" \
  "v_max3_u32 v148, v148, v20, v21\n\t" \
  "v_max3_u32 v148, v148, v22, v23\n\t" \
  "v_max3_u32 v148, v148, v24, v25\n\t" \
  "v_max3_u32 v148, v148, v26, v27\n\t" \
  "v_max3_u32 v148, v148, v28, v29\n\t" \
  "v_max3_u32 v148, v148, v30, v31\n\t" \
  "v_max3_u32 v148, v148, v32, v33\n\t" \
  "v_max3_u32 v148, v148, v34, v35\n\t" \
  "v_max3_u32 v148, v148, v36, v37\n\t" \
  "v_max3_u32 v148, v148, v38, v39\n\t" \
  "v_cmp_eq_u32 vcc, -1, v148\n\t" \
  "s_cbranch_vccz 10b\n\t" \
  "s_bitcmp1_b32 s59, 6\n\t" \
  "s_cbranch_scc0 11b\n\t" \
  "7:\n\t" \
  "92:\n\t" \
  "s_mov_b32 s42, s58\n\t" \
  "s_waitcnt vmcnt(0) lgkmcnt(0)\n\t" \
  ""

#define V53 "v0", "v1", "v2", "v3", "v4", "v5", "v6", "v7", "v8", "v9", "v10", "v11", "v12", "v13", "v14", "v15", "v16", "v17", \
  "v18", "v19", "v20", "v21", "v22", "v23", "v24", "v25", "v26", "v27", "v28", "v29", "v30", "v31", "v32", "v33", "v34", "v35", \
  "v36", "v37", "v38", "v39", "v40", "v41", "v42", "v43", "v44", "v45", "v46", "v47", "v48", "v49", "v50", "v51", "v52"
#define V100 V53, "v53", "v54", "v55", "v56", "v57", "v58", "v59", "v60", "v61", "v62", "v63", "v64", "v65", "v66", "v67", "v68", "v69", "v70", \
  "v71", "v72", "v73", "v74", "v75", "v76", "v77", "v78", "v79", "v80", "v81", "v82", "v83", "v84", "v85", "v86", "v87", "v88", \
  "v89", "v90", "v91", "v116", "v117", "v118", "v119"
#define STEP_PV
#define CB_CL "s60", "s61", "s62", "s63", "s64", "s65", "s66", "s67", "s68", "s69",
#define STEPS(BODY, ...) asm volatile(BODY : "=&{v[92:99]}"(X), "+{s[40:55]}"(P) STEP_PV \
  : "{v[120:151]}"(W.v[0]), "{v[152:183]}"(W.v[1]), "{v[184:215]}"(W.v[2]), "{v[216:247]}"(W.v[3]), "{v[100:115]}"(C), [rh] "s"(rh), [wv] "s"(w), [ru] "s"(rum), [sel] "s"(sel) __VA_ARGS__ \
  : "memory", "vcc", "scc", "s56", "s57", "s58", "s59", "s76", "s77", "s78", "s79", "s80", "s81", "s82", "s83", "s84", "s85", "s86", "s87", "s88", "s89", \
    "s90", "s91", "s92", "s93", "s94", "s95", "s96", "s97", V100)
__device__ __forceinline__ void keep_a(const Wts& W) {
#pragma unroll
  for (int i = 0; i < 32; ++i) asm volatile("" :: "a"(W.a[i]));
}
__device__ __forceinline__ char* lds_base(char* smem, int off) { asm volatile("" : "+v"(off)); return smem + off; }
__device__ __forceinline__ u32x2 tr4(unsigned p) { return __builtin_bit_cast(u32x2, __builtin_amdgcn_ds_read_tr16_b64_v4i16((LAS bf16x4*)(uintptr_t)p)); }
__device__ __forceinline__ float rb1(float x) { return __uint_as_float(pk2(0.f, x)); }

__device__ __forceinline__ void steps_init(u32x16& P, int h, int w, unsigned place) {
#pragma unroll
  for (int i = 0; i < 16; ++i) P[i] = 0;
  P[2] = 1; P[3] = CDUR; P[14] = H_MEM + (w >> 1) * CHK + (w & 1) * 4096 + (h & RHO) * 1024; P[15] = place;
}
__device__ __forceinline__ void steps_enter(u32x16& P, Clk& ck, bool dead) {
  ck.sync();
  P[2] = (P[2] & 3u) | (dead ? 4u : 0u); P[6] = ck.c2t << 15; P[11] = ck.ce; P[12] = ck.t2c; P[13] = ck.te;
#pragma unroll
  for (int i = 0; i < 16; ++i) P[i] = __builtin_amdgcn_readfirstlane(P[i]);
}

__device__ __forceinline__ void role_qk(const ChainArgs& a, int h, char* smem, const int w, Gate& G) {
  int lane = opaque_tid(0);
  const int b = h; (void)b;
  u64* prof = a.prof; (void)prof;
  PDECL(L_PRF, 4);
  const float CS = 0.08838834764831845f * 1.4426950408889634f;
  GAS unsigned* abortw = (GAS unsigned*)(a.sync + SY_ABORT);
  bool dead = false;
  Wts W;
  Clk ck; ck.init();
  u32x4 gv;
  if (QK_D) while (__builtin_amdgcn_s_memrealtime() - G.entry < QK_D) __builtin_amdgcn_s_sleep(8);
  load_weights<0>(a, h, w, lane, smem, W, G, gv);
  TP(1);
  if (!G.look(a.sync, gv)) return;
  PZERO;
  const Sel sel = G.sel;
  const __amdgpu_buffer_rsrc_t rh = rsrc_hot(a.sync, sel), rum = rsrc_u(a.sync);
  auto pm_o = [&](int l) { return 2 * CHK + h * 256 + (l & 1) * 128 + (l >> 3) * 16 + (l & 6) * 2; };
  auto fac = [&](int l, int hf) { return ((l & 7) >> 1) == 2 * (l >> 5) + ((h >> 1) & 1) && ((l >> 3) & 1) == hf ? 1.f : 0.f; };
  u32x16 C, P;
  {
    const int r = lane & 15, g = lane >> 4;
    const unsigned l0 = (unsigned)(uintptr_t)(LAS char*)smem;
    C[0] = lane * 16; C[1] = l0 + L_MEM + lane * 16; C[2] = l0 + L_WTS + w * 4096 + lane * 16; C[3] = C[2] + 65536;
    C[4] = (lane & 2) ? 0xEEEE : 0x4444;
    C[5] = l0 + L_XCH + (16 * w + 2 * (r >> 2) + (r & 1)) * 32 + g * 8 + ((r >> 1) & 1) * 4;
    C[6] = l0 + L_XK + (32 * w + r) * 16 + g * 4;
    C[7] = w == 3 ? H_PX + (h >> 3) * CHK + (h & 7) * 1024 + lane * 16 : w ? (2 * h + 1) * CHK + (w - 1) * 4096 + lane * 16 : l0 + L_XCH + (8 * g + (r >> 2)) * 32 + (r & 3) * 8;
    C[8] = l0 + L_XK + (8 * g + (r >> 2)) * 16 + (r & 1) * 8;
    C[9] = w ? (lane & 1) * 4096 : (lane == 0 ? H_ANN + h * 128 : 0x7fffffffu);
    C[10] = w == 0 ? (h >> 4) * CHK + (h & 15) * 512 + ((lane >> 1) & 1) * 256 + (lane >> 5) * 128 + (lane & 1) * 64 + ((lane >> 3) & 3) * 16 + ((lane >> 2) & 1) * 8 :
            w < 3 ? H_PX + ((w - 1) * 4 + (h >> 3)) * CHK + (h & 7) * 1024 + lane * 16 :
            lane < 32 ? (h >> 4) * CHK + (h & 15) * 512 + lane * 16 : (lane < 48 ? 2 * CHK + h * 256 + (lane - 32) * 16 : 0x7fffffffu);
    C[11] = w == 0 ? pm_o(lane) : (lane < (w == 3 ? 32 : 16) ? H_PX + (w == 3 ? 8 + g : 7 + w) * CHK + hpos(h) * 256 + r * 16 : 0x7fffffffu);
    C[12] = H_TR + (8 * w + (lane & 7)) * 128; C[13] = l0 + L_PRF + w * 1024 + (lane >> 6);
    unsigned mk = 0;
#pragma unroll
    for (int i = 0; i < 8; ++i) mk |= (16 * ((i >> 1) & 1) + 8 * (g & 1) + 4 * (g >> 1) + 2 * (i & 1) + (i >> 2) > 16 * (w - 1) + r ? 1u : 0u) << i;
    C[14] = w ? mk : __float_as_uint(fac(lane, 0)); C[15] = __float_as_uint(fac(lane, 1));
    asm volatile("" : "+v"(C));
  }
  steps_init(P, h, w, 2 * h * CHK + max(3 * w - 4, 0) * 1024);
  u32x4 k0[2];
  const unsigned seen = start_wait(a.sync, abortw, lane, dead, CT_Q + ct_near(sel) + h, 1u);
  if (w) P[3] = seen;
#pragma unroll
  for (int i = 0; i < 2; ++i) k0[i] = ld16(rum, x_slot(1, 0, h, sel) + (2 * w + i) * 1024 + lane * 16);
  G.arrive(a.sync);
#pragma unroll
  for (int i = 0; i < 2; ++i) *(u32x4*)(smem + L_KX + (2 * w + i) * 1024 + lane * 16) = k0[i];
  SSTAMP(96 + h);
  TP(2);
  GAS unsigned* kcp = ctr(a, CT_Q + ct_near(sel) + h);
  unsigned spins = 0;
  while (true) {
    u32x8 X; u32x16 pv; (void)pv;
    steps_enter(P, ck, dead);
    STEPS(QK_BODY, , [cs] "s"(CS), [kc] "s"(kcp));
    keep_a(W);
    const unsigned res = __builtin_amdgcn_readfirstlane(P[2]), s = __builtin_amdgcn_readfirstlane(P[4]);
    lane = opaque_tid(0);
    if (res & 8u) {
      auto sw = [](float v, int k) { return __int_as_float(k == 0 ? __builtin_amdgcn_mov_dpp(__float_as_int(v), 0xB1, 0xf, 0xf, false) :
                                                           k == 1 ? __builtin_amdgcn_mov_dpp(__float_as_int(v), 0x4E, 0xf, 0xf, false) :
                                                                    __builtin_amdgcn_mov_dpp(__float_as_int(v), 0x141, 0xf, 0xf, false)); };
      float x[5], d = fmaxf(__uint_as_float(X[4]), 0.f);
      d = fmaxf(d, sw(d, 0)); d = fmaxf(d, sw(d, 1)); d = fmaxf(d, sw(d, 2));
#pragma unroll
      for (int i = 0; i < 5; ++i) x[i] = __builtin_amdgcn_exp2f(__uint_as_float(X[i]) - d);
      float z = ((x[0] + x[1]) + (x[2] + x[3])) + x[4];
      z += sw(z, 0); z += sw(z, 1); z += sw(z, 2);
      z = __builtin_amdgcn_rcpf(z);
      const float y = sw(x[4], 0), f = __builtin_amdgcn_exp2f(-d) * z;
      const unsigned pm = (lane & 1) ? pk2(fac(lane, 0) * f, fac(lane, 1) * f) : pk2(x[4] * z, y * z);
      if (!(__uint_as_float(X[5]) < 1.0e18f))
        __builtin_amdgcn_raw_buffer_store_b32(nosent(pm), rh, pm_o(lane), H_P + ((s - 1) & 3) * 3 * CHK, ST_AUX);
    }
    if (res & 64u) break;
    if (res & 32u) {
      u32x4 k[3];
      P[3] = start_wait(a.sync, abortw, lane, dead, CT_Q + ct_near(sel) + h, s + 1);
#pragma unroll
      for (int i = 0; i < 3; ++i) k[i] = ld16(rum, x_slot(1, s, h, sel) + max(3 * w - 4 + i, 0) * 1024 + lane * 16);
#pragma unroll
      for (int i = 0; i < 3; ++i) *(u32x4*)(smem + L_KX + max(3 * w - 4 + i, 0) * 1024 + lane * 16) = k[i];
    }
    if (res & 17u) {
      spins |= 63u;
      if (dead || poll_fail(spins, abortw, lane)) dead = true;
    }
  }
  PDUMP(0);
}

__device__ __forceinline__ void role_vo(const ChainArgs& a, int h, char* smem, const int w, Gate& G) {
  int lane = opaque_tid(0);
  const int b = 32 + h; (void)b;
  u64* prof = a.prof; (void)prof;
  PDECL(L_PRF, 4);
  GAS unsigned* abortw = (GAS unsigned*)(a.sync + SY_ABORT);
  bool dead = false;
  Wts W;
  Clk ck; ck.init();
  u32x4 gv;
  load_weights<1>(a, h, w, lane, smem, W, G, gv);
  if (!G.look(a.sync, gv)) return;
  PZERO;
  const Sel sel = G.sel;
  const __amdgpu_buffer_rsrc_t rh = rsrc_hot(a.sync, sel), rum = rsrc_u(a.sync);
  u32x16 C, P;
  {
    const int r = lane & 15, g = lane >> 4;
    const unsigned l0 = (unsigned)(uintptr_t)(LAS char*)smem;
#pragma unroll
    for (int i = 0; i < 16; ++i) C[i] = H_TR + (8 * w + (lane & 7)) * 128;
    C[0] = lane * 16; C[1] = l0 + L_MEM + lane * 16; C[2] = l0 + L_WTS + w * 4096 + lane * 16; C[3] = C[2] + 65536;
    C[4] = (lane & 2) ? 0xEEEE : 0x4444;
    C[5] = l0 + L_XCH + (32 * w + r) * 16 + g * 4;
    C[6] = (lane & 8) ? 0xEEEE : 0x4444;
    C[7] = l0 + L_XCH + (32 * (g >> 1) + 8 * (g & 1) + (r >> 2)) * 16 + (r & 1) * 8;
    C[8] = ((g & 1) * 8 + (g >> 1)) * CHK + r * 16;
    C[13] = l0 + L_PRF + w * 1024 + (lane >> 6);
    asm volatile("" : "+v"(C));
  }
  start_wait(a.sync, abortw, lane, dead, -1, 0u);
  G.arrive(a.sync);
  SSTAMP(128 + h);
  steps_init(P, h, w, SY_UM + (16 * (h & 3) + 2 * w) * CHK + hpos(h) * 256);
  unsigned spins = 0;
  while (true) {
    u32x8 X; u32x16 pv; (void)pv;
    steps_enter(P, ck, dead);
    STEPS(VO_BODY);
    keep_a(W);
    lane = opaque_tid(0);
    if (__builtin_amdgcn_readfirstlane(P[2]) & 64u) break;
    spins |= 63u;
    if (dead || poll_fail(spins, abortw, lane)) dead = true;
  }
  PDUMP(0);
}

__device__ __forceinline__ void calibrate(const ChainArgs& a, int cal, int slot, u64 entry) {
  const u32x4 rs = own_rsrc(a.sync, 64 * PAGE);
  const unsigned so = cal * PAGE + (slot >> 1) * CHK + (slot & 1) * 4096;
  unsigned best[2] = {1u << 30, 1u << 30};
  bool sure;
  {
    unsigned z = 0;
    asm volatile("s_mov_b64 exec, 0xffff\n\tbuffer_atomic_or %0, %1, %2, %3 offen sc0 sc1\n\ts_mov_b64 exec, -1\n\ts_waitcnt vmcnt(0)"
                 : "+v"(z) : "v"((opaque_tid(0) & 8) * 1024 + (opaque_tid(0) & 7) * 256), "s"(rs), "s"(so) : "memory");
  }
  for (int i = 0;; ++i) {
    sure = min(best[0], best[1]) < CALT && max(best[0], best[1]) - min(best[0], best[1]) > 110u;
    if (i >= CALN && (sure || __builtin_amdgcn_s_memrealtime() - entry > CALM)) break;
#pragma unroll
    for (int hf = 0; hf < 2; ++hf) {
      u64 c0, c1;
      unsigned z = 0;
      asm volatile("s_mov_b64 exec, 1\n\ts_memtime %0\n\ts_waitcnt lgkmcnt(0)\n\tbuffer_atomic_or %2, off, %3, %4 sc0 sc1\n\ts_waitcnt vmcnt(0)\n\ts_memtime %1\n\ts_mov_b64 exec, -1\n\ts_waitcnt lgkmcnt(0)"
                   : "=&s"(c0), "=&s"(c1), "+v"(z) : "s"(rs), "s"(so + hf * 8192 + (i & 7) * 256) : "memory");
      best[hf] = min(best[hf], (unsigned)(c1 - c0));
    }
  }
  const u64 hs = (__builtin_amdgcn_s_memrealtime() & ~3ull) | (sure ? 2u : 0u) | ((best[1] < best[0] ? 1u : 0u) ^ die_half());
  if (opaque_tid(0) == 0) __hip_atomic_store((GAS u64*)ctr(a, CT_CAL + (slot >> 1)) + (slot & 1), hs, __ATOMIC_RELAXED, __HIP_MEMORY_SCOPE_AGENT);
}
__device__ __forceinline__ u32x4 pk8(f32x4 a, f32x4 b) { return (u32x4){pk2(a[0], a[1]), pk2(a[2], a[3]), pk2(b[0], b[1]), pk2(b[2], b[3])}; }
__device__ __forceinline__ unsigned pk2v(float a, float b) { unsigned r; asm volatile("v_cvt_pk_bf16_f32 %0, %1, %2" : "=v"(r) : "v"(a), "v"(b)); return r; }
__device__ __forceinline__ u32x4 pk8v(f32x4 a, f32x4 b) { asm volatile("s_nop 7"); return (u32x4){pk2v(a[0], a[1]), pk2v(a[2], a[3]), pk2v(b[0], b[1]), pk2v(b[2], b[3])}; }
__device__ __forceinline__ void st16g(u16* p, u32x4 v) { asm volatile("global_store_dwordx4 %0, %1, off " OUTP "\n\ts_nop 1" :: "v"(p), "v"(v) : "memory"); }
__device__ __forceinline__ void role_cb(const ChainArgs& a, int cbi, char* smem, const int w, Gate& G) {
  int lane = opaque_tid(0);
  const int b = 64 + cbi; (void)b;
  const int par = cbi >> 5, ci = cbi & 31, J = ci >> 3;
  u64* prof = a.prof; (void)prof;
  PDECL(65536, 8); PZERO;
  GAS unsigned* abortw = (GAS unsigned*)(a.sync + SY_ABORT);
  bool dead = false;
  const int tw = 2 * ci + (w & 1);
  const unsigned mo = (tw >> 5) * CHK + (tw & 31) * 256;
  Sel sel = 0;
  auto start = [&]() {
    lane = opaque_tid(0);
    SSTAMP(0 + ci);
    unsigned m0 = 0;
    if (par == 0 && w < 2) {
      const u16* m = a.mem0 + (2 * (lane >> 4) + ((lane >> 2) & 1)) * DM + 16 * tw + (lane & 8);
      m0 = m[lane & 3] | (unsigned)m[7 - (lane & 3)] << 16;
    }
    if (!G.look(a.sync, (u32x4){0u, 0u, 0u, 0u})) return false;
    sel = G.sel;
    SSTAMP(64 + ci);
    const __amdgpu_buffer_rsrc_t rh = rsrc_hot(a.sync, sel), rum = rsrc_u(a.sync);
    const unsigned s1v = ~(unsigned)(lane >> 6);
    const u32x4 sv = {s1v, s1v, s1v, s1v};
    auto um = [&](int k) {
#pragma unroll
      for (int i = 0; i < 4; ++i) st16(rum, near_half(SY_UM + (64 * k + tw) * CHK, sel) + (w >> 1) * 4096 + i * 1024 + lane * 16, sv);
    };
    um(par);
    const unsigned dv = cbi == H_DLT / CHK ? K_DEF * TU : s1v;
#pragma unroll
    for (int i = 0; i < 2; ++i) st16(rh, cbi >= 2 && cbi < 45 ? (cbi < 35 ? cbi * CHK : H_PX + (cbi - 35) * CHK) + w * 2048 + i * 1024 + lane * 16 : 0x7fffffffu, (u32x4){dv, s1v, s1v, s1v});
    if (par == 0 && w < 2) st4(rh, H_MEM + mo + lane * 4, nosent(m0));
    asm volatile("s_waitcnt vmcnt(0)" ::: "memory");
    lds_barrier();
    if (w == 0 && lane == 0) ctr_add(a, CT_INIT + J, 1u);
    SSTAMP(256 + ci);
    TP(6);
    G.arrive(a.sync);
    um(par + 2);
    {
      const __amdgpu_buffer_rsrc_t rx = mk_rsrc((void*)a.X, 4u << 20);
      u32x4 v[16];
#pragma unroll
      for (int i = 0; i < 16; ++i) v[i] = __builtin_amdgcn_raw_buffer_load_b128(rx, (unsigned)(cbi * 4096 + w * 1024 + lane * 16), (unsigned)(i * 262144), 0);
#pragma unroll
      for (int i = 0; i < 16; ++i) asm volatile("" :: "v"(v[i]));
    }
    SSTAMP(224 + ci);
    start_wait(a.sync, abortw, lane, dead, -1, 0u);
    return true;
  };
  if (!start()) return;
  Wts W;
#pragma unroll
  for (int i = 0; i < 32; ++i) asm volatile("" : "=a"(W.a[i]));
  u32x16 C, P; u32x8 C2;
  {
    const unsigned l0 = (unsigned)(uintptr_t)(LAS char*)smem, oor = 0x7fffffffu;
    const int r = lane & 7, t = (lane >> 3) & 1, g = lane >> 4, e = w & 1;
    const bool L = w < 2;
    C[0] = lane * 16;
    C[1] = L ? l0 + 32768 + e * 8192 + (lane & 2) * 512 + ((lane & 1) * 32 + (lane >> 2)) * 16 : e * CHK + t * 512 + (g & 1) * 256 + (r >> 2) * 128 + (g >> 1) * 64 + (r & 3) * 16;
    C[2] = L ? (4 * t + g) * 1024 + r * 16 : l0 + 81920 + e * 32768 + lane * 16;
    C[3] = L ? l0 + e * 1024 + lane * 8 : 2 * CHK + e * 4096 + (4 * (g >> 1) + 2 * t + (g & 1)) * 256 + 128 + r * 16;
    C[4] = ((16 * e + (lane >> 2)) * OUTW + DM + 32 * ci + 8 * (lane & 3)) * 2;
    C[5] = l0 + (L ? e * 8192 + lane * 16 : e * 512 + lane * 8); C[6] = l0 + 16384 + lane * 16; C[7] = l0 + 32768 + (L ? e * 8192 + 4096 : e * 2048) + lane * 16;
    C[8] = (lane & 31) * 128;
    C[9] = L ? (8 * (g & 1) + 4 * (g >> 1) + 2 * t + (r >> 2)) * 16 + 4 * (r < 4 ? r : 7 - r) : (8 * J + r) * 128;
    C[10] = (lane | e) ? oor : ci * 128;
    C[11] = L ? SY_CT + (CT_U + ct_near(sel) + 16 * e + (lane & 15)) * 128 : (t ? 0xEEEE : 0x4444); C[12] = L ? l0 + 81920 + e * 32768 + lane * 16 : ((lane & 2) ? 0xEEEE : 0x4444); C[13] = r < 4 ? 0x01000504u : 0x07060302u;
    C[14] = l0 + 49152 + 32 * e + (lane >> 6); C[15] = l0 + 49152 + (lane >> 6);
    C2[0] = L ? (lane < 16 ? lane * 16 : oor) : ux_tile(2 * ci) + lane * 16;
    C2[1] = lane == 0 && e && ci < 8 ? SY_CT + (CT_DONE + ct_near(~sel) + 8 * par + ci) * 128 : oor;
    C2[2] = (L ? ux_tile(2 * ci) : H_PX + 2 * e * CHK) + lane * 16;
#pragma unroll
    for (int n = 0; n < 4; ++n) C2[3 + n] = L ? H_PX + (8 + e) * CHK + (J ^ n) * 2048 + lane * 16 : C2[2] + CHK;
    C2[7] = l0 + 65536 + w * 2048 - 64 + (lane >> 6);
    asm volatile("" : "+v"(C), "+v"(C2));
#pragma unroll
    for (int i = 0; i < 16; ++i) P[i] = par - 2;
    P[2] = 48; P[6] = P[7] = K_DEF * TU; P[8] = SY_UM + tw * CHK + J * 2048; P[9] = H_MEM + mo; P[10] = 2 * CHK + J * 256; P[11] = e * 16 * UXH; P[13] = 1780; P[12] = H_DLT + par * 4096;
  }
  Clk ck; ck.init();
  unsigned spins = 0;
  while (true) {
    u32x16 pv; (void)pv;
    unsigned res = __builtin_amdgcn_readfirstlane(P[2]);
    if (res & 64u) break;
    const u32x4 rh2 = own_rsrc(a.sync + near_half(SY_HOT, sel), H_BYTES - 8192), ra = own_rsrc(a.sync, SY_BYTES), ro = own_rsrc(a.out, S_LEN * OUTW * 2);
    if (res & 1u) {
      spins |= 63u;
      if (dead || poll_fail(spins, abortw, opaque_tid(0))) dead = true;
    }
    ck.sync();
    P[2] = (res & 58u) | (dead ? 4u : 0u); P[1] = ck.ce; P[3] = ck.te; P[4] = ck.t2c; P[5] = ck.c2t << 15;
#pragma unroll
    for (int i = 0; i < 16; ++i) P[i] = __builtin_amdgcn_readfirstlane(P[i]);
    asm volatile(CB_BODY : "+{s[40:55]}"(P) STEP_PV
                 : "{v[192:207]}"(C), "{v[208:215]}"(C2), [rh] "s"(rh2), [ru] "s"(ra), [ro] "s"(ro), [wv] "s"(w), [sel] "s"(sel)
                 : "memory", "vcc", "scc", CB_CL "s56", "s57", "s58", "s59", "s76", "s77", "s78", "s79", "s80", "s81", "s82", "s83", "s84", "s85", "s86", "s87", "s88", "s89", "s90", "s91", "s92", "s93", "s94", "s95", "s96", "s97", "s98", "s99", "s100", "s101", V53, "v53", "v54", "v55", "v56", "v57", "v58", "v59", "v60", "v61", "v62", "v63", "v64", "v65", "v66", "v67", "v68", "v69", "v70", "v71", "v72", "v73", "v74", "v75", "v76", "v77", "v78", "v79", "v80", "v81", "v82", "v83", "v84", "v85", "v86", "v87", "v88", "v89", "v90", "v91", "v92", "v93", "v94", "v95", "v96", "v97", "v98", "v99", "v100", "v101", "v102", "v103", "v104", "v105", "v106", "v107", "v108", "v109", "v110", "v111", "v112", "v113", "v114", "v115", "v116", "v117", "v118", "v119", "v120", "v121", "v122", "v123", "v124", "v125", "v126", "v127", "v128", "v129", "v130", "v131", "v132", "v133", "v134", "v135", "v136", "v137", "v138", "v139", "v140", "v141", "v142", "v143", "v144", "v145", "v146", "v147", "v148", "v149", "v150", "v151", "v152", "v153", "v154", "v155", "v156", "v157", "v158", "v159", "v160", "v161", "v162", "v163", "v164", "v165", "v166", "v167", "v168", "v169", "v170", "v171", "v172", "v173", "v174", "v175", "v176", "v177", "v178", "v179", "v180", "v181", "v182", "v183", "v184", "v185", "v186", "v187", "v188", "v189", "v190", "v191");
    keep_a(W);
  }
  PDUMP(2);
}

__device__ __forceinline__ void bg_drain() {
  asm volatile("s_waitcnt vmcnt(0)" ::: "memory");
  __builtin_amdgcn_s_barrier();
  asm volatile("" ::: "memory");
}
#define BG_SLP 16
#define A3_SLP 48
__device__ __forceinline__ float blo(unsigned d) { return __uint_as_float(d << 16); }
__device__ __forceinline__ float bhi(unsigned d) { return __uint_as_float(d & 0xffff0000u); }
struct A1 {
  u32x4 kk[11], vv[8], sh[4];
  __device__ __forceinline__ void load(const ChainArgs& a, int x, int n) {
    const int lane = opaque_tid(0);
    const int d = (4 * x + (n & 3)) * 64 + 8 * (lane & 7), tb = (n >> 2) * 64 + 8 * (lane >> 3);
    const __amdgpu_buffer_rsrc_t rk = mk_rsrc(a.Ak, 4u << 20), rv = mk_rsrc(a.Av, 4u << 20), rs = mk_rsrc(a.Ashift, 8192);
#pragma unroll
    for (int i = 0; i < 4; ++i) sh[i] = __builtin_amdgcn_raw_buffer_load_b128(rs, d * 8 + 16 * i, 0, 0);
#pragma unroll
    for (int i = 0; i < 11; ++i) kk[i] = __builtin_amdgcn_raw_buffer_load_b128(rk, tb - 3 + i < 0 ? 0x7fffffffu : ((tb - 3 + i) * D_A + d) * 2, 0, 2);
#pragma unroll
    for (int i = 0; i < 8; ++i) vv[i] = __builtin_amdgcn_raw_buffer_load_b128(rv, ((tb + i) * D_A + d) * 2, 0, 2);
  }
  __device__ __forceinline__ void finish(const ChainArgs& a, int x, int n, char* T, Sel sel) {
    const int lane = opaque_tid(0);
#pragma unroll
    for (int c = 0; c < 8; ++c) {
      const unsigned t01 = sh[c >> 1][2 * (c & 1)], t23 = sh[c >> 1][2 * (c & 1) + 1];
      const float s0 = blo(t01), s1 = bhi(t01), s2 = blo(t23), s3 = bhi(t23);
      float kf[11];
#pragma unroll
      for (int i = 0; i < 11; ++i) kf[i] = (c & 1) ? bhi(kk[i][c >> 1]) : blo(kk[i][c >> 1]);
      u32x4 o;
#pragma unroll
      for (int i = 0; i < 8; i += 2) {
        float r[2];
#pragma unroll
        for (int j = 0; j < 2; ++j)
          r[j] = fmaf(kf[i + j + 3], s0, fmaf(kf[i + j + 2], s1, fmaf(kf[i + j + 1], s2, kf[i + j] * s3))) * ((c & 1) ? bhi(vv[i + j][c >> 1]) : blo(vv[i + j][c >> 1]));
        o[i >> 1] = pk2(r[0], r[1]);
      }
      *(u32x4*)(T + (8 * (lane & 7) + c) * 128 + (((lane >> 3) ^ (lane & 7)) * 16)) = o;
    }
    const __amdgpu_buffer_rsrc_t rkv = rsrc_u(a.sync);
#pragma unroll
    for (int i = 0; i < 8; ++i)
      st16(rkv, kv_row((4 * x + (n & 3)) * 64, sel) + (4 * i + (lane >> 4)) * CHK + ((lane >> 3) & 1) * 4096 + (n >> 2) * 128 + (lane & 7) * 16,
           *(const u32x4*)(T + (8 * i + (lane >> 3)) * 128 + (((lane & 7) ^ i) * 16)));
  }
};
#define WAH 4
#define DSP 4
#define SIGQ 2
__device__ __forceinline__ void task_w(const ChainArgs& a, int kind, int h, char* smem, int wv, Gate& G) {
  const int lane = opaque_tid(0), w = wv, r = lane & 15, g = lane >> 4;
  const __amdgpu_buffer_rsrc_t rw = mk_rsrc(kind == 0 ? a.Wq : (kind == 1 ? a.Wk : a.Wv), 8u << 20), rx = mk_rsrc(a.X, 4u << 20);
  const unsigned lds0 = (unsigned)(uintptr_t)(LAS char*)smem + w * 4096;
  unsigned vx[2], vw[4];
#pragma unroll
  for (int j = 0; j < 2; ++j) {
    const int rr = 8 * j + (lane >> 3);
    const int tb = kind == 0 ? rr : (kind == 1 ? 16 * ((rr >> 1) & 1) + 8 * ((rr >> 2) & 1) + 4 * (rr >> 3) + 2 * (rr & 1) : 8 * (rr >> 2) + (rr & 3));
    vx[j] = tb * DM * 2 + ((lane & 7) ^ (rr >> 1)) * 16;
  }
#pragma unroll
  for (int j = 0; j < 4; ++j) { const int row = (w * 4 + j) * 4 + g; vw[j] = row * HD * 2 + (r ^ swz_b(row)) * 16; }
  const u64 wo = kind == 2 ? bg_order(2) : (kind ? bg_order(1) : bg_order(0));
  auto xo = [&](int q) { return ((int)((wo >> (16 * q + 4 * w)) & 15) ^ (h & RHO)) * 128; };
  unsigned dT = (kind == 0 ? 16 : (kind == 1 ? 1 : 4)) * DM * 2;
  asm volatile("s_nop 4" : "+s"(dT));
  const unsigned vx0 = ((w & 1) ? vx[1] : vx[0]) + (w >> 1) * dT;
  auto st_w = [&](int n) {
#pragma unroll
    for (int j = 0; j < 4; ++j) dma16(rw, vw[j], (64 * ((int)((wo >> (4 * n)) & 15) ^ (h & RHO)) * HD + 128 * h) * 2, lds0 + (n & 7) * 16384 + j * 1024, W_NT & 16);
    dma16(rx, vx0, ((int)((wo >> (4 * n)) & 15) ^ (h & RHO)) * 128, lds0 - 3072 * w + 131072 + (n & 7) * 4096);
  };
  auto st_x = [&](int s, int b, int q, int j, bool pad) { dma16(rx, vx[j & 1], s * 65536 + (j >> 1) * dT + xo(q), lds0 + (b * 4 + q) * 16384 + j * 1024, false, pad); };
  if (kind < 2) {
    const __amdgpu_buffer_rsrc_t rs = mk_rsrc(a.Wv, 8u << 20);
#pragma unroll
    for (int n = 0; n < 4 * V_PF; ++n) dma16(rs, vw[n & 3], (64 * ((int)((bg_order(2) >> (4 * (V_PF0 + 2 * (n >> 2) + kind))) & 15) ^ (h & RHO)) * HD + 128 * h) * 2, lds0);
  }
  if (KQ_D && kind < 2) while (__builtin_amdgcn_s_memrealtime() - G.entry < KQ_D) __builtin_amdgcn_s_sleep(8);
  __builtin_amdgcn_s_waitcnt(0x0F70);
  u32x4 W[32][2], gv;
  char* xb[3][2];
#pragma unroll
  for (int i = 0; i < 6; ++i) xb[i >> 1][i & 1] = lds_base(smem, (i >> 1) * 65536 + (r >> 3) * 1024 + (r & 7) * 128 + ((4 * (i & 1) + g) ^ (r >> 1)) * 16);
  f32x4 acc[2][4];
#pragma unroll
  for (int i = 0; i < 4; ++i) acc[1][i] = vzero();
#pragma unroll
  for (int n = 0; n < WAH; ++n) st_w(n);
#pragma unroll
  for (int n = 0; n < 16; ++n) {
    asm volatile("s_waitcnt vmcnt(%0)" :: "n"(4 * WAH - 4 + (n < 16 - WAH ? WAH - 1 : 15 - n)) : "memory");
    lds_barrier();
    u32x4 f[4], y[4];
#pragma unroll
    for (int i = 0; i < 4; ++i) {
#pragma unroll
      for (int q = 0; q < 2; ++q) {
        const int row = (i & 1) * 32 + 8 * g + 4 * q + (r >> 2);
        const u32x2 u = __builtin_bit_cast(u32x2, __builtin_amdgcn_ds_read_tr16_b64_v4i16((LAS bf16x4*)(smem + (n & 7) * 16384 + row * 256 + (((4 * w + (r & 3)) ^ swz_b(row)) * 16) + 8 * (i >> 1))));
        f[i][2 * q] = u[0]; f[i][2 * q + 1] = u[1];
      }
      y[i] = *(const u32x4*)(xb[2][i & 1] + (n & 7) * 4096 + (i >> 1) * 2048);
    }
    SB0;
    if (n == 16 - WAH) G.ask(a.sync, gv);
    if (n == 15) asm volatile("" : "+v"(gv));
    if (n + WAH < 16) st_w(n + WAH);
    else {
#pragma unroll
      for (int j = 0; j < 4; ++j) st_x(1 + ((n + WAH - 16) >> 2), (n + WAH - 16) >> 2, (n + WAH) & 3, j, true);
    }
#pragma unroll
    for (int i = 0; i < 8; ++i)
      asm volatile("s_nop 1\n\tv_mfma_f32_16x16x32_bf16 %0, %1, %2, %0" : "+v"(acc[1][i & 3]) : "v"(f[2 * (i & 1) + (i >> 2)]), "v"(y[(i & 2) + (i >> 2)]));
#pragma unroll
    for (int i = 0; i < 4; ++i) { asm volatile("" : "+a"(f[i])); W[2 * n + (i & 1)][i >> 1] = f[i]; }
  }
  if (!G.look(a.sync, gv)) { bg_drain(); return; }
  TP(4);
  SSTAMP(160 + h);
  const Sel sel = G.sel;
  const __amdgpu_buffer_rsrc_t ru = mk_rsrc(a.sync, SY_BYTES);
  const unsigned sgo = (w | lane) ? 0x7fffffffu : SY_CT + (kind == 2 ? CT_V + ct_near(~sel) : CT_Q + 32 * kind + ct_near(sel)) * 128 + h * 128;
#pragma unroll
  for (int T = 0; T < 2; ++T) st16s(pk8v(acc[1][2 * T], acc[1][2 * T + 1]), ru, lane * 16 + T * 4096, x_slot(kind, 0, h, sel) + w * 1024);
  SB0;
  const u32x4 rx2 = own_rsrc(a.X, 4u << 20);
  unsigned vq[4][4];
#pragma unroll
  for (int i = 0; i < 16; ++i) { vq[i >> 2][i & 3] = vx[i & 1] + ((i >> 1) & 1) * dT + xo(i >> 2); asm volatile("" : "+v"(vq[i >> 2][i & 3])); }
  u32x4 xf[4][2], o;
  auto rd = [&](int b, int k, int T) { xf[k & 3][T] = *(const u32x4*)(xb[b][k & 1] + (k >> 1) * 4096 + T * 2048); };
  auto signal = [&](int s) {
    st4(ru, sgo, s);
    if (w == 0 && lane == 0 && s >= 0 && (s < PSEG || (s & 7) == 0 || ((s & 15) == 2 && PSEG > 2) || s > 61)) {
    }
  };
  if (kind < KQ_NOW) { bg_drain(); signal(0); }
  G.arrive(a.sync);
#pragma unroll
  for (int i = 4 * WAH; i < 28; ++i) st_x(1 + (i >> 4), i >> 4, (i >> 2) & 3, i & 3, true);
  wait_vm<26>();
  lds_barrier();
#pragma unroll
  for (int i = 0; i < 6; ++i) rd(0, i >> 1, i & 1);
  WST(kind == 2, 18);
  auto seg = [&](int s, int b) {
    const unsigned so = x_slot(kind, s - 1, h, sel) + w * 1024;
    WST(kind == 2 && s > 0 && s < 4, 18 + s);
#pragma unroll
    for (int k = 0; k < 32; ++k)
#pragma unroll
      for (int i = 0; i < 4; ++i) {
        if (k == 0) asm volatile("v_mfma_f32_16x16x32_bf16 %0, %1, %2, 0" : "=&v"(acc[b][i]) : "a"(W[k][i & 1]), "v"(xf[k & 3][i >> 1]));
        else asm volatile("v_mfma_f32_16x16x32_bf16 %0, %1, %2, %0" : "+v"(acc[b][i]) : "a"(W[k][i & 1]), "v"(xf[k & 3][i >> 1]));
        SB0;
        const int q = k >> 3, n = 4 * k + i;
        if ((k & 7) == 5 && i == 0) {
          if (q == SIGQ) wait_vm<4 * SIGQ>(); else wait_vm<20>();
          if (q == 1 && __builtin_expect(s - 2u < W_NOW, 0)) { wait_vm<4>(); __builtin_amdgcn_s_barrier(); signal(s - 1); }
          else if (q == 0 && b == 0 && __builtin_expect(s == 1, 0)) { wait_vm<32 - 4 * WAH>(); __builtin_amdgcn_s_barrier(); signal(0); }
          else __builtin_amdgcn_s_barrier();
          asm volatile("" ::: "memory");
        }
        if (i == 1 || i == 2) rd(k + 3 < 32 ? b : b ^ 1, (k + 3) & 31, i - 1);
        {
          const int m = (n + 128 - 23) & 127, j = (m & 31) / DSP, q0 = m >> 5;
          if (j < 4 && (n == 0 || ((m & 31) % DSP == 0 && n != 127))) asm volatile("s_add_i32 m0, %0, %1" :: "s"(lds0), "n"(((b ^ (q0 == 0) ^ (n < 23)) * 4 + ((q0 + 3) & 3)) * 16384 + j * 1024) : "m0", "scc");
          if (n == 0) asm volatile("s_nop 0");
          if (j < 4 && (m & 31) % DSP == 1)
            asm volatile("buffer_load_dwordx4 %0, %1, %2 offen lds" :: "v"(vq[(q0 + 3) & 3][j & 3]), "s"(rx2), "s"((s + 2 - (q0 == 0) - (n < 23)) * 65536) : "memory");
        }
        if (k == 8 * SIGQ + 5 && i == 2) signal(s - 1);
        if (k >= 1 && k < 5 && (i == 0 || i == 3)) { const f32x4 x = acc[b ^ 1][k - 1]; o[2 * (~k & 1) + (i & 1)] = pk2v(x[2 * (i & 1)], x[2 * (i & 1) + 1]); }
        if ((k == 2 || k == 4) && i == 3) { __builtin_amdgcn_raw_buffer_store_b128(o, ru, (unsigned)(lane * 16 + (k >> 2) * 4096), so, ST_AUX); asm volatile("s_nop 0" :: "v"(o)); }
        SB0;
      }
  };
  A1 a1;
#pragma unroll
  for (int i = 0; i < 11; ++i) asm volatile("" : "=v"(a1.kk[i]), "=v"(a1.vv[i & 7]), "=v"(a1.sh[i & 3]));
  const int wi = 32 * (2 - kind) + 4 * (h & 7) + w;
  for (int s = 1, ph = 0;; ++ph) {
    const int n = ph == 1 ? wi : (ph == 2 && wi >= 64 ? 32 + wi : 256);
    for (const int e = s + (ph ? A1_D : A1_0); s < e || ph == 4; s += 2) {
      seg(s, 0);
      if (s == NSEG - 1) break;
      seg(s + 1, 1);
    }
    if (ph == 4) break;
    if (ph == 3 && lane == 0) ctr_add(a, CT_A1 + ct_near(~sel) + (h >> 3), 1u);
    if (n < 128) a1.finish(a, h >> 3, n, smem + 131072 + w * 8192, sel);
    a1.load(a, h >> 3, ph == 0 ? wi : (ph == 1 && wi >= 64 ? 32 + wi : 256));
    SB0;
  }
#pragma unroll
  for (int T = 0; T < 2; ++T) st16s(pk8v(acc[0][2 * T], acc[0][2 * T + 1]), ru, lane * 16 + T * 4096, x_slot(kind, NSEG - 1, h, sel) + w * 1024);
  bg_drain();
  signal(NSEG - 1);
}
constexpr int A2_CSTR = 4160;
constexpr int A2_UOFF = 4 * A2_CSTR + 16 * 80;
constexpr int A2_USTR = 80 * 80;
constexpr int A2_WLDS = 4 * A2_CSTR + 2 * A2_USTR;
__device__ __forceinline__ void a2_wave(const ChainArgs& a, int d0, int d1, char* smem, int w, Sel sel) {
  const int lane = opaque_tid(0);
  char* Wb = smem + 1024 + w * A2_WLDS;
  char* U = Wb + A2_UOFF;
  const __amdgpu_buffer_rsrc_t rf = mk_rsrc((void*)a.Assm, 4u << 20), rd_ = mk_rsrc((void*)a.AD, 2048);
  const __amdgpu_buffer_rsrc_t rkv = rsrc_u(a.sync);
  u32x4 hi[4], uv[2][4];
  u32x2 lo[4];
  unsigned dn;
  auto ld_h = [&](int d) {
#pragma unroll
    for (int i = 0; i < 4; ++i) {
      hi[i] = __builtin_amdgcn_raw_buffer_load_b128(rf, (unsigned)((255 - 64 * i - lane) * 16), (unsigned)(d * 4096), 2);
      lo[i] = __builtin_amdgcn_raw_buffer_load_b64(rf, i == 3 && lane == 63 ? 0x7fffffffu : (255 - 64 * i - lane) * 16 - 8, (unsigned)(d * 4096), 2);
    }
    dn = __builtin_amdgcn_raw_buffer_load_b16(rd_, 0, 2 * d, 0);
  };
  auto ld_u = [&](int d) {
#pragma unroll
    for (int i = 0; i < 8; ++i) uv[i & 1][i >> 1] = __builtin_amdgcn_raw_buffer_load_b128(rkv, (unsigned)max(((i >> 1) * 64 + lane) * 16 - 8 * (i & 1), 0), kv_row(d, sel), LD_AUX);
  };
  ld_h(d0);
  {
    bool dead = false;
    ctr_wait<BG_SLP>(ctr(a, CT_A1 + ct_near(~sel) + (d0 >> 8)), 96u, (GAS unsigned*)(a.sync + SY_ABORT), lane, dead);
  }
  ld_u(d0);
  if (lane < 12) *(f32x4*)(Wb + (lane / 3) * A2_CSTR + (256 + lane % 3) * 16) = vzero();
#pragma unroll
  for (int i = 0; i < 4; ++i) if (64 * (i & 1) + lane < 80) *(f32x4*)(U + (i >> 1) * A2_USTR - 16 * 80 + (64 * (i & 1) + lane) * 16) = vzero();
  const int r = lane & 15, g = lane >> 4;
#pragma unroll 1
  for (int d = d0; d < d1; d += 96) {
    u64 at[6]; (void)at;
    AST(0);
#pragma unroll
    for (int i = 0; i < 4; ++i) {
      const unsigned src[6] = {lo[i][0], lo[i][1], hi[i][0], hi[i][1], hi[i][2], hi[i][3]};
      unsigned f[10];
#pragma unroll
      for (int x = 0; x < 10; ++x) {
        if ((x & 1) == 0) f[x] = __builtin_amdgcn_alignbit(src[5 - x / 2], src[5 - x / 2], 16);
        else f[x] = __builtin_amdgcn_perm(src[5 - (x + 1) / 2], src[5 - (x - 1) / 2], 0x07060100u);
      }
#pragma unroll
      for (int m = 0; m < 4; ++m) *(u32x4*)(Wb + m * A2_CSTR + (64 * i + lane) * 16) = (u32x4){f[3 - m], f[5 - m], f[7 - m], f[9 - m]};
    }
    if (lane == 0) uv[1][0] = (u32x4){0u, 0u, uv[1][0][0], uv[1][0][1]};
#pragma unroll
    for (int i = 0; i < 8; ++i) {
      const int ch = (i >> 1) * 64 + lane;
      *(u32x4*)(U + (i & 1) * A2_USTR + (ch >> 2) * 80 + (ch & 3) * 16) = uv[i & 1][i >> 1];
    }
    const float Dd = blo(dn);
    asm volatile("" :: "v"(Dd) : "memory");
    AST(1);
    ld_h(d + 96 < d1 ? d + 96 : D_A); ld_u(d + 96 < d1 ? d + 96 : D_A);
    f32x4 acc[4][2];
#pragma unroll
    for (int i = 0; i < 8; ++i) acc[i >> 1][i & 1] = vzero();
    const char* pa = Wb + (r & 3) * A2_CSTR + 16 * (255 - (r >> 2) + g) - 3 * 1024 - 64;
    const char* pb = U + (r - 1) * 80 + 16 * g;
    bf16x8 fa[2][4], fb[2][8];
    auto rd = [&](int b, int n, int k) {
      if (n == 0 || n > 8) fa[b][n ? n - 8 : 0] = *(const bf16x8*)(pa + 1024 * (3 - (n ? n - 8 : 0)) + 64 * (1 - k));
      else fb[b][n - 1] = *(const bf16x8*)(pb + ((n - 1) & 1) * A2_USTR + 80 * (16 * ((n - 1) >> 1) + 1 - k));
    };
#pragma unroll
    for (int n = 0; n < 12; ++n) rd(0, n, 0);
    AST(2);
#pragma unroll 1
    for (int k = 0; k < 16; k += 2) {
#pragma unroll
      for (int b = 0; b < 2; ++b) {
        int q = 0;
#pragma unroll
        for (int j = 0; j < 4; ++j)
#pragma unroll
          for (int i = j; i < 4; ++i)
#pragma unroll
            for (int mt = 0; mt < 2; ++mt) {
              asm volatile("v_mfma_f32_16x16x32_bf16 %0, %1, %2, %0" : "+v"(acc[i][mt]) : "v"(fa[b][j]), "v"(fb[b][2 * (i - j) + mt]));
              SB0;
              if (q < 12) rd(b ^ 1, q, 1);
              if (q == 12) { pa -= 64; pb -= 80; }
              ++q;
              SB0;
            }
      }
    }
    AST(3);
    u32x4 kv[4];
#pragma unroll
    for (int i = 0; i < 4; ++i) kv[i] = *(const u32x4*)(U + (16 * i + r) * 80 + 16 * g);
    asm volatile("s_nop 7\n\ts_waitcnt vmcnt(0)" ::: "memory");
#pragma unroll
    for (int i = 0; i < 8; ++i) asm volatile("" : "+v"(acc[i >> 1][i & 1]));
    if (lane == 0 && d > d0) ctr_add(a, CT_A2 + ct_near(~sel) + ((d - 96) >> 6), 1u);
#pragma unroll
    for (int i = 0; i < 4; ++i) {
      const f32x4 x = acc[i][1], y = acc[i][0];
      st16s((u32x4){pk2(fmaf(blo(kv[i][0]), Dd, x[0]), fmaf(bhi(kv[i][0]), Dd, x[1])), pk2(fmaf(blo(kv[i][1]), Dd, x[2]), fmaf(bhi(kv[i][1]), Dd, x[3])),
                    pk2(fmaf(blo(kv[i][2]), Dd, y[0]), fmaf(bhi(kv[i][2]), Dd, y[1])), pk2(fmaf(blo(kv[i][3]), Dd, y[2]), fmaf(bhi(kv[i][3]), Dd, y[3]))},
            rkv, (unsigned)(i * 1024 + r * 64 + g * 16), kv_row(d, sel));
    }
    AST(4);
  }
  wait_vm<0>();
  if (lane == 0) ctr_add(a, CT_A2 + ct_near(~sel) + ((d0 + (d1 - 1 - d0) / 96 * 96) >> 6), 1u);
}
__device__ __forceinline__ void task_u(const ChainArgs& a, int h, char* smem, int wv, Gate& G) {
  const int lane = opaque_tid(0);
  const int w = wv;
  GAS unsigned* abortw = (GAS unsigned*)(a.sync + SY_ABORT);
  bool dead = false;
  char* sb = smem + w * 32768;
  const __amdgpu_buffer_rsrc_t rwo = mk_rsrc(a.Wo, 8u << 20);
  const unsigned lds0 = (unsigned)(uintptr_t)(LAS char*)sb;
  unsigned wo4[4];
#pragma unroll
  for (int i = 0; i < 4; ++i) { const int row = i * 4 + (lane >> 4); wo4[i] = row * DM * 2 + (((lane & 15) ^ swz_b(row)) * 16); }
  asm volatile("s_nop 4");
  auto stage = [&](int n) {
    const int p = (U_ORD >> (4 * n)) & 3;
#pragma unroll
    for (int i = 0; i < 16; ++i)
      dma16(rwo, wo4[i & 3], (unsigned)(((128 * h + 64 * (p & 1) + 16 * (i >> 2)) * DM + 256 * w + 128 * (p >> 1)) * 2), lds0 + (n & 1) * 16384 + i * 1024, W_NT & 2);
  };
  __builtin_amdgcn_s_waitcnt(0x0F70);
  u32x4 W[16][4];
  stage(0); stage(1);
#pragma unroll
  for (int n = 0; n < 4; ++n) {
    const int p = (U_ORD >> (4 * n)) & 3;
    if (n < 3) wait_vm<16>(); else wait_vm<0>();
#pragma unroll
    for (int t = 0; t < 8; ++t) {
      const u32x8 f = tr_frag(sb + (n & 1) * 16384, t, lane);
      u32x4 f0 = {f[0], f[1], f[2], f[3]}, f1 = {f[4], f[5], f[6], f[7]};
      asm volatile("" : "+a"(f0), "+a"(f1));
      W[8 * (p >> 1) + t][2 * (p & 1)] = f0; W[8 * (p >> 1) + t][2 * (p & 1) + 1] = f1;
    }
    asm volatile("s_waitcnt lgkmcnt(0)" ::: "memory");
    if (n + 2 < 4) stage(n + 2);
  }
  lds_barrier();
  if (!G.pass(a.sync)) return;
  SSTAMP(192 + h);
  const Sel sel = G.sel;
  const u32x4 ru = own_rsrc(a.sync, SY_BYTES);
  u32x4 V[2][8], pc[2], o[2];
  f32x4 c[2][2];
  unsigned cv, cw, cx, vo[8], lk[2], sgo = (w | lane) ? 0x7fffffffu : SY_CT + (CT_U + ct_near(sel) + h) * 128, nd = (lane & 1) ? 3 - URING : 2;
#pragma unroll
  for (int i = 0; i < 8; ++i) { vo[i] = lane * 16 + ux_tile(16 * w + 2 * i); asm volatile("" : "+v"(vo[i])); }
#pragma unroll
  for (int i = 0; i < 2; ++i) lk[i] = SY_CT + (ct_near(~sel) + ((lane & 1) ? CT_DONE + 8 * i + (h & 7) : CT_V + h)) * 128;
  char* vl = lds_base(smem, lane * 16);
  const unsigned vl2 = lane * 16 + w * 2048;
  auto ld_v = [&](int s, bool pad = false) {
    if (pad) asm volatile("s_nop 4\n\tbuffer_load_dwordx4 %0, %2, %3, %4 offen sc1\n\tbuffer_load_dwordx4 %1, %2, %3, %4 offen offset:1024 sc1" : "=&v"(pc[0]), "=&v"(pc[1]) : "v"(vl2), "s"(ru), "s"(x_slot(2, s, h, sel)) : "memory");
    else asm volatile("buffer_load_dwordx4 %0, %2, %3, %4 offen sc1\n\tbuffer_load_dwordx4 %1, %2, %3, %4 offen offset:1024 sc1" : "=&v"(pc[0]), "=&v"(pc[1]) : "v"(vl2), "s"(ru), "s"(x_slot(2, s, h, sel)) : "memory");
  };
  auto signal = [&](int s) {
    asm volatile("buffer_store_dword %0, %1, %2, 0 offen sc1" :: "v"(s), "v"(sgo), "s"(ru) : "memory");
  };
  auto st_u = [&](int n, unsigned ub) { asm volatile("buffer_store_dwordx4 %0, %1, %2, %3 offen offset:%4 sc1" :: "v"(o[n & 1]), "v"(vo[n >> 1]), "s"(ru), "s"(ub), "n"((n & 1) * 1024) : "memory"); };
  auto fetch = [&](int s, bool seen) {
    if (s && !seen) { bg_drain(); signal(0); }
    if (!seen) ctr_wait<1>(ctr(a, CT_V + ct_near(~sel) + h), s + 1, abortw, lane, dead);
    ld_v(s, true);
    asm volatile("s_waitcnt vmcnt(0)" : "+v"(pc[0]), "+v"(pc[1]) :: "memory");
    if (seen) { __builtin_amdgcn_s_barrier(); signal(0); }
    WST(s == 0, 24);
    if (s == 0) TP(3);
#pragma unroll
    for (int i = 0; i < 2; ++i) *(u32x4*)(vl + 16384 + w * 2048 + i * 1024) = pc[i];
    lds_barrier();
#pragma unroll
    for (int i = 0; i < 8; ++i) V[s][i] = *(const u32x4*)(vl + 16384 + i * 1024);
  };
#pragma unroll
  for (int i = 0; i < 8; ++i) asm volatile("" : "=v"(V[0][i]));
  asm volatile("" : "=v"(cx));
  u64 bad = 0;
  unsigned up = 0x7f000000u;
  c[1][0] = c[1][1] = vzero();
  auto seg = [&](int s, int b) {
    const unsigned ub = ux_base(s, h, sel);
    WST(s >= 0 && s < 4, 25 + s);
#pragma unroll
    for (int n = 0; n < 16; ++n)
#pragma unroll
      for (int q = 0; q < 8; ++q) {
        const int kp = q >> 1, m = q & 1;
        if (kp == 0) asm volatile("v_mfma_f32_16x16x32_bf16 %0, %1, %2, 0" : "=&v"(c[n & 1][m]) : "v"(V[b][4 * m + kp]), "a"(W[n][kp]));
        else asm volatile("v_mfma_f32_16x16x32_bf16 %0, %1, %2, %0" : "+v"(c[n & 1][m]) : "v"(V[b][4 * m + kp]), "a"(W[n][kp]));
        SB0;
        if (q >= 2 && q < 6) { const f32x4 x = c[~n & 1][(q - 2) >> 1]; o[~n & 1][q - 2] = pk2v(x[2 * (q & 1)], x[2 * (q & 1) + 1]); }
        if (q == 6) st_u((n - 1) & 15, n ? ub : up);
        if (q == 5) asm volatile("" :: "v"(o[n & 1]));
        if (n == U_LD && q == 0) {
          if (__builtin_expect(bad != 0, 0)) {
            if (s <= U_NOW) {
              asm volatile("s_waitcnt vmcnt(%1)" : "+v"(cw) : "n"(U_LD - 2) : "memory");
              __builtin_amdgcn_s_barrier();
              signal(s - 1);
              bad = (int)__builtin_amdgcn_readfirstlane(cw) <= s;
            }
            if (bad & 1) ctr_wait<2>(ctr(a, CT_V + ct_near(~sel) + h), min(s + 2, NSEG), abortw, lane, dead);
            if (bad & 2) {
              ctr_wait<U_SLP>(ctr(a, CT_DONE + ct_near(~sel) + 8 * b + (h & 7)), max(s + 3 - URING, 0), abortw, lane, dead);
              for (int i = 0; i < U_STG * (h & 7) + U_ST0; ++i) __builtin_amdgcn_s_sleep(1);
            }
          }
          ld_v(s > 0 ? min(s + 1, NSEG - 1) : 0);
        }
        if (n == 1 && q == 7) asm volatile("buffer_load_dword %0, %1, %2, 0 offen sc1" : "=&v"(cw) : "v"(s <= U_NOW ? lk[0] : 0x7fffffffu), "s"(ru) : "memory");
        if (n == 13 && q == 0 && b == 0) asm volatile("buffer_load_dword %0, %1, %2, 0 offen sc1" : "=&v"(cx) : "v"(s == 0 ? lk[0] : 0x7fffffffu), "s"(ru) : "memory");
        if (n == U_LD && q == 7) asm volatile("buffer_load_dword %0, %1, %2, 0 offen sc1" : "=&v"(cv) : "v"(lk[b ^ 1]), "s"(ru) : "memory");
        if (n == U_SIG2 && q == 0) asm volatile("s_waitcnt vmcnt(%5)" : "+v"(pc[0]), "+v"(pc[1]), "+v"(cv), "+v"(cw), "+v"(cx) : "n"(U_SIG2 - U_LD - 1) : "memory");
        if (n == U_SIG2 && (q == 1 || q == 7)) *(u32x4*)(vl + (b ^ 1) * 8192 + w * 2048 + (q >> 2) * 1024) = pc[q >> 2];
        if (n == U_SIG2 + 1 && q == 0) { bad = __builtin_amdgcn_ballot_w64((int)cv < (s < U_NOW && lane > 1 ? NSEG : min(s + (int)nd, NSEG - 1))); asm volatile("" : "+s"(bad)); }
        if (n == U_SIG2 + 1 && q == 7) lds_barrier();
        if (n == U_SIG2 + 2 && q == 0) signal(s - 1);
        if ((n == U_SIG2 + 2 || n == U_SIG2 + 3) && (q & 1)) V[b ^ 1][4 * (n - U_SIG2 - 2) + (q >> 1)] = *(const u32x4*)(vl + (b ^ 1) * 8192 + (4 * (n - U_SIG2 - 2) + (q >> 1)) * 1024);
        SB0;
      }
    up = ub;
  };
  for (int s = 0; s < NSEG; s += 2) {
    if (__builtin_expect(s == 0, 0)) fetch(0, false);
    seg(s, 0);
    if (__builtin_expect(s == 0, 0)) {
      o[1] = pk8v(c[1][0], c[1][1]);
      st_u(15, up);
      asm volatile("s_waitcnt vmcnt(4)" : "+v"(cx) :: "memory");
      fetch(1, (int)__builtin_amdgcn_readfirstlane(cx) >= 1);
    }
    seg(s + 1, 1);
  }
  o[1] = pk8v(c[1][0], c[1][1]);
  st_u(15, up);
  bg_drain();
  signal(NSEG - 1);
}

__device__ __forceinline__ void task_a3(const ChainArgs& a, int dg, int t0, char* T, Sel sel) {
  const int lane = opaque_tid(0);
  const int d = dg * 64 + 8 * (lane & 7);
  const int tb = t0 + 8 * (lane >> 3);
  const __amdgpu_buffer_rsrc_t rz = rsrc_u(a.sync);
  u32x4 qq[16], zz[8][2];
#pragma unroll
  for (int i = 0; i < 16; ++i) qq[i] = LDNT((const u32x4*)(a.Aq + (long)(tb + 64 * (i >> 3) + (i & 7)) * D_A + d));
  {
    u32x4 zr[16];
    bool dead = false;
    ctr_wait<A3_SLP>(ctr(a, CT_A2 + ct_near(~sel) + dg), 64u, (GAS unsigned*)(a.sync + SY_ABORT), lane, dead);
  #pragma unroll
    for (int i = 0; i < 16; ++i) zr[i] = ld16(rz, kv_row(dg * 64, sel) + (2 * i + (lane >> 5)) * CHK + ((lane >> 4) & 1) * 4096 + (t0 + (lane & 15) * 8) * 2);
#pragma unroll
    for (int i = 0; i < 16; ++i) {
      const int row = 4 * i + (lane >> 4);
      *(u32x4*)(T + row * 256 + (((lane & 15) ^ (row >> 3)) * 16)) = zr[i];
    }
  }
#pragma unroll
  for (int c = 0; c < 8; ++c)
#pragma unroll
    for (int gq = 0; gq < 2; ++gq) zz[c][gq] = *(const u32x4*)(T + (8 * (lane & 7) + c) * 256 + (((gq * 8 + (lane >> 3)) ^ (lane & 7)) * 16));
#pragma unroll
  for (int i = 0; i < 16; ++i) {
    u32x4 o;
#pragma unroll
    for (int c = 0; c < 8; c += 2) {
      const unsigned z0 = zz[c][i >> 3][(i >> 1) & 3], z1 = zz[c + 1][i >> 3][(i >> 1) & 3];
      o[c >> 1] = pk2(blo(qq[i][c >> 1]) * ((i & 1) ? bhi(z0) : blo(z0)), bhi(qq[i][c >> 1]) * ((i & 1) ? bhi(z1) : blo(z1)));
    }
    st16g(a.out + (long)(tb + 64 * (i >> 3) + (i & 7)) * OUTW + d, o);
  }
}

__device__ __forceinline__ void part_a(const ChainArgs& a, char* smem, int x, int wi, int w, Sel sel) {
  char* T = smem + 1024 + w * A2_WLDS;
  if (wi >= 0) a2_wave(a, 256 * x + wi, 256 * x + 256, smem, w, sel);
  while (true) {
    unsigned t = 0;
    if (opaque_tid(0) == 0) t = __hip_atomic_fetch_add(ctr(a, CT_TK + ct_near(~sel) + x), 1u, __ATOMIC_RELAXED, __HIP_MEMORY_SCOPE_AGENT) + 1u;
    t = __builtin_amdgcn_readfirstlane(t);
    if (t >= 64) break;
    task_a3(a, 4 * x + (t >> 4), (t & 15) * 128, T, sel);
  }
}

__global__ __launch_bounds__(256, 1) void chain_kernel(ChainArgs a) {
  extern __shared__ __attribute__((aligned(16))) char smem[];
  const int b = blockIdx.x;
  {
    u32x4 rc;
    asm volatile("s_getpc_b64 s[60:61]\n\t.Lchain_here:\n\ts_sub_u32 s60, s60, .Lchain_here-_Z12chain_kernel9ChainArgs\n\ts_subb_u32 s61, s61, 0\n\ts_and_b32 s61, s61, 0xffff\n\t"
                 "s_mov_b32 s62, .Lchain_end-_Z12chain_kernel9ChainArgs\n\ts_mov_b32 s63, 0x20000\n\t"
                 "buffer_load_dwordx4 a[252:255], %1, s[60:63], 0 offen\n\tbuffer_load_dwordx4 a[252:255], %1, s[60:63], s63 offen"
                 : "={s[60:63]}"(rc) : "v"((b >> 3) * 4096 + threadIdx.x * 16) : "scc", "a252", "a253", "a254", "a255");
  }
  int w = __builtin_amdgcn_readfirstlane(threadIdx.x >> 6);
  Gate G;
  G.entry = __builtin_amdgcn_s_memrealtime(); G.open = false; G.arr = w == 0 ? b & 7 : -1;
  G.rot = ((unsigned)__builtin_amdgcn_s_getreg(20 | (3 << 11)) - b + WRONG) & 7u;
  {
    volatile LAS unsigned* t = (volatile LAS unsigned*)smem;
    if (w == 0) { t[0] = (unsigned)G.entry; t[1] = (unsigned)(G.entry >> 32); }
    __syncthreads();
    G.entry = (u64)(unsigned)__builtin_amdgcn_readfirstlane(t[0]) | ((u64)(unsigned)__builtin_amdgcn_readfirstlane(t[1]) << 32);
    __syncthreads();
  }
  G.tx = G.entry;
  if (((b + G.rot) & 4) == 0 && b < 128) {
    const int c = (b >> 6) * 32 + 8 * ((b + G.rot) & 3) + ((b >> 3) & 7);
    if (w < 2) calibrate(a, w ? 32 + (0x3200 >> (4 * (c & 3)) & 3) : c, 2 * c + w, G.entry);
    __builtin_amdgcn_s_barrier();
  }
  if (b == 0) {
    const int lane = opaque_tid(0);
    const __amdgpu_buffer_rsrc_t rs = mk_rsrc(a.sync + SY_SET, 3 * CHK);
    st16(rs, CHK + w * 8192 + lane * 128, (u32x4){SENT, SENT, SENT, SENT});
    if (w == 0 && lane >= 2) st16(rs, lane * 128, (u32x4){SENT, SENT, SENT, SENT});
    if (w == 3) {
      u32x4 v;
      bool ok;
      do {
        v = ld16(mk_rsrc(a.sync + SY_CT + CT_CAL * 128, 8192), lane * 128);
        ok = ((u64)v[1] << 32 | v[0]) - G.entry + 2000u < (1ull << 31) && ((u64)v[3] << 32 | v[2]) - G.entry + 2000u < (1ull << 31);
      } while (!__all(ok) && __builtin_amdgcn_s_memrealtime() - G.entry < 2 * CALM);
      const u64 k = lane == 32 ? 0x3333333333333333ull : (lane == 34 || lane == 35 ? 0x1111111111111111ull << (lane & 3) : 0ull);
      const int n1 = __builtin_popcountll(k & __builtin_amdgcn_ballot_w64(ok && (v[2] & 3u) == 3u)), n0 = __builtin_popcountll(k & __builtin_amdgcn_ballot_w64(ok && (v[2] & 3u) == 2u));
      if (!ok) v[0] = 0;
      if (n0 != n1) v[0] = n1 > n0 ? 3u : 2u;
      const unsigned t = (v[0] ^ lane) & 1u;
      const bool most = __builtin_popcountll(__builtin_amdgcn_ballot_w64((v[0] & 2u) && t)) > __builtin_popcountll(__builtin_amdgcn_ballot_w64((v[0] & 2u) && !t));
      const u64 m = __builtin_amdgcn_ballot_w64((v[0] & 2u) ? v[0] & 1u : (lane ^ most) & 1u);
      if (lane == 0) __hip_atomic_store((GAS u64*)(a.sync + SY_SEL), m, __ATOMIC_RELAXED, __HIP_MEMORY_SCOPE_AGENT);
    }
    bg_drain();
    if (w == 0 && lane == 0) __hip_atomic_store((GAS u64*)(a.sync + SY_EPOCH), (__builtin_amdgcn_s_memrealtime() & ~7ull) | G.rot, __ATOMIC_RELAXED, __HIP_MEMORY_SCOPE_AGENT);
  }
  while (true) {
    const unsigned rot = G.rot;
    asm volatile("" : "+s"(w));
    const int x = (b + rot) & 7, j = b >> 3, h = 8 * (x & 3) + (j & 7), role = (x & 4) + (j >> 3);
    if ((role == 0 || role == 7) && w >= 2) {
      const int n = opaque_tid(w - 2);
      const unsigned x0 = __builtin_amdgcn_raw_buffer_load_b32(mk_rsrc(a.sync, 106u << 20), n * PAGE + 4096 + (j & 31) * 128, 0, 2);
      const unsigned x1 = __builtin_amdgcn_raw_buffer_load_b32(mk_rsrc(a.out, 8u << 20), n * PAGE + (j & 31) * 128, 0, 2);
      asm volatile("" :: "v"(x0), "v"(x1));
    }
    if (role < 2) role_cb(a, 32 * role + h, smem, w, G);
    else if (role == 2) role_qk(a, h, smem, w, G);
    else if (role == 3) role_vo(a, h, smem, w, G);
    else if (role == 4) task_u(a, h, smem, w, G);
    else task_w(a, 7 - role, h, smem, w, G);
    if (G.rot == rot && role >= 4 && !NOA) part_a(a, smem, x & 3, 4 * (j - 8) + w, w, G.sel);
    if (G.rot == rot) break;
    __syncthreads();
  }
  asm volatile(".Lchain_end:");
}

extern "C" void kernel_launch(void* const* d_in, const int* in_sizes, int n_in,
                              void* d_out, int out_size, void* d_ws, size_t ws_size,
                              hipStream_t stream) {
  const u16* A_q = (const u16*)d_in[0];
  const u16* A_k = (const u16*)d_in[1];
  const u16* A_v = (const u16*)d_in[2];
  const u16* A_shift = (const u16*)d_in[3];
  const u16* A_ssmk = (const u16*)d_in[4];
  const u16* A_D = (const u16*)d_in[5];
  const u16* B_x = (const u16*)d_in[6];
  const u16* B_mem0 = (const u16*)d_in[7];
  const u16* B_Wq = (const u16*)d_in[8];
  const u16* B_Wk = (const u16*)d_in[9];
  const u16* B_Wv = (const u16*)d_in[10];
  const u16* B_Wo = (const u16*)d_in[11];
  u16* out = (u16*)d_out;
  char* ws = (char*)(((uintptr_t)d_ws + (2u << 20) - 1) & ~(uintptr_t)((2u << 20) - 1)) + WSOFF * MB;
  hipFuncSetAttribute((const void*)chain_kernel, hipFuncAttributeMaxDynamicSharedMemorySize, CHAIN_LDS);
  ChainArgs ca;
  ca.prof = (u64*)((char*)d_ws + PROF_AT);
  ca.mem0 = B_mem0; ca.Wq = B_Wq; ca.Wk = B_Wk; ca.Wv = B_Wv; ca.Wo = B_Wo; ca.X = B_x;
  ca.out = out; ca.sync = ws;
  ca.Aq = A_q; ca.Ak = A_k; ca.Av = A_v; ca.Ashift = A_shift; ca.Assm = A_ssmk; ca.AD = A_D;
  chain_kernel<<<256, 256, CHAIN_LDS, stream>>>(ca);
}
```
